```python
import torch

def _softmax(x, axis=-1):
    return torch.softmax(x, dim=axis)

def _gelu(x):
    return torch.nn.functional.gelu(x, approximate='tanh')

def _silu(x):
    return torch.nn.functional.silu(x)

def _sigmoid(x):
    return torch.sigmoid(x)

def _layernorm(x, axis=-1, eps=1e-05):
    m = x.mean(dim=axis, keepdim=True)
    v = x.var(dim=axis, keepdim=True, unbiased=False)
    return (x - m) / torch.sqrt(v + eps)

def _scatter_add(tgt, idx, src, axis=0):
    return tgt.clone().index_add_(axis, idx, src)

def _segment_sum(src, seg, n):
    out = torch.zeros((n,) + src.shape[1:], dtype=src.dtype)
    return out.index_add_(0, seg, src)

def _pad(x, pw):
    flat = tuple((v for pair in reversed(pw) for v in pair))
    return torch.nn.functional.pad(x, flat)
kernel_name = 'concat_heads__spectral_gfnet__memory_aug_recurrent_mem_tr__prompt'

def setup_inputs():

    _w = lambda a: (lambda t: t.to(torch.bfloat16) if t.is_floating_point() else t)(torch.as_tensor(a))

    def _A_setup():
        B, S, D = (1, 2048, 1024)
        Df = D // 2 + 1
        return {'x': _w((lambda *s: torch.randn(*s, dtype=torch.bfloat16) * 0.05)(B, S, D)), 'w_r': _w((lambda *s: torch.randn(*s, dtype=torch.bfloat16) * 3.2)(S, Df) * 0.02), 'w_i': _w((lambda *s: torch.randn(*s, dtype=torch.bfloat16) * 3.2)(S, Df) * 0.02)}
    _A_in = _A_setup()

    def _B_setup():
        B, S, Dm = (1, 2048, 1024)
        H, Dh = (32, 128)
        M, seg = (8, 32)
        seg = min(seg, S)
        S = S // seg * seg
        return {'x': _w((lambda *s: torch.randn(*s, dtype=torch.bfloat16) * 0.05)(B, S, Dm)), 'mem0': _w((lambda *s: torch.randn(*s, dtype=torch.bfloat16) * 0.4)(B, M, Dm)), 'Wq': _w((lambda *s: torch.randn(*s, dtype=torch.bfloat16) * 6.4)(Dm, H, Dh) * Dm ** (-0.5)), 'Wk': _w((lambda *s: torch.randn(*s, dtype=torch.bfloat16) * 6.4)(Dm, H, Dh) * Dm ** (-0.5)), 'Wv': _w((lambda *s: torch.randn(*s, dtype=torch.bfloat16) * 0.4)(Dm, H, Dh) * Dm ** (-0.5)), 'Wo': _w((lambda *s: torch.randn(*s, dtype=torch.bfloat16) * 0.4)(H, Dh, Dm) * (H * Dh) ** (-0.5)), 'seg_len': seg}
    _B_in = _B_setup()
    return {'A_x': _A_in['x'], 'A_w_r': _A_in['w_r'], 'A_w_i': _A_in['w_i'], 'B_x': _B_in['x'], 'B_mem0': _B_in['mem0'], 'B_Wq': _B_in['Wq'], 'B_Wk': _B_in['Wk'], 'B_Wv': _B_in['Wv'], 'B_Wo': _B_in['Wo'], 'B_seg_len': _B_in['seg_len']}

def reference(A_x, A_w_r, A_w_i, B_x, B_mem0, B_Wq, B_Wk, B_Wv, B_Wo, B_seg_len):

    def _A_ref(x, w_r, w_i):
        B, S, D = x.shape
        X = (lambda x, axes=(-2, -1): torch.fft.rfft2(x.float(), dim=axes))(x, axes=(-2, -1))
        W = w_r + 1j * w_i
        X = X * W
        y = (lambda x, s, axes=(-2, -1): torch.fft.irfft2(x, s=s, dim=axes).to(torch.bfloat16))(X, (S, D), axes=(-2, -1))
        return y

    def _B_ref(x, mem0, Wq, Wk, Wv, Wo, seg_len):
        B, S, Dm = x.shape
        H, Dh = (Wq.shape[1], Wq.shape[2])
        M = mem0.shape[1]
        scale = Dh ** (-0.5)
        n_seg = S // seg_len
        L = M + seg_len
        idx = torch.arange(L)
        mask = (idx[:, None] < M) | (idx[:, None] >= idx[None, :])
        mem = mem0
        outs = []
        for s in range(n_seg):
            xs = x[:, s * seg_len:(s + 1) * seg_len, :].clone()
            h = torch.concatenate([mem, xs], axis=1)
            q = torch.einsum('btd,dhe->bhte', h, Wq)
            k = torch.einsum('btd,dhe->bhte', h, Wk)
            v = torch.einsum('btd,dhe->bhte', h, Wv)
            scores = torch.einsum('bhqe,bhke->bhqk', q, k) * scale
            scores = torch.where(mask[None, None], scores, -1000000000.0)
            p = _softmax(scores, axis=-1)
            o = torch.einsum('bhqk,bhke->bhqe', p, v)
            o = torch.einsum('bhte,hed->btd', o, Wo)
            mem = o[:, :M, :].clone()
            outs.append(o[:, M:, :])
        return torch.concatenate(outs, axis=1)
    a = _A_ref(A_x, A_w_r, A_w_i)
    b = _B_ref(B_x, B_mem0, B_Wq, B_Wk, B_Wv, B_Wo, B_seg_len)
    return torch.concatenate([a, b], axis=-1)
```

```cpp
#include <hip/hip_runtime.h>
#include <hip/hip_bf16.h>
#include <stdint.h>
#include <type_traits>

typedef unsigned short u16;
typedef unsigned long long u64;
using bf16x8 = __attribute__((ext_vector_type(8))) __bf16;
using bf16x16 = __attribute__((ext_vector_type(16))) __bf16;
using f32x4 = __attribute__((ext_vector_type(4))) float;
typedef unsigned v32u __attribute__((ext_vector_type(32)));
typedef unsigned v16u __attribute__((ext_vector_type(16)));
typedef unsigned v8u __attribute__((ext_vector_type(8)));
typedef unsigned v4u __attribute__((ext_vector_type(4)));
typedef unsigned v2u __attribute__((ext_vector_type(2)));
typedef short s16x4 __attribute__((ext_vector_type(4)));
typedef float f32x2 __attribute__((ext_vector_type(2)));
#define GAS __attribute__((address_space(1)))
#define LAS __attribute__((address_space(3)))
typedef GAS unsigned gu32;
#define RLX_AGENT __ATOMIC_RELAXED, __HIP_MEMORY_SCOPE_AGENT

constexpr int S = 2048, D = 1024, H = 32, DH = 128, SEG = 32, NSEG = 64;
constexpr int DF = 513;

constexpr int NXC = 4;
constexpr int PQ = 32 / NXC, PO = 64 / NXC;

__shared__ __attribute__((aligned(16))) char g_lds[163840];
#define SPINCAP (1u << 18)
constexpr unsigned kSpinCap = SPINCAP;
constexpr int AUX_SC1 = 16;
constexpr int OOB = 0x7ff00000;
constexpr size_t UNIT = 524288;
constexpr int F_QK = 0, F_VW = 8192, CNT_FFT1 = 16384, CNT_FFT2 = 16512, CNT_ARM = 16640;
constexpr int Q_FFT1 = 16896, Q_FFT2 = 17024, Q_FFT3 = 17152;
constexpr size_t OFF_TMO = 20480;
constexpr size_t OFF_CAL = 24576, OFF_XID = 25088;
constexpr int OFF_PERM = 25600;
constexpr size_t ARM_HOST = 28672;
constexpr int OFF_REQ = 49152, OFF_ACK = 53248, CNT_HS = 17280, CNT_HSO = 17408;
constexpr size_t OFF_SCR = 65536;
constexpr size_t OFF_VM = UNIT;
constexpr int VM_SLOT = 4 << 20, VM_SLOT2 = 8 << 20;
constexpr size_t OFF_DBG = 3 * UNIT;
constexpr size_t OFF_STEP = 27 * UNIT;
constexpr int R_MEM = 0, R_EX = 131072, R_PM = 262144, R_ST = 327680, R_PX = 393216, R_DUM = 475136;
constexpr size_t OFF_QK = (size_t)48 << 20;
constexpr size_t OFF_X1 = (size_t)80 << 20;
constexpr size_t OFF_VWX = (size_t)89 << 20;
constexpr size_t WS_USED = (size_t)244 << 20;
constexpr size_t OFF_WT = (size_t)244 << 20;
constexpr int NARM = 4;

__device__ __forceinline__ int olane() {
  int t;
  asm volatile("v_mbcnt_lo_u32_b32 %0, -1, 0\n\tv_mbcnt_hi_u32_b32 %0, -1, %0" : "=v"(t));
  __builtin_assume(t >= 0 && t < 64);
  return t;
}
__device__ __forceinline__ float bf2f(u16 v) { return __uint_as_float((unsigned)v << 16); }
__device__ __forceinline__ unsigned pk2(float a, float b) {
  typedef __bf16 b2 __attribute__((ext_vector_type(2)));
  f32x2 f = {a, b};
  b2 r = __builtin_convertvector(f, b2);
  return __builtin_bit_cast(unsigned, r);
}
__device__ __forceinline__ unsigned pk2s(float a, float b) { return min(pk2(a, b), 0xfffffffeu); }
__device__ __forceinline__ u16 f2bf(float f) { return (u16)(pk2(f, 0.f) & 0xffffu); }
__device__ __forceinline__ float rbf(float f) { return __uint_as_float(pk2(0.f, f)); }
__device__ __forceinline__ float opq_one() {
  float o = 1.f;
  asm("" : "+s"(o));
  return o;
}
__device__ __forceinline__ unsigned fold_pk(f32x4 a, float one) { return pk2(a[0] + a[2], __builtin_fmaf(a[1], one, a[3])); }
__device__ __forceinline__ float mul1(float a, float c) {
  float r;
  asm("v_mul_f32 %0, %2, %1" : "=v"(r) : "v"(a), "s"(c));
  return r;
}
__device__ __forceinline__ float fma1(float a, float c, float b) {
  float r;
  asm("v_fma_f32 %0, %1, %2, %3" : "=v"(r) : "v"(a), "s"(c), "v"(b));
  return r;
}
__device__ __forceinline__ int flag_of(int seg, int h) { return h * 256 + (seg & 1) * 128 + (seg >> 1) * 4; }
__device__ __forceinline__ int xreg(int r) { return ((r & 1) << 2) | ((r >> 1) & 1) | ((r >> 1) & 2); }

__device__ __forceinline__ void tmo_report(gu32* tmo, unsigned code) {
  unsigned z = 0, c = code;
  asm volatile("" : "+v"(z), "+v"(c));
  asm volatile("global_store_dword %0, %1, %2 sc1" ::"v"(z), "v"(c), "s"(tmo) : "memory");
}
__device__ __forceinline__ bool spin_fail(unsigned& spins, gu32* tmo, unsigned code) {
  asm volatile("" ::: "memory");
  if (__builtin_expect(++spins > kSpinCap, 0)) {
    tmo_report(tmo, code);
    return true;
  }
  return false;
}
__device__ __forceinline__ unsigned wave_min(unsigned v) {
  v = min(v, (unsigned)__builtin_amdgcn_mov_dpp((int)v, 0x121, 0xf, 0xf, true));
  v = min(v, (unsigned)__builtin_amdgcn_mov_dpp((int)v, 0x122, 0xf, 0xf, true));
  v = min(v, (unsigned)__builtin_amdgcn_mov_dpp((int)v, 0x124, 0xf, 0xf, true));
  v = min(v, (unsigned)__builtin_amdgcn_mov_dpp((int)v, 0x128, 0xf, 0xf, true));
  auto r = __builtin_amdgcn_permlane32_swap(v, v, false, false);
  v = min(r[0], r[1]);
  auto q = __builtin_amdgcn_permlane16_swap(v, v, false, false);
  return min(q[0], q[1]);
}
__device__ __forceinline__ float xmax16_32(float x) {
  auto r = __builtin_amdgcn_permlane32_swap(__float_as_uint(x), __float_as_uint(x), false, false);
  x = __builtin_amdgcn_fmed3f(__uint_as_float(r[0]), __uint_as_float(r[1]), __builtin_inff());
  auto q = __builtin_amdgcn_permlane16_swap(__float_as_uint(x), __float_as_uint(x), false, false);
  return __builtin_amdgcn_fmed3f(__uint_as_float(q[0]), __uint_as_float(q[1]), __builtin_inff());
}
__device__ __forceinline__ float xsum16(float x) {
  auto q = __builtin_amdgcn_permlane16_swap(__float_as_uint(x), __float_as_uint(x), false, false);
  return __uint_as_float(q[0]) + __uint_as_float(q[1]);
}
__device__ __forceinline__ float xsum16_32(float x) {
  auto r = __builtin_amdgcn_permlane32_swap(__float_as_uint(x), __float_as_uint(x), false, false);
  return xsum16(__uint_as_float(r[0]) + __uint_as_float(r[1]));
}
__device__ __forceinline__ unsigned mx4(unsigned m, v4u v) { return max(max(max(max(m, v[0]), v[1]), v[2]), v[3]); }
__device__ __forceinline__ bool anysent(unsigned m) { return __builtin_amdgcn_ballot_w64(m == 0xffffffffu) != 0ull; }

__device__ __forceinline__ unsigned now32() { return (unsigned)__builtin_amdgcn_s_memrealtime(); }
__device__ __forceinline__ unsigned cyc32() { return (unsigned)__builtin_amdgcn_s_memtime(); }
#define CMAX 14000
__device__ __forceinline__ void wait_cyc(unsigned t) {
  t = __builtin_amdgcn_readfirstlane(t);
  int d = (int)(t - cyc32());
  if (d > CMAX) return;
  while (d > 260) {
    __builtin_amdgcn_s_sleep(1);
    d = (int)(t - cyc32());
  }
  d = __builtin_amdgcn_readfirstlane(d - 70);
  if (d > 0) asm volatile("1: s_sub_u32 %0, %0, 28\n\ts_cbranch_scc0 1b" : "+s"(d)::"scc", "memory");
}
#define CLKSPAN 124u
struct Clk {
  unsigned r, m;
  unsigned cq, inv;
};
__device__ __forceinline__ bool clk_edge(unsigned& r, unsigned& m) {
  u64 t[14], m0, m1;
  unsigned n;
#define CE_S(i) "s_memrealtime %" #i "\n\ts_nop 0\n\t"
  asm volatile("s_setprio 3\n\ts_memtime %14\n\t" CE_S(0) CE_S(1) CE_S(2) CE_S(3) CE_S(4) CE_S(5) CE_S(6) CE_S(7) CE_S(8) CE_S(9) CE_S(10)
               CE_S(11) CE_S(12) "s_memtime %15\n\ts_memrealtime %13\n\ts_setprio 0\n\ts_waitcnt lgkmcnt(0)"
               : "=s"(t[0]), "=s"(t[1]), "=s"(t[2]), "=s"(t[3]), "=s"(t[4]), "=s"(t[5]), "=s"(t[6]), "=s"(t[7]), "=s"(t[8]), "=s"(t[9]),
                 "=s"(t[10]), "=s"(t[11]), "=s"(t[12]), "=s"(t[13]), "=s"(m0), "=s"(m1));
#define CE_C(i) "s_cmp_eq_u32 %" #i ", %1\n\ts_addc_u32 %0, %0, 0\n\t"
#define CE_T(i) "s"((unsigned)t[i])
  asm("s_mov_b32 %0, 0\n\t" CE_C(2) CE_C(3) CE_C(4) CE_C(5) CE_C(6) CE_C(7) CE_C(8) CE_C(9) CE_C(10) CE_C(11) CE_C(12) CE_C(13) CE_C(14)
      : "=&s"(n)
      : CE_T(0), CE_T(1), CE_T(2), CE_T(3), CE_T(4), CE_T(5), CE_T(6), CE_T(7), CE_T(8), CE_T(9), CE_T(10), CE_T(11), CE_T(12), CE_T(13)
      : "scc");
  r = (unsigned)t[0] + 4;
  m = (unsigned)m0 + n * 8;
  return (unsigned)m1 - (unsigned)m0 <= CLKSPAN;
}
__device__ __forceinline__ void clk_init(Clk& c) {
#pragma unroll 1
  for (int k = 0; k < 3; ++k)
    if (clk_edge(c.r, c.m)) break;
  c.cq = 92500u;
  c.inv = 46432u;
}
template <int TRIES = 1>
__device__ __forceinline__ void clk_update(Clk& c, int gain) {
  unsigned r, m;
  bool ok = clk_edge(r, m);
#pragma unroll 1
  for (int k = 1; k < TRIES && !ok; ++k) ok = clk_edge(r, m);
  ok |= r - c.r >= 1500u;
  const unsigned dr = r - c.r;
  const int err = (int)(m - c.m - ((dr * c.cq) >> 12));
  unsigned q = c.cq + (unsigned)((ok & (dr * gain < 6000u)) ? err * gain : 0);
  asm("s_max_i32 %0, %0, 0x101d0\n\ts_min_i32 %0, %0, 0x1b580" : "+s"(q)::"scc");
  c.cq = q;
  c.inv -= (unsigned)__mulhi((int)c.inv, (int)(q * c.inv));
  c.r = ok ? r : c.r;
  c.m = ok ? m : c.m;
}
__device__ __forceinline__ int stamp_max(const Clk& c, v2u st, unsigned& md) {
  unsigned a = st[0] - (c.r << 4) + 0x40000000u, b = st[1], x;
#define SM_S(p) "v_max_u32_dpp %2, %2, %2 " p "\n\tv_max_u32_dpp %3, %3, %3 " p "\n\ts_nop 0\n\t"
  asm volatile("s_nop 1\n\t" SM_S("row_ror:1 row_mask:0xf bank_mask:0xf") SM_S("row_ror:2 row_mask:0xf bank_mask:0xf")
               SM_S("row_ror:4 row_mask:0xf bank_mask:0xf") SM_S("row_ror:8 row_mask:0xf bank_mask:0xf")
               SM_S("row_bcast:15 row_mask:0xa bank_mask:0xf") SM_S("row_bcast:31 row_mask:0xc bank_mask:0xf")
               "v_readlane_b32 %0, %2, 63\n\tv_readlane_b32 %1, %3, 63"
               : "=s"(x), "=s"(md), "+v"(a), "+v"(b));
  return (int)(x - 0x40000000u);
}
__device__ __forceinline__ unsigned clk_cyc(const Clk& c, int d) { return c.m + (unsigned)(((long long)d * (int)c.cq) >> 16); }
#define NS2F(ns) (((ns) * 8) / 5)
struct Pace {
  unsigned rf;
  int ed, ea, dq;
};
__device__ __forceinline__ void pace_step(Pace& p, int s, unsigned rf, unsigned md, int marg, int tol, int xlate) {
  const int arr = s ? (int)(rf - p.rf) - p.dq : 0x10000000;
  const bool late = arr > p.ea + tol;
  p.ea = min(arr, p.ea + 5);
  p.ed = (int)min(md, (unsigned)p.ed + 5u);
  p.rf = rf;
  p.dq = p.ed + marg + (late ? xlate : 0);
}

template <int SLP>
__device__ __forceinline__ void wait_flag(__amdgpu_buffer_rsrc_t rs, int off, gu32* tmo, unsigned code) {
  _Pragma("clang loop unroll(disable)") for (unsigned spins = 0;;) {
    asm volatile("" : "+v"(off));
    unsigned v = __builtin_amdgcn_raw_buffer_load_b32(rs, off, 0, AUX_SC1);
    if (__all(v == 0u)) break;
    __builtin_amdgcn_s_sleep(SLP);
    if (spin_fail(spins, tmo, code)) break;
  }
}
__device__ __forceinline__ void arm_wait(__amdgpu_buffer_rsrc_t rs, gu32* tmo) {
  _Pragma("clang loop unroll(disable)") for (unsigned spins = 0;;) {
    int off = CNT_ARM;
    asm volatile("" : "+v"(off));
    if (__all(__builtin_amdgcn_raw_buffer_load_b32(rs, off, 0, AUX_SC1) + 1u >= 64u)) break;
    __builtin_amdgcn_s_sleep(8);
    if (spin_fail(spins, tmo, 0x30007)) break;
  }
}
#define RAW_BARRIER()                                    \
  do {                                                   \
    asm volatile("" ::: "memory");                       \
    __builtin_amdgcn_s_waitcnt(0xc07f);                  \
    __builtin_amdgcn_s_barrier();                        \
    asm volatile("" ::: "memory");                       \
  } while (0)
#define BARE_BARRIER()                 \
  do {                                 \
    asm volatile("" ::: "memory");     \
    __builtin_amdgcn_s_barrier();      \
    asm volatile("" ::: "memory");     \
  } while (0)
#define VMCNT(n) asm volatile("s_waitcnt vmcnt(" #n ")" ::: "memory")
#define SB() __builtin_amdgcn_sched_barrier(0)
#define OPQ(v, e) int v = (e); asm volatile("" : "+v"(v))
template <bool NT = false>
__device__ __forceinline__ void glds16(const void* base, int voff, unsigned dst) {
  unsigned keep;
  if constexpr (NT)
    asm volatile("s_mov_b32 %0, m0\n\ts_mov_b32 m0, %3\n\ts_nop 0\n\tglobal_load_lds_dwordx4 %1, %2 nt\n\ts_mov_b32 m0, %0"
                 : "=&s"(keep)
                 : "v"(voff), "s"(base), "s"(dst)
                 : "memory");
  else
  asm volatile("s_mov_b32 %0, m0\n\ts_mov_b32 m0, %3\n\ts_nop 0\n\tglobal_load_lds_dwordx4 %1, %2\n\ts_mov_b32 m0, %0"
               : "=&s"(keep)
               : "v"(voff), "s"(base), "s"(dst)
               : "memory");
}
__device__ __forceinline__ const char* w_slow(const u16* Wq, const u16* Wk, const u16* Wv, int w, int xg) {
  const char* p = (const char*)(w < 2 || w == 6 ? Wq : (w < 4 || w == 7 ? Wk : Wv));
  const int sk = 3 - (int)((uintptr_t)p >> 7);
  const int h = w < 6 ? (xg & 3) * 8 + (w & 1) * 4 + ((sk & 7) >> 1) : ((sk + 60) & 63) >> 1;
  return (h >> 3) == (xg & 3) ? p + h * 256 : nullptr;
}
__device__ __forceinline__ void w_touch(const char* p, int row, unsigned dst) {
  unsigned keep;
  const int ln = olane();
  asm volatile("s_mov_b32 %0, m0\n\ts_mov_b32 m0, %3\n\ts_nop 0\n\tglobal_load_lds_dword %1, %2\n\ts_mov_b32 m0, %0"
               : "=&s"(keep)
               : "v"((row + (ln >> 2)) * 8192 + (ln & 3) * 64), "s"(p), "s"(dst)
               : "memory");
}
__device__ __forceinline__ f32x4 smf(v4u a, v8u b, f32x4 c, int idx) {
  return __builtin_amdgcn_smfmac_f32_16x16x64_bf16(__builtin_bit_cast(bf16x8, a), __builtin_bit_cast(bf16x16, b), c, idx, 0, 0);
}
__device__ __forceinline__ f32x4 smf1(v4u a, v8u b, f32x4 c, int idx) {
  return __builtin_amdgcn_smfmac_f32_16x16x64_bf16(__builtin_bit_cast(bf16x8, a), __builtin_bit_cast(bf16x16, b), c, idx, 0, 1);
}
__device__ __forceinline__ f32x4 mma(v4u a, v4u b, f32x4 c) {
  return __builtin_amdgcn_mfma_f32_16x16x32_bf16(__builtin_bit_cast(bf16x8, a), __builtin_bit_cast(bf16x8, b), c, 0, 0, 0);
}
__device__ __forceinline__ void st128(v4u d, __amdgpu_buffer_rsrc_t rs, int voff, int soff) {
  __builtin_amdgcn_raw_buffer_store_b128(d, rs, voff, soff, AUX_SC1);
  asm volatile("s_nop 1" ::"v"(d));
}
__device__ __forceinline__ f32x4 zero4() {
  f32x2 a, b;
  asm volatile("v_mov_b64 %0, 0" : "=v"(a));
  asm volatile("v_mov_b64 %0, 0" : "=v"(b));
  return f32x4{a[0], a[1], b[0], b[1]};
}
__device__ __forceinline__ f32x2 zero2() {
  f32x2 a;
  asm volatile("v_mov_b64 %0, 0" : "=v"(a));
  return a;
}
__device__ __forceinline__ v4u lo4(v8u v) { return v4u{v[0], v[1], v[2], v[3]}; }
__device__ __forceinline__ v4u hi4(v8u v) { return v4u{v[4], v[5], v[6], v[7]}; }
__device__ __forceinline__ v8u cat8(v4u a, v4u b) { return v8u{a[0], a[1], a[2], a[3], b[0], b[1], b[2], b[3]}; }
__device__ __forceinline__ v32u cat32(v8u a, v8u b, v8u c, v8u d) {
  const v16u x = __builtin_shufflevector(a, b, 0, 1, 2, 3, 4, 5, 6, 7, 8, 9, 10, 11, 12, 13, 14, 15);
  const v16u y = __builtin_shufflevector(c, d, 0, 1, 2, 3, 4, 5, 6, 7, 8, 9, 10, 11, 12, 13, 14, 15);
  return __builtin_shufflevector(x, y, 0, 1, 2, 3, 4, 5, 6, 7, 8, 9, 10, 11, 12, 13, 14, 15, 16, 17, 18, 19, 20, 21, 22, 23, 24, 25, 26,
                                 27, 28, 29, 30, 31);
}
__device__ __forceinline__ v2u trd(LAS char* p) {
  return __builtin_bit_cast(v2u, __builtin_amdgcn_ds_read_tr16_b64_v4i16((LAS s16x4*)p));
}
__device__ __forceinline__ int vm_slot(int s) { return (s & 1) * VM_SLOT + ((s >> 1) & 1) * VM_SLOT2; }
__device__ __forceinline__ int cls_of(unsigned perm, int x) { return (int)((perm >> (3 * x)) & 7u); }

__device__ __forceinline__ v2u numa_load(__amdgpu_buffer_rsrc_t rs, gu32* tmo) {
  unsigned c, xid;
  _Pragma("clang loop unroll(disable)") for (unsigned spins = 0;;) {
    OPQ(a, olane() * 4);
    c = __builtin_amdgcn_raw_buffer_load_b32(rs, a, (int)OFF_CAL, AUX_SC1);
    xid = __builtin_amdgcn_raw_buffer_load_b32(rs, ((a >> 5) & (NXC - 1)) * 4, (int)OFF_XID, AUX_SC1);
    if (__all((c != 0xffffffffu) & (xid != 0xffffffffu))) break;
    __builtin_amdgcn_s_sleep(16);
    if (spin_fail(spins, tmo, 0x30008)) break;
  }
  return v2u{c, xid};
}
__device__ __forceinline__ unsigned numa_perm(v2u in, LAS unsigned* tab) {
  const int lane = olane();
  const unsigned c = in[0], xid = in[1];
  tab[lane] = 0xffffu;
  tab[64 + lane] = 0xffffu;
  tab[128 + lane] = 0u;
  __builtin_amdgcn_s_waitcnt(0xc07f);
  __hip_atomic_fetch_min(tab + ((c >> 16) & 127u), c & 0xffffu, __ATOMIC_RELAXED, __HIP_MEMORY_SCOPE_WORKGROUP);
  __hip_atomic_fetch_add(tab + 128 + ((c >> 21) & 3u) * 2 + ((c >> 17) & 1u), c & 0xffffu, __ATOMIC_RELAXED, __HIP_MEMORY_SCOPE_WORKGROUP);
  __builtin_amdgcn_s_waitcnt(0xc07f);
  asm volatile("" ::: "memory");
  const unsigned s0 = tab[128 + ((xid >> 2) & 3u) * 2], s1 = tab[129 + ((xid >> 2) & 3u) * 2];
  const bool far = ((lane >> 1) & 1) != (s1 < s0 ? 1 : 0);
  unsigned key = ((tab[(xid & 15u) * 8 + (lane & 7)] + (far ? 0x10000u : 0u)) << 6) | (unsigned)lane;
  if ((lane >> 3) >= NXC) key = 0xffffffffu;
  unsigned perm = 0;
#pragma unroll 1
  for (int r = 0; r < NXC; ++r) {
    const unsigned m = wave_min(key);
    const unsigned bx = (m >> 3) & 7u, bc = m & 7u;
    perm |= bc << (3 * bx);
    if ((unsigned)(lane >> 3) == bx || (unsigned)(lane & 7) == bc) key = 0xffffffffu;
  }
  return __builtin_amdgcn_readfirstlane(perm);
}
#define VRING 64
#define VWN 0
__device__ __forceinline__ int vw_addr(unsigned pm, int tile, int seg, int gq) {
  return (int)OFF_VWX + ((((tile >> 3) * VRING + (seg & (VRING - 1))) * 8 + (tile & 7)) * 8 + gq) * 4096;
}

#define QKN 0
constexpr int QK_RT = QKN ? 32768 : 4096;
__device__ __forceinline__ int qk_addr(int mat, int seg, int h, int cl, int ra) {
  return (int)OFF_QK + ((mat * 64 + seg) * 32 + h) * 8192;
}

__device__ __forceinline__ u64 hs_post(__amdgpu_buffer_rsrc_t rs, int w) {
  const u64 n = __builtin_amdgcn_s_memrealtime();
  if (w == 0) __builtin_amdgcn_raw_buffer_store_b64(v2u{(unsigned)n, (unsigned)(n >> 32)}, rs, olane() == 0 ? 0 : OOB,
                                                    OFF_REQ + (int)blockIdx.x * 8, AUX_SC1);
  return n;
}
__device__ __forceinline__ void hs_wait(__amdgpu_buffer_rsrc_t rs, char* ws, u64 n, int cnt) {
  _Pragma("clang loop unroll(disable)") for (unsigned spins = 0; spins < (kSpinCap << 2); ++spins) {
    int z = 0;
    asm volatile("" : "+v"(z));
    const v2u v = __builtin_amdgcn_raw_buffer_load_b64(rs, z, OFF_ACK + (int)blockIdx.x * 8, AUX_SC1);
    if (__all(v[0] == (unsigned)n && v[1] == (unsigned)(n >> 32))) break;
    __builtin_amdgcn_s_sleep(8);
  }
  if (olane() == 0) __hip_atomic_fetch_add((gu32*)(ws + cnt), 1u, RLX_AGENT);
}
template <int SLP>
__device__ __forceinline__ void hs_lead(__amdgpu_buffer_rsrc_t rs, bool fill, int cnt, unsigned want) {
  const int l16 = olane() * 16;
  if (fill) {
    for (int o = 0; o < (int)ARM_HOST; o += 1024) __builtin_amdgcn_raw_buffer_store_b128(v4u{~0u, ~0u, ~0u, ~0u}, rs, l16, o, AUX_SC1);
    VMCNT(0);
  }
  _Pragma("clang loop unroll(disable)") for (unsigned spins = 0; spins < (kSpinCap << 2); ++spins) {
    int va = l16;
    asm volatile("" : "+v"(va));
    const v4u a = __builtin_amdgcn_raw_buffer_load_b128(rs, va, OFF_REQ, AUX_SC1);
    const v4u b = __builtin_amdgcn_raw_buffer_load_b128(rs, va + 1024, OFF_REQ, AUX_SC1);
    const unsigned n = __builtin_amdgcn_raw_buffer_load_b32(rs, va & 0, cnt, AUX_SC1);
    if (__all(n + 1u >= want)) break;
    __builtin_amdgcn_raw_buffer_store_b128(a, rs, va, OFF_ACK, AUX_SC1);
    __builtin_amdgcn_raw_buffer_store_b128(b, rs, va + 1024, OFF_ACK, AUX_SC1);
    __builtin_amdgcn_s_sleep(SLP);
  }
}

#define TN(k, d) (d)
#define EVDECL
#define EV(k)
#define EVDUMP(s)
#define EVCLK(s, c)
#define EVOUT
#define PX 2
#define VO_A \
  "s_branch .Lgo_%=\n" \
  ".Lr_%=:\n" \
  "s_sub_u32 %[rt], %[rt], 1\n" \
  "s_cbranch_scc1 .Lrb_%=\n" \
  "s_sleep 2\n" \
  "buffer_load_dwordx4 v[192:195], v248, %[rs], %[o0] offen sc1\n" \
  "buffer_load_dwordx4 v[196:199], v248, %[rs], %[o0] offen offset:1024 sc1\n" \
  "v_mov_b64 v[224:225], 0\n" \
  "v_mov_b64 v[226:227], 0\n" \
  "v_mov_b64 v[228:229], 0\n" \
  "v_mov_b64 v[230:231], 0\n" \
  "s_nop 1\n" \
  "s_branch .Lw_%=\n" \
  ".Lgo_%=:\n" \
  "v_add_u32 v252, 0x20000, v248\n" \
  "ds_read_b128 v[200:203], v248 offset:32768\n" \
  "ds_read_b128 v[204:207], v248 offset:33792\n" \
  "ds_read_b128 v[208:211], v248 offset:49152\n" \
  "ds_read_b128 v[212:215], v248 offset:50176\n" \
  "ds_read_b128 v[216:219], v252 offset:0\n" \
  "ds_read_b128 v[220:223], v252 offset:1024\n" \
  "ds_read_b128 v[240:243], v252 offset:16384\n" \
  "ds_read_b128 v[244:247], v252 offset:17408\n" \
  "v_mov_b64 v[224:225], 0\n" \
  "v_mov_b64 v[226:227], 0\n" \
  "v_mov_b64 v[228:229], 0\n" \
  "v_mov_b64 v[230:231], 0\n" \
  "v_xor_b32 v250, 0x800, v248\n" \
  "v_xor_b32 v251, 0x1000, v248\n" \
  "s_mov_b32 %[rt], 0x40000\n" \
  "s_setprio 2\n" \
  "s_waitcnt lgkmcnt(0)\n" \
  "s_barrier\n" \
  "buffer_load_dwordx4 v[192:195], v248, %[rs], %[o0] offen sc1\n" \
  "buffer_load_dwordx4 v[196:199], v248, %[rs], %[o0] offen offset:1024 sc1\n" \
  ".Lw_%=:\n" \
  "s_waitcnt vmcnt(1)\n" \
  "ds_write_b128 v248, v[192:195]\n" \
  "v_smfmac_f32_16x16x64_bf16 v[224:227], v[192:195], v[200:207], v249\n" \
  "v_max3_u32 v252, v192, v193, v194\n" \
  "s_waitcnt vmcnt(0)\n" \
  "ds_write_b128 v248, v[196:199] offset:1024\n" \
  "v_smfmac_f32_16x16x64_bf16 v[228:231], v[192:195], v[208:215], v249\n" \
  "v_max3_u32 v252, v252, v195, v196\n" \
  "v_smfmac_f32_16x16x64_bf16 v[224:227], v[196:199], v[216:223], v249\n" \
  "v_max3_u32 v252, v252, v197, v198\n" \
  "v_max_u32 v252, v252, v199\n" \
  "v_smfmac_f32_16x16x64_bf16 v[228:231], v[196:199], v[240:247], v249\n" \
  "v_cmp_eq_u32 vcc, -1, v252\n" \
  "v_xor_b32 v252, 0x1800, v248\n" \
  "s_cbranch_vccnz .Lr_%=\n" \
  ".Lrb_%=:\n" \
  "s_waitcnt lgkmcnt(0)\n" \
  "s_barrier\n" \
  "ds_read_b128 v[200:203], v250 offset:0\n" \
  "ds_read_b128 v[204:207], v250 offset:1024\n" \
  "ds_read_b128 v[208:211], v251 offset:0\n" \
  "ds_read_b128 v[212:215], v251 offset:1024\n" \
  "ds_read_b128 v[216:219], v252 offset:0\n" \
  "ds_read_b128 v[220:223], v252 offset:1024\n" \
  "s_waitcnt lgkmcnt(5)\n" \
  "v_smfmac_f32_16x16x64_bf16 v[224:227], v[200:203], v[0:7], v249\n" \
  "v_smfmac_f32_16x16x64_bf16 v[228:231], v[200:203], v[48:55], v249\n" \
  "s_waitcnt lgkmcnt(4)\n" \
  "v_smfmac_f32_16x16x64_bf16 v[224:227], v[204:207], v[8:15], v249\n" \
  "v_smfmac_f32_16x16x64_bf16 v[228:231], v[204:207], v[56:63], v249\n" \
  "s_waitcnt lgkmcnt(3)\n" \
  "v_smfmac_f32_16x16x64_bf16 v[224:227], v[208:211], v[16:23], v249\n" \
  "v_smfmac_f32_16x16x64_bf16 v[228:231], v[208:211], v[64:71], v249\n" \
  "s_waitcnt lgkmcnt(2)\n" \
  "v_smfmac_f32_16x16x64_bf16 v[224:227], v[212:215], v[24:31], v249\n" \
  "s_waitcnt lgkmcnt(1)\n" \
  "v_smfmac_f32_16x16x64_bf16 v[224:227], v[216:219], v[32:39], v249\n" \
  "s_waitcnt lgkmcnt(0)\n" \
  "v_smfmac_f32_16x16x64_bf16 v[224:227], v[220:223], v[40:47], v249\n" \
  "v_smfmac_f32_16x16x64_bf16 v[228:231], v[212:215], v[72:79], v249\n" \
  "v_smfmac_f32_16x16x64_bf16 v[228:231], v[216:219], v[80:87], v249\n" \
  "s_nop 0\n" \
  "v_add_f32 v224, v224, v226\n" \
  "v_smfmac_f32_16x16x64_bf16 v[228:231], v[220:223], v[88:95], v249\n" \
  "v_add_f32 v225, v225, v227\n" \
  "ds_write_b64 v248, v[224:225] offset:16384\n" \
  "s_nop 6\n" \
  "v_add_f32 v228, v228, v230\n" \
  "v_add_f32 v229, v229, v231\n" \
  "ds_write_b64 v248, v[228:229] offset:16392\n" \
  "s_setprio 0\n" \
  "ds_read_b128 v[240:243], v254 offset:0\n" \
  "ds_read_b128 v[244:247], v254 offset:1024\n" \
  "s_waitcnt lgkmcnt(2)\n" \
  "s_barrier\n" \
  "s_nop 1\n" \
  "ds_read_b128 v[224:227], v254 offset:16384\n" \
  "ds_read_b128 v[228:231], v254 offset:17408\n" \
  "v_mov_b64 v[192:193], 0\n" \
  "v_mov_b64 v[194:195], 0\n" \
  "v_mov_b64 v[196:197], 0\n" \
  "v_mov_b64 v[198:199], 0\n" \
  "v_mov_b64 v[200:201], 0\n" \
  "v_mov_b64 v[202:203], 0\n" \
  "v_mov_b64 v[204:205], 0\n" \
  "v_mov_b64 v[206:207], 0\n" \
  "s_setprio 2\n" \
  "s_barrier\n" \
  "ds_read_b64_tr_b16 v[208:209], v238 offset:16384\n" \
  "ds_read_b64_tr_b16 v[210:211], v238 offset:16576\n" \
  "ds_read_b64_tr_b16 v[212:213], v238 offset:17152\n" \
  "ds_read_b64_tr_b16 v[214:215], v238 offset:17344\n" \
  "ds_read_b64_tr_b16 v[216:217], v238 offset:17920\n" \
  "ds_read_b64_tr_b16 v[218:219], v238 offset:18112\n" \
  "ds_read_b64_tr_b16 v[220:221], v238 offset:18688\n" \
  "ds_read_b64_tr_b16 v[222:223], v238 offset:18880\n" \
  "s_waitcnt lgkmcnt(6)\n" \
  "v_smfmac_f32_16x16x64_bf16 v[200:203], v[208:211], v[240:247], v249 abid:1\n" \
  "ds_read_b128 v[240:243], v254 offset:32768\n" \
  "v_smfmac_f32_16x16x64_bf16 v[192:195], v[208:211], v[160:167], v249 abid:1\n" \
  "ds_read_b128 v[244:247], v254 offset:33792\n" \
  "v_smfmac_f32_16x16x64_bf16 v[196:199], v[208:211], v[96:103], v249 abid:1\n" \
  "v_smfmac_f32_16x16x64_bf16 v[204:207], v[208:211], v[128:135], v249 abid:1\n" \
  "s_waitcnt lgkmcnt(6)\n" \
  "v_smfmac_f32_16x16x64_bf16 v[192:195], v[212:215], v[168:175], v249 abid:1\n" \
  "v_smfmac_f32_16x16x64_bf16 v[200:203], v[212:215], v[224:231], v249 abid:1\n" \
  "ds_read_b128 v[224:227], v254 offset:49152\n" \
  "v_smfmac_f32_16x16x64_bf16 v[196:199], v[212:215], v[104:111], v249 abid:1\n" \
  "ds_read_b128 v[228:231], v254 offset:50176\n" \
  "v_smfmac_f32_16x16x64_bf16 v[204:207], v[212:215], v[136:143], v249 abid:1\n" \
  "s_waitcnt lgkmcnt(6)\n" \
  "v_smfmac_f32_16x16x64_bf16 v[192:195], v[216:219], v[176:183], v249 abid:1\n" \
  "v_smfmac_f32_16x16x64_bf16 v[196:199], v[216:219], v[112:119], v249 abid:1\n" \
  "s_waitcnt lgkmcnt(4)\n" \
  "v_smfmac_f32_16x16x64_bf16 v[192:195], v[220:223], v[184:191], v249 abid:1\n" \
  "v_smfmac_f32_16x16x64_bf16 v[196:199], v[220:223], v[120:127], v249 abid:1\n" \
  "v_smfmac_f32_16x16x64_bf16 v[204:207], v[216:219], v[144:151], v249 abid:1\n" \
  "s_nop 0\n" \
  "v_cvt_pk_bf16_f32 v192, v192, v193\n" \
  "v_smfmac_f32_16x16x64_bf16 v[204:207], v[220:223], v[152:159], v249 abid:1\n" \
  "v_cvt_pk_bf16_f32 v193, v194, v195\n" \
  "s_waitcnt lgkmcnt(2)\n" \
  "v_smfmac_f32_16x16x64_bf16 v[200:203], v[216:219], v[240:247], v249 abid:1\n" \
  "v_cvt_pk_bf16_f32 v194, v196, v197\n" \
  "s_waitcnt lgkmcnt(0)\n" \
  "v_smfmac_f32_16x16x64_bf16 v[200:203], v[220:223], v[224:231], v249 abid:1\n" \
  "v_cvt_pk_bf16_f32 v195, v198, v199\n" \
  "v_cvt_pk_bf16_f32 v204, v204, v205\n" \
  "v_permlane16_swap_b32 v192, v194\n" \
  "v_permlane16_swap_b32 v193, v195\n" \
  "buffer_store_dwordx4 v[192:195], v236, %[rs], %[vs] offen sc1\n" \
  "v_cvt_pk_bf16_f32 v205, v206, v207\n" \
  "s_nop 2\n" \
  "v_cvt_pk_bf16_f32 v203, v202, v203\n" \
  "v_cvt_pk_bf16_f32 v202, v200, v201\n" \
  "s_nop 0\n" \
  "v_permlane16_swap_b32 v203, v205\n" \
  "v_permlane16_swap_b32 v202, v204\n" \
  "buffer_store_dwordx4 v[202:205], v237, %[rs], %[vs] offen sc1\n" \
  "s_setprio 0\n" \
  "s_nop 1\n"
#define VO_B \
  "s_branch .Lgo_%=\n" \
  ".Lr_%=:\n" \
  "s_sub_u32 %[rt], %[rt], 1\n" \
  "s_cbranch_scc1 .Lrb_%=\n" \
  "s_sleep 2\n" \
  "buffer_load_dwordx4 v[192:195], v248, %[rs], %[o0] offen sc1\n" \
  "buffer_load_dwordx4 v[196:199], v248, %[rs], %[o0] offen offset:1024 sc1\n" \
  "v_mov_b64 v[224:225], 0\n" \
  "v_mov_b64 v[226:227], 0\n" \
  "v_mov_b64 v[228:229], 0\n" \
  "v_mov_b64 v[230:231], 0\n" \
  "s_nop 1\n" \
  "s_branch .Lw_%=\n" \
  ".Lgo_%=:\n" \
  "v_add_u32 v252, 0x20000, v248\n" \
  "ds_read_b128 v[200:203], v248 offset:32768\n" \
  "ds_read_b128 v[204:207], v248 offset:33792\n" \
  "ds_read_b128 v[208:211], v248 offset:49152\n" \
  "ds_read_b128 v[212:215], v248 offset:50176\n" \
  "ds_read_b128 v[216:219], v252 offset:0\n" \
  "ds_read_b128 v[220:223], v252 offset:1024\n" \
  "ds_read_b128 v[240:243], v252 offset:16384\n" \
  "ds_read_b128 v[244:247], v252 offset:17408\n" \
  "v_mov_b64 v[224:225], 0\n" \
  "v_mov_b64 v[226:227], 0\n" \
  "v_mov_b64 v[228:229], 0\n" \
  "v_mov_b64 v[230:231], 0\n" \
  "v_xor_b32 v250, 0x800, v248\n" \
  "v_xor_b32 v251, 0x1000, v248\n" \
  "s_mov_b32 %[rt], 0x40000\n" \
  "s_setprio 1\n" \
  "s_waitcnt lgkmcnt(0)\n" \
  "s_cmp_eq_u32 %[tw], 0\n" \
  "s_cbranch_scc1 .Ltq_%=\n" \
  ".Ltl_%=:\n" \
  "s_memtime vcc\n" \
  "s_waitcnt lgkmcnt(0)\n" \
  "s_sub_i32 %[cnt], %[tg], vcc_lo\n" \
  "s_cmp_lt_i32 %[cnt], 260\n" \
  "s_cbranch_scc1 .Ltf_%=\n" \
  "s_sleep 1\n" \
  "s_branch .Ltl_%=\n" \
  ".Ltf_%=:\n" \
  "s_max_i32 %[cnt], %[cnt], 0\n" \
  ".Ltg_%=:\n" \
  "s_sub_u32 %[cnt], %[cnt], 28\n" \
  "s_cbranch_scc0 .Ltg_%=\n" \
  ".Ltq_%=:\n" \
  "s_barrier\n" \
  "buffer_load_dwordx4 v[192:195], v248, %[rs], %[o0] offen sc1\n" \
  "buffer_load_dwordx4 v[196:199], v248, %[rs], %[o0] offen offset:1024 sc1\n" \
  ".Lw_%=:\n" \
  "s_waitcnt vmcnt(1)\n" \
  "ds_write_b128 v248, v[192:195]\n" \
  "v_smfmac_f32_16x16x64_bf16 v[224:227], v[192:195], v[200:207], v249\n" \
  "v_max3_u32 v252, v192, v193, v194\n" \
  "s_waitcnt vmcnt(0)\n" \
  "ds_write_b128 v248, v[196:199] offset:1024\n" \
  "v_smfmac_f32_16x16x64_bf16 v[228:231], v[192:195], v[208:215], v249\n" \
  "v_max3_u32 v252, v252, v195, v196\n" \
  "v_smfmac_f32_16x16x64_bf16 v[224:227], v[196:199], v[216:223], v249\n" \
  "v_max3_u32 v252, v252, v197, v198\n" \
  "v_max_u32 v252, v252, v199\n" \
  "v_smfmac_f32_16x16x64_bf16 v[228:231], v[196:199], v[240:247], v249\n" \
  "v_cmp_eq_u32 vcc, -1, v252\n" \
  "v_xor_b32 v252, 0x1800, v248\n" \
  "s_cbranch_vccnz .Lr_%=\n" \
  ".Lrb_%=:\n" \
  "s_waitcnt lgkmcnt(0)\n" \
  "s_barrier\n" \
  "s_memtime vcc\n" \
  "s_setprio 3\n" \
  "v_mov_b64 v[192:193], 0\n" \
  "v_mov_b64 v[194:195], 0\n" \
  "v_mov_b64 v[196:197], 0\n" \
  "v_mov_b64 v[198:199], 0\n" \
  "s_waitcnt lgkmcnt(0)\n" \
  "s_sub_u32 %[cnt], vcc_lo, %[ckm]\n" \
  "s_mul_hi_u32 %[cnt], %[cnt], %[inv]\n" \
  "s_add_u32 %[cnt], %[cnt], %[r16]\n" \
  "s_and_b32 %[cnt], %[cnt], -2\n" \
  "v_mov_b32 v233, %[cnt]\n" \
  "buffer_store_dword v233, v239, %[rs], %[sst] offen sc1\n" \
  "s_nop 5\n" \
  "ds_read_b128 v[200:203], v250 offset:0\n" \
  "ds_read_b128 v[204:207], v250 offset:1024\n" \
  "ds_read_b128 v[208:211], v251 offset:0\n" \
  "ds_read_b128 v[212:215], v251 offset:1024\n" \
  "ds_read_b128 v[216:219], v252 offset:0\n" \
  "ds_read_b128 v[220:223], v252 offset:1024\n" \
  "s_setprio 1\n" \
  "s_waitcnt lgkmcnt(5)\n" \
  "v_smfmac_f32_16x16x64_bf16 v[224:227], v[200:203], v[0:7], v249\n" \
  "v_smfmac_f32_16x16x64_bf16 v[228:231], v[200:203], v[48:55], v249\n" \
  "s_waitcnt lgkmcnt(4)\n" \
  "v_smfmac_f32_16x16x64_bf16 v[224:227], v[204:207], v[8:15], v249\n" \
  "v_smfmac_f32_16x16x64_bf16 v[228:231], v[204:207], v[56:63], v249\n" \
  "s_waitcnt lgkmcnt(3)\n" \
  "v_smfmac_f32_16x16x64_bf16 v[224:227], v[208:211], v[16:23], v249\n" \
  "ds_read_b128 v[240:243], v254 offset:0\n" \
  "v_smfmac_f32_16x16x64_bf16 v[228:231], v[208:211], v[64:71], v249\n" \
  "ds_read_b128 v[244:247], v254 offset:1024\n" \
  "s_waitcnt lgkmcnt(4)\n" \
  "v_smfmac_f32_16x16x64_bf16 v[224:227], v[212:215], v[24:31], v249\n" \
  "s_waitcnt lgkmcnt(3)\n" \
  "v_smfmac_f32_16x16x64_bf16 v[224:227], v[216:219], v[32:39], v249\n" \
  "s_waitcnt lgkmcnt(2)\n" \
  "v_smfmac_f32_16x16x64_bf16 v[224:227], v[220:223], v[40:47], v249\n" \
  "v_smfmac_f32_16x16x64_bf16 v[228:231], v[212:215], v[72:79], v249\n" \
  "s_barrier\n" \
  "ds_read_b128 v[232:235], v248 offset:24576\n" \
  "v_smfmac_f32_16x16x64_bf16 v[228:231], v[216:219], v[80:87], v249\n" \
  "s_nop 0\n" \
  "v_add_f32 v224, v224, v226\n" \
  "v_smfmac_f32_16x16x64_bf16 v[228:231], v[220:223], v[88:95], v249\n" \
  "v_add_f32 v225, v225, v227\n" \
  "s_waitcnt lgkmcnt(0)\n" \
  "v_add_f32 v232, v232, v224\n" \
  "v_add_f32 v233, v233, v225\n" \
  "v_cvt_pk_bf16_f32 v232, v232, v233\n" \
  "ds_write_b32 v253, v232\n" \
  "s_nop 2\n" \
  "v_add_f32 v234, v234, v228\n" \
  "v_add_f32 v235, v235, v229\n" \
  "v_add_f32 v234, v234, v230\n" \
  "v_add_f32 v235, v235, v231\n" \
  "v_cvt_pk_bf16_f32 v234, v234, v235\n" \
  "ds_write_b32 v253, v234 offset:1536\n" \
  "v_mov_b64 v[200:201], 0\n" \
  "v_mov_b64 v[202:203], 0\n" \
  "v_mov_b64 v[204:205], 0\n" \
  "v_mov_b64 v[206:207], 0\n" \
  "s_waitcnt lgkmcnt(0)\n" \
  "s_barrier\n" \
  "s_nop 7\n" \
  "ds_read_b64_tr_b16 v[208:209], v238 offset:16384\n" \
  "ds_read_b64_tr_b16 v[210:211], v238 offset:16576\n" \
  "ds_read_b128 v[224:227], v254 offset:16384\n" \
  "ds_read_b128 v[228:231], v254 offset:17408\n" \
  "ds_read_b64_tr_b16 v[212:213], v238 offset:17152\n" \
  "ds_read_b64_tr_b16 v[214:215], v238 offset:17344\n" \
  "ds_read_b64_tr_b16 v[216:217], v238 offset:17920\n" \
  "ds_read_b64_tr_b16 v[218:219], v238 offset:18112\n" \
  "ds_read_b64_tr_b16 v[220:221], v238 offset:18688\n" \
  "ds_read_b64_tr_b16 v[222:223], v238 offset:18880\n" \
  "s_waitcnt lgkmcnt(8)\n" \
  "v_smfmac_f32_16x16x64_bf16 v[200:203], v[208:211], v[240:247], v249 abid:1\n" \
  "ds_read_b128 v[240:243], v254 offset:32768\n" \
  "v_smfmac_f32_16x16x64_bf16 v[192:195], v[208:211], v[160:167], v249 abid:1\n" \
  "ds_read_b128 v[244:247], v254 offset:33792\n" \
  "v_smfmac_f32_16x16x64_bf16 v[196:199], v[208:211], v[96:103], v249 abid:1\n" \
  "v_smfmac_f32_16x16x64_bf16 v[204:207], v[208:211], v[128:135], v249 abid:1\n" \
  "s_waitcnt lgkmcnt(6)\n" \
  "v_smfmac_f32_16x16x64_bf16 v[192:195], v[212:215], v[168:175], v249 abid:1\n" \
  "v_smfmac_f32_16x16x64_bf16 v[200:203], v[212:215], v[224:231], v249 abid:1\n" \
  "ds_read_b128 v[224:227], v254 offset:49152\n" \
  "v_smfmac_f32_16x16x64_bf16 v[196:199], v[212:215], v[104:111], v249 abid:1\n" \
  "ds_read_b128 v[228:231], v254 offset:50176\n" \
  "v_smfmac_f32_16x16x64_bf16 v[204:207], v[212:215], v[136:143], v249 abid:1\n" \
  "s_waitcnt lgkmcnt(6)\n" \
  "v_smfmac_f32_16x16x64_bf16 v[192:195], v[216:219], v[176:183], v249 abid:1\n" \
  "v_smfmac_f32_16x16x64_bf16 v[196:199], v[216:219], v[112:119], v249 abid:1\n" \
  "s_waitcnt lgkmcnt(4)\n" \
  "v_smfmac_f32_16x16x64_bf16 v[192:195], v[220:223], v[184:191], v249 abid:1\n" \
  "v_smfmac_f32_16x16x64_bf16 v[196:199], v[220:223], v[120:127], v249 abid:1\n" \
  "v_smfmac_f32_16x16x64_bf16 v[204:207], v[216:219], v[144:151], v249 abid:1\n" \
  "s_nop 0\n" \
  "v_cvt_pk_bf16_f32 v192, v192, v193\n" \
  "v_smfmac_f32_16x16x64_bf16 v[204:207], v[220:223], v[152:159], v249 abid:1\n" \
  "v_cvt_pk_bf16_f32 v193, v194, v195\n" \
  "s_waitcnt lgkmcnt(2)\n" \
  "v_smfmac_f32_16x16x64_bf16 v[200:203], v[216:219], v[240:247], v249 abid:1\n" \
  "v_cvt_pk_bf16_f32 v194, v196, v197\n" \
  "s_waitcnt lgkmcnt(0)\n" \
  "v_smfmac_f32_16x16x64_bf16 v[200:203], v[220:223], v[224:231], v249 abid:1\n" \
  "v_cvt_pk_bf16_f32 v195, v198, v199\n" \
  "v_cvt_pk_bf16_f32 v204, v204, v205\n" \
  "v_permlane16_swap_b32 v192, v194\n" \
  "v_permlane16_swap_b32 v193, v195\n" \
  "buffer_store_dwordx4 v[192:195], v236, %[rs], %[vs] offen sc1\n" \
  "v_cvt_pk_bf16_f32 v205, v206, v207\n" \
  "s_nop 2\n" \
  "v_cvt_pk_bf16_f32 v203, v202, v203\n" \
  "v_cvt_pk_bf16_f32 v202, v200, v201\n" \
  "s_nop 0\n" \
  "v_permlane16_swap_b32 v203, v205\n" \
  "v_permlane16_swap_b32 v202, v204\n" \
  "buffer_store_dwordx4 v[202:205], v237, %[rs], %[vs] offen sc1\n" \
  "s_setprio 0\n" \
  "s_nop 1\n"
#define QK_A0 \
  "s_branch .Lgo_%=\n" \
  ".Lr_%=:\n" \
  "s_sub_u32 %[rt], %[rt], 1\n" \
  "s_cbranch_scc1 .Lrb_%=\n" \
  "s_sleep 2\n" \
  "buffer_load_dwordx4 v[192:195], v248, %[rs], %[o0] offen sc1\n" \
  "buffer_load_dwordx4 v[196:199], v248, %[rs], %[o0] offen offset:1024 sc1\n" \
  "v_mov_b64 v[224:225], 0\n" \
  "v_mov_b64 v[226:227], 0\n" \
  "v_mov_b64 v[228:229], 0\n" \
  "v_mov_b64 v[230:231], 0\n" \
  "v_mov_b64 v[232:233], 0\n" \
  "v_mov_b64 v[234:235], 0\n" \
  "v_mov_b64 v[236:237], 0\n" \
  "v_mov_b64 v[238:239], 0\n" \
  "s_nop 1\n" \
  "s_branch .Lw_%=\n" \
  ".Lw1_%=:\n" \
  "v_max_f32 v200, v192, v193\n" \
  "s_nop 1\n" \
  "v_max_f32_dpp v200, v200, v200 row_ror:8 row_mask:0xf bank_mask:0xf\n" \
  "s_nop 1\n" \
  "v_max_f32_dpp v200, v200, v200 row_ror:4 row_mask:0xf bank_mask:0xf\n" \
  "s_nop 1\n" \
  "v_max_f32_dpp v200, v200, v200 row_ror:2 row_mask:0xf bank_mask:0xf\n" \
  "s_nop 1\n" \
  "v_max_f32_dpp v200, v200, v200 row_ror:1 row_mask:0xf bank_mask:0xf\n" \
  "v_sub_f32 v194, v192, v200\n" \
  "v_sub_f32 v195, v193, v200\n" \
  "v_exp_f32 v194, v194\n" \
  "v_exp_f32 v195, v195\n" \
  "s_mov_b32 %[rt], -1.0\n" \
  "s_nop 0\n" \
  "v_cvt_pk_bf16_f32 v196, v194, v195\n" \
  "v_and_b32 v196, 0x7fffffff, v196\n" \
  "s_branch .Lw1b_%=\n" \
  ".Lw2_%=:\n" \
  "s_cmp_lt_i32 %[rt], 0\n" \
  "s_cbranch_scc1 .Lw3_%=\n" \
  "v_mov_b32 v200, 0\n" \
  ".Lw3_%=:\n" \
  "s_nop 1\n" \
  "v_max_f32_dpp v197, v192, v200 quad_perm:[0,0,0,0] row_mask:0xf bank_mask:0xf\n" \
  "s_nop 1\n" \
  "v_max_f32_dpp v197, v192, v197 quad_perm:[1,1,1,1] row_mask:0xf bank_mask:0xf\n" \
  "s_nop 1\n" \
  "v_max_f32_dpp v197, v197, v197 row_ror:4 row_mask:0xf bank_mask:0xf\n" \
  "s_nop 1\n" \
  "v_max_f32_dpp v197, v197, v197 row_ror:8 row_mask:0xf bank_mask:0xf\n" \
  "v_sub_f32 v194, v192, v197\n" \
  "v_sub_f32 v201, v200, v197\n" \
  "v_exp_f32 v194, v194\n" \
  "v_exp_f32 v201, v201\n" \
  "s_nop 1\n" \
  "v_mul_f32 v199, v201, v199\n" \
  "s_nop 1\n" \
  "v_add_f32_dpp v198, v194, v199 quad_perm:[0,0,0,0] row_mask:0xf bank_mask:0xf\n" \
  "s_nop 1\n" \
  "v_mov_b32_dpp v193, v194 quad_perm:[1,1,1,1] row_mask:0xf bank_mask:0xf\n" \
  "s_nop 1\n" \
  "v_add_f32 v198, v198, v193\n" \
  "s_nop 1\n" \
  "v_mov_b32_dpp v195, v194 quad_perm:[0,0,0,0] row_mask:0xf bank_mask:0xf\n" \
  "s_nop 1\n" \
  "v_add_f32_dpp v198, v198, v198 row_ror:4 row_mask:0xf bank_mask:0xf\n" \
  "s_nop 1\n" \
  "v_add_f32_dpp v198, v198, v198 row_ror:8 row_mask:0xf bank_mask:0xf\n" \
  "s_nop 1\n" \
  "v_rcp_f32 v198, v198\n" \
  "s_nop 1\n" \
  "v_mul_f32 v195, v195, v198\n" \
  "s_nop 1\n" \
  "v_mul_f32 v193, v193, v198\n" \
  "s_nop 1\n" \
  "v_cvt_pk_bf16_f32 v196, v195, v193\n" \
  "s_nop 1\n" \
  "v_and_b32 v196, 0x7fffffff, v196\n" \
  "s_nop 1\n" \
  "v_mul_f32 v198, v201, v198\n" \
  "s_nop 1\n" \
  "buffer_store_dword v196, v251, %[rs], %[so] offen sc1\n" \
  "s_nop 1\n" \
  "v_cvt_pk_bf16_f32 v197, v198, v198\n" \
  "s_nop 1\n" \
  "v_and_b32 v197, v252, v197\n" \
  "s_nop 1\n" \
  "buffer_store_dword v197, v251, %[rs], %[so] offen offset:64 sc1\n" \
  "s_branch .Lw2b_%=\n" \
  ".Lgo_%=:\n" \
  "v_add_u32 v252, 0x20000, v248\n" \
  "ds_read_b128 v[200:203], v248 offset:32768\n" \
  "ds_read_b128 v[204:207], v248 offset:33792\n" \
  "ds_read_b128 v[208:211], v248 offset:49152\n" \
  "ds_read_b128 v[212:215], v248 offset:50176\n" \
  "ds_read_b128 v[216:219], v252 offset:0\n" \
  "ds_read_b128 v[220:223], v252 offset:1024\n" \
  "ds_read_b128 v[240:243], v252 offset:16384\n" \
  "ds_read_b128 v[244:247], v252 offset:17408\n" \
  "v_mov_b64 v[224:225], 0\n" \
  "v_mov_b64 v[226:227], 0\n" \
  "v_mov_b64 v[228:229], 0\n" \
  "v_mov_b64 v[230:231], 0\n" \
  "v_mov_b64 v[232:233], 0\n" \
  "v_mov_b64 v[234:235], 0\n" \
  "v_mov_b64 v[236:237], 0\n" \
  "v_mov_b64 v[238:239], 0\n" \
  "v_xor_b32 v250, 0x800, v248\n" \
  "v_xor_b32 v251, 0x1000, v248\n" \
  "s_mov_b32 %[rt], 0x40000\n" \
  "s_setprio 2\n" \
  "s_waitcnt lgkmcnt(0)\n" \
  "s_barrier\n" \
  "buffer_load_dwordx4 v[192:195], v248, %[rs], %[o0] offen sc1\n" \
  "buffer_load_dwordx4 v[196:199], v248, %[rs], %[o0] offen offset:1024 sc1\n" \
  ".Lw_%=:\n" \
  "s_waitcnt vmcnt(1)\n" \
  "ds_write_b128 v248, v[192:195]\n" \
  "v_smfmac_f32_16x16x64_bf16 v[224:227], v[192:195], v[200:207], v249\n" \
  "v_max3_u32 v252, v192, v193, v194\n" \
  "s_waitcnt vmcnt(0)\n" \
  "ds_write_b128 v248, v[196:199] offset:1024\n" \
  "v_smfmac_f32_16x16x64_bf16 v[228:231], v[192:195], v[208:215], v249\n" \
  "v_max3_u32 v252, v252, v195, v196\n" \
  "v_smfmac_f32_16x16x64_bf16 v[224:227], v[196:199], v[216:223], v249\n" \
  "v_max3_u32 v252, v252, v197, v198\n" \
  "v_smfmac_f32_16x16x64_bf16 v[228:231], v[196:199], v[240:247], v249\n" \
  "v_max_u32 v252, v252, v199\n" \
  "v_smfmac_f32_16x16x64_bf16 v[232:235], v[192:195], v[96:103], v249\n" \
  "v_cmp_eq_u32 vcc, -1, v252\n" \
  "v_smfmac_f32_16x16x64_bf16 v[236:239], v[192:195], v[144:151], v249\n" \
  "v_xor_b32 v252, 0x1800, v248\n" \
  "s_cbranch_vccnz .Lr_%=\n" \
  ".Lrb_%=:\n" \
  "v_smfmac_f32_16x16x64_bf16 v[232:235], v[196:199], v[104:111], v249\n" \
  "v_smfmac_f32_16x16x64_bf16 v[236:239], v[196:199], v[152:159], v249\n" \
  "s_waitcnt lgkmcnt(0)\n" \
  "s_barrier\n" \
  "ds_read_b128 v[200:203], v250 offset:0\n" \
  "ds_read_b128 v[204:207], v250 offset:1024\n" \
  "ds_read_b128 v[208:211], v251 offset:0\n" \
  "ds_read_b128 v[212:215], v251 offset:1024\n" \
  "ds_read_b128 v[216:219], v252 offset:0\n" \
  "ds_read_b128 v[220:223], v252 offset:1024\n" \
  "s_waitcnt lgkmcnt(5)\n" \
  "v_smfmac_f32_16x16x64_bf16 v[224:227], v[200:203], v[0:7], v249\n" \
  "v_smfmac_f32_16x16x64_bf16 v[228:231], v[200:203], v[48:55], v249\n" \
  "s_waitcnt lgkmcnt(4)\n" \
  "v_smfmac_f32_16x16x64_bf16 v[224:227], v[204:207], v[8:15], v249\n" \
  "v_smfmac_f32_16x16x64_bf16 v[228:231], v[204:207], v[56:63], v249\n" \
  "s_waitcnt lgkmcnt(3)\n" \
  "v_smfmac_f32_16x16x64_bf16 v[224:227], v[208:211], v[16:23], v249\n" \
  "v_smfmac_f32_16x16x64_bf16 v[228:231], v[208:211], v[64:71], v249\n" \
  "s_waitcnt lgkmcnt(2)\n" \
  "v_smfmac_f32_16x16x64_bf16 v[224:227], v[212:215], v[24:31], v249\n" \
  "s_waitcnt lgkmcnt(1)\n" \
  "v_smfmac_f32_16x16x64_bf16 v[224:227], v[216:219], v[32:39], v249\n" \
  "s_waitcnt lgkmcnt(0)\n" \
  "v_smfmac_f32_16x16x64_bf16 v[224:227], v[220:223], v[40:47], v249\n" \
  "v_smfmac_f32_16x16x64_bf16 v[228:231], v[212:215], v[72:79], v249\n" \
  "v_smfmac_f32_16x16x64_bf16 v[228:231], v[216:219], v[80:87], v249\n" \
  "s_nop 0\n" \
  "v_add_f32 v224, v224, v226\n" \
  "v_smfmac_f32_16x16x64_bf16 v[228:231], v[220:223], v[88:95], v249\n" \
  "v_add_f32 v225, v225, v227\n" \
  "ds_write_b64 v248, v[224:225] offset:8192\n" \
  "s_nop 6\n" \
  "v_add_f32 v228, v228, v230\n" \
  "v_add_f32 v229, v229, v231\n" \
  "ds_write_b64 v248, v[228:229] offset:8200\n" \
  "s_setprio 0\n" \
  "ds_read_b128 v[240:243], v254 offset:0\n" \
  "ds_read_b128 v[244:247], v254 offset:1024\n" \
  "s_waitcnt lgkmcnt(2)\n" \
  "s_barrier\n" \
  "s_nop 1\n" \
  "ds_read_b128 v[192:195], v254 offset:16384\n" \
  "ds_read_b128 v[196:199], v254 offset:17408\n" \
  "ds_read_b128 v[224:227], v254 offset:32768\n" \
  "ds_read_b128 v[228:231], v254 offset:33792\n" \
  "s_waitcnt lgkmcnt(4)\n" \
  "v_smfmac_f32_16x16x64_bf16 v[232:235], v[200:203], v[240:247], v249\n" \
  "ds_read_b128 v[240:243], v254 offset:49152\n" \
  "ds_read_b128 v[244:247], v254 offset:50176\n" \
  "s_waitcnt lgkmcnt(4)\n" \
  "v_smfmac_f32_16x16x64_bf16 v[236:239], v[200:203], v[192:199], v249\n" \
  "v_smfmac_f32_16x16x64_bf16 v[232:235], v[204:207], v[112:119], v249\n" \
  "ds_read_b128 v[192:195], v248 offset:16384\n" \
  "v_smfmac_f32_16x16x64_bf16 v[236:239], v[204:207], v[160:167], v249\n" \
  "ds_read_b128 v[196:199], v248 offset:17408\n" \
  "v_smfmac_f32_16x16x64_bf16 v[232:235], v[212:215], v[120:127], v249\n" \
  "ds_read_b128 v[200:203], v248 offset:20480\n" \
  "v_smfmac_f32_16x16x64_bf16 v[236:239], v[212:215], v[168:175], v249\n" \
  "ds_read_b128 v[204:207], v248 offset:21504\n" \
  "s_waitcnt lgkmcnt(6)\n" \
  "v_smfmac_f32_16x16x64_bf16 v[232:235], v[208:211], v[224:231], v249\n" \
  "ds_read_b128 v[212:215], v248 offset:23552\n" \
  "s_waitcnt lgkmcnt(5)\n" \
  "v_smfmac_f32_16x16x64_bf16 v[236:239], v[208:211], v[240:247], v249\n" \
  "ds_read_b128 v[240:243], v248 offset:18432\n" \
  "v_smfmac_f32_16x16x64_bf16 v[232:235], v[216:219], v[128:135], v249\n" \
  "ds_read_b128 v[244:247], v248 offset:19456\n" \
  "v_smfmac_f32_16x16x64_bf16 v[232:235], v[220:223], v[136:143], v249\n" \
  "ds_read_b128 v[208:211], v248 offset:22528\n" \
  "v_smfmac_f32_16x16x64_bf16 v[236:239], v[216:219], v[176:183], v249\n" \
  "ds_read_b96 v[250:252], v248 offset:13312\n" \
  "v_smfmac_f32_16x16x64_bf16 v[236:239], v[220:223], v[184:191], v249\n" \
  "s_setprio 3\n" \
  "s_barrier\n" \
  "ds_read_b64_tr_b16 v[224:225], v253 offset:17408\n" \
  "ds_read_b64_tr_b16 v[226:227], v253 offset:17664\n" \
  "ds_read_b64_tr_b16 v[228:229], v253 offset:19456\n" \
  "ds_read_b64_tr_b16 v[230:231], v253 offset:19712\n" \
  "v_mov_b64 v[216:217], 0\n" \
  "v_mov_b64 v[218:219], 0\n" \
  "v_add_f32 v232, v232, v234\n" \
  "v_add_f32 v233, v233, v235\n" \
  "ds_write_b64 v248, v[232:233] offset:0\n" \
  "v_mov_b64 v[220:221], 0\n" \
  "v_mov_b64 v[222:223], 0\n" \
  "v_add_f32 v236, v236, v238\n" \
  "v_add_f32 v237, v237, v239\n" \
  "ds_write_b64 v248, v[236:237] offset:8\n" \
  "s_waitcnt lgkmcnt(4)\n" \
  "v_smfmac_f32_16x16x64_bf16 v[216:219], v[224:227], v[192:199], v249\n" \
  "v_smfmac_f32_16x16x64_bf16 v[220:223], v[224:227], v[200:207], v249\n" \
  "s_waitcnt lgkmcnt(2)\n" \
  "v_smfmac_f32_16x16x64_bf16 v[216:219], v[228:231], v[240:247], v249\n" \
  "v_smfmac_f32_16x16x64_bf16 v[220:223], v[228:231], v[208:215], v249\n" \
  "s_mov_b32 %[rt], 0x422c0000\n" \
  "s_waitcnt lgkmcnt(0)\n" \
  "s_barrier\n" \
  "s_nop 1\n" \
  "v_add_f32 v192, v216, v218\n" \
  "s_nop 2\n" \
  "v_add_f32 v193, v220, v222\n" \
  "v_exp_f32 v194, v192\n" \
  "v_exp_f32 v195, v193\n" \
  "v_max_f32 v197, |v192|, |v193|\n" \
  "v_cvt_pk_bf16_f32 v196, v194, v195\n" \
  "v_cmp_nge_f32 vcc, %[rt], v197\n" \
  "s_cbranch_vccnz .Lw1_%=\n" \
  ".Lw1b_%=:\n" \
  "buffer_store_dword v196, v250, %[rs], %[c0] offen sc1\n" \
  "buffer_store_dword v196, v250, %[rs], %[c1] offen sc1\n" \
  "buffer_store_dword v196, v250, %[rs], %[c2] offen sc1\n" \
  "buffer_store_dword v196, v250, %[rs], %[c3] offen sc1\n" \
  "v_add_f32 v199, v194, v195\n" \
  "v_xor_b32 v251, %[xr], v251\n" \
  "s_nop 0\n" \
  "v_add_f32_dpp v199, v199, v199 quad_perm:[1,0,3,2] row_mask:0xf bank_mask:0xf\n" \
  "s_nop 1\n" \
  "v_add_f32_dpp v199, v199, v199 quad_perm:[2,3,0,1] row_mask:0xf bank_mask:0xf\n" \
  "s_barrier\n" \
  "ds_read_b64_tr_b16 v[232:233], v253 offset:9216\n" \
  "ds_read_b64_tr_b16 v[234:235], v253 offset:9472\n" \
  "ds_read_b64_tr_b16 v[236:237], v253 offset:11264\n" \
  "ds_read_b64_tr_b16 v[238:239], v253 offset:11520\n" \
  "s_waitcnt lgkmcnt(2)\n" \
  "v_mfma_f32_16x16x32_bf16 v[216:219], v[224:227], v[232:235], 0\n" \
  "s_waitcnt lgkmcnt(0)\n" \
  "v_mfma_f32_16x16x32_bf16 v[216:219], v[228:231], v[236:239], v[216:219]\n" \
  "s_nop 8\n" \
  "v_add_f32_dpp v192, v218, v216 quad_perm:[2,3,2,3] row_mask:0xf bank_mask:0xf\n" \
  "v_exp_f32 v194, v192\n" \
  "v_cmp_nge_f32 vcc, %[rt], |v192|\n" \
  "s_and_b64 vcc, vcc, %[lo]\n" \
  "v_add_f32_dpp v198, v194, v199 quad_perm:[0,0,0,0] row_mask:0xf bank_mask:0xf\n" \
  "v_mov_b32_dpp v193, v194 quad_perm:[1,1,1,1] row_mask:0xf bank_mask:0xf\n" \
  "v_add_f32 v198, v198, v193\n" \
  "v_mov_b32_dpp v195, v194 quad_perm:[0,0,0,0] row_mask:0xf bank_mask:0xf\n" \
  "s_cbranch_scc1 .Lw2_%=\n" \
  "v_add_f32_dpp v198, v198, v198 row_ror:4 row_mask:0xf bank_mask:0xf\n" \
  "s_nop 1\n" \
  "v_add_f32_dpp v198, v198, v198 row_ror:8 row_mask:0xf bank_mask:0xf\n" \
  "v_rcp_f32 v198, v198\n" \
  "s_nop 0\n" \
  "v_mul_f32 v195, v195, v198\n" \
  "v_mul_f32 v193, v193, v198\n" \
  "v_cvt_pk_bf16_f32 v196, v195, v193\n" \
  "buffer_store_dword v196, v251, %[rs], %[so] offen sc1\n" \
  "v_cvt_pk_bf16_f32 v197, v198, v198\n" \
  "v_and_b32 v197, v252, v197\n" \
  "buffer_store_dword v197, v251, %[rs], %[so] offen offset:64 sc1\n" \
  ".Lw2b_%=:\n" \
  "s_setprio 0\n"
#define QK_A1 \
  "s_branch .Lgo_%=\n" \
  ".Lr_%=:\n" \
  "s_sub_u32 %[rt], %[rt], 1\n" \
  "s_cbranch_scc1 .Lrb_%=\n" \
  "s_sleep 2\n" \
  "buffer_load_dwordx4 v[192:195], v248, %[rs], %[o0] offen sc1\n" \
  "buffer_load_dwordx4 v[196:199], v248, %[rs], %[o0] offen offset:1024 sc1\n" \
  "v_mov_b64 v[224:225], 0\n" \
  "v_mov_b64 v[226:227], 0\n" \
  "v_mov_b64 v[228:229], 0\n" \
  "v_mov_b64 v[230:231], 0\n" \
  "v_mov_b64 v[232:233], 0\n" \
  "v_mov_b64 v[234:235], 0\n" \
  "v_mov_b64 v[236:237], 0\n" \
  "v_mov_b64 v[238:239], 0\n" \
  "s_nop 1\n" \
  "s_branch .Lw_%=\n" \
  ".Lw1_%=:\n" \
  "v_max_f32 v200, v192, v193\n" \
  "s_nop 1\n" \
  "v_max_f32_dpp v200, v200, v200 row_ror:8 row_mask:0xf bank_mask:0xf\n" \
  "s_nop 1\n" \
  "v_max_f32_dpp v200, v200, v200 row_ror:4 row_mask:0xf bank_mask:0xf\n" \
  "s_nop 1\n" \
  "v_max_f32_dpp v200, v200, v200 row_ror:2 row_mask:0xf bank_mask:0xf\n" \
  "s_nop 1\n" \
  "v_max_f32_dpp v200, v200, v200 row_ror:1 row_mask:0xf bank_mask:0xf\n" \
  "v_sub_f32 v194, v192, v200\n" \
  "v_sub_f32 v195, v193, v200\n" \
  "v_exp_f32 v194, v194\n" \
  "v_exp_f32 v195, v195\n" \
  "s_mov_b32 %[rt], -1.0\n" \
  "s_nop 0\n" \
  "v_cvt_pk_bf16_f32 v196, v194, v195\n" \
  "v_and_b32 v196, 0x7fffffff, v196\n" \
  "s_branch .Lw1b_%=\n" \
  ".Lw2_%=:\n" \
  "s_cmp_lt_i32 %[rt], 0\n" \
  "s_cbranch_scc1 .Lw3_%=\n" \
  "v_mov_b32 v200, 0\n" \
  ".Lw3_%=:\n" \
  "s_nop 1\n" \
  "v_max_f32_dpp v197, v192, v200 quad_perm:[0,0,0,0] row_mask:0xf bank_mask:0xf\n" \
  "s_nop 1\n" \
  "v_max_f32_dpp v197, v192, v197 quad_perm:[1,1,1,1] row_mask:0xf bank_mask:0xf\n" \
  "s_nop 1\n" \
  "v_max_f32_dpp v197, v197, v197 row_ror:4 row_mask:0xf bank_mask:0xf\n" \
  "s_nop 1\n" \
  "v_max_f32_dpp v197, v197, v197 row_ror:8 row_mask:0xf bank_mask:0xf\n" \
  "v_sub_f32 v194, v192, v197\n" \
  "v_sub_f32 v201, v200, v197\n" \
  "v_exp_f32 v194, v194\n" \
  "v_exp_f32 v201, v201\n" \
  "s_nop 1\n" \
  "v_mul_f32 v199, v201, v199\n" \
  "s_nop 1\n" \
  "v_add_f32_dpp v198, v194, v199 quad_perm:[0,0,0,0] row_mask:0xf bank_mask:0xf\n" \
  "s_nop 1\n" \
  "v_mov_b32_dpp v193, v194 quad_perm:[1,1,1,1] row_mask:0xf bank_mask:0xf\n" \
  "s_nop 1\n" \
  "v_add_f32 v198, v198, v193\n" \
  "s_nop 1\n" \
  "v_mov_b32_dpp v195, v194 quad_perm:[0,0,0,0] row_mask:0xf bank_mask:0xf\n" \
  "s_nop 1\n" \
  "v_add_f32_dpp v198, v198, v198 row_ror:4 row_mask:0xf bank_mask:0xf\n" \
  "s_nop 1\n" \
  "v_add_f32_dpp v198, v198, v198 row_ror:8 row_mask:0xf bank_mask:0xf\n" \
  "s_nop 1\n" \
  "v_rcp_f32 v198, v198\n" \
  "s_nop 1\n" \
  "v_mul_f32 v195, v195, v198\n" \
  "s_nop 1\n" \
  "v_mul_f32 v193, v193, v198\n" \
  "s_nop 1\n" \
  "v_cvt_pk_bf16_f32 v196, v195, v193\n" \
  "s_nop 1\n" \
  "v_and_b32 v196, 0x7fffffff, v196\n" \
  "s_nop 1\n" \
  "v_mul_f32 v198, v201, v198\n" \
  "s_nop 1\n" \
  "buffer_store_dword v196, v251, %[rs], %[so] offen sc1\n" \
  "s_nop 1\n" \
  "v_cvt_pk_bf16_f32 v197, v198, v198\n" \
  "s_nop 1\n" \
  "v_and_b32 v197, v252, v197\n" \
  "s_nop 1\n" \
  "buffer_store_dword v197, v251, %[rs], %[so] offen offset:64 sc1\n" \
  "s_branch .Lw2b_%=\n" \
  ".Lgo_%=:\n" \
  "v_add_u32 v252, 0x20000, v248\n" \
  "ds_read_b128 v[200:203], v248 offset:32768\n" \
  "ds_read_b128 v[204:207], v248 offset:33792\n" \
  "ds_read_b128 v[208:211], v248 offset:49152\n" \
  "ds_read_b128 v[212:215], v248 offset:50176\n" \
  "ds_read_b128 v[216:219], v252 offset:0\n" \
  "ds_read_b128 v[220:223], v252 offset:1024\n" \
  "ds_read_b128 v[240:243], v252 offset:16384\n" \
  "ds_read_b128 v[244:247], v252 offset:17408\n" \
  "v_mov_b64 v[224:225], 0\n" \
  "v_mov_b64 v[226:227], 0\n" \
  "v_mov_b64 v[228:229], 0\n" \
  "v_mov_b64 v[230:231], 0\n" \
  "v_mov_b64 v[232:233], 0\n" \
  "v_mov_b64 v[234:235], 0\n" \
  "v_mov_b64 v[236:237], 0\n" \
  "v_mov_b64 v[238:239], 0\n" \
  "v_xor_b32 v250, 0x800, v248\n" \
  "v_xor_b32 v251, 0x1000, v248\n" \
  "s_mov_b32 %[rt], 0x40000\n" \
  "s_setprio 2\n" \
  "s_waitcnt lgkmcnt(0)\n" \
  "s_barrier\n" \
  "buffer_load_dwordx4 v[192:195], v248, %[rs], %[o0] offen sc1\n" \
  "buffer_load_dwordx4 v[196:199], v248, %[rs], %[o0] offen offset:1024 sc1\n" \
  ".Lw_%=:\n" \
  "s_waitcnt vmcnt(1)\n" \
  "ds_write_b128 v248, v[192:195]\n" \
  "v_smfmac_f32_16x16x64_bf16 v[224:227], v[192:195], v[200:207], v249\n" \
  "v_max3_u32 v252, v192, v193, v194\n" \
  "s_waitcnt vmcnt(0)\n" \
  "ds_write_b128 v248, v[196:199] offset:1024\n" \
  "v_smfmac_f32_16x16x64_bf16 v[228:231], v[192:195], v[208:215], v249\n" \
  "v_max3_u32 v252, v252, v195, v196\n" \
  "v_smfmac_f32_16x16x64_bf16 v[224:227], v[196:199], v[216:223], v249\n" \
  "v_max3_u32 v252, v252, v197, v198\n" \
  "v_smfmac_f32_16x16x64_bf16 v[228:231], v[196:199], v[240:247], v249\n" \
  "v_max_u32 v252, v252, v199\n" \
  "v_smfmac_f32_16x16x64_bf16 v[232:235], v[192:195], v[96:103], v249\n" \
  "v_cmp_eq_u32 vcc, -1, v252\n" \
  "v_smfmac_f32_16x16x64_bf16 v[236:239], v[192:195], v[144:151], v249\n" \
  "v_xor_b32 v252, 0x1800, v248\n" \
  "s_cbranch_vccnz .Lr_%=\n" \
  ".Lrb_%=:\n" \
  "v_smfmac_f32_16x16x64_bf16 v[232:235], v[196:199], v[104:111], v249\n" \
  "v_smfmac_f32_16x16x64_bf16 v[236:239], v[196:199], v[152:159], v249\n" \
  "s_waitcnt lgkmcnt(0)\n" \
  "s_barrier\n" \
  "ds_read_b128 v[200:203], v250 offset:0\n" \
  "ds_read_b128 v[204:207], v250 offset:1024\n" \
  "ds_read_b128 v[208:211], v251 offset:0\n" \
  "ds_read_b128 v[212:215], v251 offset:1024\n" \
  "ds_read_b128 v[216:219], v252 offset:0\n" \
  "ds_read_b128 v[220:223], v252 offset:1024\n" \
  "s_waitcnt lgkmcnt(5)\n" \
  "v_smfmac_f32_16x16x64_bf16 v[224:227], v[200:203], v[0:7], v249\n" \
  "v_smfmac_f32_16x16x64_bf16 v[228:231], v[200:203], v[48:55], v249\n" \
  "s_waitcnt lgkmcnt(4)\n" \
  "v_smfmac_f32_16x16x64_bf16 v[224:227], v[204:207], v[8:15], v249\n" \
  "v_smfmac_f32_16x16x64_bf16 v[228:231], v[204:207], v[56:63], v249\n" \
  "s_waitcnt lgkmcnt(3)\n" \
  "v_smfmac_f32_16x16x64_bf16 v[224:227], v[208:211], v[16:23], v249\n" \
  "v_smfmac_f32_16x16x64_bf16 v[228:231], v[208:211], v[64:71], v249\n" \
  "s_waitcnt lgkmcnt(2)\n" \
  "v_smfmac_f32_16x16x64_bf16 v[224:227], v[212:215], v[24:31], v249\n" \
  "s_waitcnt lgkmcnt(1)\n" \
  "v_smfmac_f32_16x16x64_bf16 v[224:227], v[216:219], v[32:39], v249\n" \
  "s_waitcnt lgkmcnt(0)\n" \
  "v_smfmac_f32_16x16x64_bf16 v[224:227], v[220:223], v[40:47], v249\n" \
  "v_smfmac_f32_16x16x64_bf16 v[228:231], v[212:215], v[72:79], v249\n" \
  "v_smfmac_f32_16x16x64_bf16 v[228:231], v[216:219], v[80:87], v249\n" \
  "s_nop 0\n" \
  "v_add_f32 v224, v224, v226\n" \
  "v_smfmac_f32_16x16x64_bf16 v[228:231], v[220:223], v[88:95], v249\n" \
  "v_add_f32 v225, v225, v227\n" \
  "ds_write_b64 v248, v[224:225] offset:8192\n" \
  "s_nop 6\n" \
  "v_add_f32 v228, v228, v230\n" \
  "v_add_f32 v229, v229, v231\n" \
  "ds_write_b64 v248, v[228:229] offset:8200\n" \
  "s_setprio 0\n" \
  "ds_read_b128 v[240:243], v254 offset:0\n" \
  "ds_read_b128 v[244:247], v254 offset:1024\n" \
  "s_waitcnt lgkmcnt(2)\n" \
  "s_barrier\n" \
  "s_nop 1\n" \
  "ds_read_b128 v[192:195], v254 offset:16384\n" \
  "ds_read_b128 v[196:199], v254 offset:17408\n" \
  "ds_read_b128 v[224:227], v254 offset:32768\n" \
  "ds_read_b128 v[228:231], v254 offset:33792\n" \
  "s_waitcnt lgkmcnt(4)\n" \
  "v_smfmac_f32_16x16x64_bf16 v[232:235], v[200:203], v[240:247], v249\n" \
  "ds_read_b128 v[240:243], v254 offset:49152\n" \
  "ds_read_b128 v[244:247], v254 offset:50176\n" \
  "s_waitcnt lgkmcnt(4)\n" \
  "v_smfmac_f32_16x16x64_bf16 v[236:239], v[200:203], v[192:199], v249\n" \
  "v_smfmac_f32_16x16x64_bf16 v[232:235], v[204:207], v[112:119], v249\n" \
  "ds_read_b128 v[192:195], v248 offset:14336\n" \
  "v_smfmac_f32_16x16x64_bf16 v[236:239], v[204:207], v[160:167], v249\n" \
  "ds_read_b128 v[196:199], v248 offset:15360\n" \
  "v_smfmac_f32_16x16x64_bf16 v[232:235], v[212:215], v[120:127], v249\n" \
  "ds_read_b128 v[200:203], v248 offset:18432\n" \
  "v_smfmac_f32_16x16x64_bf16 v[236:239], v[212:215], v[168:175], v249\n" \
  "ds_read_b128 v[204:207], v248 offset:19456\n" \
  "s_waitcnt lgkmcnt(6)\n" \
  "v_smfmac_f32_16x16x64_bf16 v[232:235], v[208:211], v[224:231], v249\n" \
  "ds_read_b128 v[212:215], v248 offset:21504\n" \
  "s_waitcnt lgkmcnt(5)\n" \
  "v_smfmac_f32_16x16x64_bf16 v[236:239], v[208:211], v[240:247], v249\n" \
  "ds_read_b128 v[240:243], v248 offset:16384\n" \
  "v_smfmac_f32_16x16x64_bf16 v[232:235], v[216:219], v[128:135], v249\n" \
  "ds_read_b128 v[244:247], v248 offset:17408\n" \
  "v_smfmac_f32_16x16x64_bf16 v[232:235], v[220:223], v[136:143], v249\n" \
  "ds_read_b128 v[208:211], v248 offset:20480\n" \
  "v_smfmac_f32_16x16x64_bf16 v[236:239], v[216:219], v[176:183], v249\n" \
  "ds_read_b96 v[250:252], v248 offset:13312\n" \
  "v_smfmac_f32_16x16x64_bf16 v[236:239], v[220:223], v[184:191], v249\n" \
  "s_setprio 3\n" \
  "s_barrier\n" \
  "ds_read_b64_tr_b16 v[224:225], v253 offset:17408\n" \
  "ds_read_b64_tr_b16 v[226:227], v253 offset:17664\n" \
  "ds_read_b64_tr_b16 v[228:229], v253 offset:19456\n" \
  "ds_read_b64_tr_b16 v[230:231], v253 offset:19712\n" \
  "v_mov_b64 v[216:217], 0\n" \
  "v_mov_b64 v[218:219], 0\n" \
  "v_add_f32 v232, v232, v234\n" \
  "v_add_f32 v233, v233, v235\n" \
  "ds_write_b64 v248, v[232:233] offset:0\n" \
  "v_mov_b64 v[220:221], 0\n" \
  "v_mov_b64 v[222:223], 0\n" \
  "v_add_f32 v236, v236, v238\n" \
  "v_add_f32 v237, v237, v239\n" \
  "ds_write_b64 v248, v[236:237] offset:8\n" \
  "s_waitcnt lgkmcnt(4)\n" \
  "v_smfmac_f32_16x16x64_bf16 v[216:219], v[224:227], v[192:199], v249\n" \
  "v_smfmac_f32_16x16x64_bf16 v[220:223], v[224:227], v[200:207], v249\n" \
  "s_waitcnt lgkmcnt(2)\n" \
  "v_smfmac_f32_16x16x64_bf16 v[216:219], v[228:231], v[240:247], v249\n" \
  "v_smfmac_f32_16x16x64_bf16 v[220:223], v[228:231], v[208:215], v249\n" \
  "s_mov_b32 %[rt], 0x422c0000\n" \
  "s_waitcnt lgkmcnt(0)\n" \
  "s_barrier\n" \
  "s_nop 1\n" \
  "v_add_f32 v192, v217, v219\n" \
  "s_nop 2\n" \
  "v_add_f32 v193, v221, v223\n" \
  "v_exp_f32 v194, v192\n" \
  "v_exp_f32 v195, v193\n" \
  "v_max_f32 v197, |v192|, |v193|\n" \
  "v_cvt_pk_bf16_f32 v196, v194, v195\n" \
  "v_cmp_nge_f32 vcc, %[rt], v197\n" \
  "s_cbranch_vccnz .Lw1_%=\n" \
  ".Lw1b_%=:\n" \
  "buffer_store_dword v196, v250, %[rs], %[c0] offen sc1\n" \
  "buffer_store_dword v196, v250, %[rs], %[c1] offen sc1\n" \
  "buffer_store_dword v196, v250, %[rs], %[c2] offen sc1\n" \
  "buffer_store_dword v196, v250, %[rs], %[c3] offen sc1\n" \
  "v_add_f32 v199, v194, v195\n" \
  "v_xor_b32 v251, %[xr], v251\n" \
  "s_nop 0\n" \
  "v_add_f32_dpp v199, v199, v199 quad_perm:[1,0,3,2] row_mask:0xf bank_mask:0xf\n" \
  "s_nop 1\n" \
  "v_add_f32_dpp v199, v199, v199 quad_perm:[2,3,0,1] row_mask:0xf bank_mask:0xf\n" \
  "s_barrier\n" \
  "ds_read_b64_tr_b16 v[232:233], v253 offset:9216\n" \
  "ds_read_b64_tr_b16 v[234:235], v253 offset:9472\n" \
  "ds_read_b64_tr_b16 v[236:237], v253 offset:11264\n" \
  "ds_read_b64_tr_b16 v[238:239], v253 offset:11520\n" \
  "s_waitcnt lgkmcnt(2)\n" \
  "v_mfma_f32_16x16x32_bf16 v[216:219], v[224:227], v[232:235], 0\n" \
  "s_waitcnt lgkmcnt(0)\n" \
  "v_mfma_f32_16x16x32_bf16 v[216:219], v[228:231], v[236:239], v[216:219]\n" \
  "s_nop 8\n" \
  "v_add_f32_dpp v192, v219, v217 quad_perm:[2,3,2,3] row_mask:0xf bank_mask:0xf\n" \
  "v_exp_f32 v194, v192\n" \
  "v_cmp_nge_f32 vcc, %[rt], |v192|\n" \
  "s_and_b64 vcc, vcc, %[lo]\n" \
  "v_add_f32_dpp v198, v194, v199 quad_perm:[0,0,0,0] row_mask:0xf bank_mask:0xf\n" \
  "v_mov_b32_dpp v193, v194 quad_perm:[1,1,1,1] row_mask:0xf bank_mask:0xf\n" \
  "v_add_f32 v198, v198, v193\n" \
  "v_mov_b32_dpp v195, v194 quad_perm:[0,0,0,0] row_mask:0xf bank_mask:0xf\n" \
  "s_cbranch_scc1 .Lw2_%=\n" \
  "v_add_f32_dpp v198, v198, v198 row_ror:4 row_mask:0xf bank_mask:0xf\n" \
  "s_nop 1\n" \
  "v_add_f32_dpp v198, v198, v198 row_ror:8 row_mask:0xf bank_mask:0xf\n" \
  "v_rcp_f32 v198, v198\n" \
  "s_nop 0\n" \
  "v_mul_f32 v195, v195, v198\n" \
  "v_mul_f32 v193, v193, v198\n" \
  "v_cvt_pk_bf16_f32 v196, v195, v193\n" \
  "buffer_store_dword v196, v251, %[rs], %[so] offen sc1\n" \
  "v_cvt_pk_bf16_f32 v197, v198, v198\n" \
  "v_and_b32 v197, v252, v197\n" \
  "buffer_store_dword v197, v251, %[rs], %[so] offen offset:64 sc1\n" \
  ".Lw2b_%=:\n" \
  "s_setprio 0\n"
#define QK_X \
  "s_branch .Lgo_%=\n" \
  ".Lr_%=:\n" \
  "s_sub_u32 %[rt], %[rt], 1\n" \
  "s_cbranch_scc1 .Lrb_%=\n" \
  "s_sleep 2\n" \
  "buffer_load_dwordx4 v[192:195], v248, %[rs], %[o0] offen sc1\n" \
  "buffer_load_dwordx4 v[196:199], v248, %[rs], %[o0] offen offset:1024 sc1\n" \
  "v_mov_b64 v[224:225], 0\n" \
  "v_mov_b64 v[226:227], 0\n" \
  "v_mov_b64 v[228:229], 0\n" \
  "v_mov_b64 v[230:231], 0\n" \
  "v_mov_b64 v[232:233], 0\n" \
  "v_mov_b64 v[234:235], 0\n" \
  "v_mov_b64 v[236:237], 0\n" \
  "v_mov_b64 v[238:239], 0\n" \
  "s_nop 1\n" \
  "s_branch .Lw_%=\n" \
  ".Lgo_%=:\n" \
  "v_add_u32 v252, 0x20000, v248\n" \
  "ds_read_b128 v[200:203], v248 offset:32768\n" \
  "ds_read_b128 v[204:207], v248 offset:33792\n" \
  "ds_read_b128 v[208:211], v248 offset:49152\n" \
  "ds_read_b128 v[212:215], v248 offset:50176\n" \
  "ds_read_b128 v[216:219], v252 offset:0\n" \
  "ds_read_b128 v[220:223], v252 offset:1024\n" \
  "ds_read_b128 v[240:243], v252 offset:16384\n" \
  "ds_read_b128 v[244:247], v252 offset:17408\n" \
  "v_mov_b64 v[224:225], 0\n" \
  "v_mov_b64 v[226:227], 0\n" \
  "v_mov_b64 v[228:229], 0\n" \
  "v_mov_b64 v[230:231], 0\n" \
  "v_mov_b64 v[232:233], 0\n" \
  "v_mov_b64 v[234:235], 0\n" \
  "v_mov_b64 v[236:237], 0\n" \
  "v_mov_b64 v[238:239], 0\n" \
  "v_xor_b32 v250, 0x800, v248\n" \
  "v_xor_b32 v251, 0x1000, v248\n" \
  "s_mov_b32 %[rt], 0x40000\n" \
  "s_setprio 2\n" \
  "s_waitcnt lgkmcnt(0)\n" \
  "s_barrier\n" \
  "s_nop 3\n" \
  "buffer_load_dwordx4 v[192:195], v248, %[rs], %[o0] offen sc1\n" \
  "buffer_load_dwordx4 v[196:199], v248, %[rs], %[o0] offen offset:1024 sc1\n" \
  ".Lw_%=:\n" \
  "s_waitcnt vmcnt(1)\n" \
  "ds_write_b128 v248, v[192:195]\n" \
  "v_smfmac_f32_16x16x64_bf16 v[224:227], v[192:195], v[200:207], v249\n" \
  "v_max3_u32 v252, v192, v193, v194\n" \
  "s_waitcnt vmcnt(0)\n" \
  "ds_write_b128 v248, v[196:199] offset:1024\n" \
  "v_smfmac_f32_16x16x64_bf16 v[228:231], v[192:195], v[208:215], v249\n" \
  "v_max3_u32 v252, v252, v195, v196\n" \
  "v_smfmac_f32_16x16x64_bf16 v[224:227], v[196:199], v[216:223], v249\n" \
  "v_max3_u32 v252, v252, v197, v198\n" \
  "v_smfmac_f32_16x16x64_bf16 v[228:231], v[196:199], v[240:247], v249\n" \
  "v_max_u32 v252, v252, v199\n" \
  "v_smfmac_f32_16x16x64_bf16 v[232:235], v[192:195], v[96:103], v249\n" \
  "v_cmp_eq_u32 vcc, -1, v252\n" \
  "v_smfmac_f32_16x16x64_bf16 v[236:239], v[192:195], v[144:151], v249\n" \
  "v_xor_b32 v252, 0x1800, v248\n" \
  "s_cbranch_vccnz .Lr_%=\n" \
  ".Lrb_%=:\n" \
  "v_smfmac_f32_16x16x64_bf16 v[232:235], v[196:199], v[104:111], v249\n" \
  "v_smfmac_f32_16x16x64_bf16 v[236:239], v[196:199], v[152:159], v249\n" \
  "s_waitcnt lgkmcnt(0)\n" \
  "s_barrier\n" \
  "ds_read_b128 v[200:203], v250 offset:0\n" \
  "ds_read_b128 v[204:207], v250 offset:1024\n" \
  "ds_read_b128 v[208:211], v251 offset:0\n" \
  "ds_read_b128 v[212:215], v251 offset:1024\n" \
  "ds_read_b128 v[216:219], v252 offset:0\n" \
  "ds_read_b128 v[220:223], v252 offset:1024\n" \
  "s_waitcnt lgkmcnt(5)\n" \
  "v_smfmac_f32_16x16x64_bf16 v[224:227], v[200:203], v[0:7], v249\n" \
  "v_smfmac_f32_16x16x64_bf16 v[228:231], v[200:203], v[48:55], v249\n" \
  "s_waitcnt lgkmcnt(4)\n" \
  "v_smfmac_f32_16x16x64_bf16 v[224:227], v[204:207], v[8:15], v249\n" \
  "v_smfmac_f32_16x16x64_bf16 v[228:231], v[204:207], v[56:63], v249\n" \
  "s_waitcnt lgkmcnt(3)\n" \
  "v_smfmac_f32_16x16x64_bf16 v[224:227], v[208:211], v[16:23], v249\n" \
  "v_smfmac_f32_16x16x64_bf16 v[228:231], v[208:211], v[64:71], v249\n" \
  "s_waitcnt lgkmcnt(2)\n" \
  "v_smfmac_f32_16x16x64_bf16 v[224:227], v[212:215], v[24:31], v249\n" \
  "s_waitcnt lgkmcnt(1)\n" \
  "v_smfmac_f32_16x16x64_bf16 v[224:227], v[216:219], v[32:39], v249\n" \
  "s_waitcnt lgkmcnt(0)\n" \
  "v_smfmac_f32_16x16x64_bf16 v[224:227], v[220:223], v[40:47], v249\n" \
  "v_smfmac_f32_16x16x64_bf16 v[228:231], v[212:215], v[72:79], v249\n" \
  "v_and_b32 v252, 0x3ff, v248\n" \
  "v_smfmac_f32_16x16x64_bf16 v[228:231], v[216:219], v[80:87], v249\n" \
  "s_nop 0\n" \
  "v_add_f32 v224, v224, v226\n" \
  "v_smfmac_f32_16x16x64_bf16 v[228:231], v[220:223], v[88:95], v249\n" \
  "v_add_f32 v225, v225, v227\n" \
  "ds_write_b64 v248, v[224:225] offset:8192\n" \
  "s_nop 6\n" \
  "v_add_f32 v228, v228, v230\n" \
  "v_add_f32 v229, v229, v231\n" \
  "ds_write_b64 v248, v[228:229] offset:8200\n" \
  "s_setprio 0\n" \
  "ds_read_b128 v[240:243], v254 offset:0\n" \
  "ds_read_b128 v[244:247], v254 offset:1024\n" \
  "s_waitcnt lgkmcnt(2)\n" \
  "s_barrier\n" \
  "s_nop 1\n" \
  "ds_read_b128 v[192:195], v254 offset:16384\n" \
  "ds_read_b128 v[196:199], v254 offset:17408\n" \
  "ds_read_b128 v[224:227], v254 offset:32768\n" \
  "ds_read_b128 v[228:231], v254 offset:33792\n" \
  "s_waitcnt lgkmcnt(4)\n" \
  "v_smfmac_f32_16x16x64_bf16 v[232:235], v[200:203], v[240:247], v249\n" \
  "ds_read_b128 v[240:243], v254 offset:49152\n" \
  "ds_read_b128 v[244:247], v254 offset:50176\n" \
  "s_waitcnt lgkmcnt(4)\n" \
  "v_smfmac_f32_16x16x64_bf16 v[236:239], v[200:203], v[192:199], v249\n" \
  "v_smfmac_f32_16x16x64_bf16 v[232:235], v[204:207], v[112:119], v249\n" \
  "v_smfmac_f32_16x16x64_bf16 v[236:239], v[204:207], v[160:167], v249\n" \
  "v_smfmac_f32_16x16x64_bf16 v[232:235], v[212:215], v[120:127], v249\n" \
  "v_smfmac_f32_16x16x64_bf16 v[236:239], v[212:215], v[168:175], v249\n" \
  "s_waitcnt lgkmcnt(2)\n" \
  "v_smfmac_f32_16x16x64_bf16 v[232:235], v[208:211], v[224:231], v249\n" \
  "s_waitcnt lgkmcnt(0)\n" \
  "v_smfmac_f32_16x16x64_bf16 v[236:239], v[208:211], v[240:247], v249\n" \
  "v_smfmac_f32_16x16x64_bf16 v[232:235], v[216:219], v[128:135], v249\n" \
  "v_smfmac_f32_16x16x64_bf16 v[232:235], v[220:223], v[136:143], v249\n" \
  "v_smfmac_f32_16x16x64_bf16 v[236:239], v[216:219], v[176:183], v249\n" \
  "buffer_load_dwordx4 v[224:227], v252, %[rs], %[qo] offen offset:0 sc1\n" \
  "buffer_load_dwordx4 v[228:231], v252, %[rs], %[qo] offen offset:1024 sc1\n" \
  "buffer_load_dwordx4 v[240:243], v252, %[rs], %[qo] offen offset:2048 sc1\n" \
  "buffer_load_dwordx4 v[244:247], v252, %[rs], %[qo] offen offset:3072 sc1\n" \
  "v_smfmac_f32_16x16x64_bf16 v[236:239], v[220:223], v[184:191], v249\n" \
  "s_setprio 3\n" \
  "s_barrier\n" \
  "v_add_f32 v232, v232, v234\n" \
  "v_add_f32 v233, v233, v235\n" \
  "ds_write_b64 v248, v[232:233] offset:0\n" \
  "s_nop 3\n" \
  "v_add_f32 v236, v236, v238\n" \
  "v_add_f32 v237, v237, v239\n" \
  "ds_write_b64 v248, v[236:237] offset:8\n" \
  "s_waitcnt lgkmcnt(0)\n" \
  "s_barrier\n" \
  "s_setprio 0\n" \
  "s_nop 15\n" \
  "s_nop 13\n" \
  "ds_read_b128 v[192:195], v252 offset:24576\n" \
  "ds_read_b128 v[196:199], v252 offset:28672\n" \
  "ds_read_b128 v[200:203], v252 offset:25600\n" \
  "ds_read_b128 v[204:207], v252 offset:29696\n" \
  "ds_read_b128 v[208:211], v252 offset:26624\n" \
  "ds_read_b128 v[212:215], v252 offset:30720\n" \
  "ds_read_b128 v[216:219], v252 offset:27648\n" \
  "ds_read_b128 v[220:223], v252 offset:31744\n" \
  "s_waitcnt lgkmcnt(0)\n" \
  "s_barrier\n" \
  "s_nop 15\n" \
  "s_nop 3\n" \
  "ds_read_b64_tr_b16 v[232:233], v253 offset:9216\n" \
  "ds_read_b64_tr_b16 v[234:235], v253 offset:9472\n" \
  "ds_read_b64_tr_b16 v[236:237], v253 offset:11264\n" \
  "ds_read_b64_tr_b16 v[238:239], v253 offset:11520\n" \
  "s_waitcnt vmcnt(3)\n" \
  "v_mfma_f32_16x16x32_bf16 v[192:195], v[192:195], v[224:227], 0\n" \
  "v_mfma_f32_16x16x32_bf16 v[196:199], v[196:199], v[224:227], 0\n" \
  "s_waitcnt vmcnt(2)\n" \
  "v_mfma_f32_16x16x32_bf16 v[192:195], v[200:203], v[228:231], v[192:195]\n" \
  "v_mfma_f32_16x16x32_bf16 v[196:199], v[204:207], v[228:231], v[196:199]\n" \
  "s_waitcnt vmcnt(1)\n" \
  "v_mfma_f32_16x16x32_bf16 v[192:195], v[208:211], v[240:243], v[192:195]\n" \
  "v_mov_b64 v[200:201], 0\n" \
  "v_mfma_f32_16x16x32_bf16 v[196:199], v[212:215], v[240:243], v[196:199]\n" \
  "v_mov_b64 v[202:203], 0\n" \
  "s_waitcnt vmcnt(0)\n" \
  "v_mfma_f32_16x16x32_bf16 v[192:195], v[216:219], v[244:247], v[192:195]\n" \
  "v_mfma_f32_16x16x32_bf16 v[196:199], v[220:223], v[244:247], v[196:199]\n" \
  "s_waitcnt lgkmcnt(2)\n" \
  "v_smfmac_f32_16x16x64_bf16 v[200:203], v[232:235], v[224:231], v249\n" \
  "s_waitcnt lgkmcnt(0)\n" \
  "v_smfmac_f32_16x16x64_bf16 v[200:203], v[236:239], v[240:247], v249\n" \
  "s_nop 8\n"
#define QK_K \
  "s_branch .Lgo_%=\n" \
  ".Lr_%=:\n" \
  "s_sub_u32 %[rt], %[rt], 1\n" \
  "s_cbranch_scc1 .Lrb_%=\n" \
  "s_sleep 2\n" \
  "buffer_load_dwordx4 v[192:195], v248, %[rs], %[o0] offen sc1\n" \
  "buffer_load_dwordx4 v[196:199], v248, %[rs], %[o0] offen offset:1024 sc1\n" \
  "v_mov_b64 v[224:225], 0\n" \
  "v_mov_b64 v[226:227], 0\n" \
  "v_mov_b64 v[228:229], 0\n" \
  "v_mov_b64 v[230:231], 0\n" \
  "s_nop 1\n" \
  "s_branch .Lw_%=\n" \
  ".Lgo_%=:\n" \
  "v_add_u32 v252, 0x20000, v248\n" \
  "ds_read_b128 v[200:203], v248 offset:32768\n" \
  "ds_read_b128 v[204:207], v248 offset:33792\n" \
  "ds_read_b128 v[208:211], v248 offset:49152\n" \
  "ds_read_b128 v[212:215], v248 offset:50176\n" \
  "ds_read_b128 v[216:219], v252 offset:0\n" \
  "ds_read_b128 v[220:223], v252 offset:1024\n" \
  "ds_read_b128 v[240:243], v252 offset:16384\n" \
  "ds_read_b128 v[244:247], v252 offset:17408\n" \
  "v_mov_b64 v[224:225], 0\n" \
  "v_mov_b64 v[226:227], 0\n" \
  "v_mov_b64 v[228:229], 0\n" \
  "v_mov_b64 v[230:231], 0\n" \
  "v_xor_b32 v250, 0x800, v248\n" \
  "v_xor_b32 v251, 0x1000, v248\n" \
  "s_mov_b32 %[rt], 0x40000\n" \
  "s_setprio 1\n" \
  "s_waitcnt lgkmcnt(0)\n" \
  "s_cmp_eq_u32 %[tw], 0\n" \
  "s_cbranch_scc1 .Ltq_%=\n" \
  ".Ltl_%=:\n" \
  "s_memtime vcc\n" \
  "s_waitcnt lgkmcnt(0)\n" \
  "s_sub_i32 %[cnt], %[tg], vcc_lo\n" \
  "s_cmp_lt_i32 %[cnt], 260\n" \
  "s_cbranch_scc1 .Ltf_%=\n" \
  "s_sleep 1\n" \
  "s_branch .Ltl_%=\n" \
  ".Ltf_%=:\n" \
  "s_max_i32 %[cnt], %[cnt], 0\n" \
  ".Ltg_%=:\n" \
  "s_sub_u32 %[cnt], %[cnt], 28\n" \
  "s_cbranch_scc0 .Ltg_%=\n" \
  ".Ltq_%=:\n" \
  "s_barrier\n" \
  "buffer_load_dwordx4 v[192:195], v248, %[rs], %[o0] offen sc1\n" \
  "buffer_load_dwordx4 v[196:199], v248, %[rs], %[o0] offen offset:1024 sc1\n" \
  ".Lw_%=:\n" \
  "s_waitcnt vmcnt(1)\n" \
  "ds_write_b128 v248, v[192:195]\n" \
  "v_smfmac_f32_16x16x64_bf16 v[224:227], v[192:195], v[200:207], v249\n" \
  "v_max3_u32 v252, v192, v193, v194\n" \
  "s_waitcnt vmcnt(0)\n" \
  "ds_write_b128 v248, v[196:199] offset:1024\n" \
  "v_smfmac_f32_16x16x64_bf16 v[228:231], v[192:195], v[208:215], v249\n" \
  "v_max3_u32 v252, v252, v195, v196\n" \
  "v_smfmac_f32_16x16x64_bf16 v[224:227], v[196:199], v[216:223], v249\n" \
  "v_max3_u32 v252, v252, v197, v198\n" \
  "v_max_u32 v252, v252, v199\n" \
  "v_smfmac_f32_16x16x64_bf16 v[228:231], v[196:199], v[240:247], v249\n" \
  "v_cmp_eq_u32 vcc, -1, v252\n" \
  "v_xor_b32 v252, 0x1800, v248\n" \
  "s_cbranch_vccnz .Lr_%=\n" \
  ".Lrb_%=:\n" \
  "s_waitcnt lgkmcnt(0)\n" \
  "s_barrier\n" \
  "s_memtime vcc\n" \
  "s_setprio 3\n" \
  "s_waitcnt lgkmcnt(0)\n" \
  "s_sub_u32 %[cnt], vcc_lo, %[ckm]\n" \
  "s_mul_hi_u32 %[cnt], %[cnt], %[inv]\n" \
  "s_add_u32 %[cnt], %[cnt], %[r16]\n" \
  "s_and_b32 %[cnt], %[cnt], -2\n" \
  "v_mov_b32 v235, %[cnt]\n" \
  "buffer_store_dword v235, v232, %[rs], %[sst] offen sc1\n" \
  "s_nop 5\n" \
  "ds_read_b128 v[200:203], v250 offset:0\n" \
  "ds_read_b128 v[204:207], v250 offset:1024\n" \
  "ds_read_b128 v[208:211], v251 offset:0\n" \
  "ds_read_b128 v[212:215], v251 offset:1024\n" \
  "ds_read_b128 v[216:219], v252 offset:0\n" \
  "ds_read_b128 v[220:223], v252 offset:1024\n" \
  "buffer_load_dword v250, v233, %[rs], %[t0] offen sc1\n" \
  "buffer_load_dword v251, v234, %[rs], %[t1] offen sc1\n" \
  "s_setprio 1\n" \
  "s_waitcnt lgkmcnt(5)\n" \
  "v_smfmac_f32_16x16x64_bf16 v[224:227], v[200:203], v[0:7], v249\n" \
  "v_smfmac_f32_16x16x64_bf16 v[228:231], v[200:203], v[48:55], v249\n" \
  "s_waitcnt lgkmcnt(4)\n" \
  "v_smfmac_f32_16x16x64_bf16 v[224:227], v[204:207], v[8:15], v249\n" \
  "v_mov_b64 v[232:233], 0\n" \
  "v_smfmac_f32_16x16x64_bf16 v[228:231], v[204:207], v[56:63], v249\n" \
  "v_mov_b64 v[234:235], 0\n" \
  "s_waitcnt lgkmcnt(3)\n" \
  "v_smfmac_f32_16x16x64_bf16 v[224:227], v[208:211], v[16:23], v249\n" \
  "ds_read_b128 v[240:243], v254 offset:0\n" \
  "v_smfmac_f32_16x16x64_bf16 v[228:231], v[208:211], v[64:71], v249\n" \
  "ds_read_b128 v[244:247], v254 offset:1024\n" \
  "s_waitcnt lgkmcnt(4)\n" \
  "v_smfmac_f32_16x16x64_bf16 v[224:227], v[212:215], v[24:31], v249\n" \
  "s_waitcnt lgkmcnt(3)\n" \
  "v_smfmac_f32_16x16x64_bf16 v[224:227], v[216:219], v[32:39], v249\n" \
  "s_waitcnt lgkmcnt(2)\n" \
  "v_smfmac_f32_16x16x64_bf16 v[224:227], v[220:223], v[40:47], v249\n" \
  "s_barrier\n" \
  "ds_read_b128 v[236:239], v248 offset:16384\n" \
  "v_smfmac_f32_16x16x64_bf16 v[228:231], v[212:215], v[72:79], v249\n" \
  "v_smfmac_f32_16x16x64_bf16 v[228:231], v[216:219], v[80:87], v249\n" \
  "s_nop 0\n" \
  "v_add_f32 v224, v224, v226\n" \
  "v_smfmac_f32_16x16x64_bf16 v[228:231], v[220:223], v[88:95], v249\n" \
  "v_add_f32 v225, v225, v227\n" \
  "s_waitcnt lgkmcnt(0)\n" \
  "v_add_f32 v236, v236, v224\n" \
  "v_add_f32 v237, v237, v225\n" \
  "v_cvt_pk_bf16_f32 v236, v236, v237\n" \
  "s_nop 3\n" \
  "v_add_f32 v238, v238, v228\n" \
  "v_add_f32 v239, v239, v229\n" \
  "v_add_f32 v238, v238, v230\n" \
  "v_add_f32 v239, v239, v231\n" \
  "v_cvt_pk_bf16_f32 v237, v238, v239\n" \
  "ds_write_b64 v253, v[236:237] offset:17408\n" \
  "s_setprio 0\n" \
  "ds_read_b128 v[224:227], v254 offset:16384\n" \
  "ds_read_b128 v[228:231], v254 offset:17408\n" \
  "v_mov_b64 v[236:237], 0\n" \
  "v_mov_b64 v[238:239], 0\n" \
  "v_smfmac_f32_16x16x64_bf16 v[232:235], v[192:195], v[96:103], v249\n" \
  "v_smfmac_f32_16x16x64_bf16 v[236:239], v[192:195], v[144:151], v249\n" \
  "s_waitcnt lgkmcnt(2)\n" \
  "s_barrier\n" \
  "v_smfmac_f32_16x16x64_bf16 v[232:235], v[196:199], v[104:111], v249\n" \
  "v_smfmac_f32_16x16x64_bf16 v[236:239], v[196:199], v[152:159], v249\n" \
  "v_smfmac_f32_16x16x64_bf16 v[232:235], v[200:203], v[240:247], v249\n" \
  "ds_read_b128 v[192:195], v254 offset:32768\n" \
  "ds_read_b128 v[196:199], v254 offset:33792\n" \
  "s_waitcnt lgkmcnt(2)\n" \
  "v_smfmac_f32_16x16x64_bf16 v[236:239], v[200:203], v[224:231], v249\n" \
  "ds_read_b128 v[240:243], v254 offset:49152\n" \
  "ds_read_b128 v[244:247], v254 offset:50176\n" \
  "v_smfmac_f32_16x16x64_bf16 v[232:235], v[204:207], v[112:119], v249\n" \
  "v_smfmac_f32_16x16x64_bf16 v[236:239], v[204:207], v[160:167], v249\n" \
  "s_barrier\n" \
  "ds_read_b128 v[200:203], v248 offset:8192\n" \
  "v_smfmac_f32_16x16x64_bf16 v[232:235], v[212:215], v[120:127], v249\n" \
  "v_smfmac_f32_16x16x64_bf16 v[236:239], v[212:215], v[168:175], v249\n" \
  "v_smfmac_f32_16x16x64_bf16 v[232:235], v[216:219], v[128:135], v249\n" \
  "v_smfmac_f32_16x16x64_bf16 v[232:235], v[220:223], v[136:143], v249\n" \
  "s_waitcnt lgkmcnt(0)\n" \
  "v_mul_f32 v200, %[cs], v200\n" \
  "v_smfmac_f32_16x16x64_bf16 v[232:235], v[208:211], v[192:199], v249\n" \
  "v_mul_f32 v201, %[cs], v201\n" \
  "v_smfmac_f32_16x16x64_bf16 v[236:239], v[216:219], v[176:183], v249\n" \
  "v_mul_f32 v202, %[cs], v202\n" \
  "v_smfmac_f32_16x16x64_bf16 v[236:239], v[220:223], v[184:191], v249\n" \
  "v_mul_f32 v203, %[cs], v203\n" \
  "v_smfmac_f32_16x16x64_bf16 v[236:239], v[208:211], v[240:247], v249\n" \
  "v_fma_f32 v200, v232, %[cs], v200\n" \
  "v_fma_f32 v201, v233, %[cs], v201\n" \
  "v_fma_f32 v200, v234, %[cs], v200\n" \
  "v_fma_f32 v201, v235, %[cs], v201\n" \
  "v_cvt_pk_bf16_f32 v200, v200, v201\n" \
  "s_nop 3\n" \
  "v_fma_f32 v202, v236, %[cs], v202\n" \
  "v_fma_f32 v203, v237, %[cs], v203\n" \
  "v_fma_f32 v202, v238, %[cs], v202\n" \
  "v_fma_f32 v203, v239, %[cs], v203\n" \
  "v_cvt_pk_bf16_f32 v201, v202, v203\n" \
  "ds_write_b64 v253, v[200:201] offset:9216\n" \
  "s_waitcnt lgkmcnt(0)\n" \
  "s_barrier\n" \
  "s_waitcnt vmcnt(0)\n"
#define OW_F \
  "s_branch .Lgo_%=\n" \
  ".Lbad_%=:\n" \
  "s_mov_b32 %[st], 1\n" \
  "s_branch .Lend_%=\n" \
  ".Lgo_%=:\n" \
  "v_mov_b64 v[96:97], 0\n" \
  "v_mov_b64 v[98:99], 0\n" \
  "s_mov_b32 %[st], 0\n" \
  "s_barrier\n" \
  "s_nop 15\n" \
  "s_nop 15\n" \
  "s_nop 12\n" \
  "ds_read_b32 v100, v118\n" \
  "buffer_load_dwordx4 v[0:3], v112, %[rs], %[sv1] offen offset:0 sc1\n" \
  "buffer_load_dwordx4 v[4:7], v112, %[rs], %[sv1] offen offset:1024 sc1\n" \
  "buffer_load_dwordx4 v[8:11], v112, %[rs], %[sv1] offen offset:2048 sc1\n" \
  "buffer_load_dwordx4 v[12:15], v112, %[rs], %[sv1] offen offset:3072 sc1\n" \
  "s_memtime vcc\n" \
  "s_waitcnt lgkmcnt(0)\n" \
  "v_readfirstlane_b32 %[cnt], v100\n" \
  "s_nop 0\n" \
  "s_sub_i32 %[cnt], %[cnt], vcc_lo\n" \
  "s_max_i32 %[cnt], %[cnt], 0\n" \
  "s_min_i32 %[cnt], %[cnt], 2000\n" \
  ".Ltg_%=:\n" \
  "s_sub_u32 %[cnt], %[cnt], 28\n" \
  "s_cbranch_scc0 .Ltg_%=\n" \
  "buffer_load_dwordx4 v[16:19], v112, %[rs], %[sv2] offen offset:0 sc1\n" \
  "buffer_load_dwordx4 v[20:23], v112, %[rs], %[sv2] offen offset:1024 sc1\n" \
  "buffer_load_dwordx4 v[24:27], v112, %[rs], %[sv2] offen offset:2048 sc1\n" \
  "buffer_load_dwordx4 v[28:31], v112, %[rs], %[sv2] offen offset:3072 sc1\n" \
  "s_nop 3\n" \
  "buffer_load_dwordx4 v[64:67], v113, %[rs], %[sp1] offen offset:0 sc1\n" \
  "buffer_load_dwordx4 v[68:71], v113, %[rs], %[sp1] offen offset:2048 sc1\n" \
  "buffer_load_dwordx4 v[72:75], v113, %[rs], %[sp2] offen offset:0 sc1\n" \
  "buffer_load_dwordx4 v[76:79], v113, %[rs], %[sp2] offen offset:2048 sc1\n" \
  "buffer_load_dwordx4 v[80:83], v113, %[rs], %[sp1] offen offset:64 sc1\n" \
  "buffer_load_dwordx4 v[84:87], v113, %[rs], %[sp1] offen offset:2112 sc1\n" \
  "buffer_load_dwordx4 v[88:91], v113, %[rs], %[sp2] offen offset:64 sc1\n" \
  "buffer_load_dwordx4 v[92:95], v113, %[rs], %[sp2] offen offset:2112 sc1\n" \
  "s_waitcnt vmcnt(15)\n" \
  "v_permlane16_swap_b32 v0, v1\n" \
  "v_permlane16_swap_b32 v2, v3\n" \
  "s_waitcnt vmcnt(14)\n" \
  "v_permlane16_swap_b32 v4, v5\n" \
  "v_permlane16_swap_b32 v6, v7\n" \
  "s_waitcnt vmcnt(13)\n" \
  "v_permlane16_swap_b32 v8, v9\n" \
  "v_permlane16_swap_b32 v10, v11\n" \
  "s_waitcnt vmcnt(12)\n" \
  "v_permlane16_swap_b32 v12, v13\n" \
  "v_permlane16_swap_b32 v14, v15\n" \
  "s_waitcnt vmcnt(11)\n" \
  "v_permlane16_swap_b32 v16, v17\n" \
  "v_permlane16_swap_b32 v18, v19\n" \
  "s_waitcnt vmcnt(10)\n" \
  "v_permlane16_swap_b32 v20, v21\n" \
  "v_permlane16_swap_b32 v22, v23\n" \
  "s_waitcnt vmcnt(9)\n" \
  "v_permlane16_swap_b32 v24, v25\n" \
  "v_permlane16_swap_b32 v26, v27\n" \
  "s_waitcnt vmcnt(8)\n" \
  "v_permlane16_swap_b32 v28, v29\n" \
  "v_permlane16_swap_b32 v30, v31\n" \
  "s_barrier\n" \
  "ds_read_b128 v[32:35], v114 offset:0\n" \
  "ds_read_b128 v[36:39], v114 offset:1024\n" \
  "ds_read_b128 v[40:43], v114 offset:2048\n" \
  "ds_read_b128 v[44:47], v114 offset:3072\n" \
  "ds_read_b128 v[48:51], v119 offset:0\n" \
  "ds_read_b128 v[52:55], v119 offset:1024\n" \
  "ds_read_b128 v[56:59], v119 offset:2048\n" \
  "ds_read_b128 v[60:63], v119 offset:3072\n" \
  "s_waitcnt vmcnt(7)\n" \
  "v_smfmac_f32_16x16x64_bf16 v[96:99], v[64:67], v[0:7], v117\n" \
  "s_waitcnt vmcnt(6)\n" \
  "v_smfmac_f32_16x16x64_bf16 v[96:99], v[68:71], v[8:15], v117\n" \
  "s_waitcnt vmcnt(5)\n" \
  "v_smfmac_f32_16x16x64_bf16 v[96:99], v[72:75], v[16:23], v117\n" \
  "s_waitcnt vmcnt(4)\n" \
  "v_smfmac_f32_16x16x64_bf16 v[96:99], v[76:79], v[24:31], v117\n" \
  "s_waitcnt vmcnt(3) lgkmcnt(6)\n" \
  "v_smfmac_f32_16x16x64_bf16 v[96:99], v[80:83], v[32:39], v117 abid:1\n" \
  "s_waitcnt vmcnt(2) lgkmcnt(4)\n" \
  "v_smfmac_f32_16x16x64_bf16 v[96:99], v[84:87], v[40:47], v117 abid:1\n" \
  "s_waitcnt vmcnt(1) lgkmcnt(2)\n" \
  "v_smfmac_f32_16x16x64_bf16 v[96:99], v[88:91], v[48:55], v117 abid:1\n" \
  "s_waitcnt vmcnt(0) lgkmcnt(0)\n" \
  "v_smfmac_f32_16x16x64_bf16 v[96:99], v[92:95], v[56:63], v117 abid:1\n" \
  "s_nop 8\n" \
  "v_permlane32_swap_b32 v96, v98\n" \
  "v_permlane32_swap_b32 v97, v99\n" \
  "v_add_f32 v96, v96, v98\n" \
  "v_add_f32 v97, v97, v99\n" \
  "v_cvt_pk_bf16_f32 v100, v96, v97\n" \
  "v_cmp_u_f32 vcc, v96, v97\n" \
  "s_nop 0\n" \
  "v_mov_b32_dpp v101, v100 quad_perm:[1,0,3,2] row_mask:0xf bank_mask:0xf\n" \
  "v_perm_b32 v100, v101, v100, v116\n" \
  "s_cbranch_vccnz .Lbad_%=\n" \
  "buffer_store_dword v100, v115, %[rs], %[sm0] offen sc1\n" \
  "buffer_store_dword v100, v115, %[rs], %[sm1] offen sc1\n" \
  "buffer_store_dword v100, v115, %[rs], %[sm2] offen sc1\n" \
  "buffer_store_dword v100, v115, %[rs], %[sm3] offen sc1\n" \
  ".Lend_%=:\n" \
  "s_memtime %[tp]\n" \
  "v_permlane16_swap_b32 v0, v1\n" \
  "v_permlane16_swap_b32 v2, v3\n" \
  "v_permlane16_swap_b32 v4, v5\n" \
  "v_permlane16_swap_b32 v6, v7\n" \
  "v_permlane16_swap_b32 v8, v9\n" \
  "v_permlane16_swap_b32 v10, v11\n" \
  "v_permlane16_swap_b32 v12, v13\n" \
  "v_permlane16_swap_b32 v14, v15\n" \
  "v_permlane16_swap_b32 v16, v17\n" \
  "v_permlane16_swap_b32 v18, v19\n" \
  "v_permlane16_swap_b32 v20, v21\n" \
  "v_permlane16_swap_b32 v22, v23\n" \
  "v_permlane16_swap_b32 v24, v25\n" \
  "v_permlane16_swap_b32 v26, v27\n" \
  "v_permlane16_swap_b32 v28, v29\n" \
  "v_permlane16_swap_b32 v30, v31\n" \
  "s_waitcnt lgkmcnt(0)\n"
#define ST_Y \
  "s_mov_b32 %[keep], m0\n" \
  "ds_read_b128 v[240:243], v250 offset:0\n" \
  "ds_read_b128 v[244:247], v250 offset:1024\n" \
  "v_mfma_f32_16x16x32_bf16 v[192:195], v[0:3], v[208:211], 0\n" \
  "v_mfma_f32_16x16x32_bf16 v[196:199], v[64:67], v[208:211], 0\n" \
  "ds_read_b128 v[208:211], v252 offset:8192\n" \
  "v_mfma_f32_16x16x32_bf16 v[200:203], v[0:3], v[212:215], 0\n" \
  "v_mfma_f32_16x16x32_bf16 v[204:207], v[64:67], v[212:215], 0\n" \
  "ds_read_b128 v[212:215], v252 offset:9216\n" \
  "v_mfma_f32_16x16x32_bf16 v[192:195], v[128:131], v[216:219], v[192:195]\n" \
  "v_mfma_f32_16x16x32_bf16 v[196:199], v[132:135], v[216:219], v[196:199]\n" \
  "ds_read_b128 v[216:219], v252 offset:10240\n" \
  "v_mfma_f32_16x16x32_bf16 v[200:203], v[128:131], v[220:223], v[200:203]\n" \
  "v_mfma_f32_16x16x32_bf16 v[204:207], v[132:135], v[220:223], v[204:207]\n" \
  "ds_read_b128 v[220:223], v252 offset:11264\n" \
  "v_mfma_f32_16x16x32_bf16 v[192:195], v[4:7], v[224:227], v[192:195]\n" \
  "v_mfma_f32_16x16x32_bf16 v[196:199], v[68:71], v[224:227], v[196:199]\n" \
  "ds_read_b128 v[224:227], v252 offset:12288\n" \
  "v_mfma_f32_16x16x32_bf16 v[200:203], v[4:7], v[228:231], v[200:203]\n" \
  "v_mfma_f32_16x16x32_bf16 v[204:207], v[68:71], v[228:231], v[204:207]\n" \
  "ds_read_b128 v[228:231], v252 offset:13312\n" \
  "s_waitcnt lgkmcnt(7)\n" \
  "v_mfma_f32_16x16x32_bf16 v[192:195], v[240:243], v[232:235], v[192:195]\n" \
  "s_waitcnt lgkmcnt(6)\n" \
  "v_mfma_f32_16x16x32_bf16 v[196:199], v[244:247], v[232:235], v[196:199]\n" \
  "ds_read_b128 v[232:235], v252 offset:14336\n" \
  "v_mfma_f32_16x16x32_bf16 v[200:203], v[240:243], v[236:239], v[200:203]\n" \
  "v_mfma_f32_16x16x32_bf16 v[204:207], v[244:247], v[236:239], v[204:207]\n" \
  "ds_read_b128 v[236:239], v252 offset:15360\n" \
  "ds_read_b128 v[240:243], v250 offset:16384\n" \
  "ds_read_b128 v[244:247], v250 offset:17408\n" \
  "s_waitcnt lgkmcnt(9)\n" \
  "v_mfma_f32_16x16x32_bf16 v[192:195], v[8:11], v[208:211], v[192:195]\n" \
  "v_mfma_f32_16x16x32_bf16 v[196:199], v[72:75], v[208:211], v[196:199]\n" \
  "s_waitcnt lgkmcnt(8)\n" \
  "v_mfma_f32_16x16x32_bf16 v[200:203], v[8:11], v[212:215], v[200:203]\n" \
  "v_mfma_f32_16x16x32_bf16 v[204:207], v[72:75], v[212:215], v[204:207]\n" \
  "s_waitcnt vmcnt(2)\n" \
  "s_waitcnt lgkmcnt(0)\n" \
  "s_barrier\n" \
  "ds_read_b128 v[208:211], v252 offset:16384\n" \
  "ds_read_b128 v[212:215], v252 offset:17408\n" \
  "s_add_u32 m0, %[mw], 0\n" \
  "s_xor_b32 %[cnt], %[sc], 512\n" \
  "buffer_load_dwordx4 v248, %[rx], %[cnt] offen lds\n" \
  "s_add_u32 m0, %[mw], 8192\n" \
  "s_xor_b32 %[cnt], %[sc], 640\n" \
  "buffer_load_dwordx4 v248, %[rx], %[cnt] offen lds\n" \
  "v_mfma_f32_16x16x32_bf16 v[192:195], v[136:139], v[216:219], v[192:195]\n" \
  "v_mfma_f32_16x16x32_bf16 v[196:199], v[140:143], v[216:219], v[196:199]\n" \
  "ds_read_b128 v[216:219], v252 offset:18432\n" \
  "v_mfma_f32_16x16x32_bf16 v[200:203], v[136:139], v[220:223], v[200:203]\n" \
  "v_mfma_f32_16x16x32_bf16 v[204:207], v[140:143], v[220:223], v[204:207]\n" \
  "ds_read_b128 v[220:223], v252 offset:19456\n" \
  "v_mfma_f32_16x16x32_bf16 v[192:195], v[12:15], v[224:227], v[192:195]\n" \
  "v_mfma_f32_16x16x32_bf16 v[196:199], v[76:79], v[224:227], v[196:199]\n" \
  "ds_read_b128 v[224:227], v252 offset:20480\n" \
  "v_mfma_f32_16x16x32_bf16 v[200:203], v[12:15], v[228:231], v[200:203]\n" \
  "v_mfma_f32_16x16x32_bf16 v[204:207], v[76:79], v[228:231], v[204:207]\n" \
  "ds_read_b128 v[228:231], v252 offset:21504\n" \
  "v_mfma_f32_16x16x32_bf16 v[192:195], v[240:243], v[232:235], v[192:195]\n" \
  "v_mfma_f32_16x16x32_bf16 v[196:199], v[244:247], v[232:235], v[196:199]\n" \
  "ds_read_b128 v[232:235], v252 offset:22528\n" \
  "v_mfma_f32_16x16x32_bf16 v[200:203], v[240:243], v[236:239], v[200:203]\n" \
  "v_mfma_f32_16x16x32_bf16 v[204:207], v[244:247], v[236:239], v[204:207]\n" \
  "ds_read_b128 v[236:239], v252 offset:23552\n" \
  "ds_read_b128 v[240:243], v250 offset:32768\n" \
  "ds_read_b128 v[244:247], v250 offset:33792\n" \
  "s_waitcnt lgkmcnt(9)\n" \
  "v_mfma_f32_16x16x32_bf16 v[192:195], v[16:19], v[208:211], v[192:195]\n" \
  "v_mfma_f32_16x16x32_bf16 v[196:199], v[80:83], v[208:211], v[196:199]\n" \
  "ds_read_b128 v[208:211], v252 offset:24576\n" \
  "s_waitcnt lgkmcnt(9)\n" \
  "v_mfma_f32_16x16x32_bf16 v[200:203], v[16:19], v[212:215], v[200:203]\n" \
  "v_mfma_f32_16x16x32_bf16 v[204:207], v[80:83], v[212:215], v[204:207]\n" \
  "ds_read_b128 v[212:215], v252 offset:25600\n" \
  "s_waitcnt lgkmcnt(9)\n" \
  "v_mfma_f32_16x16x32_bf16 v[192:195], v[144:147], v[216:219], v[192:195]\n" \
  "v_mfma_f32_16x16x32_bf16 v[196:199], v[148:151], v[216:219], v[196:199]\n" \
  "ds_read_b128 v[216:219], v252 offset:26624\n" \
  "s_waitcnt lgkmcnt(9)\n" \
  "v_mfma_f32_16x16x32_bf16 v[200:203], v[144:147], v[220:223], v[200:203]\n" \
  "v_mfma_f32_16x16x32_bf16 v[204:207], v[148:151], v[220:223], v[204:207]\n" \
  "ds_read_b128 v[220:223], v252 offset:27648\n" \
  "s_waitcnt lgkmcnt(9)\n" \
  "v_mfma_f32_16x16x32_bf16 v[192:195], v[20:23], v[224:227], v[192:195]\n" \
  "v_mfma_f32_16x16x32_bf16 v[196:199], v[84:87], v[224:227], v[196:199]\n" \
  "ds_read_b128 v[224:227], v252 offset:28672\n" \
  "s_waitcnt lgkmcnt(9)\n" \
  "v_mfma_f32_16x16x32_bf16 v[200:203], v[20:23], v[228:231], v[200:203]\n" \
  "v_mfma_f32_16x16x32_bf16 v[204:207], v[84:87], v[228:231], v[204:207]\n" \
  "ds_read_b128 v[228:231], v252 offset:29696\n" \
  "s_waitcnt lgkmcnt(7)\n" \
  "v_mfma_f32_16x16x32_bf16 v[192:195], v[240:243], v[232:235], v[192:195]\n" \
  "s_waitcnt lgkmcnt(6)\n" \
  "v_mfma_f32_16x16x32_bf16 v[196:199], v[244:247], v[232:235], v[196:199]\n" \
  "ds_read_b128 v[232:235], v252 offset:30720\n" \
  "v_mfma_f32_16x16x32_bf16 v[200:203], v[240:243], v[236:239], v[200:203]\n" \
  "v_mfma_f32_16x16x32_bf16 v[204:207], v[244:247], v[236:239], v[204:207]\n" \
  "ds_read_b128 v[236:239], v252 offset:31744\n" \
  "ds_read_b128 v[240:243], v250 offset:49152\n" \
  "ds_read_b128 v[244:247], v250 offset:50176\n" \
  "s_waitcnt lgkmcnt(9)\n" \
  "v_mfma_f32_16x16x32_bf16 v[192:195], v[24:27], v[208:211], v[192:195]\n" \
  "v_mfma_f32_16x16x32_bf16 v[196:199], v[88:91], v[208:211], v[196:199]\n" \
  "s_waitcnt lgkmcnt(8)\n" \
  "v_mfma_f32_16x16x32_bf16 v[200:203], v[24:27], v[212:215], v[200:203]\n" \
  "v_mfma_f32_16x16x32_bf16 v[204:207], v[88:91], v[212:215], v[204:207]\n" \
  "s_waitcnt vmcnt(0)\n" \
  "s_waitcnt lgkmcnt(0)\n" \
  "s_barrier\n" \
  "ds_read_b128 v[208:211], v252 offset:0\n" \
  "ds_read_b128 v[212:215], v252 offset:1024\n" \
  "s_add_u32 m0, %[mw], 16384\n" \
  "s_xor_b32 %[cnt], %[sc], 768\n" \
  "buffer_load_dwordx4 v248, %[rx], %[cnt] offen lds\n" \
  "s_add_u32 m0, %[mw], 24576\n" \
  "s_xor_b32 %[cnt], %[sc], 896\n" \
  "buffer_load_dwordx4 v248, %[rx], %[cnt] offen lds\n" \
  "v_lshl_or_b32 v253, v251, 24, %[fm]\n" \
  "buffer_store_dword v253, v253, %[rs], %[fo] offen sc1\n" \
  "v_mfma_f32_16x16x32_bf16 v[192:195], v[152:155], v[216:219], v[192:195]\n" \
  "v_mfma_f32_16x16x32_bf16 v[196:199], v[156:159], v[216:219], v[196:199]\n" \
  "ds_read_b128 v[216:219], v252 offset:2048\n" \
  "v_mfma_f32_16x16x32_bf16 v[200:203], v[152:155], v[220:223], v[200:203]\n" \
  "v_mfma_f32_16x16x32_bf16 v[204:207], v[156:159], v[220:223], v[204:207]\n" \
  "ds_read_b128 v[220:223], v252 offset:3072\n" \
  "v_mfma_f32_16x16x32_bf16 v[192:195], v[28:31], v[224:227], v[192:195]\n" \
  "v_mfma_f32_16x16x32_bf16 v[196:199], v[92:95], v[224:227], v[196:199]\n" \
  "ds_read_b128 v[224:227], v252 offset:4096\n" \
  "v_mfma_f32_16x16x32_bf16 v[200:203], v[28:31], v[228:231], v[200:203]\n" \
  "v_mfma_f32_16x16x32_bf16 v[204:207], v[92:95], v[228:231], v[204:207]\n" \
  "ds_read_b128 v[228:231], v252 offset:5120\n" \
  "v_mfma_f32_16x16x32_bf16 v[192:195], v[240:243], v[232:235], v[192:195]\n" \
  "v_mfma_f32_16x16x32_bf16 v[196:199], v[244:247], v[232:235], v[196:199]\n" \
  "ds_read_b128 v[232:235], v252 offset:6144\n" \
  "v_mfma_f32_16x16x32_bf16 v[200:203], v[240:243], v[236:239], v[200:203]\n" \
  "v_mfma_f32_16x16x32_bf16 v[204:207], v[244:247], v[236:239], v[204:207]\n" \
  "ds_read_b128 v[236:239], v252 offset:7168\n" \
  "ds_read_b128 v[240:243], v249 offset:0\n" \
  "ds_read_b128 v[244:247], v249 offset:1024\n" \
  "s_waitcnt lgkmcnt(9)\n" \
  "v_mfma_f32_16x16x32_bf16 v[192:195], v[32:35], v[208:211], v[192:195]\n" \
  "v_mfma_f32_16x16x32_bf16 v[196:199], v[96:99], v[208:211], v[196:199]\n" \
  "ds_read_b128 v[208:211], v252 offset:8192\n" \
  "s_waitcnt lgkmcnt(9)\n" \
  "v_mfma_f32_16x16x32_bf16 v[200:203], v[32:35], v[212:215], v[200:203]\n" \
  "v_mfma_f32_16x16x32_bf16 v[204:207], v[96:99], v[212:215], v[204:207]\n" \
  "ds_read_b128 v[212:215], v252 offset:9216\n" \
  "s_waitcnt lgkmcnt(9)\n" \
  "v_mfma_f32_16x16x32_bf16 v[192:195], v[160:163], v[216:219], v[192:195]\n" \
  "v_mfma_f32_16x16x32_bf16 v[196:199], v[164:167], v[216:219], v[196:199]\n" \
  "ds_read_b128 v[216:219], v252 offset:10240\n" \
  "s_waitcnt lgkmcnt(9)\n" \
  "v_mfma_f32_16x16x32_bf16 v[200:203], v[160:163], v[220:223], v[200:203]\n" \
  "v_mfma_f32_16x16x32_bf16 v[204:207], v[164:167], v[220:223], v[204:207]\n" \
  "ds_read_b128 v[220:223], v252 offset:11264\n" \
  "s_waitcnt lgkmcnt(9)\n" \
  "v_mfma_f32_16x16x32_bf16 v[192:195], v[36:39], v[224:227], v[192:195]\n" \
  "v_mfma_f32_16x16x32_bf16 v[196:199], v[100:103], v[224:227], v[196:199]\n" \
  "ds_read_b128 v[224:227], v252 offset:12288\n" \
  "s_waitcnt lgkmcnt(9)\n" \
  "v_mfma_f32_16x16x32_bf16 v[200:203], v[36:39], v[228:231], v[200:203]\n" \
  "v_mfma_f32_16x16x32_bf16 v[204:207], v[100:103], v[228:231], v[204:207]\n" \
  "ds_read_b128 v[228:231], v252 offset:13312\n" \
  "s_waitcnt lgkmcnt(7)\n" \
  "v_mfma_f32_16x16x32_bf16 v[192:195], v[240:243], v[232:235], v[192:195]\n" \
  "s_waitcnt lgkmcnt(6)\n" \
  "v_mfma_f32_16x16x32_bf16 v[196:199], v[244:247], v[232:235], v[196:199]\n" \
  "ds_read_b128 v[232:235], v252 offset:14336\n" \
  "v_mfma_f32_16x16x32_bf16 v[200:203], v[240:243], v[236:239], v[200:203]\n" \
  "v_mfma_f32_16x16x32_bf16 v[204:207], v[244:247], v[236:239], v[204:207]\n" \
  "ds_read_b128 v[236:239], v252 offset:15360\n" \
  "ds_read_b128 v[240:243], v249 offset:16384\n" \
  "ds_read_b128 v[244:247], v249 offset:17408\n" \
  "s_waitcnt lgkmcnt(9)\n" \
  "v_mfma_f32_16x16x32_bf16 v[192:195], v[40:43], v[208:211], v[192:195]\n" \
  "v_mfma_f32_16x16x32_bf16 v[196:199], v[104:107], v[208:211], v[196:199]\n" \
  "s_waitcnt lgkmcnt(8)\n" \
  "v_mfma_f32_16x16x32_bf16 v[200:203], v[40:43], v[212:215], v[200:203]\n" \
  "v_mfma_f32_16x16x32_bf16 v[204:207], v[104:107], v[212:215], v[204:207]\n" \
  "s_waitcnt vmcnt(1)\n" \
  "s_waitcnt lgkmcnt(0)\n" \
  "s_barrier\n" \
  "ds_read_b128 v[208:211], v252 offset:16384\n" \
  "ds_read_b128 v[212:215], v252 offset:17408\n" \
  "s_add_u32 m0, %[mw], 0\n" \
  "s_xor_b32 %[cnt], %[sn], 0\n" \
  "buffer_load_dwordx4 v248, %[rx], %[cnt] offen lds\n" \
  "s_add_u32 m0, %[mw], 8192\n" \
  "s_xor_b32 %[cnt], %[sn], 128\n" \
  "buffer_load_dwordx4 v248, %[rx], %[cnt] offen lds\n" \
  "v_lshl_or_b32 v253, v251, 3, %[tm]\n" \
  "buffer_load_dword v253, v253, %[rx], %[tc] offen\n" \
  "v_mfma_f32_16x16x32_bf16 v[192:195], v[168:171], v[216:219], v[192:195]\n" \
  "v_mfma_f32_16x16x32_bf16 v[196:199], v[172:175], v[216:219], v[196:199]\n" \
  "ds_read_b128 v[216:219], v252 offset:18432\n" \
  "v_mfma_f32_16x16x32_bf16 v[200:203], v[168:171], v[220:223], v[200:203]\n" \
  "v_mfma_f32_16x16x32_bf16 v[204:207], v[172:175], v[220:223], v[204:207]\n" \
  "ds_read_b128 v[220:223], v252 offset:19456\n" \
  "v_mfma_f32_16x16x32_bf16 v[192:195], v[44:47], v[224:227], v[192:195]\n" \
  "v_mfma_f32_16x16x32_bf16 v[196:199], v[108:111], v[224:227], v[196:199]\n" \
  "ds_read_b128 v[224:227], v252 offset:20480\n" \
  "v_mfma_f32_16x16x32_bf16 v[200:203], v[44:47], v[228:231], v[200:203]\n" \
  "v_mfma_f32_16x16x32_bf16 v[204:207], v[108:111], v[228:231], v[204:207]\n" \
  "ds_read_b128 v[228:231], v252 offset:21504\n" \
  "v_mfma_f32_16x16x32_bf16 v[192:195], v[240:243], v[232:235], v[192:195]\n" \
  "v_mfma_f32_16x16x32_bf16 v[196:199], v[244:247], v[232:235], v[196:199]\n" \
  "ds_read_b128 v[232:235], v252 offset:22528\n" \
  "v_mfma_f32_16x16x32_bf16 v[200:203], v[240:243], v[236:239], v[200:203]\n" \
  "v_mfma_f32_16x16x32_bf16 v[204:207], v[244:247], v[236:239], v[204:207]\n" \
  "ds_read_b128 v[236:239], v252 offset:23552\n" \
  "ds_read_b128 v[240:243], v249 offset:32768\n" \
  "ds_read_b128 v[244:247], v249 offset:33792\n" \
  "s_waitcnt lgkmcnt(9)\n" \
  "v_mfma_f32_16x16x32_bf16 v[192:195], v[48:51], v[208:211], v[192:195]\n" \
  "v_mfma_f32_16x16x32_bf16 v[196:199], v[112:115], v[208:211], v[196:199]\n" \
  "ds_read_b128 v[208:211], v252 offset:24576\n" \
  "s_waitcnt lgkmcnt(9)\n" \
  "v_mfma_f32_16x16x32_bf16 v[200:203], v[48:51], v[212:215], v[200:203]\n" \
  "v_mfma_f32_16x16x32_bf16 v[204:207], v[112:115], v[212:215], v[204:207]\n" \
  "ds_read_b128 v[212:215], v252 offset:25600\n" \
  "s_waitcnt lgkmcnt(9)\n" \
  "v_mfma_f32_16x16x32_bf16 v[192:195], v[176:179], v[216:219], v[192:195]\n" \
  "v_mfma_f32_16x16x32_bf16 v[196:199], v[180:183], v[216:219], v[196:199]\n" \
  "ds_read_b128 v[216:219], v252 offset:26624\n" \
  "s_waitcnt lgkmcnt(9)\n" \
  "v_mfma_f32_16x16x32_bf16 v[200:203], v[176:179], v[220:223], v[200:203]\n" \
  "v_mfma_f32_16x16x32_bf16 v[204:207], v[180:183], v[220:223], v[204:207]\n" \
  "ds_read_b128 v[220:223], v252 offset:27648\n" \
  "s_waitcnt lgkmcnt(9)\n" \
  "v_mfma_f32_16x16x32_bf16 v[192:195], v[52:55], v[224:227], v[192:195]\n" \
  "v_mfma_f32_16x16x32_bf16 v[196:199], v[116:119], v[224:227], v[196:199]\n" \
  "ds_read_b128 v[224:227], v252 offset:28672\n" \
  "s_waitcnt lgkmcnt(9)\n" \
  "v_mfma_f32_16x16x32_bf16 v[200:203], v[52:55], v[228:231], v[200:203]\n" \
  "v_mfma_f32_16x16x32_bf16 v[204:207], v[116:119], v[228:231], v[204:207]\n" \
  "ds_read_b128 v[228:231], v252 offset:29696\n" \
  "s_waitcnt lgkmcnt(7)\n" \
  "v_mfma_f32_16x16x32_bf16 v[192:195], v[240:243], v[232:235], v[192:195]\n" \
  "s_waitcnt lgkmcnt(6)\n" \
  "v_mfma_f32_16x16x32_bf16 v[196:199], v[244:247], v[232:235], v[196:199]\n" \
  "ds_read_b128 v[232:235], v252 offset:30720\n" \
  "v_mfma_f32_16x16x32_bf16 v[200:203], v[240:243], v[236:239], v[200:203]\n" \
  "v_mfma_f32_16x16x32_bf16 v[204:207], v[244:247], v[236:239], v[204:207]\n" \
  "ds_read_b128 v[236:239], v252 offset:31744\n" \
  "ds_read_b128 v[240:243], v249 offset:49152\n" \
  "ds_read_b128 v[244:247], v249 offset:50176\n" \
  "s_waitcnt lgkmcnt(9)\n" \
  "v_mfma_f32_16x16x32_bf16 v[192:195], v[56:59], v[208:211], v[192:195]\n" \
  "v_mfma_f32_16x16x32_bf16 v[196:199], v[120:123], v[208:211], v[196:199]\n" \
  "s_waitcnt lgkmcnt(8)\n" \
  "v_mfma_f32_16x16x32_bf16 v[200:203], v[56:59], v[212:215], v[200:203]\n" \
  "v_mfma_f32_16x16x32_bf16 v[204:207], v[120:123], v[212:215], v[204:207]\n" \
  "s_waitcnt vmcnt(1)\n" \
  "s_waitcnt lgkmcnt(0)\n" \
  "s_barrier\n" \
  "ds_read_b128 v[208:211], v252 offset:0\n" \
  "ds_read_b128 v[212:215], v252 offset:1024\n" \
  "s_add_u32 m0, %[mw], 16384\n" \
  "s_xor_b32 %[cnt], %[sn], 256\n" \
  "buffer_load_dwordx4 v248, %[rx], %[cnt] offen lds\n" \
  "s_add_u32 m0, %[mw], 24576\n" \
  "s_xor_b32 %[cnt], %[sn], 384\n" \
  "buffer_load_dwordx4 v248, %[rx], %[cnt] offen lds\n" \
  "v_mfma_f32_16x16x32_bf16 v[192:195], v[184:187], v[216:219], v[192:195]\n" \
  "v_mfma_f32_16x16x32_bf16 v[196:199], v[188:191], v[216:219], v[196:199]\n" \
  "ds_read_b128 v[216:219], v252 offset:2048\n" \
  "v_mfma_f32_16x16x32_bf16 v[200:203], v[184:187], v[220:223], v[200:203]\n" \
  "v_mfma_f32_16x16x32_bf16 v[204:207], v[188:191], v[220:223], v[204:207]\n" \
  "ds_read_b128 v[220:223], v252 offset:3072\n" \
  "v_mfma_f32_16x16x32_bf16 v[192:195], v[60:63], v[224:227], v[192:195]\n" \
  "v_mfma_f32_16x16x32_bf16 v[196:199], v[124:127], v[224:227], v[196:199]\n" \
  "ds_read_b128 v[224:227], v252 offset:4096\n" \
  "v_mfma_f32_16x16x32_bf16 v[200:203], v[60:63], v[228:231], v[200:203]\n" \
  "v_mfma_f32_16x16x32_bf16 v[204:207], v[124:127], v[228:231], v[204:207]\n" \
  "ds_read_b128 v[228:231], v252 offset:5120\n" \
  "v_mfma_f32_16x16x32_bf16 v[192:195], v[240:243], v[232:235], v[192:195]\n" \
  "v_mfma_f32_16x16x32_bf16 v[196:199], v[244:247], v[232:235], v[196:199]\n" \
  "ds_read_b128 v[232:235], v252 offset:6144\n" \
  "v_mfma_f32_16x16x32_bf16 v[200:203], v[240:243], v[236:239], v[200:203]\n" \
  "v_mfma_f32_16x16x32_bf16 v[204:207], v[244:247], v[236:239], v[204:207]\n" \
  "ds_read_b128 v[236:239], v252 offset:7168\n" \
  "s_cmp_eq_u32 %[cs], 0\n" \
  "s_cbranch_scc1 .Lq_%=\n" \
  "v_mul_f32 v192, %[cs], v192\n" \
  "v_mul_f32 v193, %[cs], v193\n" \
  "v_mul_f32 v194, %[cs], v194\n" \
  "v_mul_f32 v195, %[cs], v195\n" \
  "v_mul_f32 v196, %[cs], v196\n" \
  "v_mul_f32 v197, %[cs], v197\n" \
  "v_mul_f32 v198, %[cs], v198\n" \
  "v_mul_f32 v199, %[cs], v199\n" \
  "v_cvt_pk_bf16_f32 v192, v192, v193\n" \
  "v_cvt_pk_bf16_f32 v193, v196, v197\n" \
  "v_cvt_pk_bf16_f32 v194, v194, v195\n" \
  "v_cvt_pk_bf16_f32 v195, v198, v199\n" \
  "buffer_store_dwordx4 v[192:195], v251, %[rs], %[so] offen sc1\n" \
  "v_mul_f32 v200, %[cs], v200\n" \
  "v_mul_f32 v201, %[cs], v201\n" \
  "v_mul_f32 v202, %[cs], v202\n" \
  "v_mul_f32 v203, %[cs], v203\n" \
  "v_mul_f32 v204, %[cs], v204\n" \
  "v_mul_f32 v205, %[cs], v205\n" \
  "v_mul_f32 v206, %[cs], v206\n" \
  "v_mul_f32 v207, %[cs], v207\n" \
  "v_cvt_pk_bf16_f32 v200, v200, v201\n" \
  "v_cvt_pk_bf16_f32 v201, v204, v205\n" \
  "v_cvt_pk_bf16_f32 v202, v202, v203\n" \
  "v_cvt_pk_bf16_f32 v203, v206, v207\n" \
  "s_add_u32 %[cnt], %[so], %c[rt]\n" \
  "buffer_store_dwordx4 v[200:203], v251, %[rs], %[cnt] offen sc1\n" \
  "s_branch .Lqe_%=\n" \
  ".Lq_%=:\n" \
  "v_cvt_pk_bf16_f32 v192, v192, v193\n" \
  "v_cvt_pk_bf16_f32 v193, v196, v197\n" \
  "v_cvt_pk_bf16_f32 v194, v194, v195\n" \
  "v_cvt_pk_bf16_f32 v195, v198, v199\n" \
  "buffer_store_dwordx4 v[192:195], v251, %[rs], %[so] offen sc1\n" \
  "v_cvt_pk_bf16_f32 v200, v200, v201\n" \
  "v_cvt_pk_bf16_f32 v201, v204, v205\n" \
  "v_cvt_pk_bf16_f32 v202, v202, v203\n" \
  "v_cvt_pk_bf16_f32 v203, v206, v207\n" \
  "s_add_u32 %[cnt], %[so], %c[rt]\n" \
  "buffer_store_dwordx4 v[200:203], v251, %[rs], %[cnt] offen sc1\n" \
  ".Lqe_%=:\n" \
  "s_mov_b32 m0, %[keep]\n" \
  "s_waitcnt lgkmcnt(0)\n"
#define ST_XA \
  "s_mov_b32 %[keep], m0\n" \
  "ds_read_b128 v[240:243], v250 offset:0\n" \
  "ds_read_b128 v[244:247], v250 offset:1024\n" \
  "v_mfma_f32_16x16x32_bf16 v[192:195], v[0:3], v[208:211], 0\n" \
  "v_mfma_f32_16x16x32_bf16 v[196:199], v[64:67], v[208:211], 0\n" \
  "ds_read_b128 v[208:211], v252 offset:8192\n" \
  "v_mfma_f32_16x16x32_bf16 v[200:203], v[0:3], v[212:215], 0\n" \
  "v_mfma_f32_16x16x32_bf16 v[204:207], v[64:67], v[212:215], 0\n" \
  "ds_read_b128 v[212:215], v252 offset:9216\n" \
  "v_mfma_f32_16x16x32_bf16 v[192:195], v[128:131], v[216:219], v[192:195]\n" \
  "v_mfma_f32_16x16x32_bf16 v[196:199], v[132:135], v[216:219], v[196:199]\n" \
  "ds_read_b128 v[216:219], v252 offset:10240\n" \
  "v_mfma_f32_16x16x32_bf16 v[200:203], v[128:131], v[220:223], v[200:203]\n" \
  "v_mfma_f32_16x16x32_bf16 v[204:207], v[132:135], v[220:223], v[204:207]\n" \
  "ds_read_b128 v[220:223], v252 offset:11264\n" \
  "v_mfma_f32_16x16x32_bf16 v[192:195], v[4:7], v[224:227], v[192:195]\n" \
  "v_mfma_f32_16x16x32_bf16 v[196:199], v[68:71], v[224:227], v[196:199]\n" \
  "v_mfma_f32_16x16x32_bf16 v[200:203], v[4:7], v[228:231], v[200:203]\n" \
  "v_mfma_f32_16x16x32_bf16 v[204:207], v[68:71], v[228:231], v[204:207]\n" \
  "s_waitcnt vmcnt(0)\n" \
  "s_waitcnt lgkmcnt(0)\n" \
  "s_barrier\n" \
  "ds_read_b128 v[224:227], v252 offset:12288\n" \
  "ds_read_b128 v[228:231], v252 offset:13312\n" \
  "s_add_u32 m0, %[mw], 0\n" \
  "s_xor_b32 %[cnt], %[sc], 384\n" \
  "buffer_load_dwordx4 v248, %[rx], %[cnt] offen lds\n" \
  "s_add_u32 m0, %[mw], 4096\n" \
  "s_xor_b32 %[cnt], %[sc], 448\n" \
  "buffer_load_dwordx4 v248, %[rx], %[cnt] offen lds\n" \
  "v_mfma_f32_16x16x32_bf16 v[192:195], v[240:243], v[232:235], v[192:195]\n" \
  "v_mfma_f32_16x16x32_bf16 v[196:199], v[244:247], v[232:235], v[196:199]\n" \
  "ds_read_b128 v[232:235], v252 offset:14336\n" \
  "v_mfma_f32_16x16x32_bf16 v[200:203], v[240:243], v[236:239], v[200:203]\n" \
  "v_mfma_f32_16x16x32_bf16 v[204:207], v[244:247], v[236:239], v[204:207]\n" \
  "ds_read_b128 v[236:239], v252 offset:15360\n" \
  "ds_read_b128 v[240:243], v250 offset:16384\n" \
  "ds_read_b128 v[244:247], v250 offset:17408\n" \
  "v_mfma_f32_16x16x32_bf16 v[192:195], v[8:11], v[208:211], v[192:195]\n" \
  "v_mfma_f32_16x16x32_bf16 v[196:199], v[72:75], v[208:211], v[196:199]\n" \
  "ds_read_b128 v[208:211], v252 offset:16384\n" \
  "v_mfma_f32_16x16x32_bf16 v[200:203], v[8:11], v[212:215], v[200:203]\n" \
  "v_mfma_f32_16x16x32_bf16 v[204:207], v[72:75], v[212:215], v[204:207]\n" \
  "ds_read_b128 v[212:215], v252 offset:17408\n" \
  "v_mfma_f32_16x16x32_bf16 v[192:195], v[136:139], v[216:219], v[192:195]\n" \
  "v_mfma_f32_16x16x32_bf16 v[196:199], v[140:143], v[216:219], v[196:199]\n" \
  "ds_read_b128 v[216:219], v252 offset:18432\n" \
  "v_mfma_f32_16x16x32_bf16 v[200:203], v[136:139], v[220:223], v[200:203]\n" \
  "v_mfma_f32_16x16x32_bf16 v[204:207], v[140:143], v[220:223], v[204:207]\n" \
  "ds_read_b128 v[220:223], v252 offset:19456\n" \
  "s_waitcnt lgkmcnt(9)\n" \
  "v_mfma_f32_16x16x32_bf16 v[192:195], v[12:15], v[224:227], v[192:195]\n" \
  "v_mfma_f32_16x16x32_bf16 v[196:199], v[76:79], v[224:227], v[196:199]\n" \
  "ds_read_b128 v[224:227], v252 offset:20480\n" \
  "s_waitcnt lgkmcnt(9)\n" \
  "v_mfma_f32_16x16x32_bf16 v[200:203], v[12:15], v[228:231], v[200:203]\n" \
  "v_mfma_f32_16x16x32_bf16 v[204:207], v[76:79], v[228:231], v[204:207]\n" \
  "ds_read_b128 v[228:231], v252 offset:21504\n" \
  "s_waitcnt lgkmcnt(7)\n" \
  "v_mfma_f32_16x16x32_bf16 v[192:195], v[240:243], v[232:235], v[192:195]\n" \
  "s_waitcnt lgkmcnt(6)\n" \
  "v_mfma_f32_16x16x32_bf16 v[196:199], v[244:247], v[232:235], v[196:199]\n" \
  "ds_read_b128 v[232:235], v252 offset:22528\n" \
  "v_mfma_f32_16x16x32_bf16 v[200:203], v[240:243], v[236:239], v[200:203]\n" \
  "v_mfma_f32_16x16x32_bf16 v[204:207], v[244:247], v[236:239], v[204:207]\n" \
  "ds_read_b128 v[236:239], v252 offset:23552\n" \
  "ds_read_b128 v[240:243], v250 offset:32768\n" \
  "ds_read_b128 v[244:247], v250 offset:33792\n" \
  "s_waitcnt lgkmcnt(9)\n" \
  "v_mfma_f32_16x16x32_bf16 v[192:195], v[16:19], v[208:211], v[192:195]\n" \
  "v_mfma_f32_16x16x32_bf16 v[196:199], v[80:83], v[208:211], v[196:199]\n" \
  "s_waitcnt lgkmcnt(8)\n" \
  "v_mfma_f32_16x16x32_bf16 v[200:203], v[16:19], v[212:215], v[200:203]\n" \
  "v_mfma_f32_16x16x32_bf16 v[204:207], v[80:83], v[212:215], v[204:207]\n" \
  "s_waitcnt vmcnt(0)\n" \
  "s_waitcnt lgkmcnt(0)\n" \
  "s_barrier\n" \
  "ds_read_b128 v[208:211], v252 offset:0\n" \
  "ds_read_b128 v[212:215], v252 offset:1024\n" \
  "s_add_u32 m0, %[mw], 12288\n" \
  "s_xor_b32 %[cnt], %[sc], 512\n" \
  "buffer_load_dwordx4 v248, %[rx], %[cnt] offen lds\n" \
  "s_add_u32 m0, %[mw], 16384\n" \
  "s_xor_b32 %[cnt], %[sc], 576\n" \
  "buffer_load_dwordx4 v248, %[rx], %[cnt] offen lds\n" \
  "s_add_u32 m0, %[mw], 20480\n" \
  "s_xor_b32 %[cnt], %[sc], 640\n" \
  "buffer_load_dwordx4 v248, %[rx], %[cnt] offen lds\n" \
  "v_lshl_or_b32 v253, v251, 24, %[fm]\n" \
  "buffer_store_dword v253, v253, %[rs], %[fo] offen sc1\n" \
  "v_mfma_f32_16x16x32_bf16 v[192:195], v[144:147], v[216:219], v[192:195]\n" \
  "v_mfma_f32_16x16x32_bf16 v[196:199], v[148:151], v[216:219], v[196:199]\n" \
  "ds_read_b128 v[216:219], v252 offset:2048\n" \
  "v_mfma_f32_16x16x32_bf16 v[200:203], v[144:147], v[220:223], v[200:203]\n" \
  "v_mfma_f32_16x16x32_bf16 v[204:207], v[148:151], v[220:223], v[204:207]\n" \
  "ds_read_b128 v[220:223], v252 offset:3072\n" \
  "v_mfma_f32_16x16x32_bf16 v[192:195], v[20:23], v[224:227], v[192:195]\n" \
  "v_mfma_f32_16x16x32_bf16 v[196:199], v[84:87], v[224:227], v[196:199]\n" \
  "ds_read_b128 v[224:227], v252 offset:4096\n" \
  "v_mfma_f32_16x16x32_bf16 v[200:203], v[20:23], v[228:231], v[200:203]\n" \
  "v_mfma_f32_16x16x32_bf16 v[204:207], v[84:87], v[228:231], v[204:207]\n" \
  "ds_read_b128 v[228:231], v252 offset:5120\n" \
  "v_mfma_f32_16x16x32_bf16 v[192:195], v[240:243], v[232:235], v[192:195]\n" \
  "v_mfma_f32_16x16x32_bf16 v[196:199], v[244:247], v[232:235], v[196:199]\n" \
  "ds_read_b128 v[232:235], v252 offset:6144\n" \
  "v_mfma_f32_16x16x32_bf16 v[200:203], v[240:243], v[236:239], v[200:203]\n" \
  "v_mfma_f32_16x16x32_bf16 v[204:207], v[244:247], v[236:239], v[204:207]\n" \
  "ds_read_b128 v[236:239], v252 offset:7168\n" \
  "ds_read_b128 v[240:243], v250 offset:49152\n" \
  "ds_read_b128 v[244:247], v250 offset:50176\n" \
  "s_waitcnt lgkmcnt(9)\n" \
  "v_mfma_f32_16x16x32_bf16 v[192:195], v[24:27], v[208:211], v[192:195]\n" \
  "v_mfma_f32_16x16x32_bf16 v[196:199], v[88:91], v[208:211], v[196:199]\n" \
  "s_waitcnt lgkmcnt(8)\n" \
  "v_mfma_f32_16x16x32_bf16 v[200:203], v[24:27], v[212:215], v[200:203]\n" \
  "v_mfma_f32_16x16x32_bf16 v[204:207], v[88:91], v[212:215], v[204:207]\n" \
  "s_waitcnt vmcnt(1)\n" \
  "s_waitcnt lgkmcnt(0)\n" \
  "s_barrier\n" \
  "ds_read_b128 v[208:211], v252 offset:12288\n" \
  "ds_read_b128 v[212:215], v252 offset:13312\n" \
  "s_add_u32 m0, %[mw], 0\n" \
  "s_xor_b32 %[cnt], %[sc], 704\n" \
  "buffer_load_dwordx4 v248, %[rx], %[cnt] offen lds\n" \
  "s_add_u32 m0, %[mw], 4096\n" \
  "s_xor_b32 %[cnt], %[sc], 768\n" \
  "buffer_load_dwordx4 v248, %[rx], %[cnt] offen lds\n" \
  "s_add_u32 m0, %[mw], 8192\n" \
  "s_xor_b32 %[cnt], %[sc], 832\n" \
  "buffer_load_dwordx4 v248, %[rx], %[cnt] offen lds\n" \
  "v_lshl_or_b32 v253, v251, 3, %[tm]\n" \
  "buffer_load_dword v253, v253, %[rx], %[tc] offen\n" \
  "v_mfma_f32_16x16x32_bf16 v[192:195], v[152:155], v[216:219], v[192:195]\n" \
  "v_mfma_f32_16x16x32_bf16 v[196:199], v[156:159], v[216:219], v[196:199]\n" \
  "ds_read_b128 v[216:219], v252 offset:14336\n" \
  "v_mfma_f32_16x16x32_bf16 v[200:203], v[152:155], v[220:223], v[200:203]\n" \
  "v_mfma_f32_16x16x32_bf16 v[204:207], v[156:159], v[220:223], v[204:207]\n" \
  "ds_read_b128 v[220:223], v252 offset:15360\n" \
  "v_mfma_f32_16x16x32_bf16 v[192:195], v[28:31], v[224:227], v[192:195]\n" \
  "v_mfma_f32_16x16x32_bf16 v[196:199], v[92:95], v[224:227], v[196:199]\n" \
  "ds_read_b128 v[224:227], v252 offset:16384\n" \
  "v_mfma_f32_16x16x32_bf16 v[200:203], v[28:31], v[228:231], v[200:203]\n" \
  "v_mfma_f32_16x16x32_bf16 v[204:207], v[92:95], v[228:231], v[204:207]\n" \
  "ds_read_b128 v[228:231], v252 offset:17408\n" \
  "v_mfma_f32_16x16x32_bf16 v[192:195], v[240:243], v[232:235], v[192:195]\n" \
  "v_mfma_f32_16x16x32_bf16 v[196:199], v[244:247], v[232:235], v[196:199]\n" \
  "ds_read_b128 v[232:235], v252 offset:18432\n" \
  "v_mfma_f32_16x16x32_bf16 v[200:203], v[240:243], v[236:239], v[200:203]\n" \
  "v_mfma_f32_16x16x32_bf16 v[204:207], v[244:247], v[236:239], v[204:207]\n" \
  "ds_read_b128 v[236:239], v252 offset:19456\n" \
  "ds_read_b128 v[240:243], v249 offset:0\n" \
  "ds_read_b128 v[244:247], v249 offset:1024\n" \
  "s_waitcnt lgkmcnt(9)\n" \
  "v_mfma_f32_16x16x32_bf16 v[192:195], v[32:35], v[208:211], v[192:195]\n" \
  "v_mfma_f32_16x16x32_bf16 v[196:199], v[96:99], v[208:211], v[196:199]\n" \
  "ds_read_b128 v[208:211], v252 offset:20480\n" \
  "s_waitcnt lgkmcnt(9)\n" \
  "v_mfma_f32_16x16x32_bf16 v[200:203], v[32:35], v[212:215], v[200:203]\n" \
  "v_mfma_f32_16x16x32_bf16 v[204:207], v[96:99], v[212:215], v[204:207]\n" \
  "ds_read_b128 v[212:215], v252 offset:21504\n" \
  "s_waitcnt lgkmcnt(9)\n" \
  "v_mfma_f32_16x16x32_bf16 v[192:195], v[160:163], v[216:219], v[192:195]\n" \
  "v_mfma_f32_16x16x32_bf16 v[196:199], v[164:167], v[216:219], v[196:199]\n" \
  "ds_read_b128 v[216:219], v252 offset:22528\n" \
  "s_waitcnt lgkmcnt(9)\n" \
  "v_mfma_f32_16x16x32_bf16 v[200:203], v[160:163], v[220:223], v[200:203]\n" \
  "v_mfma_f32_16x16x32_bf16 v[204:207], v[164:167], v[220:223], v[204:207]\n" \
  "ds_read_b128 v[220:223], v252 offset:23552\n" \
  "s_waitcnt lgkmcnt(9)\n" \
  "v_mfma_f32_16x16x32_bf16 v[192:195], v[36:39], v[224:227], v[192:195]\n" \
  "v_mfma_f32_16x16x32_bf16 v[196:199], v[100:103], v[224:227], v[196:199]\n" \
  "s_waitcnt lgkmcnt(8)\n" \
  "v_mfma_f32_16x16x32_bf16 v[200:203], v[36:39], v[228:231], v[200:203]\n" \
  "v_mfma_f32_16x16x32_bf16 v[204:207], v[100:103], v[228:231], v[204:207]\n" \
  "s_waitcnt vmcnt(1)\n" \
  "s_waitcnt lgkmcnt(0)\n" \
  "s_barrier\n" \
  "ds_read_b128 v[224:227], v252 offset:0\n" \
  "ds_read_b128 v[228:231], v252 offset:1024\n" \
  "s_add_u32 m0, %[mw], 12288\n" \
  "s_xor_b32 %[cnt], %[sc], 896\n" \
  "buffer_load_dwordx4 v248, %[rx], %[cnt] offen lds\n" \
  "s_add_u32 m0, %[mw], 16384\n" \
  "s_xor_b32 %[cnt], %[sc], 960\n" \
  "buffer_load_dwordx4 v248, %[rx], %[cnt] offen lds\n" \
  "v_mfma_f32_16x16x32_bf16 v[192:195], v[240:243], v[232:235], v[192:195]\n" \
  "v_mfma_f32_16x16x32_bf16 v[196:199], v[244:247], v[232:235], v[196:199]\n" \
  "ds_read_b128 v[232:235], v252 offset:2048\n" \
  "v_mfma_f32_16x16x32_bf16 v[200:203], v[240:243], v[236:239], v[200:203]\n" \
  "v_mfma_f32_16x16x32_bf16 v[204:207], v[244:247], v[236:239], v[204:207]\n" \
  "ds_read_b128 v[236:239], v252 offset:3072\n" \
  "ds_read_b128 v[240:243], v249 offset:16384\n" \
  "ds_read_b128 v[244:247], v249 offset:17408\n" \
  "v_mfma_f32_16x16x32_bf16 v[192:195], v[40:43], v[208:211], v[192:195]\n" \
  "v_mfma_f32_16x16x32_bf16 v[196:199], v[104:107], v[208:211], v[196:199]\n" \
  "ds_read_b128 v[208:211], v252 offset:4096\n" \
  "v_mfma_f32_16x16x32_bf16 v[200:203], v[40:43], v[212:215], v[200:203]\n" \
  "v_mfma_f32_16x16x32_bf16 v[204:207], v[104:107], v[212:215], v[204:207]\n" \
  "ds_read_b128 v[212:215], v252 offset:5120\n" \
  "v_mfma_f32_16x16x32_bf16 v[192:195], v[168:171], v[216:219], v[192:195]\n" \
  "v_mfma_f32_16x16x32_bf16 v[196:199], v[172:175], v[216:219], v[196:199]\n" \
  "ds_read_b128 v[216:219], v252 offset:6144\n" \
  "v_mfma_f32_16x16x32_bf16 v[200:203], v[168:171], v[220:223], v[200:203]\n" \
  "v_mfma_f32_16x16x32_bf16 v[204:207], v[172:175], v[220:223], v[204:207]\n" \
  "ds_read_b128 v[220:223], v252 offset:7168\n" \
  "s_waitcnt lgkmcnt(9)\n" \
  "v_mfma_f32_16x16x32_bf16 v[192:195], v[44:47], v[224:227], v[192:195]\n" \
  "v_mfma_f32_16x16x32_bf16 v[196:199], v[108:111], v[224:227], v[196:199]\n" \
  "ds_read_b128 v[224:227], v252 offset:8192\n" \
  "s_waitcnt lgkmcnt(9)\n" \
  "v_mfma_f32_16x16x32_bf16 v[200:203], v[44:47], v[228:231], v[200:203]\n" \
  "v_mfma_f32_16x16x32_bf16 v[204:207], v[108:111], v[228:231], v[204:207]\n" \
  "ds_read_b128 v[228:231], v252 offset:9216\n" \
  "s_waitcnt lgkmcnt(7)\n" \
  "v_mfma_f32_16x16x32_bf16 v[192:195], v[240:243], v[232:235], v[192:195]\n" \
  "s_waitcnt lgkmcnt(6)\n" \
  "v_mfma_f32_16x16x32_bf16 v[196:199], v[244:247], v[232:235], v[196:199]\n" \
  "ds_read_b128 v[232:235], v252 offset:10240\n" \
  "v_mfma_f32_16x16x32_bf16 v[200:203], v[240:243], v[236:239], v[200:203]\n" \
  "v_mfma_f32_16x16x32_bf16 v[204:207], v[244:247], v[236:239], v[204:207]\n" \
  "ds_read_b128 v[236:239], v252 offset:11264\n" \
  "ds_read_b128 v[240:243], v249 offset:32768\n" \
  "ds_read_b128 v[244:247], v249 offset:33792\n" \
  "s_waitcnt lgkmcnt(9)\n" \
  "v_mfma_f32_16x16x32_bf16 v[192:195], v[48:51], v[208:211], v[192:195]\n" \
  "v_mfma_f32_16x16x32_bf16 v[196:199], v[112:115], v[208:211], v[196:199]\n" \
  "s_waitcnt lgkmcnt(8)\n" \
  "v_mfma_f32_16x16x32_bf16 v[200:203], v[48:51], v[212:215], v[200:203]\n" \
  "v_mfma_f32_16x16x32_bf16 v[204:207], v[112:115], v[212:215], v[204:207]\n" \
  "s_waitcnt vmcnt(0)\n" \
  "s_waitcnt lgkmcnt(0)\n" \
  "s_barrier\n" \
  "ds_read_b128 v[208:211], v252 offset:12288\n" \
  "ds_read_b128 v[212:215], v252 offset:13312\n" \
  "s_add_u32 m0, %[mw], 0\n" \
  "s_xor_b32 %[cnt], %[sn], 0\n" \
  "buffer_load_dwordx4 v248, %[rx], %[cnt] offen lds\n" \
  "s_add_u32 m0, %[mw], 4096\n" \
  "s_xor_b32 %[cnt], %[sn], 64\n" \
  "buffer_load_dwordx4 v248, %[rx], %[cnt] offen lds\n" \
  "s_add_u32 m0, %[mw], 8192\n" \
  "s_xor_b32 %[cnt], %[sn], 128\n" \
  "buffer_load_dwordx4 v248, %[rx], %[cnt] offen lds\n" \
  "v_mfma_f32_16x16x32_bf16 v[192:195], v[176:179], v[216:219], v[192:195]\n" \
  "v_mfma_f32_16x16x32_bf16 v[196:199], v[180:183], v[216:219], v[196:199]\n" \
  "ds_read_b128 v[216:219], v252 offset:14336\n" \
  "v_mfma_f32_16x16x32_bf16 v[200:203], v[176:179], v[220:223], v[200:203]\n" \
  "v_mfma_f32_16x16x32_bf16 v[204:207], v[180:183], v[220:223], v[204:207]\n" \
  "ds_read_b128 v[220:223], v252 offset:15360\n" \
  "v_mfma_f32_16x16x32_bf16 v[192:195], v[52:55], v[224:227], v[192:195]\n" \
  "v_mfma_f32_16x16x32_bf16 v[196:199], v[116:119], v[224:227], v[196:199]\n" \
  "ds_read_b128 v[224:227], v252 offset:16384\n" \
  "v_mfma_f32_16x16x32_bf16 v[200:203], v[52:55], v[228:231], v[200:203]\n" \
  "v_mfma_f32_16x16x32_bf16 v[204:207], v[116:119], v[228:231], v[204:207]\n" \
  "ds_read_b128 v[228:231], v252 offset:17408\n" \
  "v_mfma_f32_16x16x32_bf16 v[192:195], v[240:243], v[232:235], v[192:195]\n" \
  "v_mfma_f32_16x16x32_bf16 v[196:199], v[244:247], v[232:235], v[196:199]\n" \
  "ds_read_b128 v[232:235], v252 offset:18432\n" \
  "v_mfma_f32_16x16x32_bf16 v[200:203], v[240:243], v[236:239], v[200:203]\n" \
  "v_mfma_f32_16x16x32_bf16 v[204:207], v[244:247], v[236:239], v[204:207]\n" \
  "ds_read_b128 v[236:239], v252 offset:19456\n" \
  "ds_read_b128 v[240:243], v249 offset:49152\n" \
  "ds_read_b128 v[244:247], v249 offset:50176\n" \
  "s_waitcnt lgkmcnt(9)\n" \
  "v_mfma_f32_16x16x32_bf16 v[192:195], v[56:59], v[208:211], v[192:195]\n" \
  "v_mfma_f32_16x16x32_bf16 v[196:199], v[120:123], v[208:211], v[196:199]\n" \
  "s_waitcnt lgkmcnt(8)\n" \
  "v_mfma_f32_16x16x32_bf16 v[200:203], v[56:59], v[212:215], v[200:203]\n" \
  "v_mfma_f32_16x16x32_bf16 v[204:207], v[120:123], v[212:215], v[204:207]\n" \
  "s_waitcnt vmcnt(0)\n" \
  "s_waitcnt lgkmcnt(0)\n" \
  "s_barrier\n" \
  "ds_read_b128 v[208:211], v252 offset:0\n" \
  "ds_read_b128 v[212:215], v252 offset:1024\n" \
  "s_add_u32 m0, %[mw], 12288\n" \
  "s_xor_b32 %[cnt], %[sn], 192\n" \
  "buffer_load_dwordx4 v248, %[rx], %[cnt] offen lds\n" \
  "s_add_u32 m0, %[mw], 16384\n" \
  "s_xor_b32 %[cnt], %[sn], 256\n" \
  "buffer_load_dwordx4 v248, %[rx], %[cnt] offen lds\n" \
  "s_add_u32 m0, %[mw], 20480\n" \
  "s_xor_b32 %[cnt], %[sn], 320\n" \
  "buffer_load_dwordx4 v248, %[rx], %[cnt] offen lds\n" \
  "v_mfma_f32_16x16x32_bf16 v[192:195], v[184:187], v[216:219], v[192:195]\n" \
  "v_mfma_f32_16x16x32_bf16 v[196:199], v[188:191], v[216:219], v[196:199]\n" \
  "ds_read_b128 v[216:219], v252 offset:2048\n" \
  "v_mfma_f32_16x16x32_bf16 v[200:203], v[184:187], v[220:223], v[200:203]\n" \
  "v_mfma_f32_16x16x32_bf16 v[204:207], v[188:191], v[220:223], v[204:207]\n" \
  "ds_read_b128 v[220:223], v252 offset:3072\n" \
  "v_mfma_f32_16x16x32_bf16 v[192:195], v[60:63], v[224:227], v[192:195]\n" \
  "v_mfma_f32_16x16x32_bf16 v[196:199], v[124:127], v[224:227], v[196:199]\n" \
  "ds_read_b128 v[224:227], v252 offset:4096\n" \
  "v_mfma_f32_16x16x32_bf16 v[200:203], v[60:63], v[228:231], v[200:203]\n" \
  "v_mfma_f32_16x16x32_bf16 v[204:207], v[124:127], v[228:231], v[204:207]\n" \
  "ds_read_b128 v[228:231], v252 offset:5120\n" \
  "v_mfma_f32_16x16x32_bf16 v[192:195], v[240:243], v[232:235], v[192:195]\n" \
  "v_mfma_f32_16x16x32_bf16 v[196:199], v[244:247], v[232:235], v[196:199]\n" \
  "ds_read_b128 v[232:235], v252 offset:6144\n" \
  "v_mfma_f32_16x16x32_bf16 v[200:203], v[240:243], v[236:239], v[200:203]\n" \
  "v_mfma_f32_16x16x32_bf16 v[204:207], v[244:247], v[236:239], v[204:207]\n" \
  "ds_read_b128 v[236:239], v252 offset:7168\n" \
  "v_cvt_pk_bf16_f32 v192, v192, v193\n" \
  "v_cvt_pk_bf16_f32 v193, v194, v195\n" \
  "v_cvt_pk_bf16_f32 v194, v196, v197\n" \
  "v_cvt_pk_bf16_f32 v195, v198, v199\n" \
  "v_add_u32 v196, %[so], v251\n" \
  "ds_write_b128 v196, v[192:195] offset:24576\n" \
  "v_cvt_pk_bf16_f32 v200, v200, v201\n" \
  "v_cvt_pk_bf16_f32 v201, v202, v203\n" \
  "v_cvt_pk_bf16_f32 v202, v204, v205\n" \
  "v_cvt_pk_bf16_f32 v203, v206, v207\n" \
  "ds_write_b128 v196, v[200:203] offset:28672\n" \
  ".Lqe_%=:\n" \
  "s_mov_b32 m0, %[keep]\n" \
  "s_waitcnt lgkmcnt(0)\n"
#define ST_XB \
  "s_barrier\n" \
  "ds_read_b128 v[192:195], v251 offset:24576\n" \
  "ds_read_b128 v[208:211], v251 offset:28672\n" \
  "ds_read_b128 v[196:199], v251 offset:25600\n" \
  "ds_read_b128 v[212:215], v251 offset:29696\n" \
  "ds_read_b128 v[200:203], v251 offset:26624\n" \
  "ds_read_b128 v[216:219], v251 offset:30720\n" \
  "ds_read_b128 v[204:207], v251 offset:27648\n" \
  "ds_read_b128 v[220:223], v251 offset:31744\n" \
  "ds_read_b128 v[240:243], v250 offset:0\n" \
  "ds_read_b128 v[244:247], v250 offset:16384\n" \
  "s_waitcnt lgkmcnt(9)\n" \
  "v_mfma_f32_16x16x32_bf16 v[224:227], v[192:195], v[0:3], 0\n" \
  "s_waitcnt lgkmcnt(8)\n" \
  "v_mfma_f32_16x16x32_bf16 v[228:231], v[208:211], v[0:3], 0\n" \
  "s_waitcnt lgkmcnt(7)\n" \
  "v_mfma_f32_16x16x32_bf16 v[224:227], v[196:199], v[4:7], v[224:227]\n" \
  "s_waitcnt lgkmcnt(6)\n" \
  "v_mfma_f32_16x16x32_bf16 v[228:231], v[212:215], v[4:7], v[228:231]\n" \
  "s_waitcnt lgkmcnt(5)\n" \
  "v_mfma_f32_16x16x32_bf16 v[224:227], v[200:203], v[8:11], v[224:227]\n" \
  "s_waitcnt lgkmcnt(4)\n" \
  "v_mfma_f32_16x16x32_bf16 v[228:231], v[216:219], v[8:11], v[228:231]\n" \
  "s_waitcnt lgkmcnt(3)\n" \
  "v_mfma_f32_16x16x32_bf16 v[224:227], v[204:207], v[12:15], v[224:227]\n" \
  "s_waitcnt lgkmcnt(2)\n" \
  "v_mfma_f32_16x16x32_bf16 v[228:231], v[220:223], v[12:15], v[228:231]\n" \
  "v_mfma_f32_16x16x32_bf16 v[232:235], v[192:195], v[128:131], 0\n" \
  "v_mfma_f32_16x16x32_bf16 v[236:239], v[208:211], v[128:131], 0\n" \
  "v_mfma_f32_16x16x32_bf16 v[232:235], v[200:203], v[136:139], v[232:235]\n" \
  "v_cvt_pk_bf16_f32 v224, v224, v228\n" \
  "v_cvt_pk_bf16_f32 v225, v225, v229\n" \
  "v_mfma_f32_16x16x32_bf16 v[236:239], v[216:219], v[136:139], v[236:239]\n" \
  "v_cvt_pk_bf16_f32 v226, v226, v230\n" \
  "v_cvt_pk_bf16_f32 v227, v227, v231\n" \
  "s_waitcnt lgkmcnt(1)\n" \
  "v_mfma_f32_16x16x32_bf16 v[232:235], v[196:199], v[240:243], v[232:235]\n" \
  "s_add_u32 %[cnt], %[so], 0*%c[js]+0*%c[ss]\n" \
  "buffer_store_dwordx4 v[224:227], v251, %[rs], %[cnt] offen sc1\n" \
  "v_mfma_f32_16x16x32_bf16 v[236:239], v[212:215], v[240:243], v[236:239]\n" \
  "s_waitcnt lgkmcnt(0)\n" \
  "v_mfma_f32_16x16x32_bf16 v[232:235], v[204:207], v[244:247], v[232:235]\n" \
  "v_mfma_f32_16x16x32_bf16 v[236:239], v[220:223], v[244:247], v[236:239]\n" \
  "ds_read_b128 v[240:243], v250 offset:1024\n" \
  "ds_read_b128 v[244:247], v250 offset:17408\n" \
  "v_mfma_f32_16x16x32_bf16 v[224:227], v[192:195], v[64:67], 0\n" \
  "v_mfma_f32_16x16x32_bf16 v[228:231], v[208:211], v[64:67], 0\n" \
  "v_mfma_f32_16x16x32_bf16 v[224:227], v[196:199], v[68:71], v[224:227]\n" \
  "v_cvt_pk_bf16_f32 v232, v232, v236\n" \
  "v_cvt_pk_bf16_f32 v233, v233, v237\n" \
  "v_mfma_f32_16x16x32_bf16 v[228:231], v[212:215], v[68:71], v[228:231]\n" \
  "v_cvt_pk_bf16_f32 v234, v234, v238\n" \
  "v_cvt_pk_bf16_f32 v235, v235, v239\n" \
  "v_mfma_f32_16x16x32_bf16 v[224:227], v[200:203], v[72:75], v[224:227]\n" \
  "s_add_u32 %[cnt], %[so], 4*%c[js]+0*%c[ss]\n" \
  "buffer_store_dwordx4 v[232:235], v251, %[rs], %[cnt] offen sc1\n" \
  "v_mfma_f32_16x16x32_bf16 v[228:231], v[216:219], v[72:75], v[228:231]\n" \
  "v_mfma_f32_16x16x32_bf16 v[224:227], v[204:207], v[76:79], v[224:227]\n" \
  "v_mfma_f32_16x16x32_bf16 v[228:231], v[220:223], v[76:79], v[228:231]\n" \
  "s_waitcnt vmcnt(2)\n" \
  "s_barrier\n" \
  "v_mfma_f32_16x16x32_bf16 v[232:235], v[192:195], v[132:135], 0\n" \
  "v_mfma_f32_16x16x32_bf16 v[236:239], v[208:211], v[132:135], 0\n" \
  "v_mfma_f32_16x16x32_bf16 v[232:235], v[200:203], v[140:143], v[232:235]\n" \
  "v_cvt_pk_bf16_f32 v224, v224, v228\n" \
  "v_cvt_pk_bf16_f32 v225, v225, v229\n" \
  "v_mfma_f32_16x16x32_bf16 v[236:239], v[216:219], v[140:143], v[236:239]\n" \
  "v_cvt_pk_bf16_f32 v226, v226, v230\n" \
  "v_cvt_pk_bf16_f32 v227, v227, v231\n" \
  "s_waitcnt lgkmcnt(1)\n" \
  "v_mfma_f32_16x16x32_bf16 v[232:235], v[196:199], v[240:243], v[232:235]\n" \
  "s_add_u32 %[cnt], %[so], 0*%c[js]+1*%c[ss]\n" \
  "buffer_store_dwordx4 v[224:227], v251, %[rs], %[cnt] offen sc1\n" \
  "v_mfma_f32_16x16x32_bf16 v[236:239], v[212:215], v[240:243], v[236:239]\n" \
  "s_waitcnt lgkmcnt(0)\n" \
  "v_mfma_f32_16x16x32_bf16 v[232:235], v[204:207], v[244:247], v[232:235]\n" \
  "v_mfma_f32_16x16x32_bf16 v[236:239], v[220:223], v[244:247], v[236:239]\n" \
  "ds_read_b128 v[240:243], v250 offset:32768\n" \
  "ds_read_b128 v[244:247], v250 offset:49152\n" \
  "v_mfma_f32_16x16x32_bf16 v[224:227], v[192:195], v[16:19], 0\n" \
  "v_mfma_f32_16x16x32_bf16 v[228:231], v[208:211], v[16:19], 0\n" \
  "v_mfma_f32_16x16x32_bf16 v[224:227], v[196:199], v[20:23], v[224:227]\n" \
  "v_cvt_pk_bf16_f32 v232, v232, v236\n" \
  "v_cvt_pk_bf16_f32 v233, v233, v237\n" \
  "v_mfma_f32_16x16x32_bf16 v[228:231], v[212:215], v[20:23], v[228:231]\n" \
  "v_cvt_pk_bf16_f32 v234, v234, v238\n" \
  "v_cvt_pk_bf16_f32 v235, v235, v239\n" \
  "v_mfma_f32_16x16x32_bf16 v[224:227], v[200:203], v[24:27], v[224:227]\n" \
  "s_add_u32 %[cnt], %[so], 4*%c[js]+1*%c[ss]\n" \
  "buffer_store_dwordx4 v[232:235], v251, %[rs], %[cnt] offen sc1\n" \
  "v_mfma_f32_16x16x32_bf16 v[228:231], v[216:219], v[24:27], v[228:231]\n" \
  "v_mfma_f32_16x16x32_bf16 v[224:227], v[204:207], v[28:31], v[224:227]\n" \
  "v_mfma_f32_16x16x32_bf16 v[228:231], v[220:223], v[28:31], v[228:231]\n" \
  "s_barrier\n" \
  "v_mfma_f32_16x16x32_bf16 v[232:235], v[192:195], v[144:147], 0\n" \
  "v_mfma_f32_16x16x32_bf16 v[236:239], v[208:211], v[144:147], 0\n" \
  "v_mfma_f32_16x16x32_bf16 v[232:235], v[200:203], v[152:155], v[232:235]\n" \
  "v_cvt_pk_bf16_f32 v224, v224, v228\n" \
  "v_cvt_pk_bf16_f32 v225, v225, v229\n" \
  "v_mfma_f32_16x16x32_bf16 v[236:239], v[216:219], v[152:155], v[236:239]\n" \
  "v_cvt_pk_bf16_f32 v226, v226, v230\n" \
  "v_cvt_pk_bf16_f32 v227, v227, v231\n" \
  "s_waitcnt lgkmcnt(1)\n" \
  "v_mfma_f32_16x16x32_bf16 v[232:235], v[196:199], v[240:243], v[232:235]\n" \
  "s_add_u32 %[cnt], %[so2], 1*%c[js]+0*%c[ss]\n" \
  "buffer_store_dwordx4 v[224:227], v251, %[rs], %[cnt] offen sc1\n" \
  "v_mfma_f32_16x16x32_bf16 v[236:239], v[212:215], v[240:243], v[236:239]\n" \
  "s_waitcnt lgkmcnt(0)\n" \
  "v_mfma_f32_16x16x32_bf16 v[232:235], v[204:207], v[244:247], v[232:235]\n" \
  "v_mfma_f32_16x16x32_bf16 v[236:239], v[220:223], v[244:247], v[236:239]\n" \
  "ds_read_b128 v[240:243], v250 offset:33792\n" \
  "ds_read_b128 v[244:247], v250 offset:50176\n" \
  "v_mfma_f32_16x16x32_bf16 v[224:227], v[192:195], v[80:83], 0\n" \
  "v_mfma_f32_16x16x32_bf16 v[228:231], v[208:211], v[80:83], 0\n" \
  "v_mfma_f32_16x16x32_bf16 v[224:227], v[196:199], v[84:87], v[224:227]\n" \
  "v_cvt_pk_bf16_f32 v232, v232, v236\n" \
  "v_cvt_pk_bf16_f32 v233, v233, v237\n" \
  "v_mfma_f32_16x16x32_bf16 v[228:231], v[212:215], v[84:87], v[228:231]\n" \
  "v_cvt_pk_bf16_f32 v234, v234, v238\n" \
  "v_cvt_pk_bf16_f32 v235, v235, v239\n" \
  "v_mfma_f32_16x16x32_bf16 v[224:227], v[200:203], v[88:91], v[224:227]\n" \
  "s_add_u32 %[cnt], %[so2], 5*%c[js]+0*%c[ss]\n" \
  "buffer_store_dwordx4 v[232:235], v251, %[rs], %[cnt] offen sc1\n" \
  "v_mfma_f32_16x16x32_bf16 v[228:231], v[216:219], v[88:91], v[228:231]\n" \
  "v_mfma_f32_16x16x32_bf16 v[224:227], v[204:207], v[92:95], v[224:227]\n" \
  "v_mfma_f32_16x16x32_bf16 v[228:231], v[220:223], v[92:95], v[228:231]\n" \
  "v_mfma_f32_16x16x32_bf16 v[232:235], v[192:195], v[148:151], 0\n" \
  "v_mfma_f32_16x16x32_bf16 v[236:239], v[208:211], v[148:151], 0\n" \
  "v_mfma_f32_16x16x32_bf16 v[232:235], v[200:203], v[156:159], v[232:235]\n" \
  "v_cvt_pk_bf16_f32 v224, v224, v228\n" \
  "v_cvt_pk_bf16_f32 v225, v225, v229\n" \
  "v_mfma_f32_16x16x32_bf16 v[236:239], v[216:219], v[156:159], v[236:239]\n" \
  "v_cvt_pk_bf16_f32 v226, v226, v230\n" \
  "v_cvt_pk_bf16_f32 v227, v227, v231\n" \
  "s_waitcnt lgkmcnt(1)\n" \
  "v_mfma_f32_16x16x32_bf16 v[232:235], v[196:199], v[240:243], v[232:235]\n" \
  "s_add_u32 %[cnt], %[so2], 1*%c[js]+1*%c[ss]\n" \
  "buffer_store_dwordx4 v[224:227], v251, %[rs], %[cnt] offen sc1\n" \
  "v_mfma_f32_16x16x32_bf16 v[236:239], v[212:215], v[240:243], v[236:239]\n" \
  "s_waitcnt lgkmcnt(0)\n" \
  "v_mfma_f32_16x16x32_bf16 v[232:235], v[204:207], v[244:247], v[232:235]\n" \
  "v_mfma_f32_16x16x32_bf16 v[236:239], v[220:223], v[244:247], v[236:239]\n" \
  "ds_read_b128 v[240:243], v249 offset:0\n" \
  "ds_read_b128 v[244:247], v249 offset:16384\n" \
  "s_barrier\n" \
  "v_mfma_f32_16x16x32_bf16 v[224:227], v[192:195], v[32:35], 0\n" \
  "v_mfma_f32_16x16x32_bf16 v[228:231], v[208:211], v[32:35], 0\n" \
  "v_mfma_f32_16x16x32_bf16 v[224:227], v[196:199], v[36:39], v[224:227]\n" \
  "v_cvt_pk_bf16_f32 v232, v232, v236\n" \
  "v_cvt_pk_bf16_f32 v233, v233, v237\n" \
  "v_mfma_f32_16x16x32_bf16 v[228:231], v[212:215], v[36:39], v[228:231]\n" \
  "v_cvt_pk_bf16_f32 v234, v234, v238\n" \
  "v_cvt_pk_bf16_f32 v235, v235, v239\n" \
  "v_mfma_f32_16x16x32_bf16 v[224:227], v[200:203], v[40:43], v[224:227]\n" \
  "s_add_u32 %[cnt], %[so2], 5*%c[js]+1*%c[ss]\n" \
  "buffer_store_dwordx4 v[232:235], v251, %[rs], %[cnt] offen sc1\n" \
  "v_mfma_f32_16x16x32_bf16 v[228:231], v[216:219], v[40:43], v[228:231]\n" \
  "v_mfma_f32_16x16x32_bf16 v[224:227], v[204:207], v[44:47], v[224:227]\n" \
  "v_mfma_f32_16x16x32_bf16 v[228:231], v[220:223], v[44:47], v[228:231]\n" \
  "v_mfma_f32_16x16x32_bf16 v[232:235], v[192:195], v[160:163], 0\n" \
  "v_mfma_f32_16x16x32_bf16 v[236:239], v[208:211], v[160:163], 0\n" \
  "v_mfma_f32_16x16x32_bf16 v[232:235], v[200:203], v[168:171], v[232:235]\n" \
  "v_cvt_pk_bf16_f32 v224, v224, v228\n" \
  "v_cvt_pk_bf16_f32 v225, v225, v229\n" \
  "v_mfma_f32_16x16x32_bf16 v[236:239], v[216:219], v[168:171], v[236:239]\n" \
  "v_cvt_pk_bf16_f32 v226, v226, v230\n" \
  "v_cvt_pk_bf16_f32 v227, v227, v231\n" \
  "s_waitcnt lgkmcnt(1)\n" \
  "v_mfma_f32_16x16x32_bf16 v[232:235], v[196:199], v[240:243], v[232:235]\n" \
  "s_add_u32 %[cnt], %[so], 2*%c[js]+0*%c[ss]\n" \
  "buffer_store_dwordx4 v[224:227], v251, %[rs], %[cnt] offen sc1\n" \
  "v_mfma_f32_16x16x32_bf16 v[236:239], v[212:215], v[240:243], v[236:239]\n" \
  "s_waitcnt lgkmcnt(0)\n" \
  "v_mfma_f32_16x16x32_bf16 v[232:235], v[204:207], v[244:247], v[232:235]\n" \
  "v_mfma_f32_16x16x32_bf16 v[236:239], v[220:223], v[244:247], v[236:239]\n" \
  "ds_read_b128 v[240:243], v249 offset:1024\n" \
  "ds_read_b128 v[244:247], v249 offset:17408\n" \
  "v_mfma_f32_16x16x32_bf16 v[224:227], v[192:195], v[96:99], 0\n" \
  "v_mfma_f32_16x16x32_bf16 v[228:231], v[208:211], v[96:99], 0\n" \
  "v_mfma_f32_16x16x32_bf16 v[224:227], v[196:199], v[100:103], v[224:227]\n" \
  "v_cvt_pk_bf16_f32 v232, v232, v236\n" \
  "v_cvt_pk_bf16_f32 v233, v233, v237\n" \
  "v_mfma_f32_16x16x32_bf16 v[228:231], v[212:215], v[100:103], v[228:231]\n" \
  "v_cvt_pk_bf16_f32 v234, v234, v238\n" \
  "v_cvt_pk_bf16_f32 v235, v235, v239\n" \
  "v_mfma_f32_16x16x32_bf16 v[224:227], v[200:203], v[104:107], v[224:227]\n" \
  "s_add_u32 %[cnt], %[so], 6*%c[js]+0*%c[ss]\n" \
  "buffer_store_dwordx4 v[232:235], v251, %[rs], %[cnt] offen sc1\n" \
  "v_mfma_f32_16x16x32_bf16 v[228:231], v[216:219], v[104:107], v[228:231]\n" \
  "v_mfma_f32_16x16x32_bf16 v[224:227], v[204:207], v[108:111], v[224:227]\n" \
  "v_mfma_f32_16x16x32_bf16 v[228:231], v[220:223], v[108:111], v[228:231]\n" \
  "s_barrier\n" \
  "v_mfma_f32_16x16x32_bf16 v[232:235], v[192:195], v[164:167], 0\n" \
  "v_mfma_f32_16x16x32_bf16 v[236:239], v[208:211], v[164:167], 0\n" \
  "v_mfma_f32_16x16x32_bf16 v[232:235], v[200:203], v[172:175], v[232:235]\n" \
  "v_cvt_pk_bf16_f32 v224, v224, v228\n" \
  "v_cvt_pk_bf16_f32 v225, v225, v229\n" \
  "v_mfma_f32_16x16x32_bf16 v[236:239], v[216:219], v[172:175], v[236:239]\n" \
  "v_cvt_pk_bf16_f32 v226, v226, v230\n" \
  "v_cvt_pk_bf16_f32 v227, v227, v231\n" \
  "s_waitcnt lgkmcnt(1)\n" \
  "v_mfma_f32_16x16x32_bf16 v[232:235], v[196:199], v[240:243], v[232:235]\n" \
  "s_add_u32 %[cnt], %[so], 2*%c[js]+1*%c[ss]\n" \
  "buffer_store_dwordx4 v[224:227], v251, %[rs], %[cnt] offen sc1\n" \
  "v_mfma_f32_16x16x32_bf16 v[236:239], v[212:215], v[240:243], v[236:239]\n" \
  "s_waitcnt lgkmcnt(0)\n" \
  "v_mfma_f32_16x16x32_bf16 v[232:235], v[204:207], v[244:247], v[232:235]\n" \
  "v_mfma_f32_16x16x32_bf16 v[236:239], v[220:223], v[244:247], v[236:239]\n" \
  "ds_read_b128 v[240:243], v249 offset:32768\n" \
  "ds_read_b128 v[244:247], v249 offset:49152\n" \
  "v_mfma_f32_16x16x32_bf16 v[224:227], v[192:195], v[48:51], 0\n" \
  "v_mfma_f32_16x16x32_bf16 v[228:231], v[208:211], v[48:51], 0\n" \
  "v_mfma_f32_16x16x32_bf16 v[224:227], v[196:199], v[52:55], v[224:227]\n" \
  "v_cvt_pk_bf16_f32 v232, v232, v236\n" \
  "v_cvt_pk_bf16_f32 v233, v233, v237\n" \
  "v_mfma_f32_16x16x32_bf16 v[228:231], v[212:215], v[52:55], v[228:231]\n" \
  "v_cvt_pk_bf16_f32 v234, v234, v238\n" \
  "v_cvt_pk_bf16_f32 v235, v235, v239\n" \
  "v_mfma_f32_16x16x32_bf16 v[224:227], v[200:203], v[56:59], v[224:227]\n" \
  "s_add_u32 %[cnt], %[so], 6*%c[js]+1*%c[ss]\n" \
  "buffer_store_dwordx4 v[232:235], v251, %[rs], %[cnt] offen sc1\n" \
  "v_mfma_f32_16x16x32_bf16 v[228:231], v[216:219], v[56:59], v[228:231]\n" \
  "v_mfma_f32_16x16x32_bf16 v[224:227], v[204:207], v[60:63], v[224:227]\n" \
  "v_mfma_f32_16x16x32_bf16 v[228:231], v[220:223], v[60:63], v[228:231]\n" \
  "s_barrier\n" \
  "v_mfma_f32_16x16x32_bf16 v[232:235], v[192:195], v[176:179], 0\n" \
  "v_mfma_f32_16x16x32_bf16 v[236:239], v[208:211], v[176:179], 0\n" \
  "v_mfma_f32_16x16x32_bf16 v[232:235], v[200:203], v[184:187], v[232:235]\n" \
  "v_cvt_pk_bf16_f32 v224, v224, v228\n" \
  "v_cvt_pk_bf16_f32 v225, v225, v229\n" \
  "v_mfma_f32_16x16x32_bf16 v[236:239], v[216:219], v[184:187], v[236:239]\n" \
  "v_cvt_pk_bf16_f32 v226, v226, v230\n" \
  "v_cvt_pk_bf16_f32 v227, v227, v231\n" \
  "s_waitcnt lgkmcnt(1)\n" \
  "v_mfma_f32_16x16x32_bf16 v[232:235], v[196:199], v[240:243], v[232:235]\n" \
  "s_add_u32 %[cnt], %[so2], 3*%c[js]+0*%c[ss]\n" \
  "buffer_store_dwordx4 v[224:227], v251, %[rs], %[cnt] offen sc1\n" \
  "v_mfma_f32_16x16x32_bf16 v[236:239], v[212:215], v[240:243], v[236:239]\n" \
  "s_waitcnt lgkmcnt(0)\n" \
  "v_mfma_f32_16x16x32_bf16 v[232:235], v[204:207], v[244:247], v[232:235]\n" \
  "v_mfma_f32_16x16x32_bf16 v[236:239], v[220:223], v[244:247], v[236:239]\n" \
  "ds_read_b128 v[240:243], v249 offset:33792\n" \
  "ds_read_b128 v[244:247], v249 offset:50176\n" \
  "v_mfma_f32_16x16x32_bf16 v[224:227], v[192:195], v[112:115], 0\n" \
  "v_mfma_f32_16x16x32_bf16 v[228:231], v[208:211], v[112:115], 0\n" \
  "v_mfma_f32_16x16x32_bf16 v[224:227], v[196:199], v[116:119], v[224:227]\n" \
  "v_cvt_pk_bf16_f32 v232, v232, v236\n" \
  "v_cvt_pk_bf16_f32 v233, v233, v237\n" \
  "v_mfma_f32_16x16x32_bf16 v[228:231], v[212:215], v[116:119], v[228:231]\n" \
  "v_cvt_pk_bf16_f32 v234, v234, v238\n" \
  "v_cvt_pk_bf16_f32 v235, v235, v239\n" \
  "v_mfma_f32_16x16x32_bf16 v[224:227], v[200:203], v[120:123], v[224:227]\n" \
  "s_add_u32 %[cnt], %[so2], 7*%c[js]+0*%c[ss]\n" \
  "buffer_store_dwordx4 v[232:235], v251, %[rs], %[cnt] offen sc1\n" \
  "v_mfma_f32_16x16x32_bf16 v[228:231], v[216:219], v[120:123], v[228:231]\n" \
  "v_mfma_f32_16x16x32_bf16 v[224:227], v[204:207], v[124:127], v[224:227]\n" \
  "v_mfma_f32_16x16x32_bf16 v[228:231], v[220:223], v[124:127], v[228:231]\n" \
  "v_mfma_f32_16x16x32_bf16 v[232:235], v[192:195], v[180:183], 0\n" \
  "v_mfma_f32_16x16x32_bf16 v[236:239], v[208:211], v[180:183], 0\n" \
  "v_mfma_f32_16x16x32_bf16 v[232:235], v[200:203], v[188:191], v[232:235]\n" \
  "v_cvt_pk_bf16_f32 v224, v224, v228\n" \
  "v_cvt_pk_bf16_f32 v225, v225, v229\n" \
  "v_mfma_f32_16x16x32_bf16 v[236:239], v[216:219], v[188:191], v[236:239]\n" \
  "v_cvt_pk_bf16_f32 v226, v226, v230\n" \
  "v_cvt_pk_bf16_f32 v227, v227, v231\n" \
  "s_waitcnt lgkmcnt(1)\n" \
  "v_mfma_f32_16x16x32_bf16 v[232:235], v[196:199], v[240:243], v[232:235]\n" \
  "s_add_u32 %[cnt], %[so2], 3*%c[js]+1*%c[ss]\n" \
  "buffer_store_dwordx4 v[224:227], v251, %[rs], %[cnt] offen sc1\n" \
  "v_mfma_f32_16x16x32_bf16 v[236:239], v[212:215], v[240:243], v[236:239]\n" \
  "s_waitcnt lgkmcnt(0)\n" \
  "v_mfma_f32_16x16x32_bf16 v[232:235], v[204:207], v[244:247], v[232:235]\n" \
  "v_mfma_f32_16x16x32_bf16 v[236:239], v[220:223], v[244:247], v[236:239]\n" \
  "s_nop 8\n" \
  "v_cvt_pk_bf16_f32 v232, v232, v236\n" \
  "v_cvt_pk_bf16_f32 v233, v233, v237\n" \
  "v_cvt_pk_bf16_f32 v234, v234, v238\n" \
  "v_cvt_pk_bf16_f32 v235, v235, v239\n" \
  "s_add_u32 %[cnt], %[so2], 7*%c[js]+1*%c[ss]\n" \
  "buffer_store_dwordx4 v[232:235], v251, %[rs], %[cnt] offen sc1\n" \
  "s_nop 1\n"
#define ST_XB0 \
  "ds_read_b128 v[192:195], v251 offset:24576\n" \
  "ds_read_b128 v[208:211], v251 offset:28672\n" \
  "ds_read_b128 v[196:199], v251 offset:25600\n" \
  "ds_read_b128 v[212:215], v251 offset:29696\n" \
  "ds_read_b128 v[200:203], v251 offset:26624\n" \
  "ds_read_b128 v[216:219], v251 offset:30720\n" \
  "ds_read_b128 v[204:207], v251 offset:27648\n" \
  "ds_read_b128 v[220:223], v251 offset:31744\n" \
  "ds_read_b128 v[240:243], v250 offset:0\n" \
  "ds_read_b128 v[244:247], v250 offset:16384\n" \
  "s_waitcnt lgkmcnt(9)\n" \
  "v_mfma_f32_16x16x32_bf16 v[224:227], v[192:195], v[0:3], 0\n" \
  "s_waitcnt lgkmcnt(8)\n" \
  "v_mfma_f32_16x16x32_bf16 v[228:231], v[208:211], v[0:3], 0\n" \
  "s_waitcnt lgkmcnt(7)\n" \
  "v_mfma_f32_16x16x32_bf16 v[224:227], v[196:199], v[4:7], v[224:227]\n" \
  "s_waitcnt lgkmcnt(6)\n" \
  "v_mfma_f32_16x16x32_bf16 v[228:231], v[212:215], v[4:7], v[228:231]\n" \
  "s_waitcnt lgkmcnt(5)\n" \
  "v_mfma_f32_16x16x32_bf16 v[224:227], v[200:203], v[8:11], v[224:227]\n" \
  "s_waitcnt lgkmcnt(4)\n" \
  "v_mfma_f32_16x16x32_bf16 v[228:231], v[216:219], v[8:11], v[228:231]\n" \
  "s_waitcnt lgkmcnt(3)\n" \
  "v_mfma_f32_16x16x32_bf16 v[224:227], v[204:207], v[12:15], v[224:227]\n" \
  "s_waitcnt lgkmcnt(2)\n" \
  "v_mfma_f32_16x16x32_bf16 v[228:231], v[220:223], v[12:15], v[228:231]\n" \
  "v_mfma_f32_16x16x32_bf16 v[232:235], v[192:195], v[128:131], 0\n" \
  "v_mfma_f32_16x16x32_bf16 v[236:239], v[208:211], v[128:131], 0\n" \
  "v_mfma_f32_16x16x32_bf16 v[232:235], v[200:203], v[136:139], v[232:235]\n" \
  "v_cvt_pk_bf16_f32 v224, v224, v228\n" \
  "v_cvt_pk_bf16_f32 v225, v225, v229\n" \
  "v_mfma_f32_16x16x32_bf16 v[236:239], v[216:219], v[136:139], v[236:239]\n" \
  "v_cvt_pk_bf16_f32 v226, v226, v230\n" \
  "v_cvt_pk_bf16_f32 v227, v227, v231\n" \
  "s_waitcnt lgkmcnt(1)\n" \
  "v_mfma_f32_16x16x32_bf16 v[232:235], v[196:199], v[240:243], v[232:235]\n" \
  "s_add_u32 %[cnt], %[so], 0*%c[js]+0*%c[ss]\n" \
  "buffer_store_dwordx4 v[224:227], v251, %[rs], %[cnt] offen sc1\n" \
  "v_mfma_f32_16x16x32_bf16 v[236:239], v[212:215], v[240:243], v[236:239]\n" \
  "s_waitcnt lgkmcnt(0)\n" \
  "v_mfma_f32_16x16x32_bf16 v[232:235], v[204:207], v[244:247], v[232:235]\n" \
  "v_mfma_f32_16x16x32_bf16 v[236:239], v[220:223], v[244:247], v[236:239]\n" \
  "ds_read_b128 v[240:243], v250 offset:1024\n" \
  "ds_read_b128 v[244:247], v250 offset:17408\n" \
  "v_mfma_f32_16x16x32_bf16 v[224:227], v[192:195], v[64:67], 0\n" \
  "v_mfma_f32_16x16x32_bf16 v[228:231], v[208:211], v[64:67], 0\n" \
  "v_mfma_f32_16x16x32_bf16 v[224:227], v[196:199], v[68:71], v[224:227]\n" \
  "v_cvt_pk_bf16_f32 v232, v232, v236\n" \
  "v_cvt_pk_bf16_f32 v233, v233, v237\n" \
  "v_mfma_f32_16x16x32_bf16 v[228:231], v[212:215], v[68:71], v[228:231]\n" \
  "v_cvt_pk_bf16_f32 v234, v234, v238\n" \
  "v_cvt_pk_bf16_f32 v235, v235, v239\n" \
  "v_mfma_f32_16x16x32_bf16 v[224:227], v[200:203], v[72:75], v[224:227]\n" \
  "s_add_u32 %[cnt], %[so], 4*%c[js]+0*%c[ss]\n" \
  "buffer_store_dwordx4 v[232:235], v251, %[rs], %[cnt] offen sc1\n" \
  "v_mfma_f32_16x16x32_bf16 v[228:231], v[216:219], v[72:75], v[228:231]\n" \
  "v_mfma_f32_16x16x32_bf16 v[224:227], v[204:207], v[76:79], v[224:227]\n" \
  "v_mfma_f32_16x16x32_bf16 v[228:231], v[220:223], v[76:79], v[228:231]\n" \
  "v_mfma_f32_16x16x32_bf16 v[232:235], v[192:195], v[132:135], 0\n" \
  "v_mfma_f32_16x16x32_bf16 v[236:239], v[208:211], v[132:135], 0\n" \
  "v_mfma_f32_16x16x32_bf16 v[232:235], v[200:203], v[140:143], v[232:235]\n" \
  "v_cvt_pk_bf16_f32 v224, v224, v228\n" \
  "v_cvt_pk_bf16_f32 v225, v225, v229\n" \
  "v_mfma_f32_16x16x32_bf16 v[236:239], v[216:219], v[140:143], v[236:239]\n" \
  "v_cvt_pk_bf16_f32 v226, v226, v230\n" \
  "v_cvt_pk_bf16_f32 v227, v227, v231\n" \
  "s_waitcnt lgkmcnt(1)\n" \
  "v_mfma_f32_16x16x32_bf16 v[232:235], v[196:199], v[240:243], v[232:235]\n" \
  "s_add_u32 %[cnt], %[so], 0*%c[js]+1*%c[ss]\n" \
  "buffer_store_dwordx4 v[224:227], v251, %[rs], %[cnt] offen sc1\n" \
  "v_mfma_f32_16x16x32_bf16 v[236:239], v[212:215], v[240:243], v[236:239]\n" \
  "s_waitcnt lgkmcnt(0)\n" \
  "v_mfma_f32_16x16x32_bf16 v[232:235], v[204:207], v[244:247], v[232:235]\n" \
  "v_mfma_f32_16x16x32_bf16 v[236:239], v[220:223], v[244:247], v[236:239]\n" \
  "ds_read_b128 v[240:243], v250 offset:32768\n" \
  "ds_read_b128 v[244:247], v250 offset:49152\n" \
  "v_mfma_f32_16x16x32_bf16 v[224:227], v[192:195], v[16:19], 0\n" \
  "v_mfma_f32_16x16x32_bf16 v[228:231], v[208:211], v[16:19], 0\n" \
  "v_mfma_f32_16x16x32_bf16 v[224:227], v[196:199], v[20:23], v[224:227]\n" \
  "v_cvt_pk_bf16_f32 v232, v232, v236\n" \
  "v_cvt_pk_bf16_f32 v233, v233, v237\n" \
  "v_mfma_f32_16x16x32_bf16 v[228:231], v[212:215], v[20:23], v[228:231]\n" \
  "v_cvt_pk_bf16_f32 v234, v234, v238\n" \
  "v_cvt_pk_bf16_f32 v235, v235, v239\n" \
  "v_mfma_f32_16x16x32_bf16 v[224:227], v[200:203], v[24:27], v[224:227]\n" \
  "s_add_u32 %[cnt], %[so], 4*%c[js]+1*%c[ss]\n" \
  "buffer_store_dwordx4 v[232:235], v251, %[rs], %[cnt] offen sc1\n" \
  "v_mfma_f32_16x16x32_bf16 v[228:231], v[216:219], v[24:27], v[228:231]\n" \
  "v_mfma_f32_16x16x32_bf16 v[224:227], v[204:207], v[28:31], v[224:227]\n" \
  "v_mfma_f32_16x16x32_bf16 v[228:231], v[220:223], v[28:31], v[228:231]\n" \
  "v_mfma_f32_16x16x32_bf16 v[232:235], v[192:195], v[144:147], 0\n" \
  "v_mfma_f32_16x16x32_bf16 v[236:239], v[208:211], v[144:147], 0\n" \
  "v_mfma_f32_16x16x32_bf16 v[232:235], v[200:203], v[152:155], v[232:235]\n" \
  "v_cvt_pk_bf16_f32 v224, v224, v228\n" \
  "v_cvt_pk_bf16_f32 v225, v225, v229\n" \
  "v_mfma_f32_16x16x32_bf16 v[236:239], v[216:219], v[152:155], v[236:239]\n" \
  "v_cvt_pk_bf16_f32 v226, v226, v230\n" \
  "v_cvt_pk_bf16_f32 v227, v227, v231\n" \
  "s_waitcnt lgkmcnt(1)\n" \
  "v_mfma_f32_16x16x32_bf16 v[232:235], v[196:199], v[240:243], v[232:235]\n" \
  "s_add_u32 %[cnt], %[so2], 1*%c[js]+0*%c[ss]\n" \
  "buffer_store_dwordx4 v[224:227], v251, %[rs], %[cnt] offen sc1\n" \
  "v_mfma_f32_16x16x32_bf16 v[236:239], v[212:215], v[240:243], v[236:239]\n" \
  "s_waitcnt lgkmcnt(0)\n" \
  "v_mfma_f32_16x16x32_bf16 v[232:235], v[204:207], v[244:247], v[232:235]\n" \
  "v_mfma_f32_16x16x32_bf16 v[236:239], v[220:223], v[244:247], v[236:239]\n" \
  "ds_read_b128 v[240:243], v250 offset:33792\n" \
  "ds_read_b128 v[244:247], v250 offset:50176\n" \
  "v_mfma_f32_16x16x32_bf16 v[224:227], v[192:195], v[80:83], 0\n" \
  "v_mfma_f32_16x16x32_bf16 v[228:231], v[208:211], v[80:83], 0\n" \
  "v_mfma_f32_16x16x32_bf16 v[224:227], v[196:199], v[84:87], v[224:227]\n" \
  "v_cvt_pk_bf16_f32 v232, v232, v236\n" \
  "v_cvt_pk_bf16_f32 v233, v233, v237\n" \
  "v_mfma_f32_16x16x32_bf16 v[228:231], v[212:215], v[84:87], v[228:231]\n" \
  "v_cvt_pk_bf16_f32 v234, v234, v238\n" \
  "v_cvt_pk_bf16_f32 v235, v235, v239\n" \
  "v_mfma_f32_16x16x32_bf16 v[224:227], v[200:203], v[88:91], v[224:227]\n" \
  "s_add_u32 %[cnt], %[so2], 5*%c[js]+0*%c[ss]\n" \
  "buffer_store_dwordx4 v[232:235], v251, %[rs], %[cnt] offen sc1\n" \
  "v_mfma_f32_16x16x32_bf16 v[228:231], v[216:219], v[88:91], v[228:231]\n" \
  "v_mfma_f32_16x16x32_bf16 v[224:227], v[204:207], v[92:95], v[224:227]\n" \
  "v_mfma_f32_16x16x32_bf16 v[228:231], v[220:223], v[92:95], v[228:231]\n" \
  "v_mfma_f32_16x16x32_bf16 v[232:235], v[192:195], v[148:151], 0\n" \
  "v_mfma_f32_16x16x32_bf16 v[236:239], v[208:211], v[148:151], 0\n" \
  "v_mfma_f32_16x16x32_bf16 v[232:235], v[200:203], v[156:159], v[232:235]\n" \
  "v_cvt_pk_bf16_f32 v224, v224, v228\n" \
  "v_cvt_pk_bf16_f32 v225, v225, v229\n" \
  "v_mfma_f32_16x16x32_bf16 v[236:239], v[216:219], v[156:159], v[236:239]\n" \
  "v_cvt_pk_bf16_f32 v226, v226, v230\n" \
  "v_cvt_pk_bf16_f32 v227, v227, v231\n" \
  "s_waitcnt lgkmcnt(1)\n" \
  "v_mfma_f32_16x16x32_bf16 v[232:235], v[196:199], v[240:243], v[232:235]\n" \
  "s_add_u32 %[cnt], %[so2], 1*%c[js]+1*%c[ss]\n" \
  "buffer_store_dwordx4 v[224:227], v251, %[rs], %[cnt] offen sc1\n" \
  "v_mfma_f32_16x16x32_bf16 v[236:239], v[212:215], v[240:243], v[236:239]\n" \
  "s_waitcnt lgkmcnt(0)\n" \
  "v_mfma_f32_16x16x32_bf16 v[232:235], v[204:207], v[244:247], v[232:235]\n" \
  "v_mfma_f32_16x16x32_bf16 v[236:239], v[220:223], v[244:247], v[236:239]\n" \
  "ds_read_b128 v[240:243], v249 offset:0\n" \
  "ds_read_b128 v[244:247], v249 offset:16384\n" \
  "v_mfma_f32_16x16x32_bf16 v[224:227], v[192:195], v[32:35], 0\n" \
  "v_mfma_f32_16x16x32_bf16 v[228:231], v[208:211], v[32:35], 0\n" \
  "v_mfma_f32_16x16x32_bf16 v[224:227], v[196:199], v[36:39], v[224:227]\n" \
  "v_cvt_pk_bf16_f32 v232, v232, v236\n" \
  "v_cvt_pk_bf16_f32 v233, v233, v237\n" \
  "v_mfma_f32_16x16x32_bf16 v[228:231], v[212:215], v[36:39], v[228:231]\n" \
  "v_cvt_pk_bf16_f32 v234, v234, v238\n" \
  "v_cvt_pk_bf16_f32 v235, v235, v239\n" \
  "v_mfma_f32_16x16x32_bf16 v[224:227], v[200:203], v[40:43], v[224:227]\n" \
  "s_add_u32 %[cnt], %[so2], 5*%c[js]+1*%c[ss]\n" \
  "buffer_store_dwordx4 v[232:235], v251, %[rs], %[cnt] offen sc1\n" \
  "v_mfma_f32_16x16x32_bf16 v[228:231], v[216:219], v[40:43], v[228:231]\n" \
  "v_mfma_f32_16x16x32_bf16 v[224:227], v[204:207], v[44:47], v[224:227]\n" \
  "v_mfma_f32_16x16x32_bf16 v[228:231], v[220:223], v[44:47], v[228:231]\n" \
  "v_mfma_f32_16x16x32_bf16 v[232:235], v[192:195], v[160:163], 0\n" \
  "v_mfma_f32_16x16x32_bf16 v[236:239], v[208:211], v[160:163], 0\n" \
  "v_mfma_f32_16x16x32_bf16 v[232:235], v[200:203], v[168:171], v[232:235]\n" \
  "v_cvt_pk_bf16_f32 v224, v224, v228\n" \
  "v_cvt_pk_bf16_f32 v225, v225, v229\n" \
  "v_mfma_f32_16x16x32_bf16 v[236:239], v[216:219], v[168:171], v[236:239]\n" \
  "v_cvt_pk_bf16_f32 v226, v226, v230\n" \
  "v_cvt_pk_bf16_f32 v227, v227, v231\n" \
  "s_waitcnt lgkmcnt(1)\n" \
  "v_mfma_f32_16x16x32_bf16 v[232:235], v[196:199], v[240:243], v[232:235]\n" \
  "s_add_u32 %[cnt], %[so], 2*%c[js]+0*%c[ss]\n" \
  "buffer_store_dwordx4 v[224:227], v251, %[rs], %[cnt] offen sc1\n" \
  "v_mfma_f32_16x16x32_bf16 v[236:239], v[212:215], v[240:243], v[236:239]\n" \
  "s_waitcnt lgkmcnt(0)\n" \
  "v_mfma_f32_16x16x32_bf16 v[232:235], v[204:207], v[244:247], v[232:235]\n" \
  "v_mfma_f32_16x16x32_bf16 v[236:239], v[220:223], v[244:247], v[236:239]\n" \
  "ds_read_b128 v[240:243], v249 offset:1024\n" \
  "ds_read_b128 v[244:247], v249 offset:17408\n" \
  "v_mfma_f32_16x16x32_bf16 v[224:227], v[192:195], v[96:99], 0\n" \
  "v_mfma_f32_16x16x32_bf16 v[228:231], v[208:211], v[96:99], 0\n" \
  "v_mfma_f32_16x16x32_bf16 v[224:227], v[196:199], v[100:103], v[224:227]\n" \
  "v_cvt_pk_bf16_f32 v232, v232, v236\n" \
  "v_cvt_pk_bf16_f32 v233, v233, v237\n" \
  "v_mfma_f32_16x16x32_bf16 v[228:231], v[212:215], v[100:103], v[228:231]\n" \
  "v_cvt_pk_bf16_f32 v234, v234, v238\n" \
  "v_cvt_pk_bf16_f32 v235, v235, v239\n" \
  "v_mfma_f32_16x16x32_bf16 v[224:227], v[200:203], v[104:107], v[224:227]\n" \
  "s_add_u32 %[cnt], %[so], 6*%c[js]+0*%c[ss]\n" \
  "buffer_store_dwordx4 v[232:235], v251, %[rs], %[cnt] offen sc1\n" \
  "v_mfma_f32_16x16x32_bf16 v[228:231], v[216:219], v[104:107], v[228:231]\n" \
  "v_mfma_f32_16x16x32_bf16 v[224:227], v[204:207], v[108:111], v[224:227]\n" \
  "v_mfma_f32_16x16x32_bf16 v[228:231], v[220:223], v[108:111], v[228:231]\n" \
  "v_mfma_f32_16x16x32_bf16 v[232:235], v[192:195], v[164:167], 0\n" \
  "v_mfma_f32_16x16x32_bf16 v[236:239], v[208:211], v[164:167], 0\n" \
  "v_mfma_f32_16x16x32_bf16 v[232:235], v[200:203], v[172:175], v[232:235]\n" \
  "v_cvt_pk_bf16_f32 v224, v224, v228\n" \
  "v_cvt_pk_bf16_f32 v225, v225, v229\n" \
  "v_mfma_f32_16x16x32_bf16 v[236:239], v[216:219], v[172:175], v[236:239]\n" \
  "v_cvt_pk_bf16_f32 v226, v226, v230\n" \
  "v_cvt_pk_bf16_f32 v227, v227, v231\n" \
  "s_waitcnt lgkmcnt(1)\n" \
  "v_mfma_f32_16x16x32_bf16 v[232:235], v[196:199], v[240:243], v[232:235]\n" \
  "s_add_u32 %[cnt], %[so], 2*%c[js]+1*%c[ss]\n" \
  "buffer_store_dwordx4 v[224:227], v251, %[rs], %[cnt] offen sc1\n" \
  "v_mfma_f32_16x16x32_bf16 v[236:239], v[212:215], v[240:243], v[236:239]\n" \
  "s_waitcnt lgkmcnt(0)\n" \
  "v_mfma_f32_16x16x32_bf16 v[232:235], v[204:207], v[244:247], v[232:235]\n" \
  "v_mfma_f32_16x16x32_bf16 v[236:239], v[220:223], v[244:247], v[236:239]\n" \
  "ds_read_b128 v[240:243], v249 offset:32768\n" \
  "ds_read_b128 v[244:247], v249 offset:49152\n" \
  "v_mfma_f32_16x16x32_bf16 v[224:227], v[192:195], v[48:51], 0\n" \
  "v_mfma_f32_16x16x32_bf16 v[228:231], v[208:211], v[48:51], 0\n" \
  "v_mfma_f32_16x16x32_bf16 v[224:227], v[196:199], v[52:55], v[224:227]\n" \
  "v_cvt_pk_bf16_f32 v232, v232, v236\n" \
  "v_cvt_pk_bf16_f32 v233, v233, v237\n" \
  "v_mfma_f32_16x16x32_bf16 v[228:231], v[212:215], v[52:55], v[228:231]\n" \
  "v_cvt_pk_bf16_f32 v234, v234, v238\n" \
  "v_cvt_pk_bf16_f32 v235, v235, v239\n" \
  "v_mfma_f32_16x16x32_bf16 v[224:227], v[200:203], v[56:59], v[224:227]\n" \
  "s_add_u32 %[cnt], %[so], 6*%c[js]+1*%c[ss]\n" \
  "buffer_store_dwordx4 v[232:235], v251, %[rs], %[cnt] offen sc1\n" \
  "v_mfma_f32_16x16x32_bf16 v[228:231], v[216:219], v[56:59], v[228:231]\n" \
  "v_mfma_f32_16x16x32_bf16 v[224:227], v[204:207], v[60:63], v[224:227]\n" \
  "v_mfma_f32_16x16x32_bf16 v[228:231], v[220:223], v[60:63], v[228:231]\n" \
  "v_mfma_f32_16x16x32_bf16 v[232:235], v[192:195], v[176:179], 0\n" \
  "v_mfma_f32_16x16x32_bf16 v[236:239], v[208:211], v[176:179], 0\n" \
  "v_mfma_f32_16x16x32_bf16 v[232:235], v[200:203], v[184:187], v[232:235]\n" \
  "v_cvt_pk_bf16_f32 v224, v224, v228\n" \
  "v_cvt_pk_bf16_f32 v225, v225, v229\n" \
  "v_mfma_f32_16x16x32_bf16 v[236:239], v[216:219], v[184:187], v[236:239]\n" \
  "v_cvt_pk_bf16_f32 v226, v226, v230\n" \
  "v_cvt_pk_bf16_f32 v227, v227, v231\n" \
  "s_waitcnt lgkmcnt(1)\n" \
  "v_mfma_f32_16x16x32_bf16 v[232:235], v[196:199], v[240:243], v[232:235]\n" \
  "s_add_u32 %[cnt], %[so2], 3*%c[js]+0*%c[ss]\n" \
  "buffer_store_dwordx4 v[224:227], v251, %[rs], %[cnt] offen sc1\n" \
  "v_mfma_f32_16x16x32_bf16 v[236:239], v[212:215], v[240:243], v[236:239]\n" \
  "s_waitcnt lgkmcnt(0)\n" \
  "v_mfma_f32_16x16x32_bf16 v[232:235], v[204:207], v[244:247], v[232:235]\n" \
  "v_mfma_f32_16x16x32_bf16 v[236:239], v[220:223], v[244:247], v[236:239]\n" \
  "ds_read_b128 v[240:243], v249 offset:33792\n" \
  "ds_read_b128 v[244:247], v249 offset:50176\n" \
  "v_mfma_f32_16x16x32_bf16 v[224:227], v[192:195], v[112:115], 0\n" \
  "v_mfma_f32_16x16x32_bf16 v[228:231], v[208:211], v[112:115], 0\n" \
  "v_mfma_f32_16x16x32_bf16 v[224:227], v[196:199], v[116:119], v[224:227]\n" \
  "v_cvt_pk_bf16_f32 v232, v232, v236\n" \
  "v_cvt_pk_bf16_f32 v233, v233, v237\n" \
  "v_mfma_f32_16x16x32_bf16 v[228:231], v[212:215], v[116:119], v[228:231]\n" \
  "v_cvt_pk_bf16_f32 v234, v234, v238\n" \
  "v_cvt_pk_bf16_f32 v235, v235, v239\n" \
  "v_mfma_f32_16x16x32_bf16 v[224:227], v[200:203], v[120:123], v[224:227]\n" \
  "s_add_u32 %[cnt], %[so2], 7*%c[js]+0*%c[ss]\n" \
  "buffer_store_dwordx4 v[232:235], v251, %[rs], %[cnt] offen sc1\n" \
  "v_mfma_f32_16x16x32_bf16 v[228:231], v[216:219], v[120:123], v[228:231]\n" \
  "v_mfma_f32_16x16x32_bf16 v[224:227], v[204:207], v[124:127], v[224:227]\n" \
  "v_mfma_f32_16x16x32_bf16 v[228:231], v[220:223], v[124:127], v[228:231]\n" \
  "v_mfma_f32_16x16x32_bf16 v[232:235], v[192:195], v[180:183], 0\n" \
  "v_mfma_f32_16x16x32_bf16 v[236:239], v[208:211], v[180:183], 0\n" \
  "v_mfma_f32_16x16x32_bf16 v[232:235], v[200:203], v[188:191], v[232:235]\n" \
  "v_cvt_pk_bf16_f32 v224, v224, v228\n" \
  "v_cvt_pk_bf16_f32 v225, v225, v229\n" \
  "v_mfma_f32_16x16x32_bf16 v[236:239], v[216:219], v[188:191], v[236:239]\n" \
  "v_cvt_pk_bf16_f32 v226, v226, v230\n" \
  "v_cvt_pk_bf16_f32 v227, v227, v231\n" \
  "s_waitcnt lgkmcnt(1)\n" \
  "v_mfma_f32_16x16x32_bf16 v[232:235], v[196:199], v[240:243], v[232:235]\n" \
  "s_add_u32 %[cnt], %[so2], 3*%c[js]+1*%c[ss]\n" \
  "buffer_store_dwordx4 v[224:227], v251, %[rs], %[cnt] offen sc1\n" \
  "v_mfma_f32_16x16x32_bf16 v[236:239], v[212:215], v[240:243], v[236:239]\n" \
  "s_waitcnt lgkmcnt(0)\n" \
  "v_mfma_f32_16x16x32_bf16 v[232:235], v[204:207], v[244:247], v[232:235]\n" \
  "v_mfma_f32_16x16x32_bf16 v[236:239], v[220:223], v[244:247], v[236:239]\n" \
  "s_nop 8\n" \
  "v_cvt_pk_bf16_f32 v232, v232, v236\n" \
  "v_cvt_pk_bf16_f32 v233, v233, v237\n" \
  "v_cvt_pk_bf16_f32 v234, v234, v238\n" \
  "v_cvt_pk_bf16_f32 v235, v235, v239\n" \
  "s_add_u32 %[cnt], %[so2], 7*%c[js]+1*%c[ss]\n" \
  "buffer_store_dwordx4 v[232:235], v251, %[rs], %[cnt] offen sc1\n" \
  "s_nop 1\n"
#define PRO0_0 \
  "s_cmp_lg_u32 %[hb], 0\n" \
  "s_cbranch_scc1 .Ls_%=\n" \
  "v_xor_b32 %[t0], 0x0, %[pb]\n" \
  "ds_read_b64_tr_b16 v[12:13], %[t0] offset:0\n" \
  "ds_read_b64_tr_b16 v[96:97], %[t0] offset:16384\n" \
  "v_xor_b32 %[t1], 0x20, %[pb]\n" \
  "ds_read_b64_tr_b16 v[28:29], %[t1] offset:0\n" \
  "ds_read_b64_tr_b16 v[144:145], %[t1] offset:16384\n" \
  "v_xor_b32 %[t0], 0x1000, %[pb]\n" \
  "ds_read_b64_tr_b16 v[0:1], %[t0] offset:0\n" \
  "ds_read_b64_tr_b16 v[192:193], %[t0] offset:16384\n" \
  "v_xor_b32 %[t1], 0x1020, %[pb]\n" \
  "ds_read_b64_tr_b16 v[48:49], %[t1] offset:0\n" \
  "ds_read_b64_tr_b16 v[200:201], %[t1] offset:16384\n" \
  "v_xor_b32 %[t0], 0x2000, %[pb]\n" \
  "ds_read_b64_tr_b16 v[16:17], %[t0] offset:0\n" \
  "ds_read_b64_tr_b16 v[208:209], %[t0] offset:16384\n" \
  "v_xor_b32 %[t1], 0x2020, %[pb]\n" \
  "ds_read_b64_tr_b16 v[64:65], %[t1] offset:0\n" \
  "ds_read_b64_tr_b16 v[216:217], %[t1] offset:16384\n" \
  "v_xor_b32 %[t0], 0x3000, %[pb]\n" \
  "ds_read_b64_tr_b16 v[32:33], %[t0] offset:0\n" \
  "ds_read_b64_tr_b16 v[128:129], %[t0] offset:16384\n" \
  "v_xor_b32 %[t1], 0x3020, %[pb]\n" \
  "ds_read_b64_tr_b16 v[80:81], %[t1] offset:0\n" \
  "ds_read_b64_tr_b16 v[176:177], %[t1] offset:16384\n" \
  ".Ls_%=:\n" \
  "s_waitcnt lgkmcnt(0)\n"
#define PRO0_1 \
  "s_cmp_lg_u32 %[hb], 1\n" \
  "s_cbranch_scc1 .Ls_%=\n" \
  "v_xor_b32 %[t0], 0x8000, %[pb]\n" \
  "ds_read_b64_tr_b16 v[12:13], %[t0] offset:0\n" \
  "ds_read_b64_tr_b16 v[96:97], %[t0] offset:16384\n" \
  "v_xor_b32 %[t1], 0x8020, %[pb]\n" \
  "ds_read_b64_tr_b16 v[28:29], %[t1] offset:0\n" \
  "ds_read_b64_tr_b16 v[144:145], %[t1] offset:16384\n" \
  "v_xor_b32 %[t0], 0x9000, %[pb]\n" \
  "ds_read_b64_tr_b16 v[0:1], %[t0] offset:0\n" \
  "ds_read_b64_tr_b16 v[192:193], %[t0] offset:16384\n" \
  "v_xor_b32 %[t1], 0x9020, %[pb]\n" \
  "ds_read_b64_tr_b16 v[48:49], %[t1] offset:0\n" \
  "ds_read_b64_tr_b16 v[200:201], %[t1] offset:16384\n" \
  "v_xor_b32 %[t0], 0xa000, %[pb]\n" \
  "ds_read_b64_tr_b16 v[16:17], %[t0] offset:0\n" \
  "ds_read_b64_tr_b16 v[208:209], %[t0] offset:16384\n" \
  "v_xor_b32 %[t1], 0xa020, %[pb]\n" \
  "ds_read_b64_tr_b16 v[64:65], %[t1] offset:0\n" \
  "ds_read_b64_tr_b16 v[216:217], %[t1] offset:16384\n" \
  "v_xor_b32 %[t0], 0xb000, %[pb]\n" \
  "ds_read_b64_tr_b16 v[32:33], %[t0] offset:0\n" \
  "ds_read_b64_tr_b16 v[128:129], %[t0] offset:16384\n" \
  "v_xor_b32 %[t1], 0xb020, %[pb]\n" \
  "ds_read_b64_tr_b16 v[80:81], %[t1] offset:0\n" \
  "ds_read_b64_tr_b16 v[176:177], %[t1] offset:16384\n" \
  ".Ls_%=:\n" \
  "s_waitcnt lgkmcnt(0)\n"
#define PRO0_2 \
  "s_cmp_lg_u32 %[hb], 0\n" \
  "s_cbranch_scc1 .Ls_%=\n" \
  "v_xor_b32 %[t0], 0x10000, %[pb]\n" \
  "ds_read_b64_tr_b16 v[14:15], %[t0] offset:0\n" \
  "ds_read_b64_tr_b16 v[98:99], %[t0] offset:16384\n" \
  "v_xor_b32 %[t1], 0x10020, %[pb]\n" \
  "ds_read_b64_tr_b16 v[30:31], %[t1] offset:0\n" \
  "ds_read_b64_tr_b16 v[146:147], %[t1] offset:16384\n" \
  "v_xor_b32 %[t0], 0x11000, %[pb]\n" \
  "ds_read_b64_tr_b16 v[2:3], %[t0] offset:0\n" \
  "ds_read_b64_tr_b16 v[194:195], %[t0] offset:16384\n" \
  "v_xor_b32 %[t1], 0x11020, %[pb]\n" \
  "ds_read_b64_tr_b16 v[50:51], %[t1] offset:0\n" \
  "ds_read_b64_tr_b16 v[202:203], %[t1] offset:16384\n" \
  "v_xor_b32 %[t0], 0x12000, %[pb]\n" \
  "ds_read_b64_tr_b16 v[18:19], %[t0] offset:0\n" \
  "ds_read_b64_tr_b16 v[210:211], %[t0] offset:16384\n" \
  "v_xor_b32 %[t1], 0x12020, %[pb]\n" \
  "ds_read_b64_tr_b16 v[66:67], %[t1] offset:0\n" \
  "ds_read_b64_tr_b16 v[218:219], %[t1] offset:16384\n" \
  "v_xor_b32 %[t0], 0x13000, %[pb]\n" \
  "ds_read_b64_tr_b16 v[34:35], %[t0] offset:0\n" \
  "ds_read_b64_tr_b16 v[130:131], %[t0] offset:16384\n" \
  "v_xor_b32 %[t1], 0x13020, %[pb]\n" \
  "ds_read_b64_tr_b16 v[82:83], %[t1] offset:0\n" \
  "ds_read_b64_tr_b16 v[178:179], %[t1] offset:16384\n" \
  ".Ls_%=:\n" \
  "s_waitcnt lgkmcnt(0)\n"
#define PRO0_3 \
  "s_cmp_lg_u32 %[hb], 1\n" \
  "s_cbranch_scc1 .Ls_%=\n" \
  "v_xor_b32 %[t0], 0x18000, %[pb]\n" \
  "ds_read_b64_tr_b16 v[14:15], %[t0] offset:0\n" \
  "ds_read_b64_tr_b16 v[98:99], %[t0] offset:16384\n" \
  "v_xor_b32 %[t1], 0x18020, %[pb]\n" \
  "ds_read_b64_tr_b16 v[30:31], %[t1] offset:0\n" \
  "ds_read_b64_tr_b16 v[146:147], %[t1] offset:16384\n" \
  "v_xor_b32 %[t0], 0x19000, %[pb]\n" \
  "ds_read_b64_tr_b16 v[2:3], %[t0] offset:0\n" \
  "ds_read_b64_tr_b16 v[194:195], %[t0] offset:16384\n" \
  "v_xor_b32 %[t1], 0x19020, %[pb]\n" \
  "ds_read_b64_tr_b16 v[50:51], %[t1] offset:0\n" \
  "ds_read_b64_tr_b16 v[202:203], %[t1] offset:16384\n" \
  "v_xor_b32 %[t0], 0x1a000, %[pb]\n" \
  "ds_read_b64_tr_b16 v[18:19], %[t0] offset:0\n" \
  "ds_read_b64_tr_b16 v[210:211], %[t0] offset:16384\n" \
  "v_xor_b32 %[t1], 0x1a020, %[pb]\n" \
  "ds_read_b64_tr_b16 v[66:67], %[t1] offset:0\n" \
  "ds_read_b64_tr_b16 v[218:219], %[t1] offset:16384\n" \
  "v_xor_b32 %[t0], 0x1b000, %[pb]\n" \
  "ds_read_b64_tr_b16 v[34:35], %[t0] offset:0\n" \
  "ds_read_b64_tr_b16 v[130:131], %[t0] offset:16384\n" \
  "v_xor_b32 %[t1], 0x1b020, %[pb]\n" \
  "ds_read_b64_tr_b16 v[82:83], %[t1] offset:0\n" \
  "ds_read_b64_tr_b16 v[178:179], %[t1] offset:16384\n" \
  ".Ls_%=:\n" \
  "s_waitcnt lgkmcnt(0)\n"
#define PRO0_4 \
  "s_cmp_lg_u32 %[hb], 0\n" \
  "s_cbranch_scc1 .Ls_%=\n" \
  "v_xor_b32 %[t0], 0x20000, %[pb]\n" \
  "ds_read_b64_tr_b16 v[60:61], %[t0] offset:0\n" \
  "ds_read_b64_tr_b16 v[100:101], %[t0] offset:16384\n" \
  "v_xor_b32 %[t1], 0x20020, %[pb]\n" \
  "ds_read_b64_tr_b16 v[76:77], %[t1] offset:0\n" \
  "ds_read_b64_tr_b16 v[148:149], %[t1] offset:16384\n" \
  "v_xor_b32 %[t0], 0x21000, %[pb]\n" \
  "ds_read_b64_tr_b16 v[4:5], %[t0] offset:0\n" \
  "ds_read_b64_tr_b16 v[196:197], %[t0] offset:16384\n" \
  "v_xor_b32 %[t1], 0x21020, %[pb]\n" \
  "ds_read_b64_tr_b16 v[52:53], %[t1] offset:0\n" \
  "ds_read_b64_tr_b16 v[204:205], %[t1] offset:16384\n" \
  "v_xor_b32 %[t0], 0x22000, %[pb]\n" \
  "ds_read_b64_tr_b16 v[20:21], %[t0] offset:0\n" \
  "ds_read_b64_tr_b16 v[212:213], %[t0] offset:16384\n" \
  "v_xor_b32 %[t1], 0x22020, %[pb]\n" \
  "ds_read_b64_tr_b16 v[68:69], %[t1] offset:0\n" \
  "ds_read_b64_tr_b16 v[220:221], %[t1] offset:16384\n" \
  "v_xor_b32 %[t0], 0x23000, %[pb]\n" \
  "ds_read_b64_tr_b16 v[36:37], %[t0] offset:0\n" \
  "ds_read_b64_tr_b16 v[132:133], %[t0] offset:16384\n" \
  "v_xor_b32 %[t1], 0x23020, %[pb]\n" \
  "ds_read_b64_tr_b16 v[84:85], %[t1] offset:0\n" \
  "ds_read_b64_tr_b16 v[180:181], %[t1] offset:16384\n" \
  ".Ls_%=:\n" \
  "s_waitcnt lgkmcnt(0)\n"
#define PRO0_5 \
  "s_cmp_lg_u32 %[hb], 1\n" \
  "s_cbranch_scc1 .Ls_%=\n" \
  "v_xor_b32 %[t0], 0x0, %[pb]\n" \
  "ds_read_b64_tr_b16 v[60:61], %[t0] offset:0\n" \
  "ds_read_b64_tr_b16 v[100:101], %[t0] offset:16384\n" \
  "v_xor_b32 %[t1], 0x20, %[pb]\n" \
  "ds_read_b64_tr_b16 v[76:77], %[t1] offset:0\n" \
  "ds_read_b64_tr_b16 v[148:149], %[t1] offset:16384\n" \
  "v_xor_b32 %[t0], 0x1000, %[pb]\n" \
  "ds_read_b64_tr_b16 v[4:5], %[t0] offset:0\n" \
  "ds_read_b64_tr_b16 v[196:197], %[t0] offset:16384\n" \
  "v_xor_b32 %[t1], 0x1020, %[pb]\n" \
  "ds_read_b64_tr_b16 v[52:53], %[t1] offset:0\n" \
  "ds_read_b64_tr_b16 v[204:205], %[t1] offset:16384\n" \
  "v_xor_b32 %[t0], 0x2000, %[pb]\n" \
  "ds_read_b64_tr_b16 v[20:21], %[t0] offset:0\n" \
  "ds_read_b64_tr_b16 v[212:213], %[t0] offset:16384\n" \
  "v_xor_b32 %[t1], 0x2020, %[pb]\n" \
  "ds_read_b64_tr_b16 v[68:69], %[t1] offset:0\n" \
  "ds_read_b64_tr_b16 v[220:221], %[t1] offset:16384\n" \
  "v_xor_b32 %[t0], 0x3000, %[pb]\n" \
  "ds_read_b64_tr_b16 v[36:37], %[t0] offset:0\n" \
  "ds_read_b64_tr_b16 v[132:133], %[t0] offset:16384\n" \
  "v_xor_b32 %[t1], 0x3020, %[pb]\n" \
  "ds_read_b64_tr_b16 v[84:85], %[t1] offset:0\n" \
  "ds_read_b64_tr_b16 v[180:181], %[t1] offset:16384\n" \
  ".Ls_%=:\n" \
  "s_waitcnt lgkmcnt(0)\n"
#define PRO0_6 \
  "s_cmp_lg_u32 %[hb], 0\n" \
  "s_cbranch_scc1 .Ls_%=\n" \
  "v_xor_b32 %[t0], 0x8000, %[pb]\n" \
  "ds_read_b64_tr_b16 v[62:63], %[t0] offset:0\n" \
  "ds_read_b64_tr_b16 v[102:103], %[t0] offset:16384\n" \
  "v_xor_b32 %[t1], 0x8020, %[pb]\n" \
  "ds_read_b64_tr_b16 v[78:79], %[t1] offset:0\n" \
  "ds_read_b64_tr_b16 v[150:151], %[t1] offset:16384\n" \
  "v_xor_b32 %[t0], 0x9000, %[pb]\n" \
  "ds_read_b64_tr_b16 v[6:7], %[t0] offset:0\n" \
  "ds_read_b64_tr_b16 v[198:199], %[t0] offset:16384\n" \
  "v_xor_b32 %[t1], 0x9020, %[pb]\n" \
  "ds_read_b64_tr_b16 v[54:55], %[t1] offset:0\n" \
  "ds_read_b64_tr_b16 v[206:207], %[t1] offset:16384\n" \
  "v_xor_b32 %[t0], 0xa000, %[pb]\n" \
  "ds_read_b64_tr_b16 v[22:23], %[t0] offset:0\n" \
  "ds_read_b64_tr_b16 v[214:215], %[t0] offset:16384\n" \
  "v_xor_b32 %[t1], 0xa020, %[pb]\n" \
  "ds_read_b64_tr_b16 v[70:71], %[t1] offset:0\n" \
  "ds_read_b64_tr_b16 v[222:223], %[t1] offset:16384\n" \
  "v_xor_b32 %[t0], 0xb000, %[pb]\n" \
  "ds_read_b64_tr_b16 v[38:39], %[t0] offset:0\n" \
  "ds_read_b64_tr_b16 v[134:135], %[t0] offset:16384\n" \
  "v_xor_b32 %[t1], 0xb020, %[pb]\n" \
  "ds_read_b64_tr_b16 v[86:87], %[t1] offset:0\n" \
  "ds_read_b64_tr_b16 v[182:183], %[t1] offset:16384\n" \
  ".Ls_%=:\n" \
  "s_waitcnt lgkmcnt(0)\n"
#define PRO0_7 \
  "s_cmp_lg_u32 %[hb], 1\n" \
  "s_cbranch_scc1 .Ls_%=\n" \
  "v_xor_b32 %[t0], 0x10000, %[pb]\n" \
  "ds_read_b64_tr_b16 v[62:63], %[t0] offset:0\n" \
  "ds_read_b64_tr_b16 v[102:103], %[t0] offset:16384\n" \
  "v_xor_b32 %[t1], 0x10020, %[pb]\n" \
  "ds_read_b64_tr_b16 v[78:79], %[t1] offset:0\n" \
  "ds_read_b64_tr_b16 v[150:151], %[t1] offset:16384\n" \
  "v_xor_b32 %[t0], 0x11000, %[pb]\n" \
  "ds_read_b64_tr_b16 v[6:7], %[t0] offset:0\n" \
  "ds_read_b64_tr_b16 v[198:199], %[t0] offset:16384\n" \
  "v_xor_b32 %[t1], 0x11020, %[pb]\n" \
  "ds_read_b64_tr_b16 v[54:55], %[t1] offset:0\n" \
  "ds_read_b64_tr_b16 v[206:207], %[t1] offset:16384\n" \
  "v_xor_b32 %[t0], 0x12000, %[pb]\n" \
  "ds_read_b64_tr_b16 v[22:23], %[t0] offset:0\n" \
  "ds_read_b64_tr_b16 v[214:215], %[t0] offset:16384\n" \
  "v_xor_b32 %[t1], 0x12020, %[pb]\n" \
  "ds_read_b64_tr_b16 v[70:71], %[t1] offset:0\n" \
  "ds_read_b64_tr_b16 v[222:223], %[t1] offset:16384\n" \
  "v_xor_b32 %[t0], 0x13000, %[pb]\n" \
  "ds_read_b64_tr_b16 v[38:39], %[t0] offset:0\n" \
  "ds_read_b64_tr_b16 v[134:135], %[t0] offset:16384\n" \
  "v_xor_b32 %[t1], 0x13020, %[pb]\n" \
  "ds_read_b64_tr_b16 v[86:87], %[t1] offset:0\n" \
  "ds_read_b64_tr_b16 v[182:183], %[t1] offset:16384\n" \
  ".Ls_%=:\n" \
  "s_waitcnt lgkmcnt(0)\n"
#define PRO0_8 \
  "s_cmp_lg_u32 %[hb], 0\n" \
  "s_cbranch_scc1 .Ls_%=\n" \
  "v_xor_b32 %[t0], 0x18000, %[pb]\n" \
  "ds_read_b64_tr_b16 v[224:225], %[t0] offset:0\n" \
  "ds_read_b64_tr_b16 v[104:105], %[t0] offset:16384\n" \
  "v_xor_b32 %[t1], 0x18020, %[pb]\n" \
  "ds_read_b64_tr_b16 v[232:233], %[t1] offset:0\n" \
  "ds_read_b64_tr_b16 v[152:153], %[t1] offset:16384\n" \
  "v_xor_b32 %[t0], 0x19000, %[pb]\n" \
  "ds_read_b64_tr_b16 v[8:9], %[t0] offset:0\n" \
  "ds_read_b64_tr_b16 v[112:113], %[t0] offset:16384\n" \
  "v_xor_b32 %[t1], 0x19020, %[pb]\n" \
  "ds_read_b64_tr_b16 v[56:57], %[t1] offset:0\n" \
  "ds_read_b64_tr_b16 v[160:161], %[t1] offset:16384\n" \
  "v_xor_b32 %[t0], 0x1a000, %[pb]\n" \
  "ds_read_b64_tr_b16 v[24:25], %[t0] offset:0\n" \
  "ds_read_b64_tr_b16 v[120:121], %[t0] offset:16384\n" \
  "v_xor_b32 %[t1], 0x1a020, %[pb]\n" \
  "ds_read_b64_tr_b16 v[72:73], %[t1] offset:0\n" \
  "ds_read_b64_tr_b16 v[168:169], %[t1] offset:16384\n" \
  "v_xor_b32 %[t0], 0x1b000, %[pb]\n" \
  "ds_read_b64_tr_b16 v[40:41], %[t0] offset:0\n" \
  "ds_read_b64_tr_b16 v[136:137], %[t0] offset:16384\n" \
  "v_xor_b32 %[t1], 0x1b020, %[pb]\n" \
  "ds_read_b64_tr_b16 v[88:89], %[t1] offset:0\n" \
  "ds_read_b64_tr_b16 v[184:185], %[t1] offset:16384\n" \
  ".Ls_%=:\n" \
  "s_waitcnt lgkmcnt(0)\n"
#define PRO0_9 \
  "s_cmp_lg_u32 %[hb], 1\n" \
  "s_cbranch_scc1 .Ls_%=\n" \
  "v_xor_b32 %[t0], 0x20000, %[pb]\n" \
  "ds_read_b64_tr_b16 v[224:225], %[t0] offset:0\n" \
  "ds_read_b64_tr_b16 v[104:105], %[t0] offset:16384\n" \
  "v_xor_b32 %[t1], 0x20020, %[pb]\n" \
  "ds_read_b64_tr_b16 v[232:233], %[t1] offset:0\n" \
  "ds_read_b64_tr_b16 v[152:153], %[t1] offset:16384\n" \
  "v_xor_b32 %[t0], 0x21000, %[pb]\n" \
  "ds_read_b64_tr_b16 v[8:9], %[t0] offset:0\n" \
  "ds_read_b64_tr_b16 v[112:113], %[t0] offset:16384\n" \
  "v_xor_b32 %[t1], 0x21020, %[pb]\n" \
  "ds_read_b64_tr_b16 v[56:57], %[t1] offset:0\n" \
  "ds_read_b64_tr_b16 v[160:161], %[t1] offset:16384\n" \
  "v_xor_b32 %[t0], 0x22000, %[pb]\n" \
  "ds_read_b64_tr_b16 v[24:25], %[t0] offset:0\n" \
  "ds_read_b64_tr_b16 v[120:121], %[t0] offset:16384\n" \
  "v_xor_b32 %[t1], 0x22020, %[pb]\n" \
  "ds_read_b64_tr_b16 v[72:73], %[t1] offset:0\n" \
  "ds_read_b64_tr_b16 v[168:169], %[t1] offset:16384\n" \
  "v_xor_b32 %[t0], 0x23000, %[pb]\n" \
  "ds_read_b64_tr_b16 v[40:41], %[t0] offset:0\n" \
  "ds_read_b64_tr_b16 v[136:137], %[t0] offset:16384\n" \
  "v_xor_b32 %[t1], 0x23020, %[pb]\n" \
  "ds_read_b64_tr_b16 v[88:89], %[t1] offset:0\n" \
  "ds_read_b64_tr_b16 v[184:185], %[t1] offset:16384\n" \
  ".Ls_%=:\n" \
  "s_waitcnt lgkmcnt(0)\n"
#define PRO0_10 \
  "s_cmp_lg_u32 %[hb], 0\n" \
  "s_cbranch_scc1 .Ls_%=\n" \
  "v_xor_b32 %[t0], 0x0, %[pb]\n" \
  "ds_read_b64_tr_b16 v[226:227], %[t0] offset:0\n" \
  "ds_read_b64_tr_b16 v[106:107], %[t0] offset:16384\n" \
  "v_xor_b32 %[t1], 0x20, %[pb]\n" \
  "ds_read_b64_tr_b16 v[234:235], %[t1] offset:0\n" \
  "ds_read_b64_tr_b16 v[154:155], %[t1] offset:16384\n" \
  "v_xor_b32 %[t0], 0x1000, %[pb]\n" \
  "ds_read_b64_tr_b16 v[10:11], %[t0] offset:0\n" \
  "ds_read_b64_tr_b16 v[114:115], %[t0] offset:16384\n" \
  "v_xor_b32 %[t1], 0x1020, %[pb]\n" \
  "ds_read_b64_tr_b16 v[58:59], %[t1] offset:0\n" \
  "ds_read_b64_tr_b16 v[162:163], %[t1] offset:16384\n" \
  "v_xor_b32 %[t0], 0x2000, %[pb]\n" \
  "ds_read_b64_tr_b16 v[26:27], %[t0] offset:0\n" \
  "ds_read_b64_tr_b16 v[122:123], %[t0] offset:16384\n" \
  "v_xor_b32 %[t1], 0x2020, %[pb]\n" \
  "ds_read_b64_tr_b16 v[74:75], %[t1] offset:0\n" \
  "ds_read_b64_tr_b16 v[170:171], %[t1] offset:16384\n" \
  "v_xor_b32 %[t0], 0x3000, %[pb]\n" \
  "ds_read_b64_tr_b16 v[42:43], %[t0] offset:0\n" \
  "ds_read_b64_tr_b16 v[138:139], %[t0] offset:16384\n" \
  "v_xor_b32 %[t1], 0x3020, %[pb]\n" \
  "ds_read_b64_tr_b16 v[90:91], %[t1] offset:0\n" \
  "ds_read_b64_tr_b16 v[186:187], %[t1] offset:16384\n" \
  ".Ls_%=:\n" \
  "s_waitcnt lgkmcnt(0)\n"
#define PRO0_11 \
  "s_cmp_lg_u32 %[hb], 1\n" \
  "s_cbranch_scc1 .Ls_%=\n" \
  "v_xor_b32 %[t0], 0x8000, %[pb]\n" \
  "ds_read_b64_tr_b16 v[226:227], %[t0] offset:0\n" \
  "ds_read_b64_tr_b16 v[106:107], %[t0] offset:16384\n" \
  "v_xor_b32 %[t1], 0x8020, %[pb]\n" \
  "ds_read_b64_tr_b16 v[234:235], %[t1] offset:0\n" \
  "ds_read_b64_tr_b16 v[154:155], %[t1] offset:16384\n" \
  "v_xor_b32 %[t0], 0x9000, %[pb]\n" \
  "ds_read_b64_tr_b16 v[10:11], %[t0] offset:0\n" \
  "ds_read_b64_tr_b16 v[114:115], %[t0] offset:16384\n" \
  "v_xor_b32 %[t1], 0x9020, %[pb]\n" \
  "ds_read_b64_tr_b16 v[58:59], %[t1] offset:0\n" \
  "ds_read_b64_tr_b16 v[162:163], %[t1] offset:16384\n" \
  "v_xor_b32 %[t0], 0xa000, %[pb]\n" \
  "ds_read_b64_tr_b16 v[26:27], %[t0] offset:0\n" \
  "ds_read_b64_tr_b16 v[122:123], %[t0] offset:16384\n" \
  "v_xor_b32 %[t1], 0xa020, %[pb]\n" \
  "ds_read_b64_tr_b16 v[74:75], %[t1] offset:0\n" \
  "ds_read_b64_tr_b16 v[170:171], %[t1] offset:16384\n" \
  "v_xor_b32 %[t0], 0xb000, %[pb]\n" \
  "ds_read_b64_tr_b16 v[42:43], %[t0] offset:0\n" \
  "ds_read_b64_tr_b16 v[138:139], %[t0] offset:16384\n" \
  "v_xor_b32 %[t1], 0xb020, %[pb]\n" \
  "ds_read_b64_tr_b16 v[90:91], %[t1] offset:0\n" \
  "ds_read_b64_tr_b16 v[186:187], %[t1] offset:16384\n" \
  ".Ls_%=:\n" \
  "s_waitcnt lgkmcnt(0)\n"
#define PRO0_12 \
  "v_add_u32 %[t0], 0x8000, %[b0]\n" \
  "ds_write_b128 %[t0], v[12:15]\n" \
  "ds_write_b128 %[t0], v[60:63] offset:1024\n" \
  "v_add_u32 %[t0], 0xc000, %[b0]\n" \
  "ds_write_b128 %[t0], v[28:31]\n" \
  "ds_write_b128 %[t0], v[76:79] offset:1024\n" \
  "s_cmp_lg_u32 %[hb], 0\n" \
  "s_cbranch_scc1 .Ls_%=\n" \
  "v_xor_b32 %[t0], 0x10000, %[pb]\n" \
  "ds_read_b64_tr_b16 v[228:229], %[t0] offset:0\n" \
  "ds_read_b64_tr_b16 v[108:109], %[t0] offset:16384\n" \
  "v_xor_b32 %[t1], 0x10020, %[pb]\n" \
  "ds_read_b64_tr_b16 v[236:237], %[t1] offset:0\n" \
  "ds_read_b64_tr_b16 v[156:157], %[t1] offset:16384\n" \
  "v_xor_b32 %[t0], 0x11000, %[pb]\n" \
  "ds_read_b64_tr_b16 v[12:13], %[t0] offset:0\n" \
  "ds_read_b64_tr_b16 v[116:117], %[t0] offset:16384\n" \
  "v_xor_b32 %[t1], 0x11020, %[pb]\n" \
  "ds_read_b64_tr_b16 v[60:61], %[t1] offset:0\n" \
  "ds_read_b64_tr_b16 v[164:165], %[t1] offset:16384\n" \
  "v_xor_b32 %[t0], 0x12000, %[pb]\n" \
  "ds_read_b64_tr_b16 v[28:29], %[t0] offset:0\n" \
  "ds_read_b64_tr_b16 v[124:125], %[t0] offset:16384\n" \
  "v_xor_b32 %[t1], 0x12020, %[pb]\n" \
  "ds_read_b64_tr_b16 v[76:77], %[t1] offset:0\n" \
  "ds_read_b64_tr_b16 v[172:173], %[t1] offset:16384\n" \
  "v_xor_b32 %[t0], 0x13000, %[pb]\n" \
  "ds_read_b64_tr_b16 v[44:45], %[t0] offset:0\n" \
  "ds_read_b64_tr_b16 v[140:141], %[t0] offset:16384\n" \
  "v_xor_b32 %[t1], 0x13020, %[pb]\n" \
  "ds_read_b64_tr_b16 v[92:93], %[t1] offset:0\n" \
  "ds_read_b64_tr_b16 v[188:189], %[t1] offset:16384\n" \
  ".Ls_%=:\n" \
  "s_waitcnt lgkmcnt(0)\n"
#define PRO0_13 \
  "v_add_u32 %[t0], 0x10000, %[b0]\n" \
  "ds_write_b128 %[t0], v[192:195]\n" \
  "ds_write_b128 %[t0], v[196:199] offset:1024\n" \
  "v_add_u32 %[t0], 0x14000, %[b0]\n" \
  "ds_write_b128 %[t0], v[200:203]\n" \
  "ds_write_b128 %[t0], v[204:207] offset:1024\n" \
  "s_cmp_lg_u32 %[hb], 1\n" \
  "s_cbranch_scc1 .Ls_%=\n" \
  "v_xor_b32 %[t0], 0x18000, %[pb]\n" \
  "ds_read_b64_tr_b16 v[228:229], %[t0] offset:0\n" \
  "ds_read_b64_tr_b16 v[108:109], %[t0] offset:16384\n" \
  "v_xor_b32 %[t1], 0x18020, %[pb]\n" \
  "ds_read_b64_tr_b16 v[236:237], %[t1] offset:0\n" \
  "ds_read_b64_tr_b16 v[156:157], %[t1] offset:16384\n" \
  "v_xor_b32 %[t0], 0x19000, %[pb]\n" \
  "ds_read_b64_tr_b16 v[12:13], %[t0] offset:0\n" \
  "ds_read_b64_tr_b16 v[116:117], %[t0] offset:16384\n" \
  "v_xor_b32 %[t1], 0x19020, %[pb]\n" \
  "ds_read_b64_tr_b16 v[60:61], %[t1] offset:0\n" \
  "ds_read_b64_tr_b16 v[164:165], %[t1] offset:16384\n" \
  "v_xor_b32 %[t0], 0x1a000, %[pb]\n" \
  "ds_read_b64_tr_b16 v[28:29], %[t0] offset:0\n" \
  "ds_read_b64_tr_b16 v[124:125], %[t0] offset:16384\n" \
  "v_xor_b32 %[t1], 0x1a020, %[pb]\n" \
  "ds_read_b64_tr_b16 v[76:77], %[t1] offset:0\n" \
  "ds_read_b64_tr_b16 v[172:173], %[t1] offset:16384\n" \
  "v_xor_b32 %[t0], 0x1b000, %[pb]\n" \
  "ds_read_b64_tr_b16 v[44:45], %[t0] offset:0\n" \
  "ds_read_b64_tr_b16 v[140:141], %[t0] offset:16384\n" \
  "v_xor_b32 %[t1], 0x1b020, %[pb]\n" \
  "ds_read_b64_tr_b16 v[92:93], %[t1] offset:0\n" \
  "ds_read_b64_tr_b16 v[188:189], %[t1] offset:16384\n" \
  ".Ls_%=:\n" \
  "s_waitcnt lgkmcnt(0)\n"
#define PRO0_14 \
  "v_add_u32 %[t0], 0x18000, %[b0]\n" \
  "ds_write_b128 %[t0], v[208:211]\n" \
  "ds_write_b128 %[t0], v[212:215] offset:1024\n" \
  "v_add_u32 %[t0], 0x1c000, %[b0]\n" \
  "ds_write_b128 %[t0], v[216:219]\n" \
  "ds_write_b128 %[t0], v[220:223] offset:1024\n" \
  "s_cmp_lg_u32 %[hb], 0\n" \
  "s_cbranch_scc1 .Ls_%=\n" \
  "v_xor_b32 %[t0], 0x20000, %[pb]\n" \
  "ds_read_b64_tr_b16 v[230:231], %[t0] offset:0\n" \
  "ds_read_b64_tr_b16 v[110:111], %[t0] offset:16384\n" \
  "v_xor_b32 %[t1], 0x20020, %[pb]\n" \
  "ds_read_b64_tr_b16 v[238:239], %[t1] offset:0\n" \
  "ds_read_b64_tr_b16 v[158:159], %[t1] offset:16384\n" \
  "v_xor_b32 %[t0], 0x21000, %[pb]\n" \
  "ds_read_b64_tr_b16 v[14:15], %[t0] offset:0\n" \
  "ds_read_b64_tr_b16 v[118:119], %[t0] offset:16384\n" \
  "v_xor_b32 %[t1], 0x21020, %[pb]\n" \
  "ds_read_b64_tr_b16 v[62:63], %[t1] offset:0\n" \
  "ds_read_b64_tr_b16 v[166:167], %[t1] offset:16384\n" \
  "v_xor_b32 %[t0], 0x22000, %[pb]\n" \
  "ds_read_b64_tr_b16 v[30:31], %[t0] offset:0\n" \
  "ds_read_b64_tr_b16 v[126:127], %[t0] offset:16384\n" \
  "v_xor_b32 %[t1], 0x22020, %[pb]\n" \
  "ds_read_b64_tr_b16 v[78:79], %[t1] offset:0\n" \
  "ds_read_b64_tr_b16 v[174:175], %[t1] offset:16384\n" \
  "v_xor_b32 %[t0], 0x23000, %[pb]\n" \
  "ds_read_b64_tr_b16 v[46:47], %[t0] offset:0\n" \
  "ds_read_b64_tr_b16 v[142:143], %[t0] offset:16384\n" \
  "v_xor_b32 %[t1], 0x23020, %[pb]\n" \
  "ds_read_b64_tr_b16 v[94:95], %[t1] offset:0\n" \
  "ds_read_b64_tr_b16 v[190:191], %[t1] offset:16384\n" \
  ".Ls_%=:\n" \
  "s_waitcnt lgkmcnt(0)\n"
#define PRO0_15 \
  "s_cmp_lg_u32 %[hb], 1\n" \
  "s_cbranch_scc1 .Ls_%=\n" \
  "v_xor_b32 %[t0], 0x0, %[pb]\n" \
  "ds_read_b64_tr_b16 v[230:231], %[t0] offset:0\n" \
  "ds_read_b64_tr_b16 v[110:111], %[t0] offset:16384\n" \
  "v_xor_b32 %[t1], 0x20, %[pb]\n" \
  "ds_read_b64_tr_b16 v[238:239], %[t1] offset:0\n" \
  "ds_read_b64_tr_b16 v[158:159], %[t1] offset:16384\n" \
  "v_xor_b32 %[t0], 0x1000, %[pb]\n" \
  "ds_read_b64_tr_b16 v[14:15], %[t0] offset:0\n" \
  "ds_read_b64_tr_b16 v[118:119], %[t0] offset:16384\n" \
  "v_xor_b32 %[t1], 0x1020, %[pb]\n" \
  "ds_read_b64_tr_b16 v[62:63], %[t1] offset:0\n" \
  "ds_read_b64_tr_b16 v[166:167], %[t1] offset:16384\n" \
  "v_xor_b32 %[t0], 0x2000, %[pb]\n" \
  "ds_read_b64_tr_b16 v[30:31], %[t0] offset:0\n" \
  "ds_read_b64_tr_b16 v[126:127], %[t0] offset:16384\n" \
  "v_xor_b32 %[t1], 0x2020, %[pb]\n" \
  "ds_read_b64_tr_b16 v[78:79], %[t1] offset:0\n" \
  "ds_read_b64_tr_b16 v[174:175], %[t1] offset:16384\n" \
  "v_xor_b32 %[t0], 0x3000, %[pb]\n" \
  "ds_read_b64_tr_b16 v[46:47], %[t0] offset:0\n" \
  "ds_read_b64_tr_b16 v[142:143], %[t0] offset:16384\n" \
  "v_xor_b32 %[t1], 0x3020, %[pb]\n" \
  "ds_read_b64_tr_b16 v[94:95], %[t1] offset:0\n" \
  "ds_read_b64_tr_b16 v[190:191], %[t1] offset:16384\n" \
  ".Ls_%=:\n" \
  "s_waitcnt lgkmcnt(0)\n" \
  "v_add_u32 %[t0], 0x20000, %[b0]\n" \
  "ds_write_b128 %[t0], v[224:227]\n" \
  "ds_write_b128 %[t0], v[228:231] offset:1024\n" \
  "v_add_u32 %[t0], 0x24000, %[b0]\n" \
  "ds_write_b128 %[t0], v[232:235]\n" \
  "ds_write_b128 %[t0], v[236:239] offset:1024\n"
#define PRO1_0 \
  "v_add_u32 %[t0], 0x0, %[pa]\n" \
  "ds_read_b64_tr_b16 v[96:97], %[t0] offset:0\n" \
  "ds_read_b64_tr_b16 v[100:101], %[t0] offset:1024\n" \
  "ds_read_b64_tr_b16 v[104:105], %[t0] offset:8192\n" \
  "ds_read_b64_tr_b16 v[108:109], %[t0] offset:9216\n" \
  "s_cmp_lg_u32 %[hb], 0\n" \
  "s_cbranch_scc1 .Ls_%=\n" \
  "v_xor_b32 %[t0], 0x0, %[pb]\n" \
  "ds_read_b64_tr_b16 v[192:193], %[t0] offset:0\n" \
  "v_xor_b32 %[t1], 0x80, %[pb]\n" \
  "ds_read_b64_tr_b16 v[200:201], %[t1] offset:0\n" \
  "v_xor_b32 %[t0], 0x1000, %[pb]\n" \
  "ds_read_b64_tr_b16 v[0:1], %[t0] offset:0\n" \
  "v_xor_b32 %[t1], 0x1080, %[pb]\n" \
  "ds_read_b64_tr_b16 v[48:49], %[t1] offset:0\n" \
  "v_xor_b32 %[t0], 0x2000, %[pb]\n" \
  "ds_read_b64_tr_b16 v[16:17], %[t0] offset:0\n" \
  "v_xor_b32 %[t1], 0x2080, %[pb]\n" \
  "ds_read_b64_tr_b16 v[64:65], %[t1] offset:0\n" \
  "v_xor_b32 %[t0], 0x3000, %[pb]\n" \
  "ds_read_b64_tr_b16 v[32:33], %[t0] offset:0\n" \
  "v_xor_b32 %[t1], 0x3080, %[pb]\n" \
  "ds_read_b64_tr_b16 v[80:81], %[t1] offset:0\n" \
  ".Ls_%=:\n" \
  "s_waitcnt lgkmcnt(0)\n"
#define PRO1_1 \
  "v_add_u32 %[t0], 0x8000, %[pa]\n" \
  "ds_read_b64_tr_b16 v[98:99], %[t0] offset:0\n" \
  "ds_read_b64_tr_b16 v[102:103], %[t0] offset:1024\n" \
  "ds_read_b64_tr_b16 v[106:107], %[t0] offset:8192\n" \
  "ds_read_b64_tr_b16 v[110:111], %[t0] offset:9216\n" \
  "s_cmp_lg_u32 %[hb], 1\n" \
  "s_cbranch_scc1 .Ls_%=\n" \
  "v_xor_b32 %[t0], 0x8000, %[pb]\n" \
  "ds_read_b64_tr_b16 v[192:193], %[t0] offset:0\n" \
  "v_xor_b32 %[t1], 0x8080, %[pb]\n" \
  "ds_read_b64_tr_b16 v[200:201], %[t1] offset:0\n" \
  "v_xor_b32 %[t0], 0x9000, %[pb]\n" \
  "ds_read_b64_tr_b16 v[0:1], %[t0] offset:0\n" \
  "v_xor_b32 %[t1], 0x9080, %[pb]\n" \
  "ds_read_b64_tr_b16 v[48:49], %[t1] offset:0\n" \
  "v_xor_b32 %[t0], 0xa000, %[pb]\n" \
  "ds_read_b64_tr_b16 v[16:17], %[t0] offset:0\n" \
  "v_xor_b32 %[t1], 0xa080, %[pb]\n" \
  "ds_read_b64_tr_b16 v[64:65], %[t1] offset:0\n" \
  "v_xor_b32 %[t0], 0xb000, %[pb]\n" \
  "ds_read_b64_tr_b16 v[32:33], %[t0] offset:0\n" \
  "v_xor_b32 %[t1], 0xb080, %[pb]\n" \
  "ds_read_b64_tr_b16 v[80:81], %[t1] offset:0\n" \
  ".Ls_%=:\n" \
  "s_waitcnt lgkmcnt(0)\n" \
  "v_swap_b32 v97, v98\n" \
  "v_swap_b32 v101, v102\n" \
  "v_swap_b32 v105, v106\n" \
  "v_swap_b32 v109, v110\n"
#define PRO1_2 \
  "v_add_u32 %[t0], 0x10000, %[pa]\n" \
  "ds_read_b64_tr_b16 v[112:113], %[t0] offset:0\n" \
  "ds_read_b64_tr_b16 v[116:117], %[t0] offset:1024\n" \
  "ds_read_b64_tr_b16 v[120:121], %[t0] offset:8192\n" \
  "ds_read_b64_tr_b16 v[124:125], %[t0] offset:9216\n" \
  "s_cmp_lg_u32 %[hb], 0\n" \
  "s_cbranch_scc1 .Ls_%=\n" \
  "v_xor_b32 %[t0], 0x10000, %[pb]\n" \
  "ds_read_b64_tr_b16 v[194:195], %[t0] offset:0\n" \
  "v_xor_b32 %[t1], 0x10080, %[pb]\n" \
  "ds_read_b64_tr_b16 v[202:203], %[t1] offset:0\n" \
  "v_xor_b32 %[t0], 0x11000, %[pb]\n" \
  "ds_read_b64_tr_b16 v[2:3], %[t0] offset:0\n" \
  "v_xor_b32 %[t1], 0x11080, %[pb]\n" \
  "ds_read_b64_tr_b16 v[50:51], %[t1] offset:0\n" \
  "v_xor_b32 %[t0], 0x12000, %[pb]\n" \
  "ds_read_b64_tr_b16 v[18:19], %[t0] offset:0\n" \
  "v_xor_b32 %[t1], 0x12080, %[pb]\n" \
  "ds_read_b64_tr_b16 v[66:67], %[t1] offset:0\n" \
  "v_xor_b32 %[t0], 0x13000, %[pb]\n" \
  "ds_read_b64_tr_b16 v[34:35], %[t0] offset:0\n" \
  "v_xor_b32 %[t1], 0x13080, %[pb]\n" \
  "ds_read_b64_tr_b16 v[82:83], %[t1] offset:0\n" \
  ".Ls_%=:\n" \
  "s_waitcnt lgkmcnt(0)\n"
#define PRO1_3 \
  "v_add_u32 %[t0], 0x18000, %[pa]\n" \
  "ds_read_b64_tr_b16 v[114:115], %[t0] offset:0\n" \
  "ds_read_b64_tr_b16 v[118:119], %[t0] offset:1024\n" \
  "ds_read_b64_tr_b16 v[122:123], %[t0] offset:8192\n" \
  "ds_read_b64_tr_b16 v[126:127], %[t0] offset:9216\n" \
  "s_cmp_lg_u32 %[hb], 1\n" \
  "s_cbranch_scc1 .Ls_%=\n" \
  "v_xor_b32 %[t0], 0x18000, %[pb]\n" \
  "ds_read_b64_tr_b16 v[194:195], %[t0] offset:0\n" \
  "v_xor_b32 %[t1], 0x18080, %[pb]\n" \
  "ds_read_b64_tr_b16 v[202:203], %[t1] offset:0\n" \
  "v_xor_b32 %[t0], 0x19000, %[pb]\n" \
  "ds_read_b64_tr_b16 v[2:3], %[t0] offset:0\n" \
  "v_xor_b32 %[t1], 0x19080, %[pb]\n" \
  "ds_read_b64_tr_b16 v[50:51], %[t1] offset:0\n" \
  "v_xor_b32 %[t0], 0x1a000, %[pb]\n" \
  "ds_read_b64_tr_b16 v[18:19], %[t0] offset:0\n" \
  "v_xor_b32 %[t1], 0x1a080, %[pb]\n" \
  "ds_read_b64_tr_b16 v[66:67], %[t1] offset:0\n" \
  "v_xor_b32 %[t0], 0x1b000, %[pb]\n" \
  "ds_read_b64_tr_b16 v[34:35], %[t0] offset:0\n" \
  "v_xor_b32 %[t1], 0x1b080, %[pb]\n" \
  "ds_read_b64_tr_b16 v[82:83], %[t1] offset:0\n" \
  ".Ls_%=:\n" \
  "s_waitcnt lgkmcnt(0)\n" \
  "v_swap_b32 v113, v114\n" \
  "v_swap_b32 v117, v118\n" \
  "v_swap_b32 v121, v122\n" \
  "v_swap_b32 v125, v126\n"
#define PRO1_4 \
  "v_add_u32 %[t0], 0x20000, %[pa]\n" \
  "ds_read_b64_tr_b16 v[128:129], %[t0] offset:0\n" \
  "ds_read_b64_tr_b16 v[132:133], %[t0] offset:1024\n" \
  "ds_read_b64_tr_b16 v[136:137], %[t0] offset:8192\n" \
  "ds_read_b64_tr_b16 v[140:141], %[t0] offset:9216\n" \
  "s_cmp_lg_u32 %[hb], 0\n" \
  "s_cbranch_scc1 .Ls_%=\n" \
  "v_xor_b32 %[t0], 0x20000, %[pb]\n" \
  "ds_read_b64_tr_b16 v[196:197], %[t0] offset:0\n" \
  "v_xor_b32 %[t1], 0x20080, %[pb]\n" \
  "ds_read_b64_tr_b16 v[204:205], %[t1] offset:0\n" \
  "v_xor_b32 %[t0], 0x21000, %[pb]\n" \
  "ds_read_b64_tr_b16 v[4:5], %[t0] offset:0\n" \
  "v_xor_b32 %[t1], 0x21080, %[pb]\n" \
  "ds_read_b64_tr_b16 v[52:53], %[t1] offset:0\n" \
  "v_xor_b32 %[t0], 0x22000, %[pb]\n" \
  "ds_read_b64_tr_b16 v[20:21], %[t0] offset:0\n" \
  "v_xor_b32 %[t1], 0x22080, %[pb]\n" \
  "ds_read_b64_tr_b16 v[68:69], %[t1] offset:0\n" \
  "v_xor_b32 %[t0], 0x23000, %[pb]\n" \
  "ds_read_b64_tr_b16 v[36:37], %[t0] offset:0\n" \
  "v_xor_b32 %[t1], 0x23080, %[pb]\n" \
  "ds_read_b64_tr_b16 v[84:85], %[t1] offset:0\n" \
  ".Ls_%=:\n" \
  "s_waitcnt lgkmcnt(0)\n"
#define PRO1_5 \
  "v_add_u32 %[t0], 0x0, %[pa]\n" \
  "ds_read_b64_tr_b16 v[130:131], %[t0] offset:0\n" \
  "ds_read_b64_tr_b16 v[134:135], %[t0] offset:1024\n" \
  "ds_read_b64_tr_b16 v[138:139], %[t0] offset:8192\n" \
  "ds_read_b64_tr_b16 v[142:143], %[t0] offset:9216\n" \
  "s_cmp_lg_u32 %[hb], 1\n" \
  "s_cbranch_scc1 .Ls_%=\n" \
  "v_xor_b32 %[t0], 0x0, %[pb]\n" \
  "ds_read_b64_tr_b16 v[196:197], %[t0] offset:0\n" \
  "v_xor_b32 %[t1], 0x80, %[pb]\n" \
  "ds_read_b64_tr_b16 v[204:205], %[t1] offset:0\n" \
  "v_xor_b32 %[t0], 0x1000, %[pb]\n" \
  "ds_read_b64_tr_b16 v[4:5], %[t0] offset:0\n" \
  "v_xor_b32 %[t1], 0x1080, %[pb]\n" \
  "ds_read_b64_tr_b16 v[52:53], %[t1] offset:0\n" \
  "v_xor_b32 %[t0], 0x2000, %[pb]\n" \
  "ds_read_b64_tr_b16 v[20:21], %[t0] offset:0\n" \
  "v_xor_b32 %[t1], 0x2080, %[pb]\n" \
  "ds_read_b64_tr_b16 v[68:69], %[t1] offset:0\n" \
  "v_xor_b32 %[t0], 0x3000, %[pb]\n" \
  "ds_read_b64_tr_b16 v[36:37], %[t0] offset:0\n" \
  "v_xor_b32 %[t1], 0x3080, %[pb]\n" \
  "ds_read_b64_tr_b16 v[84:85], %[t1] offset:0\n" \
  ".Ls_%=:\n" \
  "s_waitcnt lgkmcnt(0)\n" \
  "v_swap_b32 v129, v130\n" \
  "v_swap_b32 v133, v134\n" \
  "v_swap_b32 v137, v138\n" \
  "v_swap_b32 v141, v142\n"
#define PRO1_6 \
  "v_add_u32 %[t0], 0x8000, %[pa]\n" \
  "ds_read_b64_tr_b16 v[144:145], %[t0] offset:0\n" \
  "ds_read_b64_tr_b16 v[148:149], %[t0] offset:1024\n" \
  "ds_read_b64_tr_b16 v[152:153], %[t0] offset:8192\n" \
  "ds_read_b64_tr_b16 v[156:157], %[t0] offset:9216\n" \
  "s_cmp_lg_u32 %[hb], 0\n" \
  "s_cbranch_scc1 .Ls_%=\n" \
  "v_xor_b32 %[t0], 0x8000, %[pb]\n" \
  "ds_read_b64_tr_b16 v[198:199], %[t0] offset:0\n" \
  "v_xor_b32 %[t1], 0x8080, %[pb]\n" \
  "ds_read_b64_tr_b16 v[206:207], %[t1] offset:0\n" \
  "v_xor_b32 %[t0], 0x9000, %[pb]\n" \
  "ds_read_b64_tr_b16 v[6:7], %[t0] offset:0\n" \
  "v_xor_b32 %[t1], 0x9080, %[pb]\n" \
  "ds_read_b64_tr_b16 v[54:55], %[t1] offset:0\n" \
  "v_xor_b32 %[t0], 0xa000, %[pb]\n" \
  "ds_read_b64_tr_b16 v[22:23], %[t0] offset:0\n" \
  "v_xor_b32 %[t1], 0xa080, %[pb]\n" \
  "ds_read_b64_tr_b16 v[70:71], %[t1] offset:0\n" \
  "v_xor_b32 %[t0], 0xb000, %[pb]\n" \
  "ds_read_b64_tr_b16 v[38:39], %[t0] offset:0\n" \
  "v_xor_b32 %[t1], 0xb080, %[pb]\n" \
  "ds_read_b64_tr_b16 v[86:87], %[t1] offset:0\n" \
  ".Ls_%=:\n" \
  "s_waitcnt lgkmcnt(0)\n"
#define PRO1_7 \
  "v_add_u32 %[t0], 0x10000, %[pa]\n" \
  "ds_read_b64_tr_b16 v[146:147], %[t0] offset:0\n" \
  "ds_read_b64_tr_b16 v[150:151], %[t0] offset:1024\n" \
  "ds_read_b64_tr_b16 v[154:155], %[t0] offset:8192\n" \
  "ds_read_b64_tr_b16 v[158:159], %[t0] offset:9216\n" \
  "s_cmp_lg_u32 %[hb], 1\n" \
  "s_cbranch_scc1 .Ls_%=\n" \
  "v_xor_b32 %[t0], 0x10000, %[pb]\n" \
  "ds_read_b64_tr_b16 v[198:199], %[t0] offset:0\n" \
  "v_xor_b32 %[t1], 0x10080, %[pb]\n" \
  "ds_read_b64_tr_b16 v[206:207], %[t1] offset:0\n" \
  "v_xor_b32 %[t0], 0x11000, %[pb]\n" \
  "ds_read_b64_tr_b16 v[6:7], %[t0] offset:0\n" \
  "v_xor_b32 %[t1], 0x11080, %[pb]\n" \
  "ds_read_b64_tr_b16 v[54:55], %[t1] offset:0\n" \
  "v_xor_b32 %[t0], 0x12000, %[pb]\n" \
  "ds_read_b64_tr_b16 v[22:23], %[t0] offset:0\n" \
  "v_xor_b32 %[t1], 0x12080, %[pb]\n" \
  "ds_read_b64_tr_b16 v[70:71], %[t1] offset:0\n" \
  "v_xor_b32 %[t0], 0x13000, %[pb]\n" \
  "ds_read_b64_tr_b16 v[38:39], %[t0] offset:0\n" \
  "v_xor_b32 %[t1], 0x13080, %[pb]\n" \
  "ds_read_b64_tr_b16 v[86:87], %[t1] offset:0\n" \
  ".Ls_%=:\n" \
  "s_waitcnt lgkmcnt(0)\n" \
  "v_swap_b32 v145, v146\n" \
  "v_swap_b32 v149, v150\n" \
  "v_swap_b32 v153, v154\n" \
  "v_swap_b32 v157, v158\n"
#define PRO1_8 \
  "v_add_u32 %[t0], 0x18000, %[pa]\n" \
  "ds_read_b64_tr_b16 v[160:161], %[t0] offset:0\n" \
  "ds_read_b64_tr_b16 v[164:165], %[t0] offset:1024\n" \
  "ds_read_b64_tr_b16 v[168:169], %[t0] offset:8192\n" \
  "ds_read_b64_tr_b16 v[172:173], %[t0] offset:9216\n" \
  "s_cmp_lg_u32 %[hb], 0\n" \
  "s_cbranch_scc1 .Ls_%=\n" \
  "v_xor_b32 %[t0], 0x18000, %[pb]\n" \
  "ds_read_b64_tr_b16 v[208:209], %[t0] offset:0\n" \
  "v_xor_b32 %[t1], 0x18080, %[pb]\n" \
  "ds_read_b64_tr_b16 v[216:217], %[t1] offset:0\n" \
  "v_xor_b32 %[t0], 0x19000, %[pb]\n" \
  "ds_read_b64_tr_b16 v[8:9], %[t0] offset:0\n" \
  "v_xor_b32 %[t1], 0x19080, %[pb]\n" \
  "ds_read_b64_tr_b16 v[56:57], %[t1] offset:0\n" \
  "v_xor_b32 %[t0], 0x1a000, %[pb]\n" \
  "ds_read_b64_tr_b16 v[24:25], %[t0] offset:0\n" \
  "v_xor_b32 %[t1], 0x1a080, %[pb]\n" \
  "ds_read_b64_tr_b16 v[72:73], %[t1] offset:0\n" \
  "v_xor_b32 %[t0], 0x1b000, %[pb]\n" \
  "ds_read_b64_tr_b16 v[40:41], %[t0] offset:0\n" \
  "v_xor_b32 %[t1], 0x1b080, %[pb]\n" \
  "ds_read_b64_tr_b16 v[88:89], %[t1] offset:0\n" \
  ".Ls_%=:\n" \
  "s_waitcnt lgkmcnt(0)\n"
#define PRO1_9 \
  "v_add_u32 %[t0], 0x20000, %[pa]\n" \
  "ds_read_b64_tr_b16 v[162:163], %[t0] offset:0\n" \
  "ds_read_b64_tr_b16 v[166:167], %[t0] offset:1024\n" \
  "ds_read_b64_tr_b16 v[170:171], %[t0] offset:8192\n" \
  "ds_read_b64_tr_b16 v[174:175], %[t0] offset:9216\n" \
  "s_cmp_lg_u32 %[hb], 1\n" \
  "s_cbranch_scc1 .Ls_%=\n" \
  "v_xor_b32 %[t0], 0x20000, %[pb]\n" \
  "ds_read_b64_tr_b16 v[208:209], %[t0] offset:0\n" \
  "v_xor_b32 %[t1], 0x20080, %[pb]\n" \
  "ds_read_b64_tr_b16 v[216:217], %[t1] offset:0\n" \
  "v_xor_b32 %[t0], 0x21000, %[pb]\n" \
  "ds_read_b64_tr_b16 v[8:9], %[t0] offset:0\n" \
  "v_xor_b32 %[t1], 0x21080, %[pb]\n" \
  "ds_read_b64_tr_b16 v[56:57], %[t1] offset:0\n" \
  "v_xor_b32 %[t0], 0x22000, %[pb]\n" \
  "ds_read_b64_tr_b16 v[24:25], %[t0] offset:0\n" \
  "v_xor_b32 %[t1], 0x22080, %[pb]\n" \
  "ds_read_b64_tr_b16 v[72:73], %[t1] offset:0\n" \
  "v_xor_b32 %[t0], 0x23000, %[pb]\n" \
  "ds_read_b64_tr_b16 v[40:41], %[t0] offset:0\n" \
  "v_xor_b32 %[t1], 0x23080, %[pb]\n" \
  "ds_read_b64_tr_b16 v[88:89], %[t1] offset:0\n" \
  ".Ls_%=:\n" \
  "s_waitcnt lgkmcnt(0)\n" \
  "v_swap_b32 v161, v162\n" \
  "v_swap_b32 v165, v166\n" \
  "v_swap_b32 v169, v170\n" \
  "v_swap_b32 v173, v174\n"
#define PRO1_10 \
  "v_add_u32 %[t0], 0x0, %[pa]\n" \
  "ds_read_b64_tr_b16 v[176:177], %[t0] offset:0\n" \
  "ds_read_b64_tr_b16 v[180:181], %[t0] offset:1024\n" \
  "ds_read_b64_tr_b16 v[184:185], %[t0] offset:8192\n" \
  "ds_read_b64_tr_b16 v[188:189], %[t0] offset:9216\n" \
  "s_cmp_lg_u32 %[hb], 0\n" \
  "s_cbranch_scc1 .Ls_%=\n" \
  "v_xor_b32 %[t0], 0x0, %[pb]\n" \
  "ds_read_b64_tr_b16 v[210:211], %[t0] offset:0\n" \
  "v_xor_b32 %[t1], 0x80, %[pb]\n" \
  "ds_read_b64_tr_b16 v[218:219], %[t1] offset:0\n" \
  "v_xor_b32 %[t0], 0x1000, %[pb]\n" \
  "ds_read_b64_tr_b16 v[10:11], %[t0] offset:0\n" \
  "v_xor_b32 %[t1], 0x1080, %[pb]\n" \
  "ds_read_b64_tr_b16 v[58:59], %[t1] offset:0\n" \
  "v_xor_b32 %[t0], 0x2000, %[pb]\n" \
  "ds_read_b64_tr_b16 v[26:27], %[t0] offset:0\n" \
  "v_xor_b32 %[t1], 0x2080, %[pb]\n" \
  "ds_read_b64_tr_b16 v[74:75], %[t1] offset:0\n" \
  "v_xor_b32 %[t0], 0x3000, %[pb]\n" \
  "ds_read_b64_tr_b16 v[42:43], %[t0] offset:0\n" \
  "v_xor_b32 %[t1], 0x3080, %[pb]\n" \
  "ds_read_b64_tr_b16 v[90:91], %[t1] offset:0\n" \
  ".Ls_%=:\n" \
  "s_waitcnt lgkmcnt(0)\n"
#define PRO1_11 \
  "v_add_u32 %[t0], 0x8000, %[pa]\n" \
  "ds_read_b64_tr_b16 v[178:179], %[t0] offset:0\n" \
  "ds_read_b64_tr_b16 v[182:183], %[t0] offset:1024\n" \
  "ds_read_b64_tr_b16 v[186:187], %[t0] offset:8192\n" \
  "ds_read_b64_tr_b16 v[190:191], %[t0] offset:9216\n" \
  "s_cmp_lg_u32 %[hb], 1\n" \
  "s_cbranch_scc1 .Ls_%=\n" \
  "v_xor_b32 %[t0], 0x8000, %[pb]\n" \
  "ds_read_b64_tr_b16 v[210:211], %[t0] offset:0\n" \
  "v_xor_b32 %[t1], 0x8080, %[pb]\n" \
  "ds_read_b64_tr_b16 v[218:219], %[t1] offset:0\n" \
  "v_xor_b32 %[t0], 0x9000, %[pb]\n" \
  "ds_read_b64_tr_b16 v[10:11], %[t0] offset:0\n" \
  "v_xor_b32 %[t1], 0x9080, %[pb]\n" \
  "ds_read_b64_tr_b16 v[58:59], %[t1] offset:0\n" \
  "v_xor_b32 %[t0], 0xa000, %[pb]\n" \
  "ds_read_b64_tr_b16 v[26:27], %[t0] offset:0\n" \
  "v_xor_b32 %[t1], 0xa080, %[pb]\n" \
  "ds_read_b64_tr_b16 v[74:75], %[t1] offset:0\n" \
  "v_xor_b32 %[t0], 0xb000, %[pb]\n" \
  "ds_read_b64_tr_b16 v[42:43], %[t0] offset:0\n" \
  "v_xor_b32 %[t1], 0xb080, %[pb]\n" \
  "ds_read_b64_tr_b16 v[90:91], %[t1] offset:0\n" \
  ".Ls_%=:\n" \
  "s_waitcnt lgkmcnt(0)\n" \
  "v_swap_b32 v177, v178\n" \
  "v_swap_b32 v181, v182\n" \
  "v_swap_b32 v185, v186\n" \
  "v_swap_b32 v189, v190\n"
#define PRO1_12 \
  "v_add_u32 %[t0], 0x8000, %[b0]\n" \
  "ds_write_b128 %[t0], v[192:195]\n" \
  "ds_write_b128 %[t0], v[196:199] offset:1024\n" \
  "v_add_u32 %[t0], 0xc000, %[b0]\n" \
  "ds_write_b128 %[t0], v[200:203]\n" \
  "ds_write_b128 %[t0], v[204:207] offset:1024\n" \
  "v_add_u32 %[t0], 0x10000, %[pa]\n" \
  "ds_read_b64_tr_b16 v[224:225], %[t0] offset:0\n" \
  "ds_read_b64_tr_b16 v[228:229], %[t0] offset:1024\n" \
  "ds_read_b64_tr_b16 v[232:233], %[t0] offset:8192\n" \
  "ds_read_b64_tr_b16 v[236:237], %[t0] offset:9216\n" \
  "s_cmp_lg_u32 %[hb], 0\n" \
  "s_cbranch_scc1 .Ls_%=\n" \
  "v_xor_b32 %[t0], 0x10000, %[pb]\n" \
  "ds_read_b64_tr_b16 v[212:213], %[t0] offset:0\n" \
  "v_xor_b32 %[t1], 0x10080, %[pb]\n" \
  "ds_read_b64_tr_b16 v[220:221], %[t1] offset:0\n" \
  "v_xor_b32 %[t0], 0x11000, %[pb]\n" \
  "ds_read_b64_tr_b16 v[12:13], %[t0] offset:0\n" \
  "v_xor_b32 %[t1], 0x11080, %[pb]\n" \
  "ds_read_b64_tr_b16 v[60:61], %[t1] offset:0\n" \
  "v_xor_b32 %[t0], 0x12000, %[pb]\n" \
  "ds_read_b64_tr_b16 v[28:29], %[t0] offset:0\n" \
  "v_xor_b32 %[t1], 0x12080, %[pb]\n" \
  "ds_read_b64_tr_b16 v[76:77], %[t1] offset:0\n" \
  "v_xor_b32 %[t0], 0x13000, %[pb]\n" \
  "ds_read_b64_tr_b16 v[44:45], %[t0] offset:0\n" \
  "v_xor_b32 %[t1], 0x13080, %[pb]\n" \
  "ds_read_b64_tr_b16 v[92:93], %[t1] offset:0\n" \
  ".Ls_%=:\n" \
  "s_waitcnt lgkmcnt(0)\n"
#define PRO1_13 \
  "v_add_u32 %[t0], 0x18000, %[pa]\n" \
  "ds_read_b64_tr_b16 v[226:227], %[t0] offset:0\n" \
  "ds_read_b64_tr_b16 v[230:231], %[t0] offset:1024\n" \
  "ds_read_b64_tr_b16 v[234:235], %[t0] offset:8192\n" \
  "ds_read_b64_tr_b16 v[238:239], %[t0] offset:9216\n" \
  "s_cmp_lg_u32 %[hb], 1\n" \
  "s_cbranch_scc1 .Ls_%=\n" \
  "v_xor_b32 %[t0], 0x18000, %[pb]\n" \
  "ds_read_b64_tr_b16 v[212:213], %[t0] offset:0\n" \
  "v_xor_b32 %[t1], 0x18080, %[pb]\n" \
  "ds_read_b64_tr_b16 v[220:221], %[t1] offset:0\n" \
  "v_xor_b32 %[t0], 0x19000, %[pb]\n" \
  "ds_read_b64_tr_b16 v[12:13], %[t0] offset:0\n" \
  "v_xor_b32 %[t1], 0x19080, %[pb]\n" \
  "ds_read_b64_tr_b16 v[60:61], %[t1] offset:0\n" \
  "v_xor_b32 %[t0], 0x1a000, %[pb]\n" \
  "ds_read_b64_tr_b16 v[28:29], %[t0] offset:0\n" \
  "v_xor_b32 %[t1], 0x1a080, %[pb]\n" \
  "ds_read_b64_tr_b16 v[76:77], %[t1] offset:0\n" \
  "v_xor_b32 %[t0], 0x1b000, %[pb]\n" \
  "ds_read_b64_tr_b16 v[44:45], %[t0] offset:0\n" \
  "v_xor_b32 %[t1], 0x1b080, %[pb]\n" \
  "ds_read_b64_tr_b16 v[92:93], %[t1] offset:0\n" \
  ".Ls_%=:\n" \
  "s_waitcnt lgkmcnt(0)\n" \
  "v_swap_b32 v225, v226\n" \
  "v_swap_b32 v229, v230\n" \
  "v_swap_b32 v233, v234\n" \
  "v_swap_b32 v237, v238\n" \
  "v_add_u32 %[t0], 0x10000, %[b0]\n" \
  "ds_write_b128 %[t0], v[224:227]\n" \
  "ds_write_b128 %[t0], v[228:231] offset:1024\n" \
  "v_add_u32 %[t0], 0x14000, %[b0]\n" \
  "ds_write_b128 %[t0], v[232:235]\n" \
  "ds_write_b128 %[t0], v[236:239] offset:1024\n"
#define PRO1_14 \
  "v_add_u32 %[t0], 0x20000, %[pa]\n" \
  "ds_read_b64_tr_b16 v[224:225], %[t0] offset:0\n" \
  "ds_read_b64_tr_b16 v[228:229], %[t0] offset:1024\n" \
  "ds_read_b64_tr_b16 v[232:233], %[t0] offset:8192\n" \
  "ds_read_b64_tr_b16 v[236:237], %[t0] offset:9216\n" \
  "s_cmp_lg_u32 %[hb], 0\n" \
  "s_cbranch_scc1 .Ls_%=\n" \
  "v_xor_b32 %[t0], 0x20000, %[pb]\n" \
  "ds_read_b64_tr_b16 v[214:215], %[t0] offset:0\n" \
  "v_xor_b32 %[t1], 0x20080, %[pb]\n" \
  "ds_read_b64_tr_b16 v[222:223], %[t1] offset:0\n" \
  "v_xor_b32 %[t0], 0x21000, %[pb]\n" \
  "ds_read_b64_tr_b16 v[14:15], %[t0] offset:0\n" \
  "v_xor_b32 %[t1], 0x21080, %[pb]\n" \
  "ds_read_b64_tr_b16 v[62:63], %[t1] offset:0\n" \
  "v_xor_b32 %[t0], 0x22000, %[pb]\n" \
  "ds_read_b64_tr_b16 v[30:31], %[t0] offset:0\n" \
  "v_xor_b32 %[t1], 0x22080, %[pb]\n" \
  "ds_read_b64_tr_b16 v[78:79], %[t1] offset:0\n" \
  "v_xor_b32 %[t0], 0x23000, %[pb]\n" \
  "ds_read_b64_tr_b16 v[46:47], %[t0] offset:0\n" \
  "v_xor_b32 %[t1], 0x23080, %[pb]\n" \
  "ds_read_b64_tr_b16 v[94:95], %[t1] offset:0\n" \
  ".Ls_%=:\n" \
  "s_waitcnt lgkmcnt(0)\n"
#define PRO1_15 \
  "v_add_u32 %[t0], 0x0, %[pa]\n" \
  "ds_read_b64_tr_b16 v[226:227], %[t0] offset:0\n" \
  "ds_read_b64_tr_b16 v[230:231], %[t0] offset:1024\n" \
  "ds_read_b64_tr_b16 v[234:235], %[t0] offset:8192\n" \
  "ds_read_b64_tr_b16 v[238:239], %[t0] offset:9216\n" \
  "s_cmp_lg_u32 %[hb], 1\n" \
  "s_cbranch_scc1 .Ls_%=\n" \
  "v_xor_b32 %[t0], 0x0, %[pb]\n" \
  "ds_read_b64_tr_b16 v[214:215], %[t0] offset:0\n" \
  "v_xor_b32 %[t1], 0x80, %[pb]\n" \
  "ds_read_b64_tr_b16 v[222:223], %[t1] offset:0\n" \
  "v_xor_b32 %[t0], 0x1000, %[pb]\n" \
  "ds_read_b64_tr_b16 v[14:15], %[t0] offset:0\n" \
  "v_xor_b32 %[t1], 0x1080, %[pb]\n" \
  "ds_read_b64_tr_b16 v[62:63], %[t1] offset:0\n" \
  "v_xor_b32 %[t0], 0x2000, %[pb]\n" \
  "ds_read_b64_tr_b16 v[30:31], %[t0] offset:0\n" \
  "v_xor_b32 %[t1], 0x2080, %[pb]\n" \
  "ds_read_b64_tr_b16 v[78:79], %[t1] offset:0\n" \
  "v_xor_b32 %[t0], 0x3000, %[pb]\n" \
  "ds_read_b64_tr_b16 v[46:47], %[t0] offset:0\n" \
  "v_xor_b32 %[t1], 0x3080, %[pb]\n" \
  "ds_read_b64_tr_b16 v[94:95], %[t1] offset:0\n" \
  ".Ls_%=:\n" \
  "s_waitcnt lgkmcnt(0)\n" \
  "v_swap_b32 v225, v226\n" \
  "v_swap_b32 v229, v230\n" \
  "v_swap_b32 v233, v234\n" \
  "v_swap_b32 v237, v238\n" \
  "v_add_u32 %[t0], 0x18000, %[b0]\n" \
  "ds_write_b128 %[t0], v[224:227]\n" \
  "ds_write_b128 %[t0], v[228:231] offset:1024\n" \
  "v_add_u32 %[t0], 0x1c000, %[b0]\n" \
  "ds_write_b128 %[t0], v[232:235]\n" \
  "ds_write_b128 %[t0], v[236:239] offset:1024\n" \
  "v_add_u32 %[t0], 0x20000, %[b0]\n" \
  "ds_write_b128 %[t0], v[208:211]\n" \
  "ds_write_b128 %[t0], v[212:215] offset:1024\n" \
  "v_add_u32 %[t0], 0x24000, %[b0]\n" \
  "ds_write_b128 %[t0], v[216:219]\n" \
  "ds_write_b128 %[t0], v[220:223] offset:1024\n"
#define VO_CLOB "v192", "v193", "v194", "v195", "v196", "v197", "v198", "v199", "v200", "v201", "v202", "v203", "v204", "v205", "v206", "v207", "v208", "v209", "v210", "v211", "v212", "v213", "v214", "v215", "v216", "v217", "v218", "v219", "v220", "v221", "v222", "v223", "v224", "v225", "v226", "v227", "v228", "v229", "v230", "v231", "v232", "v233", "v234", "v235", "v240", "v241", "v242", "v243", "v244", "v245", "v246", "v247", "v250", "v251", "v252"
#define QK_CLOB "v204", "v205", "v206", "v207", "v208", "v209", "v210", "v211", "v212", "v213", "v214", "v215", "v216", "v217", "v218", "v219", "v220", "v221", "v222", "v223", "v224", "v225", "v226", "v227", "v228", "v229", "v230", "v231", "v236", "v237", "v238", "v239", "v240", "v241", "v242", "v243", "v244", "v245", "v246", "v247"
#define QL_CLOB "v192", "v193", "v194", "v195", "v196", "v197", "v198", "v199", "v200", "v201", "v202", "v203"
#define QH_CLOB "v232", "v233", "v234", "v235"
#define ST_CLOB "v192", "v193", "v194", "v195", "v196", "v197", "v198", "v199", "v200", "v201", "v202", "v203", "v204", "v205", "v206", "v207", "v240", "v241", "v242", "v243", "v244", "v245", "v246", "v247", "v253"
#define XB_CLOB "v192", "v193", "v194", "v195", "v196", "v197", "v198", "v199", "v200", "v201", "v202", "v203", "v204", "v205", "v206", "v207", "v208", "v209", "v210", "v211", "v212", "v213", "v214", "v215", "v216", "v217", "v218", "v219", "v220", "v221", "v222", "v223", "v224", "v225", "v226", "v227", "v228", "v229", "v230", "v231", "v232", "v233", "v234", "v235", "v236", "v237", "v238", "v239", "v240", "v241", "v242", "v243", "v244", "v245", "v246", "v247"
#define OF_CLOB "v32", "v33", "v34", "v35", "v36", "v37", "v38", "v39", "v40", "v41", "v42", "v43", "v44", "v45", "v46", "v47", "v48", "v49", "v50", "v51", "v52", "v53", "v54", "v55", "v56", "v57", "v58", "v59", "v60", "v61", "v62", "v63", "v64", "v65", "v66", "v67", "v68", "v69", "v70", "v71", "v72", "v73", "v74", "v75", "v76", "v77", "v78", "v79", "v80", "v81", "v82", "v83", "v84", "v85", "v86", "v87", "v88", "v89", "v90", "v91", "v92", "v93", "v94", "v95", "v96", "v97", "v98", "v99", "v100", "v101", "v102", "v103"
constexpr int L_W = 32768;
constexpr int L_HI = 16384;
constexpr float CSC = 0.08838834764831845f * 1.4426950408889634f;
#define TWY 3
#define MARG 150
#define ED0 700
#define EA0 1110
#define SDK 360
#define TOL 145
#define XLATE 130
#define TFR 600
#define QT 0
#define HSU 11
#define WNT 1
#define KROT 7
#define VLATE 0
#define VSL 2
#define CG1 18
#define CG2 6
#define KSL2 0
#define KSL 4
#define NCY 1600
#define NCYV NCY
#define NRY 22
#define NRX 30
#define PDU 2
#define PDY 0
#define PDC 0
#define NF1O 64
#define NPUB 2
#define RSL 2
#define PFL 4
#define EVT2R
template <int role>
__device__ __forceinline__ void wroles(const int h, const int par, const int sl, const int xg, const int w, const u64 nonce, const u16* __restrict__ x,
                                       const u16* __restrict__ Wq, const u16* __restrict__ Wk, const u16* __restrict__ Wv,
                                       const u16* __restrict__ Wo, char* ws) {
  LAS char* const L = (LAS char*)g_lds;
  gu32* tmo = (gu32*)(ws + OFF_TMO);
  const __amdgpu_buffer_rsrc_t rs = __builtin_amdgcn_make_buffer_rsrc(ws, 0, (int)WS_USED, 0x00020000);
  constexpr bool vo = role & 1;
  v8u wa[8], wb[16];
  v32u W0, W1, W2, W3, W4, W5;
  constexpr bool fuse = role >= 2;
  constexpr int RG = fuse ? 4 : 5, RB = fuse ? 32768 : 0;
  f32x4 fac[2][2];
  v2u nin = {0u, 0u};
  constexpr int TWV = 5;
  Clk ck = {0u, 0u, 92500u, 46432u};
  const int krot = role >= 2 ? (h & KROT) ^ (par * PX) : 0;
  {
    const int lane = olane();
    const int l15 = lane & 15, g = lane >> 4, trq = l15 >> 2, trp = lane & 3;
    const char* bl = (const char*)((vo ? Wv : Wq) + h * DH);
    const char* br = (const char*)(vo ? Wo + (size_t)h * DH * D : Wk + h * DH);
    const int rot = role == 1 ? (h >> 4) * 4 : 0;
    int vl[2], vr[2];
#pragma unroll
    for (int q = 0; q < 2; ++q) {
      const int row = 4 * (2 * w + q) + g;
      const int cb = ((((l15 >> 1) ^ ((row & 3) | ((row >> 1) & 4))) << 5) | ((l15 & 1) << 4));
      const int i = row & 7;
      const int prow = (row & 32) + ((i & 4) << 2) + ((row >> 1) & 12) + (i & 3);
      vl[q] = role < 2 ? (128 * (w >> 1) + 16 * (w & 1) + 8 * q + g) * 8192 + ((((l15 >> 1) ^ (4 * q + g)) << 5) | ((l15 & 1) << 4))
                       : row * 8192 + cb;
      vr[q] = vo ? (role == 1 ? row : prow) * 2048 + cb : vl[q];
    }
#define W_ISSUE(u)                                                                                   \
  {                                                                                                  \
    const size_t ro = (size_t)(role < 2 ? (8 * ((u) & 1) + ((u) >> 3)) * 64 + (((u) >> 2) & 1) * 32 + (((u) >> 1) & 1) * 4 : ((u) ^ krot) * 64) * 8192; \
    const char* pl = bl + ro;                                                                        \
    const char* pr = br + (role == 1 ? (size_t)(((u) >> 1) & 1) * 64 * 2048 + (2 * ((u) >> 2) + ((u) & 1)) * 256          \
                                     : (vo ? (size_t)((u) & 1) * 64 * 2048 + (((u) ^ (par * PX)) >> 1) * 256 : ro)); \
    _Pragma("unroll") for (int q = 0; q < 2; ++q) {                                                  \
      glds16<((WNT >> (role >> 1)) & 1) != 0>(pl, vl[q], RB + ((u) % RG) * 32768 + (2 * w + q) * 1024);                                \
      glds16<((WNT >> (role >> 1)) & 1) != 0>(pr, vr[q], RB + ((u) % RG) * 32768 + 16384 + (2 * w + q) * 1024);                        \
    }                                                                                                \
  }
    const int ta = role == 1 ? 8 + (w ^ rot) : 2 * w, tb = role < 2 ? (2 - role) * (w & 3) : 2 * w + 1;
    const int sw = trq | ((g & 1) << 2);
    int pa = (ta >> 3) * 16384 + (8 * g + trq) * 256 + (((ta & 7) ^ sw) << 5) + trp * 8;
    int pb = (role < 2 ? (w & 3) * 4096 + (4 * g + trq) * 256 : (tb >> 3) * 16384 + (8 * g + trq) * 256) + (((tb & 7) ^ sw) << 5) + trp * 8;
    v16u K0, K1, K2;
    if constexpr (role < 2)
      asm volatile("" : "={v[0:31]}"(W0), "={v[32:63]}"(W1), "={v[64:95]}"(W2), "={v[96:127]}"(W3), "={v[128:159]}"(W4), "={v[160:191]}"(W5),
                   "={v[192:207]}"(K0), "={v[208:223]}"(K1), "={v[224:239]}"(K2));
    const int b0 = lane * 16 + (w ^ 4) * 2048;
    const int pw = L_W + w * 2048 + lane * 16;
#define W_FRAG(dst, p, u)                                                                            \
  {                                                                                                  \
    int p_ = p + RB + ((u) % RG) * 32768;                                                             \
    asm volatile("" : "+v"(p_));                                                                     \
    const v2u t0 = trd(L + p_), t1 = trd(L + p_ + 1024), t2 = trd(L + p_ + 8192), t3 = trd(L + p_ + 9216); \
    dst = v8u{t0[0], t0[1], t1[0], t1[1], t2[0], t2[1], t3[0], t3[1]};                               \
  }
#define W_PUT(c)                                           \
  {                                                        \
    int p_ = pw + ((c) - 8) * 16384;                       \
    asm volatile("" : "+v"(p_));                           \
    *(LAS v4u*)(L + p_) = lo4(tmp[(c) - 8]);               \
    *(LAS v4u*)(L + p_ + 1024) = hi4(tmp[(c) - 8]);        \
  }
#define PT(i)
    PT(0);
    const int fxv = ((w & 1) + 2 * (lane >> 2)) * 2048 + (((lane ^ -g) & 3) << 4) + ((w >> 1) & 1) * 64;
    const int fxr = l15 * 64 + (((g ^ -trq) & 3) << 4);
#define X_ISSUE(j) glds16((const char*)x + par * 65536 + ((2 * (j) + (w >> 2)) ^ krot) * 128, fxv, ((j) & 3) * 8192 + w * 1024)
    W_ISSUE(0);
    if (fuse) {
#pragma unroll
      for (int j = 0; j < 4; ++j) X_ISSUE(j);
    }
#pragma unroll
    for (int u = 1; u < RG; ++u) {
      if (u == PDU && role < 3)
        for (int k = role == 2 ? TN(6, PDY) : TN(5, PDC); k > 0; --k) __builtin_amdgcn_s_sleep(8);
      W_ISSUE(u);
    }
    PT(1);
    v8u tmp[8];
#pragma unroll
    for (int u = 0; u < 16; ++u) {
      {
        int young = 4 * (u + RG - 1 < 16 ? RG - 1 : 15 - u);
        if (fuse)
          for (int i = u - RG; i < u; ++i) young += i >= 0 && i < 8 && (i & 1);
        asm volatile("s_waitcnt vmcnt(%0)" ::"n"(young) : "memory");
      }
      RAW_BARRIER();
      if ((u & 3) == 0 || u == 15) PT(2 + (u + 1) / 4);
      if constexpr (role < 2) {
        unsigned t0, t1;
#define PRO_OPS                                                                                                                    \
  : "+{v[0:31]}"(W0), "+{v[32:63]}"(W1), "+{v[64:95]}"(W2), "+{v[96:127]}"(W3), "+{v[128:159]}"(W4), "+{v[160:191]}"(W5),          \
    "+{v[192:207]}"(K0), "+{v[208:223]}"(K1), "+{v[224:239]}"(K2), [t0] "=&v"(t0), [t1] "=&v"(t1)                                  \
  : [b0] "v"(b0), [pa] "v"(pa), [pb] "v"(pb), [hb] "s"(w >> 2)                                                                     \
  : "scc", "memory"
#define PRO_U(n)                                         \
  if (u == n) {                                          \
    if constexpr (role == 0) asm volatile(PRO0_##n PRO_OPS); \
    else asm volatile(PRO1_##n PRO_OPS);                 \
  }
        PRO_U(0) PRO_U(1) PRO_U(2) PRO_U(3) PRO_U(4) PRO_U(5) PRO_U(6) PRO_U(7)
        PRO_U(8) PRO_U(9) PRO_U(10) PRO_U(11) PRO_U(12) PRO_U(13) PRO_U(14) PRO_U(15)
      } else {
        if (u >= 13) { W_PUT(u >= 13 ? 2 * u - 18 : 8); W_PUT(u >= 13 ? 2 * u - 17 : 8); }
        v8u fa, fb;
        W_FRAG(fa, pa, u)
        W_FRAG(fb, pb, u)
        if (u < 8) {
          wa[u & 7] = fa;
          wb[u] = fb;
        } else {
          wb[u] = cat8(lo4(fa), lo4(fb));
          tmp[u & 7] = cat8(hi4(fa), hi4(fb));
        }
#pragma unroll
        for (int hf = 0; hf < 2; ++hf)
#pragma unroll
          for (int rt = 0; rt < 2; ++rt) {
            const v4u xq = *(const LAS v4u*)(L + fxr + (u & 7) * 4096 + hf * 2048 + rt * 1024);
            fac[0][rt] = mma(hf ? hi4(fa) : lo4(fa), xq, u + hf == 0 ? f32x4{0.f, 0.f, 0.f, 0.f} : fac[0][rt]);
            fac[1][rt] = mma(hf ? hi4(fb) : lo4(fb), xq, u + hf == 0 ? f32x4{0.f, 0.f, 0.f, 0.f} : fac[1][rt]);
          }
        asm volatile("" : "+v"(fac[0][0]), "+v"(fac[0][1]), "+v"(fac[1][0]), "+v"(fac[1][1]));
      }
      if (u == HSU && w == 0) hs_wait(rs, ws, nonce, CNT_HS);
      if (((VWN && role == 3) || (QKN && role == 2)) && u == HSU + 2 && w == 7) nin = numa_load(rs, tmo);
      if (role < 2 && u == HSU + 2 && w == 7) {
        arm_wait(rs, tmo);
        OPQ(off, OFF_PERM);
        nin[0] = __builtin_amdgcn_raw_buffer_load_b32(rs, off, 0, AUX_SC1);
      }
      if (role < 2 && u == 14 && w == TWV) clk_init(ck);
      if (u + RG < 16) {
        RAW_BARRIER();
        W_ISSUE(u + RG);
        if (fuse && u < 8 && (u & 1)) X_ISSUE(4 + (u >> 1));
      }
    }
    PT(7);
    RAW_BARRIER();
    PT(8);
    if (fuse) { W_PUT(14); W_PUT(15); }
  }
  const int lane = olane();
  const int l15 = lane & 15, g = lane >> 4, trq = l15 >> 2, trp = lane & 3;
  int pw0 = L_W + w * 2048 + lane * 16, pw1 = pw0 + 65536;
  asm volatile("" : "+v"(pw0), "+v"(pw1));
#define WA_P(c) (L + ((c) < 12 ? pw0 : pw1) + ((c) & 3) * 16384)
#define WA_LDS(c) cat8(*(const LAS v4u*)WA_P(c), *(const LAS v4u*)(WA_P(c) + 1024))
  const int r_a = (int)(((uintptr_t)ws >> 19) & 7);
  const int r_s = r_a + (int)(OFF_STEP / UNIT);

  typedef int v4i __attribute__((ext_vector_type(4)));
#define WPIN "{v[0:31]}"(W0), "{v[32:63]}"(W1), "{v[64:95]}"(W2), "{v[96:127]}"(W3), "{v[128:159]}"(W4), "{v[160:191]}"(W5)
  const v4u rsv = {(unsigned)(uintptr_t)ws, (unsigned)((uintptr_t)ws >> 32) & 0xffffu, (unsigned)WS_USED, 0x00020000u};
  if constexpr (role >= 2) {
    constexpr bool isx = role == 3;
    W0 = cat32(wa[0], wa[1], wa[2], wa[3]), W1 = cat32(wa[4], wa[5], wa[6], wa[7]), W2 = cat32(wb[0], wb[1], wb[2], wb[3]);
    W3 = cat32(wb[4], wb[5], wb[6], wb[7]), W4 = cat32(wb[8], wb[9], wb[10], wb[11]), W5 = cat32(wb[12], wb[13], wb[14], wb[15]);
    const v4u rxv = {(unsigned)(uintptr_t)x, (unsigned)((uintptr_t)x >> 32) & 0xffffu, (unsigned)(S * D * 2), 0x00020000u};
    v4i adr;
    adr[3] = lane * 16;
    adr[2] = pw0;
    adr[1] = pw1;
    adr[0] = ((w & 1) + 2 * (lane >> 2)) * 2048 + (((lane ^ -g) & 3) << 4) + ((w >> 1) & 1) * 1024 + (isx ? 0 : (w >> 2) * 64);
    const int xr = l15 * 64 + (((g ^ -trq) & 3) << 4);
    constexpr int fo = isx ? F_VW : F_QK;
    const int nrel = isx ? TN(8, NRX) : TN(7, NRY);
    const int nit = par ? nrel : NSEG - nrel;
#define SEGOF(i) ((i) < nrel ? 2 * (i) + par : (i) + nrel)
#define ST_FLAG(sg)                                                                                                   \
  {                                                                                                                   \
    VMCNT(0);                                                                                                         \
    RAW_BARRIER();                                                                                                    \
    if (w == 0) __builtin_amdgcn_raw_buffer_store_b32(0u, rs, olane() << 28, fo + flag_of(sg, h), AUX_SC1);               \
  }
    unsigned vpm_ = 0;
    if ((VWN && isx) || (QKN && !isx)) {
      if (w == 7) *(volatile LAS unsigned*)L = numa_perm(nin, (LAS unsigned*)(L + 512));
      RAW_BARRIER();
      vpm_ = __builtin_amdgcn_readfirstlane(*(volatile LAS unsigned*)L) | (unsigned)((r_a + (int)(OFF_VWX / UNIT)) & 7) << 12;
      RAW_BARRIER();
    }
    const int qcl = cls_of(vpm_, h >> 3) ^ (QKN == 2 ? 2 : 0);
#define XB_OPS(sg)                                                                                                      \
  : [cnt] "=&s"(cnt) EVOUT                                                                                              \
  : WPIN, "{v[248:251]}"(adr), [rs] "s"(rsv), [so] "s"(vw_addr(vpm_, 2 * (w - 4), sg, h >> 2) + (h & 3) * 1024 + par * (PX / 2 * VRING * 262144)), \
    [so2] "s"(vw_addr(vpm_, 2 * (w - 4), sg, h >> 2) + (h & 3) * 1024 - par * (PX / 2 * VRING * 262144)),               \
    [js] "i"(VWN ? 32 * 524288 : VRING * 262144), [ss] "i"(VWN ? 65536 : 32768)                                          \
  : XB_CLOB, "scc", "memory"
    if (!isx || w < 4) {
#pragma unroll
      for (int rt = 0; rt < 2; ++rt) {
        const float ks = !isx && w >= 4 ? CSC : 1.f;
        const f32x4 f0 = fac[0][rt] * ks, f1 = fac[1][rt] * ks;
        const v4u o = {pk2(f0[0], f0[1]), pk2(f0[2], f0[3]), pk2(f1[0], f1[1]), pk2(f1[2], f1[3])};
        if (isx) *(LAS v4u*)(L + 24576 + rt * 4096 + w * 1024 + adr[3]) = o;
        else st128(v4u{o[0], o[2], o[1], o[3]}, rs, adr[3], qk_addr(w >> 2, par, h, qcl, r_a) + rt * QK_RT + (w & 3) * 1024);
      }
      constexpr int B0 = isx ? 6 : 8, PER = isx ? 2 : 4, SL = isx ? 12288 : 16384;
#pragma unroll
      for (int k = 0; k < 2; ++k)
#pragma unroll
        for (int j = 0; j < B0 / PER; ++j)
          glds16((const char*)x + SEGOF(1) * 65536 + (((k * B0 + j * PER) / 2 * 64) ^ (krot * 128)), adr[0], k * SL + j * PER * 2048 + w * 1024);
    }
    PT(9);
    if (!isx) VMCNT(4);
    RAW_BARRIER();
    PT(10);
    if (!isx && w == 0) __builtin_amdgcn_raw_buffer_store_b32(0u, rs, olane() << 28, fo + flag_of(par, h), AUX_SC1);
    bool last = false;
  again:
    if (isx && w >= 4) {
      EVDECL;
      unsigned cnt;
      asm volatile(ST_XB0 XB_OPS(last ? SEGOF(nit - 1) : par));
    }
    ST_FLAG(last ? SEGOF(nit - 1) : par)
    if (!last) PT(11);
    if (last) {
      if (isx && w == 0) __builtin_amdgcn_raw_buffer_store_b32(0u, rs, olane() << 28, fo + flag_of(SEGOF(nit - 2), h), AUX_SC1);
      return;
    }
    if (!isx || w < 4) {
      v32u xf;
#pragma unroll
      for (int q = 0; q < 8; ++q) {
        const v4u t = *(const LAS v4u*)(L + xr + q * 1024);
#pragma unroll
        for (int j = 0; j < 4; ++j) xf[4 * q + j] = t[j];
      }
#pragma unroll 1
      for (int it = 1; it < nit; ++it) {
        EVDECL;
        const int seg = SEGOF(it), ip = it - (isx ? 2 : 1);
        unsigned cnt, keep;
#define ST_OPS                                                                                                                 \
  : [cnt] "=&s"(cnt), [keep] "=&s"(keep), "+{v[208:239]}"(xf) EVOUT                                                            \
  : WPIN, "{v[248:251]}"(adr), "{v252}"(xr), [rs] "s"(rsv), [rx] "s"(rxv), [sc] "s"(seg * 65536 + krot * 128),                    \
    [sn] "s"(SEGOF(min(it + 1, nit - 1)) * 65536 + krot * 128),                                                                  \
    [mw] "s"(w * 1024), [so] "s"(isx ? w * 1024 : qk_addr(w >> 2, seg, h, qcl, r_a) + (w & 3) * 1024), [rt] "i"(QK_RT), \
    [fo] "s"(fo + flag_of(SEGOF(max(ip, 0)), h)), [fm] "s"(w == 0 && ip >= 0 ? 0 : 1 << 30), [tm] "s"(w == (isx ? 3 : TWY) ? 0 : 1 << 30),            \
    [tc] "s"(min(SEGOF(it + 2), NSEG - 1) * 65536 + sl * 8192), [cs] "s"((w >> 2) * __float_as_uint(CSC))                                                                   \
  : ST_CLOB, "scc", "memory"
        if constexpr (isx) asm volatile(ST_XA ST_OPS);
        else asm volatile(ST_Y ST_OPS);
        EVT2R
        if (isx) {
          if (it >= 2 && it <= NPUB) ST_FLAG(SEGOF(it - 1))
        } else if (it < NPUB) ST_FLAG(seg)
      }
    } else {
#pragma unroll
      for (int k = 0; k < 6; ++k) BARE_BARRIER();
#pragma unroll 1
      for (int it = 2; it < nit; ++it) {
        EVDECL;
        unsigned cnt;
        asm volatile(ST_XB XB_OPS(SEGOF(it - 1)));
        EVT2R
        if (it <= NPUB) ST_FLAG(SEGOF(it - 1))
      }
    }
    if (isx) RAW_BARRIER();
    last = true;
    goto again;
  }

  volatile LAS unsigned* sword = (volatile LAS unsigned*)(L + L_HI + 6400);
  if (w == 7) sword[0] = nin[0];
  RAW_BARRIER();
  const unsigned perm = __builtin_amdgcn_readfirstlane(sword[0]);
  PT(9);
  const int cown = cls_of(perm, xg);
  const int sidx = ((l15 & 2) ? 0xeeee : 0x4444) | ((l15 & 8) ? 0xeeee0000 : 0x44440000);
  int ntw = min(w ^ TWV, 1) << 30;
  asm volatile("" : "+s"(ntw));
  v2u bs = {(unsigned)(lane * 16 + (w ^ 4) * 2048), (unsigned)sidx};
  unsigned pl1 = bs[0] + 65536;
  asm volatile("" : "+v"(bs), "+v"(pl1));
  const int mg_ = NS2F(TN(0, MARG)), tol_ = NS2F(TN(13, TOL)), xl_ = NS2F(TN(14, XLATE));
  const int ncy_ = TN(1, NCY), vd_ = role ? NS2F(VLATE) : 0;
  Pace pc = {0u, NS2F(ED0), NS2F(EA0), 0};
  unsigned tg = 0, tw = 0;
  const int mo0 = R_MEM + (w >> 1) * 32768 + (w & 1) * 2048 - (w ^ 4) * 2048;
#define CH_IN WPIN, "{v[248:249]}"(bs), "{v253}"(pk), "{v254}"(pl1), [rs] "s"(rsv)
#define TLB_TOUCH                    \
  if (((r_s + s + 2) & 3) == 0)      \
    tch = __builtin_amdgcn_raw_buffer_load_b32(rs, 0, so + 2 * (int)UNIT + R_DUM + (role * 32 + h) * 128, AUX_SC1);
#define ST_ADR (lst * 32 + 32768 + 2048)
#define TIMING_WAVE(slp)                                                                             \
  {                                                                                                  \
    v2u st;                                                                                          \
    {                                                                                                \
      const int lst = olane();                                                                       \
      st = __builtin_amdgcn_raw_buffer_load_b64(rs, ST_ADR, so + R_ST + ((cown ^ xr) << 12), AUX_SC1); \
    }                                                                                                \
    SB();                                                                                            \
    if (slp) __builtin_amdgcn_s_sleep(slp);                             \
    clk_update(ck, s < 2 ? CG1 : CG2);                                                \
    if (role == 0) EV(7);                                                                            \
    SB();                                                                                            \
    if (__builtin_expect(anysent(st[0]), 0)) _Pragma("clang loop unroll(disable)") for (unsigned spins = 0;;) { \
      if (spins) __builtin_amdgcn_s_sleep(4);                                                        \
      if (spin_fail(spins, tmo, 0x30009)) break;                                                     \
      const int lst = olane();                                                                       \
      OPQ(va, ST_ADR - 2048);                                                                        \
      st = __builtin_amdgcn_raw_buffer_load_b64(rs, va, so + R_ST + ((cown ^ xr) << 12), AUX_SC1);   \
      if (!anysent(st[0])) break;                                                                       \
    }                                                                                                \
    unsigned md;                                                                                     \
    const int dm = stamp_max(ck, st, md);                                                            \
    const unsigned rf = (ck.r << 4) + dm;                                                            \
    pace_step(pc, s, rf, md, mg_, tol_, xl_);                                                     \
    tg = clk_cyc(ck, dm + pc.dq + vd_);                                                                    \
    tw = (unsigned)max(CMAX - (int)(tg - ck.m), 0);                                                  \
    EV(6);                                                                                           \
    EVDUMP(s);                                                                                       \
    EVCLK(s, ck);                                                                                    \
  }

  if constexpr (role == 0) {
    unsigned pk = w < 4 ? (g >> 1) * 512 + (g & 1) * 128 + trq * 32 + 8 * (trp ^ (2 * (g & 1)))
                        : (w & 2) * 1024 + (w & 1) * 512 + trp * 32 + (l15 >> 3) * 128 + (trq & 1) * 256 + 8 * (g ^ (trq & 2));
    asm volatile("" : "+v"(pk));
#define K_STAGE(sn)                                                                                                   \
  {                                                                                                                   \
    v4u kn[4];                                                                                                        \
    _Pragma("clang loop unroll(disable)") for (unsigned spins = 0; krdy <= (sn);) {    \
      OPQ(vfl, F_QK + flag_of(min((sn) + (olane() & 15), NSEG - 1), h));                                           \
      const u64 miss = __builtin_amdgcn_ballot_w64(__builtin_amdgcn_raw_buffer_load_b32(rs, vfl, 0, AUX_SC1) != 0u);  \
      krdy = (sn) + (miss ? __builtin_ctzll(miss) : 16);                                                              \
      if (krdy > (sn)) break;                                                                                         \
      __builtin_amdgcn_s_sleep(8);                                                                                    \
      if (spin_fail(spins, tmo, 0x30000)) break;                                                                      \
    }                                                                                                                 \
    const int lk = olane() * 16;                                                                                      \
    _Pragma("unroll") for (int q = 0; q < 4; ++q) kn[q] = __builtin_amdgcn_raw_buffer_load_b128(                      \
        rs, lk + q * 1024, qk_addr(1, sn, h, cown, r_a) + (w & 1) * QK_RT, AUX_SC1);                 \
    _Pragma("unroll") for (int q = 0; q < 4; ++q) *(LAS v4u*)(L + L_HI + 8192 + (w & 1) * 4096 + q * 1024 + lk) = kn[q]; \
  }
    int krdy = 0;
    const bool kst = w >= 4 && ((w ^ TWV) & 2);
    PT(10);
    if (kst) K_STAGE(0)
    RAW_BARRIER();
    PT(11);
    if (w < 2) {
      {
        const int lane = olane(), l15 = lane & 15, g = lane >> 4, d = ((l15 >> 1) & 2) | (l15 >> 3);
        *(LAS v4u*)(L + L_HI + 5120 + w * 2048 + lane * 16) =
            v4u{(unsigned)(R_EX + (h >> 3) * 32768 + (h & 7) * 512 + w * 256 + g * 16 + ((l15 >> 3) | (l15 & 2)) * 64 + (l15 & 4) * 2 + (l15 & 1) * 4),
                (unsigned)(R_PM + (cls_of(perm, l15 & 3) << 12) + (h >> 4) * 32768 + (h & 15) * 256 + w * 128 + g * 16 + d * 4),
                d == 2 * (h & 1) + w ? 0xffffu << (16 * (g & 1)) : 0u, 0u};
      }
#pragma unroll 1
      for (int s = 0; s < NSEG; ++s) {
        EVDECL;
        const int so = (int)OFF_STEP + s * (int)UNIT, xr = xreg(r_s + s);
#define CG(r) (so + ((cls_of(perm, (r) ^ (2 * w)) ^ xr) << 12))
        unsigned rt;
#define QA_OPS                                                                                                                    \
  : [rt] "=&s"(rt) EVOUT                                                                                                          \
  : CH_IN, [o0] "s"(so + mo0 + ((cown ^ xr) << 12)), [c0] "s"(CG(0)), [c1] "s"(CG(1)), [c2] "s"(CG(2)), [c3] "s"(CG(3)),          \
    [lo] "s"(0x3333333333333333ull), [so] "s"(so), [xr] "s"(xr << 12)                                                                                              \
  : QL_CLOB, QK_CLOB, QH_CLOB, "v250", "v251", "v252", "vcc", "scc", "memory"
        if (w == 0) asm volatile(QK_A0 QA_OPS);
        else asm volatile(QK_A1 QA_OPS);
        EVDUMP(s);
      }
    } else if (w < 4) {
#pragma unroll 1
      for (int s = 0; s < NSEG; ++s) {
        {
          EVDECL;
          const int so = (int)OFF_STEP + s * (int)UNIT, xr = xreg(r_s + s);
          unsigned rt;
          f32x4 sc[3];
          asm volatile(QK_X
                       : [rt] "=&s"(rt), "={v[192:195]}"(sc[0]), "={v[196:199]}"(sc[1]), "={v[200:203]}"(sc[2]) EVOUT
                       : CH_IN, [o0] "s"(so + mo0 + ((cown ^ xr) << 12)), [qo] "s"(qk_addr(0, s, h, cown, r_a) + (w - 2) * QK_RT)
                       : QK_CLOB, QH_CLOB, "v250", "v251", "v252", "vcc", "scc", "memory");
          const int ln = olane(), l15 = ln & 15, g = ln >> 4, l16 = ln * 16;
          EV(4);
          const int tok = 2 * l15 + w - 2 - 8 * g;
#pragma unroll
          for (int t = 0; t < 2; ++t)
#pragma unroll
            for (int j = 0; j < 4; ++j)
              if (2 * j + t > tok) sc[t][j] = -1000000000.0f * 1.4426950408889634f;
          float m0 = sc[2][0] + sc[2][2], m1 = sc[2][1] + sc[2][3];
          float mx;
          asm("v_max3_f32 %0, %1, %2, %3\n\tv_max3_f32 %0, %0, %4, %5\n\tv_max3_f32 %0, %0, %6, %7\n\tv_max3_f32 %0, %0, %8, %9\n\t"
              "v_max_f32 %0, %0, %10\n\ts_nop 1"
              : "=&v"(mx)
              : "v"(sc[0][0]), "v"(sc[0][1]), "v"(sc[0][2]), "v"(sc[0][3]), "v"(sc[1][0]), "v"(sc[1][1]), "v"(sc[1][2]), "v"(sc[1][3]),
                "v"(m0), "v"(m1));
          mx = xmax16_32(mx);
#pragma unroll
          for (int t = 0; t < 2; ++t)
#pragma unroll
            for (int j = 0; j < 4; ++j) sc[t][j] = __builtin_amdgcn_exp2f(sc[t][j] - mx);
          m0 = __builtin_amdgcn_exp2f(m0 - mx);
          m1 = __builtin_amdgcn_exp2f(m1 - mx);
          const f32x4 s4 = sc[0] + sc[1];
          const float inv = __builtin_amdgcn_rcpf(xsum16_32(((s4[0] + s4[1]) + (s4[2] + s4[3])) + (m0 + m1)));
          sc[0] *= inv;
          sc[1] *= inv;
          const v4u o = {pk2(sc[0][0], sc[1][0]), pk2(sc[0][1], sc[1][1]), pk2(sc[0][2], sc[1][2]), pk2(sc[0][3], sc[1][3])};
          const int pb = so + R_PX + (w - 2) * 40960;
          st128(o, rs, l16, pb + h * 1024);
          __builtin_amdgcn_raw_buffer_store_b32(pk2(m0 * inv, m1 * inv), rs, l15 * 16 + g * 4, pb + (32 + (h >> 2)) * 1024 + (h & 3) * 256, AUX_SC1);
          EV(6);
          EVDUMP(s);
        }
      }
    } else {
#pragma unroll 1
      for (int s = 0; s < NSEG; ++s) {
        EVDECL;
        const int so = (int)OFF_STEP + s * (int)UNIT, xr = xreg(r_s + s), sc_ = so + ((cown ^ xr) << 12);
        const int lst = olane();
        const bool fq = kst && krdy <= s + 2;
        int i0 = ((cls_of(perm, lst & 3) ^ xr) << 12) | ((lst & 4) << 9) | ((lst & 8) << 12) | ((lst >> 4) << 28) | ntw;
        int i2 = ((lst & 31) * 128 + (lst >> 5) * QK_RT) | (krdy > s + 1 ? 0 : 1 << 30);
        int i1 = (F_QK + flag_of(min(s + 1 + (lst & 15), NSEG - 1), h)) | (fq ? 0 : 1 << 30);
        v2u st;
        unsigned cnt, rt;
        int o0 = sc_ + mo0, sst = so + R_ST + role * 1024 + h * 32;
        int t1 = qk_addr(w & 1, min(s + 1, NSEG - 1), h, cown, r_a), t0 = 0;
        asm volatile("" : "+{v232}"(i0), "+{v233}"(i1), "+{v234}"(i2));
        asm volatile("" : "+s"(o0), "+s"(sst), "+s"(t0), "+s"(t1));
        asm volatile(QK_K
                     : [cnt] "=&s"(cnt), [rt] "=&s"(rt), "+{v232}"(i0), "+{v233}"(i1), "+{v234}"(i2), "={v[250:251]}"(st) EVOUT
                     : CH_IN, [o0] "s"(o0), [ckm] "s"(ck.m - ncy_), [inv] "s"(ck.inv << 16), [r16] "s"(ck.r << 4), [sst] "s"(sst), [tg] "s"(tg),
                       [tw] "s"(tw), [t0] "s"(t0), [t1] "s"(t1), [cs] "s"(CSC)
                     : QL_CLOB, QK_CLOB, "v235", "v252", "vcc", "scc", "memory");
        if (s < NSEG - 1 && w == TWV) {
          TIMING_WAVE(KSL)
        } else if (s < NSEG - 1 && kst) {
          if (fq) {
            const u64 miss = __builtin_amdgcn_ballot_w64(st[0] != 0u);
            krdy = s + 1 + (miss ? __builtin_ctzll(miss) : 16);
          }
          if (KSL2) __builtin_amdgcn_s_sleep(KSL2);
          EV(7);
          K_STAGE(s + 1)
        } else {
          __builtin_amdgcn_s_sleep(KSL);
          unsigned tch = 0;
          TLB_TOUCH
          asm volatile("" ::"v"(tch));
        }
        if (w != TWV) EV(6);
        if (w != TWV) EVDUMP(s);
      }
    }
  } else {
    int adr[4];
#pragma unroll
    for (int q = 0; q < 2; ++q) {
      const int c = 8 * (2 * q + (g >> 1) + ((g & 1) ? 0 : 4)) + (w ^ ((h >> 4) * 4));
      const int blk = (c % PO) * 2 + (h >> 4);
      adr[q] = (int)OFF_VM + blk * 32768 + ((cls_of(perm, c / PO) ^ xreg(r_a + (int)(OFF_VM / UNIT) + (blk >> 4))) << 12) +
                   (h & 15) * 256 + l15 * 16;
    }
    adr[2] = (16 * (g & 1) + 4 * (g >> 1) + trq) * 24 + (trp & 1) * 8;
    unsigned pk = L_HI + (16 * (w & 3) + l15) * 24 + 4 * g;
    asm volatile("" : "+v"(pk), "+v"(adr[0]), "+v"(adr[1]), "+v"(adr[2]));
    PT(10);
    PT(11);
    if (w < 4) {
#pragma unroll 1
      for (int s = 0; s < NSEG; ++s) {
        EVDECL;
        const int so = (int)OFF_STEP + s * (int)UNIT, xr = xreg(r_s + s);
        unsigned rt;
        asm volatile(VO_A
                     : [rt] "=&s"(rt) EVOUT
                     : CH_IN, "{v236}"(adr[0]), "{v237}"(adr[1]), "{v238}"(adr[2]), [o0] "s"(so + mo0 + ((cown ^ xr) << 12)), [vs] "s"(vm_slot(s))
                     : VO_CLOB, "vcc", "scc", "memory");
        if (w == 0) {
          unsigned tch = 0;
          TLB_TOUCH
          asm volatile("" ::"v"(tch));
        }
        EVDUMP(s);
      }
    } else {
#pragma unroll 1
      for (int s = 0; s < NSEG; ++s) {
        EVDECL;
        const int so = (int)OFF_STEP + s * (int)UNIT, xr = xreg(r_s + s);
        const int lst = olane();
        adr[3] = ((cls_of(perm, lst & 3) ^ xr) << 12) | ((lst & 4) << 9) | ((lst & 8) << 12) | ((lst >> 4) << 28) | ntw;
        unsigned cnt, rt;
        int o0 = so + mo0 + ((cown ^ xr) << 12), vs = vm_slot(s), sst = so + R_ST + role * 1024 + h * 32;
        asm volatile("" : "+{v239}"(adr[3]));
        asm volatile("" : "+s"(o0), "+s"(vs), "+s"(sst));
        asm volatile(VO_B
                     : [cnt] "=&s"(cnt), [rt] "=&s"(rt) EVOUT
                     : CH_IN, "{v236}"(adr[0]), "{v237}"(adr[1]), "{v238}"(adr[2]), "{v239}"(adr[3]), [o0] "s"(o0), [vs] "s"(vs),
                       [ckm] "s"(ck.m - ncy_), [inv] "s"(ck.inv << 16), [r16] "s"(ck.r << 4), [sst] "s"(sst), [tg] "s"(tg), [tw] "s"(tw)
                     : VO_CLOB, "vcc", "scc", "memory");
        if (w == TWV && s < NSEG - 1) TIMING_WAVE(VSL)
        if (w != TWV) EVDUMP(s);
      }
    }
  }
  __builtin_amdgcn_endpgm();
}
typedef f32x2 c32;
template <bool CJ>
__device__ __forceinline__ c32 cmul(c32 a, c32 b) {
  c32 d;
  if (CJ)
    asm("v_pk_mul_f32 %0, %1, %2 op_sel:[1,1] op_sel_hi:[1,0]\n\tv_pk_fma_f32 %0, %1, %2, %0 op_sel_hi:[0,1,1] neg_hi:[0,1,0]"
        : "=&v"(d) : "v"(a), "v"(b));
  else
    asm("v_pk_mul_f32 %0, %1, %2 op_sel:[1,1] op_sel_hi:[1,0] neg_lo:[0,1]\n\tv_pk_fma_f32 %0, %1, %2, %0 op_sel_hi:[0,1,1]"
        : "=&v"(d) : "v"(a), "v"(b));
  return d;
}
__device__ __forceinline__ c32 cmuls(c32 a, c32 b) {
  c32 d;
  asm("v_pk_mul_f32 %0, %1, %2 op_sel:[1,1] op_sel_hi:[1,0] neg_lo:[0,1]\n\tv_pk_fma_f32 %0, %1, %2, %0 op_sel_hi:[0,1,1]"
      : "=&v"(d) : "v"(a), "s"(b));
  return d;
}
template <int SG>
__device__ __forceinline__ c32 addi(c32 a, c32 b) {
  c32 d;
  if (SG > 0) asm("v_pk_add_f32 %0, %1, %2 op_sel:[0,1] op_sel_hi:[1,0] neg_lo:[0,1]" : "=v"(d) : "v"(a), "v"(b));
  else asm("v_pk_add_f32 %0, %1, %2 op_sel:[0,1] op_sel_hi:[1,0] neg_hi:[0,1]" : "=v"(d) : "v"(a), "v"(b));
  return d;
}
template <int SG, bool I2 = false>
__device__ __forceinline__ void dft4(c32& x0, c32& x1, c32& x2, c32& x3) {
  const c32 a = I2 ? addi<SG>(x0, x2) : x0 + x2, b = I2 ? addi<-SG>(x0, x2) : x0 - x2, c = x1 + x3, d = x1 - x3;
  x0 = a + c;
  x1 = addi<SG>(b, d);
  x2 = a - c;
  x3 = addi<-SG>(b, d);
}
template <int SG>
__device__ __forceinline__ c32 w16(int m) {
  constexpr float C[16] = {1.f, 0.92387953251128674f, 0.70710678118654752f, 0.38268343236508977f, 0.f, -0.38268343236508977f,
                           -0.70710678118654752f, -0.92387953251128674f, -1.f, -0.92387953251128674f, -0.70710678118654752f,
                           -0.38268343236508977f, 0.f, 0.38268343236508977f, 0.70710678118654752f, 0.92387953251128674f};
  return c32{C[m & 15], (float)SG * C[(m + 12) & 15]};
}
template <int SG>
__device__ __forceinline__ void dft16(c32 (&v)[16]) {
#pragma unroll
  for (int n2 = 0; n2 < 4; ++n2) dft4<SG>(v[n2], v[4 + n2], v[8 + n2], v[12 + n2]);
#pragma unroll
  for (int k1 = 1; k1 < 4; ++k1)
#pragma unroll
    for (int n2 = 1; n2 < 4; ++n2)
      if (n2 * k1 != 4) v[4 * k1 + n2] = cmuls(v[4 * k1 + n2], w16<SG>(n2 * k1));
#pragma unroll
  for (int k1 = 0; k1 < 4; ++k1)
    if (k1 == 2) dft4<SG, true>(v[8], v[9], v[10], v[11]);
    else dft4<SG>(v[4 * k1], v[4 * k1 + 1], v[4 * k1 + 2], v[4 * k1 + 3]);
  c32 t[16];
#pragma unroll
  for (int k1 = 0; k1 < 4; ++k1)
#pragma unroll
    for (int k2 = 0; k2 < 4; ++k2) t[k1 + 4 * k2] = v[4 * k1 + k2];
#pragma unroll
  for (int i = 0; i < 16; ++i) v[i] = t[i];
}
template <int SG>
__device__ __forceinline__ void dft8(c32 (&v)[8]) {
#pragma unroll
  for (int n2 = 0; n2 < 2; ++n2) dft4<SG>(v[n2], v[2 + n2], v[4 + n2], v[6 + n2]);
#pragma unroll
  for (int k1 = 1; k1 < 4; k1 += 2) v[2 * k1 + 1] = cmuls(v[2 * k1 + 1], w16<SG>(2 * k1));
  c32 t[8];
#pragma unroll
  for (int k1 = 0; k1 < 4; ++k1) {
    t[k1] = k1 == 2 ? addi<SG>(v[4], v[5]) : v[2 * k1] + v[2 * k1 + 1];
    t[k1 + 4] = k1 == 2 ? addi<-SG>(v[4], v[5]) : v[2 * k1] - v[2 * k1 + 1];
  }
#pragma unroll
  for (int i = 0; i < 8; ++i) v[i] = t[i];
}
__device__ __forceinline__ void lds_fence_wave() {
  __builtin_amdgcn_s_waitcnt(0xc07f);
  asm volatile("" ::: "memory");
}
template <int SG>
__device__ __forceinline__ void twiddle16(c32 (&u)[16], c32 w1, c32 w4) {
  const c32 w2 = cmul<false>(w1, w1), w3 = cmul<false>(w2, w1), w8 = cmul<false>(w4, w4), w12 = cmul<false>(w8, w4);
  const c32 lo[4] = {w1, w1, w2, w3}, hi[4] = {w4, w4, w8, w12};
#pragma unroll
  for (int k = 1; k < 16; ++k)
    u[k] = cmul<(SG > 0)>(u[k], (k & 3) ? ((k >> 2) ? cmul<false>(hi[k >> 2], lo[k & 3]) : lo[k & 3]) : hi[k >> 2]);
}
template <int N, int SG>
__device__ __forceinline__ void wave_fft(c32 (&v)[N / 64], c32* eb, const c32* tw, int lane) {
  constexpr int P = N / 64, B = P / 16, R3 = N / 256, M = N / 16, S1 = M + R3, S2 = 256 + 16 / R3, TS = 2048 / N;
  asm volatile("" : "+v"(lane));
#pragma unroll
  for (int b = 0; b < B; ++b) {
    c32 u[16];
#pragma unroll
    for (int n1 = 0; n1 < 16; ++n1) u[n1] = v[b + B * n1];
    dft16<SG>(u);
    const int n2 = lane + 64 * b;
    twiddle16<SG>(u, tw[n2 * TS], tw[n2 * TS * 4]);
#pragma unroll
    for (int k1 = 0; k1 < 16; ++k1) eb[k1 * S1 + n2] = u[k1];
    __builtin_amdgcn_sched_barrier(0);
  }
  lds_fence_wave();
  {
    c32 u[B][16];
#pragma unroll
    for (int b = 0; b < B; ++b) {
      const int q = lane + 64 * b, k1 = q / R3, m2 = q % R3;
#pragma unroll
      for (int m1 = 0; m1 < 16; ++m1) u[b][m1] = eb[k1 * S1 + m1 * R3 + m2];
    }
    const c32 w1 = tw[16 * (lane % R3) * TS], w4 = tw[64 * (lane % R3) * TS];
    lds_fence_wave();
#pragma unroll
    for (int b = 0; b < B; ++b) {
      const int q = lane + 64 * b, k1 = q / R3, m2 = q % R3;
      dft16<SG>(u[b]);
      twiddle16<SG>(u[b], w1, w4);
#pragma unroll
      for (int j1 = 0; j1 < 16; ++j1) eb[m2 * S2 + k1 + 16 * j1] = u[b][j1];
      __builtin_amdgcn_sched_barrier(0);
    }
  }
  lds_fence_wave();
#pragma unroll
  for (int c = 0; c < 4; ++c) {
    c32 u[R3];
#pragma unroll
    for (int m2 = 0; m2 < R3; ++m2) u[m2] = eb[m2 * S2 + lane + 64 * c];
    if constexpr (R3 == 4) dft4<SG>(u[0], u[1], u[2], u[3]);
    else dft8<SG>(u);
#pragma unroll
    for (int j2 = 0; j2 < R3; ++j2) v[c + 4 * j2] = u[j2];
  }
  lds_fence_wave();
}

constexpr int FFT_EB = 2176;
constexpr int LDS_TW = 0;
constexpr int LDS_EB = 16384;
constexpr int LDS_STG = 16384 + 8 * 1088 * 8;

__device__ __forceinline__ void fft_table(char* lds, int w) {
  c32* tw = (c32*)(lds + LDS_TW);
  {
    const int m = w * 64 + olane();
    float sn, cs;
    sincospif(-(float)m * (1.f / 1024.f), &sn, &cs);
    tw[m] = c32{cs, sn};
  }
}
#define XSK 12
__device__ __forceinline__ int x1_chunk(int kb, int rb) { return kb * 128 + ((rb + XSK * kb) & 127); }
#define F1NT true
__device__ __forceinline__ void fft_in1(const u16* x, const u16* wr, const u16* wi, int task, int w) {
  const int lane = olane(), r0 = task * 16 + 2 * w;
  const int eo = LDS_EB + w * 8704, fo = r0 * DF * 2;
#pragma unroll
  for (int q = 0; q < 4; ++q) glds16<F1NT>(x, r0 * D * 2 + q * 1024 + lane * 16, eo + q * 1024);
#pragma unroll
  for (int q = 0; q < 3; ++q)
    if (q < 2 || lane < 2) {
      const int o = min((fo & ~15) + q * 1024 + lane * 16, S * DF * 2 - 16);
      glds16<F1NT>(wr, o, eo + 4096 + q * 1024);
      glds16<F1NT>(wi, o, eo + 6176 + q * 1024);
    }
}
__device__ __forceinline__ void fft_pass1(char* ws, const u16* __restrict__ x, const u16* __restrict__ wr,
                                          const u16* __restrict__ wi, int task, int w, const u16* xb) {
  char* lds = g_lds;
  const __amdgpu_buffer_rsrc_t rx = __builtin_amdgcn_make_buffer_rsrc(ws + OFF_X1, 0, 65 * 131072, 0x00020000);
  const int lane = olane(), tid_ = w * 64 + lane;
  const c32* tw = (const c32*)(lds + LDS_TW);
  c32* eb = (c32*)(lds + LDS_EB) + w * 1088;
  c32(*stg)[16] = (c32(*)[16])(lds + LDS_STG);
  const int r0 = task * 16 + 2 * w;
#define F1T(i)
  F1T(0);
  const int fo = r0 * DF * 2;
  if (xb) {
    unsigned keep;
    asm volatile("s_mov_b32 %0, m0\n\ts_mov_b32 m0, %3\n\ts_nop 0\n\tglobal_load_lds_dword %1, %2\n\ts_mov_b32 m0, %0"
                 : "=&s"(keep) : "v"(task * 65536 + tid_ * 128), "s"(xb), "s"(160000 + w * 256) : "memory");
  } else {
    fft_in1(x, wr, wi, task, w);
    VMCNT(0);
  }
  F1T(1);
  c32 v[16];
  const u16* in = (const u16*)eb;
#pragma unroll
  for (int t = 0; t < 16; ++t) v[t] = c32{bf2f(in[lane + 64 * t]), bf2f(in[D + lane + 64 * t])};
  unsigned fw[2][9];
#pragma unroll
  for (int r = 0; r < 2; ++r)
#pragma unroll
    for (int t = 0; t < 9; ++t) {
      const int o = 2048 + ((fo & 15) >> 1) + r * DF + min(lane + 64 * t, 512);
      fw[r][t] = (unsigned)in[o] | ((unsigned)in[o + 1040] << 16);
    }
  lds_fence_wave();
  F1T(2);
  wave_fft<1024, -1>(v, eb, tw, lane);
  F1T(3);
#pragma unroll
  for (int t = 0; t < 16; ++t) eb[lane + 64 * t] = v[t];
  lds_fence_wave();
#pragma unroll
  for (int t = 0; t < 9; ++t) {
    const int k = lane + 64 * t;
    if (k <= 512) {
      c32 z = eb[k], zc = eb[(1024 - k) & 1023];
      *(f32x4*)&stg[k][2 * (w ^ (k & 7))] = f32x4{0.5f * (z.x + zc.x), 0.5f * (z.y - zc.y), 0.5f * (z.y + zc.y), -0.5f * (z.x - zc.x)};
    }
  }
  __syncthreads();
  F1T(4);
#pragma unroll
  for (int i = 0; i < 9; ++i) {
    const int idx = tid_ + 512 * i, k = min(idx >> 3, 512), p = idx & 7;
    v4u d = *(const v4u*)&stg[k][2 * (p ^ (k & 7))];
    st128(d, rx, idx < 513 * 8 ? lane * 16 : OOB, x1_chunk(min(w + 8 * i, 64), task) * 1024);
  }
  {
    const __amdgpu_buffer_rsrc_t rt = __builtin_amdgcn_make_buffer_rsrc(ws + OFF_WT, 0, 65 * 65536, 0x00020000);
    v2u* s2 = (v2u*)(lds + LDS_EB);
#pragma unroll
    for (int t = 0; t < 9; ++t) {
      const int k = lane + 64 * t;
      if (k <= 512) s2[k * 8 + (w ^ (k & 6))] = v2u{fw[0][t], fw[1][t]};
    }
    __syncthreads();
#pragma unroll
    for (int i = 0; i < 5; ++i) {
      const int idx = tid_ + 512 * i, k = min(idx >> 2, 512), p = idx & 3;
      const v4u d = *(const v4u*)&s2[k * 8 + ((2 * p) ^ (k & 6))];
      __builtin_amdgcn_raw_buffer_store_b128(d, rt, idx < 514 * 4 ? x1_chunk(idx >> 5, task) * 512 + (idx & 31) * 16 : OOB, 0, AUX_SC1);
    }
  }
}
__device__ __forceinline__ void fft_pass2(char* ws, int task, int w) {
  char* lds = g_lds;
  const __amdgpu_buffer_rsrc_t rx = __builtin_amdgcn_make_buffer_rsrc(ws + OFF_X1, 0, 65 * 131072, 0x00020000);
  const int lane = olane();
  const int k = task * 8 + w;
  const c32* tw = (const c32*)(lds + LDS_TW);
  c32* eb = (c32*)(lds + LDS_EB) + w * FFT_EB;
#define F2T(i)
  if (k > 512) return;
  F2T(0);
  c32 v[32];
  const int xoff = (lane >> 4) * 1024 + w * 128 + (lane & 15) * 8;
#pragma unroll
  for (int t = 0; t < 32; ++t) {
    v2u d = __builtin_amdgcn_raw_buffer_load_b64(rx, xoff, x1_chunk(task, 4 * t) * 1024, AUX_SC1);
    v[t] = c32{__uint_as_float(d[0]), __uint_as_float(d[1])};
  }
  unsigned wv[32];
  asm volatile("" : "+s"(task));
  {
    const __amdgpu_buffer_rsrc_t rt = __builtin_amdgcn_make_buffer_rsrc(ws + OFF_WT, 0, 65 * 65536, 0x00020000);
#pragma unroll
    for (int t = 0; t < 32; ++t)
      wv[t] = __builtin_amdgcn_raw_buffer_load_b32(rt, xoff >> 1, x1_chunk(task, 4 * t) * 512, AUX_SC1);
  }
  F2T(1);
  wave_fft<2048, -1>(v, eb, tw, lane);
  F2T(2);
#pragma unroll
  for (int t = 0; t < 32; ++t) v[t] = cmul<false>(v[t], c32{__uint_as_float(wv[t] << 16), __uint_as_float(wv[t] & 0xffff0000u)});
  F2T(3);
  wave_fft<2048, 1>(v, eb, tw, lane);
  F2T(4);
  asm volatile("" : "+s"(task));
#pragma unroll
  for (int t = 0; t < 32; ++t) {
    v2u d = {__float_as_uint(v[t].x), __float_as_uint(v[t].y)};
    __builtin_amdgcn_raw_buffer_store_b64(d, rx, xoff, x1_chunk(task, 4 * t) * 1024, AUX_SC1);
  }
  F2T(5);
  VMCNT(0);
  F2T(6);
}
#define OAUX3 0
#define OAUXX 0
__device__ __forceinline__ void fft_pass3(const char* ws, u16* __restrict__ out, int task, int w) {
  char* lds = g_lds;
  const __amdgpu_buffer_rsrc_t rx = __builtin_amdgcn_make_buffer_rsrc((void*)(ws + OFF_X1), 0, 65 * 131072, 0x00020000);
  const int lane = olane(), tid_ = w * 64 + lane;
  const c32* tw = (const c32*)(lds + LDS_TW);
  c32* eb = (c32*)(lds + LDS_EB) + w * 1088;
  c32(*stg)[16] = (c32(*)[16])(lds + LDS_STG);
  {
    v4u d[9];
#pragma unroll
    for (int i = 0; i < 9; ++i) {
      d[i] = __builtin_amdgcn_raw_buffer_load_b128(rx, (i < 8 ? lane : lane & 7) * 16, x1_chunk(min(w + 8 * i, 64), task) * 1024, AUX_SC1);
    }
#pragma unroll
    for (int i = 0; i < 9; ++i) {
      const int idx = tid_ + 512 * i, k = min(idx >> 3, 512), p = idx & 7;
      *(v4u*)&stg[k][2 * (p ^ (k & 7))] = d[i];
    }
  }
  __syncthreads();
  c32 v[16];
#pragma unroll
  for (int t = 0; t < 16; ++t) {
    const int n = lane + 64 * t;
    const int k = n <= 512 ? n : 1024 - n;
    const f32x4 xx = *(const f32x4*)&stg[k][2 * (w ^ (k & 7))];
    c32 xa = {xx[0], xx[1]}, xb = {xx[2], xx[3]};
    if (k == 0 || k == 512) {
      xa.y = 0.f;
      xb.y = 0.f;
    }
    v[t] = n <= 512 ? c32{xa.x - xb.y, xa.y + xb.x} : c32{xa.x + xb.y, -xa.y + xb.x};
  }
  wave_fft<1024, 1>(v, eb, tw, lane);
  const float sc = 1.f / (2048.f * 1024.f);
  const int r0 = task * 16 + 2 * w;
  const __amdgpu_buffer_rsrc_t ro = __builtin_amdgcn_make_buffer_rsrc(out, 0, S * 4096, 0x00020000);
#pragma unroll
  for (int t = 0; t < 16; ++t) {
    __builtin_amdgcn_raw_buffer_store_b16(f2bf(v[t].x * sc), ro, lane * 2 + t * 128, r0 * 4096, OAUX3);
    __builtin_amdgcn_raw_buffer_store_b16(f2bf(v[t].y * sc), ro, lane * 2 + t * 128, r0 * 4096 + 4096, OAUX3);
  }
}

#define OCK 800
#define OTRY 3
#define APR 0
#define OGN 5
#define OK1 150
#define NX2 0
#define WTO 2
#define WTS 30
#define WTN 2
#define RSO 5
#define NCAL 6
#define DOX2 300
#define DTC 100
#define NX1 4
#define NXG 2
#define NXK 5
#define OFD 200
#define STI 600
#define TNX 0
#define NXP 0
#define PACE 150
#define PFB 4
#define CV1 -20
#define CV2 125
#define EDL 0
constexpr int L_D = 16384, L_R = 32768, L_F = 122880;
__device__ __forceinline__ volatile LAS unsigned* flw(int i, int m) {
  return (volatile LAS unsigned*)((LAS char*)g_lds + L_F + i * 4 + (olane() & m) * 4);
}
template <int SLP = 0>
__device__ __forceinline__ void lds_wait_ge(int i, int m, unsigned v, gu32* tmo, unsigned code) {
  volatile LAS unsigned* p = flw(i, m);
  _Pragma("clang loop unroll(disable)") for (unsigned spins = 0;;) {
    if (__all(*p >= v)) break;
    if (SLP) __builtin_amdgcn_s_sleep(SLP);
    if (++spins > (kSpinCap << 5)) {
      tmo_report(tmo, code);
      break;
    }
  }
  asm volatile("" ::: "memory");
}
template <bool PRE>
__device__ __forceinline__ unsigned owner(const int c, const int w, const u64 nonce, const u16* __restrict__ mem0, char* ws,
                                          u16* __restrict__ out, unsigned perm, const char* wsl, const u16* ax, const u16* awr,
                                          const u16* awi) {
  LAS char* const L = (LAS char*)g_lds;
  gu32* tmo = (gu32*)(ws + OFF_TMO);
  const int lane = olane();
  const int l15 = lane & 15, g = lane >> 4;
  const __amdgpu_buffer_rsrc_t rs = __builtin_amdgcn_make_buffer_rsrc(ws, 0, (int)WS_USED, 0x00020000);
  const int xg = c / PO, li = c % PO;
  const int r_a = (int)(((uintptr_t)ws >> 19) & 7);
  const int r_s = r_a + (int)(OFF_STEP / UNIT);
  volatile LAS unsigned* fl = (volatile LAS unsigned*)(L + L_F);
  unsigned m0v = 0;
  if constexpr (PRE) {
  if (c == 0 && w == 6) hs_lead<4>(rs, true, CNT_HSO, 64u);
  if (WTO == 1 && wsl) {
    __builtin_amdgcn_s_sleep(WTS);
    for (int k = 0; k < WTN; ++k) w_touch(wsl, (li >> 1) * 128 + 64 + (li & 1) * 32 + (k & 1) * 16 - (k >> 1) * 64, 160000 + w * 256);
  }
  unsigned cal = 0;
  if (w == 0)
    st128(v4u{(unsigned)lane, 0u, 0u, 0u}, rs, (lane & 31) * 16 + (lane >> 5) * 32768, (int)OFF_SCR + (((li & 7) ^ xreg(r_a)) << 12) + (c >> 3) * 512);
  if (w == 0 && lane == 0) {
    const int cl = li & 7;
    int off = (int)OFF_SCR + ((cl ^ xreg(r_a)) << 12) + (c >> 3) * 512;
    unsigned best = 0xfffeu;
#pragma unroll 1
    for (int r = 0; r < NCAL; ++r) {
      VMCNT(0);
      const u64 t1 = __builtin_amdgcn_s_memtime();
      asm volatile("s_waitcnt lgkmcnt(0)" : "+v"(off)::"memory");
      const unsigned v = __builtin_amdgcn_raw_buffer_load_b32(rs, off, (r & 3) * 128 + (r >> 2) * 32768, AUX_SC1);
      asm volatile("s_waitcnt vmcnt(0)" ::"v"(v) : "memory");
      const u64 t2 = __builtin_amdgcn_s_memtime();
      best = min(best, (unsigned)(t2 - t1));
    }
    cal = ((unsigned)cl << 16) | best;
  }
  if (w == 0) {
    m0v = *(const unsigned*)(mem0 + (lane >> 3) * D + 16 * c + 2 * (lane & 7));
    hs_wait(rs, ws, nonce, CNT_HSO);
    unsigned id;
    asm volatile("s_getreg_b32 %0, hwreg(HW_REG_XCC_ID)" : "=s"(id));
    if (lane == 0) {
      __builtin_amdgcn_raw_buffer_store_b32(((id & 15u) << 19) | cal, rs, c * 4, (int)OFF_CAL, AUX_SC1);
      if (li == 0) __builtin_amdgcn_raw_buffer_store_b32(id & 15u, rs, xg * 4, (int)OFF_XID, AUX_SC1);
    }
  }
  __syncthreads();
  if (WTO == 2 && wsl)
    for (int k = 0; k < WTN; ++k) w_touch(wsl, (li >> 1) * 128 + 64 + (li & 1) * 32 + (k & 1) * 16 - (k >> 1) * 64, 160000 + w * 256);
  if (w == 7) {
    const unsigned pm = numa_perm(numa_load(rs, tmo), (LAS unsigned*)(L + L_F + 512));
    fl[10] = pm;
  }
  __syncthreads();
  perm = __builtin_amdgcn_readfirstlane(fl[10]);
  }
  const int xr0 = xreg(r_s + (PRE ? w : NARM + (li & 1)));
  const v4u sent = {0xffffffffu, 0xffffffffu, 0xffffffffu, 0xffffffffu};
  const int ibs = (int)OFF_VM + li * 65536 + ((cls_of(perm, xg) ^ xreg(r_a + (int)(OFF_VM / UNIT) + (li >> 3))) << 12);

  int am = (int)OFF_STEP + R_MEM + (c >> 4) * 32768 + ((cls_of(perm, g) ^ xr0) << 12) + (c & 15) * 256 + l15 * 16;
  int ap[2];
#pragma unroll
  for (int q = 0; q < 2; ++q) {
    const int o = c * 384 + (lane & 31) * 16;
    ap[q] = (lane & 31) < 24 ? (int)OFF_STEP + R_EX + (o >> 12) * 32768 + ((cls_of(perm, 2 * q + (lane >> 5)) ^ xr0) << 12) + (o & 4095)
                             : OOB;
  }
  int as = lane < 8 * NXC ? (int)OFF_STEP + R_ST + ((cls_of(perm, lane >> 3) ^ xr0) << 12) + (c >> 5) * 32768 + (c & 31) * 128 + (lane & 7) * 16
                          : OOB;
  const int ax1 = (int)OFF_STEP + R_PX + c * 1280 + lane * 16;
  const int ax2 = lane < 16 ? ax1 + 1024 : (lane < 24 ? (int)OFF_STEP + R_DUM + c * 128 + (lane - 16) * 16 : OOB);
#define ARM_REC(r)                                                                                                   \
  {                                                                                                                  \
    const int so_ = (r) * (int)UNIT, dx_ = (xreg(r_s + (r)) ^ xreg(r_s + (r) + 1)) << 12;                            \
    __builtin_amdgcn_raw_buffer_store_b128(sent, rs, am, so_, AUX_SC1);                                              \
    am ^= dx_;                                                                                                       \
    _Pragma("unroll") for (int q = 0; q < 2; ++q) {                                                                  \
      __builtin_amdgcn_raw_buffer_store_b128(sent, rs, ap[q], so_, AUX_SC1);                                         \
      ap[q] ^= dx_;                                                                                                  \
    }                                                                                                                \
    __builtin_amdgcn_raw_buffer_store_b128(sent, rs, as, so_, AUX_SC1);                                              \
    as ^= dx_;                                                                                                       \
    __builtin_amdgcn_raw_buffer_store_b128(sent, rs, ax1, so_, AUX_SC1);                                              \
    __builtin_amdgcn_raw_buffer_store_b128(sent, rs, ax2, so_, AUX_SC1);                                             \
  }
  if constexpr (PRE) {
    if (WTO == 3 && wsl)
      for (int k = 0; k < WTN; ++k) w_touch(wsl, (li >> 1) * 128 + 64 + (li & 1) * 32 + (k & 1) * 16 - (k >> 1) * 64, 160000 + w * 256);
    fft_in1(ax, awr, awi, c, w);
    if (w < NARM + (li & 1)) {
      if (w == 0) {
        const int row = lane >> 3, np = lane & 7;
#pragma unroll
        for (int r = 0; r < NXC; ++r)
          __builtin_amdgcn_raw_buffer_store_b32(min(m0v, 0xfffffffeu), rs,
                                                (int)OFF_STEP + R_MEM + (c >> 4) * 32768 + ((cls_of(perm, r) ^ xr0) << 12) + (c & 15) * 256 +
                                                    ((row & 1) + 2 * (np & 1) + 4 * (row >> 1)) * 16 + (np >> 1) * 4,
                                                0, AUX_SC1);
        am = OOB;
      }
      ARM_REC(w)
    }
#pragma unroll
    for (int k = 0; k < 4; ++k)
      __builtin_amdgcn_raw_buffer_store_b128(sent, rs, lane * 16 + k * 1024, ibs + vm_slot(w >> 1) + (w & 1) * 32768, AUX_SC1);
    if (c == 0 && w == 7) __builtin_amdgcn_raw_buffer_store_b32(perm, rs, lane << 28, OFF_PERM, AUX_SC1);
  }
  if constexpr (PRE) return perm;
  __syncthreads();
  if (w == 7) {
    if (lane < 32) fl[lane] = 0;
    arm_wait(rs, tmo);
  }
  __syncthreads();

  const int team = li & 1, c0 = c & ~1;
  const int l16 = lane * 16;
  const int cown = cls_of(perm, xg);
  if (w < 2) {
    const int eg = (c0 >> 2) & 1;
    typedef int v8i __attribute__((ext_vector_type(8)));
    v8i adr;
    adr[0] = l16;
    adr[1] = (lane >> 2) * 128 + (lane & 3) * 16;
    adr[2] = L_D + w * 8192 + eg * 4096 + l16;
    adr[3] = ((g & 1) + (l15 & 2) + 4 * ((g & 2) + (l15 & 1))) * 16 + (l15 >> 2) * 4;
    adr[4] = (l15 & 1) ? 0x03020706 : 0x05040100;
    adr[5] = ((l15 & 8) ? 0xeeee : 0x4444) | ((l15 & 2) ? 0xeeee0000 : 0x44440000);
    adr[6] = L_F + 48;
    adr[7] = adr[2] ^ 4096;
    const v4u rsv = {(unsigned)(uintptr_t)ws, (unsigned)((uintptr_t)ws >> 32) & 0xffffu, (unsigned)WS_USED, 0x00020000u};
    const int dtc_ = TN(12, DTC);
#pragma unroll 1
    for (int s = team; s < NSEG; s += 2) {
      EVDECL;
      const int so = (int)OFF_STEP + s * (int)UNIT;
      const int sp = so + R_PM + ((cown ^ xreg(r_s + s)) << 12);
      int sv = ibs + (w - team) * 65536 + vm_slot(s);
      int sv1 = sv + eg * 32768, sv2 = sv + (1 - eg) * 32768, sp1 = sp + eg * 32768, sp2 = sp + (1 - eg) * 32768;
      const int xn = xreg(r_s + s + 1);
      int sm[NXC];
#pragma unroll
      for (int q = 0; q < NXC; ++q) {
        sm[q] = so + (int)UNIT + R_MEM + (c0 >> 4) * 32768 + ((cls_of(perm, q) ^ xn) << 12) + ((c0 + w) & 15) * 256;
        asm volatile("" : "+s"(sm[q]));
      }
      asm volatile("" : "+s"(sv1), "+s"(sv2), "+s"(sp1), "+s"(sp2));
      if (s >= 2) lds_wait_ge<4>(20, 3, s - 1, tmo, 0x30017);
      v4u bl[8];
      u64 tp64 = 0;
      if (s < NSEG - 1) {
        v32u blv;
        unsigned cnt, bad;
        asm volatile(OW_F
                     : [cnt] "=&s"(cnt), [st] "=&s"(bad), [tp] "=&s"(tp64), "={v[0:31]}"(blv) EVOUT
                     : "{v[112:119]}"(adr), [rs] "s"(rsv), [sv1] "s"(sv1), [sv2] "s"(sv2), [sp1] "s"(sp1), [sp2] "s"(sp2),
                       [sm0] "s"(sm[0]), [sm1] "s"(sm[1]), [sm2] "s"(sm[2]), [sm3] "s"(sm[3])
                     : OF_CLOB, "vcc", "scc", "memory");
#pragma unroll
        for (int k = 0; k < 8; ++k) bl[k] = v4u{blv[4 * k], blv[4 * k + 1], blv[4 * k + 2], blv[4 * k + 3]};
        if (__builtin_expect(bad != 0u, 0)) {
          v4u pv[8];
          _Pragma("clang loop unroll(disable)") for (unsigned spins = 0;;) {
            OPQ(va, l16);
            OPQ(v2, adr[1]);
#pragma unroll
            for (int k = 0; k < 8; ++k) bl[k] = __builtin_amdgcn_raw_buffer_load_b128(rs, va + (k & 3) * 1024, k < 4 ? sv1 : sv2, AUX_SC1);
#pragma unroll
            for (int k = 0; k < 8; ++k)
              pv[k] = __builtin_amdgcn_raw_buffer_load_b128(rs, v2 + (k & 1) * 2048 + (k >> 2) * 64, (k & 2) ? sp2 : sp1, AUX_SC1);
            unsigned m = 0;
#pragma unroll
            for (int k = 0; k < 8; ++k) m = mx4(mx4(m, pv[k]), bl[k]);
            if (!anysent(m)) break;
            __builtin_amdgcn_s_sleep(RSO);
            if (spin_fail(spins, tmo, 0x20000 + s)) break;
          }
          f32x4 a0 = {0.f, 0.f, 0.f, 0.f};
#pragma unroll
          for (int k = 0; k < 4; ++k) {
            v8u b = cat8(bl[2 * k], bl[2 * k + 1]);
#pragma unroll
            for (int j = 0; j < 8; j += 2) {
              const auto q = __builtin_amdgcn_permlane16_swap(b[j], b[j + 1], false, false);
              b[j] = q[0];
              b[j + 1] = q[1];
            }
            a0 = smf(pv[k], b, a0, adr[5]);
          }
#pragma unroll
          for (int k = 0; k < 4; ++k) {
            LAS char* const pd = L + adr[k < 2 ? 2 : 7] + (k & 1) * 2048;
            a0 = smf1(pv[4 + k], cat8(*(const LAS v4u*)pd, *(const LAS v4u*)(pd + 1024)), a0, adr[5]);
          }
          const auto q0 = __builtin_amdgcn_permlane32_swap(__float_as_uint(a0[0]), __float_as_uint(a0[2]), false, false);
          const auto q1 = __builtin_amdgcn_permlane32_swap(__float_as_uint(a0[1]), __float_as_uint(a0[3]), false, false);
          const unsigned own = pk2(__uint_as_float(q0[0]) + __uint_as_float(q0[1]), __uint_as_float(q1[0]) + __uint_as_float(q1[1]));
          const unsigned nb = (unsigned)__builtin_amdgcn_mov_dpp((int)own, 0xb1, 0xf, 0xf, true);
          const unsigned o = __builtin_amdgcn_perm(nb, own, (unsigned)adr[4]);
#pragma unroll
          for (int q = 0; q < NXC; ++q) __builtin_amdgcn_raw_buffer_store_b32(o, rs, adr[3], sm[q], AUX_SC1);
          tp64 = cyc32();
          EV(3);
        }
      } else {
        BARE_BARRIER();
        wait_cyc(*flw(12, 0));
        _Pragma("clang loop unroll(disable)") for (unsigned spins = 0;;) {
          OPQ(va, l16);
          unsigned m = 0;
#pragma unroll
          for (int k = 0; k < 8; ++k) bl[k] = __builtin_amdgcn_raw_buffer_load_b128(rs, va + (k & 3) * 1024, k < 4 ? sv1 : sv2, AUX_SC1);
#pragma unroll
          for (int k = 0; k < 8; ++k) m = mx4(m, bl[k]);
          if (!anysent(m)) break;
          __builtin_amdgcn_s_sleep(RSO);
          if (spin_fail(spins, tmo, 0x20000 + s)) break;
        }
      }
      const unsigned tp = (unsigned)tp64;
      *flw(2 + w, 0) = s + 1;
      if (s < NSEG - 1) {
        const v4u tv = *(volatile LAS v4u*)flw(12, 0);
        const int lr = olane();
        __builtin_amdgcn_raw_buffer_store_b32(min(((tp - tv[1]) * tv[2]) >> 16, 60000u), rs,
                                              ((cls_of(perm, lr & 3) ^ xn) << 12) | ((lr & 4) << 9) | ((lr & 8) << 12) | ((lr >> 4) << 28),
                                              so + (int)UNIT + R_ST + (c0 + w) * 32 + 4, AUX_SC1);
      }
      OPQ(vs, l16);
#pragma unroll
      for (int k = 0; k < 8; ++k) *(LAS v4u*)(L + w * 8192 + ((k ^ (4 * eg)) * 1024) + vs) = bl[k];
      asm volatile("s_waitcnt lgkmcnt(0)" ::: "memory");
      *flw(4 + w, 0) = s + 1;
      if (s < NSEG - 4) {
        lds_wait_ge(2, 1, s + 1, tmo, 0x3001a);
        v4u se;
        asm volatile("v_mov_b32 %0, -1" : "=v"(se[0]));
        se[1] = se[2] = se[3] = se[0];
#pragma unroll
        for (int k = 0; k < 8; ++k) __builtin_amdgcn_raw_buffer_store_b128(se, rs, vs + (k & 3) * 1024, sv + (k >> 2) * 32768, AUX_SC1);
        asm volatile("s_nop 1" ::"v"(se));
      }
      if (s < NSEG - 2) {
        wait_cyc(tp + dtc_);
        const int lt = olane();
        const unsigned tc = __builtin_amdgcn_raw_buffer_load_b32(rs, lt < 40 ? lt * 128 : OOB, so + R_PX + ((li & ~1) + w) * 5120, AUX_SC1);
        asm volatile("" ::"v"(tc));
      }
      EVDUMP(s);
    }
  } else if (w < 6) {
    const int n = w - 2;
    const unsigned vpm_ = perm | (unsigned)((r_a + (int)(OFF_VWX / UNIT)) & 7) << 12;
    const int dox_ = TN(10, DOX2), pace_ = TN(11, PACE);
    v4u raw[2][8];
#define RAW_LD(dst, sg)                                                                                       \
  _Pragma("unroll") for (int t = 0; t < 2; ++t) _Pragma("unroll") for (int k = 0; k < 8; ++k) dst[t][k] =     \
      __builtin_amdgcn_raw_buffer_load_b128(rs, l16 + (k & 3) * 1024, vw_addr(vpm_, c0 + t, sg, 2 * n + (k >> 2)), AUX_SC1);
    wait_flag<8>(rs, F_VW + flag_of(team, 8 * n + (lane & 7)), tmo, 0x30018);
    RAW_LD(raw, team)
    int nrdy = team;
#pragma unroll 1
    for (int s = team; s < NSEG; s += 2) {
      EVDECL;
      const int so = (int)OFF_STEP + s * (int)UNIT;
      if (s < NSEG - 1) {
        const int ln = olane();
        OPQ(eidx, (ln & 4) ? 0xeeee : 0x4444);
        OPQ(vea, (ln & 4) * 128 + (ln & 8) * 32 + (ln >> 4) * 64 + (ln & 3) * 16);
        int se = so + R_EX + n * 32768 + ((cown ^ xreg(r_s + s)) << 12);
        v8u bx[2][4];
#pragma unroll
        for (int t = 0; t < 2; ++t)
#pragma unroll
          for (int k = 0; k < 4; ++k) {
            const v4u u = raw[t][2 * k], v = raw[t][2 * k + 1];
            bx[t][k] = v8u{u[0], v[0], u[1], v[1], u[2], v[2], u[3], v[3]};
            asm volatile("" : "+v"(bx[t][k]));
          }
        OPQ(vd, L_D + n * 2048 + (ln & 31) * 16 + (ln >> 5) * 8);
        f32x4 d[2][4];
#pragma unroll
        for (int t = 0; t < 2; ++t)
#pragma unroll
          for (int k = 0; k < 4; ++k) d[t][k] = zero4();
        asm volatile("" : "+s"(se));
        EV(3);
        BARE_BARRIER();
        if (EDL) asm volatile("s_nop %0" ::"n"(EDL > 0 ? EDL - 1 : 0));
        EV(0);
#define E_PACK(t, k) v2u{pk2(d[t][k][0], d[t][k][1]), pk2(d[t][k][2], d[t][k][3])}
        _Pragma("clang loop unroll(disable)") for (unsigned spins = 0;;) {
          v4u pv[4];
          asm volatile("" : "+v"(vea));
#pragma unroll
          for (int k = 0; k < 4; ++k) pv[k] = __builtin_amdgcn_raw_buffer_load_b128(rs, vea + k * 1024, se, AUX_SC1);
          unsigned m = 0;
          v2u cv[2][4];
          SB();
#pragma unroll
          for (int k = 0; k < 4; ++k) {
            d[0][k] = smf(pv[k], bx[0][k], d[0][k], eidx);
            m = mx4(m, pv[k]);
            asm volatile("" : "+v"(m), "+v"(d[0][k]));
            d[1][k] = smf(pv[k], bx[1][k], d[1][k], eidx);
            asm volatile("" : "+v"(d[1][k]));
            if (k > 0) {
              const int j = k > 0 ? k - 1 : 0;
              asm volatile("" : "+v"(d[0][j]), "+v"(d[1][j]));
              cv[0][j] = E_PACK(0, j);
              cv[1][j] = E_PACK(1, j);
              *(LAS v2u*)(L + vd + j * 512) = cv[0][j];
              *(LAS v2u*)(L + vd + j * 512 + 8192) = cv[1][j];
            }
            SB();
          }
          EV(1);
          *(LAS v2u*)(L + vd + 1536) = E_PACK(0, 3);
          SB();
          *(LAS v2u*)(L + vd + 9728) = E_PACK(1, 3);
          if (__builtin_expect(!anysent(m), 1)) break;
          __builtin_amdgcn_s_sleep(RSO);
          if (spin_fail(spins, tmo, 0x10000 + s)) break;
#pragma unroll
          for (int t = 0; t < 2; ++t)
#pragma unroll
            for (int k = 0; k < 4; ++k) d[t][k] = zero4();
        }
        EV(2);
        RAW_BARRIER();
      } else BARE_BARRIER();
      v4u nx[2][8];
      const int sn = min(s + 2, NSEG - 1);
      unsigned tpb = 0;
      if (s < NSEG - 2) {
        __builtin_amdgcn_s_sleep(5);
        lds_wait_ge(2, 1, s + 1, tmo, 0x30014);
        tpb = cyc32();
        const unsigned tw = tpb + dox_;
        _Pragma("clang loop unroll(disable)") for (unsigned spins = 0; nrdy < s + 2;) {
          const int lf = olane();
          OPQ(vfl, F_VW + flag_of(min(s + 2 + 2 * (lf >> 3), NSEG - 2 + team), 8 * n + (lf & 7)));
          const u64 miss = __builtin_amdgcn_ballot_w64(__builtin_amdgcn_raw_buffer_load_b32(rs, vfl, 0, AUX_SC1) != 0u);
          nrdy = s + 2 * (miss ? __builtin_ctzll(miss) >> 3 : 8);
          if (nrdy >= s + 2) break;
          __builtin_amdgcn_s_sleep(8);
          if (spin_fail(spins, tmo, 0x30018)) break;
        }
        wait_cyc(tw);
      }
      EV(6);
      OPQ(va, s < NSEG - 2 ? l16 : OOB);
#define NX_LD(dst, q0, q1)                                                                              \
  _Pragma("unroll") for (int q = q0; q < q1; ++q) dst[q >> 3][q & 7] = __builtin_amdgcn_raw_buffer_load_b128( \
      rs, va + (q & 3) * 1024, vw_addr(vpm_, c0 + (q >> 3), sn, 2 * n + ((q >> 2) & 1)), AUX_SC1);
      NX_LD(nx, 0, NX1)
      SB();
      if (s < NSEG - 2) wait_cyc(cyc32() + ((n + (li >> 1)) & 3) * pace_);
      else lds_wait_ge(4, 1, s + 1, tmo, 0x30014);
      EV(4);
      f32x4 ac[2][2];
      const int sx = so + R_PX + n * 8192, sy = so + R_PX + 32768 + n * 2048;
      v4u pv[2][10], vm[2][2];
      OPQ(vb, l16);
#define P_LD                                                                                         \
  _Pragma("unroll") for (int k = 0; k < 10; ++k) _Pragma("unroll") for (int rt = 0; rt < 2; ++rt) pv[rt][k] = \
      __builtin_amdgcn_raw_buffer_load_b128(rs, vb + (k & 3) * 1024, (k < 8 ? sx + (k >> 2) * 4096 : sy) + rt * 40960, AUX_SC1);
#define P_MM(k0, k1)                                                                                 \
  _Pragma("unroll") for (int k = k0; k < k1; ++k) _Pragma("unroll") for (int rt = 0; rt < 2; ++rt) {  \
    _Pragma("unroll") for (int t = 0; t < 2; ++t)                                                    \
      ac[rt][t] = mma(k < 8 ? raw[t][k & 7] : vm[t][k & 1], pv[rt][k], k == 0 ? f32x4{0.f, 0.f, 0.f, 0.f} : ac[rt][t]); \
    asm volatile("" : "+v"(ac[rt][0]), "+v"(ac[rt][1]));                                             \
  }
      P_LD
      SB();
      NX_LD(nx, NX1, NX1 + NX2)
      SB();
      lds_wait_ge(4, 1, s + 1, tmo, 0x30014);
#pragma unroll
      for (int t = 0; t < 2; ++t)
#pragma unroll
        for (int k = 0; k < 2; ++k) vm[t][k] = *(const LAS v4u*)(L + vb + t * 8192 + n * 2048 + k * 1024);
      SB();
      P_MM(0, 10)
      if (__builtin_expect(__builtin_amdgcn_ballot_w64(__builtin_isunordered(ac[0][0][0], ac[1][0][0])) != 0ull, 0)) {
        _Pragma("clang loop unroll(disable)") for (unsigned spins = 0;;) {
          asm volatile("" : "+v"(vb));
          P_LD
          unsigned m = 0;
#pragma unroll
          for (int k = 0; k < 10; ++k) m = mx4(mx4(m, pv[0][k]), pv[1][k]);
          if (!anysent(m)) break;
          __builtin_amdgcn_s_sleep(8);
          if (spin_fail(spins, tmo, 0x30015)) break;
        }
        P_MM(0, 10)
      }
      EV(7);
      NX_LD(raw, NX1 + NX2, 16)
      SB();
      OPQ(vr, L_R + (s & 2) * 8192 + l16);
#pragma unroll
      for (int q = 0; q < 4; ++q) *(LAS f32x4*)(L + vr + q * 4096 + n * 1024) = ac[q >> 1][q & 1];
      asm volatile("s_waitcnt lgkmcnt(0)" ::: "memory");
      fl[20 + n] = s + 1;
      lds_wait_ge(20, 3, s + 1, tmo, 0x30016);
      {
        const LAS f32x4* pr = (const LAS f32x4*)(L + vr + n * 4096);
        const f32x4 o = (pr[0] + pr[64]) + (pr[128] + pr[192]);
        const int lo = olane();
        __builtin_amdgcn_raw_buffer_store_b64(v2u{pk2(o[0], o[1]), pk2(o[2], o[3])}, __builtin_amdgcn_make_buffer_rsrc(out, 0, S * 4096, 0x00020000),
                                              (lo & 15) * 8192 + (lo >> 4) * 8, (s * 32 + (n >> 1)) * 4096 + 2048 + 32 * (c0 + (n & 1)), OAUXX);
      }
      EV(5);
#pragma unroll
      for (int q = 0; q < NX1 + NX2; ++q) raw[q >> 3][q & 7] = nx[q >> 3][q & 7];
      EVDUMP(s);
    }
  } else if (w == 6) {
    if (c == 1) hs_lead<64>(rs, false, CNT_HS, 192u);
#pragma unroll 1
    for (int s = team; s + NARM - 2 < NSEG; s += 2) {
      VMCNT(0);
      BARE_BARRIER();
      if (s + NARM >= NSEG) break;
      BARE_BARRIER();
      __builtin_amdgcn_s_sleep(5);
      lds_wait_ge(2, 1, s + 1, tmo, 0x3001b);
      if (APR) __builtin_amdgcn_s_setprio(APR);
      ARM_REC(s + NARM)
      if (s + NARM + 1 < NSEG) ARM_REC(s + NARM + 1)
      if (APR) __builtin_amdgcn_s_setprio(0);
    }
  } else {
    const int sdk_ = NS2F(TN(2, SDK)), cv1_ = NS2F(TN(3, CV1)), cv2_ = NS2F(TN(4, CV2));
    Clk ck;
    clk_init(ck);
    unsigned t1 = cyc32();
    const int mg_ = NS2F(TN(0, MARG)), tol_ = NS2F(TN(13, TOL)), xl_ = NS2F(TN(14, XLATE));
    Pace pc = {0u, NS2F(ED0), NS2F(EA0), 0};
    const int sti_ = TN(15, STI), ofd_ = NS2F(TN(9, OFD));
#pragma unroll 1
    for (int s = 0; s < NSEG; ++s) {
      EVDECL;
      const bool on = (s & 1) == team;
      EV(3);
      if (on ? (int)(t1 - cyc32()) > OCK : s < 4) {
        wait_cyc(t1 - OCK);
        clk_update<OTRY>(ck, s < 4 ? 12 : OGN);
      }
      int k1 = ((cv1_ * (int)ck.cq) >> 16) - OK1, k2 = (cv2_ * (int)ck.cq) >> 16;
      int sst = (int)OFF_STEP + s * (int)UNIT + R_ST + ((cown ^ xreg(r_s + s)) << 12);
      asm volatile("" : "+s"(k1), "+s"(k2), "+s"(sst));
      OPQ(va, lane * 32);
      OPQ(vt, L_F + 48);
      wait_cyc(t1);
      __builtin_amdgcn_s_setprio(3);
      EV(0);
      v2u st = __builtin_amdgcn_raw_buffer_load_b64(rs, va, sst, AUX_SC1);
      if (__builtin_expect(anysent(st[0]), 0)) _Pragma("clang loop unroll(disable)") for (unsigned spins = 0;;) {
        if (spins >= 2) __builtin_amdgcn_s_sleep(4);
        if (spin_fail(spins, tmo, 0x3000b)) break;
        asm volatile("" : "+v"(va));
        wait_cyc(t1 + spins * sti_);
        st = __builtin_amdgcn_raw_buffer_load_b64(rs, va, sst + (spins == 1 ? 2048 : (spins == 2 ? 32768 : 0)), AUX_SC1);
        if (spins > 2) clk_update(ck, 8);
        if (!anysent(st[0])) break;
      }
      EV(4);
      u64 tn;
      asm volatile("s_memtime %0" : "=s"(tn), "+v"(st));
      unsigned md;
      const int dm = stamp_max(ck, st, md);
      const unsigned tr = clk_cyc(ck, dm);
      if (on) {
        asm volatile("s_waitcnt lgkmcnt(0)" : "+s"(tn));
        *(volatile LAS v4u*)(L + vt) = v4u{tr + k2, tr, ck.inv, 0u};
        EV(1);
        int d = (int)(tr + k1 - (unsigned)tn);
        d = (unsigned)d > (unsigned)CMAX ? 0 : d;
        asm volatile("1: s_sub_u32 %0, %0, 28\n\ts_cbranch_scc0 1b\n\ts_barrier" : "+s"(d)::"scc", "memory");
        if (s < NSEG - 1) BARE_BARRIER();
      }
      __builtin_amdgcn_s_setprio(0);
      EV(5);
      pace_step(pc, s, (ck.r << 4) + dm, md, mg_, tol_, xl_);
      t1 = clk_cyc(ck, dm + pc.dq + pc.ea - sdk_ + (on ? ofd_ : 0));
    }
  }
  return 0u;
}
#define FSL 32
__global__ __launch_bounds__(512, 2) void mega_kernel(const u16* __restrict__ x, const u16* __restrict__ mem0,
                                                      const u16* __restrict__ Wq, const u16* __restrict__ Wk,
                                                      const u16* __restrict__ Wv, const u16* __restrict__ Wo,
                                                      char* __restrict__ ws, u16* __restrict__ out,
                                                      const u16* __restrict__ ax, const u16* __restrict__ awr,
                                                      const u16* __restrict__ awi) {
  ws += (size_t)(-(uintptr_t)ws) & (UNIT - 1);
  const int bx = blockIdx.x;
  const int w = __builtin_amdgcn_readfirstlane(threadIdx.x >> 6);
  const int xg = bx & 7, idx = bx >> 3;
#define TEND
#define TMID(k)
  const u64 nonce = hs_post(__builtin_amdgcn_make_buffer_rsrc(ws, 0, 65536, 0x00020000), w);
  TMID(0);
  int role, h, par = 0;
  if (xg < NXC) {
    role = idx >= 2 * PQ ? 4 : idx >= PQ;
    h = xg * PQ + idx % PQ;
  } else {
    role = 2 + (idx / PQ & 1);
    par = idx / (2 * PQ);
    h = (xg - NXC) * PQ + idx % PQ;
  }
  if (role == 0) wroles<0>(h, par, idx & 7, xg, w, nonce, x, Wq, Wk, Wv, Wo, ws);
  if (role == 1) wroles<1>(h, par, idx & 7, xg, w, nonce, x, Wq, Wk, Wv, Wo, ws);
  if (role == 2) wroles<2>(h, par, idx & 7, xg, w, nonce, x, Wq, Wk, Wv, Wo, ws);
  if (role == 3) wroles<3>(h, par, idx & 7, xg, w, nonce, x, Wq, Wk, Wv, Wo, ws);
  const int oc = xg * PO + idx - 2 * PQ;
  unsigned perm = 0;
  if (role == 4) perm = owner<true>(oc, w, nonce, mem0, ws, out, 0u, w_slow(Wq, Wk, Wv, w, xg), ax, awr, awi);
  TMID(2);
  char* lds = g_lds;
  const __amdgpu_buffer_rsrc_t rs = __builtin_amdgcn_make_buffer_rsrc(ws, 0, 65536, 0x00020000);
  gu32* tmo = (gu32*)(ws + OFF_TMO);
  volatile LAS int* sact = (volatile LAS int*)(g_lds + 163584);
  unsigned idle = 0;
#define TLOG(id)
  auto pick = [&]() __attribute__((always_inline)) {
    int off = CNT_FFT1 + (olane() & 7) * 128;
    asm volatile("" : "+v"(off));
    const unsigned v = __builtin_amdgcn_raw_buffer_load_b32(rs, off, 0, AUX_SC1) + 1u;
    const unsigned d1 = __builtin_amdgcn_readlane(v, 0), d2 = __builtin_amdgcn_readlane(v, 1);
    const unsigned q1 = __builtin_amdgcn_readlane(v, 4), q2 = __builtin_amdgcn_readlane(v, 5), q3 = __builtin_amdgcn_readlane(v, 6);
    int kind = -1, qoff = 0;
    unsigned lim = 0;
    if (q1 < 64u) kind = 1, qoff = Q_FFT1, lim = 64u;
    else if (d1 >= 128u && q2 < 65u) kind = 2, qoff = Q_FFT2, lim = 65u;
    else if (d2 >= 65u && q3 < 128u) kind = 3, qoff = Q_FFT3, lim = 128u;
    else if (q3 >= 128u) kind = -2;
    int act = kind;
    if (kind >= 0) {
      unsigned t = 0;
      if (olane() == 0) t = __hip_atomic_fetch_add((gu32*)(ws + qoff), 1u, RLX_AGENT) + 1u;
      t = __builtin_amdgcn_readfirstlane(t);
      act = t < lim ? (kind << 16) | (int)t : -3;
    }
    return act;
  };
  fft_table(lds, w);
  int done = role == 4 ? CNT_ARM : 0;
  for (;;) {
    if (w == 7) *sact = role == 4 ? (done == CNT_ARM ? 0x10040 | oc : -2) : pick();
    VMCNT(0);
    RAW_BARRIER();
    if (done && w == 0 && olane() == 0) __hip_atomic_fetch_add((gu32*)(ws + done), 1u, RLX_AGENT);
    done = 0;
    const int act = __builtin_amdgcn_readfirstlane(*sact);
    RAW_BARRIER();
    if (act == -2) break;
    if (act >= 0) {
      const int kind = act >> 16, t = act & 0xffff;
      TLOG(1000 + kind);
      if (kind == 1) {
        fft_pass1(ws, ax, awr, awi, t ^ 64, w, role == 4 ? x : nullptr);
        done = CNT_FFT1;
      } else if (kind == 2) {
        fft_pass2(ws, t == 0 ? 64 : t - 1, w);
        done = CNT_FFT2;
      } else fft_pass3(ws, out, t, w);
      TLOG(999);
    } else if (act == -1) {
      __builtin_amdgcn_s_sleep(FSL);
      if (spin_fail(idle, tmo, 0x30022)) break;
    }
  }
  if (role == 4) {
    owner<false>(oc, w, nonce, mem0, ws, out, perm, nullptr, nullptr, nullptr, nullptr);
    return;
  }
  TEND;
}

extern "C" void kernel_launch(void* const* d_in, const int* in_sizes, int n_in, void* d_out, int out_size, void* d_ws,
                              size_t ws_size, hipStream_t stream) {
  const u16* A_x = (const u16*)d_in[0];
  const u16* A_wr = (const u16*)d_in[1];
  const u16* A_wi = (const u16*)d_in[2];
  const u16* B_x = (const u16*)d_in[3];
  const u16* B_mem0 = (const u16*)d_in[4];
  const u16* Wq = (const u16*)d_in[5];
  const u16* Wk = (const u16*)d_in[6];
  const u16* Wv = (const u16*)d_in[7];
  const u16* Wo = (const u16*)d_in[8];
  u16* out = (u16*)d_out;
  char* ws = (char*)d_ws;

  mega_kernel<<<256, 512, 0, stream>>>(B_x, B_mem0, Wq, Wk, Wv, Wo, ws, out, A_x, A_wr, A_wi);
}
```

```cpp
#include <hip/hip_runtime.h>
#include <hip/hip_bf16.h>
#include <stdint.h>
#include <type_traits>

typedef unsigned short u16;
typedef unsigned long long u64;
using bf16x8 = __attribute__((ext_vector_type(8))) __bf16;
using bf16x16 = __attribute__((ext_vector_type(16))) __bf16;
using f32x4 = __attribute__((ext_vector_type(4))) float;
typedef unsigned v32u __attribute__((ext_vector_type(32)));
typedef unsigned v16u __attribute__((ext_vector_type(16)));
typedef unsigned v8u __attribute__((ext_vector_type(8)));
typedef unsigned v4u __attribute__((ext_vector_type(4)));
typedef unsigned v2u __attribute__((ext_vector_type(2)));
typedef short s16x4 __attribute__((ext_vector_type(4)));
typedef float f32x2 __attribute__((ext_vector_type(2)));
#define GAS __attribute__((address_space(1)))
#define LAS __attribute__((address_space(3)))
typedef GAS unsigned gu32;
#define RLX_AGENT __ATOMIC_RELAXED, __HIP_MEMORY_SCOPE_AGENT

constexpr int S = 2048, D = 1024, H = 32, DH = 128, SEG = 32, NSEG = 64;
constexpr int DF = 513;

constexpr int NXC = 4;
constexpr int PQ = 32 / NXC, PO = 64 / NXC;

__shared__ __attribute__((aligned(16))) char g_lds[163840];
#define SPINCAP (1u << 18)
constexpr unsigned kSpinCap = SPINCAP;
constexpr int AUX_SC1 = 16;
constexpr int OOB = 0x7ff00000;
constexpr size_t UNIT = 524288;
constexpr int F_QK = 0, F_VW = 8192, CNT_FFT1 = 16384, CNT_FFT2 = 16512, CNT_ARM = 16640;
constexpr int Q_FFT1 = 16896, Q_FFT2 = 17024, Q_FFT3 = 17152;
constexpr size_t OFF_TMO = 20480;
constexpr size_t OFF_CAL = 24576, OFF_XID = 25088;
constexpr int OFF_PERM = 25600;
constexpr size_t ARM_HOST = 28672;
constexpr int OFF_REQ = 49152, OFF_ACK = 53248, CNT_HS = 17280, CNT_HSO = 17408;
constexpr size_t OFF_SCR = 65536;
constexpr size_t OFF_VM = UNIT;
constexpr int VM_SLOT = 4 << 20, VM_SLOT2 = 8 << 20;
constexpr size_t OFF_DBG = 3 * UNIT;
constexpr size_t OFF_STEP = 27 * UNIT;
constexpr int R_MEM = 0, R_EX = 131072, R_PM = 262144, R_ST = 327680, R_PX = 393216, R_DUM = 475136;
constexpr size_t OFF_QK = (size_t)48 << 20;
constexpr size_t OFF_X1 = (size_t)80 << 20;
constexpr size_t OFF_VWX = (size_t)89 << 20;
constexpr size_t WS_USED = (size_t)244 << 20;
constexpr size_t OFF_WT = (size_t)244 << 20;
constexpr int NARM = 4;

__device__ __forceinline__ int olane() {
  int t;
  asm volatile("v_mbcnt_lo_u32_b32 %0, -1, 0\n\tv_mbcnt_hi_u32_b32 %0, -1, %0" : "=v"(t));
  __builtin_assume(t >= 0 && t < 64);
  return t;
}
__device__ __forceinline__ float bf2f(u16 v) { return __uint_as_float((unsigned)v << 16); }
__device__ __forceinline__ unsigned pk2(float a, float b) {
  typedef __bf16 b2 __attribute__((ext_vector_type(2)));
  f32x2 f = {a, b};
  b2 r = __builtin_convertvector(f, b2);
  return __builtin_bit_cast(unsigned, r);
}
__device__ __forceinline__ unsigned pk2s(float a, float b) { return min(pk2(a, b), 0xfffffffeu); }
__device__ __forceinline__ u16 f2bf(float f) { return (u16)(pk2(f, 0.f) & 0xffffu); }
__device__ __forceinline__ float rbf(float f) { return __uint_as_float(pk2(0.f, f)); }
__device__ __forceinline__ float opq_one() {
  float o = 1.f;
  asm("" : "+s"(o));
  return o;
}
__device__ __forceinline__ unsigned fold_pk(f32x4 a, float one) { return pk2(a[0] + a[2], __builtin_fmaf(a[1], one, a[3])); }
__device__ __forceinline__ float mul1(float a, float c) {
  float r;
  asm("v_mul_f32 %0, %2, %1" : "=v"(r) : "v"(a), "s"(c));
  return r;
}
__device__ __forceinline__ float fma1(float a, float c, float b) {
  float r;
  asm("v_fma_f32 %0, %1, %2, %3" : "=v"(r) : "v"(a), "s"(c), "v"(b));
  return r;
}
__device__ __forceinline__ int flag_of(int seg, int h) { return h * 256 + (seg & 1) * 128 + (seg >> 1) * 4; }
__device__ __forceinline__ int xreg(int r) { return ((r & 1) << 2) | ((r >> 1) & 1) | ((r >> 1) & 2); }

__device__ __forceinline__ void tmo_report(gu32* tmo, unsigned code) {
  unsigned z = 0, c = code;
  asm volatile("" : "+v"(z), "+v"(c));
  asm volatile("global_store_dword %0, %1, %2 sc1" ::"v"(z), "v"(c), "s"(tmo) : "memory");
}
__device__ __forceinline__ bool spin_fail(unsigned& spins, gu32* tmo, unsigned code) {
  asm volatile("" ::: "memory");
  if (__builtin_expect(++spins > kSpinCap, 0)) {
    tmo_report(tmo, code);
    return true;
  }
  return false;
}
__device__ __forceinline__ unsigned wave_min(unsigned v) {
  v = min(v, (unsigned)__builtin_amdgcn_mov_dpp((int)v, 0x121, 0xf, 0xf, true));
  v = min(v, (unsigned)__builtin_amdgcn_mov_dpp((int)v, 0x122, 0xf, 0xf, true));
  v = min(v, (unsigned)__builtin_amdgcn_mov_dpp((int)v, 0x124, 0xf, 0xf, true));
  v = min(v, (unsigned)__builtin_amdgcn_mov_dpp((int)v, 0x128, 0xf, 0xf, true));
  auto r = __builtin_amdgcn_permlane32_swap(v, v, false, false);
  v = min(r[0], r[1]);
  auto q = __builtin_amdgcn_permlane16_swap(v, v, false, false);
  return min(q[0], q[1]);
}
__device__ __forceinline__ float xmax16_32(float x) {
  auto r = __builtin_amdgcn_permlane32_swap(__float_as_uint(x), __float_as_uint(x), false, false);
  x = __builtin_amdgcn_fmed3f(__uint_as_float(r[0]), __uint_as_float(r[1]), __builtin_inff());
  auto q = __builtin_amdgcn_permlane16_swap(__float_as_uint(x), __float_as_uint(x), false, false);
  return __builtin_amdgcn_fmed3f(__uint_as_float(q[0]), __uint_as_float(q[1]), __builtin_inff());
}
__device__ __forceinline__ float xsum16(float x) {
  auto q = __builtin_amdgcn_permlane16_swap(__float_as_uint(x), __float_as_uint(x), false, false);
  return __uint_as_float(q[0]) + __uint_as_float(q[1]);
}
__device__ __forceinline__ float xsum16_32(float x) {
  auto r = __builtin_amdgcn_permlane32_swap(__float_as_uint(x), __float_as_uint(x), false, false);
  return xsum16(__uint_as_float(r[0]) + __uint_as_float(r[1]));
}
__device__ __forceinline__ unsigned mx4(unsigned m, v4u v) { return max(max(max(max(m, v[0]), v[1]), v[2]), v[3]); }
__device__ __forceinline__ bool anysent(unsigned m) { return __builtin_amdgcn_ballot_w64(m == 0xffffffffu) != 0ull; }

__device__ __forceinline__ unsigned now32() { return (unsigned)__builtin_amdgcn_s_memrealtime(); }
__device__ __forceinline__ unsigned cyc32() { return (unsigned)__builtin_amdgcn_s_memtime(); }
#define CMAX 14000
__device__ __forceinline__ void wait_cyc(unsigned t) {
  t = __builtin_amdgcn_readfirstlane(t);
  int d = (int)(t - cyc32());
  if (d > CMAX) return;
  while (d > 260) {
    __builtin_amdgcn_s_sleep(1);
    d = (int)(t - cyc32());
  }
  d = __builtin_amdgcn_readfirstlane(d - 70);
  if (d > 0) asm volatile("1: s_sub_u32 %0, %0, 28\n\ts_cbranch_scc0 1b" : "+s"(d)::"scc", "memory");
}
#define CLKSPAN 124u
struct Clk {
  unsigned r, m;
  unsigned cq, inv;
};
__device__ __forceinline__ bool clk_edge(unsigned& r, unsigned& m) {
  u64 t[14], m0, m1;
  unsigned n;
#define CE_S(i) "s_memrealtime %" #i "\n\ts_nop 0\n\t"
  asm volatile("s_setprio 3\n\ts_memtime %14\n\t" CE_S(0) CE_S(1) CE_S(2) CE_S(3) CE_S(4) CE_S(5) CE_S(6) CE_S(7) CE_S(8) CE_S(9) CE_S(10)
               CE_S(11) CE_S(12) "s_memtime %15\n\ts_memrealtime %13\n\ts_setprio 0\n\ts_waitcnt lgkmcnt(0)"
               : "=s"(t[0]), "=s"(t[1]), "=s"(t[2]), "=s"(t[3]), "=s"(t[4]), "=s"(t[5]), "=s"(t[6]), "=s"(t[7]), "=s"(t[8]), "=s"(t[9]),
                 "=s"(t[10]), "=s"(t[11]), "=s"(t[12]), "=s"(t[13]), "=s"(m0), "=s"(m1));
#define CE_C(i) "s_cmp_eq_u32 %" #i ", %1\n\ts_addc_u32 %0, %0, 0\n\t"
#define CE_T(i) "s"((unsigned)t[i])
  asm("s_mov_b32 %0, 0\n\t" CE_C(2) CE_C(3) CE_C(4) CE_C(5) CE_C(6) CE_C(7) CE_C(8) CE_C(9) CE_C(10) CE_C(11) CE_C(12) CE_C(13) CE_C(14)
      : "=&s"(n)
      : CE_T(0), CE_T(1), CE_T(2), CE_T(3), CE_T(4), CE_T(5), CE_T(6), CE_T(7), CE_T(8), CE_T(9), CE_T(10), CE_T(11), CE_T(12), CE_T(13)
      : "scc");
  r = (unsigned)t[0] + 4;
  m = (unsigned)m0 + n * 8;
  return (unsigned)m1 - (unsigned)m0 <= CLKSPAN;
}
__device__ __forceinline__ void clk_init(Clk& c) {
#pragma unroll 1
  for (int k = 0; k < 3; ++k)
    if (clk_edge(c.r, c.m)) break;
  c.cq = 92500u;
  c.inv = 46432u;
}
template <int TRIES = 1>
__device__ __forceinline__ void clk_update(Clk& c, int gain) {
  unsigned r, m;
  bool ok = clk_edge(r, m);
#pragma unroll 1
  for (int k = 1; k < TRIES && !ok; ++k) ok = clk_edge(r, m);
  ok |= r - c.r >= 1500u;
  const unsigned dr = r - c.r;
  const int err = (int)(m - c.m - ((dr * c.cq) >> 12));
  unsigned q = c.cq + (unsigned)((ok & (dr * gain < 6000u)) ? err * gain : 0);
  asm("s_max_i32 %0, %0, 0x101d0\n\ts_min_i32 %0, %0, 0x1b580" : "+s"(q)::"scc");
  c.cq = q;
  c.inv -= (unsigned)__mulhi((int)c.inv, (int)(q * c.inv));
  c.r = ok ? r : c.r;
  c.m = ok ? m : c.m;
}
__device__ __forceinline__ int stamp_max(const Clk& c, v2u st, unsigned& md) {
  unsigned a = st[0] - (c.r << 4) + 0x40000000u, b = st[1], x;
#define SM_S(p) "v_max_u32_dpp %2, %2, %2 " p "\n\tv_max_u32_dpp %3, %3, %3 " p "\n\ts_nop 0\n\t"
  asm volatile("s_nop 1\n\t" SM_S("row_ror:1 row_mask:0xf bank_mask:0xf") SM_S("row_ror:2 row_mask:0xf bank_mask:0xf")
               SM_S("row_ror:4 row_mask:0xf bank_mask:0xf") SM_S("row_ror:8 row_mask:0xf bank_mask:0xf")
               SM_S("row_bcast:15 row_mask:0xa bank_mask:0xf") SM_S("row_bcast:31 row_mask:0xc bank_mask:0xf")
               "v_readlane_b32 %0, %2, 63\n\tv_readlane_b32 %1, %3, 63"
               : "=s"(x), "=s"(md), "+v"(a), "+v"(b));
  return (int)(x - 0x40000000u);
}
__device__ __forceinline__ unsigned clk_cyc(const Clk& c, int d) { return c.m + (unsigned)(((long long)d * (int)c.cq) >> 16); }
#define NS2F(ns) (((ns) * 8) / 5)
struct Pace {
  unsigned rf;
  int ed, ea, dq;
};
__device__ __forceinline__ void pace_step(Pace& p, int s, unsigned rf, unsigned md, int marg, int tol, int xlate) {
  const int arr = s ? (int)(rf - p.rf) - p.dq : 0x10000000;
  const bool late = arr > p.ea + tol;
  p.ea = min(arr, p.ea + 5);
  p.ed = (int)min(md, (unsigned)p.ed + 5u);
  p.rf = rf;
  p.dq = p.ed + marg + (late ? xlate : 0);
}

template <int SLP>
__device__ __forceinline__ void wait_flag(__amdgpu_buffer_rsrc_t rs, int off, gu32* tmo, unsigned code) {
  _Pragma("clang loop unroll(disable)") for (unsigned spins = 0;;) {
    asm volatile("" : "+v"(off));
    unsigned v = __builtin_amdgcn_raw_buffer_load_b32(rs, off, 0, AUX_SC1);
    if (__all(v == 0u)) break;
    __builtin_amdgcn_s_sleep(SLP);
    if (spin_fail(spins, tmo, code)) break;
  }
}
__device__ __forceinline__ void arm_wait(__amdgpu_buffer_rsrc_t rs, gu32* tmo) {
  _Pragma("clang loop unroll(disable)") for (unsigned spins = 0;;) {
    int off = CNT_ARM;
    asm volatile("" : "+v"(off));
    if (__all(__builtin_amdgcn_raw_buffer_load_b32(rs, off, 0, AUX_SC1) + 1u >= 64u)) break;
    __builtin_amdgcn_s_sleep(8);
    if (spin_fail(spins, tmo, 0x30007)) break;
  }
}
#define RAW_BARRIER()                                    \
  do {                                                   \
    asm volatile("" ::: "memory");                       \
    __builtin_amdgcn_s_waitcnt(0xc07f);                  \
    __builtin_amdgcn_s_barrier();                        \
    asm volatile("" ::: "memory");                       \
  } while (0)
#define BARE_BARRIER()                 \
  do {                                 \
    asm volatile("" ::: "memory");     \
    __builtin_amdgcn_s_barrier();      \
    asm volatile("" ::: "memory");     \
  } while (0)
#define VMCNT(n) asm volatile("s_waitcnt vmcnt(" #n ")" ::: "memory")
#define SB() __builtin_amdgcn_sched_barrier(0)
#define OPQ(v, e) int v = (e); asm volatile("" : "+v"(v))
template <bool NT = false>
__device__ __forceinline__ void glds16(const void* base, int voff, unsigned dst) {
  unsigned keep;
  if constexpr (NT)
    asm volatile("s_mov_b32 %0, m0\n\ts_mov_b32 m0, %3\n\ts_nop 0\n\tglobal_load_lds_dwordx4 %1, %2 nt\n\ts_mov_b32 m0, %0"
                 : "=&s"(keep)
                 : "v"(voff), "s"(base), "s"(dst)
                 : "memory");
  else
  asm volatile("s_mov_b32 %0, m0\n\ts_mov_b32 m0, %3\n\ts_nop 0\n\tglobal_load_lds_dwordx4 %1, %2\n\ts_mov_b32 m0, %0"
               : "=&s"(keep)
               : "v"(voff), "s"(base), "s"(dst)
               : "memory");
}
__device__ __forceinline__ const char* w_slow(const u16* Wq, const u16* Wk, const u16* Wv, int w, int xg) {
  const char* p = (const char*)(w < 2 || w == 6 ? Wq : (w < 4 || w == 7 ? Wk : Wv));
  const int sk = 3 - (int)((uintptr_t)p >> 7);
  const int h = w < 6 ? (xg & 3) * 8 + (w & 1) * 4 + ((sk & 7) >> 1) : ((sk + 60) & 63) >> 1;
  return (h >> 3) == (xg & 3) ? p + h * 256 : nullptr;
}
__device__ __forceinline__ void w_touch(const char* p, int row, unsigned dst) {
  unsigned keep;
  const int ln = olane();
  asm volatile("s_mov_b32 %0, m0\n\ts_mov_b32 m0, %3\n\ts_nop 0\n\tglobal_load_lds_dword %1, %2\n\ts_mov_b32 m0, %0"
               : "=&s"(keep)
               : "v"((row + (ln >> 2)) * 8192 + (ln & 3) * 64), "s"(p), "s"(dst)
               : "memory");
}
#define CPF 1
constexpr int CODE_LEN = 168960;
__device__ __forceinline__ void code_touch(int part, unsigned dst) {
  unsigned keep;
  const int v = min(part * 8192 + olane() * 128, CODE_LEN - 128);
  asm volatile("s_getpc_b64 vcc\n.Lct%=:\n\ts_sub_u32 vcc_lo, vcc_lo, .Lct%=-.Lkbeg\n\ts_subb_u32 vcc_hi, vcc_hi, 0\n\t"
               "s_mov_b32 %0, m0\n\ts_mov_b32 m0, %2\n\ts_nop 0\n\t"
               "global_load_lds_dword %1, vcc\n\ts_mov_b32 m0, %0"
               : "=&s"(keep)
               : "v"(v), "s"(dst)
               : "vcc", "scc", "memory");
}
__device__ __forceinline__ void code_ahead(int part, unsigned dst) {
  unsigned keep;
  const int v = part * 2048 + (olane() & 15) * 128;
  asm volatile("s_getpc_b64 vcc\n\ts_mov_b32 %0, m0\n\ts_mov_b32 m0, %2\n\ts_nop 0\n\tglobal_load_lds_dword %1, vcc ; AHEAD\n\ts_mov_b32 m0, %0"
               : "=&s"(keep)
               : "v"(v), "s"(dst)
               : "vcc", "memory");
}
__device__ __forceinline__ f32x4 smf(v4u a, v8u b, f32x4 c, int idx) {
  return __builtin_amdgcn_smfmac_f32_16x16x64_bf16(__builtin_bit_cast(bf16x8, a), __builtin_bit_cast(bf16x16, b), c, idx, 0, 0);
}
__device__ __forceinline__ f32x4 smf1(v4u a, v8u b, f32x4 c, int idx) {
  return __builtin_amdgcn_smfmac_f32_16x16x64_bf16(__builtin_bit_cast(bf16x8, a), __builtin_bit_cast(bf16x16, b), c, idx, 0, 1);
}
__device__ __forceinline__ f32x4 mma(v4u a, v4u b, f32x4 c) {
  return __builtin_amdgcn_mfma_f32_16x16x32_bf16(__builtin_bit_cast(bf16x8, a), __builtin_bit_cast(bf16x8, b), c, 0, 0, 0);
}
__device__ __forceinline__ void st128(v4u d, __amdgpu_buffer_rsrc_t rs, int voff, int soff) {
  __builtin_amdgcn_raw_buffer_store_b128(d, rs, voff, soff, AUX_SC1);
  asm volatile("s_nop 1" ::"v"(d));
}
__device__ __forceinline__ f32x4 zero4() {
  f32x2 a, b;
  asm volatile("v_mov_b64 %0, 0" : "=v"(a));
  asm volatile("v_mov_b64 %0, 0" : "=v"(b));
  return f32x4{a[0], a[1], b[0], b[1]};
}
__device__ __forceinline__ f32x2 zero2() {
  f32x2 a;
  asm volatile("v_mov_b64 %0, 0" : "=v"(a));
  return a;
}
__device__ __forceinline__ v4u lo4(v8u v) { return v4u{v[0], v[1], v[2], v[3]}; }
__device__ __forceinline__ v4u hi4(v8u v) { return v4u{v[4], v[5], v[6], v[7]}; }
__device__ __forceinline__ v8u cat8(v4u a, v4u b) { return v8u{a[0], a[1], a[2], a[3], b[0], b[1], b[2], b[3]}; }
__device__ __forceinline__ v32u cat32(v8u a, v8u b, v8u c, v8u d) {
  const v16u x = __builtin_shufflevector(a, b, 0, 1, 2, 3, 4, 5, 6, 7, 8, 9, 10, 11, 12, 13, 14, 15);
  const v16u y = __builtin_shufflevector(c, d, 0, 1, 2, 3, 4, 5, 6, 7, 8, 9, 10, 11, 12, 13, 14, 15);
  return __builtin_shufflevector(x, y, 0, 1, 2, 3, 4, 5, 6, 7, 8, 9, 10, 11, 12, 13, 14, 15, 16, 17, 18, 19, 20, 21, 22, 23, 24, 25, 26,
                                 27, 28, 29, 30, 31);
}
__device__ __forceinline__ v2u trd(LAS char* p) {
  return __builtin_bit_cast(v2u, __builtin_amdgcn_ds_read_tr16_b64_v4i16((LAS s16x4*)p));
}
__device__ __forceinline__ int vm_slot(int s) { return (s & 1) * VM_SLOT + ((s >> 1) & 1) * VM_SLOT2; }
__device__ __forceinline__ int cls_of(unsigned perm, int x) { return (int)((perm >> (3 * x)) & 7u); }

__device__ __forceinline__ v2u numa_load(__amdgpu_buffer_rsrc_t rs, gu32* tmo) {
  unsigned c, xid;
  _Pragma("clang loop unroll(disable)") for (unsigned spins = 0;;) {
    OPQ(a, olane() * 4);
    c = __builtin_amdgcn_raw_buffer_load_b32(rs, a, (int)OFF_CAL, AUX_SC1);
    xid = __builtin_amdgcn_raw_buffer_load_b32(rs, ((a >> 5) & (NXC - 1)) * 4, (int)OFF_XID, AUX_SC1);
    if (__all((c != 0xffffffffu) & (xid != 0xffffffffu))) break;
    __builtin_amdgcn_s_sleep(16);
    if (spin_fail(spins, tmo, 0x30008)) break;
  }
  return v2u{c, xid};
}
__device__ __forceinline__ unsigned numa_perm(v2u in, LAS unsigned* tab) {
  const int lane = olane();
  const unsigned c = in[0], xid = in[1];
  tab[lane] = 0xffffu;
  tab[64 + lane] = 0xffffu;
  tab[128 + lane] = 0u;
  __builtin_amdgcn_s_waitcnt(0xc07f);
  __hip_atomic_fetch_min(tab + ((c >> 16) & 127u), c & 0xffffu, __ATOMIC_RELAXED, __HIP_MEMORY_SCOPE_WORKGROUP);
  __hip_atomic_fetch_add(tab + 128 + ((c >> 21) & 3u) * 2 + ((c >> 17) & 1u), c & 0xffffu, __ATOMIC_RELAXED, __HIP_MEMORY_SCOPE_WORKGROUP);
  __builtin_amdgcn_s_waitcnt(0xc07f);
  asm volatile("" ::: "memory");
  const unsigned s0 = tab[128 + ((xid >> 2) & 3u) * 2], s1 = tab[129 + ((xid >> 2) & 3u) * 2];
  const bool far = ((lane >> 1) & 1) != (s1 < s0 ? 1 : 0);
  unsigned key = ((tab[(xid & 15u) * 8 + (lane & 7)] + (far ? 0x10000u : 0u)) << 6) | (unsigned)lane;
  if ((lane >> 3) >= NXC) key = 0xffffffffu;
  unsigned perm = 0;
#pragma unroll 1
  for (int r = 0; r < NXC; ++r) {
    const unsigned m = wave_min(key);
    const unsigned bx = (m >> 3) & 7u, bc = m & 7u;
    perm |= bc << (3 * bx);
    if ((unsigned)(lane >> 3) == bx || (unsigned)(lane & 7) == bc) key = 0xffffffffu;
  }
  return __builtin_amdgcn_readfirstlane(perm);
}
#define VRING 64
#define VWN 0
__device__ __forceinline__ int vw_addr(unsigned pm, int tile, int seg, int gq) {
  return (int)OFF_VWX + ((((tile >> 3) * VRING + (seg & (VRING - 1))) * 8 + (tile & 7)) * 8 + gq) * 4096;
}

#define QKN 0
constexpr int QK_RT = QKN ? 32768 : 4096;
__device__ __forceinline__ int qk_addr(int mat, int seg, int h, int cl, int ra) {
  return (int)OFF_QK + ((mat * 64 + seg) * 32 + h) * 8192;
}

__device__ __forceinline__ u64 hs_post(__amdgpu_buffer_rsrc_t rs, int w) {
  const u64 n = __builtin_amdgcn_s_memrealtime();
  if (w == 0) __builtin_amdgcn_raw_buffer_store_b64(v2u{(unsigned)n, (unsigned)(n >> 32)}, rs, olane() == 0 ? 0 : OOB,
                                                    OFF_REQ + (int)blockIdx.x * 8, AUX_SC1);
  return n;
}
__device__ __forceinline__ void hs_wait(__amdgpu_buffer_rsrc_t rs, char* ws, u64 n, int cnt) {
  _Pragma("clang loop unroll(disable)") for (unsigned spins = 0; spins < (kSpinCap << 2); ++spins) {
    int z = 0;
    asm volatile("" : "+v"(z));
    const v2u v = __builtin_amdgcn_raw_buffer_load_b64(rs, z, OFF_ACK + (int)blockIdx.x * 8, AUX_SC1);
    if (__all(v[0] == (unsigned)n && v[1] == (unsigned)(n >> 32))) break;
    __builtin_amdgcn_s_sleep(8);
  }
  if (olane() == 0) __hip_atomic_fetch_add((gu32*)(ws + cnt), 1u, RLX_AGENT);
}
template <int SLP>
__device__ __forceinline__ void hs_lead(__amdgpu_buffer_rsrc_t rs, bool fill, int cnt, unsigned want) {
  const int l16 = olane() * 16;
  if (fill) {
    for (int o = 0; o < (int)ARM_HOST; o += 1024) __builtin_amdgcn_raw_buffer_store_b128(v4u{~0u, ~0u, ~0u, ~0u}, rs, l16, o, AUX_SC1);
    VMCNT(0);
  }
  _Pragma("clang loop unroll(disable)") for (unsigned spins = 0; spins < (kSpinCap << 2); ++spins) {
    int va = l16;
    asm volatile("" : "+v"(va));
    const v4u a = __builtin_amdgcn_raw_buffer_load_b128(rs, va, OFF_REQ, AUX_SC1);
    const v4u b = __builtin_amdgcn_raw_buffer_load_b128(rs, va + 1024, OFF_REQ, AUX_SC1);
    const unsigned n = __builtin_amdgcn_raw_buffer_load_b32(rs, va & 0, cnt, AUX_SC1);
    if (__all(n + 1u >= want)) break;
    __builtin_amdgcn_raw_buffer_store_b128(a, rs, va, OFF_ACK, AUX_SC1);
    __builtin_amdgcn_raw_buffer_store_b128(b, rs, va + 1024, OFF_ACK, AUX_SC1);
    __builtin_amdgcn_s_sleep(SLP);
  }
}

#define TN(k, d) (d)
#define EVDECL
#define EV(k)
#define EVDUMP(s)
#define EVCLK(s, c)
#define EVOUT
#define PX 2
#define NOB 3
#define VO_A \
  "s_branch .Lgo_%=\n" \
  ".Lr_%=:\n" \
  "s_sub_u32 %[rt], %[rt], 1\n" \
  "s_cbranch_scc1 .Lrb_%=\n" \
  "s_sleep 2\n" \
  "buffer_load_dwordx4 v[192:195], v248, %[rs], %[o0] offen sc1\n" \
  "buffer_load_dwordx4 v[196:199], v248, %[rs], %[o0] offen offset:1024 sc1\n" \
  "v_mov_b64 v[224:225], 0\n" \
  "v_mov_b64 v[226:227], 0\n" \
  "v_mov_b64 v[228:229], 0\n" \
  "v_mov_b64 v[230:231], 0\n" \
  "s_nop 1\n" \
  "s_branch .Lw_%=\n" \
  ".Lgo_%=:\n" \
  "v_add_u32 v252, 0x20000, v248\n" \
  "ds_read_b128 v[200:203], v248 offset:32768\n" \
  "ds_read_b128 v[204:207], v248 offset:33792\n" \
  "ds_read_b128 v[208:211], v248 offset:49152\n" \
  "ds_read_b128 v[212:215], v248 offset:50176\n" \
  "ds_read_b128 v[216:219], v252 offset:0\n" \
  "ds_read_b128 v[220:223], v252 offset:1024\n" \
  "ds_read_b128 v[240:243], v252 offset:16384\n" \
  "ds_read_b128 v[244:247], v252 offset:17408\n" \
  "v_mov_b64 v[224:225], 0\n" \
  "v_mov_b64 v[226:227], 0\n" \
  "v_mov_b64 v[228:229], 0\n" \
  "v_mov_b64 v[230:231], 0\n" \
  "v_xor_b32 v250, 0x800, v248\n" \
  "v_xor_b32 v251, 0x1000, v248\n" \
  "s_mov_b32 %[rt], 0x40000\n" \
  "s_setprio 2\n" \
  "s_waitcnt lgkmcnt(0)\n" \
  "s_barrier\n" \
  "buffer_load_dwordx4 v[192:195], v248, %[rs], %[o0] offen sc1\n" \
  "buffer_load_dwordx4 v[196:199], v248, %[rs], %[o0] offen offset:1024 sc1\n" \
  ".Lw_%=:\n" \
  "s_waitcnt vmcnt(1)\n" \
  "ds_write_b128 v248, v[192:195]\n" \
  "v_smfmac_f32_16x16x64_bf16 v[224:227], v[192:195], v[200:207], v249\n" \
  "v_max3_u32 v252, v192, v193, v194\n" \
  "s_waitcnt vmcnt(0)\n" \
  "ds_write_b128 v248, v[196:199] offset:1024\n" \
  "v_smfmac_f32_16x16x64_bf16 v[228:231], v[192:195], v[208:215], v249\n" \
  "v_max3_u32 v252, v252, v195, v196\n" \
  "v_smfmac_f32_16x16x64_bf16 v[224:227], v[196:199], v[216:223], v249\n" \
  "v_max3_u32 v252, v252, v197, v198\n" \
  "v_max_u32 v252, v252, v199\n" \
  "v_smfmac_f32_16x16x64_bf16 v[228:231], v[196:199], v[240:247], v249\n" \
  "v_cmp_eq_u32 vcc, -1, v252\n" \
  "v_xor_b32 v252, 0x1800, v248\n" \
  "s_cbranch_vccnz .Lr_%=\n" \
  ".Lrb_%=:\n" \
  "s_waitcnt lgkmcnt(0)\n" \
  "s_barrier\n" \
  "ds_read_b128 v[200:203], v250 offset:0\n" \
  "ds_read_b128 v[204:207], v250 offset:1024\n" \
  "ds_read_b128 v[208:211], v251 offset:0\n" \
  "ds_read_b128 v[212:215], v251 offset:1024\n" \
  "ds_read_b128 v[216:219], v252 offset:0\n" \
  "ds_read_b128 v[220:223], v252 offset:1024\n" \
  "s_waitcnt lgkmcnt(5)\n" \
  "v_smfmac_f32_16x16x64_bf16 v[224:227], v[200:203], v[0:7], v249\n" \
  "v_smfmac_f32_16x16x64_bf16 v[228:231], v[200:203], v[48:55], v249\n" \
  "s_waitcnt lgkmcnt(4)\n" \
  "v_smfmac_f32_16x16x64_bf16 v[224:227], v[204:207], v[8:15], v249\n" \
  "v_smfmac_f32_16x16x64_bf16 v[228:231], v[204:207], v[56:63], v249\n" \
  "s_waitcnt lgkmcnt(3)\n" \
  "v_smfmac_f32_16x16x64_bf16 v[224:227], v[208:211], v[16:23], v249\n" \
  "v_smfmac_f32_16x16x64_bf16 v[228:231], v[208:211], v[64:71], v249\n" \
  "s_waitcnt lgkmcnt(2)\n" \
  "v_smfmac_f32_16x16x64_bf16 v[224:227], v[212:215], v[24:31], v249\n" \
  "s_waitcnt lgkmcnt(1)\n" \
  "v_smfmac_f32_16x16x64_bf16 v[224:227], v[216:219], v[32:39], v249\n" \
  "s_waitcnt lgkmcnt(0)\n" \
  "v_smfmac_f32_16x16x64_bf16 v[224:227], v[220:223], v[40:47], v249\n" \
  "v_smfmac_f32_16x16x64_bf16 v[228:231], v[212:215], v[72:79], v249\n" \
  "v_smfmac_f32_16x16x64_bf16 v[228:231], v[216:219], v[80:87], v249\n" \
  "s_nop 0\n" \
  "v_add_f32 v224, v224, v226\n" \
  "v_smfmac_f32_16x16x64_bf16 v[228:231], v[220:223], v[88:95], v249\n" \
  "v_add_f32 v225, v225, v227\n" \
  "ds_write_b64 v248, v[224:225] offset:16384\n" \
  "s_nop 6\n" \
  "v_add_f32 v228, v228, v230\n" \
  "v_add_f32 v229, v229, v231\n" \
  "ds_write_b64 v248, v[228:229] offset:16392\n" \
  "s_setprio 0\n" \
  "ds_read_b128 v[240:243], v254 offset:0\n" \
  "ds_read_b128 v[244:247], v254 offset:1024\n" \
  "s_waitcnt lgkmcnt(2)\n" \
  "s_barrier\n" \
  "s_nop 1\n" \
  "ds_read_b128 v[224:227], v254 offset:16384\n" \
  "ds_read_b128 v[228:231], v254 offset:17408\n" \
  "v_mov_b64 v[192:193], 0\n" \
  "v_mov_b64 v[194:195], 0\n" \
  "v_mov_b64 v[196:197], 0\n" \
  "v_mov_b64 v[198:199], 0\n" \
  "v_mov_b64 v[200:201], 0\n" \
  "v_mov_b64 v[202:203], 0\n" \
  "v_mov_b64 v[204:205], 0\n" \
  "v_mov_b64 v[206:207], 0\n" \
  "s_setprio 2\n" \
  "s_barrier\n" \
  "ds_read_b64_tr_b16 v[208:209], v238 offset:16384\n" \
  "ds_read_b64_tr_b16 v[210:211], v238 offset:16576\n" \
  "ds_read_b64_tr_b16 v[212:213], v238 offset:17152\n" \
  "ds_read_b64_tr_b16 v[214:215], v238 offset:17344\n" \
  "ds_read_b64_tr_b16 v[216:217], v238 offset:17920\n" \
  "ds_read_b64_tr_b16 v[218:219], v238 offset:18112\n" \
  "ds_read_b64_tr_b16 v[220:221], v238 offset:18688\n" \
  "ds_read_b64_tr_b16 v[222:223], v238 offset:18880\n" \
  "s_waitcnt lgkmcnt(6)\n" \
  "v_smfmac_f32_16x16x64_bf16 v[200:203], v[208:211], v[240:247], v249 abid:1\n" \
  "ds_read_b128 v[240:243], v254 offset:32768\n" \
  "v_smfmac_f32_16x16x64_bf16 v[192:195], v[208:211], v[160:167], v249 abid:1\n" \
  "ds_read_b128 v[244:247], v254 offset:33792\n" \
  "v_smfmac_f32_16x16x64_bf16 v[196:199], v[208:211], v[96:103], v249 abid:1\n" \
  "v_smfmac_f32_16x16x64_bf16 v[204:207], v[208:211], v[128:135], v249 abid:1\n" \
  "s_waitcnt lgkmcnt(6)\n" \
  "v_smfmac_f32_16x16x64_bf16 v[192:195], v[212:215], v[168:175], v249 abid:1\n" \
  "v_smfmac_f32_16x16x64_bf16 v[200:203], v[212:215], v[224:231], v249 abid:1\n" \
  "ds_read_b128 v[224:227], v254 offset:49152\n" \
  "v_smfmac_f32_16x16x64_bf16 v[196:199], v[212:215], v[104:111], v249 abid:1\n" \
  "ds_read_b128 v[228:231], v254 offset:50176\n" \
  "v_smfmac_f32_16x16x64_bf16 v[204:207], v[212:215], v[136:143], v249 abid:1\n" \
  "s_waitcnt lgkmcnt(6)\n" \
  "v_smfmac_f32_16x16x64_bf16 v[192:195], v[216:219], v[176:183], v249 abid:1\n" \
  "v_smfmac_f32_16x16x64_bf16 v[196:199], v[216:219], v[112:119], v249 abid:1\n" \
  "s_waitcnt lgkmcnt(4)\n" \
  "v_smfmac_f32_16x16x64_bf16 v[192:195], v[220:223], v[184:191], v249 abid:1\n" \
  "v_smfmac_f32_16x16x64_bf16 v[196:199], v[220:223], v[120:127], v249 abid:1\n" \
  "v_smfmac_f32_16x16x64_bf16 v[204:207], v[216:219], v[144:151], v249 abid:1\n" \
  "s_nop 0\n" \
  "v_cvt_pk_bf16_f32 v192, v192, v193\n" \
  "v_smfmac_f32_16x16x64_bf16 v[204:207], v[220:223], v[152:159], v249 abid:1\n" \
  "v_cvt_pk_bf16_f32 v193, v194, v195\n" \
  "s_waitcnt lgkmcnt(2)\n" \
  "v_smfmac_f32_16x16x64_bf16 v[200:203], v[216:219], v[240:247], v249 abid:1\n" \
  "v_cvt_pk_bf16_f32 v194, v196, v197\n" \
  "s_waitcnt lgkmcnt(0)\n" \
  "v_smfmac_f32_16x16x64_bf16 v[200:203], v[220:223], v[224:231], v249 abid:1\n" \
  "v_cvt_pk_bf16_f32 v195, v198, v199\n" \
  "v_cvt_pk_bf16_f32 v204, v204, v205\n" \
  "v_permlane16_swap_b32 v192, v194\n" \
  "v_permlane16_swap_b32 v193, v195\n" \
  "buffer_store_dwordx4 v[192:195], v236, %[rs], %[vs] offen sc1\n" \
  "v_cvt_pk_bf16_f32 v205, v206, v207\n" \
  "s_nop 2\n" \
  "v_cvt_pk_bf16_f32 v203, v202, v203\n" \
  "v_cvt_pk_bf16_f32 v202, v200, v201\n" \
  "s_nop 0\n" \
  "v_permlane16_swap_b32 v203, v205\n" \
  "v_permlane16_swap_b32 v202, v204\n" \
  "buffer_store_dwordx4 v[202:205], v237, %[rs], %[vs] offen sc1\n" \
  "s_setprio 0\n" \
  "s_nop 1\n"
#define VO_B \
  "s_branch .Lgo_%=\n" \
  ".Lr_%=:\n" \
  "s_sub_u32 %[rt], %[rt], 1\n" \
  "s_cbranch_scc1 .Lrb_%=\n" \
  "s_sleep 2\n" \
  "buffer_load_dwordx4 v[192:195], v248, %[rs], %[o0] offen sc1\n" \
  "buffer_load_dwordx4 v[196:199], v248, %[rs], %[o0] offen offset:1024 sc1\n" \
  "v_mov_b64 v[224:225], 0\n" \
  "v_mov_b64 v[226:227], 0\n" \
  "v_mov_b64 v[228:229], 0\n" \
  "v_mov_b64 v[230:231], 0\n" \
  "s_nop 1\n" \
  "s_branch .Lw_%=\n" \
  ".Lgo_%=:\n" \
  "v_add_u32 v252, 0x20000, v248\n" \
  "ds_read_b128 v[200:203], v248 offset:32768\n" \
  "ds_read_b128 v[204:207], v248 offset:33792\n" \
  "ds_read_b128 v[208:211], v248 offset:49152\n" \
  "ds_read_b128 v[212:215], v248 offset:50176\n" \
  "ds_read_b128 v[216:219], v252 offset:0\n" \
  "ds_read_b128 v[220:223], v252 offset:1024\n" \
  "ds_read_b128 v[240:243], v252 offset:16384\n" \
  "ds_read_b128 v[244:247], v252 offset:17408\n" \
  "v_mov_b64 v[224:225], 0\n" \
  "v_mov_b64 v[226:227], 0\n" \
  "v_mov_b64 v[228:229], 0\n" \
  "v_mov_b64 v[230:231], 0\n" \
  "v_xor_b32 v250, 0x800, v248\n" \
  "v_xor_b32 v251, 0x1000, v248\n" \
  "s_mov_b32 %[rt], 0x40000\n" \
  "s_setprio 1\n" \
  "s_waitcnt lgkmcnt(0)\n" \
  "s_cmp_eq_u32 %[tw], 0\n" \
  "s_cbranch_scc1 .Ltq_%=\n" \
  ".Ltl_%=:\n" \
  "s_memtime vcc\n" \
  "s_waitcnt lgkmcnt(0)\n" \
  "s_sub_i32 %[cnt], %[tg], vcc_lo\n" \
  "s_cmp_lt_i32 %[cnt], 260\n" \
  "s_cbranch_scc1 .Ltf_%=\n" \
  "s_sleep 1\n" \
  "s_branch .Ltl_%=\n" \
  ".Ltf_%=:\n" \
  "s_max_i32 %[cnt], %[cnt], 0\n" \
  ".Ltg_%=:\n" \
  "s_sub_u32 %[cnt], %[cnt], 28\n" \
  "s_cbranch_scc0 .Ltg_%=\n" \
  ".Ltq_%=:\n" \
  "s_barrier\n" \
  "buffer_load_dwordx4 v[192:195], v248, %[rs], %[o0] offen sc1\n" \
  "buffer_load_dwordx4 v[196:199], v248, %[rs], %[o0] offen offset:1024 sc1\n" \
  ".Lw_%=:\n" \
  "s_waitcnt vmcnt(1)\n" \
  "ds_write_b128 v248, v[192:195]\n" \
  "v_smfmac_f32_16x16x64_bf16 v[224:227], v[192:195], v[200:207], v249\n" \
  "v_max3_u32 v252, v192, v193, v194\n" \
  "s_waitcnt vmcnt(0)\n" \
  "ds_write_b128 v248, v[196:199] offset:1024\n" \
  "v_smfmac_f32_16x16x64_bf16 v[228:231], v[192:195], v[208:215], v249\n" \
  "v_max3_u32 v252, v252, v195, v196\n" \
  "v_smfmac_f32_16x16x64_bf16 v[224:227], v[196:199], v[216:223], v249\n" \
  "v_max3_u32 v252, v252, v197, v198\n" \
  "v_max_u32 v252, v252, v199\n" \
  "v_smfmac_f32_16x16x64_bf16 v[228:231], v[196:199], v[240:247], v249\n" \
  "v_cmp_eq_u32 vcc, -1, v252\n" \
  "v_xor_b32 v252, 0x1800, v248\n" \
  "s_cbranch_vccnz .Lr_%=\n" \
  ".Lrb_%=:\n" \
  "s_waitcnt lgkmcnt(0)\n" \
  "s_barrier\n" \
  "s_memtime vcc\n" \
  "s_setprio 3\n" \
  "v_mov_b64 v[192:193], 0\n" \
  "v_mov_b64 v[194:195], 0\n" \
  "v_mov_b64 v[196:197], 0\n" \
  "v_mov_b64 v[198:199], 0\n" \
  "s_waitcnt lgkmcnt(0)\n" \
  "s_sub_u32 %[cnt], vcc_lo, %[ckm]\n" \
  "s_mul_hi_u32 %[cnt], %[cnt], %[inv]\n" \
  "s_add_u32 %[cnt], %[cnt], %[r16]\n" \
  "s_and_b32 %[cnt], %[cnt], -2\n" \
  "v_mov_b32 v233, %[cnt]\n" \
  "buffer_store_dword v233, v239, %[rs], %[sst] offen sc1\n" \
  "s_nop 5\n" \
  "ds_read_b128 v[200:203], v250 offset:0\n" \
  "ds_read_b128 v[204:207], v250 offset:1024\n" \
  "ds_read_b128 v[208:211], v251 offset:0\n" \
  "ds_read_b128 v[212:215], v251 offset:1024\n" \
  "ds_read_b128 v[216:219], v252 offset:0\n" \
  "ds_read_b128 v[220:223], v252 offset:1024\n" \
  "s_setprio 1\n" \
  "s_waitcnt lgkmcnt(5)\n" \
  "v_smfmac_f32_16x16x64_bf16 v[224:227], v[200:203], v[0:7], v249\n" \
  "v_smfmac_f32_16x16x64_bf16 v[228:231], v[200:203], v[48:55], v249\n" \
  "s_waitcnt lgkmcnt(4)\n" \
  "v_smfmac_f32_16x16x64_bf16 v[224:227], v[204:207], v[8:15], v249\n" \
  "v_smfmac_f32_16x16x64_bf16 v[228:231], v[204:207], v[56:63], v249\n" \
  "s_waitcnt lgkmcnt(3)\n" \
  "v_smfmac_f32_16x16x64_bf16 v[224:227], v[208:211], v[16:23], v249\n" \
  "ds_read_b128 v[240:243], v254 offset:0\n" \
  "v_smfmac_f32_16x16x64_bf16 v[228:231], v[208:211], v[64:71], v249\n" \
  "ds_read_b128 v[244:247], v254 offset:1024\n" \
  "s_waitcnt lgkmcnt(4)\n" \
  "v_smfmac_f32_16x16x64_bf16 v[224:227], v[212:215], v[24:31], v249\n" \
  "s_waitcnt lgkmcnt(3)\n" \
  "v_smfmac_f32_16x16x64_bf16 v[224:227], v[216:219], v[32:39], v249\n" \
  "s_waitcnt lgkmcnt(2)\n" \
  "v_smfmac_f32_16x16x64_bf16 v[224:227], v[220:223], v[40:47], v249\n" \
  "v_smfmac_f32_16x16x64_bf16 v[228:231], v[212:215], v[72:79], v249\n" \
  "s_barrier\n" \
  "ds_read_b128 v[232:235], v248 offset:24576\n" \
  "v_smfmac_f32_16x16x64_bf16 v[228:231], v[216:219], v[80:87], v249\n" \
  "s_nop 0\n" \
  "v_add_f32 v224, v224, v226\n" \
  "v_smfmac_f32_16x16x64_bf16 v[228:231], v[220:223], v[88:95], v249\n" \
  "v_add_f32 v225, v225, v227\n" \
  "s_waitcnt lgkmcnt(0)\n" \
  "v_add_f32 v232, v232, v224\n" \
  "v_add_f32 v233, v233, v225\n" \
  "v_cvt_pk_bf16_f32 v232, v232, v233\n" \
  "ds_write_b32 v253, v232\n" \
  "s_nop 2\n" \
  "v_add_f32 v234, v234, v228\n" \
  "v_add_f32 v235, v235, v229\n" \
  "v_add_f32 v234, v234, v230\n" \
  "v_add_f32 v235, v235, v231\n" \
  "v_cvt_pk_bf16_f32 v234, v234, v235\n" \
  "ds_write_b32 v253, v234 offset:1536\n" \
  "v_mov_b64 v[200:201], 0\n" \
  "v_mov_b64 v[202:203], 0\n" \
  "v_mov_b64 v[204:205], 0\n" \
  "v_mov_b64 v[206:207], 0\n" \
  "s_waitcnt lgkmcnt(0)\n" \
  "s_barrier\n" \
  "s_nop 7\n" \
  "ds_read_b64_tr_b16 v[208:209], v238 offset:16384\n" \
  "ds_read_b64_tr_b16 v[210:211], v238 offset:16576\n" \
  "ds_read_b128 v[224:227], v254 offset:16384\n" \
  "ds_read_b128 v[228:231], v254 offset:17408\n" \
  "ds_read_b64_tr_b16 v[212:213], v238 offset:17152\n" \
  "ds_read_b64_tr_b16 v[214:215], v238 offset:17344\n" \
  "ds_read_b64_tr_b16 v[216:217], v238 offset:17920\n" \
  "ds_read_b64_tr_b16 v[218:219], v238 offset:18112\n" \
  "ds_read_b64_tr_b16 v[220:221], v238 offset:18688\n" \
  "ds_read_b64_tr_b16 v[222:223], v238 offset:18880\n" \
  "s_waitcnt lgkmcnt(8)\n" \
  "v_smfmac_f32_16x16x64_bf16 v[200:203], v[208:211], v[240:247], v249 abid:1\n" \
  "ds_read_b128 v[240:243], v254 offset:32768\n" \
  "v_smfmac_f32_16x16x64_bf16 v[192:195], v[208:211], v[160:167], v249 abid:1\n" \
  "ds_read_b128 v[244:247], v254 offset:33792\n" \
  "v_smfmac_f32_16x16x64_bf16 v[196:199], v[208:211], v[96:103], v249 abid:1\n" \
  "v_smfmac_f32_16x16x64_bf16 v[204:207], v[208:211], v[128:135], v249 abid:1\n" \
  "s_waitcnt lgkmcnt(6)\n" \
  "v_smfmac_f32_16x16x64_bf16 v[192:195], v[212:215], v[168:175], v249 abid:1\n" \
  "v_smfmac_f32_16x16x64_bf16 v[200:203], v[212:215], v[224:231], v249 abid:1\n" \
  "ds_read_b128 v[224:227], v254 offset:49152\n" \
  "v_smfmac_f32_16x16x64_bf16 v[196:199], v[212:215], v[104:111], v249 abid:1\n" \
  "ds_read_b128 v[228:231], v254 offset:50176\n" \
  "v_smfmac_f32_16x16x64_bf16 v[204:207], v[212:215], v[136:143], v249 abid:1\n" \
  "s_waitcnt lgkmcnt(6)\n" \
  "v_smfmac_f32_16x16x64_bf16 v[192:195], v[216:219], v[176:183], v249 abid:1\n" \
  "v_smfmac_f32_16x16x64_bf16 v[196:199], v[216:219], v[112:119], v249 abid:1\n" \
  "s_waitcnt lgkmcnt(4)\n" \
  "v_smfmac_f32_16x16x64_bf16 v[192:195], v[220:223], v[184:191], v249 abid:1\n" \
  "v_smfmac_f32_16x16x64_bf16 v[196:199], v[220:223], v[120:127], v249 abid:1\n" \
  "v_smfmac_f32_16x16x64_bf16 v[204:207], v[216:219], v[144:151], v249 abid:1\n" \
  "s_nop 0\n" \
  "v_cvt_pk_bf16_f32 v192, v192, v193\n" \
  "v_smfmac_f32_16x16x64_bf16 v[204:207], v[220:223], v[152:159], v249 abid:1\n" \
  "v_cvt_pk_bf16_f32 v193, v194, v195\n" \
  "s_waitcnt lgkmcnt(2)\n" \
  "v_smfmac_f32_16x16x64_bf16 v[200:203], v[216:219], v[240:247], v249 abid:1\n" \
  "v_cvt_pk_bf16_f32 v194, v196, v197\n" \
  "s_waitcnt lgkmcnt(0)\n" \
  "v_smfmac_f32_16x16x64_bf16 v[200:203], v[220:223], v[224:231], v249 abid:1\n" \
  "v_cvt_pk_bf16_f32 v195, v198, v199\n" \
  "v_cvt_pk_bf16_f32 v204, v204, v205\n" \
  "v_permlane16_swap_b32 v192, v194\n" \
  "v_permlane16_swap_b32 v193, v195\n" \
  "buffer_store_dwordx4 v[192:195], v236, %[rs], %[vs] offen sc1\n" \
  "v_cvt_pk_bf16_f32 v205, v206, v207\n" \
  "s_nop 2\n" \
  "v_cvt_pk_bf16_f32 v203, v202, v203\n" \
  "v_cvt_pk_bf16_f32 v202, v200, v201\n" \
  "s_nop 0\n" \
  "v_permlane16_swap_b32 v203, v205\n" \
  "v_permlane16_swap_b32 v202, v204\n" \
  "buffer_store_dwordx4 v[202:205], v237, %[rs], %[vs] offen sc1\n" \
  "s_setprio 0\n" \
  "s_nop 1\n"
#define QK_A0 \
  "s_branch .Lgo_%=\n" \
  ".Lr_%=:\n" \
  "s_sub_u32 %[rt], %[rt], 1\n" \
  "s_cbranch_scc1 .Lrb_%=\n" \
  "s_sleep 2\n" \
  "buffer_load_dwordx4 v[192:195], v248, %[rs], %[o0] offen sc1\n" \
  "buffer_load_dwordx4 v[196:199], v248, %[rs], %[o0] offen offset:1024 sc1\n" \
  "v_mov_b64 v[224:225], 0\n" \
  "v_mov_b64 v[226:227], 0\n" \
  "v_mov_b64 v[228:229], 0\n" \
  "v_mov_b64 v[230:231], 0\n" \
  "v_mov_b64 v[232:233], 0\n" \
  "v_mov_b64 v[234:235], 0\n" \
  "v_mov_b64 v[236:237], 0\n" \
  "v_mov_b64 v[238:239], 0\n" \
  "s_nop 1\n" \
  "s_branch .Lw_%=\n" \
  ".Lw1_%=:\n" \
  "v_max_f32 v200, v192, v193\n" \
  "s_nop 1\n" \
  "v_max_f32_dpp v200, v200, v200 row_ror:8 row_mask:0xf bank_mask:0xf\n" \
  "s_nop 1\n" \
  "v_max_f32_dpp v200, v200, v200 row_ror:4 row_mask:0xf bank_mask:0xf\n" \
  "s_nop 1\n" \
  "v_max_f32_dpp v200, v200, v200 row_ror:2 row_mask:0xf bank_mask:0xf\n" \
  "s_nop 1\n" \
  "v_max_f32_dpp v200, v200, v200 row_ror:1 row_mask:0xf bank_mask:0xf\n" \
  "v_sub_f32 v194, v192, v200\n" \
  "v_sub_f32 v195, v193, v200\n" \
  "v_exp_f32 v194, v194\n" \
  "v_exp_f32 v195, v195\n" \
  "s_mov_b32 %[rt], -1.0\n" \
  "s_nop 0\n" \
  "v_cvt_pk_bf16_f32 v196, v194, v195\n" \
  "v_and_b32 v196, 0x7fffffff, v196\n" \
  "s_branch .Lw1b_%=\n" \
  ".Lw2_%=:\n" \
  "s_cmp_lt_i32 %[rt], 0\n" \
  "s_cbranch_scc1 .Lw3_%=\n" \
  "v_mov_b32 v200, 0\n" \
  ".Lw3_%=:\n" \
  "s_nop 1\n" \
  "v_max_f32_dpp v197, v192, v200 quad_perm:[0,0,0,0] row_mask:0xf bank_mask:0xf\n" \
  "s_nop 1\n" \
  "v_max_f32_dpp v197, v192, v197 quad_perm:[1,1,1,1] row_mask:0xf bank_mask:0xf\n" \
  "s_nop 1\n" \
  "v_max_f32_dpp v197, v197, v197 row_ror:4 row_mask:0xf bank_mask:0xf\n" \
  "s_nop 1\n" \
  "v_max_f32_dpp v197, v197, v197 row_ror:8 row_mask:0xf bank_mask:0xf\n" \
  "v_sub_f32 v194, v192, v197\n" \
  "v_sub_f32 v201, v200, v197\n" \
  "v_exp_f32 v194, v194\n" \
  "v_exp_f32 v201, v201\n" \
  "s_nop 1\n" \
  "v_mul_f32 v199, v201, v199\n" \
  "s_nop 1\n" \
  "v_add_f32_dpp v198, v194, v199 quad_perm:[0,0,0,0] row_mask:0xf bank_mask:0xf\n" \
  "s_nop 1\n" \
  "v_mov_b32_dpp v193, v194 quad_perm:[1,1,1,1] row_mask:0xf bank_mask:0xf\n" \
  "s_nop 1\n" \
  "v_add_f32 v198, v198, v193\n" \
  "s_nop 1\n" \
  "v_mov_b32_dpp v195, v194 quad_perm:[0,0,0,0] row_mask:0xf bank_mask:0xf\n" \
  "s_nop 1\n" \
  "v_add_f32_dpp v198, v198, v198 row_ror:4 row_mask:0xf bank_mask:0xf\n" \
  "s_nop 1\n" \
  "v_add_f32_dpp v198, v198, v198 row_ror:8 row_mask:0xf bank_mask:0xf\n" \
  "s_nop 1\n" \
  "v_rcp_f32 v198, v198\n" \
  "s_nop 1\n" \
  "v_mul_f32 v195, v195, v198\n" \
  "s_nop 1\n" \
  "v_mul_f32 v193, v193, v198\n" \
  "s_nop 1\n" \
  "v_cvt_pk_bf16_f32 v196, v195, v193\n" \
  "s_nop 1\n" \
  "v_and_b32 v196, 0x7fffffff, v196\n" \
  "s_nop 1\n" \
  "v_mul_f32 v198, v201, v198\n" \
  "s_nop 1\n" \
  "buffer_store_dword v196, v251, %[rs], %[so] offen sc1\n" \
  "s_nop 1\n" \
  "v_cvt_pk_bf16_f32 v197, v198, v198\n" \
  "s_nop 1\n" \
  "v_and_b32 v197, v252, v197\n" \
  "s_nop 1\n" \
  "buffer_store_dword v197, v251, %[rs], %[so] offen offset:64 sc1\n" \
  "s_branch .Lw2b_%=\n" \
  ".Lgo_%=:\n" \
  "v_add_u32 v252, 0x20000, v248\n" \
  "ds_read_b128 v[200:203], v248 offset:32768\n" \
  "ds_read_b128 v[204:207], v248 offset:33792\n" \
  "ds_read_b128 v[208:211], v248 offset:49152\n" \
  "ds_read_b128 v[212:215], v248 offset:50176\n" \
  "ds_read_b128 v[216:219], v252 offset:0\n" \
  "ds_read_b128 v[220:223], v252 offset:1024\n" \
  "ds_read_b128 v[240:243], v252 offset:16384\n" \
  "ds_read_b128 v[244:247], v252 offset:17408\n" \
  "v_mov_b64 v[224:225], 0\n" \
  "v_mov_b64 v[226:227], 0\n" \
  "v_mov_b64 v[228:229], 0\n" \
  "v_mov_b64 v[230:231], 0\n" \
  "v_mov_b64 v[232:233], 0\n" \
  "v_mov_b64 v[234:235], 0\n" \
  "v_mov_b64 v[236:237], 0\n" \
  "v_mov_b64 v[238:239], 0\n" \
  "v_xor_b32 v250, 0x800, v248\n" \
  "v_xor_b32 v251, 0x1000, v248\n" \
  "s_mov_b32 %[rt], 0x40000\n" \
  "s_setprio 2\n" \
  "s_waitcnt lgkmcnt(0)\n" \
  "s_barrier\n" \
  "buffer_load_dwordx4 v[192:195], v248, %[rs], %[o0] offen sc1\n" \
  "buffer_load_dwordx4 v[196:199], v248, %[rs], %[o0] offen offset:1024 sc1\n" \
  ".Lw_%=:\n" \
  "s_waitcnt vmcnt(1)\n" \
  "ds_write_b128 v248, v[192:195]\n" \
  "v_smfmac_f32_16x16x64_bf16 v[224:227], v[192:195], v[200:207], v249\n" \
  "v_max3_u32 v252, v192, v193, v194\n" \
  "s_waitcnt vmcnt(0)\n" \
  "ds_write_b128 v248, v[196:199] offset:1024\n" \
  "v_smfmac_f32_16x16x64_bf16 v[228:231], v[192:195], v[208:215], v249\n" \
  "v_max3_u32 v252, v252, v195, v196\n" \
  "v_smfmac_f32_16x16x64_bf16 v[224:227], v[196:199], v[216:223], v249\n" \
  "v_max3_u32 v252, v252, v197, v198\n" \
  "v_smfmac_f32_16x16x64_bf16 v[228:231], v[196:199], v[240:247], v249\n" \
  "v_max_u32 v252, v252, v199\n" \
  "v_smfmac_f32_16x16x64_bf16 v[232:235], v[192:195], v[96:103], v249\n" \
  "v_cmp_eq_u32 vcc, -1, v252\n" \
  "v_smfmac_f32_16x16x64_bf16 v[236:239], v[192:195], v[144:151], v249\n" \
  "v_xor_b32 v252, 0x1800, v248\n" \
  "s_cbranch_vccnz .Lr_%=\n" \
  ".Lrb_%=:\n" \
  "v_smfmac_f32_16x16x64_bf16 v[232:235], v[196:199], v[104:111], v249\n" \
  "v_smfmac_f32_16x16x64_bf16 v[236:239], v[196:199], v[152:159], v249\n" \
  "s_waitcnt lgkmcnt(0)\n" \
  "s_barrier\n" \
  "ds_read_b128 v[200:203], v250 offset:0\n" \
  "ds_read_b128 v[204:207], v250 offset:1024\n" \
  "ds_read_b128 v[208:211], v251 offset:0\n" \
  "ds_read_b128 v[212:215], v251 offset:1024\n" \
  "ds_read_b128 v[216:219], v252 offset:0\n" \
  "ds_read_b128 v[220:223], v252 offset:1024\n" \
  "s_waitcnt lgkmcnt(5)\n" \
  "v_smfmac_f32_16x16x64_bf16 v[224:227], v[200:203], v[0:7], v249\n" \
  "v_smfmac_f32_16x16x64_bf16 v[228:231], v[200:203], v[48:55], v249\n" \
  "s_waitcnt lgkmcnt(4)\n" \
  "v_smfmac_f32_16x16x64_bf16 v[224:227], v[204:207], v[8:15], v249\n" \
  "v_smfmac_f32_16x16x64_bf16 v[228:231], v[204:207], v[56:63], v249\n" \
  "s_waitcnt lgkmcnt(3)\n" \
  "v_smfmac_f32_16x16x64_bf16 v[224:227], v[208:211], v[16:23], v249\n" \
  "v_smfmac_f32_16x16x64_bf16 v[228:231], v[208:211], v[64:71], v249\n" \
  "s_waitcnt lgkmcnt(2)\n" \
  "v_smfmac_f32_16x16x64_bf16 v[224:227], v[212:215], v[24:31], v249\n" \
  "s_waitcnt lgkmcnt(1)\n" \
  "v_smfmac_f32_16x16x64_bf16 v[224:227], v[216:219], v[32:39], v249\n" \
  "s_waitcnt lgkmcnt(0)\n" \
  "v_smfmac_f32_16x16x64_bf16 v[224:227], v[220:223], v[40:47], v249\n" \
  "v_smfmac_f32_16x16x64_bf16 v[228:231], v[212:215], v[72:79], v249\n" \
  "v_smfmac_f32_16x16x64_bf16 v[228:231], v[216:219], v[80:87], v249\n" \
  "s_nop 0\n" \
  "v_add_f32 v224, v224, v226\n" \
  "v_smfmac_f32_16x16x64_bf16 v[228:231], v[220:223], v[88:95], v249\n" \
  "v_add_f32 v225, v225, v227\n" \
  "ds_write_b64 v248, v[224:225] offset:8192\n" \
  "s_nop 6\n" \
  "v_add_f32 v228, v228, v230\n" \
  "v_add_f32 v229, v229, v231\n" \
  "ds_write_b64 v248, v[228:229] offset:8200\n" \
  "s_setprio 0\n" \
  "ds_read_b128 v[240:243], v254 offset:0\n" \
  "ds_read_b128 v[244:247], v254 offset:1024\n" \
  "s_waitcnt lgkmcnt(2)\n" \
  "s_barrier\n" \
  "s_nop 1\n" \
  "ds_read_b128 v[192:195], v254 offset:16384\n" \
  "ds_read_b128 v[196:199], v254 offset:17408\n" \
  "ds_read_b128 v[224:227], v254 offset:32768\n" \
  "ds_read_b128 v[228:231], v254 offset:33792\n" \
  "s_waitcnt lgkmcnt(4)\n" \
  "v_smfmac_f32_16x16x64_bf16 v[232:235], v[200:203], v[240:247], v249\n" \
  "ds_read_b128 v[240:243], v254 offset:49152\n" \
  "ds_read_b128 v[244:247], v254 offset:50176\n" \
  "s_waitcnt lgkmcnt(4)\n" \
  "v_smfmac_f32_16x16x64_bf16 v[236:239], v[200:203], v[192:199], v249\n" \
  "v_smfmac_f32_16x16x64_bf16 v[232:235], v[204:207], v[112:119], v249\n" \
  "ds_read_b128 v[192:195], v248 offset:16384\n" \
  "v_smfmac_f32_16x16x64_bf16 v[236:239], v[204:207], v[160:167], v249\n" \
  "ds_read_b128 v[196:199], v248 offset:17408\n" \
  "v_smfmac_f32_16x16x64_bf16 v[232:235], v[212:215], v[120:127], v249\n" \
  "ds_read_b128 v[200:203], v248 offset:20480\n" \
  "v_smfmac_f32_16x16x64_bf16 v[236:239], v[212:215], v[168:175], v249\n" \
  "ds_read_b128 v[204:207], v248 offset:21504\n" \
  "s_waitcnt lgkmcnt(6)\n" \
  "v_smfmac_f32_16x16x64_bf16 v[232:235], v[208:211], v[224:231], v249\n" \
  "ds_read_b128 v[212:215], v248 offset:23552\n" \
  "s_waitcnt lgkmcnt(5)\n" \
  "v_smfmac_f32_16x16x64_bf16 v[236:239], v[208:211], v[240:247], v249\n" \
  "ds_read_b128 v[240:243], v248 offset:18432\n" \
  "v_smfmac_f32_16x16x64_bf16 v[232:235], v[216:219], v[128:135], v249\n" \
  "ds_read_b128 v[244:247], v248 offset:19456\n" \
  "v_smfmac_f32_16x16x64_bf16 v[232:235], v[220:223], v[136:143], v249\n" \
  "ds_read_b128 v[208:211], v248 offset:22528\n" \
  "v_smfmac_f32_16x16x64_bf16 v[236:239], v[216:219], v[176:183], v249\n" \
  "ds_read_b96 v[250:252], v248 offset:13312\n" \
  "v_smfmac_f32_16x16x64_bf16 v[236:239], v[220:223], v[184:191], v249\n" \
  "s_setprio 3\n" \
  "s_barrier\n" \
  "ds_read_b64_tr_b16 v[224:225], v253 offset:17408\n" \
  "ds_read_b64_tr_b16 v[226:227], v253 offset:17664\n" \
  "ds_read_b64_tr_b16 v[228:229], v253 offset:19456\n" \
  "ds_read_b64_tr_b16 v[230:231], v253 offset:19712\n" \
  "v_mov_b64 v[216:217], 0\n" \
  "v_mov_b64 v[218:219], 0\n" \
  "v_add_f32 v232, v232, v234\n" \
  "v_add_f32 v233, v233, v235\n" \
  "ds_write_b64 v248, v[232:233] offset:0\n" \
  "v_mov_b64 v[220:221], 0\n" \
  "v_mov_b64 v[222:223], 0\n" \
  "v_add_f32 v236, v236, v238\n" \
  "v_add_f32 v237, v237, v239\n" \
  "ds_write_b64 v248, v[236:237] offset:8\n" \
  "s_waitcnt lgkmcnt(4)\n" \
  "v_smfmac_f32_16x16x64_bf16 v[216:219], v[224:227], v[192:199], v249\n" \
  "v_smfmac_f32_16x16x64_bf16 v[220:223], v[224:227], v[200:207], v249\n" \
  "s_waitcnt lgkmcnt(2)\n" \
  "v_smfmac_f32_16x16x64_bf16 v[216:219], v[228:231], v[240:247], v249\n" \
  "v_smfmac_f32_16x16x64_bf16 v[220:223], v[228:231], v[208:215], v249\n" \
  "s_mov_b32 %[rt], 0x422c0000\n" \
  "s_waitcnt lgkmcnt(0)\n" \
  "s_barrier\n" \
  "s_nop 1\n" \
  "v_add_f32 v192, v216, v218\n" \
  "s_nop 2\n" \
  "v_add_f32 v193, v220, v222\n" \
  "v_exp_f32 v194, v192\n" \
  "v_exp_f32 v195, v193\n" \
  "v_max_f32 v197, |v192|, |v193|\n" \
  "v_cvt_pk_bf16_f32 v196, v194, v195\n" \
  "v_cmp_nge_f32 vcc, %[rt], v197\n" \
  "s_cbranch_vccnz .Lw1_%=\n" \
  ".Lw1b_%=:\n" \
  "buffer_store_dword v196, v250, %[rs], %[c0] offen sc1\n" \
  "buffer_store_dword v196, v250, %[rs], %[c1] offen sc1\n" \
  "buffer_store_dword v196, v250, %[rs], %[c2] offen sc1\n" \
  "buffer_store_dword v196, v250, %[rs], %[c3] offen sc1\n" \
  "v_add_f32 v199, v194, v195\n" \
  "v_xor_b32 v251, %[xr], v251\n" \
  "s_nop 0\n" \
  "v_add_f32_dpp v199, v199, v199 quad_perm:[1,0,3,2] row_mask:0xf bank_mask:0xf\n" \
  "s_nop 1\n" \
  "v_add_f32_dpp v199, v199, v199 quad_perm:[2,3,0,1] row_mask:0xf bank_mask:0xf\n" \
  "s_barrier\n" \
  "ds_read_b64_tr_b16 v[232:233], v253 offset:9216\n" \
  "ds_read_b64_tr_b16 v[234:235], v253 offset:9472\n" \
  "ds_read_b64_tr_b16 v[236:237], v253 offset:11264\n" \
  "ds_read_b64_tr_b16 v[238:239], v253 offset:11520\n" \
  "s_waitcnt lgkmcnt(2)\n" \
  "v_mfma_f32_16x16x32_bf16 v[216:219], v[224:227], v[232:235], 0\n" \
  "s_waitcnt lgkmcnt(0)\n" \
  "v_mfma_f32_16x16x32_bf16 v[216:219], v[228:231], v[236:239], v[216:219]\n" \
  "s_nop 8\n" \
  "v_add_f32_dpp v192, v218, v216 quad_perm:[2,3,2,3] row_mask:0xf bank_mask:0xf\n" \
  "v_exp_f32 v194, v192\n" \
  "v_cmp_nge_f32 vcc, %[rt], |v192|\n" \
  "s_and_b64 vcc, vcc, %[lo]\n" \
  "v_add_f32_dpp v198, v194, v199 quad_perm:[0,0,0,0] row_mask:0xf bank_mask:0xf\n" \
  "v_mov_b32_dpp v193, v194 quad_perm:[1,1,1,1] row_mask:0xf bank_mask:0xf\n" \
  "v_add_f32 v198, v198, v193\n" \
  "v_mov_b32_dpp v195, v194 quad_perm:[0,0,0,0] row_mask:0xf bank_mask:0xf\n" \
  "s_cbranch_scc1 .Lw2_%=\n" \
  "v_add_f32_dpp v198, v198, v198 row_ror:4 row_mask:0xf bank_mask:0xf\n" \
  "s_nop 1\n" \
  "v_add_f32_dpp v198, v198, v198 row_ror:8 row_mask:0xf bank_mask:0xf\n" \
  "v_rcp_f32 v198, v198\n" \
  "s_nop 0\n" \
  "v_mul_f32 v195, v195, v198\n" \
  "v_mul_f32 v193, v193, v198\n" \
  "v_cvt_pk_bf16_f32 v196, v195, v193\n" \
  "buffer_store_dword v196, v251, %[rs], %[so] offen sc1\n" \
  "v_cvt_pk_bf16_f32 v197, v198, v198\n" \
  "v_and_b32 v197, v252, v197\n" \
  "buffer_store_dword v197, v251, %[rs], %[so] offen offset:64 sc1\n" \
  ".Lw2b_%=:\n" \
  "s_setprio 0\n"
#define QK_A1 \
  "s_branch .Lgo_%=\n" \
  ".Lr_%=:\n" \
  "s_sub_u32 %[rt], %[rt], 1\n" \
  "s_cbranch_scc1 .Lrb_%=\n" \
  "s_sleep 2\n" \
  "buffer_load_dwordx4 v[192:195], v248, %[rs], %[o0] offen sc1\n" \
  "buffer_load_dwordx4 v[196:199], v248, %[rs], %[o0] offen offset:1024 sc1\n" \
  "v_mov_b64 v[224:225], 0\n" \
  "v_mov_b64 v[226:227], 0\n" \
  "v_mov_b64 v[228:229], 0\n" \
  "v_mov_b64 v[230:231], 0\n" \
  "v_mov_b64 v[232:233], 0\n" \
  "v_mov_b64 v[234:235], 0\n" \
  "v_mov_b64 v[236:237], 0\n" \
  "v_mov_b64 v[238:239], 0\n" \
  "s_nop 1\n" \
  "s_branch .Lw_%=\n" \
  ".Lw1_%=:\n" \
  "v_max_f32 v200, v192, v193\n" \
  "s_nop 1\n" \
  "v_max_f32_dpp v200, v200, v200 row_ror:8 row_mask:0xf bank_mask:0xf\n" \
  "s_nop 1\n" \
  "v_max_f32_dpp v200, v200, v200 row_ror:4 row_mask:0xf bank_mask:0xf\n" \
  "s_nop 1\n" \
  "v_max_f32_dpp v200, v200, v200 row_ror:2 row_mask:0xf bank_mask:0xf\n" \
  "s_nop 1\n" \
  "v_max_f32_dpp v200, v200, v200 row_ror:1 row_mask:0xf bank_mask:0xf\n" \
  "v_sub_f32 v194, v192, v200\n" \
  "v_sub_f32 v195, v193, v200\n" \
  "v_exp_f32 v194, v194\n" \
  "v_exp_f32 v195, v195\n" \
  "s_mov_b32 %[rt], -1.0\n" \
  "s_nop 0\n" \
  "v_cvt_pk_bf16_f32 v196, v194, v195\n" \
  "v_and_b32 v196, 0x7fffffff, v196\n" \
  "s_branch .Lw1b_%=\n" \
  ".Lw2_%=:\n" \
  "s_cmp_lt_i32 %[rt], 0\n" \
  "s_cbranch_scc1 .Lw3_%=\n" \
  "v_mov_b32 v200, 0\n" \
  ".Lw3_%=:\n" \
  "s_nop 1\n" \
  "v_max_f32_dpp v197, v192, v200 quad_perm:[0,0,0,0] row_mask:0xf bank_mask:0xf\n" \
  "s_nop 1\n" \
  "v_max_f32_dpp v197, v192, v197 quad_perm:[1,1,1,1] row_mask:0xf bank_mask:0xf\n" \
  "s_nop 1\n" \
  "v_max_f32_dpp v197, v197, v197 row_ror:4 row_mask:0xf bank_mask:0xf\n" \
  "s_nop 1\n" \
  "v_max_f32_dpp v197, v197, v197 row_ror:8 row_mask:0xf bank_mask:0xf\n" \
  "v_sub_f32 v194, v192, v197\n" \
  "v_sub_f32 v201, v200, v197\n" \
  "v_exp_f32 v194, v194\n" \
  "v_exp_f32 v201, v201\n" \
  "s_nop 1\n" \
  "v_mul_f32 v199, v201, v199\n" \
  "s_nop 1\n" \
  "v_add_f32_dpp v198, v194, v199 quad_perm:[0,0,0,0] row_mask:0xf bank_mask:0xf\n" \
  "s_nop 1\n" \
  "v_mov_b32_dpp v193, v194 quad_perm:[1,1,1,1] row_mask:0xf bank_mask:0xf\n" \
  "s_nop 1\n" \
  "v_add_f32 v198, v198, v193\n" \
  "s_nop 1\n" \
  "v_mov_b32_dpp v195, v194 quad_perm:[0,0,0,0] row_mask:0xf bank_mask:0xf\n" \
  "s_nop 1\n" \
  "v_add_f32_dpp v198, v198, v198 row_ror:4 row_mask:0xf bank_mask:0xf\n" \
  "s_nop 1\n" \
  "v_add_f32_dpp v198, v198, v198 row_ror:8 row_mask:0xf bank_mask:0xf\n" \
  "s_nop 1\n" \
  "v_rcp_f32 v198, v198\n" \
  "s_nop 1\n" \
  "v_mul_f32 v195, v195, v198\n" \
  "s_nop 1\n" \
  "v_mul_f32 v193, v193, v198\n" \
  "s_nop 1\n" \
  "v_cvt_pk_bf16_f32 v196, v195, v193\n" \
  "s_nop 1\n" \
  "v_and_b32 v196, 0x7fffffff, v196\n" \
  "s_nop 1\n" \
  "v_mul_f32 v198, v201, v198\n" \
  "s_nop 1\n" \
  "buffer_store_dword v196, v251, %[rs], %[so] offen sc1\n" \
  "s_nop 1\n" \
  "v_cvt_pk_bf16_f32 v197, v198, v198\n" \
  "s_nop 1\n" \
  "v_and_b32 v197, v252, v197\n" \
  "s_nop 1\n" \
  "buffer_store_dword v197, v251, %[rs], %[so] offen offset:64 sc1\n" \
  "s_branch .Lw2b_%=\n" \
  ".Lgo_%=:\n" \
  "v_add_u32 v252, 0x20000, v248\n" \
  "ds_read_b128 v[200:203], v248 offset:32768\n" \
  "ds_read_b128 v[204:207], v248 offset:33792\n" \
  "ds_read_b128 v[208:211], v248 offset:49152\n" \
  "ds_read_b128 v[212:215], v248 offset:50176\n" \
  "ds_read_b128 v[216:219], v252 offset:0\n" \
  "ds_read_b128 v[220:223], v252 offset:1024\n" \
  "ds_read_b128 v[240:243], v252 offset:16384\n" \
  "ds_read_b128 v[244:247], v252 offset:17408\n" \
  "v_mov_b64 v[224:225], 0\n" \
  "v_mov_b64 v[226:227], 0\n" \
  "v_mov_b64 v[228:229], 0\n" \
  "v_mov_b64 v[230:231], 0\n" \
  "v_mov_b64 v[232:233], 0\n" \
  "v_mov_b64 v[234:235], 0\n" \
  "v_mov_b64 v[236:237], 0\n" \
  "v_mov_b64 v[238:239], 0\n" \
  "v_xor_b32 v250, 0x800, v248\n" \
  "v_xor_b32 v251, 0x1000, v248\n" \
  "s_mov_b32 %[rt], 0x40000\n" \
  "s_setprio 2\n" \
  "s_waitcnt lgkmcnt(0)\n" \
  "s_barrier\n" \
  "buffer_load_dwordx4 v[192:195], v248, %[rs], %[o0] offen sc1\n" \
  "buffer_load_dwordx4 v[196:199], v248, %[rs], %[o0] offen offset:1024 sc1\n" \
  ".Lw_%=:\n" \
  "s_waitcnt vmcnt(1)\n" \
  "ds_write_b128 v248, v[192:195]\n" \
  "v_smfmac_f32_16x16x64_bf16 v[224:227], v[192:195], v[200:207], v249\n" \
  "v_max3_u32 v252, v192, v193, v194\n" \
  "s_waitcnt vmcnt(0)\n" \
  "ds_write_b128 v248, v[196:199] offset:1024\n" \
  "v_smfmac_f32_16x16x64_bf16 v[228:231], v[192:195], v[208:215], v249\n" \
  "v_max3_u32 v252, v252, v195, v196\n" \
  "v_smfmac_f32_16x16x64_bf16 v[224:227], v[196:199], v[216:223], v249\n" \
  "v_max3_u32 v252, v252, v197, v198\n" \
  "v_smfmac_f32_16x16x64_bf16 v[228:231], v[196:199], v[240:247], v249\n" \
  "v_max_u32 v252, v252, v199\n" \
  "v_smfmac_f32_16x16x64_bf16 v[232:235], v[192:195], v[96:103], v249\n" \
  "v_cmp_eq_u32 vcc, -1, v252\n" \
  "v_smfmac_f32_16x16x64_bf16 v[236:239], v[192:195], v[144:151], v249\n" \
  "v_xor_b32 v252, 0x1800, v248\n" \
  "s_cbranch_vccnz .Lr_%=\n" \
  ".Lrb_%=:\n" \
  "v_smfmac_f32_16x16x64_bf16 v[232:235], v[196:199], v[104:111], v249\n" \
  "v_smfmac_f32_16x16x64_bf16 v[236:239], v[196:199], v[152:159], v249\n" \
  "s_waitcnt lgkmcnt(0)\n" \
  "s_barrier\n" \
  "ds_read_b128 v[200:203], v250 offset:0\n" \
  "ds_read_b128 v[204:207], v250 offset:1024\n" \
  "ds_read_b128 v[208:211], v251 offset:0\n" \
  "ds_read_b128 v[212:215], v251 offset:1024\n" \
  "ds_read_b128 v[216:219], v252 offset:0\n" \
  "ds_read_b128 v[220:223], v252 offset:1024\n" \
  "s_waitcnt lgkmcnt(5)\n" \
  "v_smfmac_f32_16x16x64_bf16 v[224:227], v[200:203], v[0:7], v249\n" \
  "v_smfmac_f32_16x16x64_bf16 v[228:231], v[200:203], v[48:55], v249\n" \
  "s_waitcnt lgkmcnt(4)\n" \
  "v_smfmac_f32_16x16x64_bf16 v[224:227], v[204:207], v[8:15], v249\n" \
  "v_smfmac_f32_16x16x64_bf16 v[228:231], v[204:207], v[56:63], v249\n" \
  "s_waitcnt lgkmcnt(3)\n" \
  "v_smfmac_f32_16x16x64_bf16 v[224:227], v[208:211], v[16:23], v249\n" \
  "v_smfmac_f32_16x16x64_bf16 v[228:231], v[208:211], v[64:71], v249\n" \
  "s_waitcnt lgkmcnt(2)\n" \
  "v_smfmac_f32_16x16x64_bf16 v[224:227], v[212:215], v[24:31], v249\n" \
  "s_waitcnt lgkmcnt(1)\n" \
  "v_smfmac_f32_16x16x64_bf16 v[224:227], v[216:219], v[32:39], v249\n" \
  "s_waitcnt lgkmcnt(0)\n" \
  "v_smfmac_f32_16x16x64_bf16 v[224:227], v[220:223], v[40:47], v249\n" \
  "v_smfmac_f32_16x16x64_bf16 v[228:231], v[212:215], v[72:79], v249\n" \
  "v_smfmac_f32_16x16x64_bf16 v[228:231], v[216:219], v[80:87], v249\n" \
  "s_nop 0\n" \
  "v_add_f32 v224, v224, v226\n" \
  "v_smfmac_f32_16x16x64_bf16 v[228:231], v[220:223], v[88:95], v249\n" \
  "v_add_f32 v225, v225, v227\n" \
  "ds_write_b64 v248, v[224:225] offset:8192\n" \
  "s_nop 6\n" \
  "v_add_f32 v228, v228, v230\n" \
  "v_add_f32 v229, v229, v231\n" \
  "ds_write_b64 v248, v[228:229] offset:8200\n" \
  "s_setprio 0\n" \
  "ds_read_b128 v[240:243], v254 offset:0\n" \
  "ds_read_b128 v[244:247], v254 offset:1024\n" \
  "s_waitcnt lgkmcnt(2)\n" \
  "s_barrier\n" \
  "s_nop 1\n" \
  "ds_read_b128 v[192:195], v254 offset:16384\n" \
  "ds_read_b128 v[196:199], v254 offset:17408\n" \
  "ds_read_b128 v[224:227], v254 offset:32768\n" \
  "ds_read_b128 v[228:231], v254 offset:33792\n" \
  "s_waitcnt lgkmcnt(4)\n" \
  "v_smfmac_f32_16x16x64_bf16 v[232:235], v[200:203], v[240:247], v249\n" \
  "ds_read_b128 v[240:243], v254 offset:49152\n" \
  "ds_read_b128 v[244:247], v254 offset:50176\n" \
  "s_waitcnt lgkmcnt(4)\n" \
  "v_smfmac_f32_16x16x64_bf16 v[236:239], v[200:203], v[192:199], v249\n" \
  "v_smfmac_f32_16x16x64_bf16 v[232:235], v[204:207], v[112:119], v249\n" \
  "ds_read_b128 v[192:195], v248 offset:14336\n" \
  "v_smfmac_f32_16x16x64_bf16 v[236:239], v[204:207], v[160:167], v249\n" \
  "ds_read_b128 v[196:199], v248 offset:15360\n" \
  "v_smfmac_f32_16x16x64_bf16 v[232:235], v[212:215], v[120:127], v249\n" \
  "ds_read_b128 v[200:203], v248 offset:18432\n" \
  "v_smfmac_f32_16x16x64_bf16 v[236:239], v[212:215], v[168:175], v249\n" \
  "ds_read_b128 v[204:207], v248 offset:19456\n" \
  "s_waitcnt lgkmcnt(6)\n" \
  "v_smfmac_f32_16x16x64_bf16 v[232:235], v[208:211], v[224:231], v249\n" \
  "ds_read_b128 v[212:215], v248 offset:21504\n" \
  "s_waitcnt lgkmcnt(5)\n" \
  "v_smfmac_f32_16x16x64_bf16 v[236:239], v[208:211], v[240:247], v249\n" \
  "ds_read_b128 v[240:243], v248 offset:16384\n" \
  "v_smfmac_f32_16x16x64_bf16 v[232:235], v[216:219], v[128:135], v249\n" \
  "ds_read_b128 v[244:247], v248 offset:17408\n" \
  "v_smfmac_f32_16x16x64_bf16 v[232:235], v[220:223], v[136:143], v249\n" \
  "ds_read_b128 v[208:211], v248 offset:20480\n" \
  "v_smfmac_f32_16x16x64_bf16 v[236:239], v[216:219], v[176:183], v249\n" \
  "ds_read_b96 v[250:252], v248 offset:13312\n" \
  "v_smfmac_f32_16x16x64_bf16 v[236:239], v[220:223], v[184:191], v249\n" \
  "s_setprio 3\n" \
  "s_barrier\n" \
  "ds_read_b64_tr_b16 v[224:225], v253 offset:17408\n" \
  "ds_read_b64_tr_b16 v[226:227], v253 offset:17664\n" \
  "ds_read_b64_tr_b16 v[228:229], v253 offset:19456\n" \
  "ds_read_b64_tr_b16 v[230:231], v253 offset:19712\n" \
  "v_mov_b64 v[216:217], 0\n" \
  "v_mov_b64 v[218:219], 0\n" \
  "v_add_f32 v232, v232, v234\n" \
  "v_add_f32 v233, v233, v235\n" \
  "ds_write_b64 v248, v[232:233] offset:0\n" \
  "v_mov_b64 v[220:221], 0\n" \
  "v_mov_b64 v[222:223], 0\n" \
  "v_add_f32 v236, v236, v238\n" \
  "v_add_f32 v237, v237, v239\n" \
  "ds_write_b64 v248, v[236:237] offset:8\n" \
  "s_waitcnt lgkmcnt(4)\n" \
  "v_smfmac_f32_16x16x64_bf16 v[216:219], v[224:227], v[192:199], v249\n" \
  "v_smfmac_f32_16x16x64_bf16 v[220:223], v[224:227], v[200:207], v249\n" \
  "s_waitcnt lgkmcnt(2)\n" \
  "v_smfmac_f32_16x16x64_bf16 v[216:219], v[228:231], v[240:247], v249\n" \
  "v_smfmac_f32_16x16x64_bf16 v[220:223], v[228:231], v[208:215], v249\n" \
  "s_mov_b32 %[rt], 0x422c0000\n" \
  "s_waitcnt lgkmcnt(0)\n" \
  "s_barrier\n" \
  "s_nop 1\n" \
  "v_add_f32 v192, v217, v219\n" \
  "s_nop 2\n" \
  "v_add_f32 v193, v221, v223\n" \
  "v_exp_f32 v194, v192\n" \
  "v_exp_f32 v195, v193\n" \
  "v_max_f32 v197, |v192|, |v193|\n" \
  "v_cvt_pk_bf16_f32 v196, v194, v195\n" \
  "v_cmp_nge_f32 vcc, %[rt], v197\n" \
  "s_cbranch_vccnz .Lw1_%=\n" \
  ".Lw1b_%=:\n" \
  "buffer_store_dword v196, v250, %[rs], %[c0] offen sc1\n" \
  "buffer_store_dword v196, v250, %[rs], %[c1] offen sc1\n" \
  "buffer_store_dword v196, v250, %[rs], %[c2] offen sc1\n" \
  "buffer_store_dword v196, v250, %[rs], %[c3] offen sc1\n" \
  "v_add_f32 v199, v194, v195\n" \
  "v_xor_b32 v251, %[xr], v251\n" \
  "s_nop 0\n" \
  "v_add_f32_dpp v199, v199, v199 quad_perm:[1,0,3,2] row_mask:0xf bank_mask:0xf\n" \
  "s_nop 1\n" \
  "v_add_f32_dpp v199, v199, v199 quad_perm:[2,3,0,1] row_mask:0xf bank_mask:0xf\n" \
  "s_barrier\n" \
  "ds_read_b64_tr_b16 v[232:233], v253 offset:9216\n" \
  "ds_read_b64_tr_b16 v[234:235], v253 offset:9472\n" \
  "ds_read_b64_tr_b16 v[236:237], v253 offset:11264\n" \
  "ds_read_b64_tr_b16 v[238:239], v253 offset:11520\n" \
  "s_waitcnt lgkmcnt(2)\n" \
  "v_mfma_f32_16x16x32_bf16 v[216:219], v[224:227], v[232:235], 0\n" \
  "s_waitcnt lgkmcnt(0)\n" \
  "v_mfma_f32_16x16x32_bf16 v[216:219], v[228:231], v[236:239], v[216:219]\n" \
  "s_nop 8\n" \
  "v_add_f32_dpp v192, v219, v217 quad_perm:[2,3,2,3] row_mask:0xf bank_mask:0xf\n" \
  "v_exp_f32 v194, v192\n" \
  "v_cmp_nge_f32 vcc, %[rt], |v192|\n" \
  "s_and_b64 vcc, vcc, %[lo]\n" \
  "v_add_f32_dpp v198, v194, v199 quad_perm:[0,0,0,0] row_mask:0xf bank_mask:0xf\n" \
  "v_mov_b32_dpp v193, v194 quad_perm:[1,1,1,1] row_mask:0xf bank_mask:0xf\n" \
  "v_add_f32 v198, v198, v193\n" \
  "v_mov_b32_dpp v195, v194 quad_perm:[0,0,0,0] row_mask:0xf bank_mask:0xf\n" \
  "s_cbranch_scc1 .Lw2_%=\n" \
  "v_add_f32_dpp v198, v198, v198 row_ror:4 row_mask:0xf bank_mask:0xf\n" \
  "s_nop 1\n" \
  "v_add_f32_dpp v198, v198, v198 row_ror:8 row_mask:0xf bank_mask:0xf\n" \
  "v_rcp_f32 v198, v198\n" \
  "s_nop 0\n" \
  "v_mul_f32 v195, v195, v198\n" \
  "v_mul_f32 v193, v193, v198\n" \
  "v_cvt_pk_bf16_f32 v196, v195, v193\n" \
  "buffer_store_dword v196, v251, %[rs], %[so] offen sc1\n" \
  "v_cvt_pk_bf16_f32 v197, v198, v198\n" \
  "v_and_b32 v197, v252, v197\n" \
  "buffer_store_dword v197, v251, %[rs], %[so] offen offset:64 sc1\n" \
  ".Lw2b_%=:\n" \
  "s_setprio 0\n"
#define QK_X \
  "s_branch .Lgo_%=\n" \
  ".Lr_%=:\n" \
  "s_sub_u32 %[rt], %[rt], 1\n" \
  "s_cbranch_scc1 .Lrb_%=\n" \
  "s_sleep 2\n" \
  "buffer_load_dwordx4 v[192:195], v248, %[rs], %[o0] offen sc1\n" \
  "buffer_load_dwordx4 v[196:199], v248, %[rs], %[o0] offen offset:1024 sc1\n" \
  "v_mov_b64 v[224:225], 0\n" \
  "v_mov_b64 v[226:227], 0\n" \
  "v_mov_b64 v[228:229], 0\n" \
  "v_mov_b64 v[230:231], 0\n" \
  "v_mov_b64 v[232:233], 0\n" \
  "v_mov_b64 v[234:235], 0\n" \
  "v_mov_b64 v[236:237], 0\n" \
  "v_mov_b64 v[238:239], 0\n" \
  "s_nop 1\n" \
  "s_branch .Lw_%=\n" \
  ".Lgo_%=:\n" \
  "v_add_u32 v252, 0x20000, v248\n" \
  "ds_read_b128 v[200:203], v248 offset:32768\n" \
  "ds_read_b128 v[204:207], v248 offset:33792\n" \
  "ds_read_b128 v[208:211], v248 offset:49152\n" \
  "ds_read_b128 v[212:215], v248 offset:50176\n" \
  "ds_read_b128 v[216:219], v252 offset:0\n" \
  "ds_read_b128 v[220:223], v252 offset:1024\n" \
  "ds_read_b128 v[240:243], v252 offset:16384\n" \
  "ds_read_b128 v[244:247], v252 offset:17408\n" \
  "v_mov_b64 v[224:225], 0\n" \
  "v_mov_b64 v[226:227], 0\n" \
  "v_mov_b64 v[228:229], 0\n" \
  "v_mov_b64 v[230:231], 0\n" \
  "v_mov_b64 v[232:233], 0\n" \
  "v_mov_b64 v[234:235], 0\n" \
  "v_mov_b64 v[236:237], 0\n" \
  "v_mov_b64 v[238:239], 0\n" \
  "v_xor_b32 v250, 0x800, v248\n" \
  "v_xor_b32 v251, 0x1000, v248\n" \
  "s_mov_b32 %[rt], 0x40000\n" \
  "s_setprio 2\n" \
  "s_waitcnt lgkmcnt(0)\n" \
  "s_barrier\n" \
  "s_nop 3\n" \
  "buffer_load_dwordx4 v[192:195], v248, %[rs], %[o0] offen sc1\n" \
  "buffer_load_dwordx4 v[196:199], v248, %[rs], %[o0] offen offset:1024 sc1\n" \
  ".Lw_%=:\n" \
  "s_waitcnt vmcnt(1)\n" \
  "ds_write_b128 v248, v[192:195]\n" \
  "v_smfmac_f32_16x16x64_bf16 v[224:227], v[192:195], v[200:207], v249\n" \
  "v_max3_u32 v252, v192, v193, v194\n" \
  "s_waitcnt vmcnt(0)\n" \
  "ds_write_b128 v248, v[196:199] offset:1024\n" \
  "v_smfmac_f32_16x16x64_bf16 v[228:231], v[192:195], v[208:215], v249\n" \
  "v_max3_u32 v252, v252, v195, v196\n" \
  "v_smfmac_f32_16x16x64_bf16 v[224:227], v[196:199], v[216:223], v249\n" \
  "v_max3_u32 v252, v252, v197, v198\n" \
  "v_smfmac_f32_16x16x64_bf16 v[228:231], v[196:199], v[240:247], v249\n" \
  "v_max_u32 v252, v252, v199\n" \
  "v_smfmac_f32_16x16x64_bf16 v[232:235], v[192:195], v[96:103], v249\n" \
  "v_cmp_eq_u32 vcc, -1, v252\n" \
  "v_smfmac_f32_16x16x64_bf16 v[236:239], v[192:195], v[144:151], v249\n" \
  "v_xor_b32 v252, 0x1800, v248\n" \
  "s_cbranch_vccnz .Lr_%=\n" \
  ".Lrb_%=:\n" \
  "v_smfmac_f32_16x16x64_bf16 v[232:235], v[196:199], v[104:111], v249\n" \
  "v_smfmac_f32_16x16x64_bf16 v[236:239], v[196:199], v[152:159], v249\n" \
  "s_waitcnt lgkmcnt(0)\n" \
  "s_barrier\n" \
  "ds_read_b128 v[200:203], v250 offset:0\n" \
  "ds_read_b128 v[204:207], v250 offset:1024\n" \
  "ds_read_b128 v[208:211], v251 offset:0\n" \
  "ds_read_b128 v[212:215], v251 offset:1024\n" \
  "ds_read_b128 v[216:219], v252 offset:0\n" \
  "ds_read_b128 v[220:223], v252 offset:1024\n" \
  "s_waitcnt lgkmcnt(5)\n" \
  "v_smfmac_f32_16x16x64_bf16 v[224:227], v[200:203], v[0:7], v249\n" \
  "v_smfmac_f32_16x16x64_bf16 v[228:231], v[200:203], v[48:55], v249\n" \
  "s_waitcnt lgkmcnt(4)\n" \
  "v_smfmac_f32_16x16x64_bf16 v[224:227], v[204:207], v[8:15], v249\n" \
  "v_smfmac_f32_16x16x64_bf16 v[228:231], v[204:207], v[56:63], v249\n" \
  "s_waitcnt lgkmcnt(3)\n" \
  "v_smfmac_f32_16x16x64_bf16 v[224:227], v[208:211], v[16:23], v249\n" \
  "v_smfmac_f32_16x16x64_bf16 v[228:231], v[208:211], v[64:71], v249\n" \
  "s_waitcnt lgkmcnt(2)\n" \
  "v_smfmac_f32_16x16x64_bf16 v[224:227], v[212:215], v[24:31], v249\n" \
  "s_waitcnt lgkmcnt(1)\n" \
  "v_smfmac_f32_16x16x64_bf16 v[224:227], v[216:219], v[32:39], v249\n" \
  "s_waitcnt lgkmcnt(0)\n" \
  "v_smfmac_f32_16x16x64_bf16 v[224:227], v[220:223], v[40:47], v249\n" \
  "v_smfmac_f32_16x16x64_bf16 v[228:231], v[212:215], v[72:79], v249\n" \
  "v_and_b32 v252, 0x3ff, v248\n" \
  "v_smfmac_f32_16x16x64_bf16 v[228:231], v[216:219], v[80:87], v249\n" \
  "s_nop 0\n" \
  "v_add_f32 v224, v224, v226\n" \
  "v_smfmac_f32_16x16x64_bf16 v[228:231], v[220:223], v[88:95], v249\n" \
  "v_add_f32 v225, v225, v227\n" \
  "ds_write_b64 v248, v[224:225] offset:8192\n" \
  "s_nop 6\n" \
  "v_add_f32 v228, v228, v230\n" \
  "v_add_f32 v229, v229, v231\n" \
  "ds_write_b64 v248, v[228:229] offset:8200\n" \
  "s_setprio 0\n" \
  "ds_read_b128 v[240:243], v254 offset:0\n" \
  "ds_read_b128 v[244:247], v254 offset:1024\n" \
  "s_waitcnt lgkmcnt(2)\n" \
  "s_barrier\n" \
  "s_nop 1\n" \
  "ds_read_b128 v[192:195], v254 offset:16384\n" \
  "ds_read_b128 v[196:199], v254 offset:17408\n" \
  "ds_read_b128 v[224:227], v254 offset:32768\n" \
  "ds_read_b128 v[228:231], v254 offset:33792\n" \
  "s_waitcnt lgkmcnt(4)\n" \
  "v_smfmac_f32_16x16x64_bf16 v[232:235], v[200:203], v[240:247], v249\n" \
  "ds_read_b128 v[240:243], v254 offset:49152\n" \
  "ds_read_b128 v[244:247], v254 offset:50176\n" \
  "s_waitcnt lgkmcnt(4)\n" \
  "v_smfmac_f32_16x16x64_bf16 v[236:239], v[200:203], v[192:199], v249\n" \
  "v_smfmac_f32_16x16x64_bf16 v[232:235], v[204:207], v[112:119], v249\n" \
  "v_smfmac_f32_16x16x64_bf16 v[236:239], v[204:207], v[160:167], v249\n" \
  "v_smfmac_f32_16x16x64_bf16 v[232:235], v[212:215], v[120:127], v249\n" \
  "v_smfmac_f32_16x16x64_bf16 v[236:239], v[212:215], v[168:175], v249\n" \
  "s_waitcnt lgkmcnt(2)\n" \
  "v_smfmac_f32_16x16x64_bf16 v[232:235], v[208:211], v[224:231], v249\n" \
  "s_waitcnt lgkmcnt(0)\n" \
  "v_smfmac_f32_16x16x64_bf16 v[236:239], v[208:211], v[240:247], v249\n" \
  "v_smfmac_f32_16x16x64_bf16 v[232:235], v[216:219], v[128:135], v249\n" \
  "v_smfmac_f32_16x16x64_bf16 v[232:235], v[220:223], v[136:143], v249\n" \
  "v_smfmac_f32_16x16x64_bf16 v[236:239], v[216:219], v[176:183], v249\n" \
  "buffer_load_dwordx4 v[224:227], v252, %[rs], %[qo] offen offset:0 sc1\n" \
  "buffer_load_dwordx4 v[228:231], v252, %[rs], %[qo] offen offset:1024 sc1\n" \
  "buffer_load_dwordx4 v[240:243], v252, %[rs], %[qo] offen offset:2048 sc1\n" \
  "buffer_load_dwordx4 v[244:247], v252, %[rs], %[qo] offen offset:3072 sc1\n" \
  "v_smfmac_f32_16x16x64_bf16 v[236:239], v[220:223], v[184:191], v249\n" \
  "s_setprio 3\n" \
  "s_barrier\n" \
  "v_add_f32 v232, v232, v234\n" \
  "v_add_f32 v233, v233, v235\n" \
  "ds_write_b64 v248, v[232:233] offset:0\n" \
  "s_nop 3\n" \
  "v_add_f32 v236, v236, v238\n" \
  "v_add_f32 v237, v237, v239\n" \
  "ds_write_b64 v248, v[236:237] offset:8\n" \
  "s_waitcnt lgkmcnt(0)\n" \
  "s_barrier\n" \
  "s_setprio 0\n" \
  "s_nop 15\n" \
  "s_nop 13\n" \
  "ds_read_b128 v[192:195], v252 offset:24576\n" \
  "ds_read_b128 v[196:199], v252 offset:28672\n" \
  "ds_read_b128 v[200:203], v252 offset:25600\n" \
  "ds_read_b128 v[204:207], v252 offset:29696\n" \
  "ds_read_b128 v[208:211], v252 offset:26624\n" \
  "ds_read_b128 v[212:215], v252 offset:30720\n" \
  "ds_read_b128 v[216:219], v252 offset:27648\n" \
  "ds_read_b128 v[220:223], v252 offset:31744\n" \
  "s_waitcnt lgkmcnt(0)\n" \
  "s_barrier\n" \
  "s_nop 15\n" \
  "s_nop 3\n" \
  "ds_read_b64_tr_b16 v[232:233], v253 offset:9216\n" \
  "ds_read_b64_tr_b16 v[234:235], v253 offset:9472\n" \
  "ds_read_b64_tr_b16 v[236:237], v253 offset:11264\n" \
  "ds_read_b64_tr_b16 v[238:239], v253 offset:11520\n" \
  "s_waitcnt vmcnt(3)\n" \
  "v_mfma_f32_16x16x32_bf16 v[192:195], v[192:195], v[224:227], 0\n" \
  "v_mfma_f32_16x16x32_bf16 v[196:199], v[196:199], v[224:227], 0\n" \
  "s_waitcnt vmcnt(2)\n" \
  "v_mfma_f32_16x16x32_bf16 v[192:195], v[200:203], v[228:231], v[192:195]\n" \
  "v_mfma_f32_16x16x32_bf16 v[196:199], v[204:207], v[228:231], v[196:199]\n" \
  "s_waitcnt vmcnt(1)\n" \
  "v_mfma_f32_16x16x32_bf16 v[192:195], v[208:211], v[240:243], v[192:195]\n" \
  "v_mov_b64 v[200:201], 0\n" \
  "v_mfma_f32_16x16x32_bf16 v[196:199], v[212:215], v[240:243], v[196:199]\n" \
  "v_mov_b64 v[202:203], 0\n" \
  "s_waitcnt vmcnt(0)\n" \
  "v_mfma_f32_16x16x32_bf16 v[192:195], v[216:219], v[244:247], v[192:195]\n" \
  "v_mfma_f32_16x16x32_bf16 v[196:199], v[220:223], v[244:247], v[196:199]\n" \
  "s_waitcnt lgkmcnt(2)\n" \
  "v_smfmac_f32_16x16x64_bf16 v[200:203], v[232:235], v[224:231], v249\n" \
  "s_waitcnt lgkmcnt(0)\n" \
  "v_smfmac_f32_16x16x64_bf16 v[200:203], v[236:239], v[240:247], v249\n" \
  "s_nop 8\n"
#define QK_K \
  "s_branch .Lgo_%=\n" \
  ".Lr_%=:\n" \
  "s_sub_u32 %[rt], %[rt], 1\n" \
  "s_cbranch_scc1 .Lrb_%=\n" \
  "s_sleep 2\n" \
  "buffer_load_dwordx4 v[192:195], v248, %[rs], %[o0] offen sc1\n" \
  "buffer_load_dwordx4 v[196:199], v248, %[rs], %[o0] offen offset:1024 sc1\n" \
  "v_mov_b64 v[224:225], 0\n" \
  "v_mov_b64 v[226:227], 0\n" \
  "v_mov_b64 v[228:229], 0\n" \
  "v_mov_b64 v[230:231], 0\n" \
  "s_nop 1\n" \
  "s_branch .Lw_%=\n" \
  ".Lgo_%=:\n" \
  "v_add_u32 v252, 0x20000, v248\n" \
  "ds_read_b128 v[200:203], v248 offset:32768\n" \
  "ds_read_b128 v[204:207], v248 offset:33792\n" \
  "ds_read_b128 v[208:211], v248 offset:49152\n" \
  "ds_read_b128 v[212:215], v248 offset:50176\n" \
  "ds_read_b128 v[216:219], v252 offset:0\n" \
  "ds_read_b128 v[220:223], v252 offset:1024\n" \
  "ds_read_b128 v[240:243], v252 offset:16384\n" \
  "ds_read_b128 v[244:247], v252 offset:17408\n" \
  "v_mov_b64 v[224:225], 0\n" \
  "v_mov_b64 v[226:227], 0\n" \
  "v_mov_b64 v[228:229], 0\n" \
  "v_mov_b64 v[230:231], 0\n" \
  "v_xor_b32 v250, 0x800, v248\n" \
  "v_xor_b32 v251, 0x1000, v248\n" \
  "s_mov_b32 %[rt], 0x40000\n" \
  "s_setprio 1\n" \
  "s_waitcnt lgkmcnt(0)\n" \
  "s_cmp_eq_u32 %[tw], 0\n" \
  "s_cbranch_scc1 .Ltq_%=\n" \
  ".Ltl_%=:\n" \
  "s_memtime vcc\n" \
  "s_waitcnt lgkmcnt(0)\n" \
  "s_sub_i32 %[cnt], %[tg], vcc_lo\n" \
  "s_cmp_lt_i32 %[cnt], 260\n" \
  "s_cbranch_scc1 .Ltf_%=\n" \
  "s_sleep 1\n" \
  "s_branch .Ltl_%=\n" \
  ".Ltf_%=:\n" \
  "s_max_i32 %[cnt], %[cnt], 0\n" \
  ".Ltg_%=:\n" \
  "s_sub_u32 %[cnt], %[cnt], 28\n" \
  "s_cbranch_scc0 .Ltg_%=\n" \
  ".Ltq_%=:\n" \
  "s_barrier\n" \
  "buffer_load_dwordx4 v[192:195], v248, %[rs], %[o0] offen sc1\n" \
  "buffer_load_dwordx4 v[196:199], v248, %[rs], %[o0] offen offset:1024 sc1\n" \
  ".Lw_%=:\n" \
  "s_waitcnt vmcnt(1)\n" \
  "ds_write_b128 v248, v[192:195]\n" \
  "v_smfmac_f32_16x16x64_bf16 v[224:227], v[192:195], v[200:207], v249\n" \
  "v_max3_u32 v252, v192, v193, v194\n" \
  "s_waitcnt vmcnt(0)\n" \
  "ds_write_b128 v248, v[196:199] offset:1024\n" \
  "v_smfmac_f32_16x16x64_bf16 v[228:231], v[192:195], v[208:215], v249\n" \
  "v_max3_u32 v252, v252, v195, v196\n" \
  "v_smfmac_f32_16x16x64_bf16 v[224:227], v[196:199], v[216:223], v249\n" \
  "v_max3_u32 v252, v252, v197, v198\n" \
  "v_max_u32 v252, v252, v199\n" \
  "v_smfmac_f32_16x16x64_bf16 v[228:231], v[196:199], v[240:247], v249\n" \
  "v_cmp_eq_u32 vcc, -1, v252\n" \
  "v_xor_b32 v252, 0x1800, v248\n" \
  "s_cbranch_vccnz .Lr_%=\n" \
  ".Lrb_%=:\n" \
  "s_waitcnt lgkmcnt(0)\n" \
  "s_barrier\n" \
  "s_memtime vcc\n" \
  "s_setprio 3\n" \
  "s_waitcnt lgkmcnt(0)\n" \
  "s_sub_u32 %[cnt], vcc_lo, %[ckm]\n" \
  "s_mul_hi_u32 %[cnt], %[cnt], %[inv]\n" \
  "s_add_u32 %[cnt], %[cnt], %[r16]\n" \
  "s_and_b32 %[cnt], %[cnt], -2\n" \
  "v_mov_b32 v235, %[cnt]\n" \
  "buffer_store_dword v235, v232, %[rs], %[sst] offen sc1\n" \
  "s_nop 5\n" \
  "ds_read_b128 v[200:203], v250 offset:0\n" \
  "ds_read_b128 v[204:207], v250 offset:1024\n" \
  "ds_read_b128 v[208:211], v251 offset:0\n" \
  "ds_read_b128 v[212:215], v251 offset:1024\n" \
  "ds_read_b128 v[216:219], v252 offset:0\n" \
  "ds_read_b128 v[220:223], v252 offset:1024\n" \
  "buffer_load_dword v250, v233, %[rs], %[t0] offen sc1\n" \
  "buffer_load_dword v251, v234, %[rs], %[t1] offen sc1\n" \
  "s_setprio 1\n" \
  "s_waitcnt lgkmcnt(5)\n" \
  "v_smfmac_f32_16x16x64_bf16 v[224:227], v[200:203], v[0:7], v249\n" \
  "v_smfmac_f32_16x16x64_bf16 v[228:231], v[200:203], v[48:55], v249\n" \
  "s_waitcnt lgkmcnt(4)\n" \
  "v_smfmac_f32_16x16x64_bf16 v[224:227], v[204:207], v[8:15], v249\n" \
  "v_mov_b64 v[232:233], 0\n" \
  "v_smfmac_f32_16x16x64_bf16 v[228:231], v[204:207], v[56:63], v249\n" \
  "v_mov_b64 v[234:235], 0\n" \
  "s_waitcnt lgkmcnt(3)\n" \
  "v_smfmac_f32_16x16x64_bf16 v[224:227], v[208:211], v[16:23], v249\n" \
  "ds_read_b128 v[240:243], v254 offset:0\n" \
  "v_smfmac_f32_16x16x64_bf16 v[228:231], v[208:211], v[64:71], v249\n" \
  "ds_read_b128 v[244:247], v254 offset:1024\n" \
  "s_waitcnt lgkmcnt(4)\n" \
  "v_smfmac_f32_16x16x64_bf16 v[224:227], v[212:215], v[24:31], v249\n" \
  "s_waitcnt lgkmcnt(3)\n" \
  "v_smfmac_f32_16x16x64_bf16 v[224:227], v[216:219], v[32:39], v249\n" \
  "s_waitcnt lgkmcnt(2)\n" \
  "v_smfmac_f32_16x16x64_bf16 v[224:227], v[220:223], v[40:47], v249\n" \
  "s_barrier\n" \
  "ds_read_b128 v[236:239], v248 offset:16384\n" \
  "v_smfmac_f32_16x16x64_bf16 v[228:231], v[212:215], v[72:79], v249\n" \
  "v_smfmac_f32_16x16x64_bf16 v[228:231], v[216:219], v[80:87], v249\n" \
  "s_nop 0\n" \
  "v_add_f32 v224, v224, v226\n" \
  "v_smfmac_f32_16x16x64_bf16 v[228:231], v[220:223], v[88:95], v249\n" \
  "v_add_f32 v225, v225, v227\n" \
  "s_waitcnt lgkmcnt(0)\n" \
  "v_add_f32 v236, v236, v224\n" \
  "v_add_f32 v237, v237, v225\n" \
  "v_cvt_pk_bf16_f32 v236, v236, v237\n" \
  "s_nop 3\n" \
  "v_add_f32 v238, v238, v228\n" \
  "v_add_f32 v239, v239, v229\n" \
  "v_add_f32 v238, v238, v230\n" \
  "v_add_f32 v239, v239, v231\n" \
  "v_cvt_pk_bf16_f32 v237, v238, v239\n" \
  "ds_write_b64 v253, v[236:237] offset:17408\n" \
  "s_setprio 0\n" \
  "ds_read_b128 v[224:227], v254 offset:16384\n" \
  "ds_read_b128 v[228:231], v254 offset:17408\n" \
  "v_mov_b64 v[236:237], 0\n" \
  "v_mov_b64 v[238:239], 0\n" \
  "v_smfmac_f32_16x16x64_bf16 v[232:235], v[192:195], v[96:103], v249\n" \
  "v_smfmac_f32_16x16x64_bf16 v[236:239], v[192:195], v[144:151], v249\n" \
  "s_waitcnt lgkmcnt(2)\n" \
  "s_barrier\n" \
  "v_smfmac_f32_16x16x64_bf16 v[232:235], v[196:199], v[104:111], v249\n" \
  "v_smfmac_f32_16x16x64_bf16 v[236:239], v[196:199], v[152:159], v249\n" \
  "v_smfmac_f32_16x16x64_bf16 v[232:235], v[200:203], v[240:247], v249\n" \
  "ds_read_b128 v[192:195], v254 offset:32768\n" \
  "ds_read_b128 v[196:199], v254 offset:33792\n" \
  "s_waitcnt lgkmcnt(2)\n" \
  "v_smfmac_f32_16x16x64_bf16 v[236:239], v[200:203], v[224:231], v249\n" \
  "ds_read_b128 v[240:243], v254 offset:49152\n" \
  "ds_read_b128 v[244:247], v254 offset:50176\n" \
  "v_smfmac_f32_16x16x64_bf16 v[232:235], v[204:207], v[112:119], v249\n" \
  "v_smfmac_f32_16x16x64_bf16 v[236:239], v[204:207], v[160:167], v249\n" \
  "s_barrier\n" \
  "ds_read_b128 v[200:203], v248 offset:8192\n" \
  "v_smfmac_f32_16x16x64_bf16 v[232:235], v[212:215], v[120:127], v249\n" \
  "v_smfmac_f32_16x16x64_bf16 v[236:239], v[212:215], v[168:175], v249\n" \
  "v_smfmac_f32_16x16x64_bf16 v[232:235], v[216:219], v[128:135], v249\n" \
  "v_smfmac_f32_16x16x64_bf16 v[232:235], v[220:223], v[136:143], v249\n" \
  "s_waitcnt lgkmcnt(0)\n" \
  "v_mul_f32 v200, %[cs], v200\n" \
  "v_smfmac_f32_16x16x64_bf16 v[232:235], v[208:211], v[192:199], v249\n" \
  "v_mul_f32 v201, %[cs], v201\n" \
  "v_smfmac_f32_16x16x64_bf16 v[236:239], v[216:219], v[176:183], v249\n" \
  "v_mul_f32 v202, %[cs], v202\n" \
  "v_smfmac_f32_16x16x64_bf16 v[236:239], v[220:223], v[184:191], v249\n" \
  "v_mul_f32 v203, %[cs], v203\n" \
  "v_smfmac_f32_16x16x64_bf16 v[236:239], v[208:211], v[240:247], v249\n" \
  "v_fma_f32 v200, v232, %[cs], v200\n" \
  "v_fma_f32 v201, v233, %[cs], v201\n" \
  "v_fma_f32 v200, v234, %[cs], v200\n" \
  "v_fma_f32 v201, v235, %[cs], v201\n" \
  "v_cvt_pk_bf16_f32 v200, v200, v201\n" \
  "s_nop 3\n" \
  "v_fma_f32 v202, v236, %[cs], v202\n" \
  "v_fma_f32 v203, v237, %[cs], v203\n" \
  "v_fma_f32 v202, v238, %[cs], v202\n" \
  "v_fma_f32 v203, v239, %[cs], v203\n" \
  "v_cvt_pk_bf16_f32 v201, v202, v203\n" \
  "ds_write_b64 v253, v[200:201] offset:9216\n" \
  "s_waitcnt lgkmcnt(0)\n" \
  "s_barrier\n" \
  "s_waitcnt vmcnt(0)\n"
#define OW_F \
  "s_branch .Lgo_%=\n" \
  ".Lbad_%=:\n" \
  "s_mov_b32 %[st], 1\n" \
  "s_branch .Lend_%=\n" \
  ".Lgo_%=:\n" \
  "v_mov_b64 v[96:97], 0\n" \
  "v_mov_b64 v[98:99], 0\n" \
  "s_mov_b32 %[st], 0\n" \
  "s_barrier\n" \
  "s_nop 15\n" \
  "s_nop 15\n" \
  "s_nop 12\n" \
  "ds_read_b32 v100, v118\n" \
  "buffer_load_dwordx4 v[0:3], v112, %[rs], %[sv1] offen offset:0 sc1\n" \
  "buffer_load_dwordx4 v[4:7], v112, %[rs], %[sv1] offen offset:1024 sc1\n" \
  "buffer_load_dwordx4 v[8:11], v112, %[rs], %[sv1] offen offset:2048 sc1\n" \
  "buffer_load_dwordx4 v[12:15], v112, %[rs], %[sv1] offen offset:3072 sc1\n" \
  "s_memtime vcc\n" \
  "s_waitcnt lgkmcnt(0)\n" \
  "v_readfirstlane_b32 %[cnt], v100\n" \
  "s_nop 0\n" \
  "s_sub_i32 %[cnt], %[cnt], vcc_lo\n" \
  "s_max_i32 %[cnt], %[cnt], 0\n" \
  "s_min_i32 %[cnt], %[cnt], 2000\n" \
  ".Ltg_%=:\n" \
  "s_sub_u32 %[cnt], %[cnt], 28\n" \
  "s_cbranch_scc0 .Ltg_%=\n" \
  "buffer_load_dwordx4 v[16:19], v112, %[rs], %[sv2] offen offset:0 sc1\n" \
  "buffer_load_dwordx4 v[20:23], v112, %[rs], %[sv2] offen offset:1024 sc1\n" \
  "buffer_load_dwordx4 v[24:27], v112, %[rs], %[sv2] offen offset:2048 sc1\n" \
  "buffer_load_dwordx4 v[28:31], v112, %[rs], %[sv2] offen offset:3072 sc1\n" \
  "s_nop 3\n" \
  "buffer_load_dwordx4 v[64:67], v113, %[rs], %[sp1] offen offset:0 sc1\n" \
  "buffer_load_dwordx4 v[68:71], v113, %[rs], %[sp1] offen offset:2048 sc1\n" \
  "buffer_load_dwordx4 v[72:75], v113, %[rs], %[sp2] offen offset:0 sc1\n" \
  "buffer_load_dwordx4 v[76:79], v113, %[rs], %[sp2] offen offset:2048 sc1\n" \
  "buffer_load_dwordx4 v[80:83], v113, %[rs], %[sp1] offen offset:64 sc1\n" \
  "buffer_load_dwordx4 v[84:87], v113, %[rs], %[sp1] offen offset:2112 sc1\n" \
  "buffer_load_dwordx4 v[88:91], v113, %[rs], %[sp2] offen offset:64 sc1\n" \
  "buffer_load_dwordx4 v[92:95], v113, %[rs], %[sp2] offen offset:2112 sc1\n" \
  "s_waitcnt vmcnt(15)\n" \
  "v_permlane16_swap_b32 v0, v1\n" \
  "v_permlane16_swap_b32 v2, v3\n" \
  "s_waitcnt vmcnt(14)\n" \
  "v_permlane16_swap_b32 v4, v5\n" \
  "v_permlane16_swap_b32 v6, v7\n" \
  "s_waitcnt vmcnt(13)\n" \
  "v_permlane16_swap_b32 v8, v9\n" \
  "v_permlane16_swap_b32 v10, v11\n" \
  "s_waitcnt vmcnt(12)\n" \
  "v_permlane16_swap_b32 v12, v13\n" \
  "v_permlane16_swap_b32 v14, v15\n" \
  "s_waitcnt vmcnt(11)\n" \
  "v_permlane16_swap_b32 v16, v17\n" \
  "v_permlane16_swap_b32 v18, v19\n" \
  "s_waitcnt vmcnt(10)\n" \
  "v_permlane16_swap_b32 v20, v21\n" \
  "v_permlane16_swap_b32 v22, v23\n" \
  "s_waitcnt vmcnt(9)\n" \
  "v_permlane16_swap_b32 v24, v25\n" \
  "v_permlane16_swap_b32 v26, v27\n" \
  "s_waitcnt vmcnt(8)\n" \
  "v_permlane16_swap_b32 v28, v29\n" \
  "v_permlane16_swap_b32 v30, v31\n" \
  "s_barrier\n" \
  "ds_read_b128 v[32:35], v114 offset:0\n" \
  "ds_read_b128 v[36:39], v114 offset:1024\n" \
  "ds_read_b128 v[40:43], v114 offset:2048\n" \
  "ds_read_b128 v[44:47], v114 offset:3072\n" \
  "ds_read_b128 v[48:51], v119 offset:0\n" \
  "ds_read_b128 v[52:55], v119 offset:1024\n" \
  "ds_read_b128 v[56:59], v119 offset:2048\n" \
  "ds_read_b128 v[60:63], v119 offset:3072\n" \
  "s_waitcnt vmcnt(7)\n" \
  "v_smfmac_f32_16x16x64_bf16 v[96:99], v[64:67], v[0:7], v117\n" \
  "s_waitcnt vmcnt(6)\n" \
  "v_smfmac_f32_16x16x64_bf16 v[96:99], v[68:71], v[8:15], v117\n" \
  "s_waitcnt vmcnt(5)\n" \
  "v_smfmac_f32_16x16x64_bf16 v[96:99], v[72:75], v[16:23], v117\n" \
  "s_waitcnt vmcnt(4)\n" \
  "v_smfmac_f32_16x16x64_bf16 v[96:99], v[76:79], v[24:31], v117\n" \
  "s_waitcnt vmcnt(3) lgkmcnt(6)\n" \
  "v_smfmac_f32_16x16x64_bf16 v[96:99], v[80:83], v[32:39], v117 abid:1\n" \
  "s_waitcnt vmcnt(2) lgkmcnt(4)\n" \
  "v_smfmac_f32_16x16x64_bf16 v[96:99], v[84:87], v[40:47], v117 abid:1\n" \
  "s_waitcnt vmcnt(1) lgkmcnt(2)\n" \
  "v_smfmac_f32_16x16x64_bf16 v[96:99], v[88:91], v[48:55], v117 abid:1\n" \
  "s_waitcnt vmcnt(0) lgkmcnt(0)\n" \
  "v_smfmac_f32_16x16x64_bf16 v[96:99], v[92:95], v[56:63], v117 abid:1\n" \
  "s_nop 8\n" \
  "v_permlane32_swap_b32 v96, v98\n" \
  "v_permlane32_swap_b32 v97, v99\n" \
  "v_add_f32 v96, v96, v98\n" \
  "v_add_f32 v97, v97, v99\n" \
  "v_cvt_pk_bf16_f32 v100, v96, v97\n" \
  "v_cmp_u_f32 vcc, v96, v97\n" \
  "s_nop 0\n" \
  "v_mov_b32_dpp v101, v100 quad_perm:[1,0,3,2] row_mask:0xf bank_mask:0xf\n" \
  "v_perm_b32 v100, v101, v100, v116\n" \
  "s_cbranch_vccnz .Lbad_%=\n" \
  "buffer_store_dword v100, v115, %[rs], %[sm0] offen sc1\n" \
  "buffer_store_dword v100, v115, %[rs], %[sm1] offen sc1\n" \
  "buffer_store_dword v100, v115, %[rs], %[sm2] offen sc1\n" \
  "buffer_store_dword v100, v115, %[rs], %[sm3] offen sc1\n" \
  ".Lend_%=:\n" \
  "s_memtime %[tp]\n" \
  "v_permlane16_swap_b32 v0, v1\n" \
  "v_permlane16_swap_b32 v2, v3\n" \
  "v_permlane16_swap_b32 v4, v5\n" \
  "v_permlane16_swap_b32 v6, v7\n" \
  "v_permlane16_swap_b32 v8, v9\n" \
  "v_permlane16_swap_b32 v10, v11\n" \
  "v_permlane16_swap_b32 v12, v13\n" \
  "v_permlane16_swap_b32 v14, v15\n" \
  "v_permlane16_swap_b32 v16, v17\n" \
  "v_permlane16_swap_b32 v18, v19\n" \
  "v_permlane16_swap_b32 v20, v21\n" \
  "v_permlane16_swap_b32 v22, v23\n" \
  "v_permlane16_swap_b32 v24, v25\n" \
  "v_permlane16_swap_b32 v26, v27\n" \
  "v_permlane16_swap_b32 v28, v29\n" \
  "v_permlane16_swap_b32 v30, v31\n" \
  "s_waitcnt lgkmcnt(0)\n"
#define ST_Y \
  "s_mov_b32 %[keep], m0\n" \
  "ds_read_b128 v[240:243], v250 offset:0\n" \
  "ds_read_b128 v[244:247], v250 offset:1024\n" \
  "v_mfma_f32_16x16x32_bf16 v[192:195], v[0:3], v[208:211], 0\n" \
  "v_mfma_f32_16x16x32_bf16 v[196:199], v[64:67], v[208:211], 0\n" \
  "ds_read_b128 v[208:211], v252 offset:8192\n" \
  "v_mfma_f32_16x16x32_bf16 v[200:203], v[0:3], v[212:215], 0\n" \
  "v_mfma_f32_16x16x32_bf16 v[204:207], v[64:67], v[212:215], 0\n" \
  "ds_read_b128 v[212:215], v252 offset:9216\n" \
  "v_mfma_f32_16x16x32_bf16 v[192:195], v[128:131], v[216:219], v[192:195]\n" \
  "v_mfma_f32_16x16x32_bf16 v[196:199], v[132:135], v[216:219], v[196:199]\n" \
  "ds_read_b128 v[216:219], v252 offset:10240\n" \
  "v_mfma_f32_16x16x32_bf16 v[200:203], v[128:131], v[220:223], v[200:203]\n" \
  "v_mfma_f32_16x16x32_bf16 v[204:207], v[132:135], v[220:223], v[204:207]\n" \
  "ds_read_b128 v[220:223], v252 offset:11264\n" \
  "v_mfma_f32_16x16x32_bf16 v[192:195], v[4:7], v[224:227], v[192:195]\n" \
  "v_mfma_f32_16x16x32_bf16 v[196:199], v[68:71], v[224:227], v[196:199]\n" \
  "ds_read_b128 v[224:227], v252 offset:12288\n" \
  "v_mfma_f32_16x16x32_bf16 v[200:203], v[4:7], v[228:231], v[200:203]\n" \
  "v_mfma_f32_16x16x32_bf16 v[204:207], v[68:71], v[228:231], v[204:207]\n" \
  "ds_read_b128 v[228:231], v252 offset:13312\n" \
  "s_waitcnt lgkmcnt(7)\n" \
  "v_mfma_f32_16x16x32_bf16 v[192:195], v[240:243], v[232:235], v[192:195]\n" \
  "s_waitcnt lgkmcnt(6)\n" \
  "v_mfma_f32_16x16x32_bf16 v[196:199], v[244:247], v[232:235], v[196:199]\n" \
  "ds_read_b128 v[232:235], v252 offset:14336\n" \
  "v_mfma_f32_16x16x32_bf16 v[200:203], v[240:243], v[236:239], v[200:203]\n" \
  "v_mfma_f32_16x16x32_bf16 v[204:207], v[244:247], v[236:239], v[204:207]\n" \
  "ds_read_b128 v[236:239], v252 offset:15360\n" \
  "ds_read_b128 v[240:243], v250 offset:16384\n" \
  "ds_read_b128 v[244:247], v250 offset:17408\n" \
  "s_waitcnt lgkmcnt(9)\n" \
  "v_mfma_f32_16x16x32_bf16 v[192:195], v[8:11], v[208:211], v[192:195]\n" \
  "v_mfma_f32_16x16x32_bf16 v[196:199], v[72:75], v[208:211], v[196:199]\n" \
  "s_waitcnt lgkmcnt(8)\n" \
  "v_mfma_f32_16x16x32_bf16 v[200:203], v[8:11], v[212:215], v[200:203]\n" \
  "v_mfma_f32_16x16x32_bf16 v[204:207], v[72:75], v[212:215], v[204:207]\n" \
  "s_waitcnt vmcnt(2)\n" \
  "s_waitcnt lgkmcnt(0)\n" \
  "s_barrier\n" \
  "ds_read_b128 v[208:211], v252 offset:16384\n" \
  "ds_read_b128 v[212:215], v252 offset:17408\n" \
  "s_add_u32 m0, %[mw], 0\n" \
  "s_xor_b32 %[cnt], %[sc], 512\n" \
  "buffer_load_dwordx4 v248, %[rx], %[cnt] offen lds\n" \
  "s_add_u32 m0, %[mw], 8192\n" \
  "s_xor_b32 %[cnt], %[sc], 640\n" \
  "buffer_load_dwordx4 v248, %[rx], %[cnt] offen lds\n" \
  "v_mfma_f32_16x16x32_bf16 v[192:195], v[136:139], v[216:219], v[192:195]\n" \
  "v_mfma_f32_16x16x32_bf16 v[196:199], v[140:143], v[216:219], v[196:199]\n" \
  "ds_read_b128 v[216:219], v252 offset:18432\n" \
  "v_mfma_f32_16x16x32_bf16 v[200:203], v[136:139], v[220:223], v[200:203]\n" \
  "v_mfma_f32_16x16x32_bf16 v[204:207], v[140:143], v[220:223], v[204:207]\n" \
  "ds_read_b128 v[220:223], v252 offset:19456\n" \
  "v_mfma_f32_16x16x32_bf16 v[192:195], v[12:15], v[224:227], v[192:195]\n" \
  "v_mfma_f32_16x16x32_bf16 v[196:199], v[76:79], v[224:227], v[196:199]\n" \
  "ds_read_b128 v[224:227], v252 offset:20480\n" \
  "v_mfma_f32_16x16x32_bf16 v[200:203], v[12:15], v[228:231], v[200:203]\n" \
  "v_mfma_f32_16x16x32_bf16 v[204:207], v[76:79], v[228:231], v[204:207]\n" \
  "ds_read_b128 v[228:231], v252 offset:21504\n" \
  "v_mfma_f32_16x16x32_bf16 v[192:195], v[240:243], v[232:235], v[192:195]\n" \
  "v_mfma_f32_16x16x32_bf16 v[196:199], v[244:247], v[232:235], v[196:199]\n" \
  "ds_read_b128 v[232:235], v252 offset:22528\n" \
  "v_mfma_f32_16x16x32_bf16 v[200:203], v[240:243], v[236:239], v[200:203]\n" \
  "v_mfma_f32_16x16x32_bf16 v[204:207], v[244:247], v[236:239], v[204:207]\n" \
  "ds_read_b128 v[236:239], v252 offset:23552\n" \
  "ds_read_b128 v[240:243], v250 offset:32768\n" \
  "ds_read_b128 v[244:247], v250 offset:33792\n" \
  "s_waitcnt lgkmcnt(9)\n" \
  "v_mfma_f32_16x16x32_bf16 v[192:195], v[16:19], v[208:211], v[192:195]\n" \
  "v_mfma_f32_16x16x32_bf16 v[196:199], v[80:83], v[208:211], v[196:199]\n" \
  "ds_read_b128 v[208:211], v252 offset:24576\n" \
  "s_waitcnt lgkmcnt(9)\n" \
  "v_mfma_f32_16x16x32_bf16 v[200:203], v[16:19], v[212:215], v[200:203]\n" \
  "v_mfma_f32_16x16x32_bf16 v[204:207], v[80:83], v[212:215], v[204:207]\n" \
  "ds_read_b128 v[212:215], v252 offset:25600\n" \
  "s_waitcnt lgkmcnt(9)\n" \
  "v_mfma_f32_16x16x32_bf16 v[192:195], v[144:147], v[216:219], v[192:195]\n" \
  "v_mfma_f32_16x16x32_bf16 v[196:199], v[148:151], v[216:219], v[196:199]\n" \
  "ds_read_b128 v[216:219], v252 offset:26624\n" \
  "s_waitcnt lgkmcnt(9)\n" \
  "v_mfma_f32_16x16x32_bf16 v[200:203], v[144:147], v[220:223], v[200:203]\n" \
  "v_mfma_f32_16x16x32_bf16 v[204:207], v[148:151], v[220:223], v[204:207]\n" \
  "ds_read_b128 v[220:223], v252 offset:27648\n" \
  "s_waitcnt lgkmcnt(9)\n" \
  "v_mfma_f32_16x16x32_bf16 v[192:195], v[20:23], v[224:227], v[192:195]\n" \
  "v_mfma_f32_16x16x32_bf16 v[196:199], v[84:87], v[224:227], v[196:199]\n" \
  "ds_read_b128 v[224:227], v252 offset:28672\n" \
  "s_waitcnt lgkmcnt(9)\n" \
  "v_mfma_f32_16x16x32_bf16 v[200:203], v[20:23], v[228:231], v[200:203]\n" \
  "v_mfma_f32_16x16x32_bf16 v[204:207], v[84:87], v[228:231], v[204:207]\n" \
  "ds_read_b128 v[228:231], v252 offset:29696\n" \
  "s_waitcnt lgkmcnt(7)\n" \
  "v_mfma_f32_16x16x32_bf16 v[192:195], v[240:243], v[232:235], v[192:195]\n" \
  "s_waitcnt lgkmcnt(6)\n" \
  "v_mfma_f32_16x16x32_bf16 v[196:199], v[244:247], v[232:235], v[196:199]\n" \
  "ds_read_b128 v[232:235], v252 offset:30720\n" \
  "v_mfma_f32_16x16x32_bf16 v[200:203], v[240:243], v[236:239], v[200:203]\n" \
  "v_mfma_f32_16x16x32_bf16 v[204:207], v[244:247], v[236:239], v[204:207]\n" \
  "ds_read_b128 v[236:239], v252 offset:31744\n" \
  "ds_read_b128 v[240:243], v250 offset:49152\n" \
  "ds_read_b128 v[244:247], v250 offset:50176\n" \
  "s_waitcnt lgkmcnt(9)\n" \
  "v_mfma_f32_16x16x32_bf16 v[192:195], v[24:27], v[208:211], v[192:195]\n" \
  "v_mfma_f32_16x16x32_bf16 v[196:199], v[88:91], v[208:211], v[196:199]\n" \
  "s_waitcnt lgkmcnt(8)\n" \
  "v_mfma_f32_16x16x32_bf16 v[200:203], v[24:27], v[212:215], v[200:203]\n" \
  "v_mfma_f32_16x16x32_bf16 v[204:207], v[88:91], v[212:215], v[204:207]\n" \
  "s_waitcnt vmcnt(0)\n" \
  "s_waitcnt lgkmcnt(0)\n" \
  "s_barrier\n" \
  "ds_read_b128 v[208:211], v252 offset:0\n" \
  "ds_read_b128 v[212:215], v252 offset:1024\n" \
  "s_add_u32 m0, %[mw], 16384\n" \
  "s_xor_b32 %[cnt], %[sc], 768\n" \
  "buffer_load_dwordx4 v248, %[rx], %[cnt] offen lds\n" \
  "s_add_u32 m0, %[mw], 24576\n" \
  "s_xor_b32 %[cnt], %[sc], 896\n" \
  "buffer_load_dwordx4 v248, %[rx], %[cnt] offen lds\n" \
  "v_lshl_or_b32 v253, v251, 24, %[fm]\n" \
  "buffer_store_dword v253, v253, %[rs], %[fo] offen sc1\n" \
  "v_mfma_f32_16x16x32_bf16 v[192:195], v[152:155], v[216:219], v[192:195]\n" \
  "v_mfma_f32_16x16x32_bf16 v[196:199], v[156:159], v[216:219], v[196:199]\n" \
  "ds_read_b128 v[216:219], v252 offset:2048\n" \
  "v_mfma_f32_16x16x32_bf16 v[200:203], v[152:155], v[220:223], v[200:203]\n" \
  "v_mfma_f32_16x16x32_bf16 v[204:207], v[156:159], v[220:223], v[204:207]\n" \
  "ds_read_b128 v[220:223], v252 offset:3072\n" \
  "v_mfma_f32_16x16x32_bf16 v[192:195], v[28:31], v[224:227], v[192:195]\n" \
  "v_mfma_f32_16x16x32_bf16 v[196:199], v[92:95], v[224:227], v[196:199]\n" \
  "ds_read_b128 v[224:227], v252 offset:4096\n" \
  "v_mfma_f32_16x16x32_bf16 v[200:203], v[28:31], v[228:231], v[200:203]\n" \
  "v_mfma_f32_16x16x32_bf16 v[204:207], v[92:95], v[228:231], v[204:207]\n" \
  "ds_read_b128 v[228:231], v252 offset:5120\n" \
  "v_mfma_f32_16x16x32_bf16 v[192:195], v[240:243], v[232:235], v[192:195]\n" \
  "v_mfma_f32_16x16x32_bf16 v[196:199], v[244:247], v[232:235], v[196:199]\n" \
  "ds_read_b128 v[232:235], v252 offset:6144\n" \
  "v_mfma_f32_16x16x32_bf16 v[200:203], v[240:243], v[236:239], v[200:203]\n" \
  "v_mfma_f32_16x16x32_bf16 v[204:207], v[244:247], v[236:239], v[204:207]\n" \
  "ds_read_b128 v[236:239], v252 offset:7168\n" \
  "ds_read_b128 v[240:243], v249 offset:0\n" \
  "ds_read_b128 v[244:247], v249 offset:1024\n" \
  "s_waitcnt lgkmcnt(9)\n" \
  "v_mfma_f32_16x16x32_bf16 v[192:195], v[32:35], v[208:211], v[192:195]\n" \
  "v_mfma_f32_16x16x32_bf16 v[196:199], v[96:99], v[208:211], v[196:199]\n" \
  "ds_read_b128 v[208:211], v252 offset:8192\n" \
  "s_waitcnt lgkmcnt(9)\n" \
  "v_mfma_f32_16x16x32_bf16 v[200:203], v[32:35], v[212:215], v[200:203]\n" \
  "v_mfma_f32_16x16x32_bf16 v[204:207], v[96:99], v[212:215], v[204:207]\n" \
  "ds_read_b128 v[212:215], v252 offset:9216\n" \
  "s_waitcnt lgkmcnt(9)\n" \
  "v_mfma_f32_16x16x32_bf16 v[192:195], v[160:163], v[216:219], v[192:195]\n" \
  "v_mfma_f32_16x16x32_bf16 v[196:199], v[164:167], v[216:219], v[196:199]\n" \
  "ds_read_b128 v[216:219], v252 offset:10240\n" \
  "s_waitcnt lgkmcnt(9)\n" \
  "v_mfma_f32_16x16x32_bf16 v[200:203], v[160:163], v[220:223], v[200:203]\n" \
  "v_mfma_f32_16x16x32_bf16 v[204:207], v[164:167], v[220:223], v[204:207]\n" \
  "ds_read_b128 v[220:223], v252 offset:11264\n" \
  "s_waitcnt lgkmcnt(9)\n" \
  "v_mfma_f32_16x16x32_bf16 v[192:195], v[36:39], v[224:227], v[192:195]\n" \
  "v_mfma_f32_16x16x32_bf16 v[196:199], v[100:103], v[224:227], v[196:199]\n" \
  "ds_read_b128 v[224:227], v252 offset:12288\n" \
  "s_waitcnt lgkmcnt(9)\n" \
  "v_mfma_f32_16x16x32_bf16 v[200:203], v[36:39], v[228:231], v[200:203]\n" \
  "v_mfma_f32_16x16x32_bf16 v[204:207], v[100:103], v[228:231], v[204:207]\n" \
  "ds_read_b128 v[228:231], v252 offset:13312\n" \
  "s_waitcnt lgkmcnt(7)\n" \
  "v_mfma_f32_16x16x32_bf16 v[192:195], v[240:243], v[232:235], v[192:195]\n" \
  "s_waitcnt lgkmcnt(6)\n" \
  "v_mfma_f32_16x16x32_bf16 v[196:199], v[244:247], v[232:235], v[196:199]\n" \
  "ds_read_b128 v[232:235], v252 offset:14336\n" \
  "v_mfma_f32_16x16x32_bf16 v[200:203], v[240:243], v[236:239], v[200:203]\n" \
  "v_mfma_f32_16x16x32_bf16 v[204:207], v[244:247], v[236:239], v[204:207]\n" \
  "ds_read_b128 v[236:239], v252 offset:15360\n" \
  "ds_read_b128 v[240:243], v249 offset:16384\n" \
  "ds_read_b128 v[244:247], v249 offset:17408\n" \
  "s_waitcnt lgkmcnt(9)\n" \
  "v_mfma_f32_16x16x32_bf16 v[192:195], v[40:43], v[208:211], v[192:195]\n" \
  "v_mfma_f32_16x16x32_bf16 v[196:199], v[104:107], v[208:211], v[196:199]\n" \
  "s_waitcnt lgkmcnt(8)\n" \
  "v_mfma_f32_16x16x32_bf16 v[200:203], v[40:43], v[212:215], v[200:203]\n" \
  "v_mfma_f32_16x16x32_bf16 v[204:207], v[104:107], v[212:215], v[204:207]\n" \
  "s_waitcnt vmcnt(1)\n" \
  "s_waitcnt lgkmcnt(0)\n" \
  "s_barrier\n" \
  "ds_read_b128 v[208:211], v252 offset:16384\n" \
  "ds_read_b128 v[212:215], v252 offset:17408\n" \
  "s_add_u32 m0, %[mw], 0\n" \
  "s_xor_b32 %[cnt], %[sn], 0\n" \
  "buffer_load_dwordx4 v248, %[rx], %[cnt] offen lds\n" \
  "s_add_u32 m0, %[mw], 8192\n" \
  "s_xor_b32 %[cnt], %[sn], 128\n" \
  "buffer_load_dwordx4 v248, %[rx], %[cnt] offen lds\n" \
  "v_lshl_or_b32 v253, v251, 3, %[tm]\n" \
  "buffer_load_dword v253, v253, %[rx], %[tc] offen\n" \
  "v_mfma_f32_16x16x32_bf16 v[192:195], v[168:171], v[216:219], v[192:195]\n" \
  "v_mfma_f32_16x16x32_bf16 v[196:199], v[172:175], v[216:219], v[196:199]\n" \
  "ds_read_b128 v[216:219], v252 offset:18432\n" \
  "v_mfma_f32_16x16x32_bf16 v[200:203], v[168:171], v[220:223], v[200:203]\n" \
  "v_mfma_f32_16x16x32_bf16 v[204:207], v[172:175], v[220:223], v[204:207]\n" \
  "ds_read_b128 v[220:223], v252 offset:19456\n" \
  "v_mfma_f32_16x16x32_bf16 v[192:195], v[44:47], v[224:227], v[192:195]\n" \
  "v_mfma_f32_16x16x32_bf16 v[196:199], v[108:111], v[224:227], v[196:199]\n" \
  "ds_read_b128 v[224:227], v252 offset:20480\n" \
  "v_mfma_f32_16x16x32_bf16 v[200:203], v[44:47], v[228:231], v[200:203]\n" \
  "v_mfma_f32_16x16x32_bf16 v[204:207], v[108:111], v[228:231], v[204:207]\n" \
  "ds_read_b128 v[228:231], v252 offset:21504\n" \
  "v_mfma_f32_16x16x32_bf16 v[192:195], v[240:243], v[232:235], v[192:195]\n" \
  "v_mfma_f32_16x16x32_bf16 v[196:199], v[244:247], v[232:235], v[196:199]\n" \
  "ds_read_b128 v[232:235], v252 offset:22528\n" \
  "v_mfma_f32_16x16x32_bf16 v[200:203], v[240:243], v[236:239], v[200:203]\n" \
  "v_mfma_f32_16x16x32_bf16 v[204:207], v[244:247], v[236:239], v[204:207]\n" \
  "ds_read_b128 v[236:239], v252 offset:23552\n" \
  "ds_read_b128 v[240:243], v249 offset:32768\n" \
  "ds_read_b128 v[244:247], v249 offset:33792\n" \
  "s_waitcnt lgkmcnt(9)\n" \
  "v_mfma_f32_16x16x32_bf16 v[192:195], v[48:51], v[208:211], v[192:195]\n" \
  "v_mfma_f32_16x16x32_bf16 v[196:199], v[112:115], v[208:211], v[196:199]\n" \
  "ds_read_b128 v[208:211], v252 offset:24576\n" \
  "s_waitcnt lgkmcnt(9)\n" \
  "v_mfma_f32_16x16x32_bf16 v[200:203], v[48:51], v[212:215], v[200:203]\n" \
  "v_mfma_f32_16x16x32_bf16 v[204:207], v[112:115], v[212:215], v[204:207]\n" \
  "ds_read_b128 v[212:215], v252 offset:25600\n" \
  "s_waitcnt lgkmcnt(9)\n" \
  "v_mfma_f32_16x16x32_bf16 v[192:195], v[176:179], v[216:219], v[192:195]\n" \
  "v_mfma_f32_16x16x32_bf16 v[196:199], v[180:183], v[216:219], v[196:199]\n" \
  "ds_read_b128 v[216:219], v252 offset:26624\n" \
  "s_waitcnt lgkmcnt(9)\n" \
  "v_mfma_f32_16x16x32_bf16 v[200:203], v[176:179], v[220:223], v[200:203]\n" \
  "v_mfma_f32_16x16x32_bf16 v[204:207], v[180:183], v[220:223], v[204:207]\n" \
  "ds_read_b128 v[220:223], v252 offset:27648\n" \
  "s_waitcnt lgkmcnt(9)\n" \
  "v_mfma_f32_16x16x32_bf16 v[192:195], v[52:55], v[224:227], v[192:195]\n" \
  "v_mfma_f32_16x16x32_bf16 v[196:199], v[116:119], v[224:227], v[196:199]\n" \
  "ds_read_b128 v[224:227], v252 offset:28672\n" \
  "s_waitcnt lgkmcnt(9)\n" \
  "v_mfma_f32_16x16x32_bf16 v[200:203], v[52:55], v[228:231], v[200:203]\n" \
  "v_mfma_f32_16x16x32_bf16 v[204:207], v[116:119], v[228:231], v[204:207]\n" \
  "ds_read_b128 v[228:231], v252 offset:29696\n" \
  "s_waitcnt lgkmcnt(7)\n" \
  "v_mfma_f32_16x16x32_bf16 v[192:195], v[240:243], v[232:235], v[192:195]\n" \
  "s_waitcnt lgkmcnt(6)\n" \
  "v_mfma_f32_16x16x32_bf16 v[196:199], v[244:247], v[232:235], v[196:199]\n" \
  "ds_read_b128 v[232:235], v252 offset:30720\n" \
  "v_mfma_f32_16x16x32_bf16 v[200:203], v[240:243], v[236:239], v[200:203]\n" \
  "v_mfma_f32_16x16x32_bf16 v[204:207], v[244:247], v[236:239], v[204:207]\n" \
  "ds_read_b128 v[236:239], v252 offset:31744\n" \
  "ds_read_b128 v[240:243], v249 offset:49152\n" \
  "ds_read_b128 v[244:247], v249 offset:50176\n" \
  "s_waitcnt lgkmcnt(9)\n" \
  "v_mfma_f32_16x16x32_bf16 v[192:195], v[56:59], v[208:211], v[192:195]\n" \
  "v_mfma_f32_16x16x32_bf16 v[196:199], v[120:123], v[208:211], v[196:199]\n" \
  "s_waitcnt lgkmcnt(8)\n" \
  "v_mfma_f32_16x16x32_bf16 v[200:203], v[56:59], v[212:215], v[200:203]\n" \
  "v_mfma_f32_16x16x32_bf16 v[204:207], v[120:123], v[212:215], v[204:207]\n" \
  "s_waitcnt vmcnt(1)\n" \
  "s_waitcnt lgkmcnt(0)\n" \
  "s_barrier\n" \
  "ds_read_b128 v[208:211], v252 offset:0\n" \
  "ds_read_b128 v[212:215], v252 offset:1024\n" \
  "s_add_u32 m0, %[mw], 16384\n" \
  "s_xor_b32 %[cnt], %[sn], 256\n" \
  "buffer_load_dwordx4 v248, %[rx], %[cnt] offen lds\n" \
  "s_add_u32 m0, %[mw], 24576\n" \
  "s_xor_b32 %[cnt], %[sn], 384\n" \
  "buffer_load_dwordx4 v248, %[rx], %[cnt] offen lds\n" \
  "v_mfma_f32_16x16x32_bf16 v[192:195], v[184:187], v[216:219], v[192:195]\n" \
  "v_mfma_f32_16x16x32_bf16 v[196:199], v[188:191], v[216:219], v[196:199]\n" \
  "ds_read_b128 v[216:219], v252 offset:2048\n" \
  "v_mfma_f32_16x16x32_bf16 v[200:203], v[184:187], v[220:223], v[200:203]\n" \
  "v_mfma_f32_16x16x32_bf16 v[204:207], v[188:191], v[220:223], v[204:207]\n" \
  "ds_read_b128 v[220:223], v252 offset:3072\n" \
  "v_mfma_f32_16x16x32_bf16 v[192:195], v[60:63], v[224:227], v[192:195]\n" \
  "v_mfma_f32_16x16x32_bf16 v[196:199], v[124:127], v[224:227], v[196:199]\n" \
  "ds_read_b128 v[224:227], v252 offset:4096\n" \
  "v_mfma_f32_16x16x32_bf16 v[200:203], v[60:63], v[228:231], v[200:203]\n" \
  "v_mfma_f32_16x16x32_bf16 v[204:207], v[124:127], v[228:231], v[204:207]\n" \
  "ds_read_b128 v[228:231], v252 offset:5120\n" \
  "v_mfma_f32_16x16x32_bf16 v[192:195], v[240:243], v[232:235], v[192:195]\n" \
  "v_mfma_f32_16x16x32_bf16 v[196:199], v[244:247], v[232:235], v[196:199]\n" \
  "ds_read_b128 v[232:235], v252 offset:6144\n" \
  "v_mfma_f32_16x16x32_bf16 v[200:203], v[240:243], v[236:239], v[200:203]\n" \
  "v_mfma_f32_16x16x32_bf16 v[204:207], v[244:247], v[236:239], v[204:207]\n" \
  "ds_read_b128 v[236:239], v252 offset:7168\n" \
  "s_cmp_eq_u32 %[cs], 0\n" \
  "s_cbranch_scc1 .Lq_%=\n" \
  "v_mul_f32 v192, %[cs], v192\n" \
  "v_mul_f32 v193, %[cs], v193\n" \
  "v_mul_f32 v194, %[cs], v194\n" \
  "v_mul_f32 v195, %[cs], v195\n" \
  "v_mul_f32 v196, %[cs], v196\n" \
  "v_mul_f32 v197, %[cs], v197\n" \
  "v_mul_f32 v198, %[cs], v198\n" \
  "v_mul_f32 v199, %[cs], v199\n" \
  "v_cvt_pk_bf16_f32 v192, v192, v193\n" \
  "v_cvt_pk_bf16_f32 v193, v196, v197\n" \
  "v_cvt_pk_bf16_f32 v194, v194, v195\n" \
  "v_cvt_pk_bf16_f32 v195, v198, v199\n" \
  "buffer_store_dwordx4 v[192:195], v251, %[rs], %[so] offen sc1\n" \
  "v_mul_f32 v200, %[cs], v200\n" \
  "v_mul_f32 v201, %[cs], v201\n" \
  "v_mul_f32 v202, %[cs], v202\n" \
  "v_mul_f32 v203, %[cs], v203\n" \
  "v_mul_f32 v204, %[cs], v204\n" \
  "v_mul_f32 v205, %[cs], v205\n" \
  "v_mul_f32 v206, %[cs], v206\n" \
  "v_mul_f32 v207, %[cs], v207\n" \
  "v_cvt_pk_bf16_f32 v200, v200, v201\n" \
  "v_cvt_pk_bf16_f32 v201, v204, v205\n" \
  "v_cvt_pk_bf16_f32 v202, v202, v203\n" \
  "v_cvt_pk_bf16_f32 v203, v206, v207\n" \
  "s_add_u32 %[cnt], %[so], %c[rt]\n" \
  "buffer_store_dwordx4 v[200:203], v251, %[rs], %[cnt] offen sc1\n" \
  "s_branch .Lqe_%=\n" \
  ".Lq_%=:\n" \
  "v_cvt_pk_bf16_f32 v192, v192, v193\n" \
  "v_cvt_pk_bf16_f32 v193, v196, v197\n" \
  "v_cvt_pk_bf16_f32 v194, v194, v195\n" \
  "v_cvt_pk_bf16_f32 v195, v198, v199\n" \
  "buffer_store_dwordx4 v[192:195], v251, %[rs], %[so] offen sc1\n" \
  "v_cvt_pk_bf16_f32 v200, v200, v201\n" \
  "v_cvt_pk_bf16_f32 v201, v204, v205\n" \
  "v_cvt_pk_bf16_f32 v202, v202, v203\n" \
  "v_cvt_pk_bf16_f32 v203, v206, v207\n" \
  "s_add_u32 %[cnt], %[so], %c[rt]\n" \
  "buffer_store_dwordx4 v[200:203], v251, %[rs], %[cnt] offen sc1\n" \
  ".Lqe_%=:\n" \
  "s_mov_b32 m0, %[keep]\n" \
  "s_waitcnt lgkmcnt(0)\n"
#define ST_XA \
  "s_mov_b32 %[keep], m0\n" \
  "ds_read_b128 v[240:243], v250 offset:0\n" \
  "ds_read_b128 v[244:247], v250 offset:1024\n" \
  "v_mfma_f32_16x16x32_bf16 v[192:195], v[0:3], v[208:211], 0\n" \
  "v_mfma_f32_16x16x32_bf16 v[196:199], v[64:67], v[208:211], 0\n" \
  "ds_read_b128 v[208:211], v252 offset:8192\n" \
  "v_mfma_f32_16x16x32_bf16 v[200:203], v[0:3], v[212:215], 0\n" \
  "v_mfma_f32_16x16x32_bf16 v[204:207], v[64:67], v[212:215], 0\n" \
  "ds_read_b128 v[212:215], v252 offset:9216\n" \
  "v_mfma_f32_16x16x32_bf16 v[192:195], v[128:131], v[216:219], v[192:195]\n" \
  "v_mfma_f32_16x16x32_bf16 v[196:199], v[132:135], v[216:219], v[196:199]\n" \
  "ds_read_b128 v[216:219], v252 offset:10240\n" \
  "v_mfma_f32_16x16x32_bf16 v[200:203], v[128:131], v[220:223], v[200:203]\n" \
  "v_mfma_f32_16x16x32_bf16 v[204:207], v[132:135], v[220:223], v[204:207]\n" \
  "ds_read_b128 v[220:223], v252 offset:11264\n" \
  "v_mfma_f32_16x16x32_bf16 v[192:195], v[4:7], v[224:227], v[192:195]\n" \
  "v_mfma_f32_16x16x32_bf16 v[196:199], v[68:71], v[224:227], v[196:199]\n" \
  "v_mfma_f32_16x16x32_bf16 v[200:203], v[4:7], v[228:231], v[200:203]\n" \
  "v_mfma_f32_16x16x32_bf16 v[204:207], v[68:71], v[228:231], v[204:207]\n" \
  "s_waitcnt vmcnt(0)\n" \
  "s_waitcnt lgkmcnt(0)\n" \
  "s_barrier\n" \
  "ds_read_b128 v[224:227], v252 offset:12288\n" \
  "ds_read_b128 v[228:231], v252 offset:13312\n" \
  "s_add_u32 m0, %[mw], 0\n" \
  "s_xor_b32 %[cnt], %[sc], 384\n" \
  "buffer_load_dwordx4 v248, %[rx], %[cnt] offen lds\n" \
  "s_add_u32 m0, %[mw], 4096\n" \
  "s_xor_b32 %[cnt], %[sc], 448\n" \
  "buffer_load_dwordx4 v248, %[rx], %[cnt] offen lds\n" \
  "v_mfma_f32_16x16x32_bf16 v[192:195], v[240:243], v[232:235], v[192:195]\n" \
  "v_mfma_f32_16x16x32_bf16 v[196:199], v[244:247], v[232:235], v[196:199]\n" \
  "ds_read_b128 v[232:235], v252 offset:14336\n" \
  "v_mfma_f32_16x16x32_bf16 v[200:203], v[240:243], v[236:239], v[200:203]\n" \
  "v_mfma_f32_16x16x32_bf16 v[204:207], v[244:247], v[236:239], v[204:207]\n" \
  "ds_read_b128 v[236:239], v252 offset:15360\n" \
  "ds_read_b128 v[240:243], v250 offset:16384\n" \
  "ds_read_b128 v[244:247], v250 offset:17408\n" \
  "v_mfma_f32_16x16x32_bf16 v[192:195], v[8:11], v[208:211], v[192:195]\n" \
  "v_mfma_f32_16x16x32_bf16 v[196:199], v[72:75], v[208:211], v[196:199]\n" \
  "ds_read_b128 v[208:211], v252 offset:16384\n" \
  "v_mfma_f32_16x16x32_bf16 v[200:203], v[8:11], v[212:215], v[200:203]\n" \
  "v_mfma_f32_16x16x32_bf16 v[204:207], v[72:75], v[212:215], v[204:207]\n" \
  "ds_read_b128 v[212:215], v252 offset:17408\n" \
  "v_mfma_f32_16x16x32_bf16 v[192:195], v[136:139], v[216:219], v[192:195]\n" \
  "v_mfma_f32_16x16x32_bf16 v[196:199], v[140:143], v[216:219], v[196:199]\n" \
  "ds_read_b128 v[216:219], v252 offset:18432\n" \
  "v_mfma_f32_16x16x32_bf16 v[200:203], v[136:139], v[220:223], v[200:203]\n" \
  "v_mfma_f32_16x16x32_bf16 v[204:207], v[140:143], v[220:223], v[204:207]\n" \
  "ds_read_b128 v[220:223], v252 offset:19456\n" \
  "s_waitcnt lgkmcnt(9)\n" \
  "v_mfma_f32_16x16x32_bf16 v[192:195], v[12:15], v[224:227], v[192:195]\n" \
  "v_mfma_f32_16x16x32_bf16 v[196:199], v[76:79], v[224:227], v[196:199]\n" \
  "ds_read_b128 v[224:227], v252 offset:20480\n" \
  "s_waitcnt lgkmcnt(9)\n" \
  "v_mfma_f32_16x16x32_bf16 v[200:203], v[12:15], v[228:231], v[200:203]\n" \
  "v_mfma_f32_16x16x32_bf16 v[204:207], v[76:79], v[228:231], v[204:207]\n" \
  "ds_read_b128 v[228:231], v252 offset:21504\n" \
  "s_waitcnt lgkmcnt(7)\n" \
  "v_mfma_f32_16x16x32_bf16 v[192:195], v[240:243], v[232:235], v[192:195]\n" \
  "s_waitcnt lgkmcnt(6)\n" \
  "v_mfma_f32_16x16x32_bf16 v[196:199], v[244:247], v[232:235], v[196:199]\n" \
  "ds_read_b128 v[232:235], v252 offset:22528\n" \
  "v_mfma_f32_16x16x32_bf16 v[200:203], v[240:243], v[236:239], v[200:203]\n" \
  "v_mfma_f32_16x16x32_bf16 v[204:207], v[244:247], v[236:239], v[204:207]\n" \
  "ds_read_b128 v[236:239], v252 offset:23552\n" \
  "ds_read_b128 v[240:243], v250 offset:32768\n" \
  "ds_read_b128 v[244:247], v250 offset:33792\n" \
  "s_waitcnt lgkmcnt(9)\n" \
  "v_mfma_f32_16x16x32_bf16 v[192:195], v[16:19], v[208:211], v[192:195]\n" \
  "v_mfma_f32_16x16x32_bf16 v[196:199], v[80:83], v[208:211], v[196:199]\n" \
  "s_waitcnt lgkmcnt(8)\n" \
  "v_mfma_f32_16x16x32_bf16 v[200:203], v[16:19], v[212:215], v[200:203]\n" \
  "v_mfma_f32_16x16x32_bf16 v[204:207], v[80:83], v[212:215], v[204:207]\n" \
  "s_waitcnt vmcnt(0)\n" \
  "s_waitcnt lgkmcnt(0)\n" \
  "s_barrier\n" \
  "ds_read_b128 v[208:211], v252 offset:0\n" \
  "ds_read_b128 v[212:215], v252 offset:1024\n" \
  "s_add_u32 m0, %[mw], 12288\n" \
  "s_xor_b32 %[cnt], %[sc], 512\n" \
  "buffer_load_dwordx4 v248, %[rx], %[cnt] offen lds\n" \
  "s_add_u32 m0, %[mw], 16384\n" \
  "s_xor_b32 %[cnt], %[sc], 576\n" \
  "buffer_load_dwordx4 v248, %[rx], %[cnt] offen lds\n" \
  "s_add_u32 m0, %[mw], 20480\n" \
  "s_xor_b32 %[cnt], %[sc], 640\n" \
  "buffer_load_dwordx4 v248, %[rx], %[cnt] offen lds\n" \
  "v_lshl_or_b32 v253, v251, 24, %[fm]\n" \
  "buffer_store_dword v253, v253, %[rs], %[fo] offen sc1\n" \
  "v_mfma_f32_16x16x32_bf16 v[192:195], v[144:147], v[216:219], v[192:195]\n" \
  "v_mfma_f32_16x16x32_bf16 v[196:199], v[148:151], v[216:219], v[196:199]\n" \
  "ds_read_b128 v[216:219], v252 offset:2048\n" \
  "v_mfma_f32_16x16x32_bf16 v[200:203], v[144:147], v[220:223], v[200:203]\n" \
  "v_mfma_f32_16x16x32_bf16 v[204:207], v[148:151], v[220:223], v[204:207]\n" \
  "ds_read_b128 v[220:223], v252 offset:3072\n" \
  "v_mfma_f32_16x16x32_bf16 v[192:195], v[20:23], v[224:227], v[192:195]\n" \
  "v_mfma_f32_16x16x32_bf16 v[196:199], v[84:87], v[224:227], v[196:199]\n" \
  "ds_read_b128 v[224:227], v252 offset:4096\n" \
  "v_mfma_f32_16x16x32_bf16 v[200:203], v[20:23], v[228:231], v[200:203]\n" \
  "v_mfma_f32_16x16x32_bf16 v[204:207], v[84:87], v[228:231], v[204:207]\n" \
  "ds_read_b128 v[228:231], v252 offset:5120\n" \
  "v_mfma_f32_16x16x32_bf16 v[192:195], v[240:243], v[232:235], v[192:195]\n" \
  "v_mfma_f32_16x16x32_bf16 v[196:199], v[244:247], v[232:235], v[196:199]\n" \
  "ds_read_b128 v[232:235], v252 offset:6144\n" \
  "v_mfma_f32_16x16x32_bf16 v[200:203], v[240:243], v[236:239], v[200:203]\n" \
  "v_mfma_f32_16x16x32_bf16 v[204:207], v[244:247], v[236:239], v[204:207]\n" \
  "ds_read_b128 v[236:239], v252 offset:7168\n" \
  "ds_read_b128 v[240:243], v250 offset:49152\n" \
  "ds_read_b128 v[244:247], v250 offset:50176\n" \
  "s_waitcnt lgkmcnt(9)\n" \
  "v_mfma_f32_16x16x32_bf16 v[192:195], v[24:27], v[208:211], v[192:195]\n" \
  "v_mfma_f32_16x16x32_bf16 v[196:199], v[88:91], v[208:211], v[196:199]\n" \
  "s_waitcnt lgkmcnt(8)\n" \
  "v_mfma_f32_16x16x32_bf16 v[200:203], v[24:27], v[212:215], v[200:203]\n" \
  "v_mfma_f32_16x16x32_bf16 v[204:207], v[88:91], v[212:215], v[204:207]\n" \
  "s_waitcnt vmcnt(1)\n" \
  "s_waitcnt lgkmcnt(0)\n" \
  "s_barrier\n" \
  "ds_read_b128 v[208:211], v252 offset:12288\n" \
  "ds_read_b128 v[212:215], v252 offset:13312\n" \
  "s_add_u32 m0, %[mw], 0\n" \
  "s_xor_b32 %[cnt], %[sc], 704\n" \
  "buffer_load_dwordx4 v248, %[rx], %[cnt] offen lds\n" \
  "s_add_u32 m0, %[mw], 4096\n" \
  "s_xor_b32 %[cnt], %[sc], 768\n" \
  "buffer_load_dwordx4 v248, %[rx], %[cnt] offen lds\n" \
  "s_add_u32 m0, %[mw], 8192\n" \
  "s_xor_b32 %[cnt], %[sc], 832\n" \
  "buffer_load_dwordx4 v248, %[rx], %[cnt] offen lds\n" \
  "v_lshl_or_b32 v253, v251, 3, %[tm]\n" \
  "buffer_load_dword v253, v253, %[rx], %[tc] offen\n" \
  "v_mfma_f32_16x16x32_bf16 v[192:195], v[152:155], v[216:219], v[192:195]\n" \
  "v_mfma_f32_16x16x32_bf16 v[196:199], v[156:159], v[216:219], v[196:199]\n" \
  "ds_read_b128 v[216:219], v252 offset:14336\n" \
  "v_mfma_f32_16x16x32_bf16 v[200:203], v[152:155], v[220:223], v[200:203]\n" \
  "v_mfma_f32_16x16x32_bf16 v[204:207], v[156:159], v[220:223], v[204:207]\n" \
  "ds_read_b128 v[220:223], v252 offset:15360\n" \
  "v_mfma_f32_16x16x32_bf16 v[192:195], v[28:31], v[224:227], v[192:195]\n" \
  "v_mfma_f32_16x16x32_bf16 v[196:199], v[92:95], v[224:227], v[196:199]\n" \
  "ds_read_b128 v[224:227], v252 offset:16384\n" \
  "v_mfma_f32_16x16x32_bf16 v[200:203], v[28:31], v[228:231], v[200:203]\n" \
  "v_mfma_f32_16x16x32_bf16 v[204:207], v[92:95], v[228:231], v[204:207]\n" \
  "ds_read_b128 v[228:231], v252 offset:17408\n" \
  "v_mfma_f32_16x16x32_bf16 v[192:195], v[240:243], v[232:235], v[192:195]\n" \
  "v_mfma_f32_16x16x32_bf16 v[196:199], v[244:247], v[232:235], v[196:199]\n" \
  "ds_read_b128 v[232:235], v252 offset:18432\n" \
  "v_mfma_f32_16x16x32_bf16 v[200:203], v[240:243], v[236:239], v[200:203]\n" \
  "v_mfma_f32_16x16x32_bf16 v[204:207], v[244:247], v[236:239], v[204:207]\n" \
  "ds_read_b128 v[236:239], v252 offset:19456\n" \
  "ds_read_b128 v[240:243], v249 offset:0\n" \
  "ds_read_b128 v[244:247], v249 offset:1024\n" \
  "s_waitcnt lgkmcnt(9)\n" \
  "v_mfma_f32_16x16x32_bf16 v[192:195], v[32:35], v[208:211], v[192:195]\n" \
  "v_mfma_f32_16x16x32_bf16 v[196:199], v[96:99], v[208:211], v[196:199]\n" \
  "ds_read_b128 v[208:211], v252 offset:20480\n" \
  "s_waitcnt lgkmcnt(9)\n" \
  "v_mfma_f32_16x16x32_bf16 v[200:203], v[32:35], v[212:215], v[200:203]\n" \
  "v_mfma_f32_16x16x32_bf16 v[204:207], v[96:99], v[212:215], v[204:207]\n" \
  "ds_read_b128 v[212:215], v252 offset:21504\n" \
  "s_waitcnt lgkmcnt(9)\n" \
  "v_mfma_f32_16x16x32_bf16 v[192:195], v[160:163], v[216:219], v[192:195]\n" \
  "v_mfma_f32_16x16x32_bf16 v[196:199], v[164:167], v[216:219], v[196:199]\n" \
  "ds_read_b128 v[216:219], v252 offset:22528\n" \
  "s_waitcnt lgkmcnt(9)\n" \
  "v_mfma_f32_16x16x32_bf16 v[200:203], v[160:163], v[220:223], v[200:203]\n" \
  "v_mfma_f32_16x16x32_bf16 v[204:207], v[164:167], v[220:223], v[204:207]\n" \
  "ds_read_b128 v[220:223], v252 offset:23552\n" \
  "s_waitcnt lgkmcnt(9)\n" \
  "v_mfma_f32_16x16x32_bf16 v[192:195], v[36:39], v[224:227], v[192:195]\n" \
  "v_mfma_f32_16x16x32_bf16 v[196:199], v[100:103], v[224:227], v[196:199]\n" \
  "s_waitcnt lgkmcnt(8)\n" \
  "v_mfma_f32_16x16x32_bf16 v[200:203], v[36:39], v[228:231], v[200:203]\n" \
  "v_mfma_f32_16x16x32_bf16 v[204:207], v[100:103], v[228:231], v[204:207]\n" \
  "s_waitcnt vmcnt(1)\n" \
  "s_waitcnt lgkmcnt(0)\n" \
  "s_barrier\n" \
  "ds_read_b128 v[224:227], v252 offset:0\n" \
  "ds_read_b128 v[228:231], v252 offset:1024\n" \
  "s_add_u32 m0, %[mw], 12288\n" \
  "s_xor_b32 %[cnt], %[sc], 896\n" \
  "buffer_load_dwordx4 v248, %[rx], %[cnt] offen lds\n" \
  "s_add_u32 m0, %[mw], 16384\n" \
  "s_xor_b32 %[cnt], %[sc], 960\n" \
  "buffer_load_dwordx4 v248, %[rx], %[cnt] offen lds\n" \
  "v_mfma_f32_16x16x32_bf16 v[192:195], v[240:243], v[232:235], v[192:195]\n" \
  "v_mfma_f32_16x16x32_bf16 v[196:199], v[244:247], v[232:235], v[196:199]\n" \
  "ds_read_b128 v[232:235], v252 offset:2048\n" \
  "v_mfma_f32_16x16x32_bf16 v[200:203], v[240:243], v[236:239], v[200:203]\n" \
  "v_mfma_f32_16x16x32_bf16 v[204:207], v[244:247], v[236:239], v[204:207]\n" \
  "ds_read_b128 v[236:239], v252 offset:3072\n" \
  "ds_read_b128 v[240:243], v249 offset:16384\n" \
  "ds_read_b128 v[244:247], v249 offset:17408\n" \
  "v_mfma_f32_16x16x32_bf16 v[192:195], v[40:43], v[208:211], v[192:195]\n" \
  "v_mfma_f32_16x16x32_bf16 v[196:199], v[104:107], v[208:211], v[196:199]\n" \
  "ds_read_b128 v[208:211], v252 offset:4096\n" \
  "v_mfma_f32_16x16x32_bf16 v[200:203], v[40:43], v[212:215], v[200:203]\n" \
  "v_mfma_f32_16x16x32_bf16 v[204:207], v[104:107], v[212:215], v[204:207]\n" \
  "ds_read_b128 v[212:215], v252 offset:5120\n" \
  "v_mfma_f32_16x16x32_bf16 v[192:195], v[168:171], v[216:219], v[192:195]\n" \
  "v_mfma_f32_16x16x32_bf16 v[196:199], v[172:175], v[216:219], v[196:199]\n" \
  "ds_read_b128 v[216:219], v252 offset:6144\n" \
  "v_mfma_f32_16x16x32_bf16 v[200:203], v[168:171], v[220:223], v[200:203]\n" \
  "v_mfma_f32_16x16x32_bf16 v[204:207], v[172:175], v[220:223], v[204:207]\n" \
  "ds_read_b128 v[220:223], v252 offset:7168\n" \
  "s_waitcnt lgkmcnt(9)\n" \
  "v_mfma_f32_16x16x32_bf16 v[192:195], v[44:47], v[224:227], v[192:195]\n" \
  "v_mfma_f32_16x16x32_bf16 v[196:199], v[108:111], v[224:227], v[196:199]\n" \
  "ds_read_b128 v[224:227], v252 offset:8192\n" \
  "s_waitcnt lgkmcnt(9)\n" \
  "v_mfma_f32_16x16x32_bf16 v[200:203], v[44:47], v[228:231], v[200:203]\n" \
  "v_mfma_f32_16x16x32_bf16 v[204:207], v[108:111], v[228:231], v[204:207]\n" \
  "ds_read_b128 v[228:231], v252 offset:9216\n" \
  "s_waitcnt lgkmcnt(7)\n" \
  "v_mfma_f32_16x16x32_bf16 v[192:195], v[240:243], v[232:235], v[192:195]\n" \
  "s_waitcnt lgkmcnt(6)\n" \
  "v_mfma_f32_16x16x32_bf16 v[196:199], v[244:247], v[232:235], v[196:199]\n" \
  "ds_read_b128 v[232:235], v252 offset:10240\n" \
  "v_mfma_f32_16x16x32_bf16 v[200:203], v[240:243], v[236:239], v[200:203]\n" \
  "v_mfma_f32_16x16x32_bf16 v[204:207], v[244:247], v[236:239], v[204:207]\n" \
  "ds_read_b128 v[236:239], v252 offset:11264\n" \
  "ds_read_b128 v[240:243], v249 offset:32768\n" \
  "ds_read_b128 v[244:247], v249 offset:33792\n" \
  "s_waitcnt lgkmcnt(9)\n" \
  "v_mfma_f32_16x16x32_bf16 v[192:195], v[48:51], v[208:211], v[192:195]\n" \
  "v_mfma_f32_16x16x32_bf16 v[196:199], v[112:115], v[208:211], v[196:199]\n" \
  "s_waitcnt lgkmcnt(8)\n" \
  "v_mfma_f32_16x16x32_bf16 v[200:203], v[48:51], v[212:215], v[200:203]\n" \
  "v_mfma_f32_16x16x32_bf16 v[204:207], v[112:115], v[212:215], v[204:207]\n" \
  "s_waitcnt vmcnt(0)\n" \
  "s_waitcnt lgkmcnt(0)\n" \
  "s_barrier\n" \
  "ds_read_b128 v[208:211], v252 offset:12288\n" \
  "ds_read_b128 v[212:215], v252 offset:13312\n" \
  "s_add_u32 m0, %[mw], 0\n" \
  "s_xor_b32 %[cnt], %[sn], 0\n" \
  "buffer_load_dwordx4 v248, %[rx], %[cnt] offen lds\n" \
  "s_add_u32 m0, %[mw], 4096\n" \
  "s_xor_b32 %[cnt], %[sn], 64\n" \
  "buffer_load_dwordx4 v248, %[rx], %[cnt] offen lds\n" \
  "s_add_u32 m0, %[mw], 8192\n" \
  "s_xor_b32 %[cnt], %[sn], 128\n" \
  "buffer_load_dwordx4 v248, %[rx], %[cnt] offen lds\n" \
  "v_mfma_f32_16x16x32_bf16 v[192:195], v[176:179], v[216:219], v[192:195]\n" \
  "v_mfma_f32_16x16x32_bf16 v[196:199], v[180:183], v[216:219], v[196:199]\n" \
  "ds_read_b128 v[216:219], v252 offset:14336\n" \
  "v_mfma_f32_16x16x32_bf16 v[200:203], v[176:179], v[220:223], v[200:203]\n" \
  "v_mfma_f32_16x16x32_bf16 v[204:207], v[180:183], v[220:223], v[204:207]\n" \
  "ds_read_b128 v[220:223], v252 offset:15360\n" \
  "v_mfma_f32_16x16x32_bf16 v[192:195], v[52:55], v[224:227], v[192:195]\n" \
  "v_mfma_f32_16x16x32_bf16 v[196:199], v[116:119], v[224:227], v[196:199]\n" \
  "ds_read_b128 v[224:227], v252 offset:16384\n" \
  "v_mfma_f32_16x16x32_bf16 v[200:203], v[52:55], v[228:231], v[200:203]\n" \
  "v_mfma_f32_16x16x32_bf16 v[204:207], v[116:119], v[228:231], v[204:207]\n" \
  "ds_read_b128 v[228:231], v252 offset:17408\n" \
  "v_mfma_f32_16x16x32_bf16 v[192:195], v[240:243], v[232:235], v[192:195]\n" \
  "v_mfma_f32_16x16x32_bf16 v[196:199], v[244:247], v[232:235], v[196:199]\n" \
  "ds_read_b128 v[232:235], v252 offset:18432\n" \
  "v_mfma_f32_16x16x32_bf16 v[200:203], v[240:243], v[236:239], v[200:203]\n" \
  "v_mfma_f32_16x16x32_bf16 v[204:207], v[244:247], v[236:239], v[204:207]\n" \
  "ds_read_b128 v[236:239], v252 offset:19456\n" \
  "ds_read_b128 v[240:243], v249 offset:49152\n" \
  "ds_read_b128 v[244:247], v249 offset:50176\n" \
  "s_waitcnt lgkmcnt(9)\n" \
  "v_mfma_f32_16x16x32_bf16 v[192:195], v[56:59], v[208:211], v[192:195]\n" \
  "v_mfma_f32_16x16x32_bf16 v[196:199], v[120:123], v[208:211], v[196:199]\n" \
  "s_waitcnt lgkmcnt(8)\n" \
  "v_mfma_f32_16x16x32_bf16 v[200:203], v[56:59], v[212:215], v[200:203]\n" \
  "v_mfma_f32_16x16x32_bf16 v[204:207], v[120:123], v[212:215], v[204:207]\n" \
  "s_waitcnt vmcnt(0)\n" \
  "s_waitcnt lgkmcnt(0)\n" \
  "s_barrier\n" \
  "ds_read_b128 v[208:211], v252 offset:0\n" \
  "ds_read_b128 v[212:215], v252 offset:1024\n" \
  "s_add_u32 m0, %[mw], 12288\n" \
  "s_xor_b32 %[cnt], %[sn], 192\n" \
  "buffer_load_dwordx4 v248, %[rx], %[cnt] offen lds\n" \
  "s_add_u32 m0, %[mw], 16384\n" \
  "s_xor_b32 %[cnt], %[sn], 256\n" \
  "buffer_load_dwordx4 v248, %[rx], %[cnt] offen lds\n" \
  "s_add_u32 m0, %[mw], 20480\n" \
  "s_xor_b32 %[cnt], %[sn], 320\n" \
  "buffer_load_dwordx4 v248, %[rx], %[cnt] offen lds\n" \
  "v_mfma_f32_16x16x32_bf16 v[192:195], v[184:187], v[216:219], v[192:195]\n" \
  "v_mfma_f32_16x16x32_bf16 v[196:199], v[188:191], v[216:219], v[196:199]\n" \
  "ds_read_b128 v[216:219], v252 offset:2048\n" \
  "v_mfma_f32_16x16x32_bf16 v[200:203], v[184:187], v[220:223], v[200:203]\n" \
  "v_mfma_f32_16x16x32_bf16 v[204:207], v[188:191], v[220:223], v[204:207]\n" \
  "ds_read_b128 v[220:223], v252 offset:3072\n" \
  "v_mfma_f32_16x16x32_bf16 v[192:195], v[60:63], v[224:227], v[192:195]\n" \
  "v_mfma_f32_16x16x32_bf16 v[196:199], v[124:127], v[224:227], v[196:199]\n" \
  "ds_read_b128 v[224:227], v252 offset:4096\n" \
  "v_mfma_f32_16x16x32_bf16 v[200:203], v[60:63], v[228:231], v[200:203]\n" \
  "v_mfma_f32_16x16x32_bf16 v[204:207], v[124:127], v[228:231], v[204:207]\n" \
  "ds_read_b128 v[228:231], v252 offset:5120\n" \
  "v_mfma_f32_16x16x32_bf16 v[192:195], v[240:243], v[232:235], v[192:195]\n" \
  "v_mfma_f32_16x16x32_bf16 v[196:199], v[244:247], v[232:235], v[196:199]\n" \
  "ds_read_b128 v[232:235], v252 offset:6144\n" \
  "v_mfma_f32_16x16x32_bf16 v[200:203], v[240:243], v[236:239], v[200:203]\n" \
  "v_mfma_f32_16x16x32_bf16 v[204:207], v[244:247], v[236:239], v[204:207]\n" \
  "ds_read_b128 v[236:239], v252 offset:7168\n" \
  "v_cvt_pk_bf16_f32 v192, v192, v193\n" \
  "v_cvt_pk_bf16_f32 v193, v194, v195\n" \
  "v_cvt_pk_bf16_f32 v194, v196, v197\n" \
  "v_cvt_pk_bf16_f32 v195, v198, v199\n" \
  "v_add_u32 v196, %[so], v251\n" \
  "ds_write_b128 v196, v[192:195] offset:24576\n" \
  "v_cvt_pk_bf16_f32 v200, v200, v201\n" \
  "v_cvt_pk_bf16_f32 v201, v202, v203\n" \
  "v_cvt_pk_bf16_f32 v202, v204, v205\n" \
  "v_cvt_pk_bf16_f32 v203, v206, v207\n" \
  "ds_write_b128 v196, v[200:203] offset:28672\n" \
  ".Lqe_%=:\n" \
  "s_mov_b32 m0, %[keep]\n" \
  "s_waitcnt lgkmcnt(0)\n"
#define ST_XB \
  "s_barrier\n" \
  "ds_read_b128 v[192:195], v251 offset:24576\n" \
  "ds_read_b128 v[208:211], v251 offset:28672\n" \
  "ds_read_b128 v[196:199], v251 offset:25600\n" \
  "ds_read_b128 v[212:215], v251 offset:29696\n" \
  "ds_read_b128 v[200:203], v251 offset:26624\n" \
  "ds_read_b128 v[216:219], v251 offset:30720\n" \
  "ds_read_b128 v[204:207], v251 offset:27648\n" \
  "ds_read_b128 v[220:223], v251 offset:31744\n" \
  "ds_read_b128 v[240:243], v250 offset:0\n" \
  "ds_read_b128 v[244:247], v250 offset:16384\n" \
  "s_waitcnt lgkmcnt(9)\n" \
  "v_mfma_f32_16x16x32_bf16 v[224:227], v[192:195], v[0:3], 0\n" \
  "s_waitcnt lgkmcnt(8)\n" \
  "v_mfma_f32_16x16x32_bf16 v[228:231], v[208:211], v[0:3], 0\n" \
  "s_waitcnt lgkmcnt(7)\n" \
  "v_mfma_f32_16x16x32_bf16 v[224:227], v[196:199], v[4:7], v[224:227]\n" \
  "s_waitcnt lgkmcnt(6)\n" \
  "v_mfma_f32_16x16x32_bf16 v[228:231], v[212:215], v[4:7], v[228:231]\n" \
  "s_waitcnt lgkmcnt(5)\n" \
  "v_mfma_f32_16x16x32_bf16 v[224:227], v[200:203], v[8:11], v[224:227]\n" \
  "s_waitcnt lgkmcnt(4)\n" \
  "v_mfma_f32_16x16x32_bf16 v[228:231], v[216:219], v[8:11], v[228:231]\n" \
  "s_waitcnt lgkmcnt(3)\n" \
  "v_mfma_f32_16x16x32_bf16 v[224:227], v[204:207], v[12:15], v[224:227]\n" \
  "s_waitcnt lgkmcnt(2)\n" \
  "v_mfma_f32_16x16x32_bf16 v[228:231], v[220:223], v[12:15], v[228:231]\n" \
  "v_mfma_f32_16x16x32_bf16 v[232:235], v[192:195], v[128:131], 0\n" \
  "v_mfma_f32_16x16x32_bf16 v[236:239], v[208:211], v[128:131], 0\n" \
  "v_mfma_f32_16x16x32_bf16 v[232:235], v[200:203], v[136:139], v[232:235]\n" \
  "v_cvt_pk_bf16_f32 v224, v224, v228\n" \
  "v_cvt_pk_bf16_f32 v225, v225, v229\n" \
  "v_mfma_f32_16x16x32_bf16 v[236:239], v[216:219], v[136:139], v[236:239]\n" \
  "v_cvt_pk_bf16_f32 v226, v226, v230\n" \
  "v_cvt_pk_bf16_f32 v227, v227, v231\n" \
  "s_waitcnt lgkmcnt(1)\n" \
  "v_mfma_f32_16x16x32_bf16 v[232:235], v[196:199], v[240:243], v[232:235]\n" \
  "s_add_u32 %[cnt], %[so], 0*%c[js]+0*%c[ss]\n" \
  "buffer_store_dwordx4 v[224:227], v251, %[rs], %[cnt] offen sc1\n" \
  "v_mfma_f32_16x16x32_bf16 v[236:239], v[212:215], v[240:243], v[236:239]\n" \
  "s_waitcnt lgkmcnt(0)\n" \
  "v_mfma_f32_16x16x32_bf16 v[232:235], v[204:207], v[244:247], v[232:235]\n" \
  "v_mfma_f32_16x16x32_bf16 v[236:239], v[220:223], v[244:247], v[236:239]\n" \
  "ds_read_b128 v[240:243], v250 offset:1024\n" \
  "ds_read_b128 v[244:247], v250 offset:17408\n" \
  "v_mfma_f32_16x16x32_bf16 v[224:227], v[192:195], v[64:67], 0\n" \
  "v_mfma_f32_16x16x32_bf16 v[228:231], v[208:211], v[64:67], 0\n" \
  "v_mfma_f32_16x16x32_bf16 v[224:227], v[196:199], v[68:71], v[224:227]\n" \
  "v_cvt_pk_bf16_f32 v232, v232, v236\n" \
  "v_cvt_pk_bf16_f32 v233, v233, v237\n" \
  "v_mfma_f32_16x16x32_bf16 v[228:231], v[212:215], v[68:71], v[228:231]\n" \
  "v_cvt_pk_bf16_f32 v234, v234, v238\n" \
  "v_cvt_pk_bf16_f32 v235, v235, v239\n" \
  "v_mfma_f32_16x16x32_bf16 v[224:227], v[200:203], v[72:75], v[224:227]\n" \
  "s_add_u32 %[cnt], %[so], 4*%c[js]+0*%c[ss]\n" \
  "buffer_store_dwordx4 v[232:235], v251, %[rs], %[cnt] offen sc1\n" \
  "v_mfma_f32_16x16x32_bf16 v[228:231], v[216:219], v[72:75], v[228:231]\n" \
  "v_mfma_f32_16x16x32_bf16 v[224:227], v[204:207], v[76:79], v[224:227]\n" \
  "v_mfma_f32_16x16x32_bf16 v[228:231], v[220:223], v[76:79], v[228:231]\n" \
  "s_waitcnt vmcnt(2)\n" \
  "s_barrier\n" \
  "v_mfma_f32_16x16x32_bf16 v[232:235], v[192:195], v[132:135], 0\n" \
  "v_mfma_f32_16x16x32_bf16 v[236:239], v[208:211], v[132:135], 0\n" \
  "v_mfma_f32_16x16x32_bf16 v[232:235], v[200:203], v[140:143], v[232:235]\n" \
  "v_cvt_pk_bf16_f32 v224, v224, v228\n" \
  "v_cvt_pk_bf16_f32 v225, v225, v229\n" \
  "v_mfma_f32_16x16x32_bf16 v[236:239], v[216:219], v[140:143], v[236:239]\n" \
  "v_cvt_pk_bf16_f32 v226, v226, v230\n" \
  "v_cvt_pk_bf16_f32 v227, v227, v231\n" \
  "s_waitcnt lgkmcnt(1)\n" \
  "v_mfma_f32_16x16x32_bf16 v[232:235], v[196:199], v[240:243], v[232:235]\n" \
  "s_add_u32 %[cnt], %[so], 0*%c[js]+1*%c[ss]\n" \
  "buffer_store_dwordx4 v[224:227], v251, %[rs], %[cnt] offen sc1\n" \
  "v_mfma_f32_16x16x32_bf16 v[236:239], v[212:215], v[240:243], v[236:239]\n" \
  "s_waitcnt lgkmcnt(0)\n" \
  "v_mfma_f32_16x16x32_bf16 v[232:235], v[204:207], v[244:247], v[232:235]\n" \
  "v_mfma_f32_16x16x32_bf16 v[236:239], v[220:223], v[244:247], v[236:239]\n" \
  "ds_read_b128 v[240:243], v250 offset:32768\n" \
  "ds_read_b128 v[244:247], v250 offset:49152\n" \
  "v_mfma_f32_16x16x32_bf16 v[224:227], v[192:195], v[16:19], 0\n" \
  "v_mfma_f32_16x16x32_bf16 v[228:231], v[208:211], v[16:19], 0\n" \
  "v_mfma_f32_16x16x32_bf16 v[224:227], v[196:199], v[20:23], v[224:227]\n" \
  "v_cvt_pk_bf16_f32 v232, v232, v236\n" \
  "v_cvt_pk_bf16_f32 v233, v233, v237\n" \
  "v_mfma_f32_16x16x32_bf16 v[228:231], v[212:215], v[20:23], v[228:231]\n" \
  "v_cvt_pk_bf16_f32 v234, v234, v238\n" \
  "v_cvt_pk_bf16_f32 v235, v235, v239\n" \
  "v_mfma_f32_16x16x32_bf16 v[224:227], v[200:203], v[24:27], v[224:227]\n" \
  "s_add_u32 %[cnt], %[so], 4*%c[js]+1*%c[ss]\n" \
  "buffer_store_dwordx4 v[232:235], v251, %[rs], %[cnt] offen sc1\n" \
  "v_mfma_f32_16x16x32_bf16 v[228:231], v[216:219], v[24:27], v[228:231]\n" \
  "v_mfma_f32_16x16x32_bf16 v[224:227], v[204:207], v[28:31], v[224:227]\n" \
  "v_mfma_f32_16x16x32_bf16 v[228:231], v[220:223], v[28:31], v[228:231]\n" \
  "s_barrier\n" \
  "v_mfma_f32_16x16x32_bf16 v[232:235], v[192:195], v[144:147], 0\n" \
  "v_mfma_f32_16x16x32_bf16 v[236:239], v[208:211], v[144:147], 0\n" \
  "v_mfma_f32_16x16x32_bf16 v[232:235], v[200:203], v[152:155], v[232:235]\n" \
  "v_cvt_pk_bf16_f32 v224, v224, v228\n" \
  "v_cvt_pk_bf16_f32 v225, v225, v229\n" \
  "v_mfma_f32_16x16x32_bf16 v[236:239], v[216:219], v[152:155], v[236:239]\n" \
  "v_cvt_pk_bf16_f32 v226, v226, v230\n" \
  "v_cvt_pk_bf16_f32 v227, v227, v231\n" \
  "s_waitcnt lgkmcnt(1)\n" \
  "v_mfma_f32_16x16x32_bf16 v[232:235], v[196:199], v[240:243], v[232:235]\n" \
  "s_add_u32 %[cnt], %[so2], 1*%c[js]+0*%c[ss]\n" \
  "buffer_store_dwordx4 v[224:227], v251, %[rs], %[cnt] offen sc1\n" \
  "v_mfma_f32_16x16x32_bf16 v[236:239], v[212:215], v[240:243], v[236:239]\n" \
  "s_waitcnt lgkmcnt(0)\n" \
  "v_mfma_f32_16x16x32_bf16 v[232:235], v[204:207], v[244:247], v[232:235]\n" \
  "v_mfma_f32_16x16x32_bf16 v[236:239], v[220:223], v[244:247], v[236:239]\n" \
  "ds_read_b128 v[240:243], v250 offset:33792\n" \
  "ds_read_b128 v[244:247], v250 offset:50176\n" \
  "v_mfma_f32_16x16x32_bf16 v[224:227], v[192:195], v[80:83], 0\n" \
  "v_mfma_f32_16x16x32_bf16 v[228:231], v[208:211], v[80:83], 0\n" \
  "v_mfma_f32_16x16x32_bf16 v[224:227], v[196:199], v[84:87], v[224:227]\n" \
  "v_cvt_pk_bf16_f32 v232, v232, v236\n" \
  "v_cvt_pk_bf16_f32 v233, v233, v237\n" \
  "v_mfma_f32_16x16x32_bf16 v[228:231], v[212:215], v[84:87], v[228:231]\n" \
  "v_cvt_pk_bf16_f32 v234, v234, v238\n" \
  "v_cvt_pk_bf16_f32 v235, v235, v239\n" \
  "v_mfma_f32_16x16x32_bf16 v[224:227], v[200:203], v[88:91], v[224:227]\n" \
  "s_add_u32 %[cnt], %[so2], 5*%c[js]+0*%c[ss]\n" \
  "buffer_store_dwordx4 v[232:235], v251, %[rs], %[cnt] offen sc1\n" \
  "v_mfma_f32_16x16x32_bf16 v[228:231], v[216:219], v[88:91], v[228:231]\n" \
  "v_mfma_f32_16x16x32_bf16 v[224:227], v[204:207], v[92:95], v[224:227]\n" \
  "v_mfma_f32_16x16x32_bf16 v[228:231], v[220:223], v[92:95], v[228:231]\n" \
  "v_mfma_f32_16x16x32_bf16 v[232:235], v[192:195], v[148:151], 0\n" \
  "v_mfma_f32_16x16x32_bf16 v[236:239], v[208:211], v[148:151], 0\n" \
  "v_mfma_f32_16x16x32_bf16 v[232:235], v[200:203], v[156:159], v[232:235]\n" \
  "v_cvt_pk_bf16_f32 v224, v224, v228\n" \
  "v_cvt_pk_bf16_f32 v225, v225, v229\n" \
  "v_mfma_f32_16x16x32_bf16 v[236:239], v[216:219], v[156:159], v[236:239]\n" \
  "v_cvt_pk_bf16_f32 v226, v226, v230\n" \
  "v_cvt_pk_bf16_f32 v227, v227, v231\n" \
  "s_waitcnt lgkmcnt(1)\n" \
  "v_mfma_f32_16x16x32_bf16 v[232:235], v[196:199], v[240:243], v[232:235]\n" \
  "s_add_u32 %[cnt], %[so2], 1*%c[js]+1*%c[ss]\n" \
  "buffer_store_dwordx4 v[224:227], v251, %[rs], %[cnt] offen sc1\n" \
  "v_mfma_f32_16x16x32_bf16 v[236:239], v[212:215], v[240:243], v[236:239]\n" \
  "s_waitcnt lgkmcnt(0)\n" \
  "v_mfma_f32_16x16x32_bf16 v[232:235], v[204:207], v[244:247], v[232:235]\n" \
  "v_mfma_f32_16x16x32_bf16 v[236:239], v[220:223], v[244:247], v[236:239]\n" \
  "ds_read_b128 v[240:243], v249 offset:0\n" \
  "ds_read_b128 v[244:247], v249 offset:16384\n" \
  "s_barrier\n" \
  "v_mfma_f32_16x16x32_bf16 v[224:227], v[192:195], v[32:35], 0\n" \
  "v_mfma_f32_16x16x32_bf16 v[228:231], v[208:211], v[32:35], 0\n" \
  "v_mfma_f32_16x16x32_bf16 v[224:227], v[196:199], v[36:39], v[224:227]\n" \
  "v_cvt_pk_bf16_f32 v232, v232, v236\n" \
  "v_cvt_pk_bf16_f32 v233, v233, v237\n" \
  "v_mfma_f32_16x16x32_bf16 v[228:231], v[212:215], v[36:39], v[228:231]\n" \
  "v_cvt_pk_bf16_f32 v234, v234, v238\n" \
  "v_cvt_pk_bf16_f32 v235, v235, v239\n" \
  "v_mfma_f32_16x16x32_bf16 v[224:227], v[200:203], v[40:43], v[224:227]\n" \
  "s_add_u32 %[cnt], %[so2], 5*%c[js]+1*%c[ss]\n" \
  "buffer_store_dwordx4 v[232:235], v251, %[rs], %[cnt] offen sc1\n" \
  "v_mfma_f32_16x16x32_bf16 v[228:231], v[216:219], v[40:43], v[228:231]\n" \
  "v_mfma_f32_16x16x32_bf16 v[224:227], v[204:207], v[44:47], v[224:227]\n" \
  "v_mfma_f32_16x16x32_bf16 v[228:231], v[220:223], v[44:47], v[228:231]\n" \
  "v_mfma_f32_16x16x32_bf16 v[232:235], v[192:195], v[160:163], 0\n" \
  "v_mfma_f32_16x16x32_bf16 v[236:239], v[208:211], v[160:163], 0\n" \
  "v_mfma_f32_16x16x32_bf16 v[232:235], v[200:203], v[168:171], v[232:235]\n" \
  "v_cvt_pk_bf16_f32 v224, v224, v228\n" \
  "v_cvt_pk_bf16_f32 v225, v225, v229\n" \
  "v_mfma_f32_16x16x32_bf16 v[236:239], v[216:219], v[168:171], v[236:239]\n" \
  "v_cvt_pk_bf16_f32 v226, v226, v230\n" \
  "v_cvt_pk_bf16_f32 v227, v227, v231\n" \
  "s_waitcnt lgkmcnt(1)\n" \
  "v_mfma_f32_16x16x32_bf16 v[232:235], v[196:199], v[240:243], v[232:235]\n" \
  "s_add_u32 %[cnt], %[so], 2*%c[js]+0*%c[ss]\n" \
  "buffer_store_dwordx4 v[224:227], v251, %[rs], %[cnt] offen sc1\n" \
  "v_mfma_f32_16x16x32_bf16 v[236:239], v[212:215], v[240:243], v[236:239]\n" \
  "s_waitcnt lgkmcnt(0)\n" \
  "v_mfma_f32_16x16x32_bf16 v[232:235], v[204:207], v[244:247], v[232:235]\n" \
  "v_mfma_f32_16x16x32_bf16 v[236:239], v[220:223], v[244:247], v[236:239]\n" \
  "ds_read_b128 v[240:243], v249 offset:1024\n" \
  "ds_read_b128 v[244:247], v249 offset:17408\n" \
  "v_mfma_f32_16x16x32_bf16 v[224:227], v[192:195], v[96:99], 0\n" \
  "v_mfma_f32_16x16x32_bf16 v[228:231], v[208:211], v[96:99], 0\n" \
  "v_mfma_f32_16x16x32_bf16 v[224:227], v[196:199], v[100:103], v[224:227]\n" \
  "v_cvt_pk_bf16_f32 v232, v232, v236\n" \
  "v_cvt_pk_bf16_f32 v233, v233, v237\n" \
  "v_mfma_f32_16x16x32_bf16 v[228:231], v[212:215], v[100:103], v[228:231]\n" \
  "v_cvt_pk_bf16_f32 v234, v234, v238\n" \
  "v_cvt_pk_bf16_f32 v235, v235, v239\n" \
  "v_mfma_f32_16x16x32_bf16 v[224:227], v[200:203], v[104:107], v[224:227]\n" \
  "s_add_u32 %[cnt], %[so], 6*%c[js]+0*%c[ss]\n" \
  "buffer_store_dwordx4 v[232:235], v251, %[rs], %[cnt] offen sc1\n" \
  "v_mfma_f32_16x16x32_bf16 v[228:231], v[216:219], v[104:107], v[228:231]\n" \
  "v_mfma_f32_16x16x32_bf16 v[224:227], v[204:207], v[108:111], v[224:227]\n" \
  "v_mfma_f32_16x16x32_bf16 v[228:231], v[220:223], v[108:111], v[228:231]\n" \
  "s_barrier\n" \
  "v_mfma_f32_16x16x32_bf16 v[232:235], v[192:195], v[164:167], 0\n" \
  "v_mfma_f32_16x16x32_bf16 v[236:239], v[208:211], v[164:167], 0\n" \
  "v_mfma_f32_16x16x32_bf16 v[232:235], v[200:203], v[172:175], v[232:235]\n" \
  "v_cvt_pk_bf16_f32 v224, v224, v228\n" \
  "v_cvt_pk_bf16_f32 v225, v225, v229\n" \
  "v_mfma_f32_16x16x32_bf16 v[236:239], v[216:219], v[172:175], v[236:239]\n" \
  "v_cvt_pk_bf16_f32 v226, v226, v230\n" \
  "v_cvt_pk_bf16_f32 v227, v227, v231\n" \
  "s_waitcnt lgkmcnt(1)\n" \
  "v_mfma_f32_16x16x32_bf16 v[232:235], v[196:199], v[240:243], v[232:235]\n" \
  "s_add_u32 %[cnt], %[so], 2*%c[js]+1*%c[ss]\n" \
  "buffer_store_dwordx4 v[224:227], v251, %[rs], %[cnt] offen sc1\n" \
  "v_mfma_f32_16x16x32_bf16 v[236:239], v[212:215], v[240:243], v[236:239]\n" \
  "s_waitcnt lgkmcnt(0)\n" \
  "v_mfma_f32_16x16x32_bf16 v[232:235], v[204:207], v[244:247], v[232:235]\n" \
  "v_mfma_f32_16x16x32_bf16 v[236:239], v[220:223], v[244:247], v[236:239]\n" \
  "ds_read_b128 v[240:243], v249 offset:32768\n" \
  "ds_read_b128 v[244:247], v249 offset:49152\n" \
  "v_mfma_f32_16x16x32_bf16 v[224:227], v[192:195], v[48:51], 0\n" \
  "v_mfma_f32_16x16x32_bf16 v[228:231], v[208:211], v[48:51], 0\n" \
  "v_mfma_f32_16x16x32_bf16 v[224:227], v[196:199], v[52:55], v[224:227]\n" \
  "v_cvt_pk_bf16_f32 v232, v232, v236\n" \
  "v_cvt_pk_bf16_f32 v233, v233, v237\n" \
  "v_mfma_f32_16x16x32_bf16 v[228:231], v[212:215], v[52:55], v[228:231]\n" \
  "v_cvt_pk_bf16_f32 v234, v234, v238\n" \
  "v_cvt_pk_bf16_f32 v235, v235, v239\n" \
  "v_mfma_f32_16x16x32_bf16 v[224:227], v[200:203], v[56:59], v[224:227]\n" \
  "s_add_u32 %[cnt], %[so], 6*%c[js]+1*%c[ss]\n" \
  "buffer_store_dwordx4 v[232:235], v251, %[rs], %[cnt] offen sc1\n" \
  "v_mfma_f32_16x16x32_bf16 v[228:231], v[216:219], v[56:59], v[228:231]\n" \
  "v_mfma_f32_16x16x32_bf16 v[224:227], v[204:207], v[60:63], v[224:227]\n" \
  "v_mfma_f32_16x16x32_bf16 v[228:231], v[220:223], v[60:63], v[228:231]\n" \
  "s_barrier\n" \
  "v_mfma_f32_16x16x32_bf16 v[232:235], v[192:195], v[176:179], 0\n" \
  "v_mfma_f32_16x16x32_bf16 v[236:239], v[208:211], v[176:179], 0\n" \
  "v_mfma_f32_16x16x32_bf16 v[232:235], v[200:203], v[184:187], v[232:235]\n" \
  "v_cvt_pk_bf16_f32 v224, v224, v228\n" \
  "v_cvt_pk_bf16_f32 v225, v225, v229\n" \
  "v_mfma_f32_16x16x32_bf16 v[236:239], v[216:219], v[184:187], v[236:239]\n" \
  "v_cvt_pk_bf16_f32 v226, v226, v230\n" \
  "v_cvt_pk_bf16_f32 v227, v227, v231\n" \
  "s_waitcnt lgkmcnt(1)\n" \
  "v_mfma_f32_16x16x32_bf16 v[232:235], v[196:199], v[240:243], v[232:235]\n" \
  "s_add_u32 %[cnt], %[so2], 3*%c[js]+0*%c[ss]\n" \
  "buffer_store_dwordx4 v[224:227], v251, %[rs], %[cnt] offen sc1\n" \
  "v_mfma_f32_16x16x32_bf16 v[236:239], v[212:215], v[240:243], v[236:239]\n" \
  "s_waitcnt lgkmcnt(0)\n" \
  "v_mfma_f32_16x16x32_bf16 v[232:235], v[204:207], v[244:247], v[232:235]\n" \
  "v_mfma_f32_16x16x32_bf16 v[236:239], v[220:223], v[244:247], v[236:239]\n" \
  "ds_read_b128 v[240:243], v249 offset:33792\n" \
  "ds_read_b128 v[244:247], v249 offset:50176\n" \
  "v_mfma_f32_16x16x32_bf16 v[224:227], v[192:195], v[112:115], 0\n" \
  "v_mfma_f32_16x16x32_bf16 v[228:231], v[208:211], v[112:115], 0\n" \
  "v_mfma_f32_16x16x32_bf16 v[224:227], v[196:199], v[116:119], v[224:227]\n" \
  "v_cvt_pk_bf16_f32 v232, v232, v236\n" \
  "v_cvt_pk_bf16_f32 v233, v233, v237\n" \
  "v_mfma_f32_16x16x32_bf16 v[228:231], v[212:215], v[116:119], v[228:231]\n" \
  "v_cvt_pk_bf16_f32 v234, v234, v238\n" \
  "v_cvt_pk_bf16_f32 v235, v235, v239\n" \
  "v_mfma_f32_16x16x32_bf16 v[224:227], v[200:203], v[120:123], v[224:227]\n" \
  "s_add_u32 %[cnt], %[so2], 7*%c[js]+0*%c[ss]\n" \
  "buffer_store_dwordx4 v[232:235], v251, %[rs], %[cnt] offen sc1\n" \
  "v_mfma_f32_16x16x32_bf16 v[228:231], v[216:219], v[120:123], v[228:231]\n" \
  "v_mfma_f32_16x16x32_bf16 v[224:227], v[204:207], v[124:127], v[224:227]\n" \
  "v_mfma_f32_16x16x32_bf16 v[228:231], v[220:223], v[124:127], v[228:231]\n" \
  "v_mfma_f32_16x16x32_bf16 v[232:235], v[192:195], v[180:183], 0\n" \
  "v_mfma_f32_16x16x32_bf16 v[236:239], v[208:211], v[180:183], 0\n" \
  "v_mfma_f32_16x16x32_bf16 v[232:235], v[200:203], v[188:191], v[232:235]\n" \
  "v_cvt_pk_bf16_f32 v224, v224, v228\n" \
  "v_cvt_pk_bf16_f32 v225, v225, v229\n" \
  "v_mfma_f32_16x16x32_bf16 v[236:239], v[216:219], v[188:191], v[236:239]\n" \
  "v_cvt_pk_bf16_f32 v226, v226, v230\n" \
  "v_cvt_pk_bf16_f32 v227, v227, v231\n" \
  "s_waitcnt lgkmcnt(1)\n" \
  "v_mfma_f32_16x16x32_bf16 v[232:235], v[196:199], v[240:243], v[232:235]\n" \
  "s_add_u32 %[cnt], %[so2], 3*%c[js]+1*%c[ss]\n" \
  "buffer_store_dwordx4 v[224:227], v251, %[rs], %[cnt] offen sc1\n" \
  "v_mfma_f32_16x16x32_bf16 v[236:239], v[212:215], v[240:243], v[236:239]\n" \
  "s_waitcnt lgkmcnt(0)\n" \
  "v_mfma_f32_16x16x32_bf16 v[232:235], v[204:207], v[244:247], v[232:235]\n" \
  "v_mfma_f32_16x16x32_bf16 v[236:239], v[220:223], v[244:247], v[236:239]\n" \
  "s_nop 8\n" \
  "v_cvt_pk_bf16_f32 v232, v232, v236\n" \
  "v_cvt_pk_bf16_f32 v233, v233, v237\n" \
  "v_cvt_pk_bf16_f32 v234, v234, v238\n" \
  "v_cvt_pk_bf16_f32 v235, v235, v239\n" \
  "s_add_u32 %[cnt], %[so2], 7*%c[js]+1*%c[ss]\n" \
  "buffer_store_dwordx4 v[232:235], v251, %[rs], %[cnt] offen sc1\n" \
  "s_nop 1\n"
#define ST_XB0 \
  "s_setprio 1\n" \
  "ds_read_b128 v[192:195], v251 offset:24576\n" \
  "ds_read_b128 v[208:211], v251 offset:28672\n" \
  "ds_read_b128 v[196:199], v251 offset:25600\n" \
  "ds_read_b128 v[212:215], v251 offset:29696\n" \
  "ds_read_b128 v[200:203], v251 offset:26624\n" \
  "ds_read_b128 v[216:219], v251 offset:30720\n" \
  "ds_read_b128 v[204:207], v251 offset:27648\n" \
  "ds_read_b128 v[220:223], v251 offset:31744\n" \
  "ds_read_b128 v[240:243], v250 offset:0\n" \
  "ds_read_b128 v[244:247], v250 offset:16384\n" \
  "s_waitcnt lgkmcnt(9)\n" \
  "v_mfma_f32_16x16x32_bf16 v[224:227], v[192:195], v[0:3], 0\n" \
  "s_waitcnt lgkmcnt(8)\n" \
  "v_mfma_f32_16x16x32_bf16 v[228:231], v[208:211], v[0:3], 0\n" \
  "s_waitcnt lgkmcnt(7)\n" \
  "v_mfma_f32_16x16x32_bf16 v[224:227], v[196:199], v[4:7], v[224:227]\n" \
  "s_waitcnt lgkmcnt(6)\n" \
  "v_mfma_f32_16x16x32_bf16 v[228:231], v[212:215], v[4:7], v[228:231]\n" \
  "s_waitcnt lgkmcnt(5)\n" \
  "v_mfma_f32_16x16x32_bf16 v[224:227], v[200:203], v[8:11], v[224:227]\n" \
  "s_waitcnt lgkmcnt(4)\n" \
  "v_mfma_f32_16x16x32_bf16 v[228:231], v[216:219], v[8:11], v[228:231]\n" \
  "s_waitcnt lgkmcnt(3)\n" \
  "v_mfma_f32_16x16x32_bf16 v[224:227], v[204:207], v[12:15], v[224:227]\n" \
  "s_waitcnt lgkmcnt(2)\n" \
  "v_mfma_f32_16x16x32_bf16 v[228:231], v[220:223], v[12:15], v[228:231]\n" \
  "v_mfma_f32_16x16x32_bf16 v[232:235], v[192:195], v[128:131], 0\n" \
  "v_mfma_f32_16x16x32_bf16 v[236:239], v[208:211], v[128:131], 0\n" \
  "v_mfma_f32_16x16x32_bf16 v[232:235], v[200:203], v[136:139], v[232:235]\n" \
  "v_cvt_pk_bf16_f32 v224, v224, v228\n" \
  "v_cvt_pk_bf16_f32 v225, v225, v229\n" \
  "v_mfma_f32_16x16x32_bf16 v[236:239], v[216:219], v[136:139], v[236:239]\n" \
  "v_cvt_pk_bf16_f32 v226, v226, v230\n" \
  "v_cvt_pk_bf16_f32 v227, v227, v231\n" \
  "s_waitcnt lgkmcnt(1)\n" \
  "v_mfma_f32_16x16x32_bf16 v[232:235], v[196:199], v[240:243], v[232:235]\n" \
  "s_add_u32 %[cnt], %[so], 0*%c[js]+0*%c[ss]\n" \
  "buffer_store_dwordx4 v[224:227], v251, %[rs], %[cnt] offen sc1\n" \
  "v_mfma_f32_16x16x32_bf16 v[236:239], v[212:215], v[240:243], v[236:239]\n" \
  "s_waitcnt lgkmcnt(0)\n" \
  "v_mfma_f32_16x16x32_bf16 v[232:235], v[204:207], v[244:247], v[232:235]\n" \
  "v_mfma_f32_16x16x32_bf16 v[236:239], v[220:223], v[244:247], v[236:239]\n" \
  "ds_read_b128 v[240:243], v250 offset:1024\n" \
  "ds_read_b128 v[244:247], v250 offset:17408\n" \
  "v_mfma_f32_16x16x32_bf16 v[224:227], v[192:195], v[64:67], 0\n" \
  "v_mfma_f32_16x16x32_bf16 v[228:231], v[208:211], v[64:67], 0\n" \
  "v_mfma_f32_16x16x32_bf16 v[224:227], v[196:199], v[68:71], v[224:227]\n" \
  "v_cvt_pk_bf16_f32 v232, v232, v236\n" \
  "v_cvt_pk_bf16_f32 v233, v233, v237\n" \
  "v_mfma_f32_16x16x32_bf16 v[228:231], v[212:215], v[68:71], v[228:231]\n" \
  "v_cvt_pk_bf16_f32 v234, v234, v238\n" \
  "v_cvt_pk_bf16_f32 v235, v235, v239\n" \
  "v_mfma_f32_16x16x32_bf16 v[224:227], v[200:203], v[72:75], v[224:227]\n" \
  "s_add_u32 %[cnt], %[so], 4*%c[js]+0*%c[ss]\n" \
  "buffer_store_dwordx4 v[232:235], v251, %[rs], %[cnt] offen sc1\n" \
  "v_mfma_f32_16x16x32_bf16 v[228:231], v[216:219], v[72:75], v[228:231]\n" \
  "v_mfma_f32_16x16x32_bf16 v[224:227], v[204:207], v[76:79], v[224:227]\n" \
  "v_mfma_f32_16x16x32_bf16 v[228:231], v[220:223], v[76:79], v[228:231]\n" \
  "v_mfma_f32_16x16x32_bf16 v[232:235], v[192:195], v[132:135], 0\n" \
  "v_mfma_f32_16x16x32_bf16 v[236:239], v[208:211], v[132:135], 0\n" \
  "v_mfma_f32_16x16x32_bf16 v[232:235], v[200:203], v[140:143], v[232:235]\n" \
  "v_cvt_pk_bf16_f32 v224, v224, v228\n" \
  "v_cvt_pk_bf16_f32 v225, v225, v229\n" \
  "v_mfma_f32_16x16x32_bf16 v[236:239], v[216:219], v[140:143], v[236:239]\n" \
  "v_cvt_pk_bf16_f32 v226, v226, v230\n" \
  "v_cvt_pk_bf16_f32 v227, v227, v231\n" \
  "s_waitcnt lgkmcnt(1)\n" \
  "v_mfma_f32_16x16x32_bf16 v[232:235], v[196:199], v[240:243], v[232:235]\n" \
  "s_add_u32 %[cnt], %[so], 0*%c[js]+1*%c[ss]\n" \
  "buffer_store_dwordx4 v[224:227], v251, %[rs], %[cnt] offen sc1\n" \
  "v_mfma_f32_16x16x32_bf16 v[236:239], v[212:215], v[240:243], v[236:239]\n" \
  "s_waitcnt lgkmcnt(0)\n" \
  "v_mfma_f32_16x16x32_bf16 v[232:235], v[204:207], v[244:247], v[232:235]\n" \
  "v_mfma_f32_16x16x32_bf16 v[236:239], v[220:223], v[244:247], v[236:239]\n" \
  "ds_read_b128 v[240:243], v250 offset:32768\n" \
  "ds_read_b128 v[244:247], v250 offset:49152\n" \
  "v_mfma_f32_16x16x32_bf16 v[224:227], v[192:195], v[16:19], 0\n" \
  "v_mfma_f32_16x16x32_bf16 v[228:231], v[208:211], v[16:19], 0\n" \
  "v_mfma_f32_16x16x32_bf16 v[224:227], v[196:199], v[20:23], v[224:227]\n" \
  "v_cvt_pk_bf16_f32 v232, v232, v236\n" \
  "v_cvt_pk_bf16_f32 v233, v233, v237\n" \
  "v_mfma_f32_16x16x32_bf16 v[228:231], v[212:215], v[20:23], v[228:231]\n" \
  "v_cvt_pk_bf16_f32 v234, v234, v238\n" \
  "v_cvt_pk_bf16_f32 v235, v235, v239\n" \
  "v_mfma_f32_16x16x32_bf16 v[224:227], v[200:203], v[24:27], v[224:227]\n" \
  "s_add_u32 %[cnt], %[so], 4*%c[js]+1*%c[ss]\n" \
  "buffer_store_dwordx4 v[232:235], v251, %[rs], %[cnt] offen sc1\n" \
  "v_mfma_f32_16x16x32_bf16 v[228:231], v[216:219], v[24:27], v[228:231]\n" \
  "v_mfma_f32_16x16x32_bf16 v[224:227], v[204:207], v[28:31], v[224:227]\n" \
  "v_mfma_f32_16x16x32_bf16 v[228:231], v[220:223], v[28:31], v[228:231]\n" \
  "v_mfma_f32_16x16x32_bf16 v[232:235], v[192:195], v[144:147], 0\n" \
  "v_mfma_f32_16x16x32_bf16 v[236:239], v[208:211], v[144:147], 0\n" \
  "v_mfma_f32_16x16x32_bf16 v[232:235], v[200:203], v[152:155], v[232:235]\n" \
  "v_cvt_pk_bf16_f32 v224, v224, v228\n" \
  "v_cvt_pk_bf16_f32 v225, v225, v229\n" \
  "v_mfma_f32_16x16x32_bf16 v[236:239], v[216:219], v[152:155], v[236:239]\n" \
  "v_cvt_pk_bf16_f32 v226, v226, v230\n" \
  "v_cvt_pk_bf16_f32 v227, v227, v231\n" \
  "s_waitcnt lgkmcnt(1)\n" \
  "v_mfma_f32_16x16x32_bf16 v[232:235], v[196:199], v[240:243], v[232:235]\n" \
  "s_add_u32 %[cnt], %[so2], 1*%c[js]+0*%c[ss]\n" \
  "buffer_store_dwordx4 v[224:227], v251, %[rs], %[cnt] offen sc1\n" \
  "v_mfma_f32_16x16x32_bf16 v[236:239], v[212:215], v[240:243], v[236:239]\n" \
  "s_waitcnt lgkmcnt(0)\n" \
  "v_mfma_f32_16x16x32_bf16 v[232:235], v[204:207], v[244:247], v[232:235]\n" \
  "v_mfma_f32_16x16x32_bf16 v[236:239], v[220:223], v[244:247], v[236:239]\n" \
  "ds_read_b128 v[240:243], v250 offset:33792\n" \
  "ds_read_b128 v[244:247], v250 offset:50176\n" \
  "s_cmp_eq_u32 %[ob], 0\n" \
  "s_cbranch_scc1 .Lo6_%=\n" \
  "s_barrier\n" \
  ".Lo6_%=:\n" \
  "v_mfma_f32_16x16x32_bf16 v[224:227], v[192:195], v[80:83], 0\n" \
  "v_mfma_f32_16x16x32_bf16 v[228:231], v[208:211], v[80:83], 0\n" \
  "v_mfma_f32_16x16x32_bf16 v[224:227], v[196:199], v[84:87], v[224:227]\n" \
  "v_cvt_pk_bf16_f32 v232, v232, v236\n" \
  "v_cvt_pk_bf16_f32 v233, v233, v237\n" \
  "v_mfma_f32_16x16x32_bf16 v[228:231], v[212:215], v[84:87], v[228:231]\n" \
  "v_cvt_pk_bf16_f32 v234, v234, v238\n" \
  "v_cvt_pk_bf16_f32 v235, v235, v239\n" \
  "v_mfma_f32_16x16x32_bf16 v[224:227], v[200:203], v[88:91], v[224:227]\n" \
  "s_add_u32 %[cnt], %[so2], 5*%c[js]+0*%c[ss]\n" \
  "buffer_store_dwordx4 v[232:235], v251, %[rs], %[cnt] offen sc1\n" \
  "v_mfma_f32_16x16x32_bf16 v[228:231], v[216:219], v[88:91], v[228:231]\n" \
  "v_mfma_f32_16x16x32_bf16 v[224:227], v[204:207], v[92:95], v[224:227]\n" \
  "v_mfma_f32_16x16x32_bf16 v[228:231], v[220:223], v[92:95], v[228:231]\n" \
  "v_mfma_f32_16x16x32_bf16 v[232:235], v[192:195], v[148:151], 0\n" \
  "v_mfma_f32_16x16x32_bf16 v[236:239], v[208:211], v[148:151], 0\n" \
  "v_mfma_f32_16x16x32_bf16 v[232:235], v[200:203], v[156:159], v[232:235]\n" \
  "v_cvt_pk_bf16_f32 v224, v224, v228\n" \
  "v_cvt_pk_bf16_f32 v225, v225, v229\n" \
  "v_mfma_f32_16x16x32_bf16 v[236:239], v[216:219], v[156:159], v[236:239]\n" \
  "v_cvt_pk_bf16_f32 v226, v226, v230\n" \
  "v_cvt_pk_bf16_f32 v227, v227, v231\n" \
  "s_waitcnt lgkmcnt(1)\n" \
  "v_mfma_f32_16x16x32_bf16 v[232:235], v[196:199], v[240:243], v[232:235]\n" \
  "s_add_u32 %[cnt], %[so2], 1*%c[js]+1*%c[ss]\n" \
  "buffer_store_dwordx4 v[224:227], v251, %[rs], %[cnt] offen sc1\n" \
  "v_mfma_f32_16x16x32_bf16 v[236:239], v[212:215], v[240:243], v[236:239]\n" \
  "s_waitcnt lgkmcnt(0)\n" \
  "v_mfma_f32_16x16x32_bf16 v[232:235], v[204:207], v[244:247], v[232:235]\n" \
  "v_mfma_f32_16x16x32_bf16 v[236:239], v[220:223], v[244:247], v[236:239]\n" \
  "ds_read_b128 v[240:243], v249 offset:0\n" \
  "ds_read_b128 v[244:247], v249 offset:16384\n" \
  "s_cmp_eq_u32 %[ob], 0\n" \
  "s_cbranch_scc1 .Lo8_%=\n" \
  "s_barrier\n" \
  ".Lo8_%=:\n" \
  "v_mfma_f32_16x16x32_bf16 v[224:227], v[192:195], v[32:35], 0\n" \
  "v_mfma_f32_16x16x32_bf16 v[228:231], v[208:211], v[32:35], 0\n" \
  "v_mfma_f32_16x16x32_bf16 v[224:227], v[196:199], v[36:39], v[224:227]\n" \
  "v_cvt_pk_bf16_f32 v232, v232, v236\n" \
  "v_cvt_pk_bf16_f32 v233, v233, v237\n" \
  "v_mfma_f32_16x16x32_bf16 v[228:231], v[212:215], v[36:39], v[228:231]\n" \
  "v_cvt_pk_bf16_f32 v234, v234, v238\n" \
  "v_cvt_pk_bf16_f32 v235, v235, v239\n" \
  "v_mfma_f32_16x16x32_bf16 v[224:227], v[200:203], v[40:43], v[224:227]\n" \
  "s_add_u32 %[cnt], %[so2], 5*%c[js]+1*%c[ss]\n" \
  "buffer_store_dwordx4 v[232:235], v251, %[rs], %[cnt] offen sc1\n" \
  "v_mfma_f32_16x16x32_bf16 v[228:231], v[216:219], v[40:43], v[228:231]\n" \
  "v_mfma_f32_16x16x32_bf16 v[224:227], v[204:207], v[44:47], v[224:227]\n" \
  "v_mfma_f32_16x16x32_bf16 v[228:231], v[220:223], v[44:47], v[228:231]\n" \
  "v_mfma_f32_16x16x32_bf16 v[232:235], v[192:195], v[160:163], 0\n" \
  "v_mfma_f32_16x16x32_bf16 v[236:239], v[208:211], v[160:163], 0\n" \
  "v_mfma_f32_16x16x32_bf16 v[232:235], v[200:203], v[168:171], v[232:235]\n" \
  "v_cvt_pk_bf16_f32 v224, v224, v228\n" \
  "v_cvt_pk_bf16_f32 v225, v225, v229\n" \
  "v_mfma_f32_16x16x32_bf16 v[236:239], v[216:219], v[168:171], v[236:239]\n" \
  "v_cvt_pk_bf16_f32 v226, v226, v230\n" \
  "v_cvt_pk_bf16_f32 v227, v227, v231\n" \
  "s_waitcnt lgkmcnt(1)\n" \
  "v_mfma_f32_16x16x32_bf16 v[232:235], v[196:199], v[240:243], v[232:235]\n" \
  "s_add_u32 %[cnt], %[so], 2*%c[js]+0*%c[ss]\n" \
  "buffer_store_dwordx4 v[224:227], v251, %[rs], %[cnt] offen sc1\n" \
  "v_mfma_f32_16x16x32_bf16 v[236:239], v[212:215], v[240:243], v[236:239]\n" \
  "s_waitcnt lgkmcnt(0)\n" \
  "v_mfma_f32_16x16x32_bf16 v[232:235], v[204:207], v[244:247], v[232:235]\n" \
  "v_mfma_f32_16x16x32_bf16 v[236:239], v[220:223], v[244:247], v[236:239]\n" \
  "ds_read_b128 v[240:243], v249 offset:1024\n" \
  "ds_read_b128 v[244:247], v249 offset:17408\n" \
  "v_mfma_f32_16x16x32_bf16 v[224:227], v[192:195], v[96:99], 0\n" \
  "v_mfma_f32_16x16x32_bf16 v[228:231], v[208:211], v[96:99], 0\n" \
  "v_mfma_f32_16x16x32_bf16 v[224:227], v[196:199], v[100:103], v[224:227]\n" \
  "v_cvt_pk_bf16_f32 v232, v232, v236\n" \
  "v_cvt_pk_bf16_f32 v233, v233, v237\n" \
  "v_mfma_f32_16x16x32_bf16 v[228:231], v[212:215], v[100:103], v[228:231]\n" \
  "v_cvt_pk_bf16_f32 v234, v234, v238\n" \
  "v_cvt_pk_bf16_f32 v235, v235, v239\n" \
  "v_mfma_f32_16x16x32_bf16 v[224:227], v[200:203], v[104:107], v[224:227]\n" \
  "s_add_u32 %[cnt], %[so], 6*%c[js]+0*%c[ss]\n" \
  "buffer_store_dwordx4 v[232:235], v251, %[rs], %[cnt] offen sc1\n" \
  "v_mfma_f32_16x16x32_bf16 v[228:231], v[216:219], v[104:107], v[228:231]\n" \
  "v_mfma_f32_16x16x32_bf16 v[224:227], v[204:207], v[108:111], v[224:227]\n" \
  "v_mfma_f32_16x16x32_bf16 v[228:231], v[220:223], v[108:111], v[228:231]\n" \
  "v_mfma_f32_16x16x32_bf16 v[232:235], v[192:195], v[164:167], 0\n" \
  "v_mfma_f32_16x16x32_bf16 v[236:239], v[208:211], v[164:167], 0\n" \
  "v_mfma_f32_16x16x32_bf16 v[232:235], v[200:203], v[172:175], v[232:235]\n" \
  "v_cvt_pk_bf16_f32 v224, v224, v228\n" \
  "v_cvt_pk_bf16_f32 v225, v225, v229\n" \
  "v_mfma_f32_16x16x32_bf16 v[236:239], v[216:219], v[172:175], v[236:239]\n" \
  "v_cvt_pk_bf16_f32 v226, v226, v230\n" \
  "v_cvt_pk_bf16_f32 v227, v227, v231\n" \
  "s_waitcnt lgkmcnt(1)\n" \
  "v_mfma_f32_16x16x32_bf16 v[232:235], v[196:199], v[240:243], v[232:235]\n" \
  "s_add_u32 %[cnt], %[so], 2*%c[js]+1*%c[ss]\n" \
  "buffer_store_dwordx4 v[224:227], v251, %[rs], %[cnt] offen sc1\n" \
  "v_mfma_f32_16x16x32_bf16 v[236:239], v[212:215], v[240:243], v[236:239]\n" \
  "s_waitcnt lgkmcnt(0)\n" \
  "v_mfma_f32_16x16x32_bf16 v[232:235], v[204:207], v[244:247], v[232:235]\n" \
  "v_mfma_f32_16x16x32_bf16 v[236:239], v[220:223], v[244:247], v[236:239]\n" \
  "ds_read_b128 v[240:243], v249 offset:32768\n" \
  "ds_read_b128 v[244:247], v249 offset:49152\n" \
  "s_cmp_eq_u32 %[ob], 0\n" \
  "s_cbranch_scc1 .Lo12_%=\n" \
  "s_barrier\n" \
  ".Lo12_%=:\n" \
  "v_mfma_f32_16x16x32_bf16 v[224:227], v[192:195], v[48:51], 0\n" \
  "v_mfma_f32_16x16x32_bf16 v[228:231], v[208:211], v[48:51], 0\n" \
  "v_mfma_f32_16x16x32_bf16 v[224:227], v[196:199], v[52:55], v[224:227]\n" \
  "v_cvt_pk_bf16_f32 v232, v232, v236\n" \
  "v_cvt_pk_bf16_f32 v233, v233, v237\n" \
  "v_mfma_f32_16x16x32_bf16 v[228:231], v[212:215], v[52:55], v[228:231]\n" \
  "v_cvt_pk_bf16_f32 v234, v234, v238\n" \
  "v_cvt_pk_bf16_f32 v235, v235, v239\n" \
  "v_mfma_f32_16x16x32_bf16 v[224:227], v[200:203], v[56:59], v[224:227]\n" \
  "s_add_u32 %[cnt], %[so], 6*%c[js]+1*%c[ss]\n" \
  "buffer_store_dwordx4 v[232:235], v251, %[rs], %[cnt] offen sc1\n" \
  "v_mfma_f32_16x16x32_bf16 v[228:231], v[216:219], v[56:59], v[228:231]\n" \
  "v_mfma_f32_16x16x32_bf16 v[224:227], v[204:207], v[60:63], v[224:227]\n" \
  "v_mfma_f32_16x16x32_bf16 v[228:231], v[220:223], v[60:63], v[228:231]\n" \
  "v_mfma_f32_16x16x32_bf16 v[232:235], v[192:195], v[176:179], 0\n" \
  "v_mfma_f32_16x16x32_bf16 v[236:239], v[208:211], v[176:179], 0\n" \
  "v_mfma_f32_16x16x32_bf16 v[232:235], v[200:203], v[184:187], v[232:235]\n" \
  "v_cvt_pk_bf16_f32 v224, v224, v228\n" \
  "v_cvt_pk_bf16_f32 v225, v225, v229\n" \
  "v_mfma_f32_16x16x32_bf16 v[236:239], v[216:219], v[184:187], v[236:239]\n" \
  "v_cvt_pk_bf16_f32 v226, v226, v230\n" \
  "v_cvt_pk_bf16_f32 v227, v227, v231\n" \
  "s_waitcnt lgkmcnt(1)\n" \
  "v_mfma_f32_16x16x32_bf16 v[232:235], v[196:199], v[240:243], v[232:235]\n" \
  "s_add_u32 %[cnt], %[so2], 3*%c[js]+0*%c[ss]\n" \
  "buffer_store_dwordx4 v[224:227], v251, %[rs], %[cnt] offen sc1\n" \
  "v_mfma_f32_16x16x32_bf16 v[236:239], v[212:215], v[240:243], v[236:239]\n" \
  "s_waitcnt lgkmcnt(0)\n" \
  "v_mfma_f32_16x16x32_bf16 v[232:235], v[204:207], v[244:247], v[232:235]\n" \
  "v_mfma_f32_16x16x32_bf16 v[236:239], v[220:223], v[244:247], v[236:239]\n" \
  "ds_read_b128 v[240:243], v249 offset:33792\n" \
  "ds_read_b128 v[244:247], v249 offset:50176\n" \
  "v_mfma_f32_16x16x32_bf16 v[224:227], v[192:195], v[112:115], 0\n" \
  "v_mfma_f32_16x16x32_bf16 v[228:231], v[208:211], v[112:115], 0\n" \
  "v_mfma_f32_16x16x32_bf16 v[224:227], v[196:199], v[116:119], v[224:227]\n" \
  "v_cvt_pk_bf16_f32 v232, v232, v236\n" \
  "v_cvt_pk_bf16_f32 v233, v233, v237\n" \
  "v_mfma_f32_16x16x32_bf16 v[228:231], v[212:215], v[116:119], v[228:231]\n" \
  "v_cvt_pk_bf16_f32 v234, v234, v238\n" \
  "v_cvt_pk_bf16_f32 v235, v235, v239\n" \
  "v_mfma_f32_16x16x32_bf16 v[224:227], v[200:203], v[120:123], v[224:227]\n" \
  "s_add_u32 %[cnt], %[so2], 7*%c[js]+0*%c[ss]\n" \
  "buffer_store_dwordx4 v[232:235], v251, %[rs], %[cnt] offen sc1\n" \
  "v_mfma_f32_16x16x32_bf16 v[228:231], v[216:219], v[120:123], v[228:231]\n" \
  "v_mfma_f32_16x16x32_bf16 v[224:227], v[204:207], v[124:127], v[224:227]\n" \
  "v_mfma_f32_16x16x32_bf16 v[228:231], v[220:223], v[124:127], v[228:231]\n" \
  "v_mfma_f32_16x16x32_bf16 v[232:235], v[192:195], v[180:183], 0\n" \
  "v_mfma_f32_16x16x32_bf16 v[236:239], v[208:211], v[180:183], 0\n" \
  "v_mfma_f32_16x16x32_bf16 v[232:235], v[200:203], v[188:191], v[232:235]\n" \
  "v_cvt_pk_bf16_f32 v224, v224, v228\n" \
  "v_cvt_pk_bf16_f32 v225, v225, v229\n" \
  "v_mfma_f32_16x16x32_bf16 v[236:239], v[216:219], v[188:191], v[236:239]\n" \
  "v_cvt_pk_bf16_f32 v226, v226, v230\n" \
  "v_cvt_pk_bf16_f32 v227, v227, v231\n" \
  "s_waitcnt lgkmcnt(1)\n" \
  "v_mfma_f32_16x16x32_bf16 v[232:235], v[196:199], v[240:243], v[232:235]\n" \
  "s_add_u32 %[cnt], %[so2], 3*%c[js]+1*%c[ss]\n" \
  "buffer_store_dwordx4 v[224:227], v251, %[rs], %[cnt] offen sc1\n" \
  "v_mfma_f32_16x16x32_bf16 v[236:239], v[212:215], v[240:243], v[236:239]\n" \
  "s_waitcnt lgkmcnt(0)\n" \
  "v_mfma_f32_16x16x32_bf16 v[232:235], v[204:207], v[244:247], v[232:235]\n" \
  "v_mfma_f32_16x16x32_bf16 v[236:239], v[220:223], v[244:247], v[236:239]\n" \
  "s_nop 8\n" \
  "v_cvt_pk_bf16_f32 v232, v232, v236\n" \
  "v_cvt_pk_bf16_f32 v233, v233, v237\n" \
  "v_cvt_pk_bf16_f32 v234, v234, v238\n" \
  "v_cvt_pk_bf16_f32 v235, v235, v239\n" \
  "s_add_u32 %[cnt], %[so2], 7*%c[js]+1*%c[ss]\n" \
  "buffer_store_dwordx4 v[232:235], v251, %[rs], %[cnt] offen sc1\n" \
  "s_setprio 0\n" \
  "s_nop 1\n"
#define PRO0_0 \
  "s_cmp_lg_u32 %[hb], 0\n" \
  "s_cbranch_scc1 .Ls_%=\n" \
  "v_xor_b32 %[t0], 0x0, %[pb]\n" \
  "ds_read_b64_tr_b16 v[12:13], %[t0] offset:0\n" \
  "ds_read_b64_tr_b16 v[96:97], %[t0] offset:16384\n" \
  "v_xor_b32 %[t1], 0x20, %[pb]\n" \
  "ds_read_b64_tr_b16 v[28:29], %[t1] offset:0\n" \
  "ds_read_b64_tr_b16 v[144:145], %[t1] offset:16384\n" \
  "v_xor_b32 %[t0], 0x1000, %[pb]\n" \
  "ds_read_b64_tr_b16 v[0:1], %[t0] offset:0\n" \
  "ds_read_b64_tr_b16 v[192:193], %[t0] offset:16384\n" \
  "v_xor_b32 %[t1], 0x1020, %[pb]\n" \
  "ds_read_b64_tr_b16 v[48:49], %[t1] offset:0\n" \
  "ds_read_b64_tr_b16 v[200:201], %[t1] offset:16384\n" \
  "v_xor_b32 %[t0], 0x2000, %[pb]\n" \
  "ds_read_b64_tr_b16 v[16:17], %[t0] offset:0\n" \
  "ds_read_b64_tr_b16 v[208:209], %[t0] offset:16384\n" \
  "v_xor_b32 %[t1], 0x2020, %[pb]\n" \
  "ds_read_b64_tr_b16 v[64:65], %[t1] offset:0\n" \
  "ds_read_b64_tr_b16 v[216:217], %[t1] offset:16384\n" \
  "v_xor_b32 %[t0], 0x3000, %[pb]\n" \
  "ds_read_b64_tr_b16 v[32:33], %[t0] offset:0\n" \
  "ds_read_b64_tr_b16 v[128:129], %[t0] offset:16384\n" \
  "v_xor_b32 %[t1], 0x3020, %[pb]\n" \
  "ds_read_b64_tr_b16 v[80:81], %[t1] offset:0\n" \
  "ds_read_b64_tr_b16 v[176:177], %[t1] offset:16384\n" \
  ".Ls_%=:\n" \
  "s_waitcnt lgkmcnt(0)\n"
#define PRO0_1 \
  "s_cmp_lg_u32 %[hb], 1\n" \
  "s_cbranch_scc1 .Ls_%=\n" \
  "v_xor_b32 %[t0], 0x8000, %[pb]\n" \
  "ds_read_b64_tr_b16 v[12:13], %[t0] offset:0\n" \
  "ds_read_b64_tr_b16 v[96:97], %[t0] offset:16384\n" \
  "v_xor_b32 %[t1], 0x8020, %[pb]\n" \
  "ds_read_b64_tr_b16 v[28:29], %[t1] offset:0\n" \
  "ds_read_b64_tr_b16 v[144:145], %[t1] offset:16384\n" \
  "v_xor_b32 %[t0], 0x9000, %[pb]\n" \
  "ds_read_b64_tr_b16 v[0:1], %[t0] offset:0\n" \
  "ds_read_b64_tr_b16 v[192:193], %[t0] offset:16384\n" \
  "v_xor_b32 %[t1], 0x9020, %[pb]\n" \
  "ds_read_b64_tr_b16 v[48:49], %[t1] offset:0\n" \
  "ds_read_b64_tr_b16 v[200:201], %[t1] offset:16384\n" \
  "v_xor_b32 %[t0], 0xa000, %[pb]\n" \
  "ds_read_b64_tr_b16 v[16:17], %[t0] offset:0\n" \
  "ds_read_b64_tr_b16 v[208:209], %[t0] offset:16384\n" \
  "v_xor_b32 %[t1], 0xa020, %[pb]\n" \
  "ds_read_b64_tr_b16 v[64:65], %[t1] offset:0\n" \
  "ds_read_b64_tr_b16 v[216:217], %[t1] offset:16384\n" \
  "v_xor_b32 %[t0], 0xb000, %[pb]\n" \
  "ds_read_b64_tr_b16 v[32:33], %[t0] offset:0\n" \
  "ds_read_b64_tr_b16 v[128:129], %[t0] offset:16384\n" \
  "v_xor_b32 %[t1], 0xb020, %[pb]\n" \
  "ds_read_b64_tr_b16 v[80:81], %[t1] offset:0\n" \
  "ds_read_b64_tr_b16 v[176:177], %[t1] offset:16384\n" \
  ".Ls_%=:\n" \
  "s_waitcnt lgkmcnt(0)\n"
#define PRO0_2 \
  "s_cmp_lg_u32 %[hb], 0\n" \
  "s_cbranch_scc1 .Ls_%=\n" \
  "v_xor_b32 %[t0], 0x10000, %[pb]\n" \
  "ds_read_b64_tr_b16 v[14:15], %[t0] offset:0\n" \
  "ds_read_b64_tr_b16 v[98:99], %[t0] offset:16384\n" \
  "v_xor_b32 %[t1], 0x10020, %[pb]\n" \
  "ds_read_b64_tr_b16 v[30:31], %[t1] offset:0\n" \
  "ds_read_b64_tr_b16 v[146:147], %[t1] offset:16384\n" \
  "v_xor_b32 %[t0], 0x11000, %[pb]\n" \
  "ds_read_b64_tr_b16 v[2:3], %[t0] offset:0\n" \
  "ds_read_b64_tr_b16 v[194:195], %[t0] offset:16384\n" \
  "v_xor_b32 %[t1], 0x11020, %[pb]\n" \
  "ds_read_b64_tr_b16 v[50:51], %[t1] offset:0\n" \
  "ds_read_b64_tr_b16 v[202:203], %[t1] offset:16384\n" \
  "v_xor_b32 %[t0], 0x12000, %[pb]\n" \
  "ds_read_b64_tr_b16 v[18:19], %[t0] offset:0\n" \
  "ds_read_b64_tr_b16 v[210:211], %[t0] offset:16384\n" \
  "v_xor_b32 %[t1], 0x12020, %[pb]\n" \
  "ds_read_b64_tr_b16 v[66:67], %[t1] offset:0\n" \
  "ds_read_b64_tr_b16 v[218:219], %[t1] offset:16384\n" \
  "v_xor_b32 %[t0], 0x13000, %[pb]\n" \
  "ds_read_b64_tr_b16 v[34:35], %[t0] offset:0\n" \
  "ds_read_b64_tr_b16 v[130:131], %[t0] offset:16384\n" \
  "v_xor_b32 %[t1], 0x13020, %[pb]\n" \
  "ds_read_b64_tr_b16 v[82:83], %[t1] offset:0\n" \
  "ds_read_b64_tr_b16 v[178:179], %[t1] offset:16384\n" \
  ".Ls_%=:\n" \
  "s_waitcnt lgkmcnt(0)\n"
#define PRO0_3 \
  "s_cmp_lg_u32 %[hb], 1\n" \
  "s_cbranch_scc1 .Ls_%=\n" \
  "v_xor_b32 %[t0], 0x18000, %[pb]\n" \
  "ds_read_b64_tr_b16 v[14:15], %[t0] offset:0\n" \
  "ds_read_b64_tr_b16 v[98:99], %[t0] offset:16384\n" \
  "v_xor_b32 %[t1], 0x18020, %[pb]\n" \
  "ds_read_b64_tr_b16 v[30:31], %[t1] offset:0\n" \
  "ds_read_b64_tr_b16 v[146:147], %[t1] offset:16384\n" \
  "v_xor_b32 %[t0], 0x19000, %[pb]\n" \
  "ds_read_b64_tr_b16 v[2:3], %[t0] offset:0\n" \
  "ds_read_b64_tr_b16 v[194:195], %[t0] offset:16384\n" \
  "v_xor_b32 %[t1], 0x19020, %[pb]\n" \
  "ds_read_b64_tr_b16 v[50:51], %[t1] offset:0\n" \
  "ds_read_b64_tr_b16 v[202:203], %[t1] offset:16384\n" \
  "v_xor_b32 %[t0], 0x1a000, %[pb]\n" \
  "ds_read_b64_tr_b16 v[18:19], %[t0] offset:0\n" \
  "ds_read_b64_tr_b16 v[210:211], %[t0] offset:16384\n" \
  "v_xor_b32 %[t1], 0x1a020, %[pb]\n" \
  "ds_read_b64_tr_b16 v[66:67], %[t1] offset:0\n" \
  "ds_read_b64_tr_b16 v[218:219], %[t1] offset:16384\n" \
  "v_xor_b32 %[t0], 0x1b000, %[pb]\n" \
  "ds_read_b64_tr_b16 v[34:35], %[t0] offset:0\n" \
  "ds_read_b64_tr_b16 v[130:131], %[t0] offset:16384\n" \
  "v_xor_b32 %[t1], 0x1b020, %[pb]\n" \
  "ds_read_b64_tr_b16 v[82:83], %[t1] offset:0\n" \
  "ds_read_b64_tr_b16 v[178:179], %[t1] offset:16384\n" \
  ".Ls_%=:\n" \
  "s_waitcnt lgkmcnt(0)\n"
#define PRO0_4 \
  "s_cmp_lg_u32 %[hb], 0\n" \
  "s_cbranch_scc1 .Ls_%=\n" \
  "v_xor_b32 %[t0], 0x20000, %[pb]\n" \
  "ds_read_b64_tr_b16 v[60:61], %[t0] offset:0\n" \
  "ds_read_b64_tr_b16 v[100:101], %[t0] offset:16384\n" \
  "v_xor_b32 %[t1], 0x20020, %[pb]\n" \
  "ds_read_b64_tr_b16 v[76:77], %[t1] offset:0\n" \
  "ds_read_b64_tr_b16 v[148:149], %[t1] offset:16384\n" \
  "v_xor_b32 %[t0], 0x21000, %[pb]\n" \
  "ds_read_b64_tr_b16 v[4:5], %[t0] offset:0\n" \
  "ds_read_b64_tr_b16 v[196:197], %[t0] offset:16384\n" \
  "v_xor_b32 %[t1], 0x21020, %[pb]\n" \
  "ds_read_b64_tr_b16 v[52:53], %[t1] offset:0\n" \
  "ds_read_b64_tr_b16 v[204:205], %[t1] offset:16384\n" \
  "v_xor_b32 %[t0], 0x22000, %[pb]\n" \
  "ds_read_b64_tr_b16 v[20:21], %[t0] offset:0\n" \
  "ds_read_b64_tr_b16 v[212:213], %[t0] offset:16384\n" \
  "v_xor_b32 %[t1], 0x22020, %[pb]\n" \
  "ds_read_b64_tr_b16 v[68:69], %[t1] offset:0\n" \
  "ds_read_b64_tr_b16 v[220:221], %[t1] offset:16384\n" \
  "v_xor_b32 %[t0], 0x23000, %[pb]\n" \
  "ds_read_b64_tr_b16 v[36:37], %[t0] offset:0\n" \
  "ds_read_b64_tr_b16 v[132:133], %[t0] offset:16384\n" \
  "v_xor_b32 %[t1], 0x23020, %[pb]\n" \
  "ds_read_b64_tr_b16 v[84:85], %[t1] offset:0\n" \
  "ds_read_b64_tr_b16 v[180:181], %[t1] offset:16384\n" \
  ".Ls_%=:\n" \
  "s_waitcnt lgkmcnt(0)\n"
#define PRO0_5 \
  "s_cmp_lg_u32 %[hb], 1\n" \
  "s_cbranch_scc1 .Ls_%=\n" \
  "v_xor_b32 %[t0], 0x0, %[pb]\n" \
  "ds_read_b64_tr_b16 v[60:61], %[t0] offset:0\n" \
  "ds_read_b64_tr_b16 v[100:101], %[t0] offset:16384\n" \
  "v_xor_b32 %[t1], 0x20, %[pb]\n" \
  "ds_read_b64_tr_b16 v[76:77], %[t1] offset:0\n" \
  "ds_read_b64_tr_b16 v[148:149], %[t1] offset:16384\n" \
  "v_xor_b32 %[t0], 0x1000, %[pb]\n" \
  "ds_read_b64_tr_b16 v[4:5], %[t0] offset:0\n" \
  "ds_read_b64_tr_b16 v[196:197], %[t0] offset:16384\n" \
  "v_xor_b32 %[t1], 0x1020, %[pb]\n" \
  "ds_read_b64_tr_b16 v[52:53], %[t1] offset:0\n" \
  "ds_read_b64_tr_b16 v[204:205], %[t1] offset:16384\n" \
  "v_xor_b32 %[t0], 0x2000, %[pb]\n" \
  "ds_read_b64_tr_b16 v[20:21], %[t0] offset:0\n" \
  "ds_read_b64_tr_b16 v[212:213], %[t0] offset:16384\n" \
  "v_xor_b32 %[t1], 0x2020, %[pb]\n" \
  "ds_read_b64_tr_b16 v[68:69], %[t1] offset:0\n" \
  "ds_read_b64_tr_b16 v[220:221], %[t1] offset:16384\n" \
  "v_xor_b32 %[t0], 0x3000, %[pb]\n" \
  "ds_read_b64_tr_b16 v[36:37], %[t0] offset:0\n" \
  "ds_read_b64_tr_b16 v[132:133], %[t0] offset:16384\n" \
  "v_xor_b32 %[t1], 0x3020, %[pb]\n" \
  "ds_read_b64_tr_b16 v[84:85], %[t1] offset:0\n" \
  "ds_read_b64_tr_b16 v[180:181], %[t1] offset:16384\n" \
  ".Ls_%=:\n" \
  "s_waitcnt lgkmcnt(0)\n"
#define PRO0_6 \
  "s_cmp_lg_u32 %[hb], 0\n" \
  "s_cbranch_scc1 .Ls_%=\n" \
  "v_xor_b32 %[t0], 0x8000, %[pb]\n" \
  "ds_read_b64_tr_b16 v[62:63], %[t0] offset:0\n" \
  "ds_read_b64_tr_b16 v[102:103], %[t0] offset:16384\n" \
  "v_xor_b32 %[t1], 0x8020, %[pb]\n" \
  "ds_read_b64_tr_b16 v[78:79], %[t1] offset:0\n" \
  "ds_read_b64_tr_b16 v[150:151], %[t1] offset:16384\n" \
  "v_xor_b32 %[t0], 0x9000, %[pb]\n" \
  "ds_read_b64_tr_b16 v[6:7], %[t0] offset:0\n" \
  "ds_read_b64_tr_b16 v[198:199], %[t0] offset:16384\n" \
  "v_xor_b32 %[t1], 0x9020, %[pb]\n" \
  "ds_read_b64_tr_b16 v[54:55], %[t1] offset:0\n" \
  "ds_read_b64_tr_b16 v[206:207], %[t1] offset:16384\n" \
  "v_xor_b32 %[t0], 0xa000, %[pb]\n" \
  "ds_read_b64_tr_b16 v[22:23], %[t0] offset:0\n" \
  "ds_read_b64_tr_b16 v[214:215], %[t0] offset:16384\n" \
  "v_xor_b32 %[t1], 0xa020, %[pb]\n" \
  "ds_read_b64_tr_b16 v[70:71], %[t1] offset:0\n" \
  "ds_read_b64_tr_b16 v[222:223], %[t1] offset:16384\n" \
  "v_xor_b32 %[t0], 0xb000, %[pb]\n" \
  "ds_read_b64_tr_b16 v[38:39], %[t0] offset:0\n" \
  "ds_read_b64_tr_b16 v[134:135], %[t0] offset:16384\n" \
  "v_xor_b32 %[t1], 0xb020, %[pb]\n" \
  "ds_read_b64_tr_b16 v[86:87], %[t1] offset:0\n" \
  "ds_read_b64_tr_b16 v[182:183], %[t1] offset:16384\n" \
  ".Ls_%=:\n" \
  "s_waitcnt lgkmcnt(0)\n"
#define PRO0_7 \
  "s_cmp_lg_u32 %[hb], 1\n" \
  "s_cbranch_scc1 .Ls_%=\n" \
  "v_xor_b32 %[t0], 0x10000, %[pb]\n" \
  "ds_read_b64_tr_b16 v[62:63], %[t0] offset:0\n" \
  "ds_read_b64_tr_b16 v[102:103], %[t0] offset:16384\n" \
  "v_xor_b32 %[t1], 0x10020, %[pb]\n" \
  "ds_read_b64_tr_b16 v[78:79], %[t1] offset:0\n" \
  "ds_read_b64_tr_b16 v[150:151], %[t1] offset:16384\n" \
  "v_xor_b32 %[t0], 0x11000, %[pb]\n" \
  "ds_read_b64_tr_b16 v[6:7], %[t0] offset:0\n" \
  "ds_read_b64_tr_b16 v[198:199], %[t0] offset:16384\n" \
  "v_xor_b32 %[t1], 0x11020, %[pb]\n" \
  "ds_read_b64_tr_b16 v[54:55], %[t1] offset:0\n" \
  "ds_read_b64_tr_b16 v[206:207], %[t1] offset:16384\n" \
  "v_xor_b32 %[t0], 0x12000, %[pb]\n" \
  "ds_read_b64_tr_b16 v[22:23], %[t0] offset:0\n" \
  "ds_read_b64_tr_b16 v[214:215], %[t0] offset:16384\n" \
  "v_xor_b32 %[t1], 0x12020, %[pb]\n" \
  "ds_read_b64_tr_b16 v[70:71], %[t1] offset:0\n" \
  "ds_read_b64_tr_b16 v[222:223], %[t1] offset:16384\n" \
  "v_xor_b32 %[t0], 0x13000, %[pb]\n" \
  "ds_read_b64_tr_b16 v[38:39], %[t0] offset:0\n" \
  "ds_read_b64_tr_b16 v[134:135], %[t0] offset:16384\n" \
  "v_xor_b32 %[t1], 0x13020, %[pb]\n" \
  "ds_read_b64_tr_b16 v[86:87], %[t1] offset:0\n" \
  "ds_read_b64_tr_b16 v[182:183], %[t1] offset:16384\n" \
  ".Ls_%=:\n" \
  "s_waitcnt lgkmcnt(0)\n"
#define PRO0_8 \
  "s_cmp_lg_u32 %[hb], 0\n" \
  "s_cbranch_scc1 .Ls_%=\n" \
  "v_xor_b32 %[t0], 0x18000, %[pb]\n" \
  "ds_read_b64_tr_b16 v[224:225], %[t0] offset:0\n" \
  "ds_read_b64_tr_b16 v[104:105], %[t0] offset:16384\n" \
  "v_xor_b32 %[t1], 0x18020, %[pb]\n" \
  "ds_read_b64_tr_b16 v[232:233], %[t1] offset:0\n" \
  "ds_read_b64_tr_b16 v[152:153], %[t1] offset:16384\n" \
  "v_xor_b32 %[t0], 0x19000, %[pb]\n" \
  "ds_read_b64_tr_b16 v[8:9], %[t0] offset:0\n" \
  "ds_read_b64_tr_b16 v[112:113], %[t0] offset:16384\n" \
  "v_xor_b32 %[t1], 0x19020, %[pb]\n" \
  "ds_read_b64_tr_b16 v[56:57], %[t1] offset:0\n" \
  "ds_read_b64_tr_b16 v[160:161], %[t1] offset:16384\n" \
  "v_xor_b32 %[t0], 0x1a000, %[pb]\n" \
  "ds_read_b64_tr_b16 v[24:25], %[t0] offset:0\n" \
  "ds_read_b64_tr_b16 v[120:121], %[t0] offset:16384\n" \
  "v_xor_b32 %[t1], 0x1a020, %[pb]\n" \
  "ds_read_b64_tr_b16 v[72:73], %[t1] offset:0\n" \
  "ds_read_b64_tr_b16 v[168:169], %[t1] offset:16384\n" \
  "v_xor_b32 %[t0], 0x1b000, %[pb]\n" \
  "ds_read_b64_tr_b16 v[40:41], %[t0] offset:0\n" \
  "ds_read_b64_tr_b16 v[136:137], %[t0] offset:16384\n" \
  "v_xor_b32 %[t1], 0x1b020, %[pb]\n" \
  "ds_read_b64_tr_b16 v[88:89], %[t1] offset:0\n" \
  "ds_read_b64_tr_b16 v[184:185], %[t1] offset:16384\n" \
  ".Ls_%=:\n" \
  "s_waitcnt lgkmcnt(0)\n"
#define PRO0_9 \
  "s_cmp_lg_u32 %[hb], 1\n" \
  "s_cbranch_scc1 .Ls_%=\n" \
  "v_xor_b32 %[t0], 0x20000, %[pb]\n" \
  "ds_read_b64_tr_b16 v[224:225], %[t0] offset:0\n" \
  "ds_read_b64_tr_b16 v[104:105], %[t0] offset:16384\n" \
  "v_xor_b32 %[t1], 0x20020, %[pb]\n" \
  "ds_read_b64_tr_b16 v[232:233], %[t1] offset:0\n" \
  "ds_read_b64_tr_b16 v[152:153], %[t1] offset:16384\n" \
  "v_xor_b32 %[t0], 0x21000, %[pb]\n" \
  "ds_read_b64_tr_b16 v[8:9], %[t0] offset:0\n" \
  "ds_read_b64_tr_b16 v[112:113], %[t0] offset:16384\n" \
  "v_xor_b32 %[t1], 0x21020, %[pb]\n" \
  "ds_read_b64_tr_b16 v[56:57], %[t1] offset:0\n" \
  "ds_read_b64_tr_b16 v[160:161], %[t1] offset:16384\n" \
  "v_xor_b32 %[t0], 0x22000, %[pb]\n" \
  "ds_read_b64_tr_b16 v[24:25], %[t0] offset:0\n" \
  "ds_read_b64_tr_b16 v[120:121], %[t0] offset:16384\n" \
  "v_xor_b32 %[t1], 0x22020, %[pb]\n" \
  "ds_read_b64_tr_b16 v[72:73], %[t1] offset:0\n" \
  "ds_read_b64_tr_b16 v[168:169], %[t1] offset:16384\n" \
  "v_xor_b32 %[t0], 0x23000, %[pb]\n" \
  "ds_read_b64_tr_b16 v[40:41], %[t0] offset:0\n" \
  "ds_read_b64_tr_b16 v[136:137], %[t0] offset:16384\n" \
  "v_xor_b32 %[t1], 0x23020, %[pb]\n" \
  "ds_read_b64_tr_b16 v[88:89], %[t1] offset:0\n" \
  "ds_read_b64_tr_b16 v[184:185], %[t1] offset:16384\n" \
  ".Ls_%=:\n" \
  "s_waitcnt lgkmcnt(0)\n"
#define PRO0_10 \
  "s_cmp_lg_u32 %[hb], 0\n" \
  "s_cbranch_scc1 .Ls_%=\n" \
  "v_xor_b32 %[t0], 0x0, %[pb]\n" \
  "ds_read_b64_tr_b16 v[226:227], %[t0] offset:0\n" \
  "ds_read_b64_tr_b16 v[106:107], %[t0] offset:16384\n" \
  "v_xor_b32 %[t1], 0x20, %[pb]\n" \
  "ds_read_b64_tr_b16 v[234:235], %[t1] offset:0\n" \
  "ds_read_b64_tr_b16 v[154:155], %[t1] offset:16384\n" \
  "v_xor_b32 %[t0], 0x1000, %[pb]\n" \
  "ds_read_b64_tr_b16 v[10:11], %[t0] offset:0\n" \
  "ds_read_b64_tr_b16 v[114:115], %[t0] offset:16384\n" \
  "v_xor_b32 %[t1], 0x1020, %[pb]\n" \
  "ds_read_b64_tr_b16 v[58:59], %[t1] offset:0\n" \
  "ds_read_b64_tr_b16 v[162:163], %[t1] offset:16384\n" \
  "v_xor_b32 %[t0], 0x2000, %[pb]\n" \
  "ds_read_b64_tr_b16 v[26:27], %[t0] offset:0\n" \
  "ds_read_b64_tr_b16 v[122:123], %[t0] offset:16384\n" \
  "v_xor_b32 %[t1], 0x2020, %[pb]\n" \
  "ds_read_b64_tr_b16 v[74:75], %[t1] offset:0\n" \
  "ds_read_b64_tr_b16 v[170:171], %[t1] offset:16384\n" \
  "v_xor_b32 %[t0], 0x3000, %[pb]\n" \
  "ds_read_b64_tr_b16 v[42:43], %[t0] offset:0\n" \
  "ds_read_b64_tr_b16 v[138:139], %[t0] offset:16384\n" \
  "v_xor_b32 %[t1], 0x3020, %[pb]\n" \
  "ds_read_b64_tr_b16 v[90:91], %[t1] offset:0\n" \
  "ds_read_b64_tr_b16 v[186:187], %[t1] offset:16384\n" \
  ".Ls_%=:\n" \
  "s_waitcnt lgkmcnt(0)\n"
#define PRO0_11 \
  "s_cmp_lg_u32 %[hb], 1\n" \
  "s_cbranch_scc1 .Ls_%=\n" \
  "v_xor_b32 %[t0], 0x8000, %[pb]\n" \
  "ds_read_b64_tr_b16 v[226:227], %[t0] offset:0\n" \
  "ds_read_b64_tr_b16 v[106:107], %[t0] offset:16384\n" \
  "v_xor_b32 %[t1], 0x8020, %[pb]\n" \
  "ds_read_b64_tr_b16 v[234:235], %[t1] offset:0\n" \
  "ds_read_b64_tr_b16 v[154:155], %[t1] offset:16384\n" \
  "v_xor_b32 %[t0], 0x9000, %[pb]\n" \
  "ds_read_b64_tr_b16 v[10:11], %[t0] offset:0\n" \
  "ds_read_b64_tr_b16 v[114:115], %[t0] offset:16384\n" \
  "v_xor_b32 %[t1], 0x9020, %[pb]\n" \
  "ds_read_b64_tr_b16 v[58:59], %[t1] offset:0\n" \
  "ds_read_b64_tr_b16 v[162:163], %[t1] offset:16384\n" \
  "v_xor_b32 %[t0], 0xa000, %[pb]\n" \
  "ds_read_b64_tr_b16 v[26:27], %[t0] offset:0\n" \
  "ds_read_b64_tr_b16 v[122:123], %[t0] offset:16384\n" \
  "v_xor_b32 %[t1], 0xa020, %[pb]\n" \
  "ds_read_b64_tr_b16 v[74:75], %[t1] offset:0\n" \
  "ds_read_b64_tr_b16 v[170:171], %[t1] offset:16384\n" \
  "v_xor_b32 %[t0], 0xb000, %[pb]\n" \
  "ds_read_b64_tr_b16 v[42:43], %[t0] offset:0\n" \
  "ds_read_b64_tr_b16 v[138:139], %[t0] offset:16384\n" \
  "v_xor_b32 %[t1], 0xb020, %[pb]\n" \
  "ds_read_b64_tr_b16 v[90:91], %[t1] offset:0\n" \
  "ds_read_b64_tr_b16 v[186:187], %[t1] offset:16384\n" \
  ".Ls_%=:\n" \
  "s_waitcnt lgkmcnt(0)\n"
#define PRO0_12 \
  "v_add_u32 %[t0], 0x8000, %[b0]\n" \
  "ds_write_b128 %[t0], v[12:15]\n" \
  "ds_write_b128 %[t0], v[60:63] offset:1024\n" \
  "v_add_u32 %[t0], 0xc000, %[b0]\n" \
  "ds_write_b128 %[t0], v[28:31]\n" \
  "ds_write_b128 %[t0], v[76:79] offset:1024\n" \
  "s_cmp_lg_u32 %[hb], 0\n" \
  "s_cbranch_scc1 .Ls_%=\n" \
  "v_xor_b32 %[t0], 0x10000, %[pb]\n" \
  "ds_read_b64_tr_b16 v[228:229], %[t0] offset:0\n" \
  "ds_read_b64_tr_b16 v[108:109], %[t0] offset:16384\n" \
  "v_xor_b32 %[t1], 0x10020, %[pb]\n" \
  "ds_read_b64_tr_b16 v[236:237], %[t1] offset:0\n" \
  "ds_read_b64_tr_b16 v[156:157], %[t1] offset:16384\n" \
  "v_xor_b32 %[t0], 0x11000, %[pb]\n" \
  "ds_read_b64_tr_b16 v[12:13], %[t0] offset:0\n" \
  "ds_read_b64_tr_b16 v[116:117], %[t0] offset:16384\n" \
  "v_xor_b32 %[t1], 0x11020, %[pb]\n" \
  "ds_read_b64_tr_b16 v[60:61], %[t1] offset:0\n" \
  "ds_read_b64_tr_b16 v[164:165], %[t1] offset:16384\n" \
  "v_xor_b32 %[t0], 0x12000, %[pb]\n" \
  "ds_read_b64_tr_b16 v[28:29], %[t0] offset:0\n" \
  "ds_read_b64_tr_b16 v[124:125], %[t0] offset:16384\n" \
  "v_xor_b32 %[t1], 0x12020, %[pb]\n" \
  "ds_read_b64_tr_b16 v[76:77], %[t1] offset:0\n" \
  "ds_read_b64_tr_b16 v[172:173], %[t1] offset:16384\n" \
  "v_xor_b32 %[t0], 0x13000, %[pb]\n" \
  "ds_read_b64_tr_b16 v[44:45], %[t0] offset:0\n" \
  "ds_read_b64_tr_b16 v[140:141], %[t0] offset:16384\n" \
  "v_xor_b32 %[t1], 0x13020, %[pb]\n" \
  "ds_read_b64_tr_b16 v[92:93], %[t1] offset:0\n" \
  "ds_read_b64_tr_b16 v[188:189], %[t1] offset:16384\n" \
  ".Ls_%=:\n" \
  "s_waitcnt lgkmcnt(0)\n"
#define PRO0_13 \
  "v_add_u32 %[t0], 0x10000, %[b0]\n" \
  "ds_write_b128 %[t0], v[192:195]\n" \
  "ds_write_b128 %[t0], v[196:199] offset:1024\n" \
  "v_add_u32 %[t0], 0x14000, %[b0]\n" \
  "ds_write_b128 %[t0], v[200:203]\n" \
  "ds_write_b128 %[t0], v[204:207] offset:1024\n" \
  "s_cmp_lg_u32 %[hb], 1\n" \
  "s_cbranch_scc1 .Ls_%=\n" \
  "v_xor_b32 %[t0], 0x18000, %[pb]\n" \
  "ds_read_b64_tr_b16 v[228:229], %[t0] offset:0\n" \
  "ds_read_b64_tr_b16 v[108:109], %[t0] offset:16384\n" \
  "v_xor_b32 %[t1], 0x18020, %[pb]\n" \
  "ds_read_b64_tr_b16 v[236:237], %[t1] offset:0\n" \
  "ds_read_b64_tr_b16 v[156:157], %[t1] offset:16384\n" \
  "v_xor_b32 %[t0], 0x19000, %[pb]\n" \
  "ds_read_b64_tr_b16 v[12:13], %[t0] offset:0\n" \
  "ds_read_b64_tr_b16 v[116:117], %[t0] offset:16384\n" \
  "v_xor_b32 %[t1], 0x19020, %[pb]\n" \
  "ds_read_b64_tr_b16 v[60:61], %[t1] offset:0\n" \
  "ds_read_b64_tr_b16 v[164:165], %[t1] offset:16384\n" \
  "v_xor_b32 %[t0], 0x1a000, %[pb]\n" \
  "ds_read_b64_tr_b16 v[28:29], %[t0] offset:0\n" \
  "ds_read_b64_tr_b16 v[124:125], %[t0] offset:16384\n" \
  "v_xor_b32 %[t1], 0x1a020, %[pb]\n" \
  "ds_read_b64_tr_b16 v[76:77], %[t1] offset:0\n" \
  "ds_read_b64_tr_b16 v[172:173], %[t1] offset:16384\n" \
  "v_xor_b32 %[t0], 0x1b000, %[pb]\n" \
  "ds_read_b64_tr_b16 v[44:45], %[t0] offset:0\n" \
  "ds_read_b64_tr_b16 v[140:141], %[t0] offset:16384\n" \
  "v_xor_b32 %[t1], 0x1b020, %[pb]\n" \
  "ds_read_b64_tr_b16 v[92:93], %[t1] offset:0\n" \
  "ds_read_b64_tr_b16 v[188:189], %[t1] offset:16384\n" \
  ".Ls_%=:\n" \
  "s_waitcnt lgkmcnt(0)\n"
#define PRO0_14 \
  "v_add_u32 %[t0], 0x18000, %[b0]\n" \
  "ds_write_b128 %[t0], v[208:211]\n" \
  "ds_write_b128 %[t0], v[212:215] offset:1024\n" \
  "v_add_u32 %[t0], 0x1c000, %[b0]\n" \
  "ds_write_b128 %[t0], v[216:219]\n" \
  "ds_write_b128 %[t0], v[220:223] offset:1024\n" \
  "s_cmp_lg_u32 %[hb], 0\n" \
  "s_cbranch_scc1 .Ls_%=\n" \
  "v_xor_b32 %[t0], 0x20000, %[pb]\n" \
  "ds_read_b64_tr_b16 v[230:231], %[t0] offset:0\n" \
  "ds_read_b64_tr_b16 v[110:111], %[t0] offset:16384\n" \
  "v_xor_b32 %[t1], 0x20020, %[pb]\n" \
  "ds_read_b64_tr_b16 v[238:239], %[t1] offset:0\n" \
  "ds_read_b64_tr_b16 v[158:159], %[t1] offset:16384\n" \
  "v_xor_b32 %[t0], 0x21000, %[pb]\n" \
  "ds_read_b64_tr_b16 v[14:15], %[t0] offset:0\n" \
  "ds_read_b64_tr_b16 v[118:119], %[t0] offset:16384\n" \
  "v_xor_b32 %[t1], 0x21020, %[pb]\n" \
  "ds_read_b64_tr_b16 v[62:63], %[t1] offset:0\n" \
  "ds_read_b64_tr_b16 v[166:167], %[t1] offset:16384\n" \
  "v_xor_b32 %[t0], 0x22000, %[pb]\n" \
  "ds_read_b64_tr_b16 v[30:31], %[t0] offset:0\n" \
  "ds_read_b64_tr_b16 v[126:127], %[t0] offset:16384\n" \
  "v_xor_b32 %[t1], 0x22020, %[pb]\n" \
  "ds_read_b64_tr_b16 v[78:79], %[t1] offset:0\n" \
  "ds_read_b64_tr_b16 v[174:175], %[t1] offset:16384\n" \
  "v_xor_b32 %[t0], 0x23000, %[pb]\n" \
  "ds_read_b64_tr_b16 v[46:47], %[t0] offset:0\n" \
  "ds_read_b64_tr_b16 v[142:143], %[t0] offset:16384\n" \
  "v_xor_b32 %[t1], 0x23020, %[pb]\n" \
  "ds_read_b64_tr_b16 v[94:95], %[t1] offset:0\n" \
  "ds_read_b64_tr_b16 v[190:191], %[t1] offset:16384\n" \
  ".Ls_%=:\n" \
  "s_waitcnt lgkmcnt(0)\n"
#define PRO0_15 \
  "s_cmp_lg_u32 %[hb], 1\n" \
  "s_cbranch_scc1 .Ls_%=\n" \
  "v_xor_b32 %[t0], 0x0, %[pb]\n" \
  "ds_read_b64_tr_b16 v[230:231], %[t0] offset:0\n" \
  "ds_read_b64_tr_b16 v[110:111], %[t0] offset:16384\n" \
  "v_xor_b32 %[t1], 0x20, %[pb]\n" \
  "ds_read_b64_tr_b16 v[238:239], %[t1] offset:0\n" \
  "ds_read_b64_tr_b16 v[158:159], %[t1] offset:16384\n" \
  "v_xor_b32 %[t0], 0x1000, %[pb]\n" \
  "ds_read_b64_tr_b16 v[14:15], %[t0] offset:0\n" \
  "ds_read_b64_tr_b16 v[118:119], %[t0] offset:16384\n" \
  "v_xor_b32 %[t1], 0x1020, %[pb]\n" \
  "ds_read_b64_tr_b16 v[62:63], %[t1] offset:0\n" \
  "ds_read_b64_tr_b16 v[166:167], %[t1] offset:16384\n" \
  "v_xor_b32 %[t0], 0x2000, %[pb]\n" \
  "ds_read_b64_tr_b16 v[30:31], %[t0] offset:0\n" \
  "ds_read_b64_tr_b16 v[126:127], %[t0] offset:16384\n" \
  "v_xor_b32 %[t1], 0x2020, %[pb]\n" \
  "ds_read_b64_tr_b16 v[78:79], %[t1] offset:0\n" \
  "ds_read_b64_tr_b16 v[174:175], %[t1] offset:16384\n" \
  "v_xor_b32 %[t0], 0x3000, %[pb]\n" \
  "ds_read_b64_tr_b16 v[46:47], %[t0] offset:0\n" \
  "ds_read_b64_tr_b16 v[142:143], %[t0] offset:16384\n" \
  "v_xor_b32 %[t1], 0x3020, %[pb]\n" \
  "ds_read_b64_tr_b16 v[94:95], %[t1] offset:0\n" \
  "ds_read_b64_tr_b16 v[190:191], %[t1] offset:16384\n" \
  ".Ls_%=:\n" \
  "s_waitcnt lgkmcnt(0)\n" \
  "v_add_u32 %[t0], 0x20000, %[b0]\n" \
  "ds_write_b128 %[t0], v[224:227]\n" \
  "ds_write_b128 %[t0], v[228:231] offset:1024\n" \
  "v_add_u32 %[t0], 0x24000, %[b0]\n" \
  "ds_write_b128 %[t0], v[232:235]\n" \
  "ds_write_b128 %[t0], v[236:239] offset:1024\n"
#define PRO1_0 \
  "v_add_u32 %[t0], 0x0, %[pa]\n" \
  "ds_read_b64_tr_b16 v[96:97], %[t0] offset:0\n" \
  "ds_read_b64_tr_b16 v[100:101], %[t0] offset:1024\n" \
  "ds_read_b64_tr_b16 v[104:105], %[t0] offset:8192\n" \
  "ds_read_b64_tr_b16 v[108:109], %[t0] offset:9216\n" \
  "s_cmp_lg_u32 %[hb], 0\n" \
  "s_cbranch_scc1 .Ls_%=\n" \
  "v_xor_b32 %[t0], 0x0, %[pb]\n" \
  "ds_read_b64_tr_b16 v[192:193], %[t0] offset:0\n" \
  "v_xor_b32 %[t1], 0x80, %[pb]\n" \
  "ds_read_b64_tr_b16 v[200:201], %[t1] offset:0\n" \
  "v_xor_b32 %[t0], 0x1000, %[pb]\n" \
  "ds_read_b64_tr_b16 v[0:1], %[t0] offset:0\n" \
  "v_xor_b32 %[t1], 0x1080, %[pb]\n" \
  "ds_read_b64_tr_b16 v[48:49], %[t1] offset:0\n" \
  "v_xor_b32 %[t0], 0x2000, %[pb]\n" \
  "ds_read_b64_tr_b16 v[16:17], %[t0] offset:0\n" \
  "v_xor_b32 %[t1], 0x2080, %[pb]\n" \
  "ds_read_b64_tr_b16 v[64:65], %[t1] offset:0\n" \
  "v_xor_b32 %[t0], 0x3000, %[pb]\n" \
  "ds_read_b64_tr_b16 v[32:33], %[t0] offset:0\n" \
  "v_xor_b32 %[t1], 0x3080, %[pb]\n" \
  "ds_read_b64_tr_b16 v[80:81], %[t1] offset:0\n" \
  ".Ls_%=:\n" \
  "s_waitcnt lgkmcnt(0)\n"
#define PRO1_1 \
  "v_add_u32 %[t0], 0x8000, %[pa]\n" \
  "ds_read_b64_tr_b16 v[98:99], %[t0] offset:0\n" \
  "ds_read_b64_tr_b16 v[102:103], %[t0] offset:1024\n" \
  "ds_read_b64_tr_b16 v[106:107], %[t0] offset:8192\n" \
  "ds_read_b64_tr_b16 v[110:111], %[t0] offset:9216\n" \
  "s_cmp_lg_u32 %[hb], 1\n" \
  "s_cbranch_scc1 .Ls_%=\n" \
  "v_xor_b32 %[t0], 0x8000, %[pb]\n" \
  "ds_read_b64_tr_b16 v[192:193], %[t0] offset:0\n" \
  "v_xor_b32 %[t1], 0x8080, %[pb]\n" \
  "ds_read_b64_tr_b16 v[200:201], %[t1] offset:0\n" \
  "v_xor_b32 %[t0], 0x9000, %[pb]\n" \
  "ds_read_b64_tr_b16 v[0:1], %[t0] offset:0\n" \
  "v_xor_b32 %[t1], 0x9080, %[pb]\n" \
  "ds_read_b64_tr_b16 v[48:49], %[t1] offset:0\n" \
  "v_xor_b32 %[t0], 0xa000, %[pb]\n" \
  "ds_read_b64_tr_b16 v[16:17], %[t0] offset:0\n" \
  "v_xor_b32 %[t1], 0xa080, %[pb]\n" \
  "ds_read_b64_tr_b16 v[64:65], %[t1] offset:0\n" \
  "v_xor_b32 %[t0], 0xb000, %[pb]\n" \
  "ds_read_b64_tr_b16 v[32:33], %[t0] offset:0\n" \
  "v_xor_b32 %[t1], 0xb080, %[pb]\n" \
  "ds_read_b64_tr_b16 v[80:81], %[t1] offset:0\n" \
  ".Ls_%=:\n" \
  "s_waitcnt lgkmcnt(0)\n" \
  "v_swap_b32 v97, v98\n" \
  "v_swap_b32 v101, v102\n" \
  "v_swap_b32 v105, v106\n" \
  "v_swap_b32 v109, v110\n"
#define PRO1_2 \
  "v_add_u32 %[t0], 0x10000, %[pa]\n" \
  "ds_read_b64_tr_b16 v[112:113], %[t0] offset:0\n" \
  "ds_read_b64_tr_b16 v[116:117], %[t0] offset:1024\n" \
  "ds_read_b64_tr_b16 v[120:121], %[t0] offset:8192\n" \
  "ds_read_b64_tr_b16 v[124:125], %[t0] offset:9216\n" \
  "s_cmp_lg_u32 %[hb], 0\n" \
  "s_cbranch_scc1 .Ls_%=\n" \
  "v_xor_b32 %[t0], 0x10000, %[pb]\n" \
  "ds_read_b64_tr_b16 v[194:195], %[t0] offset:0\n" \
  "v_xor_b32 %[t1], 0x10080, %[pb]\n" \
  "ds_read_b64_tr_b16 v[202:203], %[t1] offset:0\n" \
  "v_xor_b32 %[t0], 0x11000, %[pb]\n" \
  "ds_read_b64_tr_b16 v[2:3], %[t0] offset:0\n" \
  "v_xor_b32 %[t1], 0x11080, %[pb]\n" \
  "ds_read_b64_tr_b16 v[50:51], %[t1] offset:0\n" \
  "v_xor_b32 %[t0], 0x12000, %[pb]\n" \
  "ds_read_b64_tr_b16 v[18:19], %[t0] offset:0\n" \
  "v_xor_b32 %[t1], 0x12080, %[pb]\n" \
  "ds_read_b64_tr_b16 v[66:67], %[t1] offset:0\n" \
  "v_xor_b32 %[t0], 0x13000, %[pb]\n" \
  "ds_read_b64_tr_b16 v[34:35], %[t0] offset:0\n" \
  "v_xor_b32 %[t1], 0x13080, %[pb]\n" \
  "ds_read_b64_tr_b16 v[82:83], %[t1] offset:0\n" \
  ".Ls_%=:\n" \
  "s_waitcnt lgkmcnt(0)\n"
#define PRO1_3 \
  "v_add_u32 %[t0], 0x18000, %[pa]\n" \
  "ds_read_b64_tr_b16 v[114:115], %[t0] offset:0\n" \
  "ds_read_b64_tr_b16 v[118:119], %[t0] offset:1024\n" \
  "ds_read_b64_tr_b16 v[122:123], %[t0] offset:8192\n" \
  "ds_read_b64_tr_b16 v[126:127], %[t0] offset:9216\n" \
  "s_cmp_lg_u32 %[hb], 1\n" \
  "s_cbranch_scc1 .Ls_%=\n" \
  "v_xor_b32 %[t0], 0x18000, %[pb]\n" \
  "ds_read_b64_tr_b16 v[194:195], %[t0] offset:0\n" \
  "v_xor_b32 %[t1], 0x18080, %[pb]\n" \
  "ds_read_b64_tr_b16 v[202:203], %[t1] offset:0\n" \
  "v_xor_b32 %[t0], 0x19000, %[pb]\n" \
  "ds_read_b64_tr_b16 v[2:3], %[t0] offset:0\n" \
  "v_xor_b32 %[t1], 0x19080, %[pb]\n" \
  "ds_read_b64_tr_b16 v[50:51], %[t1] offset:0\n" \
  "v_xor_b32 %[t0], 0x1a000, %[pb]\n" \
  "ds_read_b64_tr_b16 v[18:19], %[t0] offset:0\n" \
  "v_xor_b32 %[t1], 0x1a080, %[pb]\n" \
  "ds_read_b64_tr_b16 v[66:67], %[t1] offset:0\n" \
  "v_xor_b32 %[t0], 0x1b000, %[pb]\n" \
  "ds_read_b64_tr_b16 v[34:35], %[t0] offset:0\n" \
  "v_xor_b32 %[t1], 0x1b080, %[pb]\n" \
  "ds_read_b64_tr_b16 v[82:83], %[t1] offset:0\n" \
  ".Ls_%=:\n" \
  "s_waitcnt lgkmcnt(0)\n" \
  "v_swap_b32 v113, v114\n" \
  "v_swap_b32 v117, v118\n" \
  "v_swap_b32 v121, v122\n" \
  "v_swap_b32 v125, v126\n"
#define PRO1_4 \
  "v_add_u32 %[t0], 0x20000, %[pa]\n" \
  "ds_read_b64_tr_b16 v[128:129], %[t0] offset:0\n" \
  "ds_read_b64_tr_b16 v[132:133], %[t0] offset:1024\n" \
  "ds_read_b64_tr_b16 v[136:137], %[t0] offset:8192\n" \
  "ds_read_b64_tr_b16 v[140:141], %[t0] offset:9216\n" \
  "s_cmp_lg_u32 %[hb], 0\n" \
  "s_cbranch_scc1 .Ls_%=\n" \
  "v_xor_b32 %[t0], 0x20000, %[pb]\n" \
  "ds_read_b64_tr_b16 v[196:197], %[t0] offset:0\n" \
  "v_xor_b32 %[t1], 0x20080, %[pb]\n" \
  "ds_read_b64_tr_b16 v[204:205], %[t1] offset:0\n" \
  "v_xor_b32 %[t0], 0x21000, %[pb]\n" \
  "ds_read_b64_tr_b16 v[4:5], %[t0] offset:0\n" \
  "v_xor_b32 %[t1], 0x21080, %[pb]\n" \
  "ds_read_b64_tr_b16 v[52:53], %[t1] offset:0\n" \
  "v_xor_b32 %[t0], 0x22000, %[pb]\n" \
  "ds_read_b64_tr_b16 v[20:21], %[t0] offset:0\n" \
  "v_xor_b32 %[t1], 0x22080, %[pb]\n" \
  "ds_read_b64_tr_b16 v[68:69], %[t1] offset:0\n" \
  "v_xor_b32 %[t0], 0x23000, %[pb]\n" \
  "ds_read_b64_tr_b16 v[36:37], %[t0] offset:0\n" \
  "v_xor_b32 %[t1], 0x23080, %[pb]\n" \
  "ds_read_b64_tr_b16 v[84:85], %[t1] offset:0\n" \
  ".Ls_%=:\n" \
  "s_waitcnt lgkmcnt(0)\n"
#define PRO1_5 \
  "v_add_u32 %[t0], 0x0, %[pa]\n" \
  "ds_read_b64_tr_b16 v[130:131], %[t0] offset:0\n" \
  "ds_read_b64_tr_b16 v[134:135], %[t0] offset:1024\n" \
  "ds_read_b64_tr_b16 v[138:139], %[t0] offset:8192\n" \
  "ds_read_b64_tr_b16 v[142:143], %[t0] offset:9216\n" \
  "s_cmp_lg_u32 %[hb], 1\n" \
  "s_cbranch_scc1 .Ls_%=\n" \
  "v_xor_b32 %[t0], 0x0, %[pb]\n" \
  "ds_read_b64_tr_b16 v[196:197], %[t0] offset:0\n" \
  "v_xor_b32 %[t1], 0x80, %[pb]\n" \
  "ds_read_b64_tr_b16 v[204:205], %[t1] offset:0\n" \
  "v_xor_b32 %[t0], 0x1000, %[pb]\n" \
  "ds_read_b64_tr_b16 v[4:5], %[t0] offset:0\n" \
  "v_xor_b32 %[t1], 0x1080, %[pb]\n" \
  "ds_read_b64_tr_b16 v[52:53], %[t1] offset:0\n" \
  "v_xor_b32 %[t0], 0x2000, %[pb]\n" \
  "ds_read_b64_tr_b16 v[20:21], %[t0] offset:0\n" \
  "v_xor_b32 %[t1], 0x2080, %[pb]\n" \
  "ds_read_b64_tr_b16 v[68:69], %[t1] offset:0\n" \
  "v_xor_b32 %[t0], 0x3000, %[pb]\n" \
  "ds_read_b64_tr_b16 v[36:37], %[t0] offset:0\n" \
  "v_xor_b32 %[t1], 0x3080, %[pb]\n" \
  "ds_read_b64_tr_b16 v[84:85], %[t1] offset:0\n" \
  ".Ls_%=:\n" \
  "s_waitcnt lgkmcnt(0)\n" \
  "v_swap_b32 v129, v130\n" \
  "v_swap_b32 v133, v134\n" \
  "v_swap_b32 v137, v138\n" \
  "v_swap_b32 v141, v142\n"
#define PRO1_6 \
  "v_add_u32 %[t0], 0x8000, %[pa]\n" \
  "ds_read_b64_tr_b16 v[144:145], %[t0] offset:0\n" \
  "ds_read_b64_tr_b16 v[148:149], %[t0] offset:1024\n" \
  "ds_read_b64_tr_b16 v[152:153], %[t0] offset:8192\n" \
  "ds_read_b64_tr_b16 v[156:157], %[t0] offset:9216\n" \
  "s_cmp_lg_u32 %[hb], 0\n" \
  "s_cbranch_scc1 .Ls_%=\n" \
  "v_xor_b32 %[t0], 0x8000, %[pb]\n" \
  "ds_read_b64_tr_b16 v[198:199], %[t0] offset:0\n" \
  "v_xor_b32 %[t1], 0x8080, %[pb]\n" \
  "ds_read_b64_tr_b16 v[206:207], %[t1] offset:0\n" \
  "v_xor_b32 %[t0], 0x9000, %[pb]\n" \
  "ds_read_b64_tr_b16 v[6:7], %[t0] offset:0\n" \
  "v_xor_b32 %[t1], 0x9080, %[pb]\n" \
  "ds_read_b64_tr_b16 v[54:55], %[t1] offset:0\n" \
  "v_xor_b32 %[t0], 0xa000, %[pb]\n" \
  "ds_read_b64_tr_b16 v[22:23], %[t0] offset:0\n" \
  "v_xor_b32 %[t1], 0xa080, %[pb]\n" \
  "ds_read_b64_tr_b16 v[70:71], %[t1] offset:0\n" \
  "v_xor_b32 %[t0], 0xb000, %[pb]\n" \
  "ds_read_b64_tr_b16 v[38:39], %[t0] offset:0\n" \
  "v_xor_b32 %[t1], 0xb080, %[pb]\n" \
  "ds_read_b64_tr_b16 v[86:87], %[t1] offset:0\n" \
  ".Ls_%=:\n" \
  "s_waitcnt lgkmcnt(0)\n"
#define PRO1_7 \
  "v_add_u32 %[t0], 0x10000, %[pa]\n" \
  "ds_read_b64_tr_b16 v[146:147], %[t0] offset:0\n" \
  "ds_read_b64_tr_b16 v[150:151], %[t0] offset:1024\n" \
  "ds_read_b64_tr_b16 v[154:155], %[t0] offset:8192\n" \
  "ds_read_b64_tr_b16 v[158:159], %[t0] offset:9216\n" \
  "s_cmp_lg_u32 %[hb], 1\n" \
  "s_cbranch_scc1 .Ls_%=\n" \
  "v_xor_b32 %[t0], 0x10000, %[pb]\n" \
  "ds_read_b64_tr_b16 v[198:199], %[t0] offset:0\n" \
  "v_xor_b32 %[t1], 0x10080, %[pb]\n" \
  "ds_read_b64_tr_b16 v[206:207], %[t1] offset:0\n" \
  "v_xor_b32 %[t0], 0x11000, %[pb]\n" \
  "ds_read_b64_tr_b16 v[6:7], %[t0] offset:0\n" \
  "v_xor_b32 %[t1], 0x11080, %[pb]\n" \
  "ds_read_b64_tr_b16 v[54:55], %[t1] offset:0\n" \
  "v_xor_b32 %[t0], 0x12000, %[pb]\n" \
  "ds_read_b64_tr_b16 v[22:23], %[t0] offset:0\n" \
  "v_xor_b32 %[t1], 0x12080, %[pb]\n" \
  "ds_read_b64_tr_b16 v[70:71], %[t1] offset:0\n" \
  "v_xor_b32 %[t0], 0x13000, %[pb]\n" \
  "ds_read_b64_tr_b16 v[38:39], %[t0] offset:0\n" \
  "v_xor_b32 %[t1], 0x13080, %[pb]\n" \
  "ds_read_b64_tr_b16 v[86:87], %[t1] offset:0\n" \
  ".Ls_%=:\n" \
  "s_waitcnt lgkmcnt(0)\n" \
  "v_swap_b32 v145, v146\n" \
  "v_swap_b32 v149, v150\n" \
  "v_swap_b32 v153, v154\n" \
  "v_swap_b32 v157, v158\n"
#define PRO1_8 \
  "v_add_u32 %[t0], 0x18000, %[pa]\n" \
  "ds_read_b64_tr_b16 v[160:161], %[t0] offset:0\n" \
  "ds_read_b64_tr_b16 v[164:165], %[t0] offset:1024\n" \
  "ds_read_b64_tr_b16 v[168:169], %[t0] offset:8192\n" \
  "ds_read_b64_tr_b16 v[172:173], %[t0] offset:9216\n" \
  "s_cmp_lg_u32 %[hb], 0\n" \
  "s_cbranch_scc1 .Ls_%=\n" \
  "v_xor_b32 %[t0], 0x18000, %[pb]\n" \
  "ds_read_b64_tr_b16 v[208:209], %[t0] offset:0\n" \
  "v_xor_b32 %[t1], 0x18080, %[pb]\n" \
  "ds_read_b64_tr_b16 v[216:217], %[t1] offset:0\n" \
  "v_xor_b32 %[t0], 0x19000, %[pb]\n" \
  "ds_read_b64_tr_b16 v[8:9], %[t0] offset:0\n" \
  "v_xor_b32 %[t1], 0x19080, %[pb]\n" \
  "ds_read_b64_tr_b16 v[56:57], %[t1] offset:0\n" \
  "v_xor_b32 %[t0], 0x1a000, %[pb]\n" \
  "ds_read_b64_tr_b16 v[24:25], %[t0] offset:0\n" \
  "v_xor_b32 %[t1], 0x1a080, %[pb]\n" \
  "ds_read_b64_tr_b16 v[72:73], %[t1] offset:0\n" \
  "v_xor_b32 %[t0], 0x1b000, %[pb]\n" \
  "ds_read_b64_tr_b16 v[40:41], %[t0] offset:0\n" \
  "v_xor_b32 %[t1], 0x1b080, %[pb]\n" \
  "ds_read_b64_tr_b16 v[88:89], %[t1] offset:0\n" \
  ".Ls_%=:\n" \
  "s_waitcnt lgkmcnt(0)\n"
#define PRO1_9 \
  "v_add_u32 %[t0], 0x20000, %[pa]\n" \
  "ds_read_b64_tr_b16 v[162:163], %[t0] offset:0\n" \
  "ds_read_b64_tr_b16 v[166:167], %[t0] offset:1024\n" \
  "ds_read_b64_tr_b16 v[170:171], %[t0] offset:8192\n" \
  "ds_read_b64_tr_b16 v[174:175], %[t0] offset:9216\n" \
  "s_cmp_lg_u32 %[hb], 1\n" \
  "s_cbranch_scc1 .Ls_%=\n" \
  "v_xor_b32 %[t0], 0x20000, %[pb]\n" \
  "ds_read_b64_tr_b16 v[208:209], %[t0] offset:0\n" \
  "v_xor_b32 %[t1], 0x20080, %[pb]\n" \
  "ds_read_b64_tr_b16 v[216:217], %[t1] offset:0\n" \
  "v_xor_b32 %[t0], 0x21000, %[pb]\n" \
  "ds_read_b64_tr_b16 v[8:9], %[t0] offset:0\n" \
  "v_xor_b32 %[t1], 0x21080, %[pb]\n" \
  "ds_read_b64_tr_b16 v[56:57], %[t1] offset:0\n" \
  "v_xor_b32 %[t0], 0x22000, %[pb]\n" \
  "ds_read_b64_tr_b16 v[24:25], %[t0] offset:0\n" \
  "v_xor_b32 %[t1], 0x22080, %[pb]\n" \
  "ds_read_b64_tr_b16 v[72:73], %[t1] offset:0\n" \
  "v_xor_b32 %[t0], 0x23000, %[pb]\n" \
  "ds_read_b64_tr_b16 v[40:41], %[t0] offset:0\n" \
  "v_xor_b32 %[t1], 0x23080, %[pb]\n" \
  "ds_read_b64_tr_b16 v[88:89], %[t1] offset:0\n" \
  ".Ls_%=:\n" \
  "s_waitcnt lgkmcnt(0)\n" \
  "v_swap_b32 v161, v162\n" \
  "v_swap_b32 v165, v166\n" \
  "v_swap_b32 v169, v170\n" \
  "v_swap_b32 v173, v174\n"
#define PRO1_10 \
  "v_add_u32 %[t0], 0x0, %[pa]\n" \
  "ds_read_b64_tr_b16 v[176:177], %[t0] offset:0\n" \
  "ds_read_b64_tr_b16 v[180:181], %[t0] offset:1024\n" \
  "ds_read_b64_tr_b16 v[184:185], %[t0] offset:8192\n" \
  "ds_read_b64_tr_b16 v[188:189], %[t0] offset:9216\n" \
  "s_cmp_lg_u32 %[hb], 0\n" \
  "s_cbranch_scc1 .Ls_%=\n" \
  "v_xor_b32 %[t0], 0x0, %[pb]\n" \
  "ds_read_b64_tr_b16 v[210:211], %[t0] offset:0\n" \
  "v_xor_b32 %[t1], 0x80, %[pb]\n" \
  "ds_read_b64_tr_b16 v[218:219], %[t1] offset:0\n" \
  "v_xor_b32 %[t0], 0x1000, %[pb]\n" \
  "ds_read_b64_tr_b16 v[10:11], %[t0] offset:0\n" \
  "v_xor_b32 %[t1], 0x1080, %[pb]\n" \
  "ds_read_b64_tr_b16 v[58:59], %[t1] offset:0\n" \
  "v_xor_b32 %[t0], 0x2000, %[pb]\n" \
  "ds_read_b64_tr_b16 v[26:27], %[t0] offset:0\n" \
  "v_xor_b32 %[t1], 0x2080, %[pb]\n" \
  "ds_read_b64_tr_b16 v[74:75], %[t1] offset:0\n" \
  "v_xor_b32 %[t0], 0x3000, %[pb]\n" \
  "ds_read_b64_tr_b16 v[42:43], %[t0] offset:0\n" \
  "v_xor_b32 %[t1], 0x3080, %[pb]\n" \
  "ds_read_b64_tr_b16 v[90:91], %[t1] offset:0\n" \
  ".Ls_%=:\n" \
  "s_waitcnt lgkmcnt(0)\n"
#define PRO1_11 \
  "v_add_u32 %[t0], 0x8000, %[pa]\n" \
  "ds_read_b64_tr_b16 v[178:179], %[t0] offset:0\n" \
  "ds_read_b64_tr_b16 v[182:183], %[t0] offset:1024\n" \
  "ds_read_b64_tr_b16 v[186:187], %[t0] offset:8192\n" \
  "ds_read_b64_tr_b16 v[190:191], %[t0] offset:9216\n" \
  "s_cmp_lg_u32 %[hb], 1\n" \
  "s_cbranch_scc1 .Ls_%=\n" \
  "v_xor_b32 %[t0], 0x8000, %[pb]\n" \
  "ds_read_b64_tr_b16 v[210:211], %[t0] offset:0\n" \
  "v_xor_b32 %[t1], 0x8080, %[pb]\n" \
  "ds_read_b64_tr_b16 v[218:219], %[t1] offset:0\n" \
  "v_xor_b32 %[t0], 0x9000, %[pb]\n" \
  "ds_read_b64_tr_b16 v[10:11], %[t0] offset:0\n" \
  "v_xor_b32 %[t1], 0x9080, %[pb]\n" \
  "ds_read_b64_tr_b16 v[58:59], %[t1] offset:0\n" \
  "v_xor_b32 %[t0], 0xa000, %[pb]\n" \
  "ds_read_b64_tr_b16 v[26:27], %[t0] offset:0\n" \
  "v_xor_b32 %[t1], 0xa080, %[pb]\n" \
  "ds_read_b64_tr_b16 v[74:75], %[t1] offset:0\n" \
  "v_xor_b32 %[t0], 0xb000, %[pb]\n" \
  "ds_read_b64_tr_b16 v[42:43], %[t0] offset:0\n" \
  "v_xor_b32 %[t1], 0xb080, %[pb]\n" \
  "ds_read_b64_tr_b16 v[90:91], %[t1] offset:0\n" \
  ".Ls_%=:\n" \
  "s_waitcnt lgkmcnt(0)\n" \
  "v_swap_b32 v177, v178\n" \
  "v_swap_b32 v181, v182\n" \
  "v_swap_b32 v185, v186\n" \
  "v_swap_b32 v189, v190\n"
#define PRO1_12 \
  "v_add_u32 %[t0], 0x8000, %[b0]\n" \
  "ds_write_b128 %[t0], v[192:195]\n" \
  "ds_write_b128 %[t0], v[196:199] offset:1024\n" \
  "v_add_u32 %[t0], 0xc000, %[b0]\n" \
  "ds_write_b128 %[t0], v[200:203]\n" \
  "ds_write_b128 %[t0], v[204:207] offset:1024\n" \
  "v_add_u32 %[t0], 0x10000, %[pa]\n" \
  "ds_read_b64_tr_b16 v[224:225], %[t0] offset:0\n" \
  "ds_read_b64_tr_b16 v[228:229], %[t0] offset:1024\n" \
  "ds_read_b64_tr_b16 v[232:233], %[t0] offset:8192\n" \
  "ds_read_b64_tr_b16 v[236:237], %[t0] offset:9216\n" \
  "s_cmp_lg_u32 %[hb], 0\n" \
  "s_cbranch_scc1 .Ls_%=\n" \
  "v_xor_b32 %[t0], 0x10000, %[pb]\n" \
  "ds_read_b64_tr_b16 v[212:213], %[t0] offset:0\n" \
  "v_xor_b32 %[t1], 0x10080, %[pb]\n" \
  "ds_read_b64_tr_b16 v[220:221], %[t1] offset:0\n" \
  "v_xor_b32 %[t0], 0x11000, %[pb]\n" \
  "ds_read_b64_tr_b16 v[12:13], %[t0] offset:0\n" \
  "v_xor_b32 %[t1], 0x11080, %[pb]\n" \
  "ds_read_b64_tr_b16 v[60:61], %[t1] offset:0\n" \
  "v_xor_b32 %[t0], 0x12000, %[pb]\n" \
  "ds_read_b64_tr_b16 v[28:29], %[t0] offset:0\n" \
  "v_xor_b32 %[t1], 0x12080, %[pb]\n" \
  "ds_read_b64_tr_b16 v[76:77], %[t1] offset:0\n" \
  "v_xor_b32 %[t0], 0x13000, %[pb]\n" \
  "ds_read_b64_tr_b16 v[44:45], %[t0] offset:0\n" \
  "v_xor_b32 %[t1], 0x13080, %[pb]\n" \
  "ds_read_b64_tr_b16 v[92:93], %[t1] offset:0\n" \
  ".Ls_%=:\n" \
  "s_waitcnt lgkmcnt(0)\n"
#define PRO1_13 \
  "v_add_u32 %[t0], 0x18000, %[pa]\n" \
  "ds_read_b64_tr_b16 v[226:227], %[t0] offset:0\n" \
  "ds_read_b64_tr_b16 v[230:231], %[t0] offset:1024\n" \
  "ds_read_b64_tr_b16 v[234:235], %[t0] offset:8192\n" \
  "ds_read_b64_tr_b16 v[238:239], %[t0] offset:9216\n" \
  "s_cmp_lg_u32 %[hb], 1\n" \
  "s_cbranch_scc1 .Ls_%=\n" \
  "v_xor_b32 %[t0], 0x18000, %[pb]\n" \
  "ds_read_b64_tr_b16 v[212:213], %[t0] offset:0\n" \
  "v_xor_b32 %[t1], 0x18080, %[pb]\n" \
  "ds_read_b64_tr_b16 v[220:221], %[t1] offset:0\n" \
  "v_xor_b32 %[t0], 0x19000, %[pb]\n" \
  "ds_read_b64_tr_b16 v[12:13], %[t0] offset:0\n" \
  "v_xor_b32 %[t1], 0x19080, %[pb]\n" \
  "ds_read_b64_tr_b16 v[60:61], %[t1] offset:0\n" \
  "v_xor_b32 %[t0], 0x1a000, %[pb]\n" \
  "ds_read_b64_tr_b16 v[28:29], %[t0] offset:0\n" \
  "v_xor_b32 %[t1], 0x1a080, %[pb]\n" \
  "ds_read_b64_tr_b16 v[76:77], %[t1] offset:0\n" \
  "v_xor_b32 %[t0], 0x1b000, %[pb]\n" \
  "ds_read_b64_tr_b16 v[44:45], %[t0] offset:0\n" \
  "v_xor_b32 %[t1], 0x1b080, %[pb]\n" \
  "ds_read_b64_tr_b16 v[92:93], %[t1] offset:0\n" \
  ".Ls_%=:\n" \
  "s_waitcnt lgkmcnt(0)\n" \
  "v_swap_b32 v225, v226\n" \
  "v_swap_b32 v229, v230\n" \
  "v_swap_b32 v233, v234\n" \
  "v_swap_b32 v237, v238\n" \
  "v_add_u32 %[t0], 0x10000, %[b0]\n" \
  "ds_write_b128 %[t0], v[224:227]\n" \
  "ds_write_b128 %[t0], v[228:231] offset:1024\n" \
  "v_add_u32 %[t0], 0x14000, %[b0]\n" \
  "ds_write_b128 %[t0], v[232:235]\n" \
  "ds_write_b128 %[t0], v[236:239] offset:1024\n"
#define PRO1_14 \
  "v_add_u32 %[t0], 0x20000, %[pa]\n" \
  "ds_read_b64_tr_b16 v[224:225], %[t0] offset:0\n" \
  "ds_read_b64_tr_b16 v[228:229], %[t0] offset:1024\n" \
  "ds_read_b64_tr_b16 v[232:233], %[t0] offset:8192\n" \
  "ds_read_b64_tr_b16 v[236:237], %[t0] offset:9216\n" \
  "s_cmp_lg_u32 %[hb], 0\n" \
  "s_cbranch_scc1 .Ls_%=\n" \
  "v_xor_b32 %[t0], 0x20000, %[pb]\n" \
  "ds_read_b64_tr_b16 v[214:215], %[t0] offset:0\n" \
  "v_xor_b32 %[t1], 0x20080, %[pb]\n" \
  "ds_read_b64_tr_b16 v[222:223], %[t1] offset:0\n" \
  "v_xor_b32 %[t0], 0x21000, %[pb]\n" \
  "ds_read_b64_tr_b16 v[14:15], %[t0] offset:0\n" \
  "v_xor_b32 %[t1], 0x21080, %[pb]\n" \
  "ds_read_b64_tr_b16 v[62:63], %[t1] offset:0\n" \
  "v_xor_b32 %[t0], 0x22000, %[pb]\n" \
  "ds_read_b64_tr_b16 v[30:31], %[t0] offset:0\n" \
  "v_xor_b32 %[t1], 0x22080, %[pb]\n" \
  "ds_read_b64_tr_b16 v[78:79], %[t1] offset:0\n" \
  "v_xor_b32 %[t0], 0x23000, %[pb]\n" \
  "ds_read_b64_tr_b16 v[46:47], %[t0] offset:0\n" \
  "v_xor_b32 %[t1], 0x23080, %[pb]\n" \
  "ds_read_b64_tr_b16 v[94:95], %[t1] offset:0\n" \
  ".Ls_%=:\n" \
  "s_waitcnt lgkmcnt(0)\n"
#define PRO1_15 \
  "v_add_u32 %[t0], 0x0, %[pa]\n" \
  "ds_read_b64_tr_b16 v[226:227], %[t0] offset:0\n" \
  "ds_read_b64_tr_b16 v[230:231], %[t0] offset:1024\n" \
  "ds_read_b64_tr_b16 v[234:235], %[t0] offset:8192\n" \
  "ds_read_b64_tr_b16 v[238:239], %[t0] offset:9216\n" \
  "s_cmp_lg_u32 %[hb], 1\n" \
  "s_cbranch_scc1 .Ls_%=\n" \
  "v_xor_b32 %[t0], 0x0, %[pb]\n" \
  "ds_read_b64_tr_b16 v[214:215], %[t0] offset:0\n" \
  "v_xor_b32 %[t1], 0x80, %[pb]\n" \
  "ds_read_b64_tr_b16 v[222:223], %[t1] offset:0\n" \
  "v_xor_b32 %[t0], 0x1000, %[pb]\n" \
  "ds_read_b64_tr_b16 v[14:15], %[t0] offset:0\n" \
  "v_xor_b32 %[t1], 0x1080, %[pb]\n" \
  "ds_read_b64_tr_b16 v[62:63], %[t1] offset:0\n" \
  "v_xor_b32 %[t0], 0x2000, %[pb]\n" \
  "ds_read_b64_tr_b16 v[30:31], %[t0] offset:0\n" \
  "v_xor_b32 %[t1], 0x2080, %[pb]\n" \
  "ds_read_b64_tr_b16 v[78:79], %[t1] offset:0\n" \
  "v_xor_b32 %[t0], 0x3000, %[pb]\n" \
  "ds_read_b64_tr_b16 v[46:47], %[t0] offset:0\n" \
  "v_xor_b32 %[t1], 0x3080, %[pb]\n" \
  "ds_read_b64_tr_b16 v[94:95], %[t1] offset:0\n" \
  ".Ls_%=:\n" \
  "s_waitcnt lgkmcnt(0)\n" \
  "v_swap_b32 v225, v226\n" \
  "v_swap_b32 v229, v230\n" \
  "v_swap_b32 v233, v234\n" \
  "v_swap_b32 v237, v238\n" \
  "v_add_u32 %[t0], 0x18000, %[b0]\n" \
  "ds_write_b128 %[t0], v[224:227]\n" \
  "ds_write_b128 %[t0], v[228:231] offset:1024\n" \
  "v_add_u32 %[t0], 0x1c000, %[b0]\n" \
  "ds_write_b128 %[t0], v[232:235]\n" \
  "ds_write_b128 %[t0], v[236:239] offset:1024\n" \
  "v_add_u32 %[t0], 0x20000, %[b0]\n" \
  "ds_write_b128 %[t0], v[208:211]\n" \
  "ds_write_b128 %[t0], v[212:215] offset:1024\n" \
  "v_add_u32 %[t0], 0x24000, %[b0]\n" \
  "ds_write_b128 %[t0], v[216:219]\n" \
  "ds_write_b128 %[t0], v[220:223] offset:1024\n"
#define VO_CLOB "v192", "v193", "v194", "v195", "v196", "v197", "v198", "v199", "v200", "v201", "v202", "v203", "v204", "v205", "v206", "v207", "v208", "v209", "v210", "v211", "v212", "v213", "v214", "v215", "v216", "v217", "v218", "v219", "v220", "v221", "v222", "v223", "v224", "v225", "v226", "v227", "v228", "v229", "v230", "v231", "v232", "v233", "v234", "v235", "v240", "v241", "v242", "v243", "v244", "v245", "v246", "v247", "v250", "v251", "v252"
#define QK_CLOB "v204", "v205", "v206", "v207", "v208", "v209", "v210", "v211", "v212", "v213", "v214", "v215", "v216", "v217", "v218", "v219", "v220", "v221", "v222", "v223", "v224", "v225", "v226", "v227", "v228", "v229", "v230", "v231", "v236", "v237", "v238", "v239", "v240", "v241", "v242", "v243", "v244", "v245", "v246", "v247"
#define QL_CLOB "v192", "v193", "v194", "v195", "v196", "v197", "v198", "v199", "v200", "v201", "v202", "v203"
#define QH_CLOB "v232", "v233", "v234", "v235"
#define ST_CLOB "v192", "v193", "v194", "v195", "v196", "v197", "v198", "v199", "v200", "v201", "v202", "v203", "v204", "v205", "v206", "v207", "v240", "v241", "v242", "v243", "v244", "v245", "v246", "v247", "v253"
#define XB_CLOB "v192", "v193", "v194", "v195", "v196", "v197", "v198", "v199", "v200", "v201", "v202", "v203", "v204", "v205", "v206", "v207", "v208", "v209", "v210", "v211", "v212", "v213", "v214", "v215", "v216", "v217", "v218", "v219", "v220", "v221", "v222", "v223", "v224", "v225", "v226", "v227", "v228", "v229", "v230", "v231", "v232", "v233", "v234", "v235", "v236", "v237", "v238", "v239", "v240", "v241", "v242", "v243", "v244", "v245", "v246", "v247"
#define OF_CLOB "v32", "v33", "v34", "v35", "v36", "v37", "v38", "v39", "v40", "v41", "v42", "v43", "v44", "v45", "v46", "v47", "v48", "v49", "v50", "v51", "v52", "v53", "v54", "v55", "v56", "v57", "v58", "v59", "v60", "v61", "v62", "v63", "v64", "v65", "v66", "v67", "v68", "v69", "v70", "v71", "v72", "v73", "v74", "v75", "v76", "v77", "v78", "v79", "v80", "v81", "v82", "v83", "v84", "v85", "v86", "v87", "v88", "v89", "v90", "v91", "v92", "v93", "v94", "v95", "v96", "v97", "v98", "v99", "v100", "v101", "v102", "v103"
constexpr int L_W = 32768;
constexpr int L_HI = 16384;
constexpr float CSC = 0.08838834764831845f * 1.4426950408889634f;
#define TWY 3
#define MARG 150
#define ED0 700
#define EA0 1110
#define SDK 360
#define TOL 145
#define XLATE 130
#define TFR 600
#define QT 0
#define HSU 11
#define WNT 1
#define KROT 7
#define VLATE 0
#define VSL 2
#define CG1 18
#define CG2 6
#define KSL2 0
#define KSL 4
#define NCY 1600
#define NCYV NCY
#define NRY 22
#define NRX 30
#define PDU 1
#define PDY 4
#define PDC 4
#define NF1O 64
#define NPUB 2
#define NSER 3
#define NSB 6
#define NSB1 4
#define XTU 20
#define CPU_ 9
#define CPR 12
#define RSL 2
#define PFL 4
#define EVT2R
template <int role>
__device__ __forceinline__ void wroles(const int h, const int par, const int sl, const int xg, const int w, const u64 nonce, const u16* __restrict__ x,
                                       const u16* __restrict__ Wq, const u16* __restrict__ Wk, const u16* __restrict__ Wv,
                                       const u16* __restrict__ Wo, char* ws) {
  LAS char* const L = (LAS char*)g_lds;
  gu32* tmo = (gu32*)(ws + OFF_TMO);
  const __amdgpu_buffer_rsrc_t rs = __builtin_amdgcn_make_buffer_rsrc(ws, 0, (int)WS_USED, 0x00020000);
  constexpr bool vo = role & 1;
  v8u wa[8], wb[16];
  v32u W0, W1, W2, W3, W4, W5;
  constexpr bool fuse = role >= 2;
  constexpr int RG = fuse ? 4 : 5, RB = fuse ? 32768 : 0;
  f32x4 fac[2][2];
  v2u nin = {0u, 0u};
  constexpr int TWV = 5;
  Clk ck = {0u, 0u, 92500u, 46432u};
  const int krot = role >= 2 ? (h & KROT) ^ (par * PX) : 0;
  {
    const int lane = olane();
    const int l15 = lane & 15, g = lane >> 4, trq = l15 >> 2, trp = lane & 3;
    const char* bl = (const char*)((vo ? Wv : Wq) + h * DH);
    const char* br = (const char*)(vo ? Wo + (size_t)h * DH * D : Wk + h * DH);
    const int rot = role == 1 ? (h >> 4) * 4 : 0;
    int vl[2], vr[2];
#pragma unroll
    for (int q = 0; q < 2; ++q) {
      const int row = 4 * (2 * w + q) + g;
      const int cb = ((((l15 >> 1) ^ ((row & 3) | ((row >> 1) & 4))) << 5) | ((l15 & 1) << 4));
      const int i = row & 7;
      const int prow = (row & 32) + ((i & 4) << 2) + ((row >> 1) & 12) + (i & 3);
      vl[q] = role < 2 ? (128 * (w >> 1) + 16 * (w & 1) + 8 * q + g) * 8192 + ((((l15 >> 1) ^ (4 * q + g)) << 5) | ((l15 & 1) << 4))
                       : row * 8192 + cb;
      vr[q] = vo ? (role == 1 ? row : prow) * 2048 + cb : vl[q];
    }
#define W_ISSUE(u)                                                                                   \
  {                                                                                                  \
    const size_t ro = (size_t)(role < 2 ? (8 * ((u) & 1) + ((u) >> 3)) * 64 + (((u) >> 2) & 1) * 32 + (((u) >> 1) & 1) * 4 : ((u) ^ krot) * 64) * 8192; \
    const char* pl = bl + ro;                                                                        \
    const char* pr = br + (role == 1 ? (size_t)(((u) >> 1) & 1) * 64 * 2048 + (2 * ((u) >> 2) + ((u) & 1)) * 256          \
                                     : (vo ? (size_t)((u) & 1) * 64 * 2048 + (((u) ^ (par * PX)) >> 1) * 256 : ro)); \
    _Pragma("unroll") for (int q = 0; q < 2; ++q) {                                                  \
      glds16<((WNT >> (role >> 1)) & 1) != 0>(pl, vl[q], RB + ((u) % RG) * 32768 + (2 * w + q) * 1024);                                \
      glds16<((WNT >> (role >> 1)) & 1) != 0>(pr, vr[q], RB + ((u) % RG) * 32768 + 16384 + (2 * w + q) * 1024);                        \
    }                                                                                                \
  }
    const int ta = role == 1 ? 8 + (w ^ rot) : 2 * w, tb = role < 2 ? (2 - role) * (w & 3) : 2 * w + 1;
    const int sw = trq | ((g & 1) << 2);
    int pa = (ta >> 3) * 16384 + (8 * g + trq) * 256 + (((ta & 7) ^ sw) << 5) + trp * 8;
    int pb = (role < 2 ? (w & 3) * 4096 + (4 * g + trq) * 256 : (tb >> 3) * 16384 + (8 * g + trq) * 256) + (((tb & 7) ^ sw) << 5) + trp * 8;
    v16u K0, K1, K2;
    if constexpr (role < 2)
      asm volatile("" : "={v[0:31]}"(W0), "={v[32:63]}"(W1), "={v[64:95]}"(W2), "={v[96:127]}"(W3), "={v[128:159]}"(W4), "={v[160:191]}"(W5),
                   "={v[192:207]}"(K0), "={v[208:223]}"(K1), "={v[224:239]}"(K2));
    const int b0 = lane * 16 + (w ^ 4) * 2048;
    const int pw = L_W + w * 2048 + lane * 16;
#define W_FRAG(dst, p, u)                                                                            \
  {                                                                                                  \
    int p_ = p + RB + ((u) % RG) * 32768;                                                             \
    asm volatile("" : "+v"(p_));                                                                     \
    const v2u t0 = trd(L + p_), t1 = trd(L + p_ + 1024), t2 = trd(L + p_ + 8192), t3 = trd(L + p_ + 9216); \
    dst = v8u{t0[0], t0[1], t1[0], t1[1], t2[0], t2[1], t3[0], t3[1]};                               \
  }
#define W_PUT(c)                                           \
  {                                                        \
    int p_ = pw + ((c) - 8) * 16384;                       \
    asm volatile("" : "+v"(p_));                           \
    *(LAS v4u*)(L + p_) = lo4(tmp[(c) - 8]);               \
    *(LAS v4u*)(L + p_ + 1024) = hi4(tmp[(c) - 8]);        \
  }
#define PT(i)
#define PT4(i)
    PT(0);
    const int fxv = ((w & 1) + 2 * (lane >> 2)) * 2048 + (((lane ^ -g) & 3) << 4) + ((w >> 1) & 1) * 64;
    const int fxr = l15 * 64 + (((g ^ -trq) & 3) << 4);
    constexpr int XS = role == 3;
#define X_ISSUE(j) glds16((const char*)x + par * 65536 + ((2 * (j) + (w >> 2)) ^ krot) * 128, fxv, (((j) + XS) & 3) * 8192 + w * 1024)
    W_ISSUE(0);
    if (fuse) {
#pragma unroll
      for (int j = 0; j < 4; ++j) X_ISSUE(j);
    }
#pragma unroll
    for (int u = 1; u < RG; ++u) {
      if (u == PDU && role < 3)
        for (int k = role == 2 ? TN(6, PDY) : TN(5, PDC); k > 0; --k) __builtin_amdgcn_s_sleep(8);
      W_ISSUE(u);
    }
    PT(1);
    v8u tmp[8];
#pragma unroll
    for (int u = 0; u < 16; ++u) {
      {
        int young = 4 * (u + RG - 1 < 16 ? RG - 1 : 15 - u);
        if (fuse)
          for (int i = u - RG; i < u; ++i) young += i >= 0 && i < 8 && (i & 1);
        asm volatile("s_waitcnt vmcnt(%0)" ::"n"(young) : "memory");
      }
      RAW_BARRIER();
      if ((u & 3) == 0 || u == 15) PT(2 + (u + 1) / 4);
      if constexpr (role < 2) {
        unsigned t0, t1;
#define PRO_OPS                                                                                                                    \
  : "+{v[0:31]}"(W0), "+{v[32:63]}"(W1), "+{v[64:95]}"(W2), "+{v[96:127]}"(W3), "+{v[128:159]}"(W4), "+{v[160:191]}"(W5),          \
    "+{v[192:207]}"(K0), "+{v[208:223]}"(K1), "+{v[224:239]}"(K2), [t0] "=&v"(t0), [t1] "=&v"(t1)                                  \
  : [b0] "v"(b0), [pa] "v"(pa), [pb] "v"(pb), [hb] "s"(w >> 2)                                                                     \
  : "scc", "memory"
#define PRO_U(n)                                         \
  if (u == n) {                                          \
    if constexpr (role == 0) asm volatile(PRO0_##n PRO_OPS); \
    else asm volatile(PRO1_##n PRO_OPS);                 \
  }
        PRO_U(0) PRO_U(1) PRO_U(2) PRO_U(3) PRO_U(4) PRO_U(5) PRO_U(6) PRO_U(7)
        PRO_U(8) PRO_U(9) PRO_U(10) PRO_U(11) PRO_U(12) PRO_U(13) PRO_U(14) PRO_U(15)
      } else {
        if (u >= 13) { W_PUT(u >= 13 ? 2 * u - 18 : 8); W_PUT(u >= 13 ? 2 * u - 17 : 8); }
        v8u fa, fb;
        W_FRAG(fa, pa, u)
        W_FRAG(fb, pb, u)
        if (u < 8) {
          wa[u & 7] = fa;
          wb[u] = fb;
        } else {
          wb[u] = cat8(lo4(fa), lo4(fb));
          tmp[u & 7] = cat8(hi4(fa), hi4(fb));
        }
#pragma unroll
        for (int hf = 0; hf < 2; ++hf)
#pragma unroll
          for (int rt = 0; rt < 2; ++rt) {
            const v4u xq = *(const LAS v4u*)(L + fxr + ((u + 2 * XS) & 7) * 4096 + hf * 2048 + rt * 1024);
            fac[0][rt] = mma(hf ? hi4(fa) : lo4(fa), xq, u + hf == 0 ? f32x4{0.f, 0.f, 0.f, 0.f} : fac[0][rt]);
            fac[1][rt] = mma(hf ? hi4(fb) : lo4(fb), xq, u + hf == 0 ? f32x4{0.f, 0.f, 0.f, 0.f} : fac[1][rt]);
          }
        asm volatile("" : "+v"(fac[0][0]), "+v"(fac[0][1]), "+v"(fac[1][0]), "+v"(fac[1][1]));
      }
      if (u == HSU && w == 0) hs_wait(rs, ws, nonce, CNT_HS);
      if (((VWN && role == 3) || (QKN && role == 2)) && u == HSU + 2 && w == 7) nin = numa_load(rs, tmo);
      if (role < 2 && u == HSU + 2 && w == 7) {
        arm_wait(rs, tmo);
        OPQ(off, OFF_PERM);
        nin[0] = __builtin_amdgcn_raw_buffer_load_b32(rs, off, 0, AUX_SC1);
      }
      if (role < 2 && u == 14 && w == TWV) clk_init(ck);
      if (u + RG < 16) {
        RAW_BARRIER();
        if (fuse && u == XTU && w < 2) {
          unsigned keep;
          asm volatile("s_mov_b32 %0, m0\n\ts_mov_b32 m0, %3\n\ts_nop 0\n\tglobal_load_lds_dword %1, %2\n\ts_mov_b32 m0, %0"
                       : "=&s"(keep) : "v"(olane() * 128), "s"((const char*)x + (2 * w + 2 + par) * 65536 + sl * 8192), "s"(RB + (u % RG) * 32768 + w * 2048) : "memory");
        }
        if (u == CPU_ && ((CPR >> role) & 1) && w == 1) code_ahead(sl + 8 * par, RB + (u % RG) * 32768 + 2048);
        W_ISSUE(u + RG);
        if (fuse && u < 8 && (u & 1)) X_ISSUE(4 + (u >> 1));
      }
    }
    PT(7);
    static_assert(!QKN, "");
    if (role == 2 || (role == 3 && w < 4)) {
      const int l16 = olane() * 16;
#pragma unroll
      for (int rt = 0; rt < 2; ++rt) {
        const float ks = role == 2 && w >= 4 ? CSC : 1.f;
        const f32x4 f0 = fac[0][rt] * ks, f1 = fac[1][rt] * ks;
        const v4u o = {pk2(f0[0], f0[1]), pk2(f0[2], f0[3]), pk2(f1[0], f1[1]), pk2(f1[2], f1[3])};
        if (role == 3) *(LAS v4u*)(L + 24576 + rt * 4096 + w * 1024 + l16) = o;
        else st128(v4u{o[0], o[2], o[1], o[3]}, rs, l16, qk_addr(w >> 2, par, h, 0, 0) + rt * QK_RT + (w & 3) * 1024);
      }
    }
    RAW_BARRIER();
    PT(8);
    if (fuse) { W_PUT(14); W_PUT(15); }
  }
  const int lane = olane();
  const int l15 = lane & 15, g = lane >> 4, trq = l15 >> 2, trp = lane & 3;
  int pw0 = L_W + w * 2048 + lane * 16, pw1 = pw0 + 65536;
  asm volatile("" : "+v"(pw0), "+v"(pw1));
#define WA_P(c) (L + ((c) < 12 ? pw0 : pw1) + ((c) & 3) * 16384)
#define WA_LDS(c) cat8(*(const LAS v4u*)WA_P(c), *(const LAS v4u*)(WA_P(c) + 1024))
  const int r_a = (int)(((uintptr_t)ws >> 19) & 7);
  const int r_s = r_a + (int)(OFF_STEP / UNIT);

  typedef int v4i __attribute__((ext_vector_type(4)));
#define WPIN "{v[0:31]}"(W0), "{v[32:63]}"(W1), "{v[64:95]}"(W2), "{v[96:127]}"(W3), "{v[128:159]}"(W4), "{v[160:191]}"(W5)
  const v4u rsv = {(unsigned)(uintptr_t)ws, (unsigned)((uintptr_t)ws >> 32) & 0xffffu, (unsigned)WS_USED, 0x00020000u};
  if constexpr (role >= 2) {
    constexpr bool isx = role == 3;
    W0 = cat32(wa[0], wa[1], wa[2], wa[3]), W1 = cat32(wa[4], wa[5], wa[6], wa[7]), W2 = cat32(wb[0], wb[1], wb[2], wb[3]);
    W3 = cat32(wb[4], wb[5], wb[6], wb[7]), W4 = cat32(wb[8], wb[9], wb[10], wb[11]), W5 = cat32(wb[12], wb[13], wb[14], wb[15]);
    const v4u rxv = {(unsigned)(uintptr_t)x, (unsigned)((uintptr_t)x >> 32) & 0xffffu, (unsigned)(S * D * 2), 0x00020000u};
    v4i adr;
    adr[3] = lane * 16;
    adr[2] = pw0;
    adr[1] = pw1;
    adr[0] = ((w & 1) + 2 * (lane >> 2)) * 2048 + (((lane ^ -g) & 3) << 4) + ((w >> 1) & 1) * 1024 + (isx ? 0 : (w >> 2) * 64);
    const int xr = l15 * 64 + (((g ^ -trq) & 3) << 4);
    constexpr int fo = isx ? F_VW : F_QK;
    const int nrel = isx ? TN(8, NRX) : TN(7, NRY);
    const int nit = par ? nrel : NSEG - nrel;
#define SEGOF(i) ((i) < nrel ? 2 * (i) + par : (i) + nrel)
#define ST_FLAG(sg)                                                                                                   \
  {                                                                                                                   \
    VMCNT(0);                                                                                                         \
    RAW_BARRIER();                                                                                                    \
    if (w == 0) __builtin_amdgcn_raw_buffer_store_b32(0u, rs, olane() << 28, fo + flag_of(sg, h), AUX_SC1);               \
  }
    unsigned vpm_ = 0;
    if ((VWN && isx) || (QKN && !isx)) {
      if (w == 7) *(volatile LAS unsigned*)L = numa_perm(nin, (LAS unsigned*)(L + 512));
      RAW_BARRIER();
      vpm_ = __builtin_amdgcn_readfirstlane(*(volatile LAS unsigned*)L) | (unsigned)((r_a + (int)(OFF_VWX / UNIT)) & 7) << 12;
      RAW_BARRIER();
    }
    const int qcl = cls_of(vpm_, h >> 3) ^ (QKN == 2 ? 2 : 0);
#define XB_OPS(sg)                                                                                                      \
  : [cnt] "=&s"(cnt) EVOUT                                                                                              \
  : WPIN, "{v[248:251]}"(adr), [rs] "s"(rsv), [so] "s"(vw_addr(vpm_, 2 * (w - 4), sg, h >> 2) + (h & 3) * 1024 + par * (PX / 2 * VRING * 262144)), \
    [so2] "s"(vw_addr(vpm_, 2 * (w - 4), sg, h >> 2) + (h & 3) * 1024 - par * (PX / 2 * VRING * 262144)),               \
    [js] "i"(VWN ? 32 * 524288 : VRING * 262144), [ss] "i"(VWN ? 65536 : 32768), [ob] "s"(nit - it)                         \
  : XB_CLOB, "scc", "memory"
    if (!isx || w < 4) {
      constexpr int B0 = isx ? 6 : 8, PER = isx ? 2 : 4, SL = isx ? 12288 : 16384;
#pragma unroll
      for (int k = 0; k < 2; ++k)
#pragma unroll
        for (int j = 0; j < B0 / PER; ++j)
          glds16((const char*)x + SEGOF(1) * 65536 + (((k * B0 + j * PER) / 2 * 64) ^ (krot * 128)), adr[0], k * SL + j * PER * 2048 + w * 1024);
    }
    PT(9);
    if (!isx) {
      VMCNT(4);
      RAW_BARRIER();
    }
    PT(10);
    if (!isx && w == 0) __builtin_amdgcn_raw_buffer_store_b32(0u, rs, olane() << 28, fo + flag_of(par, h), AUX_SC1);
    if (!isx || w < 4) {
      if (isx) VMCNT(3);
      else VMCNT(0);
      RAW_BARRIER();
    }
    PT(11);
    if (!isx || w < 4) {
      v32u xf;
#pragma unroll
      for (int q = 0; q < 8; ++q) {
        const v4u t = *(const LAS v4u*)(L + xr + q * 1024);
#pragma unroll
        for (int j = 0; j < 4; ++j) xf[4 * q + j] = t[j];
      }
#pragma unroll 1
      for (int it = 1; it < nit; ++it) {
        EVDECL;
        const int seg = SEGOF(it), ip = it - (isx ? 2 : 1);
        unsigned cnt, keep;
#define ST_OPS                                                                                                                 \
  : [cnt] "=&s"(cnt), [keep] "=&s"(keep), "+{v[208:239]}"(xf) EVOUT                                                            \
  : WPIN, "{v[248:251]}"(adr), "{v252}"(xr), [rs] "s"(rsv), [rx] "s"(rxv), [sc] "s"(seg * 65536 + krot * 128),                    \
    [sn] "s"(SEGOF(min(it + 1, nit - 1)) * 65536 + krot * 128),                                                                  \
    [mw] "s"(w * 1024), [so] "s"(isx ? w * 1024 : qk_addr(w >> 2, seg, h, qcl, r_a) + (w & 3) * 1024), [rt] "i"(QK_RT), \
    [fo] "s"(fo + flag_of(SEGOF(max(ip, 0)), h)), [fm] "s"(w == 0 && ip >= 0 ? 0 : 1 << 30), [tm] "s"(w == (isx ? 3 : TWY) ? 0 : 1 << 30),            \
    [tc] "s"(min(SEGOF(it + 2), NSEG - 1) * 65536 + sl * 8192), [cs] "s"((w >> 2) * __float_as_uint(CSC))                                                                   \
  : ST_CLOB, "scc", "memory"
        if constexpr (isx) asm volatile(ST_XA ST_OPS);
        else asm volatile(ST_Y ST_OPS);
        EVT2R
        if (!isx && it < NPUB) ST_FLAG(seg)
      }
      if (isx) RAW_BARRIER();
    } else {
#pragma unroll 1
      for (int it = 1;;) {
        {
          EVDECL;
          unsigned cnt;
          asm volatile(ST_XB0 XB_OPS(SEGOF(it - 1)));
        }
        if (it == nit) break;
        if (it <= 2) PT4(8 + 4 * it);
        const int nsb = it == 1 ? NSB1 : NSB;
#pragma unroll 1
        for (int k = (it == 1 ? 0 : 2) + NOB; k < nsb; ++k) BARE_BARRIER();
        if (it <= 2) PT4(9 + 4 * it);
        VMCNT(0);
        if (it <= 2) PT4(10 + 4 * it);
        BARE_BARRIER();
        if (it <= 2) PT4(11 + 4 * it);
        if (w == 4) __builtin_amdgcn_raw_buffer_store_b32(0u, rs, olane() << 28, fo + flag_of(SEGOF(it - 1), h), AUX_SC1);
#pragma unroll 1
        for (int k = nsb; k < 6; ++k) BARE_BARRIER();
        {
          EVDECL;
          EVT2R
        }
        if (++it > NSER) {
#pragma unroll 1
          for (; it < nit; ++it) {
            EVDECL;
            unsigned cnt;
            asm volatile(ST_XB XB_OPS(SEGOF(it - 1)));
            EVT2R
          }
        }
        BARE_BARRIER();
      }
    }
    ST_FLAG(SEGOF(nit - 1))
    if (isx && w == 0) __builtin_amdgcn_raw_buffer_store_b32(0u, rs, olane() << 28, fo + flag_of(SEGOF(nit - 2), h), AUX_SC1);
    return;
  }

  volatile LAS unsigned* sword = (volatile LAS unsigned*)(L + L_HI + 6400);
  if (w == 7) sword[0] = nin[0];
  RAW_BARRIER();
  const unsigned perm = __builtin_amdgcn_readfirstlane(sword[0]);
  PT(9);
  const int cown = cls_of(perm, xg);
  const int sidx = ((l15 & 2) ? 0xeeee : 0x4444) | ((l15 & 8) ? 0xeeee0000 : 0x44440000);
  int ntw = min(w ^ TWV, 1) << 30;
  asm volatile("" : "+s"(ntw));
  v2u bs = {(unsigned)(lane * 16 + (w ^ 4) * 2048), (unsigned)sidx};
  unsigned pl1 = bs[0] + 65536;
  asm volatile("" : "+v"(bs), "+v"(pl1));
  const int mg_ = NS2F(TN(0, MARG)), tol_ = NS2F(TN(13, TOL)), xl_ = NS2F(TN(14, XLATE));
  const int ncy_ = TN(1, NCY), vd_ = role ? NS2F(VLATE) : 0;
  Pace pc = {0u, NS2F(ED0), NS2F(EA0), 0};
  unsigned tg = 0, tw = 0;
  const int mo0 = R_MEM + (w >> 1) * 32768 + (w & 1) * 2048 - (w ^ 4) * 2048;
#define CH_IN WPIN, "{v[248:249]}"(bs), "{v253}"(pk), "{v254}"(pl1), [rs] "s"(rsv)
#define TLB_TOUCH                    \
  if (((r_s + s + 2) & 3) == 0)      \
    tch = __builtin_amdgcn_raw_buffer_load_b32(rs, 0, so + 2 * (int)UNIT + R_DUM + (role * 32 + h) * 128, AUX_SC1);
#define ST_ADR (lst * 32 + 32768 + 2048)
#define TIMING_WAVE(slp)                                                                             \
  {                                                                                                  \
    v2u st;                                                                                          \
    {                                                                                                \
      const int lst = olane();                                                                       \
      st = __builtin_amdgcn_raw_buffer_load_b64(rs, ST_ADR, so + R_ST + ((cown ^ xr) << 12), AUX_SC1); \
    }                                                                                                \
    SB();                                                                                            \
    if (slp) __builtin_amdgcn_s_sleep(slp);                             \
    clk_update(ck, s < 2 ? CG1 : CG2);                                                \
    if (role == 0) EV(7);                                                                            \
    SB();                                                                                            \
    if (__builtin_expect(anysent(st[0]), 0)) _Pragma("clang loop unroll(disable)") for (unsigned spins = 0;;) { \
      if (spins) __builtin_amdgcn_s_sleep(4);                                                        \
      if (spin_fail(spins, tmo, 0x30009)) break;                                                     \
      const int lst = olane();                                                                       \
      OPQ(va, ST_ADR - 2048);                                                                        \
      st = __builtin_amdgcn_raw_buffer_load_b64(rs, va, so + R_ST + ((cown ^ xr) << 12), AUX_SC1);   \
      if (!anysent(st[0])) break;                                                                       \
    }                                                                                                \
    unsigned md;                                                                                     \
    const int dm = stamp_max(ck, st, md);                                                            \
    const unsigned rf = (ck.r << 4) + dm;                                                            \
    pace_step(pc, s, rf, md, mg_, tol_, xl_);                                                     \
    tg = clk_cyc(ck, dm + pc.dq + vd_);                                                                    \
    tw = (unsigned)max(CMAX - (int)(tg - ck.m), 0);                                                  \
    EV(6);                                                                                           \
    EVDUMP(s);                                                                                       \
    EVCLK(s, ck);                                                                                    \
  }

  if constexpr (role == 0) {
    unsigned pk = w < 4 ? (g >> 1) * 512 + (g & 1) * 128 + trq * 32 + 8 * (trp ^ (2 * (g & 1)))
                        : (w & 2) * 1024 + (w & 1) * 512 + trp * 32 + (l15 >> 3) * 128 + (trq & 1) * 256 + 8 * (g ^ (trq & 2));
    asm volatile("" : "+v"(pk));
#define K_STAGE(sn)                                                                                                   \
  {                                                                                                                   \
    v4u kn[4];                                                                                                        \
    _Pragma("clang loop unroll(disable)") for (unsigned spins = 0; krdy <= (sn);) {    \
      OPQ(vfl, F_QK + flag_of(min((sn) + (olane() & 15), NSEG - 1), h));                                           \
      const u64 miss = __builtin_amdgcn_ballot_w64(__builtin_amdgcn_raw_buffer_load_b32(rs, vfl, 0, AUX_SC1) != 0u);  \
      krdy = (sn) + (miss ? __builtin_ctzll(miss) : 16);                                                              \
      if (krdy > (sn)) break;                                                                                         \
      __builtin_amdgcn_s_sleep(8);                                                                                    \
      if (spin_fail(spins, tmo, 0x30000)) break;                                                                      \
    }                                                                                                                 \
    const int lk = olane() * 16;                                                                                      \
    _Pragma("unroll") for (int q = 0; q < 4; ++q) kn[q] = __builtin_amdgcn_raw_buffer_load_b128(                      \
        rs, lk + q * 1024, qk_addr(1, sn, h, cown, r_a) + (w & 1) * QK_RT, AUX_SC1);                 \
    _Pragma("unroll") for (int q = 0; q < 4; ++q) *(LAS v4u*)(L + L_HI + 8192 + (w & 1) * 4096 + q * 1024 + lk) = kn[q]; \
  }
    int krdy = 0;
    const bool kst = w >= 4 && ((w ^ TWV) & 2);
    PT(10);
    if (kst) K_STAGE(0)
    RAW_BARRIER();
    PT(11);
    if (w < 2) {
      {
        const int lane = olane(), l15 = lane & 15, g = lane >> 4, d = ((l15 >> 1) & 2) | (l15 >> 3);
        *(LAS v4u*)(L + L_HI + 5120 + w * 2048 + lane * 16) =
            v4u{(unsigned)(R_EX + (h >> 3) * 32768 + (h & 7) * 512 + w * 256 + g * 16 + ((l15 >> 3) | (l15 & 2)) * 64 + (l15 & 4) * 2 + (l15 & 1) * 4),
                (unsigned)(R_PM + (cls_of(perm, l15 & 3) << 12) + (h >> 4) * 32768 + (h & 15) * 256 + w * 128 + g * 16 + d * 4),
                d == 2 * (h & 1) + w ? 0xffffu << (16 * (g & 1)) : 0u, 0u};
      }
#pragma unroll 1
      for (int s = 0; s < NSEG; ++s) {
        EVDECL;
        const int so = (int)OFF_STEP + s * (int)UNIT, xr = xreg(r_s + s);
#define CG(r) (so + ((cls_of(perm, (r) ^ (2 * w)) ^ xr) << 12))
        unsigned rt;
#define QA_OPS                                                                                                                    \
  : [rt] "=&s"(rt) EVOUT                                                                                                          \
  : CH_IN, [o0] "s"(so + mo0 + ((cown ^ xr) << 12)), [c0] "s"(CG(0)), [c1] "s"(CG(1)), [c2] "s"(CG(2)), [c3] "s"(CG(3)),          \
    [lo] "s"(0x3333333333333333ull), [so] "s"(so), [xr] "s"(xr << 12)                                                                                              \
  : QL_CLOB, QK_CLOB, QH_CLOB, "v250", "v251", "v252", "vcc", "scc", "memory"
        if (w == 0) asm volatile(QK_A0 QA_OPS);
        else asm volatile(QK_A1 QA_OPS);
        EVDUMP(s);
      }
    } else if (w < 4) {
#pragma unroll 1
      for (int s = 0; s < NSEG; ++s) {
        {
          EVDECL;
          const int so = (int)OFF_STEP + s * (int)UNIT, xr = xreg(r_s + s);
          unsigned rt;
          f32x4 sc[3];
          asm volatile(QK_X
                       : [rt] "=&s"(rt), "={v[192:195]}"(sc[0]), "={v[196:199]}"(sc[1]), "={v[200:203]}"(sc[2]) EVOUT
                       : CH_IN, [o0] "s"(so + mo0 + ((cown ^ xr) << 12)), [qo] "s"(qk_addr(0, s, h, cown, r_a) + (w - 2) * QK_RT)
                       : QK_CLOB, QH_CLOB, "v250", "v251", "v252", "vcc", "scc", "memory");
          const int ln = olane(), l15 = ln & 15, g = ln >> 4, l16 = ln * 16;
          EV(4);
          const int tok = 2 * l15 + w - 2 - 8 * g;
#pragma unroll
          for (int t = 0; t < 2; ++t)
#pragma unroll
            for (int j = 0; j < 4; ++j)
              if (2 * j + t > tok) sc[t][j] = -1000000000.0f * 1.4426950408889634f;
          float m0 = sc[2][0] + sc[2][2], m1 = sc[2][1] + sc[2][3];
          float mx;
          asm("v_max3_f32 %0, %1, %2, %3\n\tv_max3_f32 %0, %0, %4, %5\n\tv_max3_f32 %0, %0, %6, %7\n\tv_max3_f32 %0, %0, %8, %9\n\t"
              "v_max_f32 %0, %0, %10\n\ts_nop 1"
              : "=&v"(mx)
              : "v"(sc[0][0]), "v"(sc[0][1]), "v"(sc[0][2]), "v"(sc[0][3]), "v"(sc[1][0]), "v"(sc[1][1]), "v"(sc[1][2]), "v"(sc[1][3]),
                "v"(m0), "v"(m1));
          mx = xmax16_32(mx);
#pragma unroll
          for (int t = 0; t < 2; ++t)
#pragma unroll
            for (int j = 0; j < 4; ++j) sc[t][j] = __builtin_amdgcn_exp2f(sc[t][j] - mx);
          m0 = __builtin_amdgcn_exp2f(m0 - mx);
          m1 = __builtin_amdgcn_exp2f(m1 - mx);
          const f32x4 s4 = sc[0] + sc[1];
          const float inv = __builtin_amdgcn_rcpf(xsum16_32(((s4[0] + s4[1]) + (s4[2] + s4[3])) + (m0 + m1)));
          sc[0] *= inv;
          sc[1] *= inv;
          const v4u o = {pk2(sc[0][0], sc[1][0]), pk2(sc[0][1], sc[1][1]), pk2(sc[0][2], sc[1][2]), pk2(sc[0][3], sc[1][3])};
          const int pb = so + R_PX + (w - 2) * 40960;
          st128(o, rs, l16, pb + h * 1024);
          __builtin_amdgcn_raw_buffer_store_b32(pk2(m0 * inv, m1 * inv), rs, l15 * 16 + g * 4, pb + (32 + (h >> 2)) * 1024 + (h & 3) * 256, AUX_SC1);
          EV(6);
          EVDUMP(s);
        }
      }
    } else {
#pragma unroll 1
      for (int s = 0; s < NSEG; ++s) {
        EVDECL;
        const int so = (int)OFF_STEP + s * (int)UNIT, xr = xreg(r_s + s), sc_ = so + ((cown ^ xr) << 12);
        const int lst = olane();
        const bool fq = kst && krdy <= s + 2;
        int i0 = ((cls_of(perm, lst & 3) ^ xr) << 12) | ((lst & 4) << 9) | ((lst & 8) << 12) | ((lst >> 4) << 28) | ntw;
        int i2 = ((lst & 31) * 128 + (lst >> 5) * QK_RT) | (krdy > s + 1 ? 0 : 1 << 30);
        int i1 = (F_QK + flag_of(min(s + 1 + (lst & 15), NSEG - 1), h)) | (fq ? 0 : 1 << 30);
        v2u st;
        unsigned cnt, rt;
        int o0 = sc_ + mo0, sst = so + R_ST + role * 1024 + h * 32;
        int t1 = qk_addr(w & 1, min(s + 1, NSEG - 1), h, cown, r_a), t0 = 0;
        asm volatile("" : "+{v232}"(i0), "+{v233}"(i1), "+{v234}"(i2));
        asm volatile("" : "+s"(o0), "+s"(sst), "+s"(t0), "+s"(t1));
        asm volatile(QK_K
                     : [cnt] "=&s"(cnt), [rt] "=&s"(rt), "+{v232}"(i0), "+{v233}"(i1), "+{v234}"(i2), "={v[250:251]}"(st) EVOUT
                     : CH_IN, [o0] "s"(o0), [ckm] "s"(ck.m - ncy_), [inv] "s"(ck.inv << 16), [r16] "s"(ck.r << 4), [sst] "s"(sst), [tg] "s"(tg),
                       [tw] "s"(tw), [t0] "s"(t0), [t1] "s"(t1), [cs] "s"(CSC)
                     : QL_CLOB, QK_CLOB, "v235", "v252", "vcc", "scc", "memory");
        if (s < NSEG - 1 && w == TWV) {
          TIMING_WAVE(KSL)
        } else if (s < NSEG - 1 && kst) {
          if (fq) {
            const u64 miss = __builtin_amdgcn_ballot_w64(st[0] != 0u);
            krdy = s + 1 + (miss ? __builtin_ctzll(miss) : 16);
          }
          if (KSL2) __builtin_amdgcn_s_sleep(KSL2);
          EV(7);
          K_STAGE(s + 1)
        } else {
          __builtin_amdgcn_s_sleep(KSL);
          unsigned tch = 0;
          TLB_TOUCH
          asm volatile("" ::"v"(tch));
        }
        if (w != TWV) EV(6);
        if (w != TWV) EVDUMP(s);
      }
    }
  } else {
    int adr[4];
#pragma unroll
    for (int q = 0; q < 2; ++q) {
      const int c = 8 * (2 * q + (g >> 1) + ((g & 1) ? 0 : 4)) + (w ^ ((h >> 4) * 4));
      const int blk = (c % PO) * 2 + (h >> 4);
      adr[q] = (int)OFF_VM + blk * 32768 + ((cls_of(perm, c / PO) ^ xreg(r_a + (int)(OFF_VM / UNIT) + (blk >> 4))) << 12) +
                   (h & 15) * 256 + l15 * 16;
    }
    adr[2] = (16 * (g & 1) + 4 * (g >> 1) + trq) * 24 + (trp & 1) * 8;
    unsigned pk = L_HI + (16 * (w & 3) + l15) * 24 + 4 * g;
    asm volatile("" : "+v"(pk), "+v"(adr[0]), "+v"(adr[1]), "+v"(adr[2]));
    PT(10);
    PT(11);
    if (w < 4) {
#pragma unroll 1
      for (int s = 0; s < NSEG; ++s) {
        EVDECL;
        const int so = (int)OFF_STEP + s * (int)UNIT, xr = xreg(r_s + s);
        unsigned rt;
        asm volatile(VO_A
                     : [rt] "=&s"(rt) EVOUT
                     : CH_IN, "{v236}"(adr[0]), "{v237}"(adr[1]), "{v238}"(adr[2]), [o0] "s"(so + mo0 + ((cown ^ xr) << 12)), [vs] "s"(vm_slot(s))
                     : VO_CLOB, "vcc", "scc", "memory");
        if (w == 0) {
          unsigned tch = 0;
          TLB_TOUCH
          asm volatile("" ::"v"(tch));
        }
        EVDUMP(s);
      }
    } else {
#pragma unroll 1
      for (int s = 0; s < NSEG; ++s) {
        EVDECL;
        const int so = (int)OFF_STEP + s * (int)UNIT, xr = xreg(r_s + s);
        const int lst = olane();
        adr[3] = ((cls_of(perm, lst & 3) ^ xr) << 12) | ((lst & 4) << 9) | ((lst & 8) << 12) | ((lst >> 4) << 28) | ntw;
        unsigned cnt, rt;
        int o0 = so + mo0 + ((cown ^ xr) << 12), vs = vm_slot(s), sst = so + R_ST + role * 1024 + h * 32;
        asm volatile("" : "+{v239}"(adr[3]));
        asm volatile("" : "+s"(o0), "+s"(vs), "+s"(sst));
        asm volatile(VO_B
                     : [cnt] "=&s"(cnt), [rt] "=&s"(rt) EVOUT
                     : CH_IN, "{v236}"(adr[0]), "{v237}"(adr[1]), "{v238}"(adr[2]), "{v239}"(adr[3]), [o0] "s"(o0), [vs] "s"(vs),
                       [ckm] "s"(ck.m - ncy_), [inv] "s"(ck.inv << 16), [r16] "s"(ck.r << 4), [sst] "s"(sst), [tg] "s"(tg), [tw] "s"(tw)
                     : VO_CLOB, "vcc", "scc", "memory");
        if (w == TWV && s < NSEG - 1) TIMING_WAVE(VSL)
        if (w != TWV) EVDUMP(s);
      }
    }
  }
  __builtin_amdgcn_endpgm();
}
typedef f32x2 c32;
template <bool CJ>
__device__ __forceinline__ c32 cmul(c32 a, c32 b) {
  c32 d;
  if (CJ)
    asm("v_pk_mul_f32 %0, %1, %2 op_sel:[1,1] op_sel_hi:[1,0]\n\tv_pk_fma_f32 %0, %1, %2, %0 op_sel_hi:[0,1,1] neg_hi:[0,1,0]"
        : "=&v"(d) : "v"(a), "v"(b));
  else
    asm("v_pk_mul_f32 %0, %1, %2 op_sel:[1,1] op_sel_hi:[1,0] neg_lo:[0,1]\n\tv_pk_fma_f32 %0, %1, %2, %0 op_sel_hi:[0,1,1]"
        : "=&v"(d) : "v"(a), "v"(b));
  return d;
}
__device__ __forceinline__ c32 cmuls(c32 a, c32 b) {
  c32 d;
  asm("v_pk_mul_f32 %0, %1, %2 op_sel:[1,1] op_sel_hi:[1,0] neg_lo:[0,1]\n\tv_pk_fma_f32 %0, %1, %2, %0 op_sel_hi:[0,1,1]"
      : "=&v"(d) : "v"(a), "s"(b));
  return d;
}
template <int SG>
__device__ __forceinline__ c32 addi(c32 a, c32 b) {
  c32 d;
  if (SG > 0) asm("v_pk_add_f32 %0, %1, %2 op_sel:[0,1] op_sel_hi:[1,0] neg_lo:[0,1]" : "=v"(d) : "v"(a), "v"(b));
  else asm("v_pk_add_f32 %0, %1, %2 op_sel:[0,1] op_sel_hi:[1,0] neg_hi:[0,1]" : "=v"(d) : "v"(a), "v"(b));
  return d;
}
template <int SG, bool I2 = false>
__device__ __forceinline__ void dft4(c32& x0, c32& x1, c32& x2, c32& x3) {
  const c32 a = I2 ? addi<SG>(x0, x2) : x0 + x2, b = I2 ? addi<-SG>(x0, x2) : x0 - x2, c = x1 + x3, d = x1 - x3;
  x0 = a + c;
  x1 = addi<SG>(b, d);
  x2 = a - c;
  x3 = addi<-SG>(b, d);
}
template <int SG>
__device__ __forceinline__ c32 w16(int m) {
  constexpr float C[16] = {1.f, 0.92387953251128674f, 0.70710678118654752f, 0.38268343236508977f, 0.f, -0.38268343236508977f,
                           -0.70710678118654752f, -0.92387953251128674f, -1.f, -0.92387953251128674f, -0.70710678118654752f,
                           -0.38268343236508977f, 0.f, 0.38268343236508977f, 0.70710678118654752f, 0.92387953251128674f};
  return c32{C[m & 15], (float)SG * C[(m + 12) & 15]};
}
template <int SG>
__device__ __forceinline__ void dft16(c32 (&v)[16]) {
#pragma unroll
  for (int n2 = 0; n2 < 4; ++n2) dft4<SG>(v[n2], v[4 + n2], v[8 + n2], v[12 + n2]);
#pragma unroll
  for (int k1 = 1; k1 < 4; ++k1)
#pragma unroll
    for (int n2 = 1; n2 < 4; ++n2)
      if (n2 * k1 != 4) v[4 * k1 + n2] = cmuls(v[4 * k1 + n2], w16<SG>(n2 * k1));
#pragma unroll
  for (int k1 = 0; k1 < 4; ++k1)
    if (k1 == 2) dft4<SG, true>(v[8], v[9], v[10], v[11]);
    else dft4<SG>(v[4 * k1], v[4 * k1 + 1], v[4 * k1 + 2], v[4 * k1 + 3]);
  c32 t[16];
#pragma unroll
  for (int k1 = 0; k1 < 4; ++k1)
#pragma unroll
    for (int k2 = 0; k2 < 4; ++k2) t[k1 + 4 * k2] = v[4 * k1 + k2];
#pragma unroll
  for (int i = 0; i < 16; ++i) v[i] = t[i];
}
template <int SG>
__device__ __forceinline__ void dft8(c32 (&v)[8]) {
#pragma unroll
  for (int n2 = 0; n2 < 2; ++n2) dft4<SG>(v[n2], v[2 + n2], v[4 + n2], v[6 + n2]);
#pragma unroll
  for (int k1 = 1; k1 < 4; k1 += 2) v[2 * k1 + 1] = cmuls(v[2 * k1 + 1], w16<SG>(2 * k1));
  c32 t[8];
#pragma unroll
  for (int k1 = 0; k1 < 4; ++k1) {
    t[k1] = k1 == 2 ? addi<SG>(v[4], v[5]) : v[2 * k1] + v[2 * k1 + 1];
    t[k1 + 4] = k1 == 2 ? addi<-SG>(v[4], v[5]) : v[2 * k1] - v[2 * k1 + 1];
  }
#pragma unroll
  for (int i = 0; i < 8; ++i) v[i] = t[i];
}
__device__ __forceinline__ void lds_fence_wave() {
  __builtin_amdgcn_s_waitcnt(0xc07f);
  asm volatile("" ::: "memory");
}
template <int SG>
__device__ __forceinline__ void twiddle16(c32 (&u)[16], c32 w1, c32 w4) {
  const c32 w2 = cmul<false>(w1, w1), w3 = cmul<false>(w2, w1), w8 = cmul<false>(w4, w4), w12 = cmul<false>(w8, w4);
  const c32 lo[4] = {w1, w1, w2, w3}, hi[4] = {w4, w4, w8, w12};
#pragma unroll
  for (int k = 1; k < 16; ++k)
    u[k] = cmul<(SG > 0)>(u[k], (k & 3) ? ((k >> 2) ? cmul<false>(hi[k >> 2], lo[k & 3]) : lo[k & 3]) : hi[k >> 2]);
}
template <int N, int SG>
__device__ __forceinline__ void wave_fft(c32 (&v)[N / 64], c32* eb, const c32* tw, int lane) {
  constexpr int P = N / 64, B = P / 16, R3 = N / 256, M = N / 16, S1 = M + R3, S2 = 256 + 16 / R3, TS = 2048 / N;
  asm volatile("" : "+v"(lane));
#pragma unroll
  for (int b = 0; b < B; ++b) {
    c32 u[16];
#pragma unroll
    for (int n1 = 0; n1 < 16; ++n1) u[n1] = v[b + B * n1];
    dft16<SG>(u);
    const int n2 = lane + 64 * b;
    twiddle16<SG>(u, tw[n2 * TS], tw[n2 * TS * 4]);
#pragma unroll
    for (int k1 = 0; k1 < 16; ++k1) eb[k1 * S1 + n2] = u[k1];
    __builtin_amdgcn_sched_barrier(0);
  }
  lds_fence_wave();
  {
    c32 u[B][16];
#pragma unroll
    for (int b = 0; b < B; ++b) {
      const int q = lane + 64 * b, k1 = q / R3, m2 = q % R3;
#pragma unroll
      for (int m1 = 0; m1 < 16; ++m1) u[b][m1] = eb[k1 * S1 + m1 * R3 + m2];
    }
    const c32 w1 = tw[16 * (lane % R3) * TS], w4 = tw[64 * (lane % R3) * TS];
    lds_fence_wave();
#pragma unroll
    for (int b = 0; b < B; ++b) {
      const int q = lane + 64 * b, k1 = q / R3, m2 = q % R3;
      dft16<SG>(u[b]);
      twiddle16<SG>(u[b], w1, w4);
#pragma unroll
      for (int j1 = 0; j1 < 16; ++j1) eb[m2 * S2 + k1 + 16 * j1] = u[b][j1];
      __builtin_amdgcn_sched_barrier(0);
    }
  }
  lds_fence_wave();
#pragma unroll
  for (int c = 0; c < 4; ++c) {
    c32 u[R3];
#pragma unroll
    for (int m2 = 0; m2 < R3; ++m2) u[m2] = eb[m2 * S2 + lane + 64 * c];
    if constexpr (R3 == 4) dft4<SG>(u[0], u[1], u[2], u[3]);
    else dft8<SG>(u);
#pragma unroll
    for (int j2 = 0; j2 < R3; ++j2) v[c + 4 * j2] = u[j2];
  }
  lds_fence_wave();
}

constexpr int FFT_EB = 2176;
constexpr int LDS_TW = 0;
constexpr int LDS_EB = 16384;
constexpr int LDS_STG = 16384 + 8 * 1088 * 8;

__device__ __forceinline__ void fft_table(char* lds, int w) {
  c32* tw = (c32*)(lds + LDS_TW);
  {
    const int m = w * 64 + olane();
    float sn, cs;
    sincospif(-(float)m * (1.f / 1024.f), &sn, &cs);
    tw[m] = c32{cs, sn};
  }
}
#define XSK 12
__device__ __forceinline__ int x1_chunk(int kb, int rb) { return kb * 128 + ((rb + XSK * kb) & 127); }
#define F1NT true
__device__ __forceinline__ void fft_in1(const u16* x, const u16* wr, const u16* wi, int task, int w) {
  const int lane = olane(), r0 = task * 16 + 2 * w;
  const int eo = LDS_EB + w * 8704, fo = r0 * DF * 2;
#pragma unroll
  for (int q = 0; q < 4; ++q) glds16<F1NT>(x, r0 * D * 2 + q * 1024 + lane * 16, eo + q * 1024);
#pragma unroll
  for (int q = 0; q < 3; ++q)
    if (q < 2 || lane < 2) {
      const int o = min((fo & ~15) + q * 1024 + lane * 16, S * DF * 2 - 16);
      glds16<F1NT>(wr, o, eo + 4096 + q * 1024);
      glds16<F1NT>(wi, o, eo + 6176 + q * 1024);
    }
}
__device__ __forceinline__ void fft_pass1(char* ws, const u16* __restrict__ x, const u16* __restrict__ wr,
                                          const u16* __restrict__ wi, int task, int w, const u16* xb) {
  char* lds = g_lds;
  const __amdgpu_buffer_rsrc_t rx = __builtin_amdgcn_make_buffer_rsrc(ws + OFF_X1, 0, 65 * 131072, 0x00020000);
  const int lane = olane(), tid_ = w * 64 + lane;
  const c32* tw = (const c32*)(lds + LDS_TW);
  c32* eb = (c32*)(lds + LDS_EB) + w * 1088;
  c32(*stg)[16] = (c32(*)[16])(lds + LDS_STG);
  const int r0 = task * 16 + 2 * w;
#define F1T(i)
  F1T(0);
  const int fo = r0 * DF * 2;
  if (xb) {
    unsigned keep;
    asm volatile("s_mov_b32 %0, m0\n\ts_mov_b32 m0, %3\n\ts_nop 0\n\tglobal_load_lds_dword %1, %2\n\ts_mov_b32 m0, %0"
                 : "=&s"(keep) : "v"(task * 65536 + tid_ * 128), "s"(xb), "s"(160000 + w * 256) : "memory");
  } else {
    fft_in1(x, wr, wi, task, w);
    VMCNT(0);
  }
  F1T(1);
  c32 v[16];
  const u16* in = (const u16*)eb;
#pragma unroll
  for (int t = 0; t < 16; ++t) v[t] = c32{bf2f(in[lane + 64 * t]), bf2f(in[D + lane + 64 * t])};
  unsigned fw[2][9];
#pragma unroll
  for (int r = 0; r < 2; ++r)
#pragma unroll
    for (int t = 0; t < 9; ++t) {
      const int o = 2048 + ((fo & 15) >> 1) + r * DF + min(lane + 64 * t, 512);
      fw[r][t] = (unsigned)in[o] | ((unsigned)in[o + 1040] << 16);
    }
  lds_fence_wave();
  F1T(2);
  wave_fft<1024, -1>(v, eb, tw, lane);
  F1T(3);
#pragma unroll
  for (int t = 0; t < 16; ++t) eb[lane + 64 * t] = v[t];
  lds_fence_wave();
#pragma unroll
  for (int t = 0; t < 9; ++t) {
    const int k = lane + 64 * t;
    if (k <= 512) {
      c32 z = eb[k], zc = eb[(1024 - k) & 1023];
      *(f32x4*)&stg[k][2 * (w ^ (k & 7))] = f32x4{0.5f * (z.x + zc.x), 0.5f * (z.y - zc.y), 0.5f * (z.y + zc.y), -0.5f * (z.x - zc.x)};
    }
  }
  __syncthreads();
  F1T(4);
#pragma unroll
  for (int i = 0; i < 9; ++i) {
    const int idx = tid_ + 512 * i, k = min(idx >> 3, 512), p = idx & 7;
    v4u d = *(const v4u*)&stg[k][2 * (p ^ (k & 7))];
    st128(d, rx, idx < 513 * 8 ? lane * 16 : OOB, x1_chunk(min(w + 8 * i, 64), task) * 1024);
  }
  {
    const __amdgpu_buffer_rsrc_t rt = __builtin_amdgcn_make_buffer_rsrc(ws + OFF_WT, 0, 65 * 65536, 0x00020000);
    v2u* s2 = (v2u*)(lds + LDS_EB);
#pragma unroll
    for (int t = 0; t < 9; ++t) {
      const int k = lane + 64 * t;
      if (k <= 512) s2[k * 8 + (w ^ (k & 6))] = v2u{fw[0][t], fw[1][t]};
    }
    __syncthreads();
#pragma unroll
    for (int i = 0; i < 5; ++i) {
      const int idx = tid_ + 512 * i, k = min(idx >> 2, 512), p = idx & 3;
      const v4u d = *(const v4u*)&s2[k * 8 + ((2 * p) ^ (k & 6))];
      __builtin_amdgcn_raw_buffer_store_b128(d, rt, idx < 514 * 4 ? x1_chunk(idx >> 5, task) * 512 + (idx & 31) * 16 : OOB, 0, AUX_SC1);
    }
  }
}
__device__ __forceinline__ void fft_pass2(char* ws, int task, int w) {
  char* lds = g_lds;
  const __amdgpu_buffer_rsrc_t rx = __builtin_amdgcn_make_buffer_rsrc(ws + OFF_X1, 0, 65 * 131072, 0x00020000);
  const int lane = olane();
  const int k = task * 8 + w;
  const c32* tw = (const c32*)(lds + LDS_TW);
  c32* eb = (c32*)(lds + LDS_EB) + w * FFT_EB;
#define F2T(i)
  if (k > 512) return;
  F2T(0);
  c32 v[32];
  const int xoff = (lane >> 4) * 1024 + w * 128 + (lane & 15) * 8;
#pragma unroll
  for (int t = 0; t < 32; ++t) {
    v2u d = __builtin_amdgcn_raw_buffer_load_b64(rx, xoff, x1_chunk(task, 4 * t) * 1024, AUX_SC1);
    v[t] = c32{__uint_as_float(d[0]), __uint_as_float(d[1])};
  }
  unsigned wv[32];
  asm volatile("" : "+s"(task));
  {
    const __amdgpu_buffer_rsrc_t rt = __builtin_amdgcn_make_buffer_rsrc(ws + OFF_WT, 0, 65 * 65536, 0x00020000);
#pragma unroll
    for (int t = 0; t < 32; ++t)
      wv[t] = __builtin_amdgcn_raw_buffer_load_b32(rt, xoff >> 1, x1_chunk(task, 4 * t) * 512, AUX_SC1);
  }
  F2T(1);
  wave_fft<2048, -1>(v, eb, tw, lane);
  F2T(2);
#pragma unroll
  for (int t = 0; t < 32; ++t) v[t] = cmul<false>(v[t], c32{__uint_as_float(wv[t] << 16), __uint_as_float(wv[t] & 0xffff0000u)});
  F2T(3);
  wave_fft<2048, 1>(v, eb, tw, lane);
  F2T(4);
  asm volatile("" : "+s"(task));
#pragma unroll
  for (int t = 0; t < 32; ++t) {
    v2u d = {__float_as_uint(v[t].x), __float_as_uint(v[t].y)};
    __builtin_amdgcn_raw_buffer_store_b64(d, rx, xoff, x1_chunk(task, 4 * t) * 1024, AUX_SC1);
  }
  F2T(5);
  VMCNT(0);
  F2T(6);
}
#define OAUX3 0
#define OAUXX 0
__device__ __forceinline__ void fft_pass3(const char* ws, u16* __restrict__ out, int task, int w) {
  char* lds = g_lds;
  const __amdgpu_buffer_rsrc_t rx = __builtin_amdgcn_make_buffer_rsrc((void*)(ws + OFF_X1), 0, 65 * 131072, 0x00020000);
  const int lane = olane(), tid_ = w * 64 + lane;
  const c32* tw = (const c32*)(lds + LDS_TW);
  c32* eb = (c32*)(lds + LDS_EB) + w * 1088;
  c32(*stg)[16] = (c32(*)[16])(lds + LDS_STG);
  {
    v4u d[9];
#pragma unroll
    for (int i = 0; i < 9; ++i) {
      d[i] = __builtin_amdgcn_raw_buffer_load_b128(rx, (i < 8 ? lane : lane & 7) * 16, x1_chunk(min(w + 8 * i, 64), task) * 1024, AUX_SC1);
    }
#pragma unroll
    for (int i = 0; i < 9; ++i) {
      const int idx = tid_ + 512 * i, k = min(idx >> 3, 512), p = idx & 7;
      *(v4u*)&stg[k][2 * (p ^ (k & 7))] = d[i];
    }
  }
  __syncthreads();
  c32 v[16];
#pragma unroll
  for (int t = 0; t < 16; ++t) {
    const int n = lane + 64 * t;
    const int k = n <= 512 ? n : 1024 - n;
    const f32x4 xx = *(const f32x4*)&stg[k][2 * (w ^ (k & 7))];
    c32 xa = {xx[0], xx[1]}, xb = {xx[2], xx[3]};
    if (k == 0 || k == 512) {
      xa.y = 0.f;
      xb.y = 0.f;
    }
    v[t] = n <= 512 ? c32{xa.x - xb.y, xa.y + xb.x} : c32{xa.x + xb.y, -xa.y + xb.x};
  }
  wave_fft<1024, 1>(v, eb, tw, lane);
  const float sc = 1.f / (2048.f * 1024.f);
  const int r0 = task * 16 + 2 * w;
  const __amdgpu_buffer_rsrc_t ro = __builtin_amdgcn_make_buffer_rsrc(out, 0, S * 4096, 0x00020000);
#pragma unroll
  for (int t = 0; t < 16; ++t) {
    __builtin_amdgcn_raw_buffer_store_b16(f2bf(v[t].x * sc), ro, lane * 2 + t * 128, r0 * 4096, OAUX3);
    __builtin_amdgcn_raw_buffer_store_b16(f2bf(v[t].y * sc), ro, lane * 2 + t * 128, r0 * 4096 + 4096, OAUX3);
  }
}

#define OSL 4

#define OCK 800
#define OTRY 3
#define APR 0
#define OGN 5
#define OK1 150
#define NX2 0
#define WTO 2
#define WTS 30
#define WTN 2
#define RSO 5
#define NCAL 6
#define DOX2 300
#define DTC 100
#define NX1 4
#define NXG 2
#define NXK 5
#define OFD 200
#define STI 600
#define TNX 0
#define NXP 0
#define PACE 150
#define PFB 4
#define CV1 -20
#define CV2 125
#define EDL 0
constexpr int L_D = 16384, L_R = 32768, L_F = 122880;
__device__ __forceinline__ volatile LAS unsigned* flw(int i, int m) {
  return (volatile LAS unsigned*)((LAS char*)g_lds + L_F + i * 4 + (olane() & m) * 4);
}
template <int SLP = 0>
__device__ __forceinline__ void lds_wait_ge(int i, int m, unsigned v, gu32* tmo, unsigned code) {
  volatile LAS unsigned* p = flw(i, m);
  _Pragma("clang loop unroll(disable)") for (unsigned spins = 0;;) {
    if (__all(*p >= v)) break;
    if (SLP) __builtin_amdgcn_s_sleep(SLP);
    if (++spins > (kSpinCap << 5)) {
      tmo_report(tmo, code);
      break;
    }
  }
  asm volatile("" ::: "memory");
}
template <bool PRE>
__device__ __forceinline__ unsigned owner(const int c, const int w, const u64 nonce, const u16* __restrict__ mem0, char* ws,
                                          u16* __restrict__ out, unsigned perm, const char* wsl, const u16* ax, const u16* awr,
                                          const u16* awi) {
  LAS char* const L = (LAS char*)g_lds;
  gu32* tmo = (gu32*)(ws + OFF_TMO);
  const int lane = olane();
  const int l15 = lane & 15, g = lane >> 4;
  const __amdgpu_buffer_rsrc_t rs = __builtin_amdgcn_make_buffer_rsrc(ws, 0, (int)WS_USED, 0x00020000);
  const int xg = c / PO, li = c % PO;
  const int r_a = (int)(((uintptr_t)ws >> 19) & 7);
  const int r_s = r_a + (int)(OFF_STEP / UNIT);
  volatile LAS unsigned* fl = (volatile LAS unsigned*)(L + L_F);
  unsigned m0v = 0;
  if constexpr (PRE) {
  if (c == 0 && w == 6) hs_lead<4>(rs, true, CNT_HSO, 64u);
  if (WTO == 1 && wsl) {
    __builtin_amdgcn_s_sleep(WTS);
    for (int k = 0; k < WTN; ++k) w_touch(wsl, (li >> 1) * 128 + 64 + (li & 1) * 32 + (k & 1) * 16 - (k >> 1) * 64, 160000 + w * 256);
  }
  unsigned cal = 0;
  if (w == 0)
    st128(v4u{(unsigned)lane, 0u, 0u, 0u}, rs, (lane & 31) * 16 + (lane >> 5) * 32768, (int)OFF_SCR + (((li & 7) ^ xreg(r_a)) << 12) + (c >> 3) * 512);
  if (w == 0 && lane == 0) {
    const int cl = li & 7;
    int off = (int)OFF_SCR + ((cl ^ xreg(r_a)) << 12) + (c >> 3) * 512;
    unsigned best = 0xfffeu;
#pragma unroll 1
    for (int r = 0; r < NCAL; ++r) {
      VMCNT(0);
      const u64 t1 = __builtin_amdgcn_s_memtime();
      asm volatile("s_waitcnt lgkmcnt(0)" : "+v"(off)::"memory");
      const unsigned v = __builtin_amdgcn_raw_buffer_load_b32(rs, off, (r & 3) * 128 + (r >> 2) * 32768, AUX_SC1);
      asm volatile("s_waitcnt vmcnt(0)" ::"v"(v) : "memory");
      const u64 t2 = __builtin_amdgcn_s_memtime();
      best = min(best, (unsigned)(t2 - t1));
    }
    cal = ((unsigned)cl << 16) | best;
  }
  if (w == 0) {
    m0v = *(const unsigned*)(mem0 + (lane >> 3) * D + 16 * c + 2 * (lane & 7));
    hs_wait(rs, ws, nonce, CNT_HSO);
    unsigned id;
    asm volatile("s_getreg_b32 %0, hwreg(HW_REG_XCC_ID)" : "=s"(id));
    if (lane == 0) {
      __builtin_amdgcn_raw_buffer_store_b32(((id & 15u) << 19) | cal, rs, c * 4, (int)OFF_CAL, AUX_SC1);
      if (li == 0) __builtin_amdgcn_raw_buffer_store_b32(id & 15u, rs, xg * 4, (int)OFF_XID, AUX_SC1);
    }
  }
  __syncthreads();
  if (WTO == 2 && wsl)
    for (int k = 0; k < WTN; ++k) w_touch(wsl, (li >> 1) * 128 + 64 + (li & 1) * 32 + (k & 1) * 16 - (k >> 1) * 64, 160000 + w * 256);
  if (w == 7) {
    const unsigned pm = numa_perm(numa_load(rs, tmo), (LAS unsigned*)(L + L_F + 512));
    fl[10] = pm;
  }
  __syncthreads();
  perm = __builtin_amdgcn_readfirstlane(fl[10]);
  }
  const int xr0 = xreg(r_s + (PRE ? w : NARM + (li & 1)));
  const v4u sent = {0xffffffffu, 0xffffffffu, 0xffffffffu, 0xffffffffu};
  const int ibs = (int)OFF_VM + li * 65536 + ((cls_of(perm, xg) ^ xreg(r_a + (int)(OFF_VM / UNIT) + (li >> 3))) << 12);

  int am = (int)OFF_STEP + R_MEM + (c >> 4) * 32768 + ((cls_of(perm, g) ^ xr0) << 12) + (c & 15) * 256 + l15 * 16;
  int ap[2];
#pragma unroll
  for (int q = 0; q < 2; ++q) {
    const int o = c * 384 + (lane & 31) * 16;
    ap[q] = (lane & 31) < 24 ? (int)OFF_STEP + R_EX + (o >> 12) * 32768 + ((cls_of(perm, 2 * q + (lane >> 5)) ^ xr0) << 12) + (o & 4095)
                             : OOB;
  }
  int as = lane < 8 * NXC ? (int)OFF_STEP + R_ST + ((cls_of(perm, lane >> 3) ^ xr0) << 12) + (c >> 5) * 32768 + (c & 31) * 128 + (lane & 7) * 16
                          : OOB;
  const int ax1 = (int)OFF_STEP + R_PX + c * 1280 + lane * 16;
  const int ax2 = lane < 16 ? ax1 + 1024 : (lane < 24 ? (int)OFF_STEP + R_DUM + c * 128 + (lane - 16) * 16 : OOB);
#define ARM_REC(r)                                                                                                   \
  {                                                                                                                  \
    const int so_ = (r) * (int)UNIT, dx_ = (xreg(r_s + (r)) ^ xreg(r_s + (r) + 1)) << 12;                            \
    __builtin_amdgcn_raw_buffer_store_b128(sent, rs, am, so_, AUX_SC1);                                              \
    am ^= dx_;                                                                                                       \
    _Pragma("unroll") for (int q = 0; q < 2; ++q) {                                                                  \
      __builtin_amdgcn_raw_buffer_store_b128(sent, rs, ap[q], so_, AUX_SC1);                                         \
      ap[q] ^= dx_;                                                                                                  \
    }                                                                                                                \
    __builtin_amdgcn_raw_buffer_store_b128(sent, rs, as, so_, AUX_SC1);                                              \
    as ^= dx_;                                                                                                       \
    __builtin_amdgcn_raw_buffer_store_b128(sent, rs, ax1, so_, AUX_SC1);                                              \
    __builtin_amdgcn_raw_buffer_store_b128(sent, rs, ax2, so_, AUX_SC1);                                             \
  }
  if constexpr (PRE) {
    if (WTO == 3 && wsl)
      for (int k = 0; k < WTN; ++k) w_touch(wsl, (li >> 1) * 128 + 64 + (li & 1) * 32 + (k & 1) * 16 - (k >> 1) * 64, 160000 + w * 256);
    fft_in1(ax, awr, awi, c, w);
    if (w < NARM + (li & 1)) {
      if (w == 0) {
        const int row = lane >> 3, np = lane & 7;
#pragma unroll
        for (int r = 0; r < NXC; ++r)
          __builtin_amdgcn_raw_buffer_store_b32(min(m0v, 0xfffffffeu), rs,
                                                (int)OFF_STEP + R_MEM + (c >> 4) * 32768 + ((cls_of(perm, r) ^ xr0) << 12) + (c & 15) * 256 +
                                                    ((row & 1) + 2 * (np & 1) + 4 * (row >> 1)) * 16 + (np >> 1) * 4,
                                                0, AUX_SC1);
        am = OOB;
      }
      ARM_REC(w)
    }
#pragma unroll
    for (int k = 0; k < 4; ++k)
      __builtin_amdgcn_raw_buffer_store_b128(sent, rs, lane * 16 + k * 1024, ibs + vm_slot(w >> 1) + (w & 1) * 32768, AUX_SC1);
    if (c == 0 && w == 7) __builtin_amdgcn_raw_buffer_store_b32(perm, rs, lane << 28, OFF_PERM, AUX_SC1);
  }
  if constexpr (PRE) return perm;
  __syncthreads();
  if (w == 7) {
    if (lane < 32) fl[lane] = 0;
    arm_wait(rs, tmo);
  }
  __syncthreads();

  const int team = li & 1, c0 = c & ~1;
  const int l16 = lane * 16;
  const int cown = cls_of(perm, xg);
  if (w < 2) {
    const int eg = (c0 >> 2) & 1;
    typedef int v8i __attribute__((ext_vector_type(8)));
    v8i adr;
    adr[0] = l16;
    adr[1] = (lane >> 2) * 128 + (lane & 3) * 16;
    adr[2] = L_D + w * 8192 + eg * 4096 + l16;
    adr[3] = ((g & 1) + (l15 & 2) + 4 * ((g & 2) + (l15 & 1))) * 16 + (l15 >> 2) * 4;
    adr[4] = (l15 & 1) ? 0x03020706 : 0x05040100;
    adr[5] = ((l15 & 8) ? 0xeeee : 0x4444) | ((l15 & 2) ? 0xeeee0000 : 0x44440000);
    adr[6] = L_F + 48;
    adr[7] = adr[2] ^ 4096;
    const v4u rsv = {(unsigned)(uintptr_t)ws, (unsigned)((uintptr_t)ws >> 32) & 0xffffu, (unsigned)WS_USED, 0x00020000u};
    const int dtc_ = TN(12, DTC);
#pragma unroll 1
    for (int s = team; s < NSEG; s += 2) {
      EVDECL;
      const int so = (int)OFF_STEP + s * (int)UNIT;
      const int sp = so + R_PM + ((cown ^ xreg(r_s + s)) << 12);
      int sv = ibs + (w - team) * 65536 + vm_slot(s);
      int sv1 = sv + eg * 32768, sv2 = sv + (1 - eg) * 32768, sp1 = sp + eg * 32768, sp2 = sp + (1 - eg) * 32768;
      const int xn = xreg(r_s + s + 1);
      int sm[NXC];
#pragma unroll
      for (int q = 0; q < NXC; ++q) {
        sm[q] = so + (int)UNIT + R_MEM + (c0 >> 4) * 32768 + ((cls_of(perm, q) ^ xn) << 12) + ((c0 + w) & 15) * 256;
        asm volatile("" : "+s"(sm[q]));
      }
      asm volatile("" : "+s"(sv1), "+s"(sv2), "+s"(sp1), "+s"(sp2));
      if (s >= 2) lds_wait_ge<4>(20, 3, s - 1, tmo, 0x30017);
      v4u bl[8];
      u64 tp64 = 0;
      if (s < NSEG - 1) {
        v32u blv;
        unsigned cnt, bad;
        asm volatile(OW_F
                     : [cnt] "=&s"(cnt), [st] "=&s"(bad), [tp] "=&s"(tp64), "={v[0:31]}"(blv) EVOUT
                     : "{v[112:119]}"(adr), [rs] "s"(rsv), [sv1] "s"(sv1), [sv2] "s"(sv2), [sp1] "s"(sp1), [sp2] "s"(sp2),
                       [sm0] "s"(sm[0]), [sm1] "s"(sm[1]), [sm2] "s"(sm[2]), [sm3] "s"(sm[3])
                     : OF_CLOB, "vcc", "scc", "memory");
#pragma unroll
        for (int k = 0; k < 8; ++k) bl[k] = v4u{blv[4 * k], blv[4 * k + 1], blv[4 * k + 2], blv[4 * k + 3]};
        if (__builtin_expect(bad != 0u, 0)) {
          v4u pv[8];
          _Pragma("clang loop unroll(disable)") for (unsigned spins = 0;;) {
            OPQ(va, l16);
            OPQ(v2, adr[1]);
#pragma unroll
            for (int k = 0; k < 8; ++k) bl[k] = __builtin_amdgcn_raw_buffer_load_b128(rs, va + (k & 3) * 1024, k < 4 ? sv1 : sv2, AUX_SC1);
#pragma unroll
            for (int k = 0; k < 8; ++k)
              pv[k] = __builtin_amdgcn_raw_buffer_load_b128(rs, v2 + (k & 1) * 2048 + (k >> 2) * 64, (k & 2) ? sp2 : sp1, AUX_SC1);
            unsigned m = 0;
#pragma unroll
            for (int k = 0; k < 8; ++k) m = mx4(mx4(m, pv[k]), bl[k]);
            if (!anysent(m)) break;
            __builtin_amdgcn_s_sleep(RSO);
            if (spin_fail(spins, tmo, 0x20000 + s)) break;
          }
          f32x4 a0 = {0.f, 0.f, 0.f, 0.f};
#pragma unroll
          for (int k = 0; k < 4; ++k) {
            v8u b = cat8(bl[2 * k], bl[2 * k + 1]);
#pragma unroll
            for (int j = 0; j < 8; j += 2) {
              const auto q = __builtin_amdgcn_permlane16_swap(b[j], b[j + 1], false, false);
              b[j] = q[0];
              b[j + 1] = q[1];
            }
            a0 = smf(pv[k], b, a0, adr[5]);
          }
#pragma unroll
          for (int k = 0; k < 4; ++k) {
            LAS char* const pd = L + adr[k < 2 ? 2 : 7] + (k & 1) * 2048;
            a0 = smf1(pv[4 + k], cat8(*(const LAS v4u*)pd, *(const LAS v4u*)(pd + 1024)), a0, adr[5]);
          }
          const auto q0 = __builtin_amdgcn_permlane32_swap(__float_as_uint(a0[0]), __float_as_uint(a0[2]), false, false);
          const auto q1 = __builtin_amdgcn_permlane32_swap(__float_as_uint(a0[1]), __float_as_uint(a0[3]), false, false);
          const unsigned own = pk2(__uint_as_float(q0[0]) + __uint_as_float(q0[1]), __uint_as_float(q1[0]) + __uint_as_float(q1[1]));
          const unsigned nb = (unsigned)__builtin_amdgcn_mov_dpp((int)own, 0xb1, 0xf, 0xf, true);
          const unsigned o = __builtin_amdgcn_perm(nb, own, (unsigned)adr[4]);
#pragma unroll
          for (int q = 0; q < NXC; ++q) __builtin_amdgcn_raw_buffer_store_b32(o, rs, adr[3], sm[q], AUX_SC1);
          tp64 = cyc32();
          EV(3);
        }
      } else {
        BARE_BARRIER();
        wait_cyc(*flw(12, 0));
        _Pragma("clang loop unroll(disable)") for (unsigned spins = 0;;) {
          OPQ(va, l16);
          unsigned m = 0;
#pragma unroll
          for (int k = 0; k < 8; ++k) bl[k] = __builtin_amdgcn_raw_buffer_load_b128(rs, va + (k & 3) * 1024, k < 4 ? sv1 : sv2, AUX_SC1);
#pragma unroll
          for (int k = 0; k < 8; ++k) m = mx4(m, bl[k]);
          if (!anysent(m)) break;
          __builtin_amdgcn_s_sleep(RSO);
          if (spin_fail(spins, tmo, 0x20000 + s)) break;
        }
      }
      const unsigned tp = (unsigned)tp64;
      *flw(2 + w, 0) = s + 1;
      if (s < NSEG - 1) {
        const v4u tv = *(volatile LAS v4u*)flw(12, 0);
        const int lr = olane();
        __builtin_amdgcn_raw_buffer_store_b32(min(((tp - tv[1]) * tv[2]) >> 16, 60000u), rs,
                                              ((cls_of(perm, lr & 3) ^ xn) << 12) | ((lr & 4) << 9) | ((lr & 8) << 12) | ((lr >> 4) << 28),
                                              so + (int)UNIT + R_ST + (c0 + w) * 32 + 4, AUX_SC1);
      }
      OPQ(vs, l16);
#pragma unroll
      for (int k = 0; k < 8; ++k) *(LAS v4u*)(L + w * 8192 + ((k ^ (4 * eg)) * 1024) + vs) = bl[k];
      asm volatile("s_waitcnt lgkmcnt(0)" ::: "memory");
      *flw(4 + w, 0) = s + 1;
      if (s < NSEG - 4) {
        lds_wait_ge(2, 1, s + 1, tmo, 0x3001a);
        v4u se;
        asm volatile("v_mov_b32 %0, -1" : "=v"(se[0]));
        se[1] = se[2] = se[3] = se[0];
#pragma unroll
        for (int k = 0; k < 8; ++k) __builtin_amdgcn_raw_buffer_store_b128(se, rs, vs + (k & 3) * 1024, sv + (k >> 2) * 32768, AUX_SC1);
        asm volatile("s_nop 1" ::"v"(se));
      }
      if (s < NSEG - 2) {
        wait_cyc(tp + dtc_);
        const int lt = olane();
        const unsigned tc = __builtin_amdgcn_raw_buffer_load_b32(rs, lt < 40 ? lt * 128 : OOB, so + R_PX + ((li & ~1) + w) * 5120, AUX_SC1);
        asm volatile("" ::"v"(tc));
      }
      EVDUMP(s);
    }
  } else if (w < 6) {
    const int n = w - 2;
    const unsigned vpm_ = perm | (unsigned)((r_a + (int)(OFF_VWX / UNIT)) & 7) << 12;
    const int dox_ = TN(10, DOX2), pace_ = TN(11, PACE);
    v4u raw[2][8];
#define RAW_LD(dst, sg)                                                                                       \
  _Pragma("unroll") for (int t = 0; t < 2; ++t) _Pragma("unroll") for (int k = 0; k < 8; ++k) dst[t][k] =     \
      __builtin_amdgcn_raw_buffer_load_b128(rs, l16 + (k & 3) * 1024, vw_addr(vpm_, c0 + t, sg, 2 * n + (k >> 2)), AUX_SC1);
#pragma unroll
    for (int k = 0; k < 16; ++k) asm volatile("" : "=v"(raw[k >> 3][k & 7]));
    _Pragma("clang loop unroll(disable)") for (unsigned spins = 0, got = 0; got != 255u;) {
      OPQ(vfl, F_VW + flag_of(team, 8 * n + (olane() & 7)));
      const unsigned nw = (unsigned)__builtin_amdgcn_ballot_w64(__builtin_amdgcn_raw_buffer_load_b32(rs, vfl, 0, AUX_SC1) == 0u) & 255u & ~got;
      got |= nw;
#pragma unroll
      for (int k = 0; k < 8; ++k)
        if (nw >> k & 1u) {
#pragma unroll
          for (int t = 0; t < 2; ++t)
            raw[t][k] = __builtin_amdgcn_raw_buffer_load_b128(rs, l16 + (k & 3) * 1024, vw_addr(vpm_, c0 + t, team, 2 * n + (k >> 2)), AUX_SC1);
        }
      if (!nw) __builtin_amdgcn_s_sleep(OSL);
      if (spin_fail(spins, tmo, 0x30018)) break;
    }
    int nrdy = team;
#pragma unroll 1
    for (int s = team; s < NSEG; s += 2) {
      EVDECL;
      const int so = (int)OFF_STEP + s * (int)UNIT;
      if (s < NSEG - 1) {
        const int ln = olane();
        OPQ(eidx, (ln & 4) ? 0xeeee : 0x4444);
        OPQ(vea, (ln & 4) * 128 + (ln & 8) * 32 + (ln >> 4) * 64 + (ln & 3) * 16);
        int se = so + R_EX + n * 32768 + ((cown ^ xreg(r_s + s)) << 12);
        v8u bx[2][4];
#pragma unroll
        for (int t = 0; t < 2; ++t)
#pragma unroll
          for (int k = 0; k < 4; ++k) {
            const v4u u = raw[t][2 * k], v = raw[t][2 * k + 1];
            bx[t][k] = v8u{u[0], v[0], u[1], v[1], u[2], v[2], u[3], v[3]};
            asm volatile("" : "+v"(bx[t][k]));
          }
        OPQ(vd, L_D + n * 2048 + (ln & 31) * 16 + (ln >> 5) * 8);
        f32x4 d[2][4];
#pragma unroll
        for (int t = 0; t < 2; ++t)
#pragma unroll
          for (int k = 0; k < 4; ++k) d[t][k] = zero4();
        asm volatile("" : "+s"(se));
        EV(3);
        BARE_BARRIER();
        if (EDL) asm volatile("s_nop %0" ::"n"(EDL > 0 ? EDL - 1 : 0));
        EV(0);
#define E_PACK(t, k) v2u{pk2(d[t][k][0], d[t][k][1]), pk2(d[t][k][2], d[t][k][3])}
        _Pragma("clang loop unroll(disable)") for (unsigned spins = 0;;) {
          v4u pv[4];
          asm volatile("" : "+v"(vea));
#pragma unroll
          for (int k = 0; k < 4; ++k) pv[k] = __builtin_amdgcn_raw_buffer_load_b128(rs, vea + k * 1024, se, AUX_SC1);
          unsigned m = 0;
          v2u cv[2][4];
          SB();
#pragma unroll
          for (int k = 0; k < 4; ++k) {
            d[0][k] = smf(pv[k], bx[0][k], d[0][k], eidx);
            m = mx4(m, pv[k]);
            asm volatile("" : "+v"(m), "+v"(d[0][k]));
            d[1][k] = smf(pv[k], bx[1][k], d[1][k], eidx);
            asm volatile("" : "+v"(d[1][k]));
            if (k > 0) {
              const int j = k > 0 ? k - 1 : 0;
              asm volatile("" : "+v"(d[0][j]), "+v"(d[1][j]));
              cv[0][j] = E_PACK(0, j);
              cv[1][j] = E_PACK(1, j);
              *(LAS v2u*)(L + vd + j * 512) = cv[0][j];
              *(LAS v2u*)(L + vd + j * 512 + 8192) = cv[1][j];
            }
            SB();
          }
          EV(1);
          *(LAS v2u*)(L + vd + 1536) = E_PACK(0, 3);
          SB();
          *(LAS v2u*)(L + vd + 9728) = E_PACK(1, 3);
          if (__builtin_expect(!anysent(m), 1)) break;
          __builtin_amdgcn_s_sleep(RSO);
          if (spin_fail(spins, tmo, 0x10000 + s)) break;
#pragma unroll
          for (int t = 0; t < 2; ++t)
#pragma unroll
            for (int k = 0; k < 4; ++k) d[t][k] = zero4();
        }
        EV(2);
        RAW_BARRIER();
      } else BARE_BARRIER();
      v4u nx[2][8];
      const int sn = min(s + 2, NSEG - 1);
      unsigned tpb = 0;
      if (s < NSEG - 2) {
        __builtin_amdgcn_s_sleep(5);
        lds_wait_ge(2, 1, s + 1, tmo, 0x30014);
        tpb = cyc32();
        const unsigned tw = tpb + dox_;
        _Pragma("clang loop unroll(disable)") for (unsigned spins = 0; nrdy < s + 2;) {
          const int lf = olane();
          OPQ(vfl, F_VW + flag_of(min(s + 2 + 2 * (lf >> 3), NSEG - 2 + team), 8 * n + (lf & 7)));
          const u64 miss = __builtin_amdgcn_ballot_w64(__builtin_amdgcn_raw_buffer_load_b32(rs, vfl, 0, AUX_SC1) != 0u);
          nrdy = s + 2 * (miss ? __builtin_ctzll(miss) >> 3 : 8);
          if (nrdy >= s + 2) break;
          __builtin_amdgcn_s_sleep(8);
          if (spin_fail(spins, tmo, 0x30018)) break;
        }
        wait_cyc(tw);
      }
      EV(6);
      OPQ(va, s < NSEG - 2 ? l16 : OOB);
#define NX_LD(dst, q0, q1)                                                                              \
  _Pragma("unroll") for (int q = q0; q < q1; ++q) dst[q >> 3][q & 7] = __builtin_amdgcn_raw_buffer_load_b128( \
      rs, va + (q & 3) * 1024, vw_addr(vpm_, c0 + (q >> 3), sn, 2 * n + ((q >> 2) & 1)), AUX_SC1);
      NX_LD(nx, 0, NX1)
      SB();
      if (s < NSEG - 2) wait_cyc(cyc32() + ((n + (li >> 1)) & 3) * pace_);
      else lds_wait_ge(4, 1, s + 1, tmo, 0x30014);
      EV(4);
      f32x4 ac[2][2];
      const int sx = so + R_PX + n * 8192, sy = so + R_PX + 32768 + n * 2048;
      v4u pv[2][10], vm[2][2];
      OPQ(vb, l16);
#define P_LD                                                                                         \
  _Pragma("unroll") for (int k = 0; k < 10; ++k) _Pragma("unroll") for (int rt = 0; rt < 2; ++rt) pv[rt][k] = \
      __builtin_amdgcn_raw_buffer_load_b128(rs, vb + (k & 3) * 1024, (k < 8 ? sx + (k >> 2) * 4096 : sy) + rt * 40960, AUX_SC1);
#define P_MM(k0, k1)                                                                                 \
  _Pragma("unroll") for (int k = k0; k < k1; ++k) _Pragma("unroll") for (int rt = 0; rt < 2; ++rt) {  \
    _Pragma("unroll") for (int t = 0; t < 2; ++t)                                                    \
      ac[rt][t] = mma(k < 8 ? raw[t][k & 7] : vm[t][k & 1], pv[rt][k], k == 0 ? f32x4{0.f, 0.f, 0.f, 0.f} : ac[rt][t]); \
    asm volatile("" : "+v"(ac[rt][0]), "+v"(ac[rt][1]));                                             \
  }
      P_LD
      SB();
      NX_LD(nx, NX1, NX1 + NX2)
      SB();
      lds_wait_ge(4, 1, s + 1, tmo, 0x30014);
#pragma unroll
      for (int t = 0; t < 2; ++t)
#pragma unroll
        for (int k = 0; k < 2; ++k) vm[t][k] = *(const LAS v4u*)(L + vb + t * 8192 + n * 2048 + k * 1024);
      SB();
      P_MM(0, 10)
      if (__builtin_expect(__builtin_amdgcn_ballot_w64(__builtin_isunordered(ac[0][0][0], ac[1][0][0])) != 0ull, 0)) {
        _Pragma("clang loop unroll(disable)") for (unsigned spins = 0;;) {
          asm volatile("" : "+v"(vb));
          P_LD
          unsigned m = 0;
#pragma unroll
          for (int k = 0; k < 10; ++k) m = mx4(mx4(m, pv[0][k]), pv[1][k]);
          if (!anysent(m)) break;
          __builtin_amdgcn_s_sleep(8);
          if (spin_fail(spins, tmo, 0x30015)) break;
        }
        P_MM(0, 10)
      }
      EV(7);
      NX_LD(raw, NX1 + NX2, 16)
      SB();
      OPQ(vr, L_R + (s & 2) * 8192 + l16);
#pragma unroll
      for (int q = 0; q < 4; ++q) *(LAS f32x4*)(L + vr + q * 4096 + n * 1024) = ac[q >> 1][q & 1];
      asm volatile("s_waitcnt lgkmcnt(0)" ::: "memory");
      fl[20 + n] = s + 1;
      lds_wait_ge(20, 3, s + 1, tmo, 0x30016);
      {
        const LAS f32x4* pr = (const LAS f32x4*)(L + vr + n * 4096);
        const f32x4 o = (pr[0] + pr[64]) + (pr[128] + pr[192]);
        const int lo = olane();
        __builtin_amdgcn_raw_buffer_store_b64(v2u{pk2(o[0], o[1]), pk2(o[2], o[3])}, __builtin_amdgcn_make_buffer_rsrc(out, 0, S * 4096, 0x00020000),
                                              (lo & 15) * 8192 + (lo >> 4) * 8, (s * 32 + (n >> 1)) * 4096 + 2048 + 32 * (c0 + (n & 1)), OAUXX);
      }
      EV(5);
#pragma unroll
      for (int q = 0; q < NX1 + NX2; ++q) raw[q >> 3][q & 7] = nx[q >> 3][q & 7];
      EVDUMP(s);
    }
  } else if (w == 6) {
    if (c == 1) hs_lead<64>(rs, false, CNT_HS, 192u);
#pragma unroll 1
    for (int s = team; s + NARM - 2 < NSEG; s += 2) {
      VMCNT(0);
      BARE_BARRIER();
      if (s + NARM >= NSEG) break;
      BARE_BARRIER();
      __builtin_amdgcn_s_sleep(5);
      lds_wait_ge(2, 1, s + 1, tmo, 0x3001b);
      if (APR) __builtin_amdgcn_s_setprio(APR);
      ARM_REC(s + NARM)
      if (s + NARM + 1 < NSEG) ARM_REC(s + NARM + 1)
      if (APR) __builtin_amdgcn_s_setprio(0);
    }
  } else {
    const int sdk_ = NS2F(TN(2, SDK)), cv1_ = NS2F(TN(3, CV1)), cv2_ = NS2F(TN(4, CV2));
    Clk ck;
    clk_init(ck);
    unsigned t1 = cyc32();
    const int mg_ = NS2F(TN(0, MARG)), tol_ = NS2F(TN(13, TOL)), xl_ = NS2F(TN(14, XLATE));
    Pace pc = {0u, NS2F(ED0), NS2F(EA0), 0};
    const int sti_ = TN(15, STI), ofd_ = NS2F(TN(9, OFD));
#pragma unroll 1
    for (int s = 0; s < NSEG; ++s) {
      EVDECL;
      const bool on = (s & 1) == team;
      EV(3);
      if (on ? (int)(t1 - cyc32()) > OCK : s < 4) {
        wait_cyc(t1 - OCK);
        clk_update<OTRY>(ck, s < 4 ? 12 : OGN);
      }
      int k1 = ((cv1_ * (int)ck.cq) >> 16) - OK1, k2 = (cv2_ * (int)ck.cq) >> 16;
      int sst = (int)OFF_STEP + s * (int)UNIT + R_ST + ((cown ^ xreg(r_s + s)) << 12);
      asm volatile("" : "+s"(k1), "+s"(k2), "+s"(sst));
      OPQ(va, lane * 32);
      OPQ(vt, L_F + 48);
      wait_cyc(t1);
      __builtin_amdgcn_s_setprio(3);
      EV(0);
      v2u st = __builtin_amdgcn_raw_buffer_load_b64(rs, va, sst, AUX_SC1);
      if (__builtin_expect(anysent(st[0]), 0)) _Pragma("clang loop unroll(disable)") for (unsigned spins = 0;;) {
        if (spins >= 2) __builtin_amdgcn_s_sleep(4);
        if (spin_fail(spins, tmo, 0x3000b)) break;
        asm volatile("" : "+v"(va));
        wait_cyc(t1 + spins * sti_);
        st = __builtin_amdgcn_raw_buffer_load_b64(rs, va, sst + (spins == 1 ? 2048 : (spins == 2 ? 32768 : 0)), AUX_SC1);
        if (spins > 2) clk_update(ck, 8);
        if (!anysent(st[0])) break;
      }
      EV(4);
      u64 tn;
      asm volatile("s_memtime %0" : "=s"(tn), "+v"(st));
      unsigned md;
      const int dm = stamp_max(ck, st, md);
      const unsigned tr = clk_cyc(ck, dm);
      if (on) {
        asm volatile("s_waitcnt lgkmcnt(0)" : "+s"(tn));
        *(volatile LAS v4u*)(L + vt) = v4u{tr + k2, tr, ck.inv, 0u};
        EV(1);
        int d = (int)(tr + k1 - (unsigned)tn);
        d = (unsigned)d > (unsigned)CMAX ? 0 : d;
        asm volatile("1: s_sub_u32 %0, %0, 28\n\ts_cbranch_scc0 1b\n\ts_barrier" : "+s"(d)::"scc", "memory");
        if (s < NSEG - 1) BARE_BARRIER();
      }
      __builtin_amdgcn_s_setprio(0);
      EV(5);
      pace_step(pc, s, (ck.r << 4) + dm, md, mg_, tol_, xl_);
      t1 = clk_cyc(ck, dm + pc.dq + pc.ea - sdk_ + (on ? ofd_ : 0));
    }
  }
  return 0u;
}
#define FSL 32
__global__ __launch_bounds__(512, 2) void mega_kernel(const u16* __restrict__ x, const u16* __restrict__ mem0,
                                                      const u16* __restrict__ Wq, const u16* __restrict__ Wk,
                                                      const u16* __restrict__ Wv, const u16* __restrict__ Wo,
                                                      char* __restrict__ ws, u16* __restrict__ out,
                                                      const u16* __restrict__ ax, const u16* __restrict__ awr,
                                                      const u16* __restrict__ awi) {
  asm volatile(".Lkbeg:");
  ws += (size_t)(-(uintptr_t)ws) & (UNIT - 1);
  const int bx = blockIdx.x;
  const int w = __builtin_amdgcn_readfirstlane(threadIdx.x >> 6);
  const int xg = bx & 7, idx = bx >> 3;
#define TEND
#define TMID(k)
  const u64 nonce = hs_post(__builtin_amdgcn_make_buffer_rsrc(ws, 0, 65536, 0x00020000), w);
  TMID(0);
  int role, h, par = 0;
  if (xg < NXC) {
    role = idx >= 2 * PQ ? 4 : idx >= PQ;
    h = xg * PQ + idx % PQ;
  } else {
    role = 2 + (idx / PQ & 1);
    par = idx / (2 * PQ);
    h = (xg - NXC) * PQ + idx % PQ;
  }
  if (CPF && role == 4 && (w == 1 || w == 2)) code_touch(idx - 16 * (w - 1), 160000 + 256 * w);
  if (role == 0) wroles<0>(h, par, idx & 7, xg, w, nonce, x, Wq, Wk, Wv, Wo, ws);
  if (role == 1) wroles<1>(h, par, idx & 7, xg, w, nonce, x, Wq, Wk, Wv, Wo, ws);
  if (role == 2) wroles<2>(h, par, idx & 7, xg, w, nonce, x, Wq, Wk, Wv, Wo, ws);
  if (role == 3) wroles<3>(h, par, idx & 7, xg, w, nonce, x, Wq, Wk, Wv, Wo, ws);
  const int oc = xg * PO + idx - 2 * PQ;
  unsigned perm = 0;
  if (role == 4) perm = owner<true>(oc, w, nonce, mem0, ws, out, 0u, w_slow(Wq, Wk, Wv, w, xg), ax, awr, awi);
  TMID(2);
  char* lds = g_lds;
  const __amdgpu_buffer_rsrc_t rs = __builtin_amdgcn_make_buffer_rsrc(ws, 0, 65536, 0x00020000);
  gu32* tmo = (gu32*)(ws + OFF_TMO);
  volatile LAS int* sact = (volatile LAS int*)(g_lds + 163584);
  unsigned idle = 0;
#define TLOG(id)
  auto pick = [&]() __attribute__((always_inline)) {
    int off = CNT_FFT1 + (olane() & 7) * 128;
    asm volatile("" : "+v"(off));
    const unsigned v = __builtin_amdgcn_raw_buffer_load_b32(rs, off, 0, AUX_SC1) + 1u;
    const unsigned d1 = __builtin_amdgcn_readlane(v, 0), d2 = __builtin_amdgcn_readlane(v, 1);
    const unsigned q1 = __builtin_amdgcn_readlane(v, 4), q2 = __builtin_amdgcn_readlane(v, 5), q3 = __builtin_amdgcn_readlane(v, 6);
    int kind = -1, qoff = 0;
    unsigned lim = 0;
    if (q1 < 64u) kind = 1, qoff = Q_FFT1, lim = 64u;
    else if (d1 >= 128u && q2 < 65u) kind = 2, qoff = Q_FFT2, lim = 65u;
    else if (d2 >= 65u && q3 < 128u) kind = 3, qoff = Q_FFT3, lim = 128u;
    else if (q3 >= 128u) kind = -2;
    int act = kind;
    if (kind >= 0) {
      unsigned t = 0;
      if (olane() == 0) t = __hip_atomic_fetch_add((gu32*)(ws + qoff), 1u, RLX_AGENT) + 1u;
      t = __builtin_amdgcn_readfirstlane(t);
      act = t < lim ? (kind << 16) | (int)t : -3;
    }
    return act;
  };
  fft_table(lds, w);
  int done = role == 4 ? CNT_ARM : 0;
  for (;;) {
    if (w == 7) *sact = role == 4 ? (done == CNT_ARM ? 0x10040 | oc : -2) : pick();
    VMCNT(0);
    RAW_BARRIER();
    if (done && w == 0 && olane() == 0) __hip_atomic_fetch_add((gu32*)(ws + done), 1u, RLX_AGENT);
    done = 0;
    const int act = __builtin_amdgcn_readfirstlane(*sact);
    RAW_BARRIER();
    if (act == -2) break;
    if (act >= 0) {
      const int kind = act >> 16, t = act & 0xffff;
      TLOG(1000 + kind);
      if (kind == 1) {
        fft_pass1(ws, ax, awr, awi, t ^ 64, w, role == 4 ? x : nullptr);
        done = CNT_FFT1;
      } else if (kind == 2) {
        fft_pass2(ws, t == 0 ? 64 : t - 1, w);
        done = CNT_FFT2;
      } else fft_pass3(ws, out, t, w);
      TLOG(999);
    } else if (act == -1) {
      __builtin_amdgcn_s_sleep(FSL);
      if (spin_fail(idle, tmo, 0x30022)) break;
    }
  }
  if (role == 4) {
    owner<false>(oc, w, nonce, mem0, ws, out, perm, nullptr, nullptr, nullptr, nullptr);
    return;
  }
  TEND;
}

extern "C" void kernel_launch(void* const* d_in, const int* in_sizes, int n_in, void* d_out, int out_size, void* d_ws,
                              size_t ws_size, hipStream_t stream) {
  const u16* A_x = (const u16*)d_in[0];
  const u16* A_wr = (const u16*)d_in[1];
  const u16* A_wi = (const u16*)d_in[2];
  const u16* B_x = (const u16*)d_in[3];
  const u16* B_mem0 = (const u16*)d_in[4];
  const u16* Wq = (const u16*)d_in[5];
  const u16* Wk = (const u16*)d_in[6];
  const u16* Wv = (const u16*)d_in[7];
  const u16* Wo = (const u16*)d_in[8];
  u16* out = (u16*)d_out;
  char* ws = (char*)d_ws;

  mega_kernel<<<256, 512, 0, stream>>>(B_x, B_mem0, Wq, Wk, Wv, Wo, ws, out, A_x, A_wr, A_wi);
}
```

```cpp
#include <hip/hip_runtime.h>
#include <hip/hip_bf16.h>
#include <stdint.h>
#include <type_traits>

typedef unsigned short u16;
typedef unsigned long long u64;
using bf16x8 = __attribute__((ext_vector_type(8))) __bf16;
using bf16x16 = __attribute__((ext_vector_type(16))) __bf16;
using f32x4 = __attribute__((ext_vector_type(4))) float;
typedef unsigned v32u __attribute__((ext_vector_type(32)));
typedef unsigned v16u __attribute__((ext_vector_type(16)));
typedef unsigned v8u __attribute__((ext_vector_type(8)));
typedef unsigned v4u __attribute__((ext_vector_type(4)));
typedef unsigned v2u __attribute__((ext_vector_type(2)));
typedef short s16x4 __attribute__((ext_vector_type(4)));
typedef float f32x2 __attribute__((ext_vector_type(2)));
#define GAS __attribute__((address_space(1)))
#define LAS __attribute__((address_space(3)))
typedef GAS unsigned gu32;
#define RLX_AGENT __ATOMIC_RELAXED, __HIP_MEMORY_SCOPE_AGENT

constexpr int S = 2048, D = 1024, H = 32, DH = 128, SEG = 32, NSEG = 64;
constexpr int DF = 513;

constexpr int NXC = 4;
constexpr int PQ = 32 / NXC, PO = 64 / NXC;

__shared__ __attribute__((aligned(16))) char g_lds[163840];
#define SPINCAP (1u << 18)
constexpr unsigned kSpinCap = SPINCAP;
constexpr int AUX_SC1 = 16;
constexpr int OOB = 0x7ff00000;
constexpr size_t UNIT = 524288;
constexpr int F_QK = 0, F_VW = 8192, CNT_FFT1 = 16384, CNT_FFT2 = 16512, CNT_ARM = 16640;
constexpr int Q_FFT1 = 16896, Q_FFT2 = 17024, Q_FFT3 = 17152;
constexpr size_t OFF_TMO = 20480;
constexpr size_t OFF_CAL = 24576, OFF_XID = 25088;
constexpr int OFF_PERM = 25600;
constexpr size_t ARM_HOST = 28672;
constexpr int OFF_REQ = 49152, OFF_ACK = 53248, CNT_HS = 17280, CNT_HSO = 17408;
constexpr size_t OFF_SCR = 65536;
constexpr size_t OFF_VM = UNIT;
constexpr int VM_SLOT = 4 << 20, VM_SLOT2 = 8 << 20;
constexpr size_t OFF_DBG = 3 * UNIT;
constexpr size_t OFF_STEP = 27 * UNIT;
constexpr int R_MEM = 0, R_EX = 131072, R_PM = 262144, R_ST = 327680, R_PX = 393216, R_DUM = 475136;
constexpr size_t OFF_QK = (size_t)48 << 20;
constexpr size_t OFF_X1 = (size_t)80 << 20;
constexpr size_t OFF_VWX = (size_t)89 << 20;
constexpr size_t WS_USED = (size_t)244 << 20;
constexpr size_t OFF_WT = (size_t)244 << 20;
constexpr int NARM = 4;

__device__ __forceinline__ int olane() {
  int t;
  asm volatile("v_mbcnt_lo_u32_b32 %0, -1, 0\n\tv_mbcnt_hi_u32_b32 %0, -1, %0" : "=v"(t));
  __builtin_assume(t >= 0 && t < 64);
  return t;
}
__device__ __forceinline__ float bf2f(u16 v) { return __uint_as_float((unsigned)v << 16); }
__device__ __forceinline__ unsigned pk2(float a, float b) {
  typedef __bf16 b2 __attribute__((ext_vector_type(2)));
  f32x2 f = {a, b};
  b2 r = __builtin_convertvector(f, b2);
  return __builtin_bit_cast(unsigned, r);
}
__device__ __forceinline__ unsigned pk2s(float a, float b) { return min(pk2(a, b), 0xfffffffeu); }
__device__ __forceinline__ u16 f2bf(float f) { return (u16)(pk2(f, 0.f) & 0xffffu); }
__device__ __forceinline__ float rbf(float f) { return __uint_as_float(pk2(0.f, f)); }
__device__ __forceinline__ float opq_one() {
  float o = 1.f;
  asm("" : "+s"(o));
  return o;
}
__device__ __forceinline__ unsigned fold_pk(f32x4 a, float one) { return pk2(a[0] + a[2], __builtin_fmaf(a[1], one, a[3])); }
__device__ __forceinline__ float mul1(float a, float c) {
  float r;
  asm("v_mul_f32 %0, %2, %1" : "=v"(r) : "v"(a), "s"(c));
  return r;
}
__device__ __forceinline__ float fma1(float a, float c, float b) {
  float r;
  asm("v_fma_f32 %0, %1, %2, %3" : "=v"(r) : "v"(a), "s"(c), "v"(b));
  return r;
}
__device__ __forceinline__ int flag_of(int seg, int h) { return h * 256 + (seg & 1) * 128 + (seg >> 1) * 4; }
__device__ __forceinline__ int xreg(int r) { return ((r & 1) << 2) | ((r >> 1) & 1) | ((r >> 1) & 2); }

__device__ __forceinline__ void tmo_report(gu32* tmo, unsigned code) {
  unsigned z = 0, c = code;
  asm volatile("" : "+v"(z), "+v"(c));
  asm volatile("global_store_dword %0, %1, %2 sc1" ::"v"(z), "v"(c), "s"(tmo) : "memory");
}
__device__ __forceinline__ bool spin_fail(unsigned& spins, gu32* tmo, unsigned code) {
  asm volatile("" ::: "memory");
  if (__builtin_expect(++spins > kSpinCap, 0)) {
    tmo_report(tmo, code);
    return true;
  }
  return false;
}
__device__ __forceinline__ unsigned wave_min(unsigned v) {
  v = min(v, (unsigned)__builtin_amdgcn_mov_dpp((int)v, 0x121, 0xf, 0xf, true));
  v = min(v, (unsigned)__builtin_amdgcn_mov_dpp((int)v, 0x122, 0xf, 0xf, true));
  v = min(v, (unsigned)__builtin_amdgcn_mov_dpp((int)v, 0x124, 0xf, 0xf, true));
  v = min(v, (unsigned)__builtin_amdgcn_mov_dpp((int)v, 0x128, 0xf, 0xf, true));
  auto r = __builtin_amdgcn_permlane32_swap(v, v, false, false);
  v = min(r[0], r[1]);
  auto q = __builtin_amdgcn_permlane16_swap(v, v, false, false);
  return min(q[0], q[1]);
}
__device__ __forceinline__ float xmax16_32(float x) {
  auto r = __builtin_amdgcn_permlane32_swap(__float_as_uint(x), __float_as_uint(x), false, false);
  x = __builtin_amdgcn_fmed3f(__uint_as_float(r[0]), __uint_as_float(r[1]), __builtin_inff());
  auto q = __builtin_amdgcn_permlane16_swap(__float_as_uint(x), __float_as_uint(x), false, false);
  return __builtin_amdgcn_fmed3f(__uint_as_float(q[0]), __uint_as_float(q[1]), __builtin_inff());
}
__device__ __forceinline__ float xsum16(float x) {
  auto q = __builtin_amdgcn_permlane16_swap(__float_as_uint(x), __float_as_uint(x), false, false);
  return __uint_as_float(q[0]) + __uint_as_float(q[1]);
}
__device__ __forceinline__ float xsum16_32(float x) {
  auto r = __builtin_amdgcn_permlane32_swap(__float_as_uint(x), __float_as_uint(x), false, false);
  return xsum16(__uint_as_float(r[0]) + __uint_as_float(r[1]));
}
__device__ __forceinline__ unsigned mx4(unsigned m, v4u v) { return max(max(max(max(m, v[0]), v[1]), v[2]), v[3]); }
__device__ __forceinline__ bool anysent(unsigned m) { return __builtin_amdgcn_ballot_w64(m == 0xffffffffu) != 0ull; }

__device__ __forceinline__ unsigned now32() { return (unsigned)__builtin_amdgcn_s_memrealtime(); }
__device__ __forceinline__ unsigned cyc32() { return (unsigned)__builtin_amdgcn_s_memtime(); }
#define CMAX 14000
__device__ __forceinline__ void wait_cyc(unsigned t) {
  t = __builtin_amdgcn_readfirstlane(t);
  int d = (int)(t - cyc32());
  if (d > CMAX) return;
  while (d > 260) {
    __builtin_amdgcn_s_sleep(1);
    d = (int)(t - cyc32());
  }
  d = __builtin_amdgcn_readfirstlane(d - 70);
  if (d > 0) asm volatile(".p2align 4\n1: s_sub_u32 %0, %0, 28\n\ts_cbranch_scc0 1b" : "+s"(d)::"scc", "memory");
}
#define CLKSPAN 124u
struct Clk {
  unsigned r, m;
  unsigned cq, inv;
};
__device__ __forceinline__ bool clk_edge(unsigned& r, unsigned& m) {
  u64 t[14], m0, m1;
  unsigned n;
#define CE_S(i) "s_memrealtime %" #i "\n\ts_nop 0\n\t"
  asm volatile("s_setprio 3\n\ts_memtime %14\n\t" CE_S(0) CE_S(1) CE_S(2) CE_S(3) CE_S(4) CE_S(5) CE_S(6) CE_S(7) CE_S(8) CE_S(9) CE_S(10)
               CE_S(11) CE_S(12) "s_memtime %15\n\ts_memrealtime %13\n\ts_setprio 0\n\ts_waitcnt lgkmcnt(0)"
               : "=s"(t[0]), "=s"(t[1]), "=s"(t[2]), "=s"(t[3]), "=s"(t[4]), "=s"(t[5]), "=s"(t[6]), "=s"(t[7]), "=s"(t[8]), "=s"(t[9]),
                 "=s"(t[10]), "=s"(t[11]), "=s"(t[12]), "=s"(t[13]), "=s"(m0), "=s"(m1));
#define CE_C(i) "s_cmp_eq_u32 %" #i ", %1\n\ts_addc_u32 %0, %0, 0\n\t"
#define CE_T(i) "s"((unsigned)t[i])
  asm("s_mov_b32 %0, 0\n\t" CE_C(2) CE_C(3) CE_C(4) CE_C(5) CE_C(6) CE_C(7) CE_C(8) CE_C(9) CE_C(10) CE_C(11) CE_C(12) CE_C(13) CE_C(14)
      : "=&s"(n)
      : CE_T(0), CE_T(1), CE_T(2), CE_T(3), CE_T(4), CE_T(5), CE_T(6), CE_T(7), CE_T(8), CE_T(9), CE_T(10), CE_T(11), CE_T(12), CE_T(13)
      : "scc");
  r = (unsigned)t[0] + 4;
  m = (unsigned)m0 + n * 8;
  return (unsigned)m1 - (unsigned)m0 <= CLKSPAN;
}
__device__ __forceinline__ void clk_init(Clk& c) {
#pragma unroll 1
  for (int k = 0; k < 3; ++k)
    if (clk_edge(c.r, c.m)) break;
  c.cq = 92500u;
  c.inv = 46432u;
}
template <int TRIES = 1>
__device__ __forceinline__ void clk_update(Clk& c, int gain) {
  unsigned r, m;
  bool ok = clk_edge(r, m);
#pragma unroll 1
  for (int k = 1; k < TRIES && !ok; ++k) ok = clk_edge(r, m);
  ok |= r - c.r >= 1500u;
  const unsigned dr = r - c.r;
  const int err = (int)(m - c.m - ((dr * c.cq) >> 12));
  unsigned q = c.cq + (unsigned)((ok & (dr * gain < 6000u)) ? err * gain : 0);
  asm("s_max_i32 %0, %0, 0x101d0\n\ts_min_i32 %0, %0, 0x1b580" : "+s"(q)::"scc");
  c.cq = q;
  c.inv -= (unsigned)__mulhi((int)c.inv, (int)(q * c.inv));
  c.r = ok ? r : c.r;
  c.m = ok ? m : c.m;
}
__device__ __forceinline__ int stamp_max(const Clk& c, v2u st, unsigned& md) {
  unsigned a = st[0] - (c.r << 4) + 0x40000000u, b = st[1], x;
#define SM_S(p) "v_max_u32_dpp %2, %2, %2 " p "\n\tv_max_u32_dpp %3, %3, %3 " p "\n\ts_nop 0\n\t"
  asm volatile("s_nop 1\n\t" SM_S("row_ror:1 row_mask:0xf bank_mask:0xf") SM_S("row_ror:2 row_mask:0xf bank_mask:0xf")
               SM_S("row_ror:4 row_mask:0xf bank_mask:0xf") SM_S("row_ror:8 row_mask:0xf bank_mask:0xf")
               SM_S("row_bcast:15 row_mask:0xa bank_mask:0xf") SM_S("row_bcast:31 row_mask:0xc bank_mask:0xf")
               "v_readlane_b32 %0, %2, 63\n\tv_readlane_b32 %1, %3, 63"
               : "=s"(x), "=s"(md), "+v"(a), "+v"(b));
  return (int)(x - 0x40000000u);
}
__device__ __forceinline__ unsigned clk_cyc(const Clk& c, int d) { return c.m + (unsigned)(((long long)d * (int)c.cq) >> 16); }
#define NS2F(ns) (((ns) * 8) / 5)
struct Pace {
  unsigned rf;
  int ed, ea, dq;
};
__device__ __forceinline__ void pace_step(Pace& p, int s, unsigned rf, unsigned md, int marg, int tol, int xlate) {
  const int arr = s ? (int)(rf - p.rf) - p.dq : 0x10000000;
  const bool late = arr > p.ea + tol;
  p.ea = min(arr, p.ea + 5);
  p.ed = (int)min(md, (unsigned)p.ed + 5u);
  p.rf = rf;
  p.dq = p.ed + marg + (late ? xlate : 0);
}

template <int SLP>
__device__ __forceinline__ void wait_flag(__amdgpu_buffer_rsrc_t rs, int off, gu32* tmo, unsigned code) {
  _Pragma("clang loop unroll(disable)") for (unsigned spins = 0;;) {
    asm volatile("" : "+v"(off));
    unsigned v = __builtin_amdgcn_raw_buffer_load_b32(rs, off, 0, AUX_SC1);
    if (__all(v == 0u)) break;
    __builtin_amdgcn_s_sleep(SLP);
    if (spin_fail(spins, tmo, code)) break;
  }
}
__device__ __forceinline__ void arm_wait(__amdgpu_buffer_rsrc_t rs, gu32* tmo) {
  _Pragma("clang loop unroll(disable)") for (unsigned spins = 0;;) {
    int off = CNT_ARM;
    asm volatile("" : "+v"(off));
    if (__all(__builtin_amdgcn_raw_buffer_load_b32(rs, off, 0, AUX_SC1) + 1u >= 64u)) break;
    __builtin_amdgcn_s_sleep(8);
    if (spin_fail(spins, tmo, 0x30007)) break;
  }
}
#define RAW_BARRIER()                                    \
  do {                                                   \
    asm volatile("" ::: "memory");                       \
    __builtin_amdgcn_s_waitcnt(0xc07f);                  \
    __builtin_amdgcn_s_barrier();                        \
    asm volatile("" ::: "memory");                       \
  } while (0)
#define BARE_BARRIER()                 \
  do {                                 \
    asm volatile("" ::: "memory");     \
    __builtin_amdgcn_s_barrier();      \
    asm volatile("" ::: "memory");     \
  } while (0)
#define VMCNT(n) asm volatile("s_waitcnt vmcnt(" #n ")" ::: "memory")
#define SB() __builtin_amdgcn_sched_barrier(0)
#define OPQ(v, e) int v = (e); asm volatile("" : "+v"(v))
template <bool NT = false>
__device__ __forceinline__ void glds16(const void* base, int voff, unsigned dst) {
  unsigned keep;
  if constexpr (NT)
    asm volatile("s_mov_b32 %0, m0\n\ts_mov_b32 m0, %3\n\ts_nop 0\n\tglobal_load_lds_dwordx4 %1, %2 nt\n\ts_mov_b32 m0, %0"
                 : "=&s"(keep)
                 : "v"(voff), "s"(base), "s"(dst)
                 : "memory");
  else
  asm volatile("s_mov_b32 %0, m0\n\ts_mov_b32 m0, %3\n\ts_nop 0\n\tglobal_load_lds_dwordx4 %1, %2\n\ts_mov_b32 m0, %0"
               : "=&s"(keep)
               : "v"(voff), "s"(base), "s"(dst)
               : "memory");
}
__device__ __forceinline__ const char* w_slow(const u16* Wq, const u16* Wk, const u16* Wv, int w, int xg) {
  const char* p = (const char*)(w < 2 || w == 6 ? Wq : (w < 4 || w == 7 ? Wk : Wv));
  const int sk = 3 - (int)((uintptr_t)p >> 7);
  const int h = w < 6 ? (xg & 3) * 8 + (w & 1) * 4 + ((sk & 7) >> 1) : ((sk + 60) & 63) >> 1;
  return (h >> 3) == (xg & 3) ? p + h * 256 : nullptr;
}
__device__ __forceinline__ void w_touch(const char* p, int row, unsigned dst) {
  unsigned keep;
  const int ln = olane();
  asm volatile("s_mov_b32 %0, m0\n\ts_mov_b32 m0, %3\n\ts_nop 0\n\tglobal_load_lds_dword %1, %2\n\ts_mov_b32 m0, %0"
               : "=&s"(keep)
               : "v"((row + (ln >> 2)) * 8192 + (ln & 3) * 64), "s"(p), "s"(dst)
               : "memory");
}
#define WBL 0
#define A_VO 4
#define A_QK 3
#define A_OW 0
#define A_VO_A A_VO
#define A_VO_B A_VO
#define A_QK_A0 A_QK
#define A_QK_A1 A_QK
#define A_QK_X A_QK
#define A_QK_K A_QK
#define A_OW_F 3
#define A_OW_D 4
#define A_OW_T 2
#define AL_(k) ".p2align 6\n.fill " #k ",4,0xbf800000\n"
#define AL(k) AL_(k)
#define CPF 1
constexpr int CODE_LEN = 168960;
__device__ __forceinline__ void code_touch(int part, unsigned dst) {
  unsigned keep;
  const int v = min(part * 8192 + olane() * 128, CODE_LEN - 128);
  asm volatile("s_getpc_b64 vcc\n.Lct%=:\n\ts_sub_u32 vcc_lo, vcc_lo, .Lct%=-.Lkbeg\n\ts_subb_u32 vcc_hi, vcc_hi, 0\n\t"
               "s_mov_b32 %0, m0\n\ts_mov_b32 m0, %2\n\ts_nop 0\n\t"
               "global_load_lds_dword %1, vcc\n\ts_mov_b32 m0, %0"
               : "=&s"(keep)
               : "v"(v), "s"(dst)
               : "vcc", "scc", "memory");
}
__device__ __forceinline__ void code_ahead(int part, unsigned dst) {
  unsigned keep;
  const int v = part * 2048 + (olane() & 15) * 128;
  asm volatile("s_getpc_b64 vcc\n\ts_mov_b32 %0, m0\n\ts_mov_b32 m0, %2\n\ts_nop 0\n\tglobal_load_lds_dword %1, vcc ; AHEAD\n\ts_mov_b32 m0, %0"
               : "=&s"(keep)
               : "v"(v), "s"(dst)
               : "vcc", "memory");
}
__device__ __forceinline__ f32x4 smf(v4u a, v8u b, f32x4 c, int idx) {
  return __builtin_amdgcn_smfmac_f32_16x16x64_bf16(__builtin_bit_cast(bf16x8, a), __builtin_bit_cast(bf16x16, b), c, idx, 0, 0);
}
__device__ __forceinline__ f32x4 smf1(v4u a, v8u b, f32x4 c, int idx) {
  return __builtin_amdgcn_smfmac_f32_16x16x64_bf16(__builtin_bit_cast(bf16x8, a), __builtin_bit_cast(bf16x16, b), c, idx, 0, 1);
}
__device__ __forceinline__ f32x4 mma(v4u a, v4u b, f32x4 c) {
  return __builtin_amdgcn_mfma_f32_16x16x32_bf16(__builtin_bit_cast(bf16x8, a), __builtin_bit_cast(bf16x8, b), c, 0, 0, 0);
}
__device__ __forceinline__ void st128(v4u d, __amdgpu_buffer_rsrc_t rs, int voff, int soff) {
  __builtin_amdgcn_raw_buffer_store_b128(d, rs, voff, soff, AUX_SC1);
  asm volatile("s_nop 1" ::"v"(d));
}
__device__ __forceinline__ f32x4 zero4() {
  f32x2 a, b;
  asm volatile("v_mov_b64 %0, 0" : "=v"(a));
  asm volatile("v_mov_b64 %0, 0" : "=v"(b));
  return f32x4{a[0], a[1], b[0], b[1]};
}
__device__ __forceinline__ f32x2 zero2() {
  f32x2 a;
  asm volatile("v_mov_b64 %0, 0" : "=v"(a));
  return a;
}
__device__ __forceinline__ v4u lo4(v8u v) { return v4u{v[0], v[1], v[2], v[3]}; }
__device__ __forceinline__ v4u hi4(v8u v) { return v4u{v[4], v[5], v[6], v[7]}; }
__device__ __forceinline__ v8u cat8(v4u a, v4u b) { return v8u{a[0], a[1], a[2], a[3], b[0], b[1], b[2], b[3]}; }
__device__ __forceinline__ v32u cat32(v8u a, v8u b, v8u c, v8u d) {
  const v16u x = __builtin_shufflevector(a, b, 0, 1, 2, 3, 4, 5, 6, 7, 8, 9, 10, 11, 12, 13, 14, 15);
  const v16u y = __builtin_shufflevector(c, d, 0, 1, 2, 3, 4, 5, 6, 7, 8, 9, 10, 11, 12, 13, 14, 15);
  return __builtin_shufflevector(x, y, 0, 1, 2, 3, 4, 5, 6, 7, 8, 9, 10, 11, 12, 13, 14, 15, 16, 17, 18, 19, 20, 21, 22, 23, 24, 25, 26,
                                 27, 28, 29, 30, 31);
}
__device__ __forceinline__ v2u trd(LAS char* p) {
  return __builtin_bit_cast(v2u, __builtin_amdgcn_ds_read_tr16_b64_v4i16((LAS s16x4*)p));
}
__device__ __forceinline__ int vm_slot(int s) { return (s & 1) * VM_SLOT + ((s >> 1) & 1) * VM_SLOT2; }
__device__ __forceinline__ int cls_of(unsigned perm, int x) { return (int)((perm >> (3 * x)) & 7u); }

__device__ __forceinline__ v2u numa_load(__amdgpu_buffer_rsrc_t rs, gu32* tmo) {
  unsigned c, xid;
  _Pragma("clang loop unroll(disable)") for (unsigned spins = 0;;) {
    OPQ(a, olane() * 4);
    c = __builtin_amdgcn_raw_buffer_load_b32(rs, a, (int)OFF_CAL, AUX_SC1);
    xid = __builtin_amdgcn_raw_buffer_load_b32(rs, ((a >> 5) & (NXC - 1)) * 4, (int)OFF_XID, AUX_SC1);
    if (__all((c != 0xffffffffu) & (xid != 0xffffffffu))) break;
    __builtin_amdgcn_s_sleep(16);
    if (spin_fail(spins, tmo, 0x30008)) break;
  }
  return v2u{c, xid};
}
__device__ __forceinline__ unsigned numa_perm(v2u in, LAS unsigned* tab) {
  const int lane = olane();
  const unsigned c = in[0], xid = in[1];
  tab[lane] = 0xffffu;
  tab[64 + lane] = 0xffffu;
  tab[128 + lane] = 0u;
  __builtin_amdgcn_s_waitcnt(0xc07f);
  __hip_atomic_fetch_min(tab + ((c >> 16) & 127u), c & 0xffffu, __ATOMIC_RELAXED, __HIP_MEMORY_SCOPE_WORKGROUP);
  __hip_atomic_fetch_add(tab + 128 + ((c >> 21) & 3u) * 2 + ((c >> 17) & 1u), c & 0xffffu, __ATOMIC_RELAXED, __HIP_MEMORY_SCOPE_WORKGROUP);
  __builtin_amdgcn_s_waitcnt(0xc07f);
  asm volatile("" ::: "memory");
  const unsigned s0 = tab[128 + ((xid >> 2) & 3u) * 2], s1 = tab[129 + ((xid >> 2) & 3u) * 2];
  const bool far = ((lane >> 1) & 1) != (s1 < s0 ? 1 : 0);
  unsigned key = ((tab[(xid & 15u) * 8 + (lane & 7)] + (far ? 0x10000u : 0u)) << 6) | (unsigned)lane;
  if ((lane >> 3) >= NXC) key = 0xffffffffu;
  unsigned perm = 0;
#pragma unroll 1
  for (int r = 0; r < NXC; ++r) {
    const unsigned m = wave_min(key);
    const unsigned bx = (m >> 3) & 7u, bc = m & 7u;
    perm |= bc << (3 * bx);
    if ((unsigned)(lane >> 3) == bx || (unsigned)(lane & 7) == bc) key = 0xffffffffu;
  }
  return __builtin_amdgcn_readfirstlane(perm);
}
#define VRING 64
#define VWN 0
__device__ __forceinline__ int vw_addr(unsigned pm, int tile, int seg, int gq) {
  return (int)OFF_VWX + ((((tile >> 3) * VRING + (seg & (VRING - 1))) * 8 + (tile & 7)) * 8 + gq) * 4096;
}

#define QKN 0
constexpr int QK_RT = QKN ? 32768 : 4096;
__device__ __forceinline__ int qk_addr(int mat, int seg, int h, int cl, int ra) {
  return (int)OFF_QK + ((mat * 64 + seg) * 32 + h) * 8192;
}

__device__ __forceinline__ u64 hs_post(__amdgpu_buffer_rsrc_t rs, int w) {
  const u64 n = __builtin_amdgcn_s_memrealtime();
  if (w == 0) __builtin_amdgcn_raw_buffer_store_b64(v2u{(unsigned)n, (unsigned)(n >> 32)}, rs, olane() == 0 ? 0 : OOB,
                                                    OFF_REQ + (int)blockIdx.x * 8, AUX_SC1);
  return n;
}
__device__ __forceinline__ void hs_wait(__amdgpu_buffer_rsrc_t rs, char* ws, u64 n, int cnt) {
  _Pragma("clang loop unroll(disable)") for (unsigned spins = 0; spins < (kSpinCap << 2); ++spins) {
    int z = 0;
    asm volatile("" : "+v"(z));
    const v2u v = __builtin_amdgcn_raw_buffer_load_b64(rs, z, OFF_ACK + (int)blockIdx.x * 8, AUX_SC1);
    if (__all(v[0] == (unsigned)n && v[1] == (unsigned)(n >> 32))) break;
    __builtin_amdgcn_s_sleep(8);
  }
  if (olane() == 0) __hip_atomic_fetch_add((gu32*)(ws + cnt), 1u, RLX_AGENT);
}
template <int SLP>
__device__ __forceinline__ void hs_lead(__amdgpu_buffer_rsrc_t rs, bool fill, int cnt, unsigned want) {
  const int l16 = olane() * 16;
  if (fill) {
    for (int o = 0; o < (int)ARM_HOST; o += 1024) __builtin_amdgcn_raw_buffer_store_b128(v4u{~0u, ~0u, ~0u, ~0u}, rs, l16, o, AUX_SC1);
    VMCNT(0);
  }
  _Pragma("clang loop unroll(disable)") for (unsigned spins = 0; spins < (kSpinCap << 2); ++spins) {
    int va = l16;
    asm volatile("" : "+v"(va));
    const v4u a = __builtin_amdgcn_raw_buffer_load_b128(rs, va, OFF_REQ, AUX_SC1);
    const v4u b = __builtin_amdgcn_raw_buffer_load_b128(rs, va + 1024, OFF_REQ, AUX_SC1);
    const unsigned n = __builtin_amdgcn_raw_buffer_load_b32(rs, va & 0, cnt, AUX_SC1);
    if (__all(n + 1u >= want)) break;
    __builtin_amdgcn_raw_buffer_store_b128(a, rs, va, OFF_ACK, AUX_SC1);
    __builtin_amdgcn_raw_buffer_store_b128(b, rs, va + 1024, OFF_ACK, AUX_SC1);
    __builtin_amdgcn_s_sleep(SLP);
  }
}

#define TN(k, d) (d)
#define EVDECL
#define EV(k)
#define EVDUMP(s)
#define EVCLK(s, c)
#define EVOUT
#define PX 2
#define NOB 3
#define VO_A \
  "s_branch .Lgo_%=\n" \
  AL(A_VO_A) \
  ".Lr_%=:\n" \
  "s_sub_u32 %[rt], %[rt], 1\n" \
  "s_cbranch_scc1 .Lrb_%=\n" \
  "s_sleep 2\n" \
  "buffer_load_dwordx4 v[192:195], v248, %[rs], %[o0] offen sc1\n" \
  "buffer_load_dwordx4 v[196:199], v248, %[rs], %[o0] offen offset:1024 sc1\n" \
  "v_mov_b64 v[224:225], 0\n" \
  "v_mov_b64 v[226:227], 0\n" \
  "v_mov_b64 v[228:229], 0\n" \
  "v_mov_b64 v[230:231], 0\n" \
  "s_nop 1\n" \
  "s_branch .Lw_%=\n" \
  ".Lgo_%=:\n" \
  "v_add_u32 v252, 0x20000, v248\n" \
  "ds_read_b128 v[200:203], v248 offset:32768\n" \
  "ds_read_b128 v[204:207], v248 offset:33792\n" \
  "ds_read_b128 v[208:211], v248 offset:49152\n" \
  "ds_read_b128 v[212:215], v248 offset:50176\n" \
  "ds_read_b128 v[216:219], v252 offset:0\n" \
  "ds_read_b128 v[220:223], v252 offset:1024\n" \
  "ds_read_b128 v[240:243], v252 offset:16384\n" \
  "ds_read_b128 v[244:247], v252 offset:17408\n" \
  "v_mov_b64 v[224:225], 0\n" \
  "v_mov_b64 v[226:227], 0\n" \
  "v_mov_b64 v[228:229], 0\n" \
  "v_mov_b64 v[230:231], 0\n" \
  "v_xor_b32 v250, 0x800, v248\n" \
  "v_xor_b32 v251, 0x1000, v248\n" \
  "s_mov_b32 %[rt], 0x40000\n" \
  "s_setprio 2\n" \
  "s_waitcnt lgkmcnt(0)\n" \
  "s_barrier\n" \
  "buffer_load_dwordx4 v[192:195], v248, %[rs], %[o0] offen sc1\n" \
  "buffer_load_dwordx4 v[196:199], v248, %[rs], %[o0] offen offset:1024 sc1\n" \
  ".Lw_%=:\n" \
  "s_waitcnt vmcnt(1)\n" \
  "ds_write_b128 v248, v[192:195]\n" \
  "v_smfmac_f32_16x16x64_bf16 v[224:227], v[192:195], v[200:207], v249\n" \
  "v_max3_u32 v252, v192, v193, v194\n" \
  "s_waitcnt vmcnt(0)\n" \
  "ds_write_b128 v248, v[196:199] offset:1024\n" \
  "v_smfmac_f32_16x16x64_bf16 v[228:231], v[192:195], v[208:215], v249\n" \
  "v_max3_u32 v252, v252, v195, v196\n" \
  "v_smfmac_f32_16x16x64_bf16 v[224:227], v[196:199], v[216:223], v249\n" \
  "v_max3_u32 v252, v252, v197, v198\n" \
  "v_max_u32 v252, v252, v199\n" \
  "v_smfmac_f32_16x16x64_bf16 v[228:231], v[196:199], v[240:247], v249\n" \
  "v_cmp_eq_u32 vcc, -1, v252\n" \
  "v_xor_b32 v252, 0x1800, v248\n" \
  "s_cbranch_vccnz .Lr_%=\n" \
  ".Lrb_%=:\n" \
  "s_waitcnt lgkmcnt(0)\n" \
  "s_barrier\n" \
  "ds_read_b128 v[200:203], v250 offset:0\n" \
  "ds_read_b128 v[204:207], v250 offset:1024\n" \
  "ds_read_b128 v[208:211], v251 offset:0\n" \
  "ds_read_b128 v[212:215], v251 offset:1024\n" \
  "ds_read_b128 v[216:219], v252 offset:0\n" \
  "ds_read_b128 v[220:223], v252 offset:1024\n" \
  "s_waitcnt lgkmcnt(5)\n" \
  "v_smfmac_f32_16x16x64_bf16 v[224:227], v[200:203], v[0:7], v249\n" \
  "v_smfmac_f32_16x16x64_bf16 v[228:231], v[200:203], v[48:55], v249\n" \
  "s_waitcnt lgkmcnt(4)\n" \
  "v_smfmac_f32_16x16x64_bf16 v[224:227], v[204:207], v[8:15], v249\n" \
  "v_smfmac_f32_16x16x64_bf16 v[228:231], v[204:207], v[56:63], v249\n" \
  "s_waitcnt lgkmcnt(3)\n" \
  "v_smfmac_f32_16x16x64_bf16 v[224:227], v[208:211], v[16:23], v249\n" \
  "v_smfmac_f32_16x16x64_bf16 v[228:231], v[208:211], v[64:71], v249\n" \
  "s_waitcnt lgkmcnt(2)\n" \
  "v_smfmac_f32_16x16x64_bf16 v[224:227], v[212:215], v[24:31], v249\n" \
  "s_waitcnt lgkmcnt(1)\n" \
  "v_smfmac_f32_16x16x64_bf16 v[224:227], v[216:219], v[32:39], v249\n" \
  "s_waitcnt lgkmcnt(0)\n" \
  "v_smfmac_f32_16x16x64_bf16 v[224:227], v[220:223], v[40:47], v249\n" \
  "v_smfmac_f32_16x16x64_bf16 v[228:231], v[212:215], v[72:79], v249\n" \
  "v_smfmac_f32_16x16x64_bf16 v[228:231], v[216:219], v[80:87], v249\n" \
  "s_nop 0\n" \
  "v_add_f32 v224, v224, v226\n" \
  "v_smfmac_f32_16x16x64_bf16 v[228:231], v[220:223], v[88:95], v249\n" \
  "v_add_f32 v225, v225, v227\n" \
  "ds_write_b64 v248, v[224:225] offset:16384\n" \
  "s_nop 6\n" \
  "v_add_f32 v228, v228, v230\n" \
  "v_add_f32 v229, v229, v231\n" \
  "ds_write_b64 v248, v[228:229] offset:16392\n" \
  "s_setprio 0\n" \
  "ds_read_b128 v[240:243], v254 offset:0\n" \
  "ds_read_b128 v[244:247], v254 offset:1024\n" \
  "s_waitcnt lgkmcnt(2)\n" \
  "s_barrier\n" \
  "s_nop 1\n" \
  "ds_read_b128 v[224:227], v254 offset:16384\n" \
  "ds_read_b128 v[228:231], v254 offset:17408\n" \
  "v_mov_b64 v[192:193], 0\n" \
  "v_mov_b64 v[194:195], 0\n" \
  "v_mov_b64 v[196:197], 0\n" \
  "v_mov_b64 v[198:199], 0\n" \
  "v_mov_b64 v[200:201], 0\n" \
  "v_mov_b64 v[202:203], 0\n" \
  "v_mov_b64 v[204:205], 0\n" \
  "v_mov_b64 v[206:207], 0\n" \
  "s_setprio 2\n" \
  "s_barrier\n" \
  "ds_read_b64_tr_b16 v[208:209], v238 offset:16384\n" \
  "ds_read_b64_tr_b16 v[210:211], v238 offset:16576\n" \
  "ds_read_b64_tr_b16 v[212:213], v238 offset:17152\n" \
  "ds_read_b64_tr_b16 v[214:215], v238 offset:17344\n" \
  "ds_read_b64_tr_b16 v[216:217], v238 offset:17920\n" \
  "ds_read_b64_tr_b16 v[218:219], v238 offset:18112\n" \
  "ds_read_b64_tr_b16 v[220:221], v238 offset:18688\n" \
  "ds_read_b64_tr_b16 v[222:223], v238 offset:18880\n" \
  "s_waitcnt lgkmcnt(6)\n" \
  "v_smfmac_f32_16x16x64_bf16 v[200:203], v[208:211], v[240:247], v249 abid:1\n" \
  "ds_read_b128 v[240:243], v254 offset:32768\n" \
  "v_smfmac_f32_16x16x64_bf16 v[192:195], v[208:211], v[160:167], v249 abid:1\n" \
  "ds_read_b128 v[244:247], v254 offset:33792\n" \
  "v_smfmac_f32_16x16x64_bf16 v[196:199], v[208:211], v[96:103], v249 abid:1\n" \
  "v_smfmac_f32_16x16x64_bf16 v[204:207], v[208:211], v[128:135], v249 abid:1\n" \
  "s_waitcnt lgkmcnt(6)\n" \
  "v_smfmac_f32_16x16x64_bf16 v[192:195], v[212:215], v[168:175], v249 abid:1\n" \
  "v_smfmac_f32_16x16x64_bf16 v[200:203], v[212:215], v[224:231], v249 abid:1\n" \
  "ds_read_b128 v[224:227], v254 offset:49152\n" \
  "v_smfmac_f32_16x16x64_bf16 v[196:199], v[212:215], v[104:111], v249 abid:1\n" \
  "ds_read_b128 v[228:231], v254 offset:50176\n" \
  "v_smfmac_f32_16x16x64_bf16 v[204:207], v[212:215], v[136:143], v249 abid:1\n" \
  "s_waitcnt lgkmcnt(6)\n" \
  "v_smfmac_f32_16x16x64_bf16 v[192:195], v[216:219], v[176:183], v249 abid:1\n" \
  "v_smfmac_f32_16x16x64_bf16 v[196:199], v[216:219], v[112:119], v249 abid:1\n" \
  "s_waitcnt lgkmcnt(4)\n" \
  "v_smfmac_f32_16x16x64_bf16 v[192:195], v[220:223], v[184:191], v249 abid:1\n" \
  "v_smfmac_f32_16x16x64_bf16 v[196:199], v[220:223], v[120:127], v249 abid:1\n" \
  "v_smfmac_f32_16x16x64_bf16 v[204:207], v[216:219], v[144:151], v249 abid:1\n" \
  "s_nop 0\n" \
  "v_cvt_pk_bf16_f32 v192, v192, v193\n" \
  "v_smfmac_f32_16x16x64_bf16 v[204:207], v[220:223], v[152:159], v249 abid:1\n" \
  "v_cvt_pk_bf16_f32 v193, v194, v195\n" \
  "s_waitcnt lgkmcnt(2)\n" \
  "v_smfmac_f32_16x16x64_bf16 v[200:203], v[216:219], v[240:247], v249 abid:1\n" \
  "v_cvt_pk_bf16_f32 v194, v196, v197\n" \
  "s_waitcnt lgkmcnt(0)\n" \
  "v_smfmac_f32_16x16x64_bf16 v[200:203], v[220:223], v[224:231], v249 abid:1\n" \
  "v_cvt_pk_bf16_f32 v195, v198, v199\n" \
  "v_cvt_pk_bf16_f32 v204, v204, v205\n" \
  "v_permlane16_swap_b32 v192, v194\n" \
  "v_permlane16_swap_b32 v193, v195\n" \
  "buffer_store_dwordx4 v[192:195], v236, %[rs], %[vs] offen sc1\n" \
  "v_cvt_pk_bf16_f32 v205, v206, v207\n" \
  "s_nop 2\n" \
  "v_cvt_pk_bf16_f32 v203, v202, v203\n" \
  "v_cvt_pk_bf16_f32 v202, v200, v201\n" \
  "s_nop 0\n" \
  "v_permlane16_swap_b32 v203, v205\n" \
  "v_permlane16_swap_b32 v202, v204\n" \
  "buffer_store_dwordx4 v[202:205], v237, %[rs], %[vs] offen sc1\n" \
  "s_setprio 0\n" \
  "s_nop 1\n"
#define VO_B \
  "s_branch .Lgo_%=\n" \
  AL(A_VO_B) \
  ".Lr_%=:\n" \
  "s_sub_u32 %[rt], %[rt], 1\n" \
  "s_cbranch_scc1 .Lrb_%=\n" \
  "s_sleep 2\n" \
  "buffer_load_dwordx4 v[192:195], v248, %[rs], %[o0] offen sc1\n" \
  "buffer_load_dwordx4 v[196:199], v248, %[rs], %[o0] offen offset:1024 sc1\n" \
  "v_mov_b64 v[224:225], 0\n" \
  "v_mov_b64 v[226:227], 0\n" \
  "v_mov_b64 v[228:229], 0\n" \
  "v_mov_b64 v[230:231], 0\n" \
  "s_nop 1\n" \
  "s_branch .Lw_%=\n" \
  ".Lgo_%=:\n" \
  "v_add_u32 v252, 0x20000, v248\n" \
  "ds_read_b128 v[200:203], v248 offset:32768\n" \
  "ds_read_b128 v[204:207], v248 offset:33792\n" \
  "ds_read_b128 v[208:211], v248 offset:49152\n" \
  "ds_read_b128 v[212:215], v248 offset:50176\n" \
  "ds_read_b128 v[216:219], v252 offset:0\n" \
  "ds_read_b128 v[220:223], v252 offset:1024\n" \
  "ds_read_b128 v[240:243], v252 offset:16384\n" \
  "ds_read_b128 v[244:247], v252 offset:17408\n" \
  "v_mov_b64 v[224:225], 0\n" \
  "v_mov_b64 v[226:227], 0\n" \
  "v_mov_b64 v[228:229], 0\n" \
  "v_mov_b64 v[230:231], 0\n" \
  "v_xor_b32 v250, 0x800, v248\n" \
  "v_xor_b32 v251, 0x1000, v248\n" \
  "s_mov_b32 %[rt], 0x40000\n" \
  "s_setprio 1\n" \
  "s_waitcnt lgkmcnt(0)\n" \
  "s_cmp_eq_u32 %[tw], 0\n" \
  "s_cbranch_scc1 .Ltq_%=\n" \
  ".Ltl_%=:\n" \
  "s_memtime vcc\n" \
  "s_waitcnt lgkmcnt(0)\n" \
  "s_sub_i32 %[cnt], %[tg], vcc_lo\n" \
  "s_cmp_lt_i32 %[cnt], 260\n" \
  "s_cbranch_scc1 .Ltf_%=\n" \
  "s_sleep 1\n" \
  "s_branch .Ltl_%=\n" \
  ".Ltf_%=:\n" \
  "s_max_i32 %[cnt], %[cnt], 0\n" \
  ".Ltg_%=:\n" \
  "s_sub_u32 %[cnt], %[cnt], 28\n" \
  "s_cbranch_scc0 .Ltg_%=\n" \
  ".Ltq_%=:\n" \
  "s_barrier\n" \
  "buffer_load_dwordx4 v[192:195], v248, %[rs], %[o0] offen sc1\n" \
  "buffer_load_dwordx4 v[196:199], v248, %[rs], %[o0] offen offset:1024 sc1\n" \
  ".Lw_%=:\n" \
  "s_waitcnt vmcnt(1)\n" \
  "ds_write_b128 v248, v[192:195]\n" \
  "v_smfmac_f32_16x16x64_bf16 v[224:227], v[192:195], v[200:207], v249\n" \
  "v_max3_u32 v252, v192, v193, v194\n" \
  "s_waitcnt vmcnt(0)\n" \
  "ds_write_b128 v248, v[196:199] offset:1024\n" \
  "v_smfmac_f32_16x16x64_bf16 v[228:231], v[192:195], v[208:215], v249\n" \
  "v_max3_u32 v252, v252, v195, v196\n" \
  "v_smfmac_f32_16x16x64_bf16 v[224:227], v[196:199], v[216:223], v249\n" \
  "v_max3_u32 v252, v252, v197, v198\n" \
  "v_max_u32 v252, v252, v199\n" \
  "v_smfmac_f32_16x16x64_bf16 v[228:231], v[196:199], v[240:247], v249\n" \
  "v_cmp_eq_u32 vcc, -1, v252\n" \
  "v_xor_b32 v252, 0x1800, v248\n" \
  "s_cbranch_vccnz .Lr_%=\n" \
  ".Lrb_%=:\n" \
  "s_waitcnt lgkmcnt(0)\n" \
  "s_barrier\n" \
  "s_memtime vcc\n" \
  "s_setprio 3\n" \
  "v_mov_b64 v[192:193], 0\n" \
  "v_mov_b64 v[194:195], 0\n" \
  "v_mov_b64 v[196:197], 0\n" \
  "v_mov_b64 v[198:199], 0\n" \
  "s_waitcnt lgkmcnt(0)\n" \
  "s_sub_u32 %[cnt], vcc_lo, %[ckm]\n" \
  "s_mul_hi_u32 %[cnt], %[cnt], %[inv]\n" \
  "s_add_u32 %[cnt], %[cnt], %[r16]\n" \
  "s_and_b32 %[cnt], %[cnt], -2\n" \
  "v_mov_b32 v233, %[cnt]\n" \
  "buffer_store_dword v233, v239, %[rs], %[sst] offen sc1\n" \
  "s_nop 5\n" \
  "ds_read_b128 v[200:203], v250 offset:0\n" \
  "ds_read_b128 v[204:207], v250 offset:1024\n" \
  "ds_read_b128 v[208:211], v251 offset:0\n" \
  "ds_read_b128 v[212:215], v251 offset:1024\n" \
  "ds_read_b128 v[216:219], v252 offset:0\n" \
  "ds_read_b128 v[220:223], v252 offset:1024\n" \
  "s_setprio 1\n" \
  "s_waitcnt lgkmcnt(5)\n" \
  "v_smfmac_f32_16x16x64_bf16 v[224:227], v[200:203], v[0:7], v249\n" \
  "v_smfmac_f32_16x16x64_bf16 v[228:231], v[200:203], v[48:55], v249\n" \
  "s_waitcnt lgkmcnt(4)\n" \
  "v_smfmac_f32_16x16x64_bf16 v[224:227], v[204:207], v[8:15], v249\n" \
  "v_smfmac_f32_16x16x64_bf16 v[228:231], v[204:207], v[56:63], v249\n" \
  "s_waitcnt lgkmcnt(3)\n" \
  "v_smfmac_f32_16x16x64_bf16 v[224:227], v[208:211], v[16:23], v249\n" \
  "ds_read_b128 v[240:243], v254 offset:0\n" \
  "v_smfmac_f32_16x16x64_bf16 v[228:231], v[208:211], v[64:71], v249\n" \
  "ds_read_b128 v[244:247], v254 offset:1024\n" \
  "s_waitcnt lgkmcnt(4)\n" \
  "v_smfmac_f32_16x16x64_bf16 v[224:227], v[212:215], v[24:31], v249\n" \
  "s_waitcnt lgkmcnt(3)\n" \
  "v_smfmac_f32_16x16x64_bf16 v[224:227], v[216:219], v[32:39], v249\n" \
  "s_waitcnt lgkmcnt(2)\n" \
  "v_smfmac_f32_16x16x64_bf16 v[224:227], v[220:223], v[40:47], v249\n" \
  "v_smfmac_f32_16x16x64_bf16 v[228:231], v[212:215], v[72:79], v249\n" \
  "s_barrier\n" \
  "ds_read_b128 v[232:235], v248 offset:24576\n" \
  "v_smfmac_f32_16x16x64_bf16 v[228:231], v[216:219], v[80:87], v249\n" \
  "s_nop 0\n" \
  "v_add_f32 v224, v224, v226\n" \
  "v_smfmac_f32_16x16x64_bf16 v[228:231], v[220:223], v[88:95], v249\n" \
  "v_add_f32 v225, v225, v227\n" \
  "s_waitcnt lgkmcnt(0)\n" \
  "v_add_f32 v232, v232, v224\n" \
  "v_add_f32 v233, v233, v225\n" \
  "v_cvt_pk_bf16_f32 v232, v232, v233\n" \
  "ds_write_b32 v253, v232\n" \
  "s_nop 2\n" \
  "v_add_f32 v234, v234, v228\n" \
  "v_add_f32 v235, v235, v229\n" \
  "v_add_f32 v234, v234, v230\n" \
  "v_add_f32 v235, v235, v231\n" \
  "v_cvt_pk_bf16_f32 v234, v234, v235\n" \
  "ds_write_b32 v253, v234 offset:1536\n" \
  "v_mov_b64 v[200:201], 0\n" \
  "v_mov_b64 v[202:203], 0\n" \
  "v_mov_b64 v[204:205], 0\n" \
  "v_mov_b64 v[206:207], 0\n" \
  "s_waitcnt lgkmcnt(0)\n" \
  "s_barrier\n" \
  "s_nop 7\n" \
  "ds_read_b64_tr_b16 v[208:209], v238 offset:16384\n" \
  "ds_read_b64_tr_b16 v[210:211], v238 offset:16576\n" \
  "ds_read_b128 v[224:227], v254 offset:16384\n" \
  "ds_read_b128 v[228:231], v254 offset:17408\n" \
  "ds_read_b64_tr_b16 v[212:213], v238 offset:17152\n" \
  "ds_read_b64_tr_b16 v[214:215], v238 offset:17344\n" \
  "ds_read_b64_tr_b16 v[216:217], v238 offset:17920\n" \
  "ds_read_b64_tr_b16 v[218:219], v238 offset:18112\n" \
  "ds_read_b64_tr_b16 v[220:221], v238 offset:18688\n" \
  "ds_read_b64_tr_b16 v[222:223], v238 offset:18880\n" \
  "s_waitcnt lgkmcnt(8)\n" \
  "v_smfmac_f32_16x16x64_bf16 v[200:203], v[208:211], v[240:247], v249 abid:1\n" \
  "ds_read_b128 v[240:243], v254 offset:32768\n" \
  "v_smfmac_f32_16x16x64_bf16 v[192:195], v[208:211], v[160:167], v249 abid:1\n" \
  "ds_read_b128 v[244:247], v254 offset:33792\n" \
  "v_smfmac_f32_16x16x64_bf16 v[196:199], v[208:211], v[96:103], v249 abid:1\n" \
  "v_smfmac_f32_16x16x64_bf16 v[204:207], v[208:211], v[128:135], v249 abid:1\n" \
  "s_waitcnt lgkmcnt(6)\n" \
  "v_smfmac_f32_16x16x64_bf16 v[192:195], v[212:215], v[168:175], v249 abid:1\n" \
  "v_smfmac_f32_16x16x64_bf16 v[200:203], v[212:215], v[224:231], v249 abid:1\n" \
  "ds_read_b128 v[224:227], v254 offset:49152\n" \
  "v_smfmac_f32_16x16x64_bf16 v[196:199], v[212:215], v[104:111], v249 abid:1\n" \
  "ds_read_b128 v[228:231], v254 offset:50176\n" \
  "v_smfmac_f32_16x16x64_bf16 v[204:207], v[212:215], v[136:143], v249 abid:1\n" \
  "s_waitcnt lgkmcnt(6)\n" \
  "v_smfmac_f32_16x16x64_bf16 v[192:195], v[216:219], v[176:183], v249 abid:1\n" \
  "v_smfmac_f32_16x16x64_bf16 v[196:199], v[216:219], v[112:119], v249 abid:1\n" \
  "s_waitcnt lgkmcnt(4)\n" \
  "v_smfmac_f32_16x16x64_bf16 v[192:195], v[220:223], v[184:191], v249 abid:1\n" \
  "v_smfmac_f32_16x16x64_bf16 v[196:199], v[220:223], v[120:127], v249 abid:1\n" \
  "v_smfmac_f32_16x16x64_bf16 v[204:207], v[216:219], v[144:151], v249 abid:1\n" \
  "s_nop 0\n" \
  "v_cvt_pk_bf16_f32 v192, v192, v193\n" \
  "v_smfmac_f32_16x16x64_bf16 v[204:207], v[220:223], v[152:159], v249 abid:1\n" \
  "v_cvt_pk_bf16_f32 v193, v194, v195\n" \
  "s_waitcnt lgkmcnt(2)\n" \
  "v_smfmac_f32_16x16x64_bf16 v[200:203], v[216:219], v[240:247], v249 abid:1\n" \
  "v_cvt_pk_bf16_f32 v194, v196, v197\n" \
  "s_waitcnt lgkmcnt(0)\n" \
  "v_smfmac_f32_16x16x64_bf16 v[200:203], v[220:223], v[224:231], v249 abid:1\n" \
  "v_cvt_pk_bf16_f32 v195, v198, v199\n" \
  "v_cvt_pk_bf16_f32 v204, v204, v205\n" \
  "v_permlane16_swap_b32 v192, v194\n" \
  "v_permlane16_swap_b32 v193, v195\n" \
  "buffer_store_dwordx4 v[192:195], v236, %[rs], %[vs] offen sc1\n" \
  "v_cvt_pk_bf16_f32 v205, v206, v207\n" \
  "s_nop 2\n" \
  "v_cvt_pk_bf16_f32 v203, v202, v203\n" \
  "v_cvt_pk_bf16_f32 v202, v200, v201\n" \
  "s_nop 0\n" \
  "v_permlane16_swap_b32 v203, v205\n" \
  "v_permlane16_swap_b32 v202, v204\n" \
  "buffer_store_dwordx4 v[202:205], v237, %[rs], %[vs] offen sc1\n" \
  "s_setprio 0\n" \
  "s_nop 1\n"
#define QK_A0 \
  "s_branch .Lgo_%=\n" \
  AL(A_QK_A0) \
  ".Lr_%=:\n" \
  "s_sub_u32 %[rt], %[rt], 1\n" \
  "s_cbranch_scc1 .Lrb_%=\n" \
  "s_sleep 2\n" \
  "buffer_load_dwordx4 v[192:195], v248, %[rs], %[o0] offen sc1\n" \
  "buffer_load_dwordx4 v[196:199], v248, %[rs], %[o0] offen offset:1024 sc1\n" \
  "v_mov_b64 v[224:225], 0\n" \
  "v_mov_b64 v[226:227], 0\n" \
  "v_mov_b64 v[228:229], 0\n" \
  "v_mov_b64 v[230:231], 0\n" \
  "v_mov_b64 v[232:233], 0\n" \
  "v_mov_b64 v[234:235], 0\n" \
  "v_mov_b64 v[236:237], 0\n" \
  "v_mov_b64 v[238:239], 0\n" \
  "s_nop 1\n" \
  "s_branch .Lw_%=\n" \
  ".Lw1_%=:\n" \
  "v_max_f32 v200, v192, v193\n" \
  "s_nop 1\n" \
  "v_max_f32_dpp v200, v200, v200 row_ror:8 row_mask:0xf bank_mask:0xf\n" \
  "s_nop 1\n" \
  "v_max_f32_dpp v200, v200, v200 row_ror:4 row_mask:0xf bank_mask:0xf\n" \
  "s_nop 1\n" \
  "v_max_f32_dpp v200, v200, v200 row_ror:2 row_mask:0xf bank_mask:0xf\n" \
  "s_nop 1\n" \
  "v_max_f32_dpp v200, v200, v200 row_ror:1 row_mask:0xf bank_mask:0xf\n" \
  "v_sub_f32 v194, v192, v200\n" \
  "v_sub_f32 v195, v193, v200\n" \
  "v_exp_f32 v194, v194\n" \
  "v_exp_f32 v195, v195\n" \
  "s_mov_b32 %[rt], -1.0\n" \
  "s_nop 0\n" \
  "v_cvt_pk_bf16_f32 v196, v194, v195\n" \
  "v_and_b32 v196, 0x7fffffff, v196\n" \
  "s_branch .Lw1b_%=\n" \
  ".Lw2_%=:\n" \
  "s_cmp_lt_i32 %[rt], 0\n" \
  "s_cbranch_scc1 .Lw3_%=\n" \
  "v_mov_b32 v200, 0\n" \
  ".Lw3_%=:\n" \
  "s_nop 1\n" \
  "v_max_f32_dpp v197, v192, v200 quad_perm:[0,0,0,0] row_mask:0xf bank_mask:0xf\n" \
  "s_nop 1\n" \
  "v_max_f32_dpp v197, v192, v197 quad_perm:[1,1,1,1] row_mask:0xf bank_mask:0xf\n" \
  "s_nop 1\n" \
  "v_max_f32_dpp v197, v197, v197 row_ror:4 row_mask:0xf bank_mask:0xf\n" \
  "s_nop 1\n" \
  "v_max_f32_dpp v197, v197, v197 row_ror:8 row_mask:0xf bank_mask:0xf\n" \
  "v_sub_f32 v194, v192, v197\n" \
  "v_sub_f32 v201, v200, v197\n" \
  "v_exp_f32 v194, v194\n" \
  "v_exp_f32 v201, v201\n" \
  "s_nop 1\n" \
  "v_mul_f32 v199, v201, v199\n" \
  "s_nop 1\n" \
  "v_add_f32_dpp v198, v194, v199 quad_perm:[0,0,0,0] row_mask:0xf bank_mask:0xf\n" \
  "s_nop 1\n" \
  "v_mov_b32_dpp v193, v194 quad_perm:[1,1,1,1] row_mask:0xf bank_mask:0xf\n" \
  "s_nop 1\n" \
  "v_add_f32 v198, v198, v193\n" \
  "s_nop 1\n" \
  "v_mov_b32_dpp v195, v194 quad_perm:[0,0,0,0] row_mask:0xf bank_mask:0xf\n" \
  "s_nop 1\n" \
  "v_add_f32_dpp v198, v198, v198 row_ror:4 row_mask:0xf bank_mask:0xf\n" \
  "s_nop 1\n" \
  "v_add_f32_dpp v198, v198, v198 row_ror:8 row_mask:0xf bank_mask:0xf\n" \
  "s_nop 1\n" \
  "v_rcp_f32 v198, v198\n" \
  "s_nop 1\n" \
  "v_mul_f32 v195, v195, v198\n" \
  "s_nop 1\n" \
  "v_mul_f32 v193, v193, v198\n" \
  "s_nop 1\n" \
  "v_cvt_pk_bf16_f32 v196, v195, v193\n" \
  "s_nop 1\n" \
  "v_and_b32 v196, 0x7fffffff, v196\n" \
  "s_nop 1\n" \
  "v_mul_f32 v198, v201, v198\n" \
  "s_nop 1\n" \
  "buffer_store_dword v196, v251, %[rs], %[so] offen sc1\n" \
  "s_nop 1\n" \
  "v_cvt_pk_bf16_f32 v197, v198, v198\n" \
  "s_nop 1\n" \
  "v_and_b32 v197, v252, v197\n" \
  "s_nop 1\n" \
  "buffer_store_dword v197, v251, %[rs], %[so] offen offset:64 sc1\n" \
  "s_branch .Lw2b_%=\n" \
  ".Lgo_%=:\n" \
  "v_add_u32 v252, 0x20000, v248\n" \
  "ds_read_b128 v[200:203], v248 offset:32768\n" \
  "ds_read_b128 v[204:207], v248 offset:33792\n" \
  "ds_read_b128 v[208:211], v248 offset:49152\n" \
  "ds_read_b128 v[212:215], v248 offset:50176\n" \
  "ds_read_b128 v[216:219], v252 offset:0\n" \
  "ds_read_b128 v[220:223], v252 offset:1024\n" \
  "ds_read_b128 v[240:243], v252 offset:16384\n" \
  "ds_read_b128 v[244:247], v252 offset:17408\n" \
  "v_mov_b64 v[224:225], 0\n" \
  "v_mov_b64 v[226:227], 0\n" \
  "v_mov_b64 v[228:229], 0\n" \
  "v_mov_b64 v[230:231], 0\n" \
  "v_mov_b64 v[232:233], 0\n" \
  "v_mov_b64 v[234:235], 0\n" \
  "v_mov_b64 v[236:237], 0\n" \
  "v_mov_b64 v[238:239], 0\n" \
  "v_xor_b32 v250, 0x800, v248\n" \
  "v_xor_b32 v251, 0x1000, v248\n" \
  "s_mov_b32 %[rt], 0x40000\n" \
  "s_setprio 2\n" \
  "s_waitcnt lgkmcnt(0)\n" \
  "s_barrier\n" \
  "buffer_load_dwordx4 v[192:195], v248, %[rs], %[o0] offen sc1\n" \
  "buffer_load_dwordx4 v[196:199], v248, %[rs], %[o0] offen offset:1024 sc1\n" \
  ".Lw_%=:\n" \
  "s_waitcnt vmcnt(1)\n" \
  "ds_write_b128 v248, v[192:195]\n" \
  "v_smfmac_f32_16x16x64_bf16 v[224:227], v[192:195], v[200:207], v249\n" \
  "v_max3_u32 v252, v192, v193, v194\n" \
  "s_waitcnt vmcnt(0)\n" \
  "ds_write_b128 v248, v[196:199] offset:1024\n" \
  "v_smfmac_f32_16x16x64_bf16 v[228:231], v[192:195], v[208:215], v249\n" \
  "v_max3_u32 v252, v252, v195, v196\n" \
  "v_smfmac_f32_16x16x64_bf16 v[224:227], v[196:199], v[216:223], v249\n" \
  "v_max3_u32 v252, v252, v197, v198\n" \
  "v_smfmac_f32_16x16x64_bf16 v[228:231], v[196:199], v[240:247], v249\n" \
  "v_max_u32 v252, v252, v199\n" \
  "v_smfmac_f32_16x16x64_bf16 v[232:235], v[192:195], v[96:103], v249\n" \
  "v_cmp_eq_u32 vcc, -1, v252\n" \
  "v_smfmac_f32_16x16x64_bf16 v[236:239], v[192:195], v[144:151], v249\n" \
  "v_xor_b32 v252, 0x1800, v248\n" \
  "s_cbranch_vccnz .Lr_%=\n" \
  ".Lrb_%=:\n" \
  "v_smfmac_f32_16x16x64_bf16 v[232:235], v[196:199], v[104:111], v249\n" \
  "v_smfmac_f32_16x16x64_bf16 v[236:239], v[196:199], v[152:159], v249\n" \
  "s_waitcnt lgkmcnt(0)\n" \
  "s_barrier\n" \
  "ds_read_b128 v[200:203], v250 offset:0\n" \
  "ds_read_b128 v[204:207], v250 offset:1024\n" \
  "ds_read_b128 v[208:211], v251 offset:0\n" \
  "ds_read_b128 v[212:215], v251 offset:1024\n" \
  "ds_read_b128 v[216:219], v252 offset:0\n" \
  "ds_read_b128 v[220:223], v252 offset:1024\n" \
  "s_waitcnt lgkmcnt(5)\n" \
  "v_smfmac_f32_16x16x64_bf16 v[224:227], v[200:203], v[0:7], v249\n" \
  "v_smfmac_f32_16x16x64_bf16 v[228:231], v[200:203], v[48:55], v249\n" \
  "s_waitcnt lgkmcnt(4)\n" \
  "v_smfmac_f32_16x16x64_bf16 v[224:227], v[204:207], v[8:15], v249\n" \
  "v_smfmac_f32_16x16x64_bf16 v[228:231], v[204:207], v[56:63], v249\n" \
  "s_waitcnt lgkmcnt(3)\n" \
  "v_smfmac_f32_16x16x64_bf16 v[224:227], v[208:211], v[16:23], v249\n" \
  "v_smfmac_f32_16x16x64_bf16 v[228:231], v[208:211], v[64:71], v249\n" \
  "s_waitcnt lgkmcnt(2)\n" \
  "v_smfmac_f32_16x16x64_bf16 v[224:227], v[212:215], v[24:31], v249\n" \
  "s_waitcnt lgkmcnt(1)\n" \
  "v_smfmac_f32_16x16x64_bf16 v[224:227], v[216:219], v[32:39], v249\n" \
  "s_waitcnt lgkmcnt(0)\n" \
  "v_smfmac_f32_16x16x64_bf16 v[224:227], v[220:223], v[40:47], v249\n" \
  "v_smfmac_f32_16x16x64_bf16 v[228:231], v[212:215], v[72:79], v249\n" \
  "v_smfmac_f32_16x16x64_bf16 v[228:231], v[216:219], v[80:87], v249\n" \
  "s_nop 0\n" \
  "v_add_f32 v224, v224, v226\n" \
  "v_smfmac_f32_16x16x64_bf16 v[228:231], v[220:223], v[88:95], v249\n" \
  "v_add_f32 v225, v225, v227\n" \
  "ds_write_b64 v248, v[224:225] offset:8192\n" \
  "s_nop 6\n" \
  "v_add_f32 v228, v228, v230\n" \
  "v_add_f32 v229, v229, v231\n" \
  "ds_write_b64 v248, v[228:229] offset:8200\n" \
  "s_setprio 0\n" \
  "ds_read_b128 v[240:243], v254 offset:0\n" \
  "ds_read_b128 v[244:247], v254 offset:1024\n" \
  "s_waitcnt lgkmcnt(2)\n" \
  "s_barrier\n" \
  "s_nop 1\n" \
  "ds_read_b128 v[192:195], v254 offset:16384\n" \
  "ds_read_b128 v[196:199], v254 offset:17408\n" \
  "ds_read_b128 v[224:227], v254 offset:32768\n" \
  "ds_read_b128 v[228:231], v254 offset:33792\n" \
  "s_waitcnt lgkmcnt(4)\n" \
  "v_smfmac_f32_16x16x64_bf16 v[232:235], v[200:203], v[240:247], v249\n" \
  "ds_read_b128 v[240:243], v254 offset:49152\n" \
  "ds_read_b128 v[244:247], v254 offset:50176\n" \
  "s_waitcnt lgkmcnt(4)\n" \
  "v_smfmac_f32_16x16x64_bf16 v[236:239], v[200:203], v[192:199], v249\n" \
  "v_smfmac_f32_16x16x64_bf16 v[232:235], v[204:207], v[112:119], v249\n" \
  "ds_read_b128 v[192:195], v248 offset:16384\n" \
  "v_smfmac_f32_16x16x64_bf16 v[236:239], v[204:207], v[160:167], v249\n" \
  "ds_read_b128 v[196:199], v248 offset:17408\n" \
  "v_smfmac_f32_16x16x64_bf16 v[232:235], v[212:215], v[120:127], v249\n" \
  "ds_read_b128 v[200:203], v248 offset:20480\n" \
  "v_smfmac_f32_16x16x64_bf16 v[236:239], v[212:215], v[168:175], v249\n" \
  "ds_read_b128 v[204:207], v248 offset:21504\n" \
  "s_waitcnt lgkmcnt(6)\n" \
  "v_smfmac_f32_16x16x64_bf16 v[232:235], v[208:211], v[224:231], v249\n" \
  "ds_read_b128 v[212:215], v248 offset:23552\n" \
  "s_waitcnt lgkmcnt(5)\n" \
  "v_smfmac_f32_16x16x64_bf16 v[236:239], v[208:211], v[240:247], v249\n" \
  "ds_read_b128 v[240:243], v248 offset:18432\n" \
  "v_smfmac_f32_16x16x64_bf16 v[232:235], v[216:219], v[128:135], v249\n" \
  "ds_read_b128 v[244:247], v248 offset:19456\n" \
  "v_smfmac_f32_16x16x64_bf16 v[232:235], v[220:223], v[136:143], v249\n" \
  "ds_read_b128 v[208:211], v248 offset:22528\n" \
  "v_smfmac_f32_16x16x64_bf16 v[236:239], v[216:219], v[176:183], v249\n" \
  "ds_read_b96 v[250:252], v248 offset:13312\n" \
  "v_smfmac_f32_16x16x64_bf16 v[236:239], v[220:223], v[184:191], v249\n" \
  "s_setprio 3\n" \
  "s_barrier\n" \
  "ds_read_b64_tr_b16 v[224:225], v253 offset:17408\n" \
  "ds_read_b64_tr_b16 v[226:227], v253 offset:17664\n" \
  "ds_read_b64_tr_b16 v[228:229], v253 offset:19456\n" \
  "ds_read_b64_tr_b16 v[230:231], v253 offset:19712\n" \
  "v_mov_b64 v[216:217], 0\n" \
  "v_mov_b64 v[218:219], 0\n" \
  "v_add_f32 v232, v232, v234\n" \
  "v_add_f32 v233, v233, v235\n" \
  "ds_write_b64 v248, v[232:233] offset:0\n" \
  "v_mov_b64 v[220:221], 0\n" \
  "v_mov_b64 v[222:223], 0\n" \
  "v_add_f32 v236, v236, v238\n" \
  "v_add_f32 v237, v237, v239\n" \
  "ds_write_b64 v248, v[236:237] offset:8\n" \
  "s_waitcnt lgkmcnt(4)\n" \
  "v_smfmac_f32_16x16x64_bf16 v[216:219], v[224:227], v[192:199], v249\n" \
  "v_smfmac_f32_16x16x64_bf16 v[220:223], v[224:227], v[200:207], v249\n" \
  "s_waitcnt lgkmcnt(2)\n" \
  "v_smfmac_f32_16x16x64_bf16 v[216:219], v[228:231], v[240:247], v249\n" \
  "v_smfmac_f32_16x16x64_bf16 v[220:223], v[228:231], v[208:215], v249\n" \
  "s_mov_b32 %[rt], 0x422c0000\n" \
  "s_waitcnt lgkmcnt(0)\n" \
  "s_barrier\n" \
  "s_nop 1\n" \
  "v_add_f32 v192, v216, v218\n" \
  "s_nop 2\n" \
  "v_add_f32 v193, v220, v222\n" \
  "v_exp_f32 v194, v192\n" \
  "v_exp_f32 v195, v193\n" \
  "v_max_f32 v197, |v192|, |v193|\n" \
  "v_cvt_pk_bf16_f32 v196, v194, v195\n" \
  "v_cmp_nge_f32 vcc, %[rt], v197\n" \
  "s_cbranch_vccnz .Lw1_%=\n" \
  ".Lw1b_%=:\n" \
  "buffer_store_dword v196, v250, %[rs], %[c0] offen sc1\n" \
  "buffer_store_dword v196, v250, %[rs], %[c1] offen sc1\n" \
  "buffer_store_dword v196, v250, %[rs], %[c2] offen sc1\n" \
  "buffer_store_dword v196, v250, %[rs], %[c3] offen sc1\n" \
  "v_add_f32 v199, v194, v195\n" \
  "v_xor_b32 v251, %[xr], v251\n" \
  "s_nop 0\n" \
  "v_add_f32_dpp v199, v199, v199 quad_perm:[1,0,3,2] row_mask:0xf bank_mask:0xf\n" \
  "s_nop 1\n" \
  "v_add_f32_dpp v199, v199, v199 quad_perm:[2,3,0,1] row_mask:0xf bank_mask:0xf\n" \
  "s_barrier\n" \
  "ds_read_b64_tr_b16 v[232:233], v253 offset:9216\n" \
  "ds_read_b64_tr_b16 v[234:235], v253 offset:9472\n" \
  "ds_read_b64_tr_b16 v[236:237], v253 offset:11264\n" \
  "ds_read_b64_tr_b16 v[238:239], v253 offset:11520\n" \
  "s_waitcnt lgkmcnt(2)\n" \
  "v_mfma_f32_16x16x32_bf16 v[216:219], v[224:227], v[232:235], 0\n" \
  "s_waitcnt lgkmcnt(0)\n" \
  "v_mfma_f32_16x16x32_bf16 v[216:219], v[228:231], v[236:239], v[216:219]\n" \
  "s_nop 8\n" \
  "v_add_f32_dpp v192, v218, v216 quad_perm:[2,3,2,3] row_mask:0xf bank_mask:0xf\n" \
  "v_exp_f32 v194, v192\n" \
  "v_cmp_nge_f32 vcc, %[rt], |v192|\n" \
  "s_and_b64 vcc, vcc, %[lo]\n" \
  "v_add_f32_dpp v198, v194, v199 quad_perm:[0,0,0,0] row_mask:0xf bank_mask:0xf\n" \
  "v_mov_b32_dpp v193, v194 quad_perm:[1,1,1,1] row_mask:0xf bank_mask:0xf\n" \
  "v_add_f32 v198, v198, v193\n" \
  "v_mov_b32_dpp v195, v194 quad_perm:[0,0,0,0] row_mask:0xf bank_mask:0xf\n" \
  "s_cbranch_scc1 .Lw2_%=\n" \
  "v_add_f32_dpp v198, v198, v198 row_ror:4 row_mask:0xf bank_mask:0xf\n" \
  "s_nop 1\n" \
  "v_add_f32_dpp v198, v198, v198 row_ror:8 row_mask:0xf bank_mask:0xf\n" \
  "v_rcp_f32 v198, v198\n" \
  "s_nop 0\n" \
  "v_mul_f32 v195, v195, v198\n" \
  "v_mul_f32 v193, v193, v198\n" \
  "v_cvt_pk_bf16_f32 v196, v195, v193\n" \
  "buffer_store_dword v196, v251, %[rs], %[so] offen sc1\n" \
  "v_cvt_pk_bf16_f32 v197, v198, v198\n" \
  "v_and_b32 v197, v252, v197\n" \
  "buffer_store_dword v197, v251, %[rs], %[so] offen offset:64 sc1\n" \
  ".Lw2b_%=:\n" \
  "s_setprio 0\n"
#define QK_A1 \
  "s_branch .Lgo_%=\n" \
  AL(A_QK_A1) \
  ".Lr_%=:\n" \
  "s_sub_u32 %[rt], %[rt], 1\n" \
  "s_cbranch_scc1 .Lrb_%=\n" \
  "s_sleep 2\n" \
  "buffer_load_dwordx4 v[192:195], v248, %[rs], %[o0] offen sc1\n" \
  "buffer_load_dwordx4 v[196:199], v248, %[rs], %[o0] offen offset:1024 sc1\n" \
  "v_mov_b64 v[224:225], 0\n" \
  "v_mov_b64 v[226:227], 0\n" \
  "v_mov_b64 v[228:229], 0\n" \
  "v_mov_b64 v[230:231], 0\n" \
  "v_mov_b64 v[232:233], 0\n" \
  "v_mov_b64 v[234:235], 0\n" \
  "v_mov_b64 v[236:237], 0\n" \
  "v_mov_b64 v[238:239], 0\n" \
  "s_nop 1\n" \
  "s_branch .Lw_%=\n" \
  ".Lw1_%=:\n" \
  "v_max_f32 v200, v192, v193\n" \
  "s_nop 1\n" \
  "v_max_f32_dpp v200, v200, v200 row_ror:8 row_mask:0xf bank_mask:0xf\n" \
  "s_nop 1\n" \
  "v_max_f32_dpp v200, v200, v200 row_ror:4 row_mask:0xf bank_mask:0xf\n" \
  "s_nop 1\n" \
  "v_max_f32_dpp v200, v200, v200 row_ror:2 row_mask:0xf bank_mask:0xf\n" \
  "s_nop 1\n" \
  "v_max_f32_dpp v200, v200, v200 row_ror:1 row_mask:0xf bank_mask:0xf\n" \
  "v_sub_f32 v194, v192, v200\n" \
  "v_sub_f32 v195, v193, v200\n" \
  "v_exp_f32 v194, v194\n" \
  "v_exp_f32 v195, v195\n" \
  "s_mov_b32 %[rt], -1.0\n" \
  "s_nop 0\n" \
  "v_cvt_pk_bf16_f32 v196, v194, v195\n" \
  "v_and_b32 v196, 0x7fffffff, v196\n" \
  "s_branch .Lw1b_%=\n" \
  ".Lw2_%=:\n" \
  "s_cmp_lt_i32 %[rt], 0\n" \
  "s_cbranch_scc1 .Lw3_%=\n" \
  "v_mov_b32 v200, 0\n" \
  ".Lw3_%=:\n" \
  "s_nop 1\n" \
  "v_max_f32_dpp v197, v192, v200 quad_perm:[0,0,0,0] row_mask:0xf bank_mask:0xf\n" \
  "s_nop 1\n" \
  "v_max_f32_dpp v197, v192, v197 quad_perm:[1,1,1,1] row_mask:0xf bank_mask:0xf\n" \
  "s_nop 1\n" \
  "v_max_f32_dpp v197, v197, v197 row_ror:4 row_mask:0xf bank_mask:0xf\n" \
  "s_nop 1\n" \
  "v_max_f32_dpp v197, v197, v197 row_ror:8 row_mask:0xf bank_mask:0xf\n" \
  "v_sub_f32 v194, v192, v197\n" \
  "v_sub_f32 v201, v200, v197\n" \
  "v_exp_f32 v194, v194\n" \
  "v_exp_f32 v201, v201\n" \
  "s_nop 1\n" \
  "v_mul_f32 v199, v201, v199\n" \
  "s_nop 1\n" \
  "v_add_f32_dpp v198, v194, v199 quad_perm:[0,0,0,0] row_mask:0xf bank_mask:0xf\n" \
  "s_nop 1\n" \
  "v_mov_b32_dpp v193, v194 quad_perm:[1,1,1,1] row_mask:0xf bank_mask:0xf\n" \
  "s_nop 1\n" \
  "v_add_f32 v198, v198, v193\n" \
  "s_nop 1\n" \
  "v_mov_b32_dpp v195, v194 quad_perm:[0,0,0,0] row_mask:0xf bank_mask:0xf\n" \
  "s_nop 1\n" \
  "v_add_f32_dpp v198, v198, v198 row_ror:4 row_mask:0xf bank_mask:0xf\n" \
  "s_nop 1\n" \
  "v_add_f32_dpp v198, v198, v198 row_ror:8 row_mask:0xf bank_mask:0xf\n" \
  "s_nop 1\n" \
  "v_rcp_f32 v198, v198\n" \
  "s_nop 1\n" \
  "v_mul_f32 v195, v195, v198\n" \
  "s_nop 1\n" \
  "v_mul_f32 v193, v193, v198\n" \
  "s_nop 1\n" \
  "v_cvt_pk_bf16_f32 v196, v195, v193\n" \
  "s_nop 1\n" \
  "v_and_b32 v196, 0x7fffffff, v196\n" \
  "s_nop 1\n" \
  "v_mul_f32 v198, v201, v198\n" \
  "s_nop 1\n" \
  "buffer_store_dword v196, v251, %[rs], %[so] offen sc1\n" \
  "s_nop 1\n" \
  "v_cvt_pk_bf16_f32 v197, v198, v198\n" \
  "s_nop 1\n" \
  "v_and_b32 v197, v252, v197\n" \
  "s_nop 1\n" \
  "buffer_store_dword v197, v251, %[rs], %[so] offen offset:64 sc1\n" \
  "s_branch .Lw2b_%=\n" \
  ".Lgo_%=:\n" \
  "v_add_u32 v252, 0x20000, v248\n" \
  "ds_read_b128 v[200:203], v248 offset:32768\n" \
  "ds_read_b128 v[204:207], v248 offset:33792\n" \
  "ds_read_b128 v[208:211], v248 offset:49152\n" \
  "ds_read_b128 v[212:215], v248 offset:50176\n" \
  "ds_read_b128 v[216:219], v252 offset:0\n" \
  "ds_read_b128 v[220:223], v252 offset:1024\n" \
  "ds_read_b128 v[240:243], v252 offset:16384\n" \
  "ds_read_b128 v[244:247], v252 offset:17408\n" \
  "v_mov_b64 v[224:225], 0\n" \
  "v_mov_b64 v[226:227], 0\n" \
  "v_mov_b64 v[228:229], 0\n" \
  "v_mov_b64 v[230:231], 0\n" \
  "v_mov_b64 v[232:233], 0\n" \
  "v_mov_b64 v[234:235], 0\n" \
  "v_mov_b64 v[236:237], 0\n" \
  "v_mov_b64 v[238:239], 0\n" \
  "v_xor_b32 v250, 0x800, v248\n" \
  "v_xor_b32 v251, 0x1000, v248\n" \
  "s_mov_b32 %[rt], 0x40000\n" \
  "s_setprio 2\n" \
  "s_waitcnt lgkmcnt(0)\n" \
  "s_barrier\n" \
  "buffer_load_dwordx4 v[192:195], v248, %[rs], %[o0] offen sc1\n" \
  "buffer_load_dwordx4 v[196:199], v248, %[rs], %[o0] offen offset:1024 sc1\n" \
  ".Lw_%=:\n" \
  "s_waitcnt vmcnt(1)\n" \
  "ds_write_b128 v248, v[192:195]\n" \
  "v_smfmac_f32_16x16x64_bf16 v[224:227], v[192:195], v[200:207], v249\n" \
  "v_max3_u32 v252, v192, v193, v194\n" \
  "s_waitcnt vmcnt(0)\n" \
  "ds_write_b128 v248, v[196:199] offset:1024\n" \
  "v_smfmac_f32_16x16x64_bf16 v[228:231], v[192:195], v[208:215], v249\n" \
  "v_max3_u32 v252, v252, v195, v196\n" \
  "v_smfmac_f32_16x16x64_bf16 v[224:227], v[196:199], v[216:223], v249\n" \
  "v_max3_u32 v252, v252, v197, v198\n" \
  "v_smfmac_f32_16x16x64_bf16 v[228:231], v[196:199], v[240:247], v249\n" \
  "v_max_u32 v252, v252, v199\n" \
  "v_smfmac_f32_16x16x64_bf16 v[232:235], v[192:195], v[96:103], v249\n" \
  "v_cmp_eq_u32 vcc, -1, v252\n" \
  "v_smfmac_f32_16x16x64_bf16 v[236:239], v[192:195], v[144:151], v249\n" \
  "v_xor_b32 v252, 0x1800, v248\n" \
  "s_cbranch_vccnz .Lr_%=\n" \
  ".Lrb_%=:\n" \
  "v_smfmac_f32_16x16x64_bf16 v[232:235], v[196:199], v[104:111], v249\n" \
  "v_smfmac_f32_16x16x64_bf16 v[236:239], v[196:199], v[152:159], v249\n" \
  "s_waitcnt lgkmcnt(0)\n" \
  "s_barrier\n" \
  "ds_read_b128 v[200:203], v250 offset:0\n" \
  "ds_read_b128 v[204:207], v250 offset:1024\n" \
  "ds_read_b128 v[208:211], v251 offset:0\n" \
  "ds_read_b128 v[212:215], v251 offset:1024\n" \
  "ds_read_b128 v[216:219], v252 offset:0\n" \
  "ds_read_b128 v[220:223], v252 offset:1024\n" \
  "s_waitcnt lgkmcnt(5)\n" \
  "v_smfmac_f32_16x16x64_bf16 v[224:227], v[200:203], v[0:7], v249\n" \
  "v_smfmac_f32_16x16x64_bf16 v[228:231], v[200:203], v[48:55], v249\n" \
  "s_waitcnt lgkmcnt(4)\n" \
  "v_smfmac_f32_16x16x64_bf16 v[224:227], v[204:207], v[8:15], v249\n" \
  "v_smfmac_f32_16x16x64_bf16 v[228:231], v[204:207], v[56:63], v249\n" \
  "s_waitcnt lgkmcnt(3)\n" \
  "v_smfmac_f32_16x16x64_bf16 v[224:227], v[208:211], v[16:23], v249\n" \
  "v_smfmac_f32_16x16x64_bf16 v[228:231], v[208:211], v[64:71], v249\n" \
  "s_waitcnt lgkmcnt(2)\n" \
  "v_smfmac_f32_16x16x64_bf16 v[224:227], v[212:215], v[24:31], v249\n" \
  "s_waitcnt lgkmcnt(1)\n" \
  "v_smfmac_f32_16x16x64_bf16 v[224:227], v[216:219], v[32:39], v249\n" \
  "s_waitcnt lgkmcnt(0)\n" \
  "v_smfmac_f32_16x16x64_bf16 v[224:227], v[220:223], v[40:47], v249\n" \
  "v_smfmac_f32_16x16x64_bf16 v[228:231], v[212:215], v[72:79], v249\n" \
  "v_smfmac_f32_16x16x64_bf16 v[228:231], v[216:219], v[80:87], v249\n" \
  "s_nop 0\n" \
  "v_add_f32 v224, v224, v226\n" \
  "v_smfmac_f32_16x16x64_bf16 v[228:231], v[220:223], v[88:95], v249\n" \
  "v_add_f32 v225, v225, v227\n" \
  "ds_write_b64 v248, v[224:225] offset:8192\n" \
  "s_nop 6\n" \
  "v_add_f32 v228, v228, v230\n" \
  "v_add_f32 v229, v229, v231\n" \
  "ds_write_b64 v248, v[228:229] offset:8200\n" \
  "s_setprio 0\n" \
  "ds_read_b128 v[240:243], v254 offset:0\n" \
  "ds_read_b128 v[244:247], v254 offset:1024\n" \
  "s_waitcnt lgkmcnt(2)\n" \
  "s_barrier\n" \
  "s_nop 1\n" \
  "ds_read_b128 v[192:195], v254 offset:16384\n" \
  "ds_read_b128 v[196:199], v254 offset:17408\n" \
  "ds_read_b128 v[224:227], v254 offset:32768\n" \
  "ds_read_b128 v[228:231], v254 offset:33792\n" \
  "s_waitcnt lgkmcnt(4)\n" \
  "v_smfmac_f32_16x16x64_bf16 v[232:235], v[200:203], v[240:247], v249\n" \
  "ds_read_b128 v[240:243], v254 offset:49152\n" \
  "ds_read_b128 v[244:247], v254 offset:50176\n" \
  "s_waitcnt lgkmcnt(4)\n" \
  "v_smfmac_f32_16x16x64_bf16 v[236:239], v[200:203], v[192:199], v249\n" \
  "v_smfmac_f32_16x16x64_bf16 v[232:235], v[204:207], v[112:119], v249\n" \
  "ds_read_b128 v[192:195], v248 offset:14336\n" \
  "v_smfmac_f32_16x16x64_bf16 v[236:239], v[204:207], v[160:167], v249\n" \
  "ds_read_b128 v[196:199], v248 offset:15360\n" \
  "v_smfmac_f32_16x16x64_bf16 v[232:235], v[212:215], v[120:127], v249\n" \
  "ds_read_b128 v[200:203], v248 offset:18432\n" \
  "v_smfmac_f32_16x16x64_bf16 v[236:239], v[212:215], v[168:175], v249\n" \
  "ds_read_b128 v[204:207], v248 offset:19456\n" \
  "s_waitcnt lgkmcnt(6)\n" \
  "v_smfmac_f32_16x16x64_bf16 v[232:235], v[208:211], v[224:231], v249\n" \
  "ds_read_b128 v[212:215], v248 offset:21504\n" \
  "s_waitcnt lgkmcnt(5)\n" \
  "v_smfmac_f32_16x16x64_bf16 v[236:239], v[208:211], v[240:247], v249\n" \
  "ds_read_b128 v[240:243], v248 offset:16384\n" \
  "v_smfmac_f32_16x16x64_bf16 v[232:235], v[216:219], v[128:135], v249\n" \
  "ds_read_b128 v[244:247], v248 offset:17408\n" \
  "v_smfmac_f32_16x16x64_bf16 v[232:235], v[220:223], v[136:143], v249\n" \
  "ds_read_b128 v[208:211], v248 offset:20480\n" \
  "v_smfmac_f32_16x16x64_bf16 v[236:239], v[216:219], v[176:183], v249\n" \
  "ds_read_b96 v[250:252], v248 offset:13312\n" \
  "v_smfmac_f32_16x16x64_bf16 v[236:239], v[220:223], v[184:191], v249\n" \
  "s_setprio 3\n" \
  "s_barrier\n" \
  "ds_read_b64_tr_b16 v[224:225], v253 offset:17408\n" \
  "ds_read_b64_tr_b16 v[226:227], v253 offset:17664\n" \
  "ds_read_b64_tr_b16 v[228:229], v253 offset:19456\n" \
  "ds_read_b64_tr_b16 v[230:231], v253 offset:19712\n" \
  "v_mov_b64 v[216:217], 0\n" \
  "v_mov_b64 v[218:219], 0\n" \
  "v_add_f32 v232, v232, v234\n" \
  "v_add_f32 v233, v233, v235\n" \
  "ds_write_b64 v248, v[232:233] offset:0\n" \
  "v_mov_b64 v[220:221], 0\n" \
  "v_mov_b64 v[222:223], 0\n" \
  "v_add_f32 v236, v236, v238\n" \
  "v_add_f32 v237, v237, v239\n" \
  "ds_write_b64 v248, v[236:237] offset:8\n" \
  "s_waitcnt lgkmcnt(4)\n" \
  "v_smfmac_f32_16x16x64_bf16 v[216:219], v[224:227], v[192:199], v249\n" \
  "v_smfmac_f32_16x16x64_bf16 v[220:223], v[224:227], v[200:207], v249\n" \
  "s_waitcnt lgkmcnt(2)\n" \
  "v_smfmac_f32_16x16x64_bf16 v[216:219], v[228:231], v[240:247], v249\n" \
  "v_smfmac_f32_16x16x64_bf16 v[220:223], v[228:231], v[208:215], v249\n" \
  "s_mov_b32 %[rt], 0x422c0000\n" \
  "s_waitcnt lgkmcnt(0)\n" \
  "s_barrier\n" \
  "s_nop 1\n" \
  "v_add_f32 v192, v217, v219\n" \
  "s_nop 2\n" \
  "v_add_f32 v193, v221, v223\n" \
  "v_exp_f32 v194, v192\n" \
  "v_exp_f32 v195, v193\n" \
  "v_max_f32 v197, |v192|, |v193|\n" \
  "v_cvt_pk_bf16_f32 v196, v194, v195\n" \
  "v_cmp_nge_f32 vcc, %[rt], v197\n" \
  "s_cbranch_vccnz .Lw1_%=\n" \
  ".Lw1b_%=:\n" \
  "buffer_store_dword v196, v250, %[rs], %[c0] offen sc1\n" \
  "buffer_store_dword v196, v250, %[rs], %[c1] offen sc1\n" \
  "buffer_store_dword v196, v250, %[rs], %[c2] offen sc1\n" \
  "buffer_store_dword v196, v250, %[rs], %[c3] offen sc1\n" \
  "v_add_f32 v199, v194, v195\n" \
  "v_xor_b32 v251, %[xr], v251\n" \
  "s_nop 0\n" \
  "v_add_f32_dpp v199, v199, v199 quad_perm:[1,0,3,2] row_mask:0xf bank_mask:0xf\n" \
  "s_nop 1\n" \
  "v_add_f32_dpp v199, v199, v199 quad_perm:[2,3,0,1] row_mask:0xf bank_mask:0xf\n" \
  "s_barrier\n" \
  "ds_read_b64_tr_b16 v[232:233], v253 offset:9216\n" \
  "ds_read_b64_tr_b16 v[234:235], v253 offset:9472\n" \
  "ds_read_b64_tr_b16 v[236:237], v253 offset:11264\n" \
  "ds_read_b64_tr_b16 v[238:239], v253 offset:11520\n" \
  "s_waitcnt lgkmcnt(2)\n" \
  "v_mfma_f32_16x16x32_bf16 v[216:219], v[224:227], v[232:235], 0\n" \
  "s_waitcnt lgkmcnt(0)\n" \
  "v_mfma_f32_16x16x32_bf16 v[216:219], v[228:231], v[236:239], v[216:219]\n" \
  "s_nop 8\n" \
  "v_add_f32_dpp v192, v219, v217 quad_perm:[2,3,2,3] row_mask:0xf bank_mask:0xf\n" \
  "v_exp_f32 v194, v192\n" \
  "v_cmp_nge_f32 vcc, %[rt], |v192|\n" \
  "s_and_b64 vcc, vcc, %[lo]\n" \
  "v_add_f32_dpp v198, v194, v199 quad_perm:[0,0,0,0] row_mask:0xf bank_mask:0xf\n" \
  "v_mov_b32_dpp v193, v194 quad_perm:[1,1,1,1] row_mask:0xf bank_mask:0xf\n" \
  "v_add_f32 v198, v198, v193\n" \
  "v_mov_b32_dpp v195, v194 quad_perm:[0,0,0,0] row_mask:0xf bank_mask:0xf\n" \
  "s_cbranch_scc1 .Lw2_%=\n" \
  "v_add_f32_dpp v198, v198, v198 row_ror:4 row_mask:0xf bank_mask:0xf\n" \
  "s_nop 1\n" \
  "v_add_f32_dpp v198, v198, v198 row_ror:8 row_mask:0xf bank_mask:0xf\n" \
  "v_rcp_f32 v198, v198\n" \
  "s_nop 0\n" \
  "v_mul_f32 v195, v195, v198\n" \
  "v_mul_f32 v193, v193, v198\n" \
  "v_cvt_pk_bf16_f32 v196, v195, v193\n" \
  "buffer_store_dword v196, v251, %[rs], %[so] offen sc1\n" \
  "v_cvt_pk_bf16_f32 v197, v198, v198\n" \
  "v_and_b32 v197, v252, v197\n" \
  "buffer_store_dword v197, v251, %[rs], %[so] offen offset:64 sc1\n" \
  ".Lw2b_%=:\n" \
  "s_setprio 0\n"
#define QK_X \
  "s_branch .Lgo_%=\n" \
  AL(A_QK_X) \
  ".Lr_%=:\n" \
  "s_sub_u32 %[rt], %[rt], 1\n" \
  "s_cbranch_scc1 .Lrb_%=\n" \
  "s_sleep 2\n" \
  "buffer_load_dwordx4 v[192:195], v248, %[rs], %[o0] offen sc1\n" \
  "buffer_load_dwordx4 v[196:199], v248, %[rs], %[o0] offen offset:1024 sc1\n" \
  "v_mov_b64 v[224:225], 0\n" \
  "v_mov_b64 v[226:227], 0\n" \
  "v_mov_b64 v[228:229], 0\n" \
  "v_mov_b64 v[230:231], 0\n" \
  "v_mov_b64 v[232:233], 0\n" \
  "v_mov_b64 v[234:235], 0\n" \
  "v_mov_b64 v[236:237], 0\n" \
  "v_mov_b64 v[238:239], 0\n" \
  "s_nop 1\n" \
  "s_branch .Lw_%=\n" \
  ".Lgo_%=:\n" \
  "v_add_u32 v252, 0x20000, v248\n" \
  "ds_read_b128 v[200:203], v248 offset:32768\n" \
  "ds_read_b128 v[204:207], v248 offset:33792\n" \
  "ds_read_b128 v[208:211], v248 offset:49152\n" \
  "ds_read_b128 v[212:215], v248 offset:50176\n" \
  "ds_read_b128 v[216:219], v252 offset:0\n" \
  "ds_read_b128 v[220:223], v252 offset:1024\n" \
  "ds_read_b128 v[240:243], v252 offset:16384\n" \
  "ds_read_b128 v[244:247], v252 offset:17408\n" \
  "v_mov_b64 v[224:225], 0\n" \
  "v_mov_b64 v[226:227], 0\n" \
  "v_mov_b64 v[228:229], 0\n" \
  "v_mov_b64 v[230:231], 0\n" \
  "v_mov_b64 v[232:233], 0\n" \
  "v_mov_b64 v[234:235], 0\n" \
  "v_mov_b64 v[236:237], 0\n" \
  "v_mov_b64 v[238:239], 0\n" \
  "v_xor_b32 v250, 0x800, v248\n" \
  "v_xor_b32 v251, 0x1000, v248\n" \
  "s_mov_b32 %[rt], 0x40000\n" \
  "s_setprio 2\n" \
  "s_waitcnt lgkmcnt(0)\n" \
  "s_barrier\n" \
  "s_nop 3\n" \
  "buffer_load_dwordx4 v[192:195], v248, %[rs], %[o0] offen sc1\n" \
  "buffer_load_dwordx4 v[196:199], v248, %[rs], %[o0] offen offset:1024 sc1\n" \
  ".Lw_%=:\n" \
  "s_waitcnt vmcnt(1)\n" \
  "ds_write_b128 v248, v[192:195]\n" \
  "v_smfmac_f32_16x16x64_bf16 v[224:227], v[192:195], v[200:207], v249\n" \
  "v_max3_u32 v252, v192, v193, v194\n" \
  "s_waitcnt vmcnt(0)\n" \
  "ds_write_b128 v248, v[196:199] offset:1024\n" \
  "v_smfmac_f32_16x16x64_bf16 v[228:231], v[192:195], v[208:215], v249\n" \
  "v_max3_u32 v252, v252, v195, v196\n" \
  "v_smfmac_f32_16x16x64_bf16 v[224:227], v[196:199], v[216:223], v249\n" \
  "v_max3_u32 v252, v252, v197, v198\n" \
  "v_smfmac_f32_16x16x64_bf16 v[228:231], v[196:199], v[240:247], v249\n" \
  "v_max_u32 v252, v252, v199\n" \
  "v_smfmac_f32_16x16x64_bf16 v[232:235], v[192:195], v[96:103], v249\n" \
  "v_cmp_eq_u32 vcc, -1, v252\n" \
  "v_smfmac_f32_16x16x64_bf16 v[236:239], v[192:195], v[144:151], v249\n" \
  "v_xor_b32 v252, 0x1800, v248\n" \
  "s_cbranch_vccnz .Lr_%=\n" \
  ".Lrb_%=:\n" \
  "v_smfmac_f32_16x16x64_bf16 v[232:235], v[196:199], v[104:111], v249\n" \
  "v_smfmac_f32_16x16x64_bf16 v[236:239], v[196:199], v[152:159], v249\n" \
  "s_waitcnt lgkmcnt(0)\n" \
  "s_barrier\n" \
  "ds_read_b128 v[200:203], v250 offset:0\n" \
  "ds_read_b128 v[204:207], v250 offset:1024\n" \
  "ds_read_b128 v[208:211], v251 offset:0\n" \
  "ds_read_b128 v[212:215], v251 offset:1024\n" \
  "ds_read_b128 v[216:219], v252 offset:0\n" \
  "ds_read_b128 v[220:223], v252 offset:1024\n" \
  "s_waitcnt lgkmcnt(5)\n" \
  "v_smfmac_f32_16x16x64_bf16 v[224:227], v[200:203], v[0:7], v249\n" \
  "v_smfmac_f32_16x16x64_bf16 v[228:231], v[200:203], v[48:55], v249\n" \
  "s_waitcnt lgkmcnt(4)\n" \
  "v_smfmac_f32_16x16x64_bf16 v[224:227], v[204:207], v[8:15], v249\n" \
  "v_smfmac_f32_16x16x64_bf16 v[228:231], v[204:207], v[56:63], v249\n" \
  "s_waitcnt lgkmcnt(3)\n" \
  "v_smfmac_f32_16x16x64_bf16 v[224:227], v[208:211], v[16:23], v249\n" \
  "v_smfmac_f32_16x16x64_bf16 v[228:231], v[208:211], v[64:71], v249\n" \
  "s_waitcnt lgkmcnt(2)\n" \
  "v_smfmac_f32_16x16x64_bf16 v[224:227], v[212:215], v[24:31], v249\n" \
  "s_waitcnt lgkmcnt(1)\n" \
  "v_smfmac_f32_16x16x64_bf16 v[224:227], v[216:219], v[32:39], v249\n" \
  "s_waitcnt lgkmcnt(0)\n" \
  "v_smfmac_f32_16x16x64_bf16 v[224:227], v[220:223], v[40:47], v249\n" \
  "v_smfmac_f32_16x16x64_bf16 v[228:231], v[212:215], v[72:79], v249\n" \
  "v_and_b32 v252, 0x3ff, v248\n" \
  "v_smfmac_f32_16x16x64_bf16 v[228:231], v[216:219], v[80:87], v249\n" \
  "s_nop 0\n" \
  "v_add_f32 v224, v224, v226\n" \
  "v_smfmac_f32_16x16x64_bf16 v[228:231], v[220:223], v[88:95], v249\n" \
  "v_add_f32 v225, v225, v227\n" \
  "ds_write_b64 v248, v[224:225] offset:8192\n" \
  "s_nop 6\n" \
  "v_add_f32 v228, v228, v230\n" \
  "v_add_f32 v229, v229, v231\n" \
  "ds_write_b64 v248, v[228:229] offset:8200\n" \
  "s_setprio 0\n" \
  "ds_read_b128 v[240:243], v254 offset:0\n" \
  "ds_read_b128 v[244:247], v254 offset:1024\n" \
  "s_waitcnt lgkmcnt(2)\n" \
  "s_barrier\n" \
  "s_nop 1\n" \
  "ds_read_b128 v[192:195], v254 offset:16384\n" \
  "ds_read_b128 v[196:199], v254 offset:17408\n" \
  "ds_read_b128 v[224:227], v254 offset:32768\n" \
  "ds_read_b128 v[228:231], v254 offset:33792\n" \
  "s_waitcnt lgkmcnt(4)\n" \
  "v_smfmac_f32_16x16x64_bf16 v[232:235], v[200:203], v[240:247], v249\n" \
  "ds_read_b128 v[240:243], v254 offset:49152\n" \
  "ds_read_b128 v[244:247], v254 offset:50176\n" \
  "s_waitcnt lgkmcnt(4)\n" \
  "v_smfmac_f32_16x16x64_bf16 v[236:239], v[200:203], v[192:199], v249\n" \
  "v_smfmac_f32_16x16x64_bf16 v[232:235], v[204:207], v[112:119], v249\n" \
  "v_smfmac_f32_16x16x64_bf16 v[236:239], v[204:207], v[160:167], v249\n" \
  "v_smfmac_f32_16x16x64_bf16 v[232:235], v[212:215], v[120:127], v249\n" \
  "v_smfmac_f32_16x16x64_bf16 v[236:239], v[212:215], v[168:175], v249\n" \
  "s_waitcnt lgkmcnt(2)\n" \
  "v_smfmac_f32_16x16x64_bf16 v[232:235], v[208:211], v[224:231], v249\n" \
  "s_waitcnt lgkmcnt(0)\n" \
  "v_smfmac_f32_16x16x64_bf16 v[236:239], v[208:211], v[240:247], v249\n" \
  "v_smfmac_f32_16x16x64_bf16 v[232:235], v[216:219], v[128:135], v249\n" \
  "v_smfmac_f32_16x16x64_bf16 v[232:235], v[220:223], v[136:143], v249\n" \
  "v_smfmac_f32_16x16x64_bf16 v[236:239], v[216:219], v[176:183], v249\n" \
  "buffer_load_dwordx4 v[224:227], v252, %[rs], %[qo] offen offset:0 sc1\n" \
  "buffer_load_dwordx4 v[228:231], v252, %[rs], %[qo] offen offset:1024 sc1\n" \
  "buffer_load_dwordx4 v[240:243], v252, %[rs], %[qo] offen offset:2048 sc1\n" \
  "buffer_load_dwordx4 v[244:247], v252, %[rs], %[qo] offen offset:3072 sc1\n" \
  "v_smfmac_f32_16x16x64_bf16 v[236:239], v[220:223], v[184:191], v249\n" \
  "s_setprio 3\n" \
  "s_barrier\n" \
  "v_add_f32 v232, v232, v234\n" \
  "v_add_f32 v233, v233, v235\n" \
  "ds_write_b64 v248, v[232:233] offset:0\n" \
  "s_nop 3\n" \
  "v_add_f32 v236, v236, v238\n" \
  "v_add_f32 v237, v237, v239\n" \
  "ds_write_b64 v248, v[236:237] offset:8\n" \
  "s_waitcnt lgkmcnt(0)\n" \
  "s_barrier\n" \
  "s_setprio 0\n" \
  "s_nop 15\n" \
  "s_nop 13\n" \
  "ds_read_b128 v[192:195], v252 offset:24576\n" \
  "ds_read_b128 v[196:199], v252 offset:28672\n" \
  "ds_read_b128 v[200:203], v252 offset:25600\n" \
  "ds_read_b128 v[204:207], v252 offset:29696\n" \
  "ds_read_b128 v[208:211], v252 offset:26624\n" \
  "ds_read_b128 v[212:215], v252 offset:30720\n" \
  "ds_read_b128 v[216:219], v252 offset:27648\n" \
  "ds_read_b128 v[220:223], v252 offset:31744\n" \
  "s_waitcnt lgkmcnt(0)\n" \
  "s_barrier\n" \
  "s_nop 15\n" \
  "s_nop 3\n" \
  "ds_read_b64_tr_b16 v[232:233], v253 offset:9216\n" \
  "ds_read_b64_tr_b16 v[234:235], v253 offset:9472\n" \
  "ds_read_b64_tr_b16 v[236:237], v253 offset:11264\n" \
  "ds_read_b64_tr_b16 v[238:239], v253 offset:11520\n" \
  "s_waitcnt vmcnt(3)\n" \
  "v_mfma_f32_16x16x32_bf16 v[192:195], v[192:195], v[224:227], 0\n" \
  "v_mfma_f32_16x16x32_bf16 v[196:199], v[196:199], v[224:227], 0\n" \
  "s_waitcnt vmcnt(2)\n" \
  "v_mfma_f32_16x16x32_bf16 v[192:195], v[200:203], v[228:231], v[192:195]\n" \
  "v_mfma_f32_16x16x32_bf16 v[196:199], v[204:207], v[228:231], v[196:199]\n" \
  "s_waitcnt vmcnt(1)\n" \
  "v_mfma_f32_16x16x32_bf16 v[192:195], v[208:211], v[240:243], v[192:195]\n" \
  "v_mov_b64 v[200:201], 0\n" \
  "v_mfma_f32_16x16x32_bf16 v[196:199], v[212:215], v[240:243], v[196:199]\n" \
  "v_mov_b64 v[202:203], 0\n" \
  "s_waitcnt vmcnt(0)\n" \
  "v_mfma_f32_16x16x32_bf16 v[192:195], v[216:219], v[244:247], v[192:195]\n" \
  "v_mfma_f32_16x16x32_bf16 v[196:199], v[220:223], v[244:247], v[196:199]\n" \
  "s_waitcnt lgkmcnt(2)\n" \
  "v_smfmac_f32_16x16x64_bf16 v[200:203], v[232:235], v[224:231], v249\n" \
  "s_waitcnt lgkmcnt(0)\n" \
  "v_smfmac_f32_16x16x64_bf16 v[200:203], v[236:239], v[240:247], v249\n" \
  "s_nop 8\n"
#define QK_K \
  "s_branch .Lgo_%=\n" \
  AL(A_QK_K) \
  ".Lr_%=:\n" \
  "s_sub_u32 %[rt], %[rt], 1\n" \
  "s_cbranch_scc1 .Lrb_%=\n" \
  "s_sleep 2\n" \
  "buffer_load_dwordx4 v[192:195], v248, %[rs], %[o0] offen sc1\n" \
  "buffer_load_dwordx4 v[196:199], v248, %[rs], %[o0] offen offset:1024 sc1\n" \
  "v_mov_b64 v[224:225], 0\n" \
  "v_mov_b64 v[226:227], 0\n" \
  "v_mov_b64 v[228:229], 0\n" \
  "v_mov_b64 v[230:231], 0\n" \
  "s_nop 1\n" \
  "s_branch .Lw_%=\n" \
  ".Lgo_%=:\n" \
  "v_add_u32 v252, 0x20000, v248\n" \
  "ds_read_b128 v[200:203], v248 offset:32768\n" \
  "ds_read_b128 v[204:207], v248 offset:33792\n" \
  "ds_read_b128 v[208:211], v248 offset:49152\n" \
  "ds_read_b128 v[212:215], v248 offset:50176\n" \
  "ds_read_b128 v[216:219], v252 offset:0\n" \
  "ds_read_b128 v[220:223], v252 offset:1024\n" \
  "ds_read_b128 v[240:243], v252 offset:16384\n" \
  "ds_read_b128 v[244:247], v252 offset:17408\n" \
  "v_mov_b64 v[224:225], 0\n" \
  "v_mov_b64 v[226:227], 0\n" \
  "v_mov_b64 v[228:229], 0\n" \
  "v_mov_b64 v[230:231], 0\n" \
  "v_xor_b32 v250, 0x800, v248\n" \
  "v_xor_b32 v251, 0x1000, v248\n" \
  "s_mov_b32 %[rt], 0x40000\n" \
  "s_setprio 1\n" \
  "s_waitcnt lgkmcnt(0)\n" \
  "s_cmp_eq_u32 %[tw], 0\n" \
  "s_cbranch_scc1 .Ltq_%=\n" \
  ".Ltl_%=:\n" \
  "s_memtime vcc\n" \
  "s_waitcnt lgkmcnt(0)\n" \
  "s_sub_i32 %[cnt], %[tg], vcc_lo\n" \
  "s_cmp_lt_i32 %[cnt], 260\n" \
  "s_cbranch_scc1 .Ltf_%=\n" \
  "s_sleep 1\n" \
  "s_branch .Ltl_%=\n" \
  ".Ltf_%=:\n" \
  "s_max_i32 %[cnt], %[cnt], 0\n" \
  ".Ltg_%=:\n" \
  "s_sub_u32 %[cnt], %[cnt], 28\n" \
  "s_cbranch_scc0 .Ltg_%=\n" \
  ".Ltq_%=:\n" \
  "s_barrier\n" \
  "buffer_load_dwordx4 v[192:195], v248, %[rs], %[o0] offen sc1\n" \
  "buffer_load_dwordx4 v[196:199], v248, %[rs], %[o0] offen offset:1024 sc1\n" \
  ".Lw_%=:\n" \
  "s_waitcnt vmcnt(1)\n" \
  "ds_write_b128 v248, v[192:195]\n" \
  "v_smfmac_f32_16x16x64_bf16 v[224:227], v[192:195], v[200:207], v249\n" \
  "v_max3_u32 v252, v192, v193, v194\n" \
  "s_waitcnt vmcnt(0)\n" \
  "ds_write_b128 v248, v[196:199] offset:1024\n" \
  "v_smfmac_f32_16x16x64_bf16 v[228:231], v[192:195], v[208:215], v249\n" \
  "v_max3_u32 v252, v252, v195, v196\n" \
  "v_smfmac_f32_16x16x64_bf16 v[224:227], v[196:199], v[216:223], v249\n" \
  "v_max3_u32 v252, v252, v197, v198\n" \
  "v_max_u32 v252, v252, v199\n" \
  "v_smfmac_f32_16x16x64_bf16 v[228:231], v[196:199], v[240:247], v249\n" \
  "v_cmp_eq_u32 vcc, -1, v252\n" \
  "v_xor_b32 v252, 0x1800, v248\n" \
  "s_cbranch_vccnz .Lr_%=\n" \
  ".Lrb_%=:\n" \
  "s_waitcnt lgkmcnt(0)\n" \
  "s_barrier\n" \
  "s_memtime vcc\n" \
  "s_setprio 3\n" \
  "s_waitcnt lgkmcnt(0)\n" \
  "s_sub_u32 %[cnt], vcc_lo, %[ckm]\n" \
  "s_mul_hi_u32 %[cnt], %[cnt], %[inv]\n" \
  "s_add_u32 %[cnt], %[cnt], %[r16]\n" \
  "s_and_b32 %[cnt], %[cnt], -2\n" \
  "v_mov_b32 v235, %[cnt]\n" \
  "buffer_store_dword v235, v232, %[rs], %[sst] offen sc1\n" \
  "s_nop 5\n" \
  "ds_read_b128 v[200:203], v250 offset:0\n" \
  "ds_read_b128 v[204:207], v250 offset:1024\n" \
  "ds_read_b128 v[208:211], v251 offset:0\n" \
  "ds_read_b128 v[212:215], v251 offset:1024\n" \
  "ds_read_b128 v[216:219], v252 offset:0\n" \
  "ds_read_b128 v[220:223], v252 offset:1024\n" \
  "buffer_load_dword v250, v233, %[rs], %[t0] offen sc1\n" \
  "buffer_load_dword v251, v234, %[rs], %[t1] offen sc1\n" \
  "s_setprio 1\n" \
  "s_waitcnt lgkmcnt(5)\n" \
  "v_smfmac_f32_16x16x64_bf16 v[224:227], v[200:203], v[0:7], v249\n" \
  "v_smfmac_f32_16x16x64_bf16 v[228:231], v[200:203], v[48:55], v249\n" \
  "s_waitcnt lgkmcnt(4)\n" \
  "v_smfmac_f32_16x16x64_bf16 v[224:227], v[204:207], v[8:15], v249\n" \
  "v_mov_b64 v[232:233], 0\n" \
  "v_smfmac_f32_16x16x64_bf16 v[228:231], v[204:207], v[56:63], v249\n" \
  "v_mov_b64 v[234:235], 0\n" \
  "s_waitcnt lgkmcnt(3)\n" \
  "v_smfmac_f32_16x16x64_bf16 v[224:227], v[208:211], v[16:23], v249\n" \
  "ds_read_b128 v[240:243], v254 offset:0\n" \
  "v_smfmac_f32_16x16x64_bf16 v[228:231], v[208:211], v[64:71], v249\n" \
  "ds_read_b128 v[244:247], v254 offset:1024\n" \
  "s_waitcnt lgkmcnt(4)\n" \
  "v_smfmac_f32_16x16x64_bf16 v[224:227], v[212:215], v[24:31], v249\n" \
  "s_waitcnt lgkmcnt(3)\n" \
  "v_smfmac_f32_16x16x64_bf16 v[224:227], v[216:219], v[32:39], v249\n" \
  "s_waitcnt lgkmcnt(2)\n" \
  "v_smfmac_f32_16x16x64_bf16 v[224:227], v[220:223], v[40:47], v249\n" \
  "s_barrier\n" \
  "ds_read_b128 v[236:239], v248 offset:16384\n" \
  "v_smfmac_f32_16x16x64_bf16 v[228:231], v[212:215], v[72:79], v249\n" \
  "v_smfmac_f32_16x16x64_bf16 v[228:231], v[216:219], v[80:87], v249\n" \
  "s_nop 0\n" \
  "v_add_f32 v224, v224, v226\n" \
  "v_smfmac_f32_16x16x64_bf16 v[228:231], v[220:223], v[88:95], v249\n" \
  "v_add_f32 v225, v225, v227\n" \
  "s_waitcnt lgkmcnt(0)\n" \
  "v_add_f32 v236, v236, v224\n" \
  "v_add_f32 v237, v237, v225\n" \
  "v_cvt_pk_bf16_f32 v236, v236, v237\n" \
  "s_nop 3\n" \
  "v_add_f32 v238, v238, v228\n" \
  "v_add_f32 v239, v239, v229\n" \
  "v_add_f32 v238, v238, v230\n" \
  "v_add_f32 v239, v239, v231\n" \
  "v_cvt_pk_bf16_f32 v237, v238, v239\n" \
  "ds_write_b64 v253, v[236:237] offset:17408\n" \
  "s_setprio 0\n" \
  "ds_read_b128 v[224:227], v254 offset:16384\n" \
  "ds_read_b128 v[228:231], v254 offset:17408\n" \
  "v_mov_b64 v[236:237], 0\n" \
  "v_mov_b64 v[238:239], 0\n" \
  "v_smfmac_f32_16x16x64_bf16 v[232:235], v[192:195], v[96:103], v249\n" \
  "v_smfmac_f32_16x16x64_bf16 v[236:239], v[192:195], v[144:151], v249\n" \
  "s_waitcnt lgkmcnt(2)\n" \
  "s_barrier\n" \
  "v_smfmac_f32_16x16x64_bf16 v[232:235], v[196:199], v[104:111], v249\n" \
  "v_smfmac_f32_16x16x64_bf16 v[236:239], v[196:199], v[152:159], v249\n" \
  "v_smfmac_f32_16x16x64_bf16 v[232:235], v[200:203], v[240:247], v249\n" \
  "ds_read_b128 v[192:195], v254 offset:32768\n" \
  "ds_read_b128 v[196:199], v254 offset:33792\n" \
  "s_waitcnt lgkmcnt(2)\n" \
  "v_smfmac_f32_16x16x64_bf16 v[236:239], v[200:203], v[224:231], v249\n" \
  "ds_read_b128 v[240:243], v254 offset:49152\n" \
  "ds_read_b128 v[244:247], v254 offset:50176\n" \
  "v_smfmac_f32_16x16x64_bf16 v[232:235], v[204:207], v[112:119], v249\n" \
  "v_smfmac_f32_16x16x64_bf16 v[236:239], v[204:207], v[160:167], v249\n" \
  "s_barrier\n" \
  "ds_read_b128 v[200:203], v248 offset:8192\n" \
  "v_smfmac_f32_16x16x64_bf16 v[232:235], v[212:215], v[120:127], v249\n" \
  "v_smfmac_f32_16x16x64_bf16 v[236:239], v[212:215], v[168:175], v249\n" \
  "v_smfmac_f32_16x16x64_bf16 v[232:235], v[216:219], v[128:135], v249\n" \
  "v_smfmac_f32_16x16x64_bf16 v[232:235], v[220:223], v[136:143], v249\n" \
  "s_waitcnt lgkmcnt(0)\n" \
  "v_mul_f32 v200, %[cs], v200\n" \
  "v_smfmac_f32_16x16x64_bf16 v[232:235], v[208:211], v[192:199], v249\n" \
  "v_mul_f32 v201, %[cs], v201\n" \
  "v_smfmac_f32_16x16x64_bf16 v[236:239], v[216:219], v[176:183], v249\n" \
  "v_mul_f32 v202, %[cs], v202\n" \
  "v_smfmac_f32_16x16x64_bf16 v[236:239], v[220:223], v[184:191], v249\n" \
  "v_mul_f32 v203, %[cs], v203\n" \
  "v_smfmac_f32_16x16x64_bf16 v[236:239], v[208:211], v[240:247], v249\n" \
  "v_fma_f32 v200, v232, %[cs], v200\n" \
  "v_fma_f32 v201, v233, %[cs], v201\n" \
  "v_fma_f32 v200, v234, %[cs], v200\n" \
  "v_fma_f32 v201, v235, %[cs], v201\n" \
  "v_cvt_pk_bf16_f32 v200, v200, v201\n" \
  "s_nop 3\n" \
  "v_fma_f32 v202, v236, %[cs], v202\n" \
  "v_fma_f32 v203, v237, %[cs], v203\n" \
  "v_fma_f32 v202, v238, %[cs], v202\n" \
  "v_fma_f32 v203, v239, %[cs], v203\n" \
  "v_cvt_pk_bf16_f32 v201, v202, v203\n" \
  "ds_write_b64 v253, v[200:201] offset:9216\n" \
  "s_waitcnt lgkmcnt(0)\n" \
  "s_barrier\n" \
  "s_waitcnt vmcnt(0)\n"
#define OW_F \
  "s_branch .Lgo_%=\n" \
  AL(A_OW_F) \
  ".Lbad_%=:\n" \
  "s_mov_b32 %[st], 1\n" \
  "s_branch .Lend_%=\n" \
  ".Lgo_%=:\n" \
  "v_mov_b64 v[96:97], 0\n" \
  "v_mov_b64 v[98:99], 0\n" \
  "s_mov_b32 %[st], 0\n" \
  "s_barrier\n" \
  "s_nop 15\n" \
  "s_nop 15\n" \
  "s_nop 12\n" \
  "ds_read_b32 v100, v118\n" \
  "buffer_load_dwordx4 v[0:3], v112, %[rs], %[sv1] offen offset:0 sc1\n" \
  "buffer_load_dwordx4 v[4:7], v112, %[rs], %[sv1] offen offset:1024 sc1\n" \
  "buffer_load_dwordx4 v[8:11], v112, %[rs], %[sv1] offen offset:2048 sc1\n" \
  "buffer_load_dwordx4 v[12:15], v112, %[rs], %[sv1] offen offset:3072 sc1\n" \
  "s_memtime vcc\n" \
  "s_waitcnt lgkmcnt(0)\n" \
  "v_readfirstlane_b32 %[cnt], v100\n" \
  "s_nop 0\n" \
  "s_sub_i32 %[cnt], %[cnt], vcc_lo\n" \
  "s_max_i32 %[cnt], %[cnt], 0\n" \
  "s_min_i32 %[cnt], %[cnt], 2000\n" \
  ".Ltg_%=:\n" \
  "s_sub_u32 %[cnt], %[cnt], 28\n" \
  "s_cbranch_scc0 .Ltg_%=\n" \
  "buffer_load_dwordx4 v[16:19], v112, %[rs], %[sv2] offen offset:0 sc1\n" \
  "buffer_load_dwordx4 v[20:23], v112, %[rs], %[sv2] offen offset:1024 sc1\n" \
  "buffer_load_dwordx4 v[24:27], v112, %[rs], %[sv2] offen offset:2048 sc1\n" \
  "buffer_load_dwordx4 v[28:31], v112, %[rs], %[sv2] offen offset:3072 sc1\n" \
  "s_nop 3\n" \
  "buffer_load_dwordx4 v[64:67], v113, %[rs], %[sp1] offen offset:0 sc1\n" \
  "buffer_load_dwordx4 v[68:71], v113, %[rs], %[sp1] offen offset:2048 sc1\n" \
  "buffer_load_dwordx4 v[72:75], v113, %[rs], %[sp2] offen offset:0 sc1\n" \
  "buffer_load_dwordx4 v[76:79], v113, %[rs], %[sp2] offen offset:2048 sc1\n" \
  "buffer_load_dwordx4 v[80:83], v113, %[rs], %[sp1] offen offset:64 sc1\n" \
  "buffer_load_dwordx4 v[84:87], v113, %[rs], %[sp1] offen offset:2112 sc1\n" \
  "buffer_load_dwordx4 v[88:91], v113, %[rs], %[sp2] offen offset:64 sc1\n" \
  "buffer_load_dwordx4 v[92:95], v113, %[rs], %[sp2] offen offset:2112 sc1\n" \
  "s_waitcnt vmcnt(15)\n" \
  "v_permlane16_swap_b32 v0, v1\n" \
  "v_permlane16_swap_b32 v2, v3\n" \
  "s_waitcnt vmcnt(14)\n" \
  "v_permlane16_swap_b32 v4, v5\n" \
  "v_permlane16_swap_b32 v6, v7\n" \
  "s_waitcnt vmcnt(13)\n" \
  "v_permlane16_swap_b32 v8, v9\n" \
  "v_permlane16_swap_b32 v10, v11\n" \
  "s_waitcnt vmcnt(12)\n" \
  "v_permlane16_swap_b32 v12, v13\n" \
  "v_permlane16_swap_b32 v14, v15\n" \
  "s_waitcnt vmcnt(11)\n" \
  "v_permlane16_swap_b32 v16, v17\n" \
  "v_permlane16_swap_b32 v18, v19\n" \
  "s_waitcnt vmcnt(10)\n" \
  "v_permlane16_swap_b32 v20, v21\n" \
  "v_permlane16_swap_b32 v22, v23\n" \
  "s_waitcnt vmcnt(9)\n" \
  "v_permlane16_swap_b32 v24, v25\n" \
  "v_permlane16_swap_b32 v26, v27\n" \
  "s_waitcnt vmcnt(8)\n" \
  "v_permlane16_swap_b32 v28, v29\n" \
  "v_permlane16_swap_b32 v30, v31\n" \
  "s_barrier\n" \
  "ds_read_b128 v[32:35], v114 offset:0\n" \
  "ds_read_b128 v[36:39], v114 offset:1024\n" \
  "ds_read_b128 v[40:43], v114 offset:2048\n" \
  "ds_read_b128 v[44:47], v114 offset:3072\n" \
  "ds_read_b128 v[48:51], v119 offset:0\n" \
  "ds_read_b128 v[52:55], v119 offset:1024\n" \
  "ds_read_b128 v[56:59], v119 offset:2048\n" \
  "ds_read_b128 v[60:63], v119 offset:3072\n" \
  "s_waitcnt vmcnt(7)\n" \
  "v_smfmac_f32_16x16x64_bf16 v[96:99], v[64:67], v[0:7], v117\n" \
  "s_waitcnt vmcnt(6)\n" \
  "v_smfmac_f32_16x16x64_bf16 v[96:99], v[68:71], v[8:15], v117\n" \
  "s_waitcnt vmcnt(5)\n" \
  "v_smfmac_f32_16x16x64_bf16 v[96:99], v[72:75], v[16:23], v117\n" \
  "s_waitcnt vmcnt(4)\n" \
  "v_smfmac_f32_16x16x64_bf16 v[96:99], v[76:79], v[24:31], v117\n" \
  "s_waitcnt vmcnt(3) lgkmcnt(6)\n" \
  "v_smfmac_f32_16x16x64_bf16 v[96:99], v[80:83], v[32:39], v117 abid:1\n" \
  "s_waitcnt vmcnt(2) lgkmcnt(4)\n" \
  "v_smfmac_f32_16x16x64_bf16 v[96:99], v[84:87], v[40:47], v117 abid:1\n" \
  "s_waitcnt vmcnt(1) lgkmcnt(2)\n" \
  "v_smfmac_f32_16x16x64_bf16 v[96:99], v[88:91], v[48:55], v117 abid:1\n" \
  "s_waitcnt vmcnt(0) lgkmcnt(0)\n" \
  "v_smfmac_f32_16x16x64_bf16 v[96:99], v[92:95], v[56:63], v117 abid:1\n" \
  "s_nop 8\n" \
  "v_permlane32_swap_b32 v96, v98\n" \
  "v_permlane32_swap_b32 v97, v99\n" \
  "v_add_f32 v96, v96, v98\n" \
  "v_add_f32 v97, v97, v99\n" \
  "v_cvt_pk_bf16_f32 v100, v96, v97\n" \
  "v_cmp_u_f32 vcc, v96, v97\n" \
  "s_nop 0\n" \
  "v_mov_b32_dpp v101, v100 quad_perm:[1,0,3,2] row_mask:0xf bank_mask:0xf\n" \
  "v_perm_b32 v100, v101, v100, v116\n" \
  "s_cbranch_vccnz .Lbad_%=\n" \
  "buffer_store_dword v100, v115, %[rs], %[sm0] offen sc1\n" \
  "buffer_store_dword v100, v115, %[rs], %[sm1] offen sc1\n" \
  "buffer_store_dword v100, v115, %[rs], %[sm2] offen sc1\n" \
  "buffer_store_dword v100, v115, %[rs], %[sm3] offen sc1\n" \
  ".Lend_%=:\n" \
  "s_memtime %[tp]\n" \
  "v_permlane16_swap_b32 v0, v1\n" \
  "v_permlane16_swap_b32 v2, v3\n" \
  "v_permlane16_swap_b32 v4, v5\n" \
  "v_permlane16_swap_b32 v6, v7\n" \
  "v_permlane16_swap_b32 v8, v9\n" \
  "v_permlane16_swap_b32 v10, v11\n" \
  "v_permlane16_swap_b32 v12, v13\n" \
  "v_permlane16_swap_b32 v14, v15\n" \
  "v_permlane16_swap_b32 v16, v17\n" \
  "v_permlane16_swap_b32 v18, v19\n" \
  "v_permlane16_swap_b32 v20, v21\n" \
  "v_permlane16_swap_b32 v22, v23\n" \
  "v_permlane16_swap_b32 v24, v25\n" \
  "v_permlane16_swap_b32 v26, v27\n" \
  "v_permlane16_swap_b32 v28, v29\n" \
  "v_permlane16_swap_b32 v30, v31\n" \
  "s_waitcnt lgkmcnt(0)\n"
#define ST_Y \
  "s_mov_b32 %[keep], m0\n" \
  "ds_read_b128 v[240:243], v250 offset:0\n" \
  "ds_read_b128 v[244:247], v250 offset:1024\n" \
  "v_mfma_f32_16x16x32_bf16 v[192:195], v[0:3], v[208:211], 0\n" \
  "v_mfma_f32_16x16x32_bf16 v[196:199], v[64:67], v[208:211], 0\n" \
  "ds_read_b128 v[208:211], v252 offset:8192\n" \
  "v_mfma_f32_16x16x32_bf16 v[200:203], v[0:3], v[212:215], 0\n" \
  "v_mfma_f32_16x16x32_bf16 v[204:207], v[64:67], v[212:215], 0\n" \
  "ds_read_b128 v[212:215], v252 offset:9216\n" \
  "v_mfma_f32_16x16x32_bf16 v[192:195], v[128:131], v[216:219], v[192:195]\n" \
  "v_mfma_f32_16x16x32_bf16 v[196:199], v[132:135], v[216:219], v[196:199]\n" \
  "ds_read_b128 v[216:219], v252 offset:10240\n" \
  "v_mfma_f32_16x16x32_bf16 v[200:203], v[128:131], v[220:223], v[200:203]\n" \
  "v_mfma_f32_16x16x32_bf16 v[204:207], v[132:135], v[220:223], v[204:207]\n" \
  "ds_read_b128 v[220:223], v252 offset:11264\n" \
  "v_mfma_f32_16x16x32_bf16 v[192:195], v[4:7], v[224:227], v[192:195]\n" \
  "v_mfma_f32_16x16x32_bf16 v[196:199], v[68:71], v[224:227], v[196:199]\n" \
  "ds_read_b128 v[224:227], v252 offset:12288\n" \
  "v_mfma_f32_16x16x32_bf16 v[200:203], v[4:7], v[228:231], v[200:203]\n" \
  "v_mfma_f32_16x16x32_bf16 v[204:207], v[68:71], v[228:231], v[204:207]\n" \
  "ds_read_b128 v[228:231], v252 offset:13312\n" \
  "s_waitcnt lgkmcnt(7)\n" \
  "v_mfma_f32_16x16x32_bf16 v[192:195], v[240:243], v[232:235], v[192:195]\n" \
  "s_waitcnt lgkmcnt(6)\n" \
  "v_mfma_f32_16x16x32_bf16 v[196:199], v[244:247], v[232:235], v[196:199]\n" \
  "ds_read_b128 v[232:235], v252 offset:14336\n" \
  "v_mfma_f32_16x16x32_bf16 v[200:203], v[240:243], v[236:239], v[200:203]\n" \
  "v_mfma_f32_16x16x32_bf16 v[204:207], v[244:247], v[236:239], v[204:207]\n" \
  "ds_read_b128 v[236:239], v252 offset:15360\n" \
  "ds_read_b128 v[240:243], v250 offset:16384\n" \
  "ds_read_b128 v[244:247], v250 offset:17408\n" \
  "s_waitcnt lgkmcnt(9)\n" \
  "v_mfma_f32_16x16x32_bf16 v[192:195], v[8:11], v[208:211], v[192:195]\n" \
  "v_mfma_f32_16x16x32_bf16 v[196:199], v[72:75], v[208:211], v[196:199]\n" \
  "s_waitcnt lgkmcnt(8)\n" \
  "v_mfma_f32_16x16x32_bf16 v[200:203], v[8:11], v[212:215], v[200:203]\n" \
  "v_mfma_f32_16x16x32_bf16 v[204:207], v[72:75], v[212:215], v[204:207]\n" \
  "s_waitcnt vmcnt(2)\n" \
  "s_waitcnt lgkmcnt(0)\n" \
  "s_barrier\n" \
  "ds_read_b128 v[208:211], v252 offset:16384\n" \
  "ds_read_b128 v[212:215], v252 offset:17408\n" \
  "s_add_u32 m0, %[mw], 0\n" \
  "s_xor_b32 %[cnt], %[sc], 512\n" \
  "buffer_load_dwordx4 v248, %[rx], %[cnt] offen lds\n" \
  "s_add_u32 m0, %[mw], 8192\n" \
  "s_xor_b32 %[cnt], %[sc], 640\n" \
  "buffer_load_dwordx4 v248, %[rx], %[cnt] offen lds\n" \
  "v_mfma_f32_16x16x32_bf16 v[192:195], v[136:139], v[216:219], v[192:195]\n" \
  "v_mfma_f32_16x16x32_bf16 v[196:199], v[140:143], v[216:219], v[196:199]\n" \
  "ds_read_b128 v[216:219], v252 offset:18432\n" \
  "v_mfma_f32_16x16x32_bf16 v[200:203], v[136:139], v[220:223], v[200:203]\n" \
  "v_mfma_f32_16x16x32_bf16 v[204:207], v[140:143], v[220:223], v[204:207]\n" \
  "ds_read_b128 v[220:223], v252 offset:19456\n" \
  "v_mfma_f32_16x16x32_bf16 v[192:195], v[12:15], v[224:227], v[192:195]\n" \
  "v_mfma_f32_16x16x32_bf16 v[196:199], v[76:79], v[224:227], v[196:199]\n" \
  "ds_read_b128 v[224:227], v252 offset:20480\n" \
  "v_mfma_f32_16x16x32_bf16 v[200:203], v[12:15], v[228:231], v[200:203]\n" \
  "v_mfma_f32_16x16x32_bf16 v[204:207], v[76:79], v[228:231], v[204:207]\n" \
  "ds_read_b128 v[228:231], v252 offset:21504\n" \
  "v_mfma_f32_16x16x32_bf16 v[192:195], v[240:243], v[232:235], v[192:195]\n" \
  "v_mfma_f32_16x16x32_bf16 v[196:199], v[244:247], v[232:235], v[196:199]\n" \
  "ds_read_b128 v[232:235], v252 offset:22528\n" \
  "v_mfma_f32_16x16x32_bf16 v[200:203], v[240:243], v[236:239], v[200:203]\n" \
  "v_mfma_f32_16x16x32_bf16 v[204:207], v[244:247], v[236:239], v[204:207]\n" \
  "ds_read_b128 v[236:239], v252 offset:23552\n" \
  "ds_read_b128 v[240:243], v250 offset:32768\n" \
  "ds_read_b128 v[244:247], v250 offset:33792\n" \
  "s_waitcnt lgkmcnt(9)\n" \
  "v_mfma_f32_16x16x32_bf16 v[192:195], v[16:19], v[208:211], v[192:195]\n" \
  "v_mfma_f32_16x16x32_bf16 v[196:199], v[80:83], v[208:211], v[196:199]\n" \
  "ds_read_b128 v[208:211], v252 offset:24576\n" \
  "s_waitcnt lgkmcnt(9)\n" \
  "v_mfma_f32_16x16x32_bf16 v[200:203], v[16:19], v[212:215], v[200:203]\n" \
  "v_mfma_f32_16x16x32_bf16 v[204:207], v[80:83], v[212:215], v[204:207]\n" \
  "ds_read_b128 v[212:215], v252 offset:25600\n" \
  "s_waitcnt lgkmcnt(9)\n" \
  "v_mfma_f32_16x16x32_bf16 v[192:195], v[144:147], v[216:219], v[192:195]\n" \
  "v_mfma_f32_16x16x32_bf16 v[196:199], v[148:151], v[216:219], v[196:199]\n" \
  "ds_read_b128 v[216:219], v252 offset:26624\n" \
  "s_waitcnt lgkmcnt(9)\n" \
  "v_mfma_f32_16x16x32_bf16 v[200:203], v[144:147], v[220:223], v[200:203]\n" \
  "v_mfma_f32_16x16x32_bf16 v[204:207], v[148:151], v[220:223], v[204:207]\n" \
  "ds_read_b128 v[220:223], v252 offset:27648\n" \
  "s_waitcnt lgkmcnt(9)\n" \
  "v_mfma_f32_16x16x32_bf16 v[192:195], v[20:23], v[224:227], v[192:195]\n" \
  "v_mfma_f32_16x16x32_bf16 v[196:199], v[84:87], v[224:227], v[196:199]\n" \
  "ds_read_b128 v[224:227], v252 offset:28672\n" \
  "s_waitcnt lgkmcnt(9)\n" \
  "v_mfma_f32_16x16x32_bf16 v[200:203], v[20:23], v[228:231], v[200:203]\n" \
  "v_mfma_f32_16x16x32_bf16 v[204:207], v[84:87], v[228:231], v[204:207]\n" \
  "ds_read_b128 v[228:231], v252 offset:29696\n" \
  "s_waitcnt lgkmcnt(7)\n" \
  "v_mfma_f32_16x16x32_bf16 v[192:195], v[240:243], v[232:235], v[192:195]\n" \
  "s_waitcnt lgkmcnt(6)\n" \
  "v_mfma_f32_16x16x32_bf16 v[196:199], v[244:247], v[232:235], v[196:199]\n" \
  "ds_read_b128 v[232:235], v252 offset:30720\n" \
  "v_mfma_f32_16x16x32_bf16 v[200:203], v[240:243], v[236:239], v[200:203]\n" \
  "v_mfma_f32_16x16x32_bf16 v[204:207], v[244:247], v[236:239], v[204:207]\n" \
  "ds_read_b128 v[236:239], v252 offset:31744\n" \
  "ds_read_b128 v[240:243], v250 offset:49152\n" \
  "ds_read_b128 v[244:247], v250 offset:50176\n" \
  "s_waitcnt lgkmcnt(9)\n" \
  "v_mfma_f32_16x16x32_bf16 v[192:195], v[24:27], v[208:211], v[192:195]\n" \
  "v_mfma_f32_16x16x32_bf16 v[196:199], v[88:91], v[208:211], v[196:199]\n" \
  "s_waitcnt lgkmcnt(8)\n" \
  "v_mfma_f32_16x16x32_bf16 v[200:203], v[24:27], v[212:215], v[200:203]\n" \
  "v_mfma_f32_16x16x32_bf16 v[204:207], v[88:91], v[212:215], v[204:207]\n" \
  "s_waitcnt vmcnt(0)\n" \
  "s_waitcnt lgkmcnt(0)\n" \
  "s_barrier\n" \
  "ds_read_b128 v[208:211], v252 offset:0\n" \
  "ds_read_b128 v[212:215], v252 offset:1024\n" \
  "s_add_u32 m0, %[mw], 16384\n" \
  "s_xor_b32 %[cnt], %[sc], 768\n" \
  "buffer_load_dwordx4 v248, %[rx], %[cnt] offen lds\n" \
  "s_add_u32 m0, %[mw], 24576\n" \
  "s_xor_b32 %[cnt], %[sc], 896\n" \
  "buffer_load_dwordx4 v248, %[rx], %[cnt] offen lds\n" \
  "v_lshl_or_b32 v253, v251, 24, %[fm]\n" \
  "buffer_store_dword v253, v253, %[rs], %[fo] offen sc1\n" \
  "v_mfma_f32_16x16x32_bf16 v[192:195], v[152:155], v[216:219], v[192:195]\n" \
  "v_mfma_f32_16x16x32_bf16 v[196:199], v[156:159], v[216:219], v[196:199]\n" \
  "ds_read_b128 v[216:219], v252 offset:2048\n" \
  "v_mfma_f32_16x16x32_bf16 v[200:203], v[152:155], v[220:223], v[200:203]\n" \
  "v_mfma_f32_16x16x32_bf16 v[204:207], v[156:159], v[220:223], v[204:207]\n" \
  "ds_read_b128 v[220:223], v252 offset:3072\n" \
  "v_mfma_f32_16x16x32_bf16 v[192:195], v[28:31], v[224:227], v[192:195]\n" \
  "v_mfma_f32_16x16x32_bf16 v[196:199], v[92:95], v[224:227], v[196:199]\n" \
  "ds_read_b128 v[224:227], v252 offset:4096\n" \
  "v_mfma_f32_16x16x32_bf16 v[200:203], v[28:31], v[228:231], v[200:203]\n" \
  "v_mfma_f32_16x16x32_bf16 v[204:207], v[92:95], v[228:231], v[204:207]\n" \
  "ds_read_b128 v[228:231], v252 offset:5120\n" \
  "v_mfma_f32_16x16x32_bf16 v[192:195], v[240:243], v[232:235], v[192:195]\n" \
  "v_mfma_f32_16x16x32_bf16 v[196:199], v[244:247], v[232:235], v[196:199]\n" \
  "ds_read_b128 v[232:235], v252 offset:6144\n" \
  "v_mfma_f32_16x16x32_bf16 v[200:203], v[240:243], v[236:239], v[200:203]\n" \
  "v_mfma_f32_16x16x32_bf16 v[204:207], v[244:247], v[236:239], v[204:207]\n" \
  "ds_read_b128 v[236:239], v252 offset:7168\n" \
  "ds_read_b128 v[240:243], v249 offset:0\n" \
  "ds_read_b128 v[244:247], v249 offset:1024\n" \
  "s_waitcnt lgkmcnt(9)\n" \
  "v_mfma_f32_16x16x32_bf16 v[192:195], v[32:35], v[208:211], v[192:195]\n" \
  "v_mfma_f32_16x16x32_bf16 v[196:199], v[96:99], v[208:211], v[196:199]\n" \
  "ds_read_b128 v[208:211], v252 offset:8192\n" \
  "s_waitcnt lgkmcnt(9)\n" \
  "v_mfma_f32_16x16x32_bf16 v[200:203], v[32:35], v[212:215], v[200:203]\n" \
  "v_mfma_f32_16x16x32_bf16 v[204:207], v[96:99], v[212:215], v[204:207]\n" \
  "ds_read_b128 v[212:215], v252 offset:9216\n" \
  "s_waitcnt lgkmcnt(9)\n" \
  "v_mfma_f32_16x16x32_bf16 v[192:195], v[160:163], v[216:219], v[192:195]\n" \
  "v_mfma_f32_16x16x32_bf16 v[196:199], v[164:167], v[216:219], v[196:199]\n" \
  "ds_read_b128 v[216:219], v252 offset:10240\n" \
  "s_waitcnt lgkmcnt(9)\n" \
  "v_mfma_f32_16x16x32_bf16 v[200:203], v[160:163], v[220:223], v[200:203]\n" \
  "v_mfma_f32_16x16x32_bf16 v[204:207], v[164:167], v[220:223], v[204:207]\n" \
  "ds_read_b128 v[220:223], v252 offset:11264\n" \
  "s_waitcnt lgkmcnt(9)\n" \
  "v_mfma_f32_16x16x32_bf16 v[192:195], v[36:39], v[224:227], v[192:195]\n" \
  "v_mfma_f32_16x16x32_bf16 v[196:199], v[100:103], v[224:227], v[196:199]\n" \
  "ds_read_b128 v[224:227], v252 offset:12288\n" \
  "s_waitcnt lgkmcnt(9)\n" \
  "v_mfma_f32_16x16x32_bf16 v[200:203], v[36:39], v[228:231], v[200:203]\n" \
  "v_mfma_f32_16x16x32_bf16 v[204:207], v[100:103], v[228:231], v[204:207]\n" \
  "ds_read_b128 v[228:231], v252 offset:13312\n" \
  "s_waitcnt lgkmcnt(7)\n" \
  "v_mfma_f32_16x16x32_bf16 v[192:195], v[240:243], v[232:235], v[192:195]\n" \
  "s_waitcnt lgkmcnt(6)\n" \
  "v_mfma_f32_16x16x32_bf16 v[196:199], v[244:247], v[232:235], v[196:199]\n" \
  "ds_read_b128 v[232:235], v252 offset:14336\n" \
  "v_mfma_f32_16x16x32_bf16 v[200:203], v[240:243], v[236:239], v[200:203]\n" \
  "v_mfma_f32_16x16x32_bf16 v[204:207], v[244:247], v[236:239], v[204:207]\n" \
  "ds_read_b128 v[236:239], v252 offset:15360\n" \
  "ds_read_b128 v[240:243], v249 offset:16384\n" \
  "ds_read_b128 v[244:247], v249 offset:17408\n" \
  "s_waitcnt lgkmcnt(9)\n" \
  "v_mfma_f32_16x16x32_bf16 v[192:195], v[40:43], v[208:211], v[192:195]\n" \
  "v_mfma_f32_16x16x32_bf16 v[196:199], v[104:107], v[208:211], v[196:199]\n" \
  "s_waitcnt lgkmcnt(8)\n" \
  "v_mfma_f32_16x16x32_bf16 v[200:203], v[40:43], v[212:215], v[200:203]\n" \
  "v_mfma_f32_16x16x32_bf16 v[204:207], v[104:107], v[212:215], v[204:207]\n" \
  "s_waitcnt vmcnt(1)\n" \
  "s_waitcnt lgkmcnt(0)\n" \
  "s_barrier\n" \
  "ds_read_b128 v[208:211], v252 offset:16384\n" \
  "ds_read_b128 v[212:215], v252 offset:17408\n" \
  "s_add_u32 m0, %[mw], 0\n" \
  "s_xor_b32 %[cnt], %[sn], 0\n" \
  "buffer_load_dwordx4 v248, %[rx], %[cnt] offen lds\n" \
  "s_add_u32 m0, %[mw], 8192\n" \
  "s_xor_b32 %[cnt], %[sn], 128\n" \
  "buffer_load_dwordx4 v248, %[rx], %[cnt] offen lds\n" \
  "v_lshl_or_b32 v253, v251, 3, %[tm]\n" \
  "buffer_load_dword v253, v253, %[rx], %[tc] offen\n" \
  "v_mfma_f32_16x16x32_bf16 v[192:195], v[168:171], v[216:219], v[192:195]\n" \
  "v_mfma_f32_16x16x32_bf16 v[196:199], v[172:175], v[216:219], v[196:199]\n" \
  "ds_read_b128 v[216:219], v252 offset:18432\n" \
  "v_mfma_f32_16x16x32_bf16 v[200:203], v[168:171], v[220:223], v[200:203]\n" \
  "v_mfma_f32_16x16x32_bf16 v[204:207], v[172:175], v[220:223], v[204:207]\n" \
  "ds_read_b128 v[220:223], v252 offset:19456\n" \
  "v_mfma_f32_16x16x32_bf16 v[192:195], v[44:47], v[224:227], v[192:195]\n" \
  "v_mfma_f32_16x16x32_bf16 v[196:199], v[108:111], v[224:227], v[196:199]\n" \
  "ds_read_b128 v[224:227], v252 offset:20480\n" \
  "v_mfma_f32_16x16x32_bf16 v[200:203], v[44:47], v[228:231], v[200:203]\n" \
  "v_mfma_f32_16x16x32_bf16 v[204:207], v[108:111], v[228:231], v[204:207]\n" \
  "ds_read_b128 v[228:231], v252 offset:21504\n" \
  "v_mfma_f32_16x16x32_bf16 v[192:195], v[240:243], v[232:235], v[192:195]\n" \
  "v_mfma_f32_16x16x32_bf16 v[196:199], v[244:247], v[232:235], v[196:199]\n" \
  "ds_read_b128 v[232:235], v252 offset:22528\n" \
  "v_mfma_f32_16x16x32_bf16 v[200:203], v[240:243], v[236:239], v[200:203]\n" \
  "v_mfma_f32_16x16x32_bf16 v[204:207], v[244:247], v[236:239], v[204:207]\n" \
  "ds_read_b128 v[236:239], v252 offset:23552\n" \
  "ds_read_b128 v[240:243], v249 offset:32768\n" \
  "ds_read_b128 v[244:247], v249 offset:33792\n" \
  "s_waitcnt lgkmcnt(9)\n" \
  "v_mfma_f32_16x16x32_bf16 v[192:195], v[48:51], v[208:211], v[192:195]\n" \
  "v_mfma_f32_16x16x32_bf16 v[196:199], v[112:115], v[208:211], v[196:199]\n" \
  "ds_read_b128 v[208:211], v252 offset:24576\n" \
  "s_waitcnt lgkmcnt(9)\n" \
  "v_mfma_f32_16x16x32_bf16 v[200:203], v[48:51], v[212:215], v[200:203]\n" \
  "v_mfma_f32_16x16x32_bf16 v[204:207], v[112:115], v[212:215], v[204:207]\n" \
  "ds_read_b128 v[212:215], v252 offset:25600\n" \
  "s_waitcnt lgkmcnt(9)\n" \
  "v_mfma_f32_16x16x32_bf16 v[192:195], v[176:179], v[216:219], v[192:195]\n" \
  "v_mfma_f32_16x16x32_bf16 v[196:199], v[180:183], v[216:219], v[196:199]\n" \
  "ds_read_b128 v[216:219], v252 offset:26624\n" \
  "s_waitcnt lgkmcnt(9)\n" \
  "v_mfma_f32_16x16x32_bf16 v[200:203], v[176:179], v[220:223], v[200:203]\n" \
  "v_mfma_f32_16x16x32_bf16 v[204:207], v[180:183], v[220:223], v[204:207]\n" \
  "ds_read_b128 v[220:223], v252 offset:27648\n" \
  "s_waitcnt lgkmcnt(9)\n" \
  "v_mfma_f32_16x16x32_bf16 v[192:195], v[52:55], v[224:227], v[192:195]\n" \
  "v_mfma_f32_16x16x32_bf16 v[196:199], v[116:119], v[224:227], v[196:199]\n" \
  "ds_read_b128 v[224:227], v252 offset:28672\n" \
  "s_waitcnt lgkmcnt(9)\n" \
  "v_mfma_f32_16x16x32_bf16 v[200:203], v[52:55], v[228:231], v[200:203]\n" \
  "v_mfma_f32_16x16x32_bf16 v[204:207], v[116:119], v[228:231], v[204:207]\n" \
  "ds_read_b128 v[228:231], v252 offset:29696\n" \
  "s_waitcnt lgkmcnt(7)\n" \
  "v_mfma_f32_16x16x32_bf16 v[192:195], v[240:243], v[232:235], v[192:195]\n" \
  "s_waitcnt lgkmcnt(6)\n" \
  "v_mfma_f32_16x16x32_bf16 v[196:199], v[244:247], v[232:235], v[196:199]\n" \
  "ds_read_b128 v[232:235], v252 offset:30720\n" \
  "v_mfma_f32_16x16x32_bf16 v[200:203], v[240:243], v[236:239], v[200:203]\n" \
  "v_mfma_f32_16x16x32_bf16 v[204:207], v[244:247], v[236:239], v[204:207]\n" \
  "ds_read_b128 v[236:239], v252 offset:31744\n" \
  "ds_read_b128 v[240:243], v249 offset:49152\n" \
  "ds_read_b128 v[244:247], v249 offset:50176\n" \
  "s_waitcnt lgkmcnt(9)\n" \
  "v_mfma_f32_16x16x32_bf16 v[192:195], v[56:59], v[208:211], v[192:195]\n" \
  "v_mfma_f32_16x16x32_bf16 v[196:199], v[120:123], v[208:211], v[196:199]\n" \
  "s_waitcnt lgkmcnt(8)\n" \
  "v_mfma_f32_16x16x32_bf16 v[200:203], v[56:59], v[212:215], v[200:203]\n" \
  "v_mfma_f32_16x16x32_bf16 v[204:207], v[120:123], v[212:215], v[204:207]\n" \
  "s_waitcnt vmcnt(1)\n" \
  "s_waitcnt lgkmcnt(0)\n" \
  "s_barrier\n" \
  "ds_read_b128 v[208:211], v252 offset:0\n" \
  "ds_read_b128 v[212:215], v252 offset:1024\n" \
  "s_add_u32 m0, %[mw], 16384\n" \
  "s_xor_b32 %[cnt], %[sn], 256\n" \
  "buffer_load_dwordx4 v248, %[rx], %[cnt] offen lds\n" \
  "s_add_u32 m0, %[mw], 24576\n" \
  "s_xor_b32 %[cnt], %[sn], 384\n" \
  "buffer_load_dwordx4 v248, %[rx], %[cnt] offen lds\n" \
  "v_mfma_f32_16x16x32_bf16 v[192:195], v[184:187], v[216:219], v[192:195]\n" \
  "v_mfma_f32_16x16x32_bf16 v[196:199], v[188:191], v[216:219], v[196:199]\n" \
  "ds_read_b128 v[216:219], v252 offset:2048\n" \
  "v_mfma_f32_16x16x32_bf16 v[200:203], v[184:187], v[220:223], v[200:203]\n" \
  "v_mfma_f32_16x16x32_bf16 v[204:207], v[188:191], v[220:223], v[204:207]\n" \
  "ds_read_b128 v[220:223], v252 offset:3072\n" \
  "v_mfma_f32_16x16x32_bf16 v[192:195], v[60:63], v[224:227], v[192:195]\n" \
  "v_mfma_f32_16x16x32_bf16 v[196:199], v[124:127], v[224:227], v[196:199]\n" \
  "ds_read_b128 v[224:227], v252 offset:4096\n" \
  "v_mfma_f32_16x16x32_bf16 v[200:203], v[60:63], v[228:231], v[200:203]\n" \
  "v_mfma_f32_16x16x32_bf16 v[204:207], v[124:127], v[228:231], v[204:207]\n" \
  "ds_read_b128 v[228:231], v252 offset:5120\n" \
  "v_mfma_f32_16x16x32_bf16 v[192:195], v[240:243], v[232:235], v[192:195]\n" \
  "v_mfma_f32_16x16x32_bf16 v[196:199], v[244:247], v[232:235], v[196:199]\n" \
  "ds_read_b128 v[232:235], v252 offset:6144\n" \
  "v_mfma_f32_16x16x32_bf16 v[200:203], v[240:243], v[236:239], v[200:203]\n" \
  "v_mfma_f32_16x16x32_bf16 v[204:207], v[244:247], v[236:239], v[204:207]\n" \
  "ds_read_b128 v[236:239], v252 offset:7168\n" \
  "s_cmp_eq_u32 %[cs], 0\n" \
  "s_cbranch_scc1 .Lq_%=\n" \
  "v_mul_f32 v192, %[cs], v192\n" \
  "v_mul_f32 v193, %[cs], v193\n" \
  "v_mul_f32 v194, %[cs], v194\n" \
  "v_mul_f32 v195, %[cs], v195\n" \
  "v_mul_f32 v196, %[cs], v196\n" \
  "v_mul_f32 v197, %[cs], v197\n" \
  "v_mul_f32 v198, %[cs], v198\n" \
  "v_mul_f32 v199, %[cs], v199\n" \
  "v_cvt_pk_bf16_f32 v192, v192, v193\n" \
  "v_cvt_pk_bf16_f32 v193, v196, v197\n" \
  "v_cvt_pk_bf16_f32 v194, v194, v195\n" \
  "v_cvt_pk_bf16_f32 v195, v198, v199\n" \
  "buffer_store_dwordx4 v[192:195], v251, %[rs], %[so] offen sc1\n" \
  "v_mul_f32 v200, %[cs], v200\n" \
  "v_mul_f32 v201, %[cs], v201\n" \
  "v_mul_f32 v202, %[cs], v202\n" \
  "v_mul_f32 v203, %[cs], v203\n" \
  "v_mul_f32 v204, %[cs], v204\n" \
  "v_mul_f32 v205, %[cs], v205\n" \
  "v_mul_f32 v206, %[cs], v206\n" \
  "v_mul_f32 v207, %[cs], v207\n" \
  "v_cvt_pk_bf16_f32 v200, v200, v201\n" \
  "v_cvt_pk_bf16_f32 v201, v204, v205\n" \
  "v_cvt_pk_bf16_f32 v202, v202, v203\n" \
  "v_cvt_pk_bf16_f32 v203, v206, v207\n" \
  "s_add_u32 %[cnt], %[so], %c[rt]\n" \
  "buffer_store_dwordx4 v[200:203], v251, %[rs], %[cnt] offen sc1\n" \
  "s_branch .Lqe_%=\n" \
  ".Lq_%=:\n" \
  "v_cvt_pk_bf16_f32 v192, v192, v193\n" \
  "v_cvt_pk_bf16_f32 v193, v196, v197\n" \
  "v_cvt_pk_bf16_f32 v194, v194, v195\n" \
  "v_cvt_pk_bf16_f32 v195, v198, v199\n" \
  "buffer_store_dwordx4 v[192:195], v251, %[rs], %[so] offen sc1\n" \
  "v_cvt_pk_bf16_f32 v200, v200, v201\n" \
  "v_cvt_pk_bf16_f32 v201, v204, v205\n" \
  "v_cvt_pk_bf16_f32 v202, v202, v203\n" \
  "v_cvt_pk_bf16_f32 v203, v206, v207\n" \
  "s_add_u32 %[cnt], %[so], %c[rt]\n" \
  "buffer_store_dwordx4 v[200:203], v251, %[rs], %[cnt] offen sc1\n" \
  ".Lqe_%=:\n" \
  "s_mov_b32 m0, %[keep]\n" \
  "s_waitcnt lgkmcnt(0)\n"
#define ST_XA \
  "s_mov_b32 %[keep], m0\n" \
  "ds_read_b128 v[240:243], v250 offset:0\n" \
  "ds_read_b128 v[244:247], v250 offset:1024\n" \
  "v_mfma_f32_16x16x32_bf16 v[192:195], v[0:3], v[208:211], 0\n" \
  "v_mfma_f32_16x16x32_bf16 v[196:199], v[64:67], v[208:211], 0\n" \
  "ds_read_b128 v[208:211], v252 offset:8192\n" \
  "v_mfma_f32_16x16x32_bf16 v[200:203], v[0:3], v[212:215], 0\n" \
  "v_mfma_f32_16x16x32_bf16 v[204:207], v[64:67], v[212:215], 0\n" \
  "ds_read_b128 v[212:215], v252 offset:9216\n" \
  "v_mfma_f32_16x16x32_bf16 v[192:195], v[128:131], v[216:219], v[192:195]\n" \
  "v_mfma_f32_16x16x32_bf16 v[196:199], v[132:135], v[216:219], v[196:199]\n" \
  "ds_read_b128 v[216:219], v252 offset:10240\n" \
  "v_mfma_f32_16x16x32_bf16 v[200:203], v[128:131], v[220:223], v[200:203]\n" \
  "v_mfma_f32_16x16x32_bf16 v[204:207], v[132:135], v[220:223], v[204:207]\n" \
  "ds_read_b128 v[220:223], v252 offset:11264\n" \
  "v_mfma_f32_16x16x32_bf16 v[192:195], v[4:7], v[224:227], v[192:195]\n" \
  "v_mfma_f32_16x16x32_bf16 v[196:199], v[68:71], v[224:227], v[196:199]\n" \
  "v_mfma_f32_16x16x32_bf16 v[200:203], v[4:7], v[228:231], v[200:203]\n" \
  "v_mfma_f32_16x16x32_bf16 v[204:207], v[68:71], v[228:231], v[204:207]\n" \
  "s_waitcnt vmcnt(0)\n" \
  "s_waitcnt lgkmcnt(0)\n" \
  "s_barrier\n" \
  "ds_read_b128 v[224:227], v252 offset:12288\n" \
  "ds_read_b128 v[228:231], v252 offset:13312\n" \
  "s_add_u32 m0, %[mw], 0\n" \
  "s_xor_b32 %[cnt], %[sc], 384\n" \
  "buffer_load_dwordx4 v248, %[rx], %[cnt] offen lds\n" \
  "s_add_u32 m0, %[mw], 4096\n" \
  "s_xor_b32 %[cnt], %[sc], 448\n" \
  "buffer_load_dwordx4 v248, %[rx], %[cnt] offen lds\n" \
  "v_mfma_f32_16x16x32_bf16 v[192:195], v[240:243], v[232:235], v[192:195]\n" \
  "v_mfma_f32_16x16x32_bf16 v[196:199], v[244:247], v[232:235], v[196:199]\n" \
  "ds_read_b128 v[232:235], v252 offset:14336\n" \
  "v_mfma_f32_16x16x32_bf16 v[200:203], v[240:243], v[236:239], v[200:203]\n" \
  "v_mfma_f32_16x16x32_bf16 v[204:207], v[244:247], v[236:239], v[204:207]\n" \
  "ds_read_b128 v[236:239], v252 offset:15360\n" \
  "ds_read_b128 v[240:243], v250 offset:16384\n" \
  "ds_read_b128 v[244:247], v250 offset:17408\n" \
  "v_mfma_f32_16x16x32_bf16 v[192:195], v[8:11], v[208:211], v[192:195]\n" \
  "v_mfma_f32_16x16x32_bf16 v[196:199], v[72:75], v[208:211], v[196:199]\n" \
  "ds_read_b128 v[208:211], v252 offset:16384\n" \
  "v_mfma_f32_16x16x32_bf16 v[200:203], v[8:11], v[212:215], v[200:203]\n" \
  "v_mfma_f32_16x16x32_bf16 v[204:207], v[72:75], v[212:215], v[204:207]\n" \
  "ds_read_b128 v[212:215], v252 offset:17408\n" \
  "v_mfma_f32_16x16x32_bf16 v[192:195], v[136:139], v[216:219], v[192:195]\n" \
  "v_mfma_f32_16x16x32_bf16 v[196:199], v[140:143], v[216:219], v[196:199]\n" \
  "ds_read_b128 v[216:219], v252 offset:18432\n" \
  "v_mfma_f32_16x16x32_bf16 v[200:203], v[136:139], v[220:223], v[200:203]\n" \
  "v_mfma_f32_16x16x32_bf16 v[204:207], v[140:143], v[220:223], v[204:207]\n" \
  "ds_read_b128 v[220:223], v252 offset:19456\n" \
  "s_waitcnt lgkmcnt(9)\n" \
  "v_mfma_f32_16x16x32_bf16 v[192:195], v[12:15], v[224:227], v[192:195]\n" \
  "v_mfma_f32_16x16x32_bf16 v[196:199], v[76:79], v[224:227], v[196:199]\n" \
  "ds_read_b128 v[224:227], v252 offset:20480\n" \
  "s_waitcnt lgkmcnt(9)\n" \
  "v_mfma_f32_16x16x32_bf16 v[200:203], v[12:15], v[228:231], v[200:203]\n" \
  "v_mfma_f32_16x16x32_bf16 v[204:207], v[76:79], v[228:231], v[204:207]\n" \
  "ds_read_b128 v[228:231], v252 offset:21504\n" \
  "s_waitcnt lgkmcnt(7)\n" \
  "v_mfma_f32_16x16x32_bf16 v[192:195], v[240:243], v[232:235], v[192:195]\n" \
  "s_waitcnt lgkmcnt(6)\n" \
  "v_mfma_f32_16x16x32_bf16 v[196:199], v[244:247], v[232:235], v[196:199]\n" \
  "ds_read_b128 v[232:235], v252 offset:22528\n" \
  "v_mfma_f32_16x16x32_bf16 v[200:203], v[240:243], v[236:239], v[200:203]\n" \
  "v_mfma_f32_16x16x32_bf16 v[204:207], v[244:247], v[236:239], v[204:207]\n" \
  "ds_read_b128 v[236:239], v252 offset:23552\n" \
  "ds_read_b128 v[240:243], v250 offset:32768\n" \
  "ds_read_b128 v[244:247], v250 offset:33792\n" \
  "s_waitcnt lgkmcnt(9)\n" \
  "v_mfma_f32_16x16x32_bf16 v[192:195], v[16:19], v[208:211], v[192:195]\n" \
  "v_mfma_f32_16x16x32_bf16 v[196:199], v[80:83], v[208:211], v[196:199]\n" \
  "s_waitcnt lgkmcnt(8)\n" \
  "v_mfma_f32_16x16x32_bf16 v[200:203], v[16:19], v[212:215], v[200:203]\n" \
  "v_mfma_f32_16x16x32_bf16 v[204:207], v[80:83], v[212:215], v[204:207]\n" \
  "s_waitcnt vmcnt(0)\n" \
  "s_waitcnt lgkmcnt(0)\n" \
  "s_barrier\n" \
  "ds_read_b128 v[208:211], v252 offset:0\n" \
  "ds_read_b128 v[212:215], v252 offset:1024\n" \
  "s_add_u32 m0, %[mw], 12288\n" \
  "s_xor_b32 %[cnt], %[sc], 512\n" \
  "buffer_load_dwordx4 v248, %[rx], %[cnt] offen lds\n" \
  "s_add_u32 m0, %[mw], 16384\n" \
  "s_xor_b32 %[cnt], %[sc], 576\n" \
  "buffer_load_dwordx4 v248, %[rx], %[cnt] offen lds\n" \
  "s_add_u32 m0, %[mw], 20480\n" \
  "s_xor_b32 %[cnt], %[sc], 640\n" \
  "buffer_load_dwordx4 v248, %[rx], %[cnt] offen lds\n" \
  "v_lshl_or_b32 v253, v251, 24, %[fm]\n" \
  "buffer_store_dword v253, v253, %[rs], %[fo] offen sc1\n" \
  "v_mfma_f32_16x16x32_bf16 v[192:195], v[144:147], v[216:219], v[192:195]\n" \
  "v_mfma_f32_16x16x32_bf16 v[196:199], v[148:151], v[216:219], v[196:199]\n" \
  "ds_read_b128 v[216:219], v252 offset:2048\n" \
  "v_mfma_f32_16x16x32_bf16 v[200:203], v[144:147], v[220:223], v[200:203]\n" \
  "v_mfma_f32_16x16x32_bf16 v[204:207], v[148:151], v[220:223], v[204:207]\n" \
  "ds_read_b128 v[220:223], v252 offset:3072\n" \
  "v_mfma_f32_16x16x32_bf16 v[192:195], v[20:23], v[224:227], v[192:195]\n" \
  "v_mfma_f32_16x16x32_bf16 v[196:199], v[84:87], v[224:227], v[196:199]\n" \
  "ds_read_b128 v[224:227], v252 offset:4096\n" \
  "v_mfma_f32_16x16x32_bf16 v[200:203], v[20:23], v[228:231], v[200:203]\n" \
  "v_mfma_f32_16x16x32_bf16 v[204:207], v[84:87], v[228:231], v[204:207]\n" \
  "ds_read_b128 v[228:231], v252 offset:5120\n" \
  "v_mfma_f32_16x16x32_bf16 v[192:195], v[240:243], v[232:235], v[192:195]\n" \
  "v_mfma_f32_16x16x32_bf16 v[196:199], v[244:247], v[232:235], v[196:199]\n" \
  "ds_read_b128 v[232:235], v252 offset:6144\n" \
  "v_mfma_f32_16x16x32_bf16 v[200:203], v[240:243], v[236:239], v[200:203]\n" \
  "v_mfma_f32_16x16x32_bf16 v[204:207], v[244:247], v[236:239], v[204:207]\n" \
  "ds_read_b128 v[236:239], v252 offset:7168\n" \
  "ds_read_b128 v[240:243], v250 offset:49152\n" \
  "ds_read_b128 v[244:247], v250 offset:50176\n" \
  "s_waitcnt lgkmcnt(9)\n" \
  "v_mfma_f32_16x16x32_bf16 v[192:195], v[24:27], v[208:211], v[192:195]\n" \
  "v_mfma_f32_16x16x32_bf16 v[196:199], v[88:91], v[208:211], v[196:199]\n" \
  "s_waitcnt lgkmcnt(8)\n" \
  "v_mfma_f32_16x16x32_bf16 v[200:203], v[24:27], v[212:215], v[200:203]\n" \
  "v_mfma_f32_16x16x32_bf16 v[204:207], v[88:91], v[212:215], v[204:207]\n" \
  "s_waitcnt vmcnt(1)\n" \
  "s_waitcnt lgkmcnt(0)\n" \
  "s_barrier\n" \
  "ds_read_b128 v[208:211], v252 offset:12288\n" \
  "ds_read_b128 v[212:215], v252 offset:13312\n" \
  "s_add_u32 m0, %[mw], 0\n" \
  "s_xor_b32 %[cnt], %[sc], 704\n" \
  "buffer_load_dwordx4 v248, %[rx], %[cnt] offen lds\n" \
  "s_add_u32 m0, %[mw], 4096\n" \
  "s_xor_b32 %[cnt], %[sc], 768\n" \
  "buffer_load_dwordx4 v248, %[rx], %[cnt] offen lds\n" \
  "s_add_u32 m0, %[mw], 8192\n" \
  "s_xor_b32 %[cnt], %[sc], 832\n" \
  "buffer_load_dwordx4 v248, %[rx], %[cnt] offen lds\n" \
  "v_lshl_or_b32 v253, v251, 3, %[tm]\n" \
  "buffer_load_dword v253, v253, %[rx], %[tc] offen\n" \
  "v_mfma_f32_16x16x32_bf16 v[192:195], v[152:155], v[216:219], v[192:195]\n" \
  "v_mfma_f32_16x16x32_bf16 v[196:199], v[156:159], v[216:219], v[196:199]\n" \
  "ds_read_b128 v[216:219], v252 offset:14336\n" \
  "v_mfma_f32_16x16x32_bf16 v[200:203], v[152:155], v[220:223], v[200:203]\n" \
  "v_mfma_f32_16x16x32_bf16 v[204:207], v[156:159], v[220:223], v[204:207]\n" \
  "ds_read_b128 v[220:223], v252 offset:15360\n" \
  "v_mfma_f32_16x16x32_bf16 v[192:195], v[28:31], v[224:227], v[192:195]\n" \
  "v_mfma_f32_16x16x32_bf16 v[196:199], v[92:95], v[224:227], v[196:199]\n" \
  "ds_read_b128 v[224:227], v252 offset:16384\n" \
  "v_mfma_f32_16x16x32_bf16 v[200:203], v[28:31], v[228:231], v[200:203]\n" \
  "v_mfma_f32_16x16x32_bf16 v[204:207], v[92:95], v[228:231], v[204:207]\n" \
  "ds_read_b128 v[228:231], v252 offset:17408\n" \
  "v_mfma_f32_16x16x32_bf16 v[192:195], v[240:243], v[232:235], v[192:195]\n" \
  "v_mfma_f32_16x16x32_bf16 v[196:199], v[244:247], v[232:235], v[196:199]\n" \
  "ds_read_b128 v[232:235], v252 offset:18432\n" \
  "v_mfma_f32_16x16x32_bf16 v[200:203], v[240:243], v[236:239], v[200:203]\n" \
  "v_mfma_f32_16x16x32_bf16 v[204:207], v[244:247], v[236:239], v[204:207]\n" \
  "ds_read_b128 v[236:239], v252 offset:19456\n" \
  "ds_read_b128 v[240:243], v249 offset:0\n" \
  "ds_read_b128 v[244:247], v249 offset:1024\n" \
  "s_waitcnt lgkmcnt(9)\n" \
  "v_mfma_f32_16x16x32_bf16 v[192:195], v[32:35], v[208:211], v[192:195]\n" \
  "v_mfma_f32_16x16x32_bf16 v[196:199], v[96:99], v[208:211], v[196:199]\n" \
  "ds_read_b128 v[208:211], v252 offset:20480\n" \
  "s_waitcnt lgkmcnt(9)\n" \
  "v_mfma_f32_16x16x32_bf16 v[200:203], v[32:35], v[212:215], v[200:203]\n" \
  "v_mfma_f32_16x16x32_bf16 v[204:207], v[96:99], v[212:215], v[204:207]\n" \
  "ds_read_b128 v[212:215], v252 offset:21504\n" \
  "s_waitcnt lgkmcnt(9)\n" \
  "v_mfma_f32_16x16x32_bf16 v[192:195], v[160:163], v[216:219], v[192:195]\n" \
  "v_mfma_f32_16x16x32_bf16 v[196:199], v[164:167], v[216:219], v[196:199]\n" \
  "ds_read_b128 v[216:219], v252 offset:22528\n" \
  "s_waitcnt lgkmcnt(9)\n" \
  "v_mfma_f32_16x16x32_bf16 v[200:203], v[160:163], v[220:223], v[200:203]\n" \
  "v_mfma_f32_16x16x32_bf16 v[204:207], v[164:167], v[220:223], v[204:207]\n" \
  "ds_read_b128 v[220:223], v252 offset:23552\n" \
  "s_waitcnt lgkmcnt(9)\n" \
  "v_mfma_f32_16x16x32_bf16 v[192:195], v[36:39], v[224:227], v[192:195]\n" \
  "v_mfma_f32_16x16x32_bf16 v[196:199], v[100:103], v[224:227], v[196:199]\n" \
  "s_waitcnt lgkmcnt(8)\n" \
  "v_mfma_f32_16x16x32_bf16 v[200:203], v[36:39], v[228:231], v[200:203]\n" \
  "v_mfma_f32_16x16x32_bf16 v[204:207], v[100:103], v[228:231], v[204:207]\n" \
  "s_waitcnt vmcnt(1)\n" \
  "s_waitcnt lgkmcnt(0)\n" \
  "s_barrier\n" \
  "ds_read_b128 v[224:227], v252 offset:0\n" \
  "ds_read_b128 v[228:231], v252 offset:1024\n" \
  "s_add_u32 m0, %[mw], 12288\n" \
  "s_xor_b32 %[cnt], %[sc], 896\n" \
  "buffer_load_dwordx4 v248, %[rx], %[cnt] offen lds\n" \
  "s_add_u32 m0, %[mw], 16384\n" \
  "s_xor_b32 %[cnt], %[sc], 960\n" \
  "buffer_load_dwordx4 v248, %[rx], %[cnt] offen lds\n" \
  "v_mfma_f32_16x16x32_bf16 v[192:195], v[240:243], v[232:235], v[192:195]\n" \
  "v_mfma_f32_16x16x32_bf16 v[196:199], v[244:247], v[232:235], v[196:199]\n" \
  "ds_read_b128 v[232:235], v252 offset:2048\n" \
  "v_mfma_f32_16x16x32_bf16 v[200:203], v[240:243], v[236:239], v[200:203]\n" \
  "v_mfma_f32_16x16x32_bf16 v[204:207], v[244:247], v[236:239], v[204:207]\n" \
  "ds_read_b128 v[236:239], v252 offset:3072\n" \
  "ds_read_b128 v[240:243], v249 offset:16384\n" \
  "ds_read_b128 v[244:247], v249 offset:17408\n" \
  "v_mfma_f32_16x16x32_bf16 v[192:195], v[40:43], v[208:211], v[192:195]\n" \
  "v_mfma_f32_16x16x32_bf16 v[196:199], v[104:107], v[208:211], v[196:199]\n" \
  "ds_read_b128 v[208:211], v252 offset:4096\n" \
  "v_mfma_f32_16x16x32_bf16 v[200:203], v[40:43], v[212:215], v[200:203]\n" \
  "v_mfma_f32_16x16x32_bf16 v[204:207], v[104:107], v[212:215], v[204:207]\n" \
  "ds_read_b128 v[212:215], v252 offset:5120\n" \
  "v_mfma_f32_16x16x32_bf16 v[192:195], v[168:171], v[216:219], v[192:195]\n" \
  "v_mfma_f32_16x16x32_bf16 v[196:199], v[172:175], v[216:219], v[196:199]\n" \
  "ds_read_b128 v[216:219], v252 offset:6144\n" \
  "v_mfma_f32_16x16x32_bf16 v[200:203], v[168:171], v[220:223], v[200:203]\n" \
  "v_mfma_f32_16x16x32_bf16 v[204:207], v[172:175], v[220:223], v[204:207]\n" \
  "ds_read_b128 v[220:223], v252 offset:7168\n" \
  "s_waitcnt lgkmcnt(9)\n" \
  "v_mfma_f32_16x16x32_bf16 v[192:195], v[44:47], v[224:227], v[192:195]\n" \
  "v_mfma_f32_16x16x32_bf16 v[196:199], v[108:111], v[224:227], v[196:199]\n" \
  "ds_read_b128 v[224:227], v252 offset:8192\n" \
  "s_waitcnt lgkmcnt(9)\n" \
  "v_mfma_f32_16x16x32_bf16 v[200:203], v[44:47], v[228:231], v[200:203]\n" \
  "v_mfma_f32_16x16x32_bf16 v[204:207], v[108:111], v[228:231], v[204:207]\n" \
  "ds_read_b128 v[228:231], v252 offset:9216\n" \
  "s_waitcnt lgkmcnt(7)\n" \
  "v_mfma_f32_16x16x32_bf16 v[192:195], v[240:243], v[232:235], v[192:195]\n" \
  "s_waitcnt lgkmcnt(6)\n" \
  "v_mfma_f32_16x16x32_bf16 v[196:199], v[244:247], v[232:235], v[196:199]\n" \
  "ds_read_b128 v[232:235], v252 offset:10240\n" \
  "v_mfma_f32_16x16x32_bf16 v[200:203], v[240:243], v[236:239], v[200:203]\n" \
  "v_mfma_f32_16x16x32_bf16 v[204:207], v[244:247], v[236:239], v[204:207]\n" \
  "ds_read_b128 v[236:239], v252 offset:11264\n" \
  "ds_read_b128 v[240:243], v249 offset:32768\n" \
  "ds_read_b128 v[244:247], v249 offset:33792\n" \
  "s_waitcnt lgkmcnt(9)\n" \
  "v_mfma_f32_16x16x32_bf16 v[192:195], v[48:51], v[208:211], v[192:195]\n" \
  "v_mfma_f32_16x16x32_bf16 v[196:199], v[112:115], v[208:211], v[196:199]\n" \
  "s_waitcnt lgkmcnt(8)\n" \
  "v_mfma_f32_16x16x32_bf16 v[200:203], v[48:51], v[212:215], v[200:203]\n" \
  "v_mfma_f32_16x16x32_bf16 v[204:207], v[112:115], v[212:215], v[204:207]\n" \
  "s_waitcnt vmcnt(0)\n" \
  "s_waitcnt lgkmcnt(0)\n" \
  "s_barrier\n" \
  "ds_read_b128 v[208:211], v252 offset:12288\n" \
  "ds_read_b128 v[212:215], v252 offset:13312\n" \
  "s_add_u32 m0, %[mw], 0\n" \
  "s_xor_b32 %[cnt], %[sn], 0\n" \
  "buffer_load_dwordx4 v248, %[rx], %[cnt] offen lds\n" \
  "s_add_u32 m0, %[mw], 4096\n" \
  "s_xor_b32 %[cnt], %[sn], 64\n" \
  "buffer_load_dwordx4 v248, %[rx], %[cnt] offen lds\n" \
  "s_add_u32 m0, %[mw], 8192\n" \
  "s_xor_b32 %[cnt], %[sn], 128\n" \
  "buffer_load_dwordx4 v248, %[rx], %[cnt] offen lds\n" \
  "v_mfma_f32_16x16x32_bf16 v[192:195], v[176:179], v[216:219], v[192:195]\n" \
  "v_mfma_f32_16x16x32_bf16 v[196:199], v[180:183], v[216:219], v[196:199]\n" \
  "ds_read_b128 v[216:219], v252 offset:14336\n" \
  "v_mfma_f32_16x16x32_bf16 v[200:203], v[176:179], v[220:223], v[200:203]\n" \
  "v_mfma_f32_16x16x32_bf16 v[204:207], v[180:183], v[220:223], v[204:207]\n" \
  "ds_read_b128 v[220:223], v252 offset:15360\n" \
  "v_mfma_f32_16x16x32_bf16 v[192:195], v[52:55], v[224:227], v[192:195]\n" \
  "v_mfma_f32_16x16x32_bf16 v[196:199], v[116:119], v[224:227], v[196:199]\n" \
  "ds_read_b128 v[224:227], v252 offset:16384\n" \
  "v_mfma_f32_16x16x32_bf16 v[200:203], v[52:55], v[228:231], v[200:203]\n" \
  "v_mfma_f32_16x16x32_bf16 v[204:207], v[116:119], v[228:231], v[204:207]\n" \
  "ds_read_b128 v[228:231], v252 offset:17408\n" \
  "v_mfma_f32_16x16x32_bf16 v[192:195], v[240:243], v[232:235], v[192:195]\n" \
  "v_mfma_f32_16x16x32_bf16 v[196:199], v[244:247], v[232:235], v[196:199]\n" \
  "ds_read_b128 v[232:235], v252 offset:18432\n" \
  "v_mfma_f32_16x16x32_bf16 v[200:203], v[240:243], v[236:239], v[200:203]\n" \
  "v_mfma_f32_16x16x32_bf16 v[204:207], v[244:247], v[236:239], v[204:207]\n" \
  "ds_read_b128 v[236:239], v252 offset:19456\n" \
  "ds_read_b128 v[240:243], v249 offset:49152\n" \
  "ds_read_b128 v[244:247], v249 offset:50176\n" \
  "s_waitcnt lgkmcnt(9)\n" \
  "v_mfma_f32_16x16x32_bf16 v[192:195], v[56:59], v[208:211], v[192:195]\n" \
  "v_mfma_f32_16x16x32_bf16 v[196:199], v[120:123], v[208:211], v[196:199]\n" \
  "s_waitcnt lgkmcnt(8)\n" \
  "v_mfma_f32_16x16x32_bf16 v[200:203], v[56:59], v[212:215], v[200:203]\n" \
  "v_mfma_f32_16x16x32_bf16 v[204:207], v[120:123], v[212:215], v[204:207]\n" \
  "s_waitcnt vmcnt(0)\n" \
  "s_waitcnt lgkmcnt(0)\n" \
  "s_barrier\n" \
  "ds_read_b128 v[208:211], v252 offset:0\n" \
  "ds_read_b128 v[212:215], v252 offset:1024\n" \
  "s_add_u32 m0, %[mw], 12288\n" \
  "s_xor_b32 %[cnt], %[sn], 192\n" \
  "buffer_load_dwordx4 v248, %[rx], %[cnt] offen lds\n" \
  "s_add_u32 m0, %[mw], 16384\n" \
  "s_xor_b32 %[cnt], %[sn], 256\n" \
  "buffer_load_dwordx4 v248, %[rx], %[cnt] offen lds\n" \
  "s_add_u32 m0, %[mw], 20480\n" \
  "s_xor_b32 %[cnt], %[sn], 320\n" \
  "buffer_load_dwordx4 v248, %[rx], %[cnt] offen lds\n" \
  "v_mfma_f32_16x16x32_bf16 v[192:195], v[184:187], v[216:219], v[192:195]\n" \
  "v_mfma_f32_16x16x32_bf16 v[196:199], v[188:191], v[216:219], v[196:199]\n" \
  "ds_read_b128 v[216:219], v252 offset:2048\n" \
  "v_mfma_f32_16x16x32_bf16 v[200:203], v[184:187], v[220:223], v[200:203]\n" \
  "v_mfma_f32_16x16x32_bf16 v[204:207], v[188:191], v[220:223], v[204:207]\n" \
  "ds_read_b128 v[220:223], v252 offset:3072\n" \
  "v_mfma_f32_16x16x32_bf16 v[192:195], v[60:63], v[224:227], v[192:195]\n" \
  "v_mfma_f32_16x16x32_bf16 v[196:199], v[124:127], v[224:227], v[196:199]\n" \
  "ds_read_b128 v[224:227], v252 offset:4096\n" \
  "v_mfma_f32_16x16x32_bf16 v[200:203], v[60:63], v[228:231], v[200:203]\n" \
  "v_mfma_f32_16x16x32_bf16 v[204:207], v[124:127], v[228:231], v[204:207]\n" \
  "ds_read_b128 v[228:231], v252 offset:5120\n" \
  "v_mfma_f32_16x16x32_bf16 v[192:195], v[240:243], v[232:235], v[192:195]\n" \
  "v_mfma_f32_16x16x32_bf16 v[196:199], v[244:247], v[232:235], v[196:199]\n" \
  "ds_read_b128 v[232:235], v252 offset:6144\n" \
  "v_mfma_f32_16x16x32_bf16 v[200:203], v[240:243], v[236:239], v[200:203]\n" \
  "v_mfma_f32_16x16x32_bf16 v[204:207], v[244:247], v[236:239], v[204:207]\n" \
  "ds_read_b128 v[236:239], v252 offset:7168\n" \
  "v_cvt_pk_bf16_f32 v192, v192, v193\n" \
  "v_cvt_pk_bf16_f32 v193, v194, v195\n" \
  "v_cvt_pk_bf16_f32 v194, v196, v197\n" \
  "v_cvt_pk_bf16_f32 v195, v198, v199\n" \
  "v_add_u32 v196, %[so], v251\n" \
  "ds_write_b128 v196, v[192:195] offset:24576\n" \
  "v_cvt_pk_bf16_f32 v200, v200, v201\n" \
  "v_cvt_pk_bf16_f32 v201, v202, v203\n" \
  "v_cvt_pk_bf16_f32 v202, v204, v205\n" \
  "v_cvt_pk_bf16_f32 v203, v206, v207\n" \
  "ds_write_b128 v196, v[200:203] offset:28672\n" \
  ".Lqe_%=:\n" \
  "s_mov_b32 m0, %[keep]\n" \
  "s_waitcnt lgkmcnt(0)\n"
#define ST_XB \
  "s_barrier\n" \
  "ds_read_b128 v[192:195], v251 offset:24576\n" \
  "ds_read_b128 v[208:211], v251 offset:28672\n" \
  "ds_read_b128 v[196:199], v251 offset:25600\n" \
  "ds_read_b128 v[212:215], v251 offset:29696\n" \
  "ds_read_b128 v[200:203], v251 offset:26624\n" \
  "ds_read_b128 v[216:219], v251 offset:30720\n" \
  "ds_read_b128 v[204:207], v251 offset:27648\n" \
  "ds_read_b128 v[220:223], v251 offset:31744\n" \
  "ds_read_b128 v[240:243], v250 offset:0\n" \
  "ds_read_b128 v[244:247], v250 offset:16384\n" \
  "s_waitcnt lgkmcnt(9)\n" \
  "v_mfma_f32_16x16x32_bf16 v[224:227], v[192:195], v[0:3], 0\n" \
  "s_waitcnt lgkmcnt(8)\n" \
  "v_mfma_f32_16x16x32_bf16 v[228:231], v[208:211], v[0:3], 0\n" \
  "s_waitcnt lgkmcnt(7)\n" \
  "v_mfma_f32_16x16x32_bf16 v[224:227], v[196:199], v[4:7], v[224:227]\n" \
  "s_waitcnt lgkmcnt(6)\n" \
  "v_mfma_f32_16x16x32_bf16 v[228:231], v[212:215], v[4:7], v[228:231]\n" \
  "s_waitcnt lgkmcnt(5)\n" \
  "v_mfma_f32_16x16x32_bf16 v[224:227], v[200:203], v[8:11], v[224:227]\n" \
  "s_waitcnt lgkmcnt(4)\n" \
  "v_mfma_f32_16x16x32_bf16 v[228:231], v[216:219], v[8:11], v[228:231]\n" \
  "s_waitcnt lgkmcnt(3)\n" \
  "v_mfma_f32_16x16x32_bf16 v[224:227], v[204:207], v[12:15], v[224:227]\n" \
  "s_waitcnt lgkmcnt(2)\n" \
  "v_mfma_f32_16x16x32_bf16 v[228:231], v[220:223], v[12:15], v[228:231]\n" \
  "v_mfma_f32_16x16x32_bf16 v[232:235], v[192:195], v[128:131], 0\n" \
  "v_mfma_f32_16x16x32_bf16 v[236:239], v[208:211], v[128:131], 0\n" \
  "v_mfma_f32_16x16x32_bf16 v[232:235], v[200:203], v[136:139], v[232:235]\n" \
  "v_cvt_pk_bf16_f32 v224, v224, v228\n" \
  "v_cvt_pk_bf16_f32 v225, v225, v229\n" \
  "v_mfma_f32_16x16x32_bf16 v[236:239], v[216:219], v[136:139], v[236:239]\n" \
  "v_cvt_pk_bf16_f32 v226, v226, v230\n" \
  "v_cvt_pk_bf16_f32 v227, v227, v231\n" \
  "s_waitcnt lgkmcnt(1)\n" \
  "v_mfma_f32_16x16x32_bf16 v[232:235], v[196:199], v[240:243], v[232:235]\n" \
  "s_add_u32 %[cnt], %[so], 0*%c[js]+0*%c[ss]\n" \
  "buffer_store_dwordx4 v[224:227], v251, %[rs], %[cnt] offen sc1\n" \
  "v_mfma_f32_16x16x32_bf16 v[236:239], v[212:215], v[240:243], v[236:239]\n" \
  "s_waitcnt lgkmcnt(0)\n" \
  "v_mfma_f32_16x16x32_bf16 v[232:235], v[204:207], v[244:247], v[232:235]\n" \
  "v_mfma_f32_16x16x32_bf16 v[236:239], v[220:223], v[244:247], v[236:239]\n" \
  "ds_read_b128 v[240:243], v250 offset:1024\n" \
  "ds_read_b128 v[244:247], v250 offset:17408\n" \
  "v_mfma_f32_16x16x32_bf16 v[224:227], v[192:195], v[64:67], 0\n" \
  "v_mfma_f32_16x16x32_bf16 v[228:231], v[208:211], v[64:67], 0\n" \
  "v_mfma_f32_16x16x32_bf16 v[224:227], v[196:199], v[68:71], v[224:227]\n" \
  "v_cvt_pk_bf16_f32 v232, v232, v236\n" \
  "v_cvt_pk_bf16_f32 v233, v233, v237\n" \
  "v_mfma_f32_16x16x32_bf16 v[228:231], v[212:215], v[68:71], v[228:231]\n" \
  "v_cvt_pk_bf16_f32 v234, v234, v238\n" \
  "v_cvt_pk_bf16_f32 v235, v235, v239\n" \
  "v_mfma_f32_16x16x32_bf16 v[224:227], v[200:203], v[72:75], v[224:227]\n" \
  "s_add_u32 %[cnt], %[so], 4*%c[js]+0*%c[ss]\n" \
  "buffer_store_dwordx4 v[232:235], v251, %[rs], %[cnt] offen sc1\n" \
  "v_mfma_f32_16x16x32_bf16 v[228:231], v[216:219], v[72:75], v[228:231]\n" \
  "v_mfma_f32_16x16x32_bf16 v[224:227], v[204:207], v[76:79], v[224:227]\n" \
  "v_mfma_f32_16x16x32_bf16 v[228:231], v[220:223], v[76:79], v[228:231]\n" \
  "s_waitcnt vmcnt(2)\n" \
  "s_barrier\n" \
  "v_mfma_f32_16x16x32_bf16 v[232:235], v[192:195], v[132:135], 0\n" \
  "v_mfma_f32_16x16x32_bf16 v[236:239], v[208:211], v[132:135], 0\n" \
  "v_mfma_f32_16x16x32_bf16 v[232:235], v[200:203], v[140:143], v[232:235]\n" \
  "v_cvt_pk_bf16_f32 v224, v224, v228\n" \
  "v_cvt_pk_bf16_f32 v225, v225, v229\n" \
  "v_mfma_f32_16x16x32_bf16 v[236:239], v[216:219], v[140:143], v[236:239]\n" \
  "v_cvt_pk_bf16_f32 v226, v226, v230\n" \
  "v_cvt_pk_bf16_f32 v227, v227, v231\n" \
  "s_waitcnt lgkmcnt(1)\n" \
  "v_mfma_f32_16x16x32_bf16 v[232:235], v[196:199], v[240:243], v[232:235]\n" \
  "s_add_u32 %[cnt], %[so], 0*%c[js]+1*%c[ss]\n" \
  "buffer_store_dwordx4 v[224:227], v251, %[rs], %[cnt] offen sc1\n" \
  "v_mfma_f32_16x16x32_bf16 v[236:239], v[212:215], v[240:243], v[236:239]\n" \
  "s_waitcnt lgkmcnt(0)\n" \
  "v_mfma_f32_16x16x32_bf16 v[232:235], v[204:207], v[244:247], v[232:235]\n" \
  "v_mfma_f32_16x16x32_bf16 v[236:239], v[220:223], v[244:247], v[236:239]\n" \
  "ds_read_b128 v[240:243], v250 offset:32768\n" \
  "ds_read_b128 v[244:247], v250 offset:49152\n" \
  "v_mfma_f32_16x16x32_bf16 v[224:227], v[192:195], v[16:19], 0\n" \
  "v_mfma_f32_16x16x32_bf16 v[228:231], v[208:211], v[16:19], 0\n" \
  "v_mfma_f32_16x16x32_bf16 v[224:227], v[196:199], v[20:23], v[224:227]\n" \
  "v_cvt_pk_bf16_f32 v232, v232, v236\n" \
  "v_cvt_pk_bf16_f32 v233, v233, v237\n" \
  "v_mfma_f32_16x16x32_bf16 v[228:231], v[212:215], v[20:23], v[228:231]\n" \
  "v_cvt_pk_bf16_f32 v234, v234, v238\n" \
  "v_cvt_pk_bf16_f32 v235, v235, v239\n" \
  "v_mfma_f32_16x16x32_bf16 v[224:227], v[200:203], v[24:27], v[224:227]\n" \
  "s_add_u32 %[cnt], %[so], 4*%c[js]+1*%c[ss]\n" \
  "buffer_store_dwordx4 v[232:235], v251, %[rs], %[cnt] offen sc1\n" \
  "v_mfma_f32_16x16x32_bf16 v[228:231], v[216:219], v[24:27], v[228:231]\n" \
  "v_mfma_f32_16x16x32_bf16 v[224:227], v[204:207], v[28:31], v[224:227]\n" \
  "v_mfma_f32_16x16x32_bf16 v[228:231], v[220:223], v[28:31], v[228:231]\n" \
  "s_barrier\n" \
  "v_mfma_f32_16x16x32_bf16 v[232:235], v[192:195], v[144:147], 0\n" \
  "v_mfma_f32_16x16x32_bf16 v[236:239], v[208:211], v[144:147], 0\n" \
  "v_mfma_f32_16x16x32_bf16 v[232:235], v[200:203], v[152:155], v[232:235]\n" \
  "v_cvt_pk_bf16_f32 v224, v224, v228\n" \
  "v_cvt_pk_bf16_f32 v225, v225, v229\n" \
  "v_mfma_f32_16x16x32_bf16 v[236:239], v[216:219], v[152:155], v[236:239]\n" \
  "v_cvt_pk_bf16_f32 v226, v226, v230\n" \
  "v_cvt_pk_bf16_f32 v227, v227, v231\n" \
  "s_waitcnt lgkmcnt(1)\n" \
  "v_mfma_f32_16x16x32_bf16 v[232:235], v[196:199], v[240:243], v[232:235]\n" \
  "s_add_u32 %[cnt], %[so2], 1*%c[js]+0*%c[ss]\n" \
  "buffer_store_dwordx4 v[224:227], v251, %[rs], %[cnt] offen sc1\n" \
  "v_mfma_f32_16x16x32_bf16 v[236:239], v[212:215], v[240:243], v[236:239]\n" \
  "s_waitcnt lgkmcnt(0)\n" \
  "v_mfma_f32_16x16x32_bf16 v[232:235], v[204:207], v[244:247], v[232:235]\n" \
  "v_mfma_f32_16x16x32_bf16 v[236:239], v[220:223], v[244:247], v[236:239]\n" \
  "ds_read_b128 v[240:243], v250 offset:33792\n" \
  "ds_read_b128 v[244:247], v250 offset:50176\n" \
  "v_mfma_f32_16x16x32_bf16 v[224:227], v[192:195], v[80:83], 0\n" \
  "v_mfma_f32_16x16x32_bf16 v[228:231], v[208:211], v[80:83], 0\n" \
  "v_mfma_f32_16x16x32_bf16 v[224:227], v[196:199], v[84:87], v[224:227]\n" \
  "v_cvt_pk_bf16_f32 v232, v232, v236\n" \
  "v_cvt_pk_bf16_f32 v233, v233, v237\n" \
  "v_mfma_f32_16x16x32_bf16 v[228:231], v[212:215], v[84:87], v[228:231]\n" \
  "v_cvt_pk_bf16_f32 v234, v234, v238\n" \
  "v_cvt_pk_bf16_f32 v235, v235, v239\n" \
  "v_mfma_f32_16x16x32_bf16 v[224:227], v[200:203], v[88:91], v[224:227]\n" \
  "s_add_u32 %[cnt], %[so2], 5*%c[js]+0*%c[ss]\n" \
  "buffer_store_dwordx4 v[232:235], v251, %[rs], %[cnt] offen sc1\n" \
  "v_mfma_f32_16x16x32_bf16 v[228:231], v[216:219], v[88:91], v[228:231]\n" \
  "v_mfma_f32_16x16x32_bf16 v[224:227], v[204:207], v[92:95], v[224:227]\n" \
  "v_mfma_f32_16x16x32_bf16 v[228:231], v[220:223], v[92:95], v[228:231]\n" \
  "v_mfma_f32_16x16x32_bf16 v[232:235], v[192:195], v[148:151], 0\n" \
  "v_mfma_f32_16x16x32_bf16 v[236:239], v[208:211], v[148:151], 0\n" \
  "v_mfma_f32_16x16x32_bf16 v[232:235], v[200:203], v[156:159], v[232:235]\n" \
  "v_cvt_pk_bf16_f32 v224, v224, v228\n" \
  "v_cvt_pk_bf16_f32 v225, v225, v229\n" \
  "v_mfma_f32_16x16x32_bf16 v[236:239], v[216:219], v[156:159], v[236:239]\n" \
  "v_cvt_pk_bf16_f32 v226, v226, v230\n" \
  "v_cvt_pk_bf16_f32 v227, v227, v231\n" \
  "s_waitcnt lgkmcnt(1)\n" \
  "v_mfma_f32_16x16x32_bf16 v[232:235], v[196:199], v[240:243], v[232:235]\n" \
  "s_add_u32 %[cnt], %[so2], 1*%c[js]+1*%c[ss]\n" \
  "buffer_store_dwordx4 v[224:227], v251, %[rs], %[cnt] offen sc1\n" \
  "v_mfma_f32_16x16x32_bf16 v[236:239], v[212:215], v[240:243], v[236:239]\n" \
  "s_waitcnt lgkmcnt(0)\n" \
  "v_mfma_f32_16x16x32_bf16 v[232:235], v[204:207], v[244:247], v[232:235]\n" \
  "v_mfma_f32_16x16x32_bf16 v[236:239], v[220:223], v[244:247], v[236:239]\n" \
  "ds_read_b128 v[240:243], v249 offset:0\n" \
  "ds_read_b128 v[244:247], v249 offset:16384\n" \
  "s_barrier\n" \
  "v_mfma_f32_16x16x32_bf16 v[224:227], v[192:195], v[32:35], 0\n" \
  "v_mfma_f32_16x16x32_bf16 v[228:231], v[208:211], v[32:35], 0\n" \
  "v_mfma_f32_16x16x32_bf16 v[224:227], v[196:199], v[36:39], v[224:227]\n" \
  "v_cvt_pk_bf16_f32 v232, v232, v236\n" \
  "v_cvt_pk_bf16_f32 v233, v233, v237\n" \
  "v_mfma_f32_16x16x32_bf16 v[228:231], v[212:215], v[36:39], v[228:231]\n" \
  "v_cvt_pk_bf16_f32 v234, v234, v238\n" \
  "v_cvt_pk_bf16_f32 v235, v235, v239\n" \
  "v_mfma_f32_16x16x32_bf16 v[224:227], v[200:203], v[40:43], v[224:227]\n" \
  "s_add_u32 %[cnt], %[so2], 5*%c[js]+1*%c[ss]\n" \
  "buffer_store_dwordx4 v[232:235], v251, %[rs], %[cnt] offen sc1\n" \
  "v_mfma_f32_16x16x32_bf16 v[228:231], v[216:219], v[40:43], v[228:231]\n" \
  "v_mfma_f32_16x16x32_bf16 v[224:227], v[204:207], v[44:47], v[224:227]\n" \
  "v_mfma_f32_16x16x32_bf16 v[228:231], v[220:223], v[44:47], v[228:231]\n" \
  "v_mfma_f32_16x16x32_bf16 v[232:235], v[192:195], v[160:163], 0\n" \
  "v_mfma_f32_16x16x32_bf16 v[236:239], v[208:211], v[160:163], 0\n" \
  "v_mfma_f32_16x16x32_bf16 v[232:235], v[200:203], v[168:171], v[232:235]\n" \
  "v_cvt_pk_bf16_f32 v224, v224, v228\n" \
  "v_cvt_pk_bf16_f32 v225, v225, v229\n" \
  "v_mfma_f32_16x16x32_bf16 v[236:239], v[216:219], v[168:171], v[236:239]\n" \
  "v_cvt_pk_bf16_f32 v226, v226, v230\n" \
  "v_cvt_pk_bf16_f32 v227, v227, v231\n" \
  "s_waitcnt lgkmcnt(1)\n" \
  "v_mfma_f32_16x16x32_bf16 v[232:235], v[196:199], v[240:243], v[232:235]\n" \
  "s_add_u32 %[cnt], %[so], 2*%c[js]+0*%c[ss]\n" \
  "buffer_store_dwordx4 v[224:227], v251, %[rs], %[cnt] offen sc1\n" \
  "v_mfma_f32_16x16x32_bf16 v[236:239], v[212:215], v[240:243], v[236:239]\n" \
  "s_waitcnt lgkmcnt(0)\n" \
  "v_mfma_f32_16x16x32_bf16 v[232:235], v[204:207], v[244:247], v[232:235]\n" \
  "v_mfma_f32_16x16x32_bf16 v[236:239], v[220:223], v[244:247], v[236:239]\n" \
  "ds_read_b128 v[240:243], v249 offset:1024\n" \
  "ds_read_b128 v[244:247], v249 offset:17408\n" \
  "v_mfma_f32_16x16x32_bf16 v[224:227], v[192:195], v[96:99], 0\n" \
  "v_mfma_f32_16x16x32_bf16 v[228:231], v[208:211], v[96:99], 0\n" \
  "v_mfma_f32_16x16x32_bf16 v[224:227], v[196:199], v[100:103], v[224:227]\n" \
  "v_cvt_pk_bf16_f32 v232, v232, v236\n" \
  "v_cvt_pk_bf16_f32 v233, v233, v237\n" \
  "v_mfma_f32_16x16x32_bf16 v[228:231], v[212:215], v[100:103], v[228:231]\n" \
  "v_cvt_pk_bf16_f32 v234, v234, v238\n" \
  "v_cvt_pk_bf16_f32 v235, v235, v239\n" \
  "v_mfma_f32_16x16x32_bf16 v[224:227], v[200:203], v[104:107], v[224:227]\n" \
  "s_add_u32 %[cnt], %[so], 6*%c[js]+0*%c[ss]\n" \
  "buffer_store_dwordx4 v[232:235], v251, %[rs], %[cnt] offen sc1\n" \
  "v_mfma_f32_16x16x32_bf16 v[228:231], v[216:219], v[104:107], v[228:231]\n" \
  "v_mfma_f32_16x16x32_bf16 v[224:227], v[204:207], v[108:111], v[224:227]\n" \
  "v_mfma_f32_16x16x32_bf16 v[228:231], v[220:223], v[108:111], v[228:231]\n" \
  "s_barrier\n" \
  "v_mfma_f32_16x16x32_bf16 v[232:235], v[192:195], v[164:167], 0\n" \
  "v_mfma_f32_16x16x32_bf16 v[236:239], v[208:211], v[164:167], 0\n" \
  "v_mfma_f32_16x16x32_bf16 v[232:235], v[200:203], v[172:175], v[232:235]\n" \
  "v_cvt_pk_bf16_f32 v224, v224, v228\n" \
  "v_cvt_pk_bf16_f32 v225, v225, v229\n" \
  "v_mfma_f32_16x16x32_bf16 v[236:239], v[216:219], v[172:175], v[236:239]\n" \
  "v_cvt_pk_bf16_f32 v226, v226, v230\n" \
  "v_cvt_pk_bf16_f32 v227, v227, v231\n" \
  "s_waitcnt lgkmcnt(1)\n" \
  "v_mfma_f32_16x16x32_bf16 v[232:235], v[196:199], v[240:243], v[232:235]\n" \
  "s_add_u32 %[cnt], %[so], 2*%c[js]+1*%c[ss]\n" \
  "buffer_store_dwordx4 v[224:227], v251, %[rs], %[cnt] offen sc1\n" \
  "v_mfma_f32_16x16x32_bf16 v[236:239], v[212:215], v[240:243], v[236:239]\n" \
  "s_waitcnt lgkmcnt(0)\n" \
  "v_mfma_f32_16x16x32_bf16 v[232:235], v[204:207], v[244:247], v[232:235]\n" \
  "v_mfma_f32_16x16x32_bf16 v[236:239], v[220:223], v[244:247], v[236:239]\n" \
  "ds_read_b128 v[240:243], v249 offset:32768\n" \
  "ds_read_b128 v[244:247], v249 offset:49152\n" \
  "v_mfma_f32_16x16x32_bf16 v[224:227], v[192:195], v[48:51], 0\n" \
  "v_mfma_f32_16x16x32_bf16 v[228:231], v[208:211], v[48:51], 0\n" \
  "v_mfma_f32_16x16x32_bf16 v[224:227], v[196:199], v[52:55], v[224:227]\n" \
  "v_cvt_pk_bf16_f32 v232, v232, v236\n" \
  "v_cvt_pk_bf16_f32 v233, v233, v237\n" \
  "v_mfma_f32_16x16x32_bf16 v[228:231], v[212:215], v[52:55], v[228:231]\n" \
  "v_cvt_pk_bf16_f32 v234, v234, v238\n" \
  "v_cvt_pk_bf16_f32 v235, v235, v239\n" \
  "v_mfma_f32_16x16x32_bf16 v[224:227], v[200:203], v[56:59], v[224:227]\n" \
  "s_add_u32 %[cnt], %[so], 6*%c[js]+1*%c[ss]\n" \
  "buffer_store_dwordx4 v[232:235], v251, %[rs], %[cnt] offen sc1\n" \
  "v_mfma_f32_16x16x32_bf16 v[228:231], v[216:219], v[56:59], v[228:231]\n" \
  "v_mfma_f32_16x16x32_bf16 v[224:227], v[204:207], v[60:63], v[224:227]\n" \
  "v_mfma_f32_16x16x32_bf16 v[228:231], v[220:223], v[60:63], v[228:231]\n" \
  "s_barrier\n" \
  "v_mfma_f32_16x16x32_bf16 v[232:235], v[192:195], v[176:179], 0\n" \
  "v_mfma_f32_16x16x32_bf16 v[236:239], v[208:211], v[176:179], 0\n" \
  "v_mfma_f32_16x16x32_bf16 v[232:235], v[200:203], v[184:187], v[232:235]\n" \
  "v_cvt_pk_bf16_f32 v224, v224, v228\n" \
  "v_cvt_pk_bf16_f32 v225, v225, v229\n" \
  "v_mfma_f32_16x16x32_bf16 v[236:239], v[216:219], v[184:187], v[236:239]\n" \
  "v_cvt_pk_bf16_f32 v226, v226, v230\n" \
  "v_cvt_pk_bf16_f32 v227, v227, v231\n" \
  "s_waitcnt lgkmcnt(1)\n" \
  "v_mfma_f32_16x16x32_bf16 v[232:235], v[196:199], v[240:243], v[232:235]\n" \
  "s_add_u32 %[cnt], %[so2], 3*%c[js]+0*%c[ss]\n" \
  "buffer_store_dwordx4 v[224:227], v251, %[rs], %[cnt] offen sc1\n" \
  "v_mfma_f32_16x16x32_bf16 v[236:239], v[212:215], v[240:243], v[236:239]\n" \
  "s_waitcnt lgkmcnt(0)\n" \
  "v_mfma_f32_16x16x32_bf16 v[232:235], v[204:207], v[244:247], v[232:235]\n" \
  "v_mfma_f32_16x16x32_bf16 v[236:239], v[220:223], v[244:247], v[236:239]\n" \
  "ds_read_b128 v[240:243], v249 offset:33792\n" \
  "ds_read_b128 v[244:247], v249 offset:50176\n" \
  "v_mfma_f32_16x16x32_bf16 v[224:227], v[192:195], v[112:115], 0\n" \
  "v_mfma_f32_16x16x32_bf16 v[228:231], v[208:211], v[112:115], 0\n" \
  "v_mfma_f32_16x16x32_bf16 v[224:227], v[196:199], v[116:119], v[224:227]\n" \
  "v_cvt_pk_bf16_f32 v232, v232, v236\n" \
  "v_cvt_pk_bf16_f32 v233, v233, v237\n" \
  "v_mfma_f32_16x16x32_bf16 v[228:231], v[212:215], v[116:119], v[228:231]\n" \
  "v_cvt_pk_bf16_f32 v234, v234, v238\n" \
  "v_cvt_pk_bf16_f32 v235, v235, v239\n" \
  "v_mfma_f32_16x16x32_bf16 v[224:227], v[200:203], v[120:123], v[224:227]\n" \
  "s_add_u32 %[cnt], %[so2], 7*%c[js]+0*%c[ss]\n" \
  "buffer_store_dwordx4 v[232:235], v251, %[rs], %[cnt] offen sc1\n" \
  "v_mfma_f32_16x16x32_bf16 v[228:231], v[216:219], v[120:123], v[228:231]\n" \
  "v_mfma_f32_16x16x32_bf16 v[224:227], v[204:207], v[124:127], v[224:227]\n" \
  "v_mfma_f32_16x16x32_bf16 v[228:231], v[220:223], v[124:127], v[228:231]\n" \
  "v_mfma_f32_16x16x32_bf16 v[232:235], v[192:195], v[180:183], 0\n" \
  "v_mfma_f32_16x16x32_bf16 v[236:239], v[208:211], v[180:183], 0\n" \
  "v_mfma_f32_16x16x32_bf16 v[232:235], v[200:203], v[188:191], v[232:235]\n" \
  "v_cvt_pk_bf16_f32 v224, v224, v228\n" \
  "v_cvt_pk_bf16_f32 v225, v225, v229\n" \
  "v_mfma_f32_16x16x32_bf16 v[236:239], v[216:219], v[188:191], v[236:239]\n" \
  "v_cvt_pk_bf16_f32 v226, v226, v230\n" \
  "v_cvt_pk_bf16_f32 v227, v227, v231\n" \
  "s_waitcnt lgkmcnt(1)\n" \
  "v_mfma_f32_16x16x32_bf16 v[232:235], v[196:199], v[240:243], v[232:235]\n" \
  "s_add_u32 %[cnt], %[so2], 3*%c[js]+1*%c[ss]\n" \
  "buffer_store_dwordx4 v[224:227], v251, %[rs], %[cnt] offen sc1\n" \
  "v_mfma_f32_16x16x32_bf16 v[236:239], v[212:215], v[240:243], v[236:239]\n" \
  "s_waitcnt lgkmcnt(0)\n" \
  "v_mfma_f32_16x16x32_bf16 v[232:235], v[204:207], v[244:247], v[232:235]\n" \
  "v_mfma_f32_16x16x32_bf16 v[236:239], v[220:223], v[244:247], v[236:239]\n" \
  "s_nop 8\n" \
  "v_cvt_pk_bf16_f32 v232, v232, v236\n" \
  "v_cvt_pk_bf16_f32 v233, v233, v237\n" \
  "v_cvt_pk_bf16_f32 v234, v234, v238\n" \
  "v_cvt_pk_bf16_f32 v235, v235, v239\n" \
  "s_add_u32 %[cnt], %[so2], 7*%c[js]+1*%c[ss]\n" \
  "buffer_store_dwordx4 v[232:235], v251, %[rs], %[cnt] offen sc1\n" \
  "s_nop 1\n"
#define ST_XB0 \
  "s_setprio 1\n" \
  "ds_read_b128 v[192:195], v251 offset:24576\n" \
  "ds_read_b128 v[208:211], v251 offset:28672\n" \
  "ds_read_b128 v[196:199], v251 offset:25600\n" \
  "ds_read_b128 v[212:215], v251 offset:29696\n" \
  "ds_read_b128 v[200:203], v251 offset:26624\n" \
  "ds_read_b128 v[216:219], v251 offset:30720\n" \
  "ds_read_b128 v[204:207], v251 offset:27648\n" \
  "ds_read_b128 v[220:223], v251 offset:31744\n" \
  "ds_read_b128 v[240:243], v250 offset:0\n" \
  "ds_read_b128 v[244:247], v250 offset:16384\n" \
  "s_waitcnt lgkmcnt(9)\n" \
  "v_mfma_f32_16x16x32_bf16 v[224:227], v[192:195], v[0:3], 0\n" \
  "s_waitcnt lgkmcnt(8)\n" \
  "v_mfma_f32_16x16x32_bf16 v[228:231], v[208:211], v[0:3], 0\n" \
  "s_waitcnt lgkmcnt(7)\n" \
  "v_mfma_f32_16x16x32_bf16 v[224:227], v[196:199], v[4:7], v[224:227]\n" \
  "s_waitcnt lgkmcnt(6)\n" \
  "v_mfma_f32_16x16x32_bf16 v[228:231], v[212:215], v[4:7], v[228:231]\n" \
  "s_waitcnt lgkmcnt(5)\n" \
  "v_mfma_f32_16x16x32_bf16 v[224:227], v[200:203], v[8:11], v[224:227]\n" \
  "s_waitcnt lgkmcnt(4)\n" \
  "v_mfma_f32_16x16x32_bf16 v[228:231], v[216:219], v[8:11], v[228:231]\n" \
  "s_waitcnt lgkmcnt(3)\n" \
  "v_mfma_f32_16x16x32_bf16 v[224:227], v[204:207], v[12:15], v[224:227]\n" \
  "s_waitcnt lgkmcnt(2)\n" \
  "v_mfma_f32_16x16x32_bf16 v[228:231], v[220:223], v[12:15], v[228:231]\n" \
  "v_mfma_f32_16x16x32_bf16 v[232:235], v[192:195], v[128:131], 0\n" \
  "v_mfma_f32_16x16x32_bf16 v[236:239], v[208:211], v[128:131], 0\n" \
  "v_mfma_f32_16x16x32_bf16 v[232:235], v[200:203], v[136:139], v[232:235]\n" \
  "v_cvt_pk_bf16_f32 v224, v224, v228\n" \
  "v_cvt_pk_bf16_f32 v225, v225, v229\n" \
  "v_mfma_f32_16x16x32_bf16 v[236:239], v[216:219], v[136:139], v[236:239]\n" \
  "v_cvt_pk_bf16_f32 v226, v226, v230\n" \
  "v_cvt_pk_bf16_f32 v227, v227, v231\n" \
  "s_waitcnt lgkmcnt(1)\n" \
  "v_mfma_f32_16x16x32_bf16 v[232:235], v[196:199], v[240:243], v[232:235]\n" \
  "s_add_u32 %[cnt], %[so], 0*%c[js]+0*%c[ss]\n" \
  "buffer_store_dwordx4 v[224:227], v251, %[rs], %[cnt] offen sc1\n" \
  "v_mfma_f32_16x16x32_bf16 v[236:239], v[212:215], v[240:243], v[236:239]\n" \
  "s_waitcnt lgkmcnt(0)\n" \
  "v_mfma_f32_16x16x32_bf16 v[232:235], v[204:207], v[244:247], v[232:235]\n" \
  "v_mfma_f32_16x16x32_bf16 v[236:239], v[220:223], v[244:247], v[236:239]\n" \
  "ds_read_b128 v[240:243], v250 offset:1024\n" \
  "ds_read_b128 v[244:247], v250 offset:17408\n" \
  "v_mfma_f32_16x16x32_bf16 v[224:227], v[192:195], v[64:67], 0\n" \
  "v_mfma_f32_16x16x32_bf16 v[228:231], v[208:211], v[64:67], 0\n" \
  "v_mfma_f32_16x16x32_bf16 v[224:227], v[196:199], v[68:71], v[224:227]\n" \
  "v_cvt_pk_bf16_f32 v232, v232, v236\n" \
  "v_cvt_pk_bf16_f32 v233, v233, v237\n" \
  "v_mfma_f32_16x16x32_bf16 v[228:231], v[212:215], v[68:71], v[228:231]\n" \
  "v_cvt_pk_bf16_f32 v234, v234, v238\n" \
  "v_cvt_pk_bf16_f32 v235, v235, v239\n" \
  "v_mfma_f32_16x16x32_bf16 v[224:227], v[200:203], v[72:75], v[224:227]\n" \
  "s_add_u32 %[cnt], %[so], 4*%c[js]+0*%c[ss]\n" \
  "buffer_store_dwordx4 v[232:235], v251, %[rs], %[cnt] offen sc1\n" \
  "v_mfma_f32_16x16x32_bf16 v[228:231], v[216:219], v[72:75], v[228:231]\n" \
  "v_mfma_f32_16x16x32_bf16 v[224:227], v[204:207], v[76:79], v[224:227]\n" \
  "v_mfma_f32_16x16x32_bf16 v[228:231], v[220:223], v[76:79], v[228:231]\n" \
  "v_mfma_f32_16x16x32_bf16 v[232:235], v[192:195], v[132:135], 0\n" \
  "v_mfma_f32_16x16x32_bf16 v[236:239], v[208:211], v[132:135], 0\n" \
  "v_mfma_f32_16x16x32_bf16 v[232:235], v[200:203], v[140:143], v[232:235]\n" \
  "v_cvt_pk_bf16_f32 v224, v224, v228\n" \
  "v_cvt_pk_bf16_f32 v225, v225, v229\n" \
  "v_mfma_f32_16x16x32_bf16 v[236:239], v[216:219], v[140:143], v[236:239]\n" \
  "v_cvt_pk_bf16_f32 v226, v226, v230\n" \
  "v_cvt_pk_bf16_f32 v227, v227, v231\n" \
  "s_waitcnt lgkmcnt(1)\n" \
  "v_mfma_f32_16x16x32_bf16 v[232:235], v[196:199], v[240:243], v[232:235]\n" \
  "s_add_u32 %[cnt], %[so], 0*%c[js]+1*%c[ss]\n" \
  "buffer_store_dwordx4 v[224:227], v251, %[rs], %[cnt] offen sc1\n" \
  "v_mfma_f32_16x16x32_bf16 v[236:239], v[212:215], v[240:243], v[236:239]\n" \
  "s_waitcnt lgkmcnt(0)\n" \
  "v_mfma_f32_16x16x32_bf16 v[232:235], v[204:207], v[244:247], v[232:235]\n" \
  "v_mfma_f32_16x16x32_bf16 v[236:239], v[220:223], v[244:247], v[236:239]\n" \
  "ds_read_b128 v[240:243], v250 offset:32768\n" \
  "ds_read_b128 v[244:247], v250 offset:49152\n" \
  "v_mfma_f32_16x16x32_bf16 v[224:227], v[192:195], v[16:19], 0\n" \
  "v_mfma_f32_16x16x32_bf16 v[228:231], v[208:211], v[16:19], 0\n" \
  "v_mfma_f32_16x16x32_bf16 v[224:227], v[196:199], v[20:23], v[224:227]\n" \
  "v_cvt_pk_bf16_f32 v232, v232, v236\n" \
  "v_cvt_pk_bf16_f32 v233, v233, v237\n" \
  "v_mfma_f32_16x16x32_bf16 v[228:231], v[212:215], v[20:23], v[228:231]\n" \
  "v_cvt_pk_bf16_f32 v234, v234, v238\n" \
  "v_cvt_pk_bf16_f32 v235, v235, v239\n" \
  "v_mfma_f32_16x16x32_bf16 v[224:227], v[200:203], v[24:27], v[224:227]\n" \
  "s_add_u32 %[cnt], %[so], 4*%c[js]+1*%c[ss]\n" \
  "buffer_store_dwordx4 v[232:235], v251, %[rs], %[cnt] offen sc1\n" \
  "v_mfma_f32_16x16x32_bf16 v[228:231], v[216:219], v[24:27], v[228:231]\n" \
  "v_mfma_f32_16x16x32_bf16 v[224:227], v[204:207], v[28:31], v[224:227]\n" \
  "v_mfma_f32_16x16x32_bf16 v[228:231], v[220:223], v[28:31], v[228:231]\n" \
  "v_mfma_f32_16x16x32_bf16 v[232:235], v[192:195], v[144:147], 0\n" \
  "v_mfma_f32_16x16x32_bf16 v[236:239], v[208:211], v[144:147], 0\n" \
  "v_mfma_f32_16x16x32_bf16 v[232:235], v[200:203], v[152:155], v[232:235]\n" \
  "v_cvt_pk_bf16_f32 v224, v224, v228\n" \
  "v_cvt_pk_bf16_f32 v225, v225, v229\n" \
  "v_mfma_f32_16x16x32_bf16 v[236:239], v[216:219], v[152:155], v[236:239]\n" \
  "v_cvt_pk_bf16_f32 v226, v226, v230\n" \
  "v_cvt_pk_bf16_f32 v227, v227, v231\n" \
  "s_waitcnt lgkmcnt(1)\n" \
  "v_mfma_f32_16x16x32_bf16 v[232:235], v[196:199], v[240:243], v[232:235]\n" \
  "s_add_u32 %[cnt], %[so2], 1*%c[js]+0*%c[ss]\n" \
  "buffer_store_dwordx4 v[224:227], v251, %[rs], %[cnt] offen sc1\n" \
  "v_mfma_f32_16x16x32_bf16 v[236:239], v[212:215], v[240:243], v[236:239]\n" \
  "s_waitcnt lgkmcnt(0)\n" \
  "v_mfma_f32_16x16x32_bf16 v[232:235], v[204:207], v[244:247], v[232:235]\n" \
  "v_mfma_f32_16x16x32_bf16 v[236:239], v[220:223], v[244:247], v[236:239]\n" \
  "ds_read_b128 v[240:243], v250 offset:33792\n" \
  "ds_read_b128 v[244:247], v250 offset:50176\n" \
  "s_cmp_eq_u32 %[ob], 0\n" \
  "s_cbranch_scc1 .Lo6_%=\n" \
  "s_barrier\n" \
  ".Lo6_%=:\n" \
  "v_mfma_f32_16x16x32_bf16 v[224:227], v[192:195], v[80:83], 0\n" \
  "v_mfma_f32_16x16x32_bf16 v[228:231], v[208:211], v[80:83], 0\n" \
  "v_mfma_f32_16x16x32_bf16 v[224:227], v[196:199], v[84:87], v[224:227]\n" \
  "v_cvt_pk_bf16_f32 v232, v232, v236\n" \
  "v_cvt_pk_bf16_f32 v233, v233, v237\n" \
  "v_mfma_f32_16x16x32_bf16 v[228:231], v[212:215], v[84:87], v[228:231]\n" \
  "v_cvt_pk_bf16_f32 v234, v234, v238\n" \
  "v_cvt_pk_bf16_f32 v235, v235, v239\n" \
  "v_mfma_f32_16x16x32_bf16 v[224:227], v[200:203], v[88:91], v[224:227]\n" \
  "s_add_u32 %[cnt], %[so2], 5*%c[js]+0*%c[ss]\n" \
  "buffer_store_dwordx4 v[232:235], v251, %[rs], %[cnt] offen sc1\n" \
  "v_mfma_f32_16x16x32_bf16 v[228:231], v[216:219], v[88:91], v[228:231]\n" \
  "v_mfma_f32_16x16x32_bf16 v[224:227], v[204:207], v[92:95], v[224:227]\n" \
  "v_mfma_f32_16x16x32_bf16 v[228:231], v[220:223], v[92:95], v[228:231]\n" \
  "v_mfma_f32_16x16x32_bf16 v[232:235], v[192:195], v[148:151], 0\n" \
  "v_mfma_f32_16x16x32_bf16 v[236:239], v[208:211], v[148:151], 0\n" \
  "v_mfma_f32_16x16x32_bf16 v[232:235], v[200:203], v[156:159], v[232:235]\n" \
  "v_cvt_pk_bf16_f32 v224, v224, v228\n" \
  "v_cvt_pk_bf16_f32 v225, v225, v229\n" \
  "v_mfma_f32_16x16x32_bf16 v[236:239], v[216:219], v[156:159], v[236:239]\n" \
  "v_cvt_pk_bf16_f32 v226, v226, v230\n" \
  "v_cvt_pk_bf16_f32 v227, v227, v231\n" \
  "s_waitcnt lgkmcnt(1)\n" \
  "v_mfma_f32_16x16x32_bf16 v[232:235], v[196:199], v[240:243], v[232:235]\n" \
  "s_add_u32 %[cnt], %[so2], 1*%c[js]+1*%c[ss]\n" \
  "buffer_store_dwordx4 v[224:227], v251, %[rs], %[cnt] offen sc1\n" \
  "v_mfma_f32_16x16x32_bf16 v[236:239], v[212:215], v[240:243], v[236:239]\n" \
  "s_waitcnt lgkmcnt(0)\n" \
  "v_mfma_f32_16x16x32_bf16 v[232:235], v[204:207], v[244:247], v[232:235]\n" \
  "v_mfma_f32_16x16x32_bf16 v[236:239], v[220:223], v[244:247], v[236:239]\n" \
  "ds_read_b128 v[240:243], v249 offset:0\n" \
  "ds_read_b128 v[244:247], v249 offset:16384\n" \
  "s_cmp_eq_u32 %[ob], 0\n" \
  "s_cbranch_scc1 .Lo8_%=\n" \
  "s_barrier\n" \
  ".Lo8_%=:\n" \
  "v_mfma_f32_16x16x32_bf16 v[224:227], v[192:195], v[32:35], 0\n" \
  "v_mfma_f32_16x16x32_bf16 v[228:231], v[208:211], v[32:35], 0\n" \
  "v_mfma_f32_16x16x32_bf16 v[224:227], v[196:199], v[36:39], v[224:227]\n" \
  "v_cvt_pk_bf16_f32 v232, v232, v236\n" \
  "v_cvt_pk_bf16_f32 v233, v233, v237\n" \
  "v_mfma_f32_16x16x32_bf16 v[228:231], v[212:215], v[36:39], v[228:231]\n" \
  "v_cvt_pk_bf16_f32 v234, v234, v238\n" \
  "v_cvt_pk_bf16_f32 v235, v235, v239\n" \
  "v_mfma_f32_16x16x32_bf16 v[224:227], v[200:203], v[40:43], v[224:227]\n" \
  "s_add_u32 %[cnt], %[so2], 5*%c[js]+1*%c[ss]\n" \
  "buffer_store_dwordx4 v[232:235], v251, %[rs], %[cnt] offen sc1\n" \
  "v_mfma_f32_16x16x32_bf16 v[228:231], v[216:219], v[40:43], v[228:231]\n" \
  "v_mfma_f32_16x16x32_bf16 v[224:227], v[204:207], v[44:47], v[224:227]\n" \
  "v_mfma_f32_16x16x32_bf16 v[228:231], v[220:223], v[44:47], v[228:231]\n" \
  "v_mfma_f32_16x16x32_bf16 v[232:235], v[192:195], v[160:163], 0\n" \
  "v_mfma_f32_16x16x32_bf16 v[236:239], v[208:211], v[160:163], 0\n" \
  "v_mfma_f32_16x16x32_bf16 v[232:235], v[200:203], v[168:171], v[232:235]\n" \
  "v_cvt_pk_bf16_f32 v224, v224, v228\n" \
  "v_cvt_pk_bf16_f32 v225, v225, v229\n" \
  "v_mfma_f32_16x16x32_bf16 v[236:239], v[216:219], v[168:171], v[236:239]\n" \
  "v_cvt_pk_bf16_f32 v226, v226, v230\n" \
  "v_cvt_pk_bf16_f32 v227, v227, v231\n" \
  "s_waitcnt lgkmcnt(1)\n" \
  "v_mfma_f32_16x16x32_bf16 v[232:235], v[196:199], v[240:243], v[232:235]\n" \
  "s_add_u32 %[cnt], %[so], 2*%c[js]+0*%c[ss]\n" \
  "buffer_store_dwordx4 v[224:227], v251, %[rs], %[cnt] offen sc1\n" \
  "v_mfma_f32_16x16x32_bf16 v[236:239], v[212:215], v[240:243], v[236:239]\n" \
  "s_waitcnt lgkmcnt(0)\n" \
  "v_mfma_f32_16x16x32_bf16 v[232:235], v[204:207], v[244:247], v[232:235]\n" \
  "v_mfma_f32_16x16x32_bf16 v[236:239], v[220:223], v[244:247], v[236:239]\n" \
  "ds_read_b128 v[240:243], v249 offset:1024\n" \
  "ds_read_b128 v[244:247], v249 offset:17408\n" \
  "v_mfma_f32_16x16x32_bf16 v[224:227], v[192:195], v[96:99], 0\n" \
  "v_mfma_f32_16x16x32_bf16 v[228:231], v[208:211], v[96:99], 0\n" \
  "v_mfma_f32_16x16x32_bf16 v[224:227], v[196:199], v[100:103], v[224:227]\n" \
  "v_cvt_pk_bf16_f32 v232, v232, v236\n" \
  "v_cvt_pk_bf16_f32 v233, v233, v237\n" \
  "v_mfma_f32_16x16x32_bf16 v[228:231], v[212:215], v[100:103], v[228:231]\n" \
  "v_cvt_pk_bf16_f32 v234, v234, v238\n" \
  "v_cvt_pk_bf16_f32 v235, v235, v239\n" \
  "v_mfma_f32_16x16x32_bf16 v[224:227], v[200:203], v[104:107], v[224:227]\n" \
  "s_add_u32 %[cnt], %[so], 6*%c[js]+0*%c[ss]\n" \
  "buffer_store_dwordx4 v[232:235], v251, %[rs], %[cnt] offen sc1\n" \
  "v_mfma_f32_16x16x32_bf16 v[228:231], v[216:219], v[104:107], v[228:231]\n" \
  "v_mfma_f32_16x16x32_bf16 v[224:227], v[204:207], v[108:111], v[224:227]\n" \
  "v_mfma_f32_16x16x32_bf16 v[228:231], v[220:223], v[108:111], v[228:231]\n" \
  "v_mfma_f32_16x16x32_bf16 v[232:235], v[192:195], v[164:167], 0\n" \
  "v_mfma_f32_16x16x32_bf16 v[236:239], v[208:211], v[164:167], 0\n" \
  "v_mfma_f32_16x16x32_bf16 v[232:235], v[200:203], v[172:175], v[232:235]\n" \
  "v_cvt_pk_bf16_f32 v224, v224, v228\n" \
  "v_cvt_pk_bf16_f32 v225, v225, v229\n" \
  "v_mfma_f32_16x16x32_bf16 v[236:239], v[216:219], v[172:175], v[236:239]\n" \
  "v_cvt_pk_bf16_f32 v226, v226, v230\n" \
  "v_cvt_pk_bf16_f32 v227, v227, v231\n" \
  "s_waitcnt lgkmcnt(1)\n" \
  "v_mfma_f32_16x16x32_bf16 v[232:235], v[196:199], v[240:243], v[232:235]\n" \
  "s_add_u32 %[cnt], %[so], 2*%c[js]+1*%c[ss]\n" \
  "buffer_store_dwordx4 v[224:227], v251, %[rs], %[cnt] offen sc1\n" \
  "v_mfma_f32_16x16x32_bf16 v[236:239], v[212:215], v[240:243], v[236:239]\n" \
  "s_waitcnt lgkmcnt(0)\n" \
  "v_mfma_f32_16x16x32_bf16 v[232:235], v[204:207], v[244:247], v[232:235]\n" \
  "v_mfma_f32_16x16x32_bf16 v[236:239], v[220:223], v[244:247], v[236:239]\n" \
  "ds_read_b128 v[240:243], v249 offset:32768\n" \
  "ds_read_b128 v[244:247], v249 offset:49152\n" \
  "s_cmp_eq_u32 %[ob], 0\n" \
  "s_cbranch_scc1 .Lo12_%=\n" \
  "s_barrier\n" \
  ".Lo12_%=:\n" \
  "v_mfma_f32_16x16x32_bf16 v[224:227], v[192:195], v[48:51], 0\n" \
  "v_mfma_f32_16x16x32_bf16 v[228:231], v[208:211], v[48:51], 0\n" \
  "v_mfma_f32_16x16x32_bf16 v[224:227], v[196:199], v[52:55], v[224:227]\n" \
  "v_cvt_pk_bf16_f32 v232, v232, v236\n" \
  "v_cvt_pk_bf16_f32 v233, v233, v237\n" \
  "v_mfma_f32_16x16x32_bf16 v[228:231], v[212:215], v[52:55], v[228:231]\n" \
  "v_cvt_pk_bf16_f32 v234, v234, v238\n" \
  "v_cvt_pk_bf16_f32 v235, v235, v239\n" \
  "v_mfma_f32_16x16x32_bf16 v[224:227], v[200:203], v[56:59], v[224:227]\n" \
  "s_add_u32 %[cnt], %[so], 6*%c[js]+1*%c[ss]\n" \
  "buffer_store_dwordx4 v[232:235], v251, %[rs], %[cnt] offen sc1\n" \
  "v_mfma_f32_16x16x32_bf16 v[228:231], v[216:219], v[56:59], v[228:231]\n" \
  "v_mfma_f32_16x16x32_bf16 v[224:227], v[204:207], v[60:63], v[224:227]\n" \
  "v_mfma_f32_16x16x32_bf16 v[228:231], v[220:223], v[60:63], v[228:231]\n" \
  "v_mfma_f32_16x16x32_bf16 v[232:235], v[192:195], v[176:179], 0\n" \
  "v_mfma_f32_16x16x32_bf16 v[236:239], v[208:211], v[176:179], 0\n" \
  "v_mfma_f32_16x16x32_bf16 v[232:235], v[200:203], v[184:187], v[232:235]\n" \
  "v_cvt_pk_bf16_f32 v224, v224, v228\n" \
  "v_cvt_pk_bf16_f32 v225, v225, v229\n" \
  "v_mfma_f32_16x16x32_bf16 v[236:239], v[216:219], v[184:187], v[236:239]\n" \
  "v_cvt_pk_bf16_f32 v226, v226, v230\n" \
  "v_cvt_pk_bf16_f32 v227, v227, v231\n" \
  "s_waitcnt lgkmcnt(1)\n" \
  "v_mfma_f32_16x16x32_bf16 v[232:235], v[196:199], v[240:243], v[232:235]\n" \
  "s_add_u32 %[cnt], %[so2], 3*%c[js]+0*%c[ss]\n" \
  "buffer_store_dwordx4 v[224:227], v251, %[rs], %[cnt] offen sc1\n" \
  "v_mfma_f32_16x16x32_bf16 v[236:239], v[212:215], v[240:243], v[236:239]\n" \
  "s_waitcnt lgkmcnt(0)\n" \
  "v_mfma_f32_16x16x32_bf16 v[232:235], v[204:207], v[244:247], v[232:235]\n" \
  "v_mfma_f32_16x16x32_bf16 v[236:239], v[220:223], v[244:247], v[236:239]\n" \
  "ds_read_b128 v[240:243], v249 offset:33792\n" \
  "ds_read_b128 v[244:247], v249 offset:50176\n" \
  "v_mfma_f32_16x16x32_bf16 v[224:227], v[192:195], v[112:115], 0\n" \
  "v_mfma_f32_16x16x32_bf16 v[228:231], v[208:211], v[112:115], 0\n" \
  "v_mfma_f32_16x16x32_bf16 v[224:227], v[196:199], v[116:119], v[224:227]\n" \
  "v_cvt_pk_bf16_f32 v232, v232, v236\n" \
  "v_cvt_pk_bf16_f32 v233, v233, v237\n" \
  "v_mfma_f32_16x16x32_bf16 v[228:231], v[212:215], v[116:119], v[228:231]\n" \
  "v_cvt_pk_bf16_f32 v234, v234, v238\n" \
  "v_cvt_pk_bf16_f32 v235, v235, v239\n" \
  "v_mfma_f32_16x16x32_bf16 v[224:227], v[200:203], v[120:123], v[224:227]\n" \
  "s_add_u32 %[cnt], %[so2], 7*%c[js]+0*%c[ss]\n" \
  "buffer_store_dwordx4 v[232:235], v251, %[rs], %[cnt] offen sc1\n" \
  "v_mfma_f32_16x16x32_bf16 v[228:231], v[216:219], v[120:123], v[228:231]\n" \
  "v_mfma_f32_16x16x32_bf16 v[224:227], v[204:207], v[124:127], v[224:227]\n" \
  "v_mfma_f32_16x16x32_bf16 v[228:231], v[220:223], v[124:127], v[228:231]\n" \
  "v_mfma_f32_16x16x32_bf16 v[232:235], v[192:195], v[180:183], 0\n" \
  "v_mfma_f32_16x16x32_bf16 v[236:239], v[208:211], v[180:183], 0\n" \
  "v_mfma_f32_16x16x32_bf16 v[232:235], v[200:203], v[188:191], v[232:235]\n" \
  "v_cvt_pk_bf16_f32 v224, v224, v228\n" \
  "v_cvt_pk_bf16_f32 v225, v225, v229\n" \
  "v_mfma_f32_16x16x32_bf16 v[236:239], v[216:219], v[188:191], v[236:239]\n" \
  "v_cvt_pk_bf16_f32 v226, v226, v230\n" \
  "v_cvt_pk_bf16_f32 v227, v227, v231\n" \
  "s_waitcnt lgkmcnt(1)\n" \
  "v_mfma_f32_16x16x32_bf16 v[232:235], v[196:199], v[240:243], v[232:235]\n" \
  "s_add_u32 %[cnt], %[so2], 3*%c[js]+1*%c[ss]\n" \
  "buffer_store_dwordx4 v[224:227], v251, %[rs], %[cnt] offen sc1\n" \
  "v_mfma_f32_16x16x32_bf16 v[236:239], v[212:215], v[240:243], v[236:239]\n" \
  "s_waitcnt lgkmcnt(0)\n" \
  "v_mfma_f32_16x16x32_bf16 v[232:235], v[204:207], v[244:247], v[232:235]\n" \
  "v_mfma_f32_16x16x32_bf16 v[236:239], v[220:223], v[244:247], v[236:239]\n" \
  "s_nop 8\n" \
  "v_cvt_pk_bf16_f32 v232, v232, v236\n" \
  "v_cvt_pk_bf16_f32 v233, v233, v237\n" \
  "v_cvt_pk_bf16_f32 v234, v234, v238\n" \
  "v_cvt_pk_bf16_f32 v235, v235, v239\n" \
  "s_add_u32 %[cnt], %[so2], 7*%c[js]+1*%c[ss]\n" \
  "buffer_store_dwordx4 v[232:235], v251, %[rs], %[cnt] offen sc1\n" \
  "s_setprio 0\n" \
  "s_nop 1\n"
#define PRO0_0 \
  "s_cmp_lg_u32 %[hb], 0\n" \
  "s_cbranch_scc1 .Ls_%=\n" \
  "v_xor_b32 %[t0], 0x0, %[pb]\n" \
  "ds_read_b64_tr_b16 v[12:13], %[t0] offset:0\n" \
  "ds_read_b64_tr_b16 v[96:97], %[t0] offset:16384\n" \
  "v_xor_b32 %[t1], 0x20, %[pb]\n" \
  "ds_read_b64_tr_b16 v[28:29], %[t1] offset:0\n" \
  "ds_read_b64_tr_b16 v[144:145], %[t1] offset:16384\n" \
  "v_xor_b32 %[t0], 0x1000, %[pb]\n" \
  "ds_read_b64_tr_b16 v[0:1], %[t0] offset:0\n" \
  "ds_read_b64_tr_b16 v[192:193], %[t0] offset:16384\n" \
  "v_xor_b32 %[t1], 0x1020, %[pb]\n" \
  "ds_read_b64_tr_b16 v[48:49], %[t1] offset:0\n" \
  "ds_read_b64_tr_b16 v[200:201], %[t1] offset:16384\n" \
  "v_xor_b32 %[t0], 0x2000, %[pb]\n" \
  "ds_read_b64_tr_b16 v[16:17], %[t0] offset:0\n" \
  "ds_read_b64_tr_b16 v[208:209], %[t0] offset:16384\n" \
  "v_xor_b32 %[t1], 0x2020, %[pb]\n" \
  "ds_read_b64_tr_b16 v[64:65], %[t1] offset:0\n" \
  "ds_read_b64_tr_b16 v[216:217], %[t1] offset:16384\n" \
  "v_xor_b32 %[t0], 0x3000, %[pb]\n" \
  "ds_read_b64_tr_b16 v[32:33], %[t0] offset:0\n" \
  "ds_read_b64_tr_b16 v[128:129], %[t0] offset:16384\n" \
  "v_xor_b32 %[t1], 0x3020, %[pb]\n" \
  "ds_read_b64_tr_b16 v[80:81], %[t1] offset:0\n" \
  "ds_read_b64_tr_b16 v[176:177], %[t1] offset:16384\n" \
  ".Ls_%=:\n" \
  "s_waitcnt lgkmcnt(0)\n"
#define PRO0_1 \
  "s_cmp_lg_u32 %[hb], 1\n" \
  "s_cbranch_scc1 .Ls_%=\n" \
  "v_xor_b32 %[t0], 0x8000, %[pb]\n" \
  "ds_read_b64_tr_b16 v[12:13], %[t0] offset:0\n" \
  "ds_read_b64_tr_b16 v[96:97], %[t0] offset:16384\n" \
  "v_xor_b32 %[t1], 0x8020, %[pb]\n" \
  "ds_read_b64_tr_b16 v[28:29], %[t1] offset:0\n" \
  "ds_read_b64_tr_b16 v[144:145], %[t1] offset:16384\n" \
  "v_xor_b32 %[t0], 0x9000, %[pb]\n" \
  "ds_read_b64_tr_b16 v[0:1], %[t0] offset:0\n" \
  "ds_read_b64_tr_b16 v[192:193], %[t0] offset:16384\n" \
  "v_xor_b32 %[t1], 0x9020, %[pb]\n" \
  "ds_read_b64_tr_b16 v[48:49], %[t1] offset:0\n" \
  "ds_read_b64_tr_b16 v[200:201], %[t1] offset:16384\n" \
  "v_xor_b32 %[t0], 0xa000, %[pb]\n" \
  "ds_read_b64_tr_b16 v[16:17], %[t0] offset:0\n" \
  "ds_read_b64_tr_b16 v[208:209], %[t0] offset:16384\n" \
  "v_xor_b32 %[t1], 0xa020, %[pb]\n" \
  "ds_read_b64_tr_b16 v[64:65], %[t1] offset:0\n" \
  "ds_read_b64_tr_b16 v[216:217], %[t1] offset:16384\n" \
  "v_xor_b32 %[t0], 0xb000, %[pb]\n" \
  "ds_read_b64_tr_b16 v[32:33], %[t0] offset:0\n" \
  "ds_read_b64_tr_b16 v[128:129], %[t0] offset:16384\n" \
  "v_xor_b32 %[t1], 0xb020, %[pb]\n" \
  "ds_read_b64_tr_b16 v[80:81], %[t1] offset:0\n" \
  "ds_read_b64_tr_b16 v[176:177], %[t1] offset:16384\n" \
  ".Ls_%=:\n" \
  "s_waitcnt lgkmcnt(0)\n"
#define PRO0_2 \
  "s_cmp_lg_u32 %[hb], 0\n" \
  "s_cbranch_scc1 .Ls_%=\n" \
  "v_xor_b32 %[t0], 0x10000, %[pb]\n" \
  "ds_read_b64_tr_b16 v[14:15], %[t0] offset:0\n" \
  "ds_read_b64_tr_b16 v[98:99], %[t0] offset:16384\n" \
  "v_xor_b32 %[t1], 0x10020, %[pb]\n" \
  "ds_read_b64_tr_b16 v[30:31], %[t1] offset:0\n" \
  "ds_read_b64_tr_b16 v[146:147], %[t1] offset:16384\n" \
  "v_xor_b32 %[t0], 0x11000, %[pb]\n" \
  "ds_read_b64_tr_b16 v[2:3], %[t0] offset:0\n" \
  "ds_read_b64_tr_b16 v[194:195], %[t0] offset:16384\n" \
  "v_xor_b32 %[t1], 0x11020, %[pb]\n" \
  "ds_read_b64_tr_b16 v[50:51], %[t1] offset:0\n" \
  "ds_read_b64_tr_b16 v[202:203], %[t1] offset:16384\n" \
  "v_xor_b32 %[t0], 0x12000, %[pb]\n" \
  "ds_read_b64_tr_b16 v[18:19], %[t0] offset:0\n" \
  "ds_read_b64_tr_b16 v[210:211], %[t0] offset:16384\n" \
  "v_xor_b32 %[t1], 0x12020, %[pb]\n" \
  "ds_read_b64_tr_b16 v[66:67], %[t1] offset:0\n" \
  "ds_read_b64_tr_b16 v[218:219], %[t1] offset:16384\n" \
  "v_xor_b32 %[t0], 0x13000, %[pb]\n" \
  "ds_read_b64_tr_b16 v[34:35], %[t0] offset:0\n" \
  "ds_read_b64_tr_b16 v[130:131], %[t0] offset:16384\n" \
  "v_xor_b32 %[t1], 0x13020, %[pb]\n" \
  "ds_read_b64_tr_b16 v[82:83], %[t1] offset:0\n" \
  "ds_read_b64_tr_b16 v[178:179], %[t1] offset:16384\n" \
  ".Ls_%=:\n" \
  "s_waitcnt lgkmcnt(0)\n"
#define PRO0_3 \
  "s_cmp_lg_u32 %[hb], 1\n" \
  "s_cbranch_scc1 .Ls_%=\n" \
  "v_xor_b32 %[t0], 0x18000, %[pb]\n" \
  "ds_read_b64_tr_b16 v[14:15], %[t0] offset:0\n" \
  "ds_read_b64_tr_b16 v[98:99], %[t0] offset:16384\n" \
  "v_xor_b32 %[t1], 0x18020, %[pb]\n" \
  "ds_read_b64_tr_b16 v[30:31], %[t1] offset:0\n" \
  "ds_read_b64_tr_b16 v[146:147], %[t1] offset:16384\n" \
  "v_xor_b32 %[t0], 0x19000, %[pb]\n" \
  "ds_read_b64_tr_b16 v[2:3], %[t0] offset:0\n" \
  "ds_read_b64_tr_b16 v[194:195], %[t0] offset:16384\n" \
  "v_xor_b32 %[t1], 0x19020, %[pb]\n" \
  "ds_read_b64_tr_b16 v[50:51], %[t1] offset:0\n" \
  "ds_read_b64_tr_b16 v[202:203], %[t1] offset:16384\n" \
  "v_xor_b32 %[t0], 0x1a000, %[pb]\n" \
  "ds_read_b64_tr_b16 v[18:19], %[t0] offset:0\n" \
  "ds_read_b64_tr_b16 v[210:211], %[t0] offset:16384\n" \
  "v_xor_b32 %[t1], 0x1a020, %[pb]\n" \
  "ds_read_b64_tr_b16 v[66:67], %[t1] offset:0\n" \
  "ds_read_b64_tr_b16 v[218:219], %[t1] offset:16384\n" \
  "v_xor_b32 %[t0], 0x1b000, %[pb]\n" \
  "ds_read_b64_tr_b16 v[34:35], %[t0] offset:0\n" \
  "ds_read_b64_tr_b16 v[130:131], %[t0] offset:16384\n" \
  "v_xor_b32 %[t1], 0x1b020, %[pb]\n" \
  "ds_read_b64_tr_b16 v[82:83], %[t1] offset:0\n" \
  "ds_read_b64_tr_b16 v[178:179], %[t1] offset:16384\n" \
  ".Ls_%=:\n" \
  "s_waitcnt lgkmcnt(0)\n"
#define PRO0_4 \
  "s_cmp_lg_u32 %[hb], 0\n" \
  "s_cbranch_scc1 .Ls_%=\n" \
  "v_xor_b32 %[t0], 0x20000, %[pb]\n" \
  "ds_read_b64_tr_b16 v[60:61], %[t0] offset:0\n" \
  "ds_read_b64_tr_b16 v[100:101], %[t0] offset:16384\n" \
  "v_xor_b32 %[t1], 0x20020, %[pb]\n" \
  "ds_read_b64_tr_b16 v[76:77], %[t1] offset:0\n" \
  "ds_read_b64_tr_b16 v[148:149], %[t1] offset:16384\n" \
  "v_xor_b32 %[t0], 0x21000, %[pb]\n" \
  "ds_read_b64_tr_b16 v[4:5], %[t0] offset:0\n" \
  "ds_read_b64_tr_b16 v[196:197], %[t0] offset:16384\n" \
  "v_xor_b32 %[t1], 0x21020, %[pb]\n" \
  "ds_read_b64_tr_b16 v[52:53], %[t1] offset:0\n" \
  "ds_read_b64_tr_b16 v[204:205], %[t1] offset:16384\n" \
  "v_xor_b32 %[t0], 0x22000, %[pb]\n" \
  "ds_read_b64_tr_b16 v[20:21], %[t0] offset:0\n" \
  "ds_read_b64_tr_b16 v[212:213], %[t0] offset:16384\n" \
  "v_xor_b32 %[t1], 0x22020, %[pb]\n" \
  "ds_read_b64_tr_b16 v[68:69], %[t1] offset:0\n" \
  "ds_read_b64_tr_b16 v[220:221], %[t1] offset:16384\n" \
  "v_xor_b32 %[t0], 0x23000, %[pb]\n" \
  "ds_read_b64_tr_b16 v[36:37], %[t0] offset:0\n" \
  "ds_read_b64_tr_b16 v[132:133], %[t0] offset:16384\n" \
  "v_xor_b32 %[t1], 0x23020, %[pb]\n" \
  "ds_read_b64_tr_b16 v[84:85], %[t1] offset:0\n" \
  "ds_read_b64_tr_b16 v[180:181], %[t1] offset:16384\n" \
  ".Ls_%=:\n" \
  "s_waitcnt lgkmcnt(0)\n"
#define PRO0_5 \
  "s_cmp_lg_u32 %[hb], 1\n" \
  "s_cbranch_scc1 .Ls_%=\n" \
  "v_xor_b32 %[t0], 0x0, %[pb]\n" \
  "ds_read_b64_tr_b16 v[60:61], %[t0] offset:0\n" \
  "ds_read_b64_tr_b16 v[100:101], %[t0] offset:16384\n" \
  "v_xor_b32 %[t1], 0x20, %[pb]\n" \
  "ds_read_b64_tr_b16 v[76:77], %[t1] offset:0\n" \
  "ds_read_b64_tr_b16 v[148:149], %[t1] offset:16384\n" \
  "v_xor_b32 %[t0], 0x1000, %[pb]\n" \
  "ds_read_b64_tr_b16 v[4:5], %[t0] offset:0\n" \
  "ds_read_b64_tr_b16 v[196:197], %[t0] offset:16384\n" \
  "v_xor_b32 %[t1], 0x1020, %[pb]\n" \
  "ds_read_b64_tr_b16 v[52:53], %[t1] offset:0\n" \
  "ds_read_b64_tr_b16 v[204:205], %[t1] offset:16384\n" \
  "v_xor_b32 %[t0], 0x2000, %[pb]\n" \
  "ds_read_b64_tr_b16 v[20:21], %[t0] offset:0\n" \
  "ds_read_b64_tr_b16 v[212:213], %[t0] offset:16384\n" \
  "v_xor_b32 %[t1], 0x2020, %[pb]\n" \
  "ds_read_b64_tr_b16 v[68:69], %[t1] offset:0\n" \
  "ds_read_b64_tr_b16 v[220:221], %[t1] offset:16384\n" \
  "v_xor_b32 %[t0], 0x3000, %[pb]\n" \
  "ds_read_b64_tr_b16 v[36:37], %[t0] offset:0\n" \
  "ds_read_b64_tr_b16 v[132:133], %[t0] offset:16384\n" \
  "v_xor_b32 %[t1], 0x3020, %[pb]\n" \
  "ds_read_b64_tr_b16 v[84:85], %[t1] offset:0\n" \
  "ds_read_b64_tr_b16 v[180:181], %[t1] offset:16384\n" \
  ".Ls_%=:\n" \
  "s_waitcnt lgkmcnt(0)\n"
#define PRO0_6 \
  "s_cmp_lg_u32 %[hb], 0\n" \
  "s_cbranch_scc1 .Ls_%=\n" \
  "v_xor_b32 %[t0], 0x8000, %[pb]\n" \
  "ds_read_b64_tr_b16 v[62:63], %[t0] offset:0\n" \
  "ds_read_b64_tr_b16 v[102:103], %[t0] offset:16384\n" \
  "v_xor_b32 %[t1], 0x8020, %[pb]\n" \
  "ds_read_b64_tr_b16 v[78:79], %[t1] offset:0\n" \
  "ds_read_b64_tr_b16 v[150:151], %[t1] offset:16384\n" \
  "v_xor_b32 %[t0], 0x9000, %[pb]\n" \
  "ds_read_b64_tr_b16 v[6:7], %[t0] offset:0\n" \
  "ds_read_b64_tr_b16 v[198:199], %[t0] offset:16384\n" \
  "v_xor_b32 %[t1], 0x9020, %[pb]\n" \
  "ds_read_b64_tr_b16 v[54:55], %[t1] offset:0\n" \
  "ds_read_b64_tr_b16 v[206:207], %[t1] offset:16384\n" \
  "v_xor_b32 %[t0], 0xa000, %[pb]\n" \
  "ds_read_b64_tr_b16 v[22:23], %[t0] offset:0\n" \
  "ds_read_b64_tr_b16 v[214:215], %[t0] offset:16384\n" \
  "v_xor_b32 %[t1], 0xa020, %[pb]\n" \
  "ds_read_b64_tr_b16 v[70:71], %[t1] offset:0\n" \
  "ds_read_b64_tr_b16 v[222:223], %[t1] offset:16384\n" \
  "v_xor_b32 %[t0], 0xb000, %[pb]\n" \
  "ds_read_b64_tr_b16 v[38:39], %[t0] offset:0\n" \
  "ds_read_b64_tr_b16 v[134:135], %[t0] offset:16384\n" \
  "v_xor_b32 %[t1], 0xb020, %[pb]\n" \
  "ds_read_b64_tr_b16 v[86:87], %[t1] offset:0\n" \
  "ds_read_b64_tr_b16 v[182:183], %[t1] offset:16384\n" \
  ".Ls_%=:\n" \
  "s_waitcnt lgkmcnt(0)\n"
#define PRO0_7 \
  "s_cmp_lg_u32 %[hb], 1\n" \
  "s_cbranch_scc1 .Ls_%=\n" \
  "v_xor_b32 %[t0], 0x10000, %[pb]\n" \
  "ds_read_b64_tr_b16 v[62:63], %[t0] offset:0\n" \
  "ds_read_b64_tr_b16 v[102:103], %[t0] offset:16384\n" \
  "v_xor_b32 %[t1], 0x10020, %[pb]\n" \
  "ds_read_b64_tr_b16 v[78:79], %[t1] offset:0\n" \
  "ds_read_b64_tr_b16 v[150:151], %[t1] offset:16384\n" \
  "v_xor_b32 %[t0], 0x11000, %[pb]\n" \
  "ds_read_b64_tr_b16 v[6:7], %[t0] offset:0\n" \
  "ds_read_b64_tr_b16 v[198:199], %[t0] offset:16384\n" \
  "v_xor_b32 %[t1], 0x11020, %[pb]\n" \
  "ds_read_b64_tr_b16 v[54:55], %[t1] offset:0\n" \
  "ds_read_b64_tr_b16 v[206:207], %[t1] offset:16384\n" \
  "v_xor_b32 %[t0], 0x12000, %[pb]\n" \
  "ds_read_b64_tr_b16 v[22:23], %[t0] offset:0\n" \
  "ds_read_b64_tr_b16 v[214:215], %[t0] offset:16384\n" \
  "v_xor_b32 %[t1], 0x12020, %[pb]\n" \
  "ds_read_b64_tr_b16 v[70:71], %[t1] offset:0\n" \
  "ds_read_b64_tr_b16 v[222:223], %[t1] offset:16384\n" \
  "v_xor_b32 %[t0], 0x13000, %[pb]\n" \
  "ds_read_b64_tr_b16 v[38:39], %[t0] offset:0\n" \
  "ds_read_b64_tr_b16 v[134:135], %[t0] offset:16384\n" \
  "v_xor_b32 %[t1], 0x13020, %[pb]\n" \
  "ds_read_b64_tr_b16 v[86:87], %[t1] offset:0\n" \
  "ds_read_b64_tr_b16 v[182:183], %[t1] offset:16384\n" \
  ".Ls_%=:\n" \
  "s_waitcnt lgkmcnt(0)\n"
#define PRO0_8 \
  "s_cmp_lg_u32 %[hb], 0\n" \
  "s_cbranch_scc1 .Ls_%=\n" \
  "v_xor_b32 %[t0], 0x18000, %[pb]\n" \
  "ds_read_b64_tr_b16 v[224:225], %[t0] offset:0\n" \
  "ds_read_b64_tr_b16 v[104:105], %[t0] offset:16384\n" \
  "v_xor_b32 %[t1], 0x18020, %[pb]\n" \
  "ds_read_b64_tr_b16 v[232:233], %[t1] offset:0\n" \
  "ds_read_b64_tr_b16 v[152:153], %[t1] offset:16384\n" \
  "v_xor_b32 %[t0], 0x19000, %[pb]\n" \
  "ds_read_b64_tr_b16 v[8:9], %[t0] offset:0\n" \
  "ds_read_b64_tr_b16 v[112:113], %[t0] offset:16384\n" \
  "v_xor_b32 %[t1], 0x19020, %[pb]\n" \
  "ds_read_b64_tr_b16 v[56:57], %[t1] offset:0\n" \
  "ds_read_b64_tr_b16 v[160:161], %[t1] offset:16384\n" \
  "v_xor_b32 %[t0], 0x1a000, %[pb]\n" \
  "ds_read_b64_tr_b16 v[24:25], %[t0] offset:0\n" \
  "ds_read_b64_tr_b16 v[120:121], %[t0] offset:16384\n" \
  "v_xor_b32 %[t1], 0x1a020, %[pb]\n" \
  "ds_read_b64_tr_b16 v[72:73], %[t1] offset:0\n" \
  "ds_read_b64_tr_b16 v[168:169], %[t1] offset:16384\n" \
  "v_xor_b32 %[t0], 0x1b000, %[pb]\n" \
  "ds_read_b64_tr_b16 v[40:41], %[t0] offset:0\n" \
  "ds_read_b64_tr_b16 v[136:137], %[t0] offset:16384\n" \
  "v_xor_b32 %[t1], 0x1b020, %[pb]\n" \
  "ds_read_b64_tr_b16 v[88:89], %[t1] offset:0\n" \
  "ds_read_b64_tr_b16 v[184:185], %[t1] offset:16384\n" \
  ".Ls_%=:\n" \
  "s_waitcnt lgkmcnt(0)\n"
#define PRO0_9 \
  "s_cmp_lg_u32 %[hb], 1\n" \
  "s_cbranch_scc1 .Ls_%=\n" \
  "v_xor_b32 %[t0], 0x20000, %[pb]\n" \
  "ds_read_b64_tr_b16 v[224:225], %[t0] offset:0\n" \
  "ds_read_b64_tr_b16 v[104:105], %[t0] offset:16384\n" \
  "v_xor_b32 %[t1], 0x20020, %[pb]\n" \
  "ds_read_b64_tr_b16 v[232:233], %[t1] offset:0\n" \
  "ds_read_b64_tr_b16 v[152:153], %[t1] offset:16384\n" \
  "v_xor_b32 %[t0], 0x21000, %[pb]\n" \
  "ds_read_b64_tr_b16 v[8:9], %[t0] offset:0\n" \
  "ds_read_b64_tr_b16 v[112:113], %[t0] offset:16384\n" \
  "v_xor_b32 %[t1], 0x21020, %[pb]\n" \
  "ds_read_b64_tr_b16 v[56:57], %[t1] offset:0\n" \
  "ds_read_b64_tr_b16 v[160:161], %[t1] offset:16384\n" \
  "v_xor_b32 %[t0], 0x22000, %[pb]\n" \
  "ds_read_b64_tr_b16 v[24:25], %[t0] offset:0\n" \
  "ds_read_b64_tr_b16 v[120:121], %[t0] offset:16384\n" \
  "v_xor_b32 %[t1], 0x22020, %[pb]\n" \
  "ds_read_b64_tr_b16 v[72:73], %[t1] offset:0\n" \
  "ds_read_b64_tr_b16 v[168:169], %[t1] offset:16384\n" \
  "v_xor_b32 %[t0], 0x23000, %[pb]\n" \
  "ds_read_b64_tr_b16 v[40:41], %[t0] offset:0\n" \
  "ds_read_b64_tr_b16 v[136:137], %[t0] offset:16384\n" \
  "v_xor_b32 %[t1], 0x23020, %[pb]\n" \
  "ds_read_b64_tr_b16 v[88:89], %[t1] offset:0\n" \
  "ds_read_b64_tr_b16 v[184:185], %[t1] offset:16384\n" \
  ".Ls_%=:\n" \
  "s_waitcnt lgkmcnt(0)\n"
#define PRO0_10 \
  "s_cmp_lg_u32 %[hb], 0\n" \
  "s_cbranch_scc1 .Ls_%=\n" \
  "v_xor_b32 %[t0], 0x0, %[pb]\n" \
  "ds_read_b64_tr_b16 v[226:227], %[t0] offset:0\n" \
  "ds_read_b64_tr_b16 v[106:107], %[t0] offset:16384\n" \
  "v_xor_b32 %[t1], 0x20, %[pb]\n" \
  "ds_read_b64_tr_b16 v[234:235], %[t1] offset:0\n" \
  "ds_read_b64_tr_b16 v[154:155], %[t1] offset:16384\n" \
  "v_xor_b32 %[t0], 0x1000, %[pb]\n" \
  "ds_read_b64_tr_b16 v[10:11], %[t0] offset:0\n" \
  "ds_read_b64_tr_b16 v[114:115], %[t0] offset:16384\n" \
  "v_xor_b32 %[t1], 0x1020, %[pb]\n" \
  "ds_read_b64_tr_b16 v[58:59], %[t1] offset:0\n" \
  "ds_read_b64_tr_b16 v[162:163], %[t1] offset:16384\n" \
  "v_xor_b32 %[t0], 0x2000, %[pb]\n" \
  "ds_read_b64_tr_b16 v[26:27], %[t0] offset:0\n" \
  "ds_read_b64_tr_b16 v[122:123], %[t0] offset:16384\n" \
  "v_xor_b32 %[t1], 0x2020, %[pb]\n" \
  "ds_read_b64_tr_b16 v[74:75], %[t1] offset:0\n" \
  "ds_read_b64_tr_b16 v[170:171], %[t1] offset:16384\n" \
  "v_xor_b32 %[t0], 0x3000, %[pb]\n" \
  "ds_read_b64_tr_b16 v[42:43], %[t0] offset:0\n" \
  "ds_read_b64_tr_b16 v[138:139], %[t0] offset:16384\n" \
  "v_xor_b32 %[t1], 0x3020, %[pb]\n" \
  "ds_read_b64_tr_b16 v[90:91], %[t1] offset:0\n" \
  "ds_read_b64_tr_b16 v[186:187], %[t1] offset:16384\n" \
  ".Ls_%=:\n" \
  "s_waitcnt lgkmcnt(0)\n"
#define PRO0_11 \
  "s_cmp_lg_u32 %[hb], 1\n" \
  "s_cbranch_scc1 .Ls_%=\n" \
  "v_xor_b32 %[t0], 0x8000, %[pb]\n" \
  "ds_read_b64_tr_b16 v[226:227], %[t0] offset:0\n" \
  "ds_read_b64_tr_b16 v[106:107], %[t0] offset:16384\n" \
  "v_xor_b32 %[t1], 0x8020, %[pb]\n" \
  "ds_read_b64_tr_b16 v[234:235], %[t1] offset:0\n" \
  "ds_read_b64_tr_b16 v[154:155], %[t1] offset:16384\n" \
  "v_xor_b32 %[t0], 0x9000, %[pb]\n" \
  "ds_read_b64_tr_b16 v[10:11], %[t0] offset:0\n" \
  "ds_read_b64_tr_b16 v[114:115], %[t0] offset:16384\n" \
  "v_xor_b32 %[t1], 0x9020, %[pb]\n" \
  "ds_read_b64_tr_b16 v[58:59], %[t1] offset:0\n" \
  "ds_read_b64_tr_b16 v[162:163], %[t1] offset:16384\n" \
  "v_xor_b32 %[t0], 0xa000, %[pb]\n" \
  "ds_read_b64_tr_b16 v[26:27], %[t0] offset:0\n" \
  "ds_read_b64_tr_b16 v[122:123], %[t0] offset:16384\n" \
  "v_xor_b32 %[t1], 0xa020, %[pb]\n" \
  "ds_read_b64_tr_b16 v[74:75], %[t1] offset:0\n" \
  "ds_read_b64_tr_b16 v[170:171], %[t1] offset:16384\n" \
  "v_xor_b32 %[t0], 0xb000, %[pb]\n" \
  "ds_read_b64_tr_b16 v[42:43], %[t0] offset:0\n" \
  "ds_read_b64_tr_b16 v[138:139], %[t0] offset:16384\n" \
  "v_xor_b32 %[t1], 0xb020, %[pb]\n" \
  "ds_read_b64_tr_b16 v[90:91], %[t1] offset:0\n" \
  "ds_read_b64_tr_b16 v[186:187], %[t1] offset:16384\n" \
  ".Ls_%=:\n" \
  "s_waitcnt lgkmcnt(0)\n"
#define PRO0_12 \
  "v_add_u32 %[t0], 0x8000, %[b0]\n" \
  "ds_write_b128 %[t0], v[12:15]\n" \
  "ds_write_b128 %[t0], v[60:63] offset:1024\n" \
  "v_add_u32 %[t0], 0xc000, %[b0]\n" \
  "ds_write_b128 %[t0], v[28:31]\n" \
  "ds_write_b128 %[t0], v[76:79] offset:1024\n" \
  "s_cmp_lg_u32 %[hb], 0\n" \
  "s_cbranch_scc1 .Ls_%=\n" \
  "v_xor_b32 %[t0], 0x10000, %[pb]\n" \
  "ds_read_b64_tr_b16 v[228:229], %[t0] offset:0\n" \
  "ds_read_b64_tr_b16 v[108:109], %[t0] offset:16384\n" \
  "v_xor_b32 %[t1], 0x10020, %[pb]\n" \
  "ds_read_b64_tr_b16 v[236:237], %[t1] offset:0\n" \
  "ds_read_b64_tr_b16 v[156:157], %[t1] offset:16384\n" \
  "v_xor_b32 %[t0], 0x11000, %[pb]\n" \
  "ds_read_b64_tr_b16 v[12:13], %[t0] offset:0\n" \
  "ds_read_b64_tr_b16 v[116:117], %[t0] offset:16384\n" \
  "v_xor_b32 %[t1], 0x11020, %[pb]\n" \
  "ds_read_b64_tr_b16 v[60:61], %[t1] offset:0\n" \
  "ds_read_b64_tr_b16 v[164:165], %[t1] offset:16384\n" \
  "v_xor_b32 %[t0], 0x12000, %[pb]\n" \
  "ds_read_b64_tr_b16 v[28:29], %[t0] offset:0\n" \
  "ds_read_b64_tr_b16 v[124:125], %[t0] offset:16384\n" \
  "v_xor_b32 %[t1], 0x12020, %[pb]\n" \
  "ds_read_b64_tr_b16 v[76:77], %[t1] offset:0\n" \
  "ds_read_b64_tr_b16 v[172:173], %[t1] offset:16384\n" \
  "v_xor_b32 %[t0], 0x13000, %[pb]\n" \
  "ds_read_b64_tr_b16 v[44:45], %[t0] offset:0\n" \
  "ds_read_b64_tr_b16 v[140:141], %[t0] offset:16384\n" \
  "v_xor_b32 %[t1], 0x13020, %[pb]\n" \
  "ds_read_b64_tr_b16 v[92:93], %[t1] offset:0\n" \
  "ds_read_b64_tr_b16 v[188:189], %[t1] offset:16384\n" \
  ".Ls_%=:\n" \
  "s_waitcnt lgkmcnt(0)\n"
#define PRO0_13 \
  "v_add_u32 %[t0], 0x10000, %[b0]\n" \
  "ds_write_b128 %[t0], v[192:195]\n" \
  "ds_write_b128 %[t0], v[196:199] offset:1024\n" \
  "v_add_u32 %[t0], 0x14000, %[b0]\n" \
  "ds_write_b128 %[t0], v[200:203]\n" \
  "ds_write_b128 %[t0], v[204:207] offset:1024\n" \
  "s_cmp_lg_u32 %[hb], 1\n" \
  "s_cbranch_scc1 .Ls_%=\n" \
  "v_xor_b32 %[t0], 0x18000, %[pb]\n" \
  "ds_read_b64_tr_b16 v[228:229], %[t0] offset:0\n" \
  "ds_read_b64_tr_b16 v[108:109], %[t0] offset:16384\n" \
  "v_xor_b32 %[t1], 0x18020, %[pb]\n" \
  "ds_read_b64_tr_b16 v[236:237], %[t1] offset:0\n" \
  "ds_read_b64_tr_b16 v[156:157], %[t1] offset:16384\n" \
  "v_xor_b32 %[t0], 0x19000, %[pb]\n" \
  "ds_read_b64_tr_b16 v[12:13], %[t0] offset:0\n" \
  "ds_read_b64_tr_b16 v[116:117], %[t0] offset:16384\n" \
  "v_xor_b32 %[t1], 0x19020, %[pb]\n" \
  "ds_read_b64_tr_b16 v[60:61], %[t1] offset:0\n" \
  "ds_read_b64_tr_b16 v[164:165], %[t1] offset:16384\n" \
  "v_xor_b32 %[t0], 0x1a000, %[pb]\n" \
  "ds_read_b64_tr_b16 v[28:29], %[t0] offset:0\n" \
  "ds_read_b64_tr_b16 v[124:125], %[t0] offset:16384\n" \
  "v_xor_b32 %[t1], 0x1a020, %[pb]\n" \
  "ds_read_b64_tr_b16 v[76:77], %[t1] offset:0\n" \
  "ds_read_b64_tr_b16 v[172:173], %[t1] offset:16384\n" \
  "v_xor_b32 %[t0], 0x1b000, %[pb]\n" \
  "ds_read_b64_tr_b16 v[44:45], %[t0] offset:0\n" \
  "ds_read_b64_tr_b16 v[140:141], %[t0] offset:16384\n" \
  "v_xor_b32 %[t1], 0x1b020, %[pb]\n" \
  "ds_read_b64_tr_b16 v[92:93], %[t1] offset:0\n" \
  "ds_read_b64_tr_b16 v[188:189], %[t1] offset:16384\n" \
  ".Ls_%=:\n" \
  "s_waitcnt lgkmcnt(0)\n"
#define PRO0_14 \
  "v_add_u32 %[t0], 0x18000, %[b0]\n" \
  "ds_write_b128 %[t0], v[208:211]\n" \
  "ds_write_b128 %[t0], v[212:215] offset:1024\n" \
  "v_add_u32 %[t0], 0x1c000, %[b0]\n" \
  "ds_write_b128 %[t0], v[216:219]\n" \
  "ds_write_b128 %[t0], v[220:223] offset:1024\n" \
  "s_cmp_lg_u32 %[hb], 0\n" \
  "s_cbranch_scc1 .Ls_%=\n" \
  "v_xor_b32 %[t0], 0x20000, %[pb]\n" \
  "ds_read_b64_tr_b16 v[230:231], %[t0] offset:0\n" \
  "ds_read_b64_tr_b16 v[110:111], %[t0] offset:16384\n" \
  "v_xor_b32 %[t1], 0x20020, %[pb]\n" \
  "ds_read_b64_tr_b16 v[238:239], %[t1] offset:0\n" \
  "ds_read_b64_tr_b16 v[158:159], %[t1] offset:16384\n" \
  "v_xor_b32 %[t0], 0x21000, %[pb]\n" \
  "ds_read_b64_tr_b16 v[14:15], %[t0] offset:0\n" \
  "ds_read_b64_tr_b16 v[118:119], %[t0] offset:16384\n" \
  "v_xor_b32 %[t1], 0x21020, %[pb]\n" \
  "ds_read_b64_tr_b16 v[62:63], %[t1] offset:0\n" \
  "ds_read_b64_tr_b16 v[166:167], %[t1] offset:16384\n" \
  "v_xor_b32 %[t0], 0x22000, %[pb]\n" \
  "ds_read_b64_tr_b16 v[30:31], %[t0] offset:0\n" \
  "ds_read_b64_tr_b16 v[126:127], %[t0] offset:16384\n" \
  "v_xor_b32 %[t1], 0x22020, %[pb]\n" \
  "ds_read_b64_tr_b16 v[78:79], %[t1] offset:0\n" \
  "ds_read_b64_tr_b16 v[174:175], %[t1] offset:16384\n" \
  "v_xor_b32 %[t0], 0x23000, %[pb]\n" \
  "ds_read_b64_tr_b16 v[46:47], %[t0] offset:0\n" \
  "ds_read_b64_tr_b16 v[142:143], %[t0] offset:16384\n" \
  "v_xor_b32 %[t1], 0x23020, %[pb]\n" \
  "ds_read_b64_tr_b16 v[94:95], %[t1] offset:0\n" \
  "ds_read_b64_tr_b16 v[190:191], %[t1] offset:16384\n" \
  ".Ls_%=:\n" \
  "s_waitcnt lgkmcnt(0)\n"
#define PRO0_15 \
  "s_cmp_lg_u32 %[hb], 1\n" \
  "s_cbranch_scc1 .Ls_%=\n" \
  "v_xor_b32 %[t0], 0x0, %[pb]\n" \
  "ds_read_b64_tr_b16 v[230:231], %[t0] offset:0\n" \
  "ds_read_b64_tr_b16 v[110:111], %[t0] offset:16384\n" \
  "v_xor_b32 %[t1], 0x20, %[pb]\n" \
  "ds_read_b64_tr_b16 v[238:239], %[t1] offset:0\n" \
  "ds_read_b64_tr_b16 v[158:159], %[t1] offset:16384\n" \
  "v_xor_b32 %[t0], 0x1000, %[pb]\n" \
  "ds_read_b64_tr_b16 v[14:15], %[t0] offset:0\n" \
  "ds_read_b64_tr_b16 v[118:119], %[t0] offset:16384\n" \
  "v_xor_b32 %[t1], 0x1020, %[pb]\n" \
  "ds_read_b64_tr_b16 v[62:63], %[t1] offset:0\n" \
  "ds_read_b64_tr_b16 v[166:167], %[t1] offset:16384\n" \
  "v_xor_b32 %[t0], 0x2000, %[pb]\n" \
  "ds_read_b64_tr_b16 v[30:31], %[t0] offset:0\n" \
  "ds_read_b64_tr_b16 v[126:127], %[t0] offset:16384\n" \
  "v_xor_b32 %[t1], 0x2020, %[pb]\n" \
  "ds_read_b64_tr_b16 v[78:79], %[t1] offset:0\n" \
  "ds_read_b64_tr_b16 v[174:175], %[t1] offset:16384\n" \
  "v_xor_b32 %[t0], 0x3000, %[pb]\n" \
  "ds_read_b64_tr_b16 v[46:47], %[t0] offset:0\n" \
  "ds_read_b64_tr_b16 v[142:143], %[t0] offset:16384\n" \
  "v_xor_b32 %[t1], 0x3020, %[pb]\n" \
  "ds_read_b64_tr_b16 v[94:95], %[t1] offset:0\n" \
  "ds_read_b64_tr_b16 v[190:191], %[t1] offset:16384\n" \
  ".Ls_%=:\n" \
  "s_waitcnt lgkmcnt(0)\n" \
  "v_add_u32 %[t0], 0x20000, %[b0]\n" \
  "ds_write_b128 %[t0], v[224:227]\n" \
  "ds_write_b128 %[t0], v[228:231] offset:1024\n" \
  "v_add_u32 %[t0], 0x24000, %[b0]\n" \
  "ds_write_b128 %[t0], v[232:235]\n" \
  "ds_write_b128 %[t0], v[236:239] offset:1024\n"
#define PRO1_0 \
  "v_add_u32 %[t0], 0x0, %[pa]\n" \
  "ds_read_b64_tr_b16 v[96:97], %[t0] offset:0\n" \
  "ds_read_b64_tr_b16 v[100:101], %[t0] offset:1024\n" \
  "ds_read_b64_tr_b16 v[104:105], %[t0] offset:8192\n" \
  "ds_read_b64_tr_b16 v[108:109], %[t0] offset:9216\n" \
  "s_cmp_lg_u32 %[hb], 0\n" \
  "s_cbranch_scc1 .Ls_%=\n" \
  "v_xor_b32 %[t0], 0x0, %[pb]\n" \
  "ds_read_b64_tr_b16 v[192:193], %[t0] offset:0\n" \
  "v_xor_b32 %[t1], 0x80, %[pb]\n" \
  "ds_read_b64_tr_b16 v[200:201], %[t1] offset:0\n" \
  "v_xor_b32 %[t0], 0x1000, %[pb]\n" \
  "ds_read_b64_tr_b16 v[0:1], %[t0] offset:0\n" \
  "v_xor_b32 %[t1], 0x1080, %[pb]\n" \
  "ds_read_b64_tr_b16 v[48:49], %[t1] offset:0\n" \
  "v_xor_b32 %[t0], 0x2000, %[pb]\n" \
  "ds_read_b64_tr_b16 v[16:17], %[t0] offset:0\n" \
  "v_xor_b32 %[t1], 0x2080, %[pb]\n" \
  "ds_read_b64_tr_b16 v[64:65], %[t1] offset:0\n" \
  "v_xor_b32 %[t0], 0x3000, %[pb]\n" \
  "ds_read_b64_tr_b16 v[32:33], %[t0] offset:0\n" \
  "v_xor_b32 %[t1], 0x3080, %[pb]\n" \
  "ds_read_b64_tr_b16 v[80:81], %[t1] offset:0\n" \
  ".Ls_%=:\n" \
  "s_waitcnt lgkmcnt(0)\n"
#define PRO1_1 \
  "v_add_u32 %[t0], 0x8000, %[pa]\n" \
  "ds_read_b64_tr_b16 v[98:99], %[t0] offset:0\n" \
  "ds_read_b64_tr_b16 v[102:103], %[t0] offset:1024\n" \
  "ds_read_b64_tr_b16 v[106:107], %[t0] offset:8192\n" \
  "ds_read_b64_tr_b16 v[110:111], %[t0] offset:9216\n" \
  "s_cmp_lg_u32 %[hb], 1\n" \
  "s_cbranch_scc1 .Ls_%=\n" \
  "v_xor_b32 %[t0], 0x8000, %[pb]\n" \
  "ds_read_b64_tr_b16 v[192:193], %[t0] offset:0\n" \
  "v_xor_b32 %[t1], 0x8080, %[pb]\n" \
  "ds_read_b64_tr_b16 v[200:201], %[t1] offset:0\n" \
  "v_xor_b32 %[t0], 0x9000, %[pb]\n" \
  "ds_read_b64_tr_b16 v[0:1], %[t0] offset:0\n" \
  "v_xor_b32 %[t1], 0x9080, %[pb]\n" \
  "ds_read_b64_tr_b16 v[48:49], %[t1] offset:0\n" \
  "v_xor_b32 %[t0], 0xa000, %[pb]\n" \
  "ds_read_b64_tr_b16 v[16:17], %[t0] offset:0\n" \
  "v_xor_b32 %[t1], 0xa080, %[pb]\n" \
  "ds_read_b64_tr_b16 v[64:65], %[t1] offset:0\n" \
  "v_xor_b32 %[t0], 0xb000, %[pb]\n" \
  "ds_read_b64_tr_b16 v[32:33], %[t0] offset:0\n" \
  "v_xor_b32 %[t1], 0xb080, %[pb]\n" \
  "ds_read_b64_tr_b16 v[80:81], %[t1] offset:0\n" \
  ".Ls_%=:\n" \
  "s_waitcnt lgkmcnt(0)\n" \
  "v_swap_b32 v97, v98\n" \
  "v_swap_b32 v101, v102\n" \
  "v_swap_b32 v105, v106\n" \
  "v_swap_b32 v109, v110\n"
#define PRO1_2 \
  "v_add_u32 %[t0], 0x10000, %[pa]\n" \
  "ds_read_b64_tr_b16 v[112:113], %[t0] offset:0\n" \
  "ds_read_b64_tr_b16 v[116:117], %[t0] offset:1024\n" \
  "ds_read_b64_tr_b16 v[120:121], %[t0] offset:8192\n" \
  "ds_read_b64_tr_b16 v[124:125], %[t0] offset:9216\n" \
  "s_cmp_lg_u32 %[hb], 0\n" \
  "s_cbranch_scc1 .Ls_%=\n" \
  "v_xor_b32 %[t0], 0x10000, %[pb]\n" \
  "ds_read_b64_tr_b16 v[194:195], %[t0] offset:0\n" \
  "v_xor_b32 %[t1], 0x10080, %[pb]\n" \
  "ds_read_b64_tr_b16 v[202:203], %[t1] offset:0\n" \
  "v_xor_b32 %[t0], 0x11000, %[pb]\n" \
  "ds_read_b64_tr_b16 v[2:3], %[t0] offset:0\n" \
  "v_xor_b32 %[t1], 0x11080, %[pb]\n" \
  "ds_read_b64_tr_b16 v[50:51], %[t1] offset:0\n" \
  "v_xor_b32 %[t0], 0x12000, %[pb]\n" \
  "ds_read_b64_tr_b16 v[18:19], %[t0] offset:0\n" \
  "v_xor_b32 %[t1], 0x12080, %[pb]\n" \
  "ds_read_b64_tr_b16 v[66:67], %[t1] offset:0\n" \
  "v_xor_b32 %[t0], 0x13000, %[pb]\n" \
  "ds_read_b64_tr_b16 v[34:35], %[t0] offset:0\n" \
  "v_xor_b32 %[t1], 0x13080, %[pb]\n" \
  "ds_read_b64_tr_b16 v[82:83], %[t1] offset:0\n" \
  ".Ls_%=:\n" \
  "s_waitcnt lgkmcnt(0)\n"
#define PRO1_3 \
  "v_add_u32 %[t0], 0x18000, %[pa]\n" \
  "ds_read_b64_tr_b16 v[114:115], %[t0] offset:0\n" \
  "ds_read_b64_tr_b16 v[118:119], %[t0] offset:1024\n" \
  "ds_read_b64_tr_b16 v[122:123], %[t0] offset:8192\n" \
  "ds_read_b64_tr_b16 v[126:127], %[t0] offset:9216\n" \
  "s_cmp_lg_u32 %[hb], 1\n" \
  "s_cbranch_scc1 .Ls_%=\n" \
  "v_xor_b32 %[t0], 0x18000, %[pb]\n" \
  "ds_read_b64_tr_b16 v[194:195], %[t0] offset:0\n" \
  "v_xor_b32 %[t1], 0x18080, %[pb]\n" \
  "ds_read_b64_tr_b16 v[202:203], %[t1] offset:0\n" \
  "v_xor_b32 %[t0], 0x19000, %[pb]\n" \
  "ds_read_b64_tr_b16 v[2:3], %[t0] offset:0\n" \
  "v_xor_b32 %[t1], 0x19080, %[pb]\n" \
  "ds_read_b64_tr_b16 v[50:51], %[t1] offset:0\n" \
  "v_xor_b32 %[t0], 0x1a000, %[pb]\n" \
  "ds_read_b64_tr_b16 v[18:19], %[t0] offset:0\n" \
  "v_xor_b32 %[t1], 0x1a080, %[pb]\n" \
  "ds_read_b64_tr_b16 v[66:67], %[t1] offset:0\n" \
  "v_xor_b32 %[t0], 0x1b000, %[pb]\n" \
  "ds_read_b64_tr_b16 v[34:35], %[t0] offset:0\n" \
  "v_xor_b32 %[t1], 0x1b080, %[pb]\n" \
  "ds_read_b64_tr_b16 v[82:83], %[t1] offset:0\n" \
  ".Ls_%=:\n" \
  "s_waitcnt lgkmcnt(0)\n" \
  "v_swap_b32 v113, v114\n" \
  "v_swap_b32 v117, v118\n" \
  "v_swap_b32 v121, v122\n" \
  "v_swap_b32 v125, v126\n"
#define PRO1_4 \
  "v_add_u32 %[t0], 0x20000, %[pa]\n" \
  "ds_read_b64_tr_b16 v[128:129], %[t0] offset:0\n" \
  "ds_read_b64_tr_b16 v[132:133], %[t0] offset:1024\n" \
  "ds_read_b64_tr_b16 v[136:137], %[t0] offset:8192\n" \
  "ds_read_b64_tr_b16 v[140:141], %[t0] offset:9216\n" \
  "s_cmp_lg_u32 %[hb], 0\n" \
  "s_cbranch_scc1 .Ls_%=\n" \
  "v_xor_b32 %[t0], 0x20000, %[pb]\n" \
  "ds_read_b64_tr_b16 v[196:197], %[t0] offset:0\n" \
  "v_xor_b32 %[t1], 0x20080, %[pb]\n" \
  "ds_read_b64_tr_b16 v[204:205], %[t1] offset:0\n" \
  "v_xor_b32 %[t0], 0x21000, %[pb]\n" \
  "ds_read_b64_tr_b16 v[4:5], %[t0] offset:0\n" \
  "v_xor_b32 %[t1], 0x21080, %[pb]\n" \
  "ds_read_b64_tr_b16 v[52:53], %[t1] offset:0\n" \
  "v_xor_b32 %[t0], 0x22000, %[pb]\n" \
  "ds_read_b64_tr_b16 v[20:21], %[t0] offset:0\n" \
  "v_xor_b32 %[t1], 0x22080, %[pb]\n" \
  "ds_read_b64_tr_b16 v[68:69], %[t1] offset:0\n" \
  "v_xor_b32 %[t0], 0x23000, %[pb]\n" \
  "ds_read_b64_tr_b16 v[36:37], %[t0] offset:0\n" \
  "v_xor_b32 %[t1], 0x23080, %[pb]\n" \
  "ds_read_b64_tr_b16 v[84:85], %[t1] offset:0\n" \
  ".Ls_%=:\n" \
  "s_waitcnt lgkmcnt(0)\n"
#define PRO1_5 \
  "v_add_u32 %[t0], 0x0, %[pa]\n" \
  "ds_read_b64_tr_b16 v[130:131], %[t0] offset:0\n" \
  "ds_read_b64_tr_b16 v[134:135], %[t0] offset:1024\n" \
  "ds_read_b64_tr_b16 v[138:139], %[t0] offset:8192\n" \
  "ds_read_b64_tr_b16 v[142:143], %[t0] offset:9216\n" \
  "s_cmp_lg_u32 %[hb], 1\n" \
  "s_cbranch_scc1 .Ls_%=\n" \
  "v_xor_b32 %[t0], 0x0, %[pb]\n" \
  "ds_read_b64_tr_b16 v[196:197], %[t0] offset:0\n" \
  "v_xor_b32 %[t1], 0x80, %[pb]\n" \
  "ds_read_b64_tr_b16 v[204:205], %[t1] offset:0\n" \
  "v_xor_b32 %[t0], 0x1000, %[pb]\n" \
  "ds_read_b64_tr_b16 v[4:5], %[t0] offset:0\n" \
  "v_xor_b32 %[t1], 0x1080, %[pb]\n" \
  "ds_read_b64_tr_b16 v[52:53], %[t1] offset:0\n" \
  "v_xor_b32 %[t0], 0x2000, %[pb]\n" \
  "ds_read_b64_tr_b16 v[20:21], %[t0] offset:0\n" \
  "v_xor_b32 %[t1], 0x2080, %[pb]\n" \
  "ds_read_b64_tr_b16 v[68:69], %[t1] offset:0\n" \
  "v_xor_b32 %[t0], 0x3000, %[pb]\n" \
  "ds_read_b64_tr_b16 v[36:37], %[t0] offset:0\n" \
  "v_xor_b32 %[t1], 0x3080, %[pb]\n" \
  "ds_read_b64_tr_b16 v[84:85], %[t1] offset:0\n" \
  ".Ls_%=:\n" \
  "s_waitcnt lgkmcnt(0)\n" \
  "v_swap_b32 v129, v130\n" \
  "v_swap_b32 v133, v134\n" \
  "v_swap_b32 v137, v138\n" \
  "v_swap_b32 v141, v142\n"
#define PRO1_6 \
  "v_add_u32 %[t0], 0x8000, %[pa]\n" \
  "ds_read_b64_tr_b16 v[144:145], %[t0] offset:0\n" \
  "ds_read_b64_tr_b16 v[148:149], %[t0] offset:1024\n" \
  "ds_read_b64_tr_b16 v[152:153], %[t0] offset:8192\n" \
  "ds_read_b64_tr_b16 v[156:157], %[t0] offset:9216\n" \
  "s_cmp_lg_u32 %[hb], 0\n" \
  "s_cbranch_scc1 .Ls_%=\n" \
  "v_xor_b32 %[t0], 0x8000, %[pb]\n" \
  "ds_read_b64_tr_b16 v[198:199], %[t0] offset:0\n" \
  "v_xor_b32 %[t1], 0x8080, %[pb]\n" \
  "ds_read_b64_tr_b16 v[206:207], %[t1] offset:0\n" \
  "v_xor_b32 %[t0], 0x9000, %[pb]\n" \
  "ds_read_b64_tr_b16 v[6:7], %[t0] offset:0\n" \
  "v_xor_b32 %[t1], 0x9080, %[pb]\n" \
  "ds_read_b64_tr_b16 v[54:55], %[t1] offset:0\n" \
  "v_xor_b32 %[t0], 0xa000, %[pb]\n" \
  "ds_read_b64_tr_b16 v[22:23], %[t0] offset:0\n" \
  "v_xor_b32 %[t1], 0xa080, %[pb]\n" \
  "ds_read_b64_tr_b16 v[70:71], %[t1] offset:0\n" \
  "v_xor_b32 %[t0], 0xb000, %[pb]\n" \
  "ds_read_b64_tr_b16 v[38:39], %[t0] offset:0\n" \
  "v_xor_b32 %[t1], 0xb080, %[pb]\n" \
  "ds_read_b64_tr_b16 v[86:87], %[t1] offset:0\n" \
  ".Ls_%=:\n" \
  "s_waitcnt lgkmcnt(0)\n"
#define PRO1_7 \
  "v_add_u32 %[t0], 0x10000, %[pa]\n" \
  "ds_read_b64_tr_b16 v[146:147], %[t0] offset:0\n" \
  "ds_read_b64_tr_b16 v[150:151], %[t0] offset:1024\n" \
  "ds_read_b64_tr_b16 v[154:155], %[t0] offset:8192\n" \
  "ds_read_b64_tr_b16 v[158:159], %[t0] offset:9216\n" \
  "s_cmp_lg_u32 %[hb], 1\n" \
  "s_cbranch_scc1 .Ls_%=\n" \
  "v_xor_b32 %[t0], 0x10000, %[pb]\n" \
  "ds_read_b64_tr_b16 v[198:199], %[t0] offset:0\n" \
  "v_xor_b32 %[t1], 0x10080, %[pb]\n" \
  "ds_read_b64_tr_b16 v[206:207], %[t1] offset:0\n" \
  "v_xor_b32 %[t0], 0x11000, %[pb]\n" \
  "ds_read_b64_tr_b16 v[6:7], %[t0] offset:0\n" \
  "v_xor_b32 %[t1], 0x11080, %[pb]\n" \
  "ds_read_b64_tr_b16 v[54:55], %[t1] offset:0\n" \
  "v_xor_b32 %[t0], 0x12000, %[pb]\n" \
  "ds_read_b64_tr_b16 v[22:23], %[t0] offset:0\n" \
  "v_xor_b32 %[t1], 0x12080, %[pb]\n" \
  "ds_read_b64_tr_b16 v[70:71], %[t1] offset:0\n" \
  "v_xor_b32 %[t0], 0x13000, %[pb]\n" \
  "ds_read_b64_tr_b16 v[38:39], %[t0] offset:0\n" \
  "v_xor_b32 %[t1], 0x13080, %[pb]\n" \
  "ds_read_b64_tr_b16 v[86:87], %[t1] offset:0\n" \
  ".Ls_%=:\n" \
  "s_waitcnt lgkmcnt(0)\n" \
  "v_swap_b32 v145, v146\n" \
  "v_swap_b32 v149, v150\n" \
  "v_swap_b32 v153, v154\n" \
  "v_swap_b32 v157, v158\n"
#define PRO1_8 \
  "v_add_u32 %[t0], 0x18000, %[pa]\n" \
  "ds_read_b64_tr_b16 v[160:161], %[t0] offset:0\n" \
  "ds_read_b64_tr_b16 v[164:165], %[t0] offset:1024\n" \
  "ds_read_b64_tr_b16 v[168:169], %[t0] offset:8192\n" \
  "ds_read_b64_tr_b16 v[172:173], %[t0] offset:9216\n" \
  "s_cmp_lg_u32 %[hb], 0\n" \
  "s_cbranch_scc1 .Ls_%=\n" \
  "v_xor_b32 %[t0], 0x18000, %[pb]\n" \
  "ds_read_b64_tr_b16 v[208:209], %[t0] offset:0\n" \
  "v_xor_b32 %[t1], 0x18080, %[pb]\n" \
  "ds_read_b64_tr_b16 v[216:217], %[t1] offset:0\n" \
  "v_xor_b32 %[t0], 0x19000, %[pb]\n" \
  "ds_read_b64_tr_b16 v[8:9], %[t0] offset:0\n" \
  "v_xor_b32 %[t1], 0x19080, %[pb]\n" \
  "ds_read_b64_tr_b16 v[56:57], %[t1] offset:0\n" \
  "v_xor_b32 %[t0], 0x1a000, %[pb]\n" \
  "ds_read_b64_tr_b16 v[24:25], %[t0] offset:0\n" \
  "v_xor_b32 %[t1], 0x1a080, %[pb]\n" \
  "ds_read_b64_tr_b16 v[72:73], %[t1] offset:0\n" \
  "v_xor_b32 %[t0], 0x1b000, %[pb]\n" \
  "ds_read_b64_tr_b16 v[40:41], %[t0] offset:0\n" \
  "v_xor_b32 %[t1], 0x1b080, %[pb]\n" \
  "ds_read_b64_tr_b16 v[88:89], %[t1] offset:0\n" \
  ".Ls_%=:\n" \
  "s_waitcnt lgkmcnt(0)\n"
#define PRO1_9 \
  "v_add_u32 %[t0], 0x20000, %[pa]\n" \
  "ds_read_b64_tr_b16 v[162:163], %[t0] offset:0\n" \
  "ds_read_b64_tr_b16 v[166:167], %[t0] offset:1024\n" \
  "ds_read_b64_tr_b16 v[170:171], %[t0] offset:8192\n" \
  "ds_read_b64_tr_b16 v[174:175], %[t0] offset:9216\n" \
  "s_cmp_lg_u32 %[hb], 1\n" \
  "s_cbranch_scc1 .Ls_%=\n" \
  "v_xor_b32 %[t0], 0x20000, %[pb]\n" \
  "ds_read_b64_tr_b16 v[208:209], %[t0] offset:0\n" \
  "v_xor_b32 %[t1], 0x20080, %[pb]\n" \
  "ds_read_b64_tr_b16 v[216:217], %[t1] offset:0\n" \
  "v_xor_b32 %[t0], 0x21000, %[pb]\n" \
  "ds_read_b64_tr_b16 v[8:9], %[t0] offset:0\n" \
  "v_xor_b32 %[t1], 0x21080, %[pb]\n" \
  "ds_read_b64_tr_b16 v[56:57], %[t1] offset:0\n" \
  "v_xor_b32 %[t0], 0x22000, %[pb]\n" \
  "ds_read_b64_tr_b16 v[24:25], %[t0] offset:0\n" \
  "v_xor_b32 %[t1], 0x22080, %[pb]\n" \
  "ds_read_b64_tr_b16 v[72:73], %[t1] offset:0\n" \
  "v_xor_b32 %[t0], 0x23000, %[pb]\n" \
  "ds_read_b64_tr_b16 v[40:41], %[t0] offset:0\n" \
  "v_xor_b32 %[t1], 0x23080, %[pb]\n" \
  "ds_read_b64_tr_b16 v[88:89], %[t1] offset:0\n" \
  ".Ls_%=:\n" \
  "s_waitcnt lgkmcnt(0)\n" \
  "v_swap_b32 v161, v162\n" \
  "v_swap_b32 v165, v166\n" \
  "v_swap_b32 v169, v170\n" \
  "v_swap_b32 v173, v174\n"
#define PRO1_10 \
  "v_add_u32 %[t0], 0x0, %[pa]\n" \
  "ds_read_b64_tr_b16 v[176:177], %[t0] offset:0\n" \
  "ds_read_b64_tr_b16 v[180:181], %[t0] offset:1024\n" \
  "ds_read_b64_tr_b16 v[184:185], %[t0] offset:8192\n" \
  "ds_read_b64_tr_b16 v[188:189], %[t0] offset:9216\n" \
  "s_cmp_lg_u32 %[hb], 0\n" \
  "s_cbranch_scc1 .Ls_%=\n" \
  "v_xor_b32 %[t0], 0x0, %[pb]\n" \
  "ds_read_b64_tr_b16 v[210:211], %[t0] offset:0\n" \
  "v_xor_b32 %[t1], 0x80, %[pb]\n" \
  "ds_read_b64_tr_b16 v[218:219], %[t1] offset:0\n" \
  "v_xor_b32 %[t0], 0x1000, %[pb]\n" \
  "ds_read_b64_tr_b16 v[10:11], %[t0] offset:0\n" \
  "v_xor_b32 %[t1], 0x1080, %[pb]\n" \
  "ds_read_b64_tr_b16 v[58:59], %[t1] offset:0\n" \
  "v_xor_b32 %[t0], 0x2000, %[pb]\n" \
  "ds_read_b64_tr_b16 v[26:27], %[t0] offset:0\n" \
  "v_xor_b32 %[t1], 0x2080, %[pb]\n" \
  "ds_read_b64_tr_b16 v[74:75], %[t1] offset:0\n" \
  "v_xor_b32 %[t0], 0x3000, %[pb]\n" \
  "ds_read_b64_tr_b16 v[42:43], %[t0] offset:0\n" \
  "v_xor_b32 %[t1], 0x3080, %[pb]\n" \
  "ds_read_b64_tr_b16 v[90:91], %[t1] offset:0\n" \
  ".Ls_%=:\n" \
  "s_waitcnt lgkmcnt(0)\n"
#define PRO1_11 \
  "v_add_u32 %[t0], 0x8000, %[pa]\n" \
  "ds_read_b64_tr_b16 v[178:179], %[t0] offset:0\n" \
  "ds_read_b64_tr_b16 v[182:183], %[t0] offset:1024\n" \
  "ds_read_b64_tr_b16 v[186:187], %[t0] offset:8192\n" \
  "ds_read_b64_tr_b16 v[190:191], %[t0] offset:9216\n" \
  "s_cmp_lg_u32 %[hb], 1\n" \
  "s_cbranch_scc1 .Ls_%=\n" \
  "v_xor_b32 %[t0], 0x8000, %[pb]\n" \
  "ds_read_b64_tr_b16 v[210:211], %[t0] offset:0\n" \
  "v_xor_b32 %[t1], 0x8080, %[pb]\n" \
  "ds_read_b64_tr_b16 v[218:219], %[t1] offset:0\n" \
  "v_xor_b32 %[t0], 0x9000, %[pb]\n" \
  "ds_read_b64_tr_b16 v[10:11], %[t0] offset:0\n" \
  "v_xor_b32 %[t1], 0x9080, %[pb]\n" \
  "ds_read_b64_tr_b16 v[58:59], %[t1] offset:0\n" \
  "v_xor_b32 %[t0], 0xa000, %[pb]\n" \
  "ds_read_b64_tr_b16 v[26:27], %[t0] offset:0\n" \
  "v_xor_b32 %[t1], 0xa080, %[pb]\n" \
  "ds_read_b64_tr_b16 v[74:75], %[t1] offset:0\n" \
  "v_xor_b32 %[t0], 0xb000, %[pb]\n" \
  "ds_read_b64_tr_b16 v[42:43], %[t0] offset:0\n" \
  "v_xor_b32 %[t1], 0xb080, %[pb]\n" \
  "ds_read_b64_tr_b16 v[90:91], %[t1] offset:0\n" \
  ".Ls_%=:\n" \
  "s_waitcnt lgkmcnt(0)\n" \
  "v_swap_b32 v177, v178\n" \
  "v_swap_b32 v181, v182\n" \
  "v_swap_b32 v185, v186\n" \
  "v_swap_b32 v189, v190\n"
#define PRO1_12 \
  "v_add_u32 %[t0], 0x8000, %[b0]\n" \
  "ds_write_b128 %[t0], v[192:195]\n" \
  "ds_write_b128 %[t0], v[196:199] offset:1024\n" \
  "v_add_u32 %[t0], 0xc000, %[b0]\n" \
  "ds_write_b128 %[t0], v[200:203]\n" \
  "ds_write_b128 %[t0], v[204:207] offset:1024\n" \
  "v_add_u32 %[t0], 0x10000, %[pa]\n" \
  "ds_read_b64_tr_b16 v[224:225], %[t0] offset:0\n" \
  "ds_read_b64_tr_b16 v[228:229], %[t0] offset:1024\n" \
  "ds_read_b64_tr_b16 v[232:233], %[t0] offset:8192\n" \
  "ds_read_b64_tr_b16 v[236:237], %[t0] offset:9216\n" \
  "s_cmp_lg_u32 %[hb], 0\n" \
  "s_cbranch_scc1 .Ls_%=\n" \
  "v_xor_b32 %[t0], 0x10000, %[pb]\n" \
  "ds_read_b64_tr_b16 v[212:213], %[t0] offset:0\n" \
  "v_xor_b32 %[t1], 0x10080, %[pb]\n" \
  "ds_read_b64_tr_b16 v[220:221], %[t1] offset:0\n" \
  "v_xor_b32 %[t0], 0x11000, %[pb]\n" \
  "ds_read_b64_tr_b16 v[12:13], %[t0] offset:0\n" \
  "v_xor_b32 %[t1], 0x11080, %[pb]\n" \
  "ds_read_b64_tr_b16 v[60:61], %[t1] offset:0\n" \
  "v_xor_b32 %[t0], 0x12000, %[pb]\n" \
  "ds_read_b64_tr_b16 v[28:29], %[t0] offset:0\n" \
  "v_xor_b32 %[t1], 0x12080, %[pb]\n" \
  "ds_read_b64_tr_b16 v[76:77], %[t1] offset:0\n" \
  "v_xor_b32 %[t0], 0x13000, %[pb]\n" \
  "ds_read_b64_tr_b16 v[44:45], %[t0] offset:0\n" \
  "v_xor_b32 %[t1], 0x13080, %[pb]\n" \
  "ds_read_b64_tr_b16 v[92:93], %[t1] offset:0\n" \
  ".Ls_%=:\n" \
  "s_waitcnt lgkmcnt(0)\n"
#define PRO1_13 \
  "v_add_u32 %[t0], 0x18000, %[pa]\n" \
  "ds_read_b64_tr_b16 v[226:227], %[t0] offset:0\n" \
  "ds_read_b64_tr_b16 v[230:231], %[t0] offset:1024\n" \
  "ds_read_b64_tr_b16 v[234:235], %[t0] offset:8192\n" \
  "ds_read_b64_tr_b16 v[238:239], %[t0] offset:9216\n" \
  "s_cmp_lg_u32 %[hb], 1\n" \
  "s_cbranch_scc1 .Ls_%=\n" \
  "v_xor_b32 %[t0], 0x18000, %[pb]\n" \
  "ds_read_b64_tr_b16 v[212:213], %[t0] offset:0\n" \
  "v_xor_b32 %[t1], 0x18080, %[pb]\n" \
  "ds_read_b64_tr_b16 v[220:221], %[t1] offset:0\n" \
  "v_xor_b32 %[t0], 0x19000, %[pb]\n" \
  "ds_read_b64_tr_b16 v[12:13], %[t0] offset:0\n" \
  "v_xor_b32 %[t1], 0x19080, %[pb]\n" \
  "ds_read_b64_tr_b16 v[60:61], %[t1] offset:0\n" \
  "v_xor_b32 %[t0], 0x1a000, %[pb]\n" \
  "ds_read_b64_tr_b16 v[28:29], %[t0] offset:0\n" \
  "v_xor_b32 %[t1], 0x1a080, %[pb]\n" \
  "ds_read_b64_tr_b16 v[76:77], %[t1] offset:0\n" \
  "v_xor_b32 %[t0], 0x1b000, %[pb]\n" \
  "ds_read_b64_tr_b16 v[44:45], %[t0] offset:0\n" \
  "v_xor_b32 %[t1], 0x1b080, %[pb]\n" \
  "ds_read_b64_tr_b16 v[92:93], %[t1] offset:0\n" \
  ".Ls_%=:\n" \
  "s_waitcnt lgkmcnt(0)\n" \
  "v_swap_b32 v225, v226\n" \
  "v_swap_b32 v229, v230\n" \
  "v_swap_b32 v233, v234\n" \
  "v_swap_b32 v237, v238\n" \
  "v_add_u32 %[t0], 0x10000, %[b0]\n" \
  "ds_write_b128 %[t0], v[224:227]\n" \
  "ds_write_b128 %[t0], v[228:231] offset:1024\n" \
  "v_add_u32 %[t0], 0x14000, %[b0]\n" \
  "ds_write_b128 %[t0], v[232:235]\n" \
  "ds_write_b128 %[t0], v[236:239] offset:1024\n"
#define PRO1_14 \
  "v_add_u32 %[t0], 0x20000, %[pa]\n" \
  "ds_read_b64_tr_b16 v[224:225], %[t0] offset:0\n" \
  "ds_read_b64_tr_b16 v[228:229], %[t0] offset:1024\n" \
  "ds_read_b64_tr_b16 v[232:233], %[t0] offset:8192\n" \
  "ds_read_b64_tr_b16 v[236:237], %[t0] offset:9216\n" \
  "s_cmp_lg_u32 %[hb], 0\n" \
  "s_cbranch_scc1 .Ls_%=\n" \
  "v_xor_b32 %[t0], 0x20000, %[pb]\n" \
  "ds_read_b64_tr_b16 v[214:215], %[t0] offset:0\n" \
  "v_xor_b32 %[t1], 0x20080, %[pb]\n" \
  "ds_read_b64_tr_b16 v[222:223], %[t1] offset:0\n" \
  "v_xor_b32 %[t0], 0x21000, %[pb]\n" \
  "ds_read_b64_tr_b16 v[14:15], %[t0] offset:0\n" \
  "v_xor_b32 %[t1], 0x21080, %[pb]\n" \
  "ds_read_b64_tr_b16 v[62:63], %[t1] offset:0\n" \
  "v_xor_b32 %[t0], 0x22000, %[pb]\n" \
  "ds_read_b64_tr_b16 v[30:31], %[t0] offset:0\n" \
  "v_xor_b32 %[t1], 0x22080, %[pb]\n" \
  "ds_read_b64_tr_b16 v[78:79], %[t1] offset:0\n" \
  "v_xor_b32 %[t0], 0x23000, %[pb]\n" \
  "ds_read_b64_tr_b16 v[46:47], %[t0] offset:0\n" \
  "v_xor_b32 %[t1], 0x23080, %[pb]\n" \
  "ds_read_b64_tr_b16 v[94:95], %[t1] offset:0\n" \
  ".Ls_%=:\n" \
  "s_waitcnt lgkmcnt(0)\n"
#define PRO1_15 \
  "v_add_u32 %[t0], 0x0, %[pa]\n" \
  "ds_read_b64_tr_b16 v[226:227], %[t0] offset:0\n" \
  "ds_read_b64_tr_b16 v[230:231], %[t0] offset:1024\n" \
  "ds_read_b64_tr_b16 v[234:235], %[t0] offset:8192\n" \
  "ds_read_b64_tr_b16 v[238:239], %[t0] offset:9216\n" \
  "s_cmp_lg_u32 %[hb], 1\n" \
  "s_cbranch_scc1 .Ls_%=\n" \
  "v_xor_b32 %[t0], 0x0, %[pb]\n" \
  "ds_read_b64_tr_b16 v[214:215], %[t0] offset:0\n" \
  "v_xor_b32 %[t1], 0x80, %[pb]\n" \
  "ds_read_b64_tr_b16 v[222:223], %[t1] offset:0\n" \
  "v_xor_b32 %[t0], 0x1000, %[pb]\n" \
  "ds_read_b64_tr_b16 v[14:15], %[t0] offset:0\n" \
  "v_xor_b32 %[t1], 0x1080, %[pb]\n" \
  "ds_read_b64_tr_b16 v[62:63], %[t1] offset:0\n" \
  "v_xor_b32 %[t0], 0x2000, %[pb]\n" \
  "ds_read_b64_tr_b16 v[30:31], %[t0] offset:0\n" \
  "v_xor_b32 %[t1], 0x2080, %[pb]\n" \
  "ds_read_b64_tr_b16 v[78:79], %[t1] offset:0\n" \
  "v_xor_b32 %[t0], 0x3000, %[pb]\n" \
  "ds_read_b64_tr_b16 v[46:47], %[t0] offset:0\n" \
  "v_xor_b32 %[t1], 0x3080, %[pb]\n" \
  "ds_read_b64_tr_b16 v[94:95], %[t1] offset:0\n" \
  ".Ls_%=:\n" \
  "s_waitcnt lgkmcnt(0)\n" \
  "v_swap_b32 v225, v226\n" \
  "v_swap_b32 v229, v230\n" \
  "v_swap_b32 v233, v234\n" \
  "v_swap_b32 v237, v238\n" \
  "v_add_u32 %[t0], 0x18000, %[b0]\n" \
  "ds_write_b128 %[t0], v[224:227]\n" \
  "ds_write_b128 %[t0], v[228:231] offset:1024\n" \
  "v_add_u32 %[t0], 0x1c000, %[b0]\n" \
  "ds_write_b128 %[t0], v[232:235]\n" \
  "ds_write_b128 %[t0], v[236:239] offset:1024\n" \
  "v_add_u32 %[t0], 0x20000, %[b0]\n" \
  "ds_write_b128 %[t0], v[208:211]\n" \
  "ds_write_b128 %[t0], v[212:215] offset:1024\n" \
  "v_add_u32 %[t0], 0x24000, %[b0]\n" \
  "ds_write_b128 %[t0], v[216:219]\n" \
  "ds_write_b128 %[t0], v[220:223] offset:1024\n"
#define VO_CLOB "v192", "v193", "v194", "v195", "v196", "v197", "v198", "v199", "v200", "v201", "v202", "v203", "v204", "v205", "v206", "v207", "v208", "v209", "v210", "v211", "v212", "v213", "v214", "v215", "v216", "v217", "v218", "v219", "v220", "v221", "v222", "v223", "v224", "v225", "v226", "v227", "v228", "v229", "v230", "v231", "v232", "v233", "v234", "v235", "v240", "v241", "v242", "v243", "v244", "v245", "v246", "v247", "v250", "v251", "v252"
#define QK_CLOB "v204", "v205", "v206", "v207", "v208", "v209", "v210", "v211", "v212", "v213", "v214", "v215", "v216", "v217", "v218", "v219", "v220", "v221", "v222", "v223", "v224", "v225", "v226", "v227", "v228", "v229", "v230", "v231", "v236", "v237", "v238", "v239", "v240", "v241", "v242", "v243", "v244", "v245", "v246", "v247"
#define QL_CLOB "v192", "v193", "v194", "v195", "v196", "v197", "v198", "v199", "v200", "v201", "v202", "v203"
#define QH_CLOB "v232", "v233", "v234", "v235"
#define ST_CLOB "v192", "v193", "v194", "v195", "v196", "v197", "v198", "v199", "v200", "v201", "v202", "v203", "v204", "v205", "v206", "v207", "v240", "v241", "v242", "v243", "v244", "v245", "v246", "v247", "v253"
#define XB_CLOB "v192", "v193", "v194", "v195", "v196", "v197", "v198", "v199", "v200", "v201", "v202", "v203", "v204", "v205", "v206", "v207", "v208", "v209", "v210", "v211", "v212", "v213", "v214", "v215", "v216", "v217", "v218", "v219", "v220", "v221", "v222", "v223", "v224", "v225", "v226", "v227", "v228", "v229", "v230", "v231", "v232", "v233", "v234", "v235", "v236", "v237", "v238", "v239", "v240", "v241", "v242", "v243", "v244", "v245", "v246", "v247"
#define OF_CLOB "v32", "v33", "v34", "v35", "v36", "v37", "v38", "v39", "v40", "v41", "v42", "v43", "v44", "v45", "v46", "v47", "v48", "v49", "v50", "v51", "v52", "v53", "v54", "v55", "v56", "v57", "v58", "v59", "v60", "v61", "v62", "v63", "v64", "v65", "v66", "v67", "v68", "v69", "v70", "v71", "v72", "v73", "v74", "v75", "v76", "v77", "v78", "v79", "v80", "v81", "v82", "v83", "v84", "v85", "v86", "v87", "v88", "v89", "v90", "v91", "v92", "v93", "v94", "v95", "v96", "v97", "v98", "v99", "v100", "v101", "v102", "v103"
constexpr int L_W = 32768;
constexpr int L_HI = 16384;
constexpr float CSC = 0.08838834764831845f * 1.4426950408889634f;
#define TWY 3
#define MARG 115
#define ED0 700
#define EA0 1110
#define SDK 360
#define TOL 145
#define XLATE 130
#define TFR 600
#define QT 0
#define HSU 11
#define WNT 1
#define KROT 7
#define VLATE 0
#define VSL 2
#define CG1 18
#define CG2 6
#define KSL2 0
#define KSL 4
#define NCY 1600
#define NCYV NCY
#define NRY 22
#define NRX 30
#define PDU 1
#define PDY 4
#define PDC 4
#define NF1O 64
#define NPUB 2
#define NSER 3
#define NSB 6
#define NSB1 4
#define XTU 20
#define CPU_ 9
#define CPR 12
#define TLP 0
#define CP0 0
#define RSL 2
#define PFL 4
#define EVT2R
template <int role>
__device__ __forceinline__ void wroles(const int h, const int par, const int sl, const int xg, const int w, const u64 nonce, const u16* __restrict__ x,
                                       const u16* __restrict__ Wq, const u16* __restrict__ Wk, const u16* __restrict__ Wv,
                                       const u16* __restrict__ Wo, char* ws) {
  asm volatile(".Lrole%c0:" ::"n"(role));
  LAS char* const L = (LAS char*)g_lds;
  gu32* tmo = (gu32*)(ws + OFF_TMO);
  const __amdgpu_buffer_rsrc_t rs = __builtin_amdgcn_make_buffer_rsrc(ws, 0, (int)WS_USED, 0x00020000);
  constexpr bool vo = role & 1;
  v8u wa[8], wb[16];
  v32u W0, W1, W2, W3, W4, W5;
  constexpr bool fuse = role >= 2;
  constexpr int RG = fuse ? 4 : 5, RB = fuse ? 32768 : 0;
  f32x4 fac[2][2];
  v2u nin = {0u, 0u};
  constexpr int TWV = 5;
  Clk ck = {0u, 0u, 92500u, 46432u};
  const int krot = role >= 2 ? (h & KROT) ^ (par * PX) : 0;
  {
    const int lane = olane();
    const int l15 = lane & 15, g = lane >> 4, trq = l15 >> 2, trp = lane & 3;
    const char* bl = (const char*)((vo ? Wv : Wq) + h * DH);
    const char* br = (const char*)(vo ? Wo + (size_t)h * DH * D : Wk + h * DH);
    const int rot = role == 1 ? (h >> 4) * 4 : 0;
    int vl[2], vr[2];
#pragma unroll
    for (int q = 0; q < 2; ++q) {
      const int row = 4 * (2 * w + q) + g;
      const int cb = ((((l15 >> 1) ^ ((row & 3) | ((row >> 1) & 4))) << 5) | ((l15 & 1) << 4));
      const int i = row & 7;
      const int prow = (row & 32) + ((i & 4) << 2) + ((row >> 1) & 12) + (i & 3);
      vl[q] = role < 2 ? (128 * (w >> 1) + 16 * (w & 1) + 8 * q + g) * 8192 + ((((l15 >> 1) ^ (4 * q + g)) << 5) | ((l15 & 1) << 4))
                       : row * 8192 + cb;
      vr[q] = vo ? (role == 1 ? row : prow) * 2048 + cb : vl[q];
    }
#define W_ISSUE(u)                                                                                   \
  {                                                                                                  \
    const size_t ro = (size_t)(role < 2 ? (8 * ((u) & 1) + ((u) >> 3)) * 64 + (((u) >> 2) & 1) * 32 + (((u) >> 1) & 1) * 4 : ((u) ^ krot) * 64) * 8192; \
    const char* pl = bl + ro;                                                                        \
    const char* pr = br + (role == 1 ? (size_t)(((u) >> 1) & 1) * 64 * 2048 + (2 * ((u) >> 2) + ((u) & 1)) * 256          \
                                     : (vo ? (size_t)((u) & 1) * 64 * 2048 + (((u) ^ (par * PX)) >> 1) * 256 : ro)); \
    _Pragma("unroll") for (int q = 0; q < 2; ++q) {                                                  \
      glds16<((WNT >> (role >> 1)) & 1) != 0>(pl, vl[q], RB + ((u) % RG) * 32768 + (2 * w + q) * 1024);                                \
      glds16<((WNT >> (role >> 1)) & 1) != 0>(pr, vr[q], RB + ((u) % RG) * 32768 + 16384 + (2 * w + q) * 1024);                        \
    }                                                                                                \
  }
    const int ta = role == 1 ? 8 + (w ^ rot) : 2 * w, tb = role < 2 ? (2 - role) * (w & 3) : 2 * w + 1;
    const int sw = trq | ((g & 1) << 2);
    int pa = (ta >> 3) * 16384 + (8 * g + trq) * 256 + (((ta & 7) ^ sw) << 5) + trp * 8;
    int pb = (role < 2 ? (w & 3) * 4096 + (4 * g + trq) * 256 : (tb >> 3) * 16384 + (8 * g + trq) * 256) + (((tb & 7) ^ sw) << 5) + trp * 8;
    v16u K0, K1, K2;
    if constexpr (role < 2)
      asm volatile("" : "={v[0:31]}"(W0), "={v[32:63]}"(W1), "={v[64:95]}"(W2), "={v[96:127]}"(W3), "={v[128:159]}"(W4), "={v[160:191]}"(W5),
                   "={v[192:207]}"(K0), "={v[208:223]}"(K1), "={v[224:239]}"(K2));
    const int b0 = lane * 16 + (w ^ 4) * 2048;
    const int pw = L_W + w * 2048 + lane * 16;
#define W_FRAG(dst, p, u)                                                                            \
  {                                                                                                  \
    int p_ = p + RB + ((u) % RG) * 32768;                                                             \
    asm volatile("" : "+v"(p_));                                                                     \
    const v2u t0 = trd(L + p_), t1 = trd(L + p_ + 1024), t2 = trd(L + p_ + 8192), t3 = trd(L + p_ + 9216); \
    dst = v8u{t0[0], t0[1], t1[0], t1[1], t2[0], t2[1], t3[0], t3[1]};                               \
  }
#define W_PUT(c)                                           \
  {                                                        \
    int p_ = pw + ((c) - 8) * 16384;                       \
    asm volatile("" : "+v"(p_));                           \
    *(LAS v4u*)(L + p_) = lo4(tmp[(c) - 8]);               \
    *(LAS v4u*)(L + p_ + 1024) = hi4(tmp[(c) - 8]);        \
  }
#define PT(i)
#define PT4(i)
    PT(0);
    const int fxv = ((w & 1) + 2 * (lane >> 2)) * 2048 + (((lane ^ -g) & 3) << 4) + ((w >> 1) & 1) * 64;
    const int fxr = l15 * 64 + (((g ^ -trq) & 3) << 4);
    constexpr int XS = role == 3;
#define X_ISSUE(j) glds16((const char*)x + par * 65536 + ((2 * (j) + (w >> 2)) ^ krot) * 128, fxv, (((j) + XS) & 3) * 8192 + w * 1024)
    if (TLP && w >= 6 + vo) {
      unsigned keep;
      asm volatile("s_mov_b32 %0, m0\n\ts_mov_b32 m0, %3\n\ts_nop 0\n\tglobal_load_lds_dword %1, %2\n\ts_mov_b32 m0, %0"
                   : "=&s"(keep) : "v"((olane() & (TLP - 1)) * (8388608 / (TLP ? TLP : 1))), "s"(w == 7 ? bl : br), "s"(RB + w * 2048) : "memory");
    }
    W_ISSUE(0);
    if (((CP0 >> role) & 1) && w == 1) code_ahead(sl + 8 * par, RB + 32768 + 2048);
    if (fuse) {
#pragma unroll
      for (int j = 0; j < 4; ++j) X_ISSUE(j);
    }
#pragma unroll
    for (int u = 1; u < RG; ++u) {
      if (u == PDU && role < 3)
        for (int k = role == 2 ? TN(6, PDY) : TN(5, PDC); k > 0; --k) __builtin_amdgcn_s_sleep(8);
      W_ISSUE(u);
    }
    PT(1);
    v8u tmp[8];
#pragma unroll
    for (int u = 0; u < 16; ++u) {
      {
        int young = 4 * (u + RG - 1 < 16 ? RG - 1 : 15 - u);
        if (fuse)
          for (int i = u - RG; i < u; ++i) young += i >= 0 && i < 8 && (i & 1);
        asm volatile("s_waitcnt vmcnt(%0)" ::"n"(young) : "memory");
      }
      RAW_BARRIER();
      if ((u & 3) == 0 || u == 15) PT(2 + (u + 1) / 4);
      if constexpr (role < 2) {
        unsigned t0, t1;
#define PRO_OPS                                                                                                                    \
  : "+{v[0:31]}"(W0), "+{v[32:63]}"(W1), "+{v[64:95]}"(W2), "+{v[96:127]}"(W3), "+{v[128:159]}"(W4), "+{v[160:191]}"(W5),          \
    "+{v[192:207]}"(K0), "+{v[208:223]}"(K1), "+{v[224:239]}"(K2), [t0] "=&v"(t0), [t1] "=&v"(t1)                                  \
  : [b0] "v"(b0), [pa] "v"(pa), [pb] "v"(pb), [hb] "s"(w >> 2)                                                                     \
  : "scc", "memory"
#define PRO_U(n)                                         \
  if (u == n) {                                          \
    if constexpr (role == 0) asm volatile(PRO0_##n PRO_OPS); \
    else asm volatile(PRO1_##n PRO_OPS);                 \
  }
        PRO_U(0) PRO_U(1) PRO_U(2) PRO_U(3) PRO_U(4) PRO_U(5) PRO_U(6) PRO_U(7)
        PRO_U(8) PRO_U(9) PRO_U(10) PRO_U(11) PRO_U(12) PRO_U(13) PRO_U(14) PRO_U(15)
      } else {
        if (u >= 13) { W_PUT(u >= 13 ? 2 * u - 18 : 8); W_PUT(u >= 13 ? 2 * u - 17 : 8); }
        v8u fa, fb;
        W_FRAG(fa, pa, u)
        W_FRAG(fb, pb, u)
        if (u < 8) {
          wa[u & 7] = fa;
          wb[u] = fb;
        } else {
          wb[u] = cat8(lo4(fa), lo4(fb));
          tmp[u & 7] = cat8(hi4(fa), hi4(fb));
        }
#pragma unroll
        for (int hf = 0; hf < 2; ++hf)
#pragma unroll
          for (int rt = 0; rt < 2; ++rt) {
            const v4u xq = *(const LAS v4u*)(L + fxr + ((u + 2 * XS) & 7) * 4096 + hf * 2048 + rt * 1024);
            fac[0][rt] = mma(hf ? hi4(fa) : lo4(fa), xq, u + hf == 0 ? f32x4{0.f, 0.f, 0.f, 0.f} : fac[0][rt]);
            fac[1][rt] = mma(hf ? hi4(fb) : lo4(fb), xq, u + hf == 0 ? f32x4{0.f, 0.f, 0.f, 0.f} : fac[1][rt]);
          }
        asm volatile("" : "+v"(fac[0][0]), "+v"(fac[0][1]), "+v"(fac[1][0]), "+v"(fac[1][1]));
      }
      if (u == HSU && w == 0) hs_wait(rs, ws, nonce, CNT_HS);
      if (((VWN && role == 3) || (QKN && role == 2)) && u == HSU + 2 && w == 7) nin = numa_load(rs, tmo);
      if (role < 2 && u == HSU + 2 && w == 7) {
        arm_wait(rs, tmo);
        OPQ(off, OFF_PERM);
        nin[0] = __builtin_amdgcn_raw_buffer_load_b32(rs, off, 0, AUX_SC1);
      }
      if (role < 2 && u == 14 && w == TWV) clk_init(ck);
      if (u + RG < 16) {
        RAW_BARRIER();
        if (fuse && u == XTU && w < 2) {
          unsigned keep;
          asm volatile("s_mov_b32 %0, m0\n\ts_mov_b32 m0, %3\n\ts_nop 0\n\tglobal_load_lds_dword %1, %2\n\ts_mov_b32 m0, %0"
                       : "=&s"(keep) : "v"(olane() * 128), "s"((const char*)x + (2 * w + 2 + par) * 65536 + sl * 8192), "s"(RB + (u % RG) * 32768 + w * 2048) : "memory");
        }
        if (u == CPU_ && ((CPR >> role) & 1) && w == 1) code_ahead(sl + 8 * par, RB + (u % RG) * 32768 + 2048);
        W_ISSUE(u + RG);
        if (fuse && u < 8 && (u & 1)) X_ISSUE(4 + (u >> 1));
      }
    }
    PT(7);
    static_assert(!QKN, "");
    if (role == 2 || (role == 3 && w < 4)) {
      const int l16 = olane() * 16;
#pragma unroll
      for (int rt = 0; rt < 2; ++rt) {
        const float ks = role == 2 && w >= 4 ? CSC : 1.f;
        const f32x4 f0 = fac[0][rt] * ks, f1 = fac[1][rt] * ks;
        const v4u o = {pk2(f0[0], f0[1]), pk2(f0[2], f0[3]), pk2(f1[0], f1[1]), pk2(f1[2], f1[3])};
        if (role == 3) *(LAS v4u*)(L + 24576 + rt * 4096 + w * 1024 + l16) = o;
        else st128(v4u{o[0], o[2], o[1], o[3]}, rs, l16, qk_addr(w >> 2, par, h, 0, 0) + rt * QK_RT + (w & 3) * 1024);
      }
    }
    RAW_BARRIER();
    PT(8);
    if (fuse) { W_PUT(14); W_PUT(15); }
  }
  const int lane = olane();
  const int l15 = lane & 15, g = lane >> 4, trq = l15 >> 2, trp = lane & 3;
  int pw0 = L_W + w * 2048 + lane * 16, pw1 = pw0 + 65536;
  asm volatile("" : "+v"(pw0), "+v"(pw1));
#define WA_P(c) (L + ((c) < 12 ? pw0 : pw1) + ((c) & 3) * 16384)
#define WA_LDS(c) cat8(*(const LAS v4u*)WA_P(c), *(const LAS v4u*)(WA_P(c) + 1024))
  const int r_a = (int)(((uintptr_t)ws >> 19) & 7);
  const int r_s = r_a + (int)(OFF_STEP / UNIT);

  typedef int v4i __attribute__((ext_vector_type(4)));
#define WPIN "{v[0:31]}"(W0), "{v[32:63]}"(W1), "{v[64:95]}"(W2), "{v[96:127]}"(W3), "{v[128:159]}"(W4), "{v[160:191]}"(W5)
  const v4u rsv = {(unsigned)(uintptr_t)ws, (unsigned)((uintptr_t)ws >> 32) & 0xffffu, (unsigned)WS_USED, 0x00020000u};
  if constexpr (role >= 2) {
    constexpr bool isx = role == 3;
    W0 = cat32(wa[0], wa[1], wa[2], wa[3]), W1 = cat32(wa[4], wa[5], wa[6], wa[7]), W2 = cat32(wb[0], wb[1], wb[2], wb[3]);
    W3 = cat32(wb[4], wb[5], wb[6], wb[7]), W4 = cat32(wb[8], wb[9], wb[10], wb[11]), W5 = cat32(wb[12], wb[13], wb[14], wb[15]);
    const v4u rxv = {(unsigned)(uintptr_t)x, (unsigned)((uintptr_t)x >> 32) & 0xffffu, (unsigned)(S * D * 2), 0x00020000u};
    v4i adr;
    adr[3] = lane * 16;
    adr[2] = pw0;
    adr[1] = pw1;
    adr[0] = ((w & 1) + 2 * (lane >> 2)) * 2048 + (((lane ^ -g) & 3) << 4) + ((w >> 1) & 1) * 1024 + (isx ? 0 : (w >> 2) * 64);
    const int xr = l15 * 64 + (((g ^ -trq) & 3) << 4);
    constexpr int fo = isx ? F_VW : F_QK;
    const int nrel = isx ? TN(8, NRX) : TN(7, NRY);
    const int nit = par ? nrel : NSEG - nrel;
#define SEGOF(i) ((i) < nrel ? 2 * (i) + par : (i) + nrel)
#define ST_FLAG(sg)                                                                                                   \
  {                                                                                                                   \
    VMCNT(0);                                                                                                         \
    RAW_BARRIER();                                                                                                    \
    if (w == 0) __builtin_amdgcn_raw_buffer_store_b32(0u, rs, olane() << 28, fo + flag_of(sg, h), AUX_SC1);               \
  }
    unsigned vpm_ = 0;
    if ((VWN && isx) || (QKN && !isx)) {
      if (w == 7) *(volatile LAS unsigned*)L = numa_perm(nin, (LAS unsigned*)(L + 512));
      RAW_BARRIER();
      vpm_ = __builtin_amdgcn_readfirstlane(*(volatile LAS unsigned*)L) | (unsigned)((r_a + (int)(OFF_VWX / UNIT)) & 7) << 12;
      RAW_BARRIER();
    }
    const int qcl = cls_of(vpm_, h >> 3) ^ (QKN == 2 ? 2 : 0);
#define XB_OPS(sg)                                                                                                      \
  : [cnt] "=&s"(cnt) EVOUT                                                                                              \
  : WPIN, "{v[248:251]}"(adr), [rs] "s"(rsv), [so] "s"(vw_addr(vpm_, 2 * (w - 4), sg, h >> 2) + (h & 3) * 1024 + par * (PX / 2 * VRING * 262144)), \
    [so2] "s"(vw_addr(vpm_, 2 * (w - 4), sg, h >> 2) + (h & 3) * 1024 - par * (PX / 2 * VRING * 262144)),               \
    [js] "i"(VWN ? 32 * 524288 : VRING * 262144), [ss] "i"(VWN ? 65536 : 32768), [ob] "s"(nit - it)                         \
  : XB_CLOB, "scc", "memory"
    if (!isx || w < 4) {
      constexpr int B0 = isx ? 6 : 8, PER = isx ? 2 : 4, SL = isx ? 12288 : 16384;
#pragma unroll
      for (int k = 0; k < 2; ++k)
#pragma unroll
        for (int j = 0; j < B0 / PER; ++j)
          glds16((const char*)x + SEGOF(1) * 65536 + (((k * B0 + j * PER) / 2 * 64) ^ (krot * 128)), adr[0], k * SL + j * PER * 2048 + w * 1024);
    }
    PT(9);
    if (!isx) {
      VMCNT(4);
      RAW_BARRIER();
    }
    PT(10);
    if (!isx && w == 0) __builtin_amdgcn_raw_buffer_store_b32(0u, rs, olane() << 28, fo + flag_of(par, h), AUX_SC1);
    if (!isx || w < 4) {
      if (isx) VMCNT(3);
      else VMCNT(0);
      RAW_BARRIER();
    }
    PT(11);
    if (!isx || w < 4) {
      v32u xf;
#pragma unroll
      for (int q = 0; q < 8; ++q) {
        const v4u t = *(const LAS v4u*)(L + xr + q * 1024);
#pragma unroll
        for (int j = 0; j < 4; ++j) xf[4 * q + j] = t[j];
      }
#pragma unroll 1
      for (int it = 1; it < nit; ++it) {
        EVDECL;
        const int seg = SEGOF(it), ip = it - (isx ? 2 : 1);
        unsigned cnt, keep;
#define ST_OPS                                                                                                                 \
  : [cnt] "=&s"(cnt), [keep] "=&s"(keep), "+{v[208:239]}"(xf) EVOUT                                                            \
  : WPIN, "{v[248:251]}"(adr), "{v252}"(xr), [rs] "s"(rsv), [rx] "s"(rxv), [sc] "s"(seg * 65536 + krot * 128),                    \
    [sn] "s"(SEGOF(min(it + 1, nit - 1)) * 65536 + krot * 128),                                                                  \
    [mw] "s"(w * 1024), [so] "s"(isx ? w * 1024 : qk_addr(w >> 2, seg, h, qcl, r_a) + (w & 3) * 1024), [rt] "i"(QK_RT), \
    [fo] "s"(fo + flag_of(SEGOF(max(ip, 0)), h)), [fm] "s"(w == 0 && ip >= 0 ? 0 : 1 << 30), [tm] "s"(w == (isx ? 3 : TWY) ? 0 : 1 << 30),            \
    [tc] "s"(min(SEGOF(it + 2), NSEG - 1) * 65536 + sl * 8192), [cs] "s"((w >> 2) * __float_as_uint(CSC))                                                                   \
  : ST_CLOB, "scc", "memory"
        if constexpr (isx) asm volatile(ST_XA ST_OPS);
        else asm volatile(ST_Y ST_OPS);
        EVT2R
        if (!isx && it < NPUB) ST_FLAG(seg)
      }
      if (isx) RAW_BARRIER();
    } else {
#pragma unroll 1
      for (int it = 1;;) {
        {
          EVDECL;
          unsigned cnt;
          asm volatile(ST_XB0 XB_OPS(SEGOF(it - 1)));
        }
        if (it == nit) break;
        if (it <= 2) PT4(8 + 4 * it);
        const int nsb = it == 1 ? NSB1 : NSB;
#pragma unroll 1
        for (int k = (it == 1 ? 0 : 2) + NOB; k < nsb; ++k) BARE_BARRIER();
        if (it <= 2) PT4(9 + 4 * it);
        VMCNT(0);
        if (it <= 2) PT4(10 + 4 * it);
        BARE_BARRIER();
        if (it <= 2) PT4(11 + 4 * it);
        if (w == 4) __builtin_amdgcn_raw_buffer_store_b32(0u, rs, olane() << 28, fo + flag_of(SEGOF(it - 1), h), AUX_SC1);
#pragma unroll 1
        for (int k = nsb; k < 6; ++k) BARE_BARRIER();
        {
          EVDECL;
          EVT2R
        }
        if (++it > NSER) {
#pragma unroll 1
          for (; it < nit; ++it) {
            EVDECL;
            unsigned cnt;
            asm volatile(ST_XB XB_OPS(SEGOF(it - 1)));
            EVT2R
          }
        }
        BARE_BARRIER();
      }
    }
    ST_FLAG(SEGOF(nit - 1))
    if (isx && w == 0) __builtin_amdgcn_raw_buffer_store_b32(0u, rs, olane() << 28, fo + flag_of(SEGOF(nit - 2), h), AUX_SC1);
    return;
  }

  volatile LAS unsigned* sword = (volatile LAS unsigned*)(L + L_HI + 6400);
  if (w == 7) sword[0] = nin[0];
  RAW_BARRIER();
  const unsigned perm = __builtin_amdgcn_readfirstlane(sword[0]);
  PT(9);
  const int cown = cls_of(perm, xg);
  const int sidx = ((l15 & 2) ? 0xeeee : 0x4444) | ((l15 & 8) ? 0xeeee0000 : 0x44440000);
  int ntw = min(w ^ TWV, 1) << 30;
  asm volatile("" : "+s"(ntw));
  v2u bs = {(unsigned)(lane * 16 + (w ^ 4) * 2048), (unsigned)sidx};
  unsigned pl1 = bs[0] + 65536;
  asm volatile("" : "+v"(bs), "+v"(pl1));
  const int mg_ = NS2F(TN(0, MARG)), tol_ = NS2F(TN(13, TOL)), xl_ = NS2F(TN(14, XLATE));
  const int ncy_ = TN(1, NCY), vd_ = role ? NS2F(VLATE) : 0;
  Pace pc = {0u, NS2F(ED0), NS2F(EA0), 0};
  unsigned tg = 0, tw = 0;
  const int mo0 = R_MEM + (w >> 1) * 32768 + (w & 1) * 2048 - (w ^ 4) * 2048;
#define CH_IN WPIN, "{v[248:249]}"(bs), "{v253}"(pk), "{v254}"(pl1), [rs] "s"(rsv)
#define TLB_TOUCH                    \
  if (((r_s + s + 2) & 3) == 0)      \
    tch = __builtin_amdgcn_raw_buffer_load_b32(rs, 0, so + 2 * (int)UNIT + R_DUM + (role * 32 + h) * 128, AUX_SC1);
#define ST_ADR (lst * 32 + 32768 + 2048)
#define TIMING_WAVE(slp)                                                                             \
  {                                                                                                  \
    v2u st;                                                                                          \
    {                                                                                                \
      const int lst = olane();                                                                       \
      st = __builtin_amdgcn_raw_buffer_load_b64(rs, ST_ADR, so + R_ST + ((cown ^ xr) << 12), AUX_SC1); \
    }                                                                                                \
    SB();                                                                                            \
    if (slp) __builtin_amdgcn_s_sleep(slp);                             \
    clk_update(ck, s < 2 ? CG1 : CG2);                                                \
    if (role == 0) EV(7);                                                                            \
    SB();                                                                                            \
    if (__builtin_expect(anysent(st[0]), 0)) _Pragma("clang loop unroll(disable)") for (unsigned spins = 0;;) { \
      if (spins) __builtin_amdgcn_s_sleep(4);                                                        \
      if (spin_fail(spins, tmo, 0x30009)) break;                                                     \
      const int lst = olane();                                                                       \
      OPQ(va, ST_ADR - 2048);                                                                        \
      st = __builtin_amdgcn_raw_buffer_load_b64(rs, va, so + R_ST + ((cown ^ xr) << 12), AUX_SC1);   \
      if (!anysent(st[0])) break;                                                                       \
    }                                                                                                \
    unsigned md;                                                                                     \
    const int dm = stamp_max(ck, st, md);                                                            \
    const unsigned rf = (ck.r << 4) + dm;                                                            \
    pace_step(pc, s, rf, md, mg_, tol_, xl_);                                                     \
    tg = clk_cyc(ck, dm + pc.dq + vd_);                                                                    \
    tw = (unsigned)max(CMAX - (int)(tg - ck.m), 0);                                                  \
    EV(6);                                                                                           \
    EVDUMP(s);                                                                                       \
    EVCLK(s, ck);                                                                                    \
  }

  if constexpr (role == 0) {
    unsigned pk = w < 4 ? (g >> 1) * 512 + (g & 1) * 128 + trq * 32 + 8 * (trp ^ (2 * (g & 1)))
                        : (w & 2) * 1024 + (w & 1) * 512 + trp * 32 + (l15 >> 3) * 128 + (trq & 1) * 256 + 8 * (g ^ (trq & 2));
    asm volatile("" : "+v"(pk));
#define K_STAGE(sn)                                                                                                   \
  {                                                                                                                   \
    v4u kn[4];                                                                                                        \
    _Pragma("clang loop unroll(disable)") for (unsigned spins = 0; krdy <= (sn);) {    \
      OPQ(vfl, F_QK + flag_of(min((sn) + (olane() & 15), NSEG - 1), h));                                           \
      const u64 miss = __builtin_amdgcn_ballot_w64(__builtin_amdgcn_raw_buffer_load_b32(rs, vfl, 0, AUX_SC1) != 0u);  \
      krdy = (sn) + (miss ? __builtin_ctzll(miss) : 16);                                                              \
      if (krdy > (sn)) break;                                                                                         \
      __builtin_amdgcn_s_sleep(8);                                                                                    \
      if (spin_fail(spins, tmo, 0x30000)) break;                                                                      \
    }                                                                                                                 \
    const int lk = olane() * 16;                                                                                      \
    _Pragma("unroll") for (int q = 0; q < 4; ++q) kn[q] = __builtin_amdgcn_raw_buffer_load_b128(                      \
        rs, lk + q * 1024, qk_addr(1, sn, h, cown, r_a) + (w & 1) * QK_RT, AUX_SC1);                 \
    _Pragma("unroll") for (int q = 0; q < 4; ++q) *(LAS v4u*)(L + L_HI + 8192 + (w & 1) * 4096 + q * 1024 + lk) = kn[q]; \
  }
    int krdy = 0;
    const bool kst = w >= 4 && ((w ^ TWV) & 2);
    PT(10);
    if (kst) K_STAGE(0)
    RAW_BARRIER();
    PT(11);
    if (w < 2) {
      {
        const int lane = olane(), l15 = lane & 15, g = lane >> 4, d = ((l15 >> 1) & 2) | (l15 >> 3);
        *(LAS v4u*)(L + L_HI + 5120 + w * 2048 + lane * 16) =
            v4u{(unsigned)(R_EX + (h >> 3) * 32768 + (h & 7) * 512 + w * 256 + g * 16 + ((l15 >> 3) | (l15 & 2)) * 64 + (l15 & 4) * 2 + (l15 & 1) * 4),
                (unsigned)(R_PM + (cls_of(perm, l15 & 3) << 12) + (h >> 4) * 32768 + (h & 15) * 256 + w * 128 + g * 16 + d * 4),
                d == 2 * (h & 1) + w ? 0xffffu << (16 * (g & 1)) : 0u, 0u};
      }
#pragma unroll 1
      for (int s = 0; s < NSEG; ++s) {
        EVDECL;
        const int so = (int)OFF_STEP + s * (int)UNIT, xr = xreg(r_s + s);
#define CG(r) (so + ((cls_of(perm, (r) ^ (2 * w)) ^ xr) << 12))
        unsigned rt;
#define QA_OPS                                                                                                                    \
  : [rt] "=&s"(rt) EVOUT                                                                                                          \
  : CH_IN, [o0] "s"(so + mo0 + ((cown ^ xr) << 12)), [c0] "s"(CG(0)), [c1] "s"(CG(1)), [c2] "s"(CG(2)), [c3] "s"(CG(3)),          \
    [lo] "s"(0x3333333333333333ull), [so] "s"(so), [xr] "s"(xr << 12)                                                                                              \
  : QL_CLOB, QK_CLOB, QH_CLOB, "v250", "v251", "v252", "vcc", "scc", "memory"
        if (w == 0) asm volatile(QK_A0 QA_OPS);
        else asm volatile(QK_A1 QA_OPS);
        EVDUMP(s);
      }
    } else if (w < 4) {
#pragma unroll 1
      for (int s = 0; s < NSEG; ++s) {
        {
          EVDECL;
          const int so = (int)OFF_STEP + s * (int)UNIT, xr = xreg(r_s + s);
          unsigned rt;
          f32x4 sc[3];
          asm volatile(QK_X
                       : [rt] "=&s"(rt), "={v[192:195]}"(sc[0]), "={v[196:199]}"(sc[1]), "={v[200:203]}"(sc[2]) EVOUT
                       : CH_IN, [o0] "s"(so + mo0 + ((cown ^ xr) << 12)), [qo] "s"(qk_addr(0, s, h, cown, r_a) + (w - 2) * QK_RT)
                       : QK_CLOB, QH_CLOB, "v250", "v251", "v252", "vcc", "scc", "memory");
          const int ln = olane(), l15 = ln & 15, g = ln >> 4, l16 = ln * 16;
          EV(4);
          const int tok = 2 * l15 + w - 2 - 8 * g;
#pragma unroll
          for (int t = 0; t < 2; ++t)
#pragma unroll
            for (int j = 0; j < 4; ++j)
              if (2 * j + t > tok) sc[t][j] = -1000000000.0f * 1.4426950408889634f;
          float m0 = sc[2][0] + sc[2][2], m1 = sc[2][1] + sc[2][3];
          float mx;
          asm("v_max3_f32 %0, %1, %2, %3\n\tv_max3_f32 %0, %0, %4, %5\n\tv_max3_f32 %0, %0, %6, %7\n\tv_max3_f32 %0, %0, %8, %9\n\t"
              "v_max_f32 %0, %0, %10\n\ts_nop 1"
              : "=&v"(mx)
              : "v"(sc[0][0]), "v"(sc[0][1]), "v"(sc[0][2]), "v"(sc[0][3]), "v"(sc[1][0]), "v"(sc[1][1]), "v"(sc[1][2]), "v"(sc[1][3]),
                "v"(m0), "v"(m1));
          mx = xmax16_32(mx);
#pragma unroll
          for (int t = 0; t < 2; ++t)
#pragma unroll
            for (int j = 0; j < 4; ++j) sc[t][j] = __builtin_amdgcn_exp2f(sc[t][j] - mx);
          m0 = __builtin_amdgcn_exp2f(m0 - mx);
          m1 = __builtin_amdgcn_exp2f(m1 - mx);
          const f32x4 s4 = sc[0] + sc[1];
          const float inv = __builtin_amdgcn_rcpf(xsum16_32(((s4[0] + s4[1]) + (s4[2] + s4[3])) + (m0 + m1)));
          sc[0] *= inv;
          sc[1] *= inv;
          const v4u o = {pk2(sc[0][0], sc[1][0]), pk2(sc[0][1], sc[1][1]), pk2(sc[0][2], sc[1][2]), pk2(sc[0][3], sc[1][3])};
          const int pb = so + R_PX + (w - 2) * 40960;
          st128(o, rs, l16, pb + h * 1024);
          __builtin_amdgcn_raw_buffer_store_b32(pk2(m0 * inv, m1 * inv), rs, l15 * 16 + g * 4, pb + (32 + (h >> 2)) * 1024 + (h & 3) * 256, AUX_SC1);
          EV(6);
          EVDUMP(s);
        }
      }
    } else {
#pragma unroll 1
      for (int s = 0; s < NSEG; ++s) {
        EVDECL;
        const int so = (int)OFF_STEP + s * (int)UNIT, xr = xreg(r_s + s), sc_ = so + ((cown ^ xr) << 12);
        const int lst = olane();
        const bool fq = kst && krdy <= s + 2;
        int i0 = ((cls_of(perm, lst & 3) ^ xr) << 12) | ((lst & 4) << 9) | ((lst & 8) << 12) | ((lst >> 4) << 28) | ntw;
        int i2 = ((lst & 31) * 128 + (lst >> 5) * QK_RT) | (krdy > s + 1 ? 0 : 1 << 30);
        int i1 = (F_QK + flag_of(min(s + 1 + (lst & 15), NSEG - 1), h)) | (fq ? 0 : 1 << 30);
        v2u st;
        unsigned cnt, rt;
        int o0 = sc_ + mo0, sst = so + R_ST + role * 1024 + h * 32;
        int t1 = qk_addr(w & 1, min(s + 1, NSEG - 1), h, cown, r_a), t0 = 0;
        asm volatile("" : "+{v232}"(i0), "+{v233}"(i1), "+{v234}"(i2));
        asm volatile("" : "+s"(o0), "+s"(sst), "+s"(t0), "+s"(t1));
        asm volatile(QK_K
                     : [cnt] "=&s"(cnt), [rt] "=&s"(rt), "+{v232}"(i0), "+{v233}"(i1), "+{v234}"(i2), "={v[250:251]}"(st) EVOUT
                     : CH_IN, [o0] "s"(o0), [ckm] "s"(ck.m - ncy_), [inv] "s"(ck.inv << 16), [r16] "s"(ck.r << 4), [sst] "s"(sst), [tg] "s"(tg),
                       [tw] "s"(tw), [t0] "s"(t0), [t1] "s"(t1), [cs] "s"(CSC)
                     : QL_CLOB, QK_CLOB, "v235", "v252", "vcc", "scc", "memory");
        if (s < NSEG - 1 && w == TWV) {
          TIMING_WAVE(KSL)
        } else if (s < NSEG - 1 && kst) {
          if (fq) {
            const u64 miss = __builtin_amdgcn_ballot_w64(st[0] != 0u);
            krdy = s + 1 + (miss ? __builtin_ctzll(miss) : 16);
          }
          if (KSL2) __builtin_amdgcn_s_sleep(KSL2);
          EV(7);
          K_STAGE(s + 1)
        } else {
          __builtin_amdgcn_s_sleep(KSL);
          unsigned tch = 0;
          TLB_TOUCH
          asm volatile("" ::"v"(tch));
        }
        if (w != TWV) EV(6);
        if (w != TWV) EVDUMP(s);
      }
    }
  } else {
    int adr[4];
#pragma unroll
    for (int q = 0; q < 2; ++q) {
      const int c = 8 * (2 * q + (g >> 1) + ((g & 1) ? 0 : 4)) + (w ^ ((h >> 4) * 4));
      const int blk = (c % PO) * 2 + (h >> 4);
      adr[q] = (int)OFF_VM + blk * 32768 + ((cls_of(perm, c / PO) ^ xreg(r_a + (int)(OFF_VM / UNIT) + (blk >> 4))) << 12) +
                   (h & 15) * 256 + l15 * 16;
    }
    adr[2] = (16 * (g & 1) + 4 * (g >> 1) + trq) * 24 + (trp & 1) * 8;
    unsigned pk = L_HI + (16 * (w & 3) + l15) * 24 + 4 * g;
    asm volatile("" : "+v"(pk), "+v"(adr[0]), "+v"(adr[1]), "+v"(adr[2]));
    PT(10);
    PT(11);
    if (w < 4) {
#pragma unroll 1
      for (int s = 0; s < NSEG; ++s) {
        EVDECL;
        const int so = (int)OFF_STEP + s * (int)UNIT, xr = xreg(r_s + s);
        unsigned rt;
        asm volatile(VO_A
                     : [rt] "=&s"(rt) EVOUT
                     : CH_IN, "{v236}"(adr[0]), "{v237}"(adr[1]), "{v238}"(adr[2]), [o0] "s"(so + mo0 + ((cown ^ xr) << 12)), [vs] "s"(vm_slot(s))
                     : VO_CLOB, "vcc", "scc", "memory");
        if (w == 0) {
          unsigned tch = 0;
          TLB_TOUCH
          asm volatile("" ::"v"(tch));
        }
        EVDUMP(s);
      }
    } else {
#pragma unroll 1
      for (int s = 0; s < NSEG; ++s) {
        EVDECL;
        const int so = (int)OFF_STEP + s * (int)UNIT, xr = xreg(r_s + s);
        const int lst = olane();
        adr[3] = ((cls_of(perm, lst & 3) ^ xr) << 12) | ((lst & 4) << 9) | ((lst & 8) << 12) | ((lst >> 4) << 28) | ntw;
        unsigned cnt, rt;
        int o0 = so + mo0 + ((cown ^ xr) << 12), vs = vm_slot(s), sst = so + R_ST + role * 1024 + h * 32;
        asm volatile("" : "+{v239}"(adr[3]));
        asm volatile("" : "+s"(o0), "+s"(vs), "+s"(sst));
        asm volatile(VO_B
                     : [cnt] "=&s"(cnt), [rt] "=&s"(rt) EVOUT
                     : CH_IN, "{v236}"(adr[0]), "{v237}"(adr[1]), "{v238}"(adr[2]), "{v239}"(adr[3]), [o0] "s"(o0), [vs] "s"(vs),
                       [ckm] "s"(ck.m - ncy_), [inv] "s"(ck.inv << 16), [r16] "s"(ck.r << 4), [sst] "s"(sst), [tg] "s"(tg), [tw] "s"(tw)
                     : VO_CLOB, "vcc", "scc", "memory");
        if (w == TWV && s < NSEG - 1) TIMING_WAVE(VSL)
        if (w != TWV) EVDUMP(s);
      }
    }
  }
  if ((WBL & 2) && role == 1)
    if (w == 0 && sl == 0) asm volatile("buffer_wbl2 sc1\n\ts_waitcnt vmcnt(0)" ::: "memory");
  __builtin_amdgcn_endpgm();
}
typedef f32x2 c32;
template <bool CJ>
__device__ __forceinline__ c32 cmul(c32 a, c32 b) {
  c32 d;
  if (CJ)
    asm("v_pk_mul_f32 %0, %1, %2 op_sel:[1,1] op_sel_hi:[1,0]\n\tv_pk_fma_f32 %0, %1, %2, %0 op_sel_hi:[0,1,1] neg_hi:[0,1,0]"
        : "=&v"(d) : "v"(a), "v"(b));
  else
    asm("v_pk_mul_f32 %0, %1, %2 op_sel:[1,1] op_sel_hi:[1,0] neg_lo:[0,1]\n\tv_pk_fma_f32 %0, %1, %2, %0 op_sel_hi:[0,1,1]"
        : "=&v"(d) : "v"(a), "v"(b));
  return d;
}
__device__ __forceinline__ c32 cmuls(c32 a, c32 b) {
  c32 d;
  asm("v_pk_mul_f32 %0, %1, %2 op_sel:[1,1] op_sel_hi:[1,0] neg_lo:[0,1]\n\tv_pk_fma_f32 %0, %1, %2, %0 op_sel_hi:[0,1,1]"
      : "=&v"(d) : "v"(a), "s"(b));
  return d;
}
template <int SG>
__device__ __forceinline__ c32 addi(c32 a, c32 b) {
  c32 d;
  if (SG > 0) asm("v_pk_add_f32 %0, %1, %2 op_sel:[0,1] op_sel_hi:[1,0] neg_lo:[0,1]" : "=v"(d) : "v"(a), "v"(b));
  else asm("v_pk_add_f32 %0, %1, %2 op_sel:[0,1] op_sel_hi:[1,0] neg_hi:[0,1]" : "=v"(d) : "v"(a), "v"(b));
  return d;
}
template <int SG, bool I2 = false>
__device__ __forceinline__ void dft4(c32& x0, c32& x1, c32& x2, c32& x3) {
  const c32 a = I2 ? addi<SG>(x0, x2) : x0 + x2, b = I2 ? addi<-SG>(x0, x2) : x0 - x2, c = x1 + x3, d = x1 - x3;
  x0 = a + c;
  x1 = addi<SG>(b, d);
  x2 = a - c;
  x3 = addi<-SG>(b, d);
}
template <int SG>
__device__ __forceinline__ c32 w16(int m) {
  constexpr float C[16] = {1.f, 0.92387953251128674f, 0.70710678118654752f, 0.38268343236508977f, 0.f, -0.38268343236508977f,
                           -0.70710678118654752f, -0.92387953251128674f, -1.f, -0.92387953251128674f, -0.70710678118654752f,
                           -0.38268343236508977f, 0.f, 0.38268343236508977f, 0.70710678118654752f, 0.92387953251128674f};
  return c32{C[m & 15], (float)SG * C[(m + 12) & 15]};
}
template <int SG>
__device__ __forceinline__ void dft16(c32 (&v)[16]) {
#pragma unroll
  for (int n2 = 0; n2 < 4; ++n2) dft4<SG>(v[n2], v[4 + n2], v[8 + n2], v[12 + n2]);
#pragma unroll
  for (int k1 = 1; k1 < 4; ++k1)
#pragma unroll
    for (int n2 = 1; n2 < 4; ++n2)
      if (n2 * k1 != 4) v[4 * k1 + n2] = cmuls(v[4 * k1 + n2], w16<SG>(n2 * k1));
#pragma unroll
  for (int k1 = 0; k1 < 4; ++k1)
    if (k1 == 2) dft4<SG, true>(v[8], v[9], v[10], v[11]);
    else dft4<SG>(v[4 * k1], v[4 * k1 + 1], v[4 * k1 + 2], v[4 * k1 + 3]);
  c32 t[16];
#pragma unroll
  for (int k1 = 0; k1 < 4; ++k1)
#pragma unroll
    for (int k2 = 0; k2 < 4; ++k2) t[k1 + 4 * k2] = v[4 * k1 + k2];
#pragma unroll
  for (int i = 0; i < 16; ++i) v[i] = t[i];
}
template <int SG>
__device__ __forceinline__ void dft8(c32 (&v)[8]) {
#pragma unroll
  for (int n2 = 0; n2 < 2; ++n2) dft4<SG>(v[n2], v[2 + n2], v[4 + n2], v[6 + n2]);
#pragma unroll
  for (int k1 = 1; k1 < 4; k1 += 2) v[2 * k1 + 1] = cmuls(v[2 * k1 + 1], w16<SG>(2 * k1));
  c32 t[8];
#pragma unroll
  for (int k1 = 0; k1 < 4; ++k1) {
    t[k1] = k1 == 2 ? addi<SG>(v[4], v[5]) : v[2 * k1] + v[2 * k1 + 1];
    t[k1 + 4] = k1 == 2 ? addi<-SG>(v[4], v[5]) : v[2 * k1] - v[2 * k1 + 1];
  }
#pragma unroll
  for (int i = 0; i < 8; ++i) v[i] = t[i];
}
__device__ __forceinline__ void lds_fence_wave() {
  __builtin_amdgcn_s_waitcnt(0xc07f);
  asm volatile("" ::: "memory");
}
template <int SG>
__device__ __forceinline__ void twiddle16(c32 (&u)[16], c32 w1, c32 w4) {
  const c32 w2 = cmul<false>(w1, w1), w3 = cmul<false>(w2, w1), w8 = cmul<false>(w4, w4), w12 = cmul<false>(w8, w4);
  const c32 lo[4] = {w1, w1, w2, w3}, hi[4] = {w4, w4, w8, w12};
#pragma unroll
  for (int k = 1; k < 16; ++k)
    u[k] = cmul<(SG > 0)>(u[k], (k & 3) ? ((k >> 2) ? cmul<false>(hi[k >> 2], lo[k & 3]) : lo[k & 3]) : hi[k >> 2]);
}
template <int N, int SG>
__device__ __forceinline__ void wave_fft(c32 (&v)[N / 64], c32* eb, const c32* tw, int lane) {
  constexpr int P = N / 64, B = P / 16, R3 = N / 256, M = N / 16, S1 = M + R3, S2 = 256 + 16 / R3, TS = 2048 / N;
  asm volatile("" : "+v"(lane));
#pragma unroll
  for (int b = 0; b < B; ++b) {
    c32 u[16];
#pragma unroll
    for (int n1 = 0; n1 < 16; ++n1) u[n1] = v[b + B * n1];
    dft16<SG>(u);
    const int n2 = lane + 64 * b;
    twiddle16<SG>(u, tw[n2 * TS], tw[n2 * TS * 4]);
#pragma unroll
    for (int k1 = 0; k1 < 16; ++k1) eb[k1 * S1 + n2] = u[k1];
    __builtin_amdgcn_sched_barrier(0);
  }
  lds_fence_wave();
  {
    c32 u[B][16];
#pragma unroll
    for (int b = 0; b < B; ++b) {
      const int q = lane + 64 * b, k1 = q / R3, m2 = q % R3;
#pragma unroll
      for (int m1 = 0; m1 < 16; ++m1) u[b][m1] = eb[k1 * S1 + m1 * R3 + m2];
    }
    const c32 w1 = tw[16 * (lane % R3) * TS], w4 = tw[64 * (lane % R3) * TS];
    lds_fence_wave();
#pragma unroll
    for (int b = 0; b < B; ++b) {
      const int q = lane + 64 * b, k1 = q / R3, m2 = q % R3;
      dft16<SG>(u[b]);
      twiddle16<SG>(u[b], w1, w4);
#pragma unroll
      for (int j1 = 0; j1 < 16; ++j1) eb[m2 * S2 + k1 + 16 * j1] = u[b][j1];
      __builtin_amdgcn_sched_barrier(0);
    }
  }
  lds_fence_wave();
#pragma unroll
  for (int c = 0; c < 4; ++c) {
    c32 u[R3];
#pragma unroll
    for (int m2 = 0; m2 < R3; ++m2) u[m2] = eb[m2 * S2 + lane + 64 * c];
    if constexpr (R3 == 4) dft4<SG>(u[0], u[1], u[2], u[3]);
    else dft8<SG>(u);
#pragma unroll
    for (int j2 = 0; j2 < R3; ++j2) v[c + 4 * j2] = u[j2];
  }
  lds_fence_wave();
}

constexpr int FFT_EB = 2176;
constexpr int LDS_TW = 0;
constexpr int LDS_EB = 16384;
constexpr int LDS_STG = 16384 + 8 * 1088 * 8;

__device__ __forceinline__ void fft_table(char* lds, int w) {
  c32* tw = (c32*)(lds + LDS_TW);
  {
    const int m = w * 64 + olane();
    float sn, cs;
    sincospif(-(float)m * (1.f / 1024.f), &sn, &cs);
    tw[m] = c32{cs, sn};
  }
}
#define XSK 12
__device__ __forceinline__ int x1_chunk(int kb, int rb) { return kb * 128 + ((rb + XSK * kb) & 127); }
#define F1NT true
__device__ __forceinline__ void fft_in1(const u16* x, const u16* wr, const u16* wi, int task, int w) {
  const int lane = olane(), r0 = task * 16 + 2 * w;
  const int eo = LDS_EB + w * 8704, fo = r0 * DF * 2;
#pragma unroll
  for (int q = 0; q < 4; ++q) glds16<F1NT>(x, r0 * D * 2 + q * 1024 + lane * 16, eo + q * 1024);
#pragma unroll
  for (int q = 0; q < 3; ++q)
    if (q < 2 || lane < 2) {
      const int o = min((fo & ~15) + q * 1024 + lane * 16, S * DF * 2 - 16);
      glds16<F1NT>(wr, o, eo + 4096 + q * 1024);
      glds16<F1NT>(wi, o, eo + 6176 + q * 1024);
    }
}
__device__ __forceinline__ void fft_pass1(char* ws, const u16* __restrict__ x, const u16* __restrict__ wr,
                                          const u16* __restrict__ wi, int task, int w, const u16* xb) {
  char* lds = g_lds;
  const __amdgpu_buffer_rsrc_t rx = __builtin_amdgcn_make_buffer_rsrc(ws + OFF_X1, 0, 65 * 131072, 0x00020000);
  const int lane = olane(), tid_ = w * 64 + lane;
  const c32* tw = (const c32*)(lds + LDS_TW);
  c32* eb = (c32*)(lds + LDS_EB) + w * 1088;
  c32(*stg)[16] = (c32(*)[16])(lds + LDS_STG);
  const int r0 = task * 16 + 2 * w;
#define F1T(i)
  F1T(0);
  const int fo = r0 * DF * 2;
  if (xb) {
    unsigned keep;
    asm volatile("s_mov_b32 %0, m0\n\ts_mov_b32 m0, %3\n\ts_nop 0\n\tglobal_load_lds_dword %1, %2\n\ts_mov_b32 m0, %0"
                 : "=&s"(keep) : "v"(task * 65536 + tid_ * 128), "s"(xb), "s"(160000 + w * 256) : "memory");
  } else {
    fft_in1(x, wr, wi, task, w);
    VMCNT(0);
  }
  F1T(1);
  c32 v[16];
  const u16* in = (const u16*)eb;
#pragma unroll
  for (int t = 0; t < 16; ++t) v[t] = c32{bf2f(in[lane + 64 * t]), bf2f(in[D + lane + 64 * t])};
  unsigned fw[2][9];
#pragma unroll
  for (int r = 0; r < 2; ++r)
#pragma unroll
    for (int t = 0; t < 9; ++t) {
      const int o = 2048 + ((fo & 15) >> 1) + r * DF + min(lane + 64 * t, 512);
      fw[r][t] = (unsigned)in[o] | ((unsigned)in[o + 1040] << 16);
    }
  lds_fence_wave();
  F1T(2);
  wave_fft<1024, -1>(v, eb, tw, lane);
  F1T(3);
#pragma unroll
  for (int t = 0; t < 16; ++t) eb[lane + 64 * t] = v[t];
  lds_fence_wave();
#pragma unroll
  for (int t = 0; t < 9; ++t) {
    const int k = lane + 64 * t;
    if (k <= 512) {
      c32 z = eb[k], zc = eb[(1024 - k) & 1023];
      *(f32x4*)&stg[k][2 * (w ^ (k & 7))] = f32x4{0.5f * (z.x + zc.x), 0.5f * (z.y - zc.y), 0.5f * (z.y + zc.y), -0.5f * (z.x - zc.x)};
    }
  }
  __syncthreads();
  F1T(4);
#pragma unroll
  for (int i = 0; i < 9; ++i) {
    const int idx = tid_ + 512 * i, k = min(idx >> 3, 512), p = idx & 7;
    v4u d = *(const v4u*)&stg[k][2 * (p ^ (k & 7))];
    st128(d, rx, idx < 513 * 8 ? lane * 16 : OOB, x1_chunk(min(w + 8 * i, 64), task) * 1024);
  }
  {
    const __amdgpu_buffer_rsrc_t rt = __builtin_amdgcn_make_buffer_rsrc(ws + OFF_WT, 0, 65 * 65536, 0x00020000);
    v2u* s2 = (v2u*)(lds + LDS_EB);
#pragma unroll
    for (int t = 0; t < 9; ++t) {
      const int k = lane + 64 * t;
      if (k <= 512) s2[k * 8 + (w ^ (k & 6))] = v2u{fw[0][t], fw[1][t]};
    }
    __syncthreads();
#pragma unroll
    for (int i = 0; i < 5; ++i) {
      const int idx = tid_ + 512 * i, k = min(idx >> 2, 512), p = idx & 3;
      const v4u d = *(const v4u*)&s2[k * 8 + ((2 * p) ^ (k & 6))];
      __builtin_amdgcn_raw_buffer_store_b128(d, rt, idx < 514 * 4 ? x1_chunk(idx >> 5, task) * 512 + (idx & 31) * 16 : OOB, 0, AUX_SC1);
    }
  }
}
__device__ __forceinline__ void fft_pass2(char* ws, int task, int w) {
  char* lds = g_lds;
  const __amdgpu_buffer_rsrc_t rx = __builtin_amdgcn_make_buffer_rsrc(ws + OFF_X1, 0, 65 * 131072, 0x00020000);
  const int lane = olane();
  const int k = task * 8 + w;
  const c32* tw = (const c32*)(lds + LDS_TW);
  c32* eb = (c32*)(lds + LDS_EB) + w * FFT_EB;
#define F2T(i)
  if (k > 512) return;
  F2T(0);
  c32 v[32];
  const int xoff = (lane >> 4) * 1024 + w * 128 + (lane & 15) * 8;
#pragma unroll
  for (int t = 0; t < 32; ++t) {
    v2u d = __builtin_amdgcn_raw_buffer_load_b64(rx, xoff, x1_chunk(task, 4 * t) * 1024, AUX_SC1);
    v[t] = c32{__uint_as_float(d[0]), __uint_as_float(d[1])};
  }
  unsigned wv[32];
  asm volatile("" : "+s"(task));
  {
    const __amdgpu_buffer_rsrc_t rt = __builtin_amdgcn_make_buffer_rsrc(ws + OFF_WT, 0, 65 * 65536, 0x00020000);
#pragma unroll
    for (int t = 0; t < 32; ++t)
      wv[t] = __builtin_amdgcn_raw_buffer_load_b32(rt, xoff >> 1, x1_chunk(task, 4 * t) * 512, AUX_SC1);
  }
  F2T(1);
  wave_fft<2048, -1>(v, eb, tw, lane);
  F2T(2);
#pragma unroll
  for (int t = 0; t < 32; ++t) v[t] = cmul<false>(v[t], c32{__uint_as_float(wv[t] << 16), __uint_as_float(wv[t] & 0xffff0000u)});
  F2T(3);
  wave_fft<2048, 1>(v, eb, tw, lane);
  F2T(4);
  asm volatile("" : "+s"(task));
#pragma unroll
  for (int t = 0; t < 32; ++t) {
    v2u d = {__float_as_uint(v[t].x), __float_as_uint(v[t].y)};
    __builtin_amdgcn_raw_buffer_store_b64(d, rx, xoff, x1_chunk(task, 4 * t) * 1024, AUX_SC1);
  }
  F2T(5);
  VMCNT(0);
  F2T(6);
}
#define OAUX3 16
#define OAUXX 0
__device__ __forceinline__ void fft_pass3(const char* ws, u16* __restrict__ out, int task, int w) {
  char* lds = g_lds;
  const __amdgpu_buffer_rsrc_t rx = __builtin_amdgcn_make_buffer_rsrc((void*)(ws + OFF_X1), 0, 65 * 131072, 0x00020000);
  const int lane = olane(), tid_ = w * 64 + lane;
  const c32* tw = (const c32*)(lds + LDS_TW);
  c32* eb = (c32*)(lds + LDS_EB) + w * 1088;
  c32(*stg)[16] = (c32(*)[16])(lds + LDS_STG);
  {
    v4u d[9];
#pragma unroll
    for (int i = 0; i < 9; ++i) {
      d[i] = __builtin_amdgcn_raw_buffer_load_b128(rx, (i < 8 ? lane : lane & 7) * 16, x1_chunk(min(w + 8 * i, 64), task) * 1024, AUX_SC1);
    }
#pragma unroll
    for (int i = 0; i < 9; ++i) {
      const int idx = tid_ + 512 * i, k = min(idx >> 3, 512), p = idx & 7;
      *(v4u*)&stg[k][2 * (p ^ (k & 7))] = d[i];
    }
  }
  __syncthreads();
  c32 v[16];
#pragma unroll
  for (int t = 0; t < 16; ++t) {
    const int n = lane + 64 * t;
    const int k = n <= 512 ? n : 1024 - n;
    const f32x4 xx = *(const f32x4*)&stg[k][2 * (w ^ (k & 7))];
    c32 xa = {xx[0], xx[1]}, xb = {xx[2], xx[3]};
    if (k == 0 || k == 512) {
      xa.y = 0.f;
      xb.y = 0.f;
    }
    v[t] = n <= 512 ? c32{xa.x - xb.y, xa.y + xb.x} : c32{xa.x + xb.y, -xa.y + xb.x};
  }
  wave_fft<1024, 1>(v, eb, tw, lane);
  const float sc = 1.f / (2048.f * 1024.f);
  const int r0 = task * 16 + 2 * w;
  const __amdgpu_buffer_rsrc_t ro = __builtin_amdgcn_make_buffer_rsrc(out, 0, S * 4096, 0x00020000);
#pragma unroll
  for (int t = 0; t < 16; ++t) {
    __builtin_amdgcn_raw_buffer_store_b16(f2bf(v[t].x * sc), ro, lane * 2 + t * 128, r0 * 4096, OAUX3);
    __builtin_amdgcn_raw_buffer_store_b16(f2bf(v[t].y * sc), ro, lane * 2 + t * 128, r0 * 4096 + 4096, OAUX3);
  }
}

#define WBP 8
#define WBQ 2
#define COT 100
#define CUP 24
#define CDN 1
#define CLO -40
#define CHI 30
#define TLW 900
#define OSL 4

#define OCK 800
#define OTRY 3
#define APR 0
#define OGN 5
#define OK1 150
#define NX2 0
#define WTO 2
#define WTS 30
#define WTN 2
#define RSO 5
#define NCAL 6
#define DOX2 200
#define DTC 100
#define NX1 4
#define NXG 2
#define NXK 5
#define OFD 200
#define STI 600
#define TNX 0
#define NXP 0
#define PACE 150
#define PFB 4
#define CV1 -20
#define CV2 125
#define EDL 0
constexpr int L_D = 16384, L_R = 32768, L_F = 122880;
__device__ __forceinline__ volatile LAS unsigned* flw(int i, int m) {
  return (volatile LAS unsigned*)((LAS char*)g_lds + L_F + i * 4 + (olane() & m) * 4);
}
template <int SLP = 0>
__device__ __forceinline__ void lds_wait_ge(int i, int m, unsigned v, gu32* tmo, unsigned code) {
  volatile LAS unsigned* p = flw(i, m);
  _Pragma("clang loop unroll(disable)") for (unsigned spins = 0;;) {
    if (__all(*p >= v)) break;
    if (SLP) __builtin_amdgcn_s_sleep(SLP);
    if (++spins > (kSpinCap << 5)) {
      tmo_report(tmo, code);
      break;
    }
  }
  asm volatile("" ::: "memory");
}
template <bool PRE>
__device__ __forceinline__ unsigned owner(const int c, const int w, const u64 nonce, const u16* __restrict__ mem0, char* ws,
                                          u16* __restrict__ out, unsigned perm, const char* wsl, const u16* ax, const u16* awr,
                                          const u16* awi) {
  LAS char* const L = (LAS char*)g_lds;
  gu32* tmo = (gu32*)(ws + OFF_TMO);
  const int lane = olane();
  const int l15 = lane & 15, g = lane >> 4;
  const __amdgpu_buffer_rsrc_t rs = __builtin_amdgcn_make_buffer_rsrc(ws, 0, (int)WS_USED, 0x00020000);
  const int xg = c / PO, li = c % PO;
  const int r_a = (int)(((uintptr_t)ws >> 19) & 7);
  const int r_s = r_a + (int)(OFF_STEP / UNIT);
  volatile LAS unsigned* fl = (volatile LAS unsigned*)(L + L_F);
  unsigned m0v = 0;
  if constexpr (PRE) {
  if (c == 0 && w == 6) hs_lead<4>(rs, true, CNT_HSO, 64u);
  if (WTO == 1 && wsl) {
    __builtin_amdgcn_s_sleep(WTS);
    for (int k = 0; k < WTN; ++k) w_touch(wsl, (li >> 1) * 128 + 64 + (li & 1) * 32 + (k & 1) * 16 - (k >> 1) * 64, 160000 + w * 256);
  }
  unsigned cal = 0;
  if (w == 0)
    st128(v4u{(unsigned)lane, 0u, 0u, 0u}, rs, (lane & 31) * 16 + (lane >> 5) * 32768, (int)OFF_SCR + (((li & 7) ^ xreg(r_a)) << 12) + (c >> 3) * 512);
  if (w == 0 && lane == 0) {
    const int cl = li & 7;
    int off = (int)OFF_SCR + ((cl ^ xreg(r_a)) << 12) + (c >> 3) * 512;
    unsigned best = 0xfffeu;
#pragma unroll 1
    for (int r = 0; r < NCAL; ++r) {
      VMCNT(0);
      const u64 t1 = __builtin_amdgcn_s_memtime();
      asm volatile("s_waitcnt lgkmcnt(0)" : "+v"(off)::"memory");
      const unsigned v = __builtin_amdgcn_raw_buffer_load_b32(rs, off, (r & 3) * 128 + (r >> 2) * 32768, AUX_SC1);
      asm volatile("s_waitcnt vmcnt(0)" ::"v"(v) : "memory");
      const u64 t2 = __builtin_amdgcn_s_memtime();
      best = min(best, (unsigned)(t2 - t1));
    }
    cal = ((unsigned)cl << 16) | best;
  }
  if (w == 0) {
    m0v = *(const unsigned*)(mem0 + (lane >> 3) * D + 16 * c + 2 * (lane & 7));
    hs_wait(rs, ws, nonce, CNT_HSO);
    unsigned id;
    asm volatile("s_getreg_b32 %0, hwreg(HW_REG_XCC_ID)" : "=s"(id));
    if (lane == 0) {
      __builtin_amdgcn_raw_buffer_store_b32(((id & 15u) << 19) | cal, rs, c * 4, (int)OFF_CAL, AUX_SC1);
      if (li == 0) __builtin_amdgcn_raw_buffer_store_b32(id & 15u, rs, xg * 4, (int)OFF_XID, AUX_SC1);
    }
  }
  __syncthreads();
  if (WTO == 2 && wsl)
    for (int k = 0; k < WTN; ++k) w_touch(wsl, (li >> 1) * 128 + 64 + (li & 1) * 32 + (k & 1) * 16 - (k >> 1) * 64, 160000 + w * 256);
  if (w == 7) {
    const unsigned pm = numa_perm(numa_load(rs, tmo), (LAS unsigned*)(L + L_F + 512));
    fl[10] = pm;
  }
  __syncthreads();
  perm = __builtin_amdgcn_readfirstlane(fl[10]);
  }
  const int xr0 = xreg(r_s + (PRE ? w : NARM + (li & 1)));
  const v4u sent = {0xffffffffu, 0xffffffffu, 0xffffffffu, 0xffffffffu};
  const int ibs = (int)OFF_VM + li * 65536 + ((cls_of(perm, xg) ^ xreg(r_a + (int)(OFF_VM / UNIT) + (li >> 3))) << 12);

  int am = (int)OFF_STEP + R_MEM + (c >> 4) * 32768 + ((cls_of(perm, g) ^ xr0) << 12) + (c & 15) * 256 + l15 * 16;
  int ap[2];
#pragma unroll
  for (int q = 0; q < 2; ++q) {
    const int o = c * 384 + (lane & 31) * 16;
    ap[q] = (lane & 31) < 24 ? (int)OFF_STEP + R_EX + (o >> 12) * 32768 + ((cls_of(perm, 2 * q + (lane >> 5)) ^ xr0) << 12) + (o & 4095)
                             : OOB;
  }
  int as = lane < 8 * NXC ? (int)OFF_STEP + R_ST + ((cls_of(perm, lane >> 3) ^ xr0) << 12) + (c >> 5) * 32768 + (c & 31) * 128 + (lane & 7) * 16
                          : OOB;
  const int ax1 = (int)OFF_STEP + R_PX + c * 1280 + lane * 16;
  const int ax2 = lane < 16 ? ax1 + 1024 : (lane < 24 ? (int)OFF_STEP + R_DUM + c * 128 + (lane - 16) * 16 : OOB);
#define ARM_REC(r)                                                                                                   \
  {                                                                                                                  \
    const int so_ = (r) * (int)UNIT, dx_ = (xreg(r_s + (r)) ^ xreg(r_s + (r) + 1)) << 12;                            \
    __builtin_amdgcn_raw_buffer_store_b128(sent, rs, am, so_, AUX_SC1);                                              \
    am ^= dx_;                                                                                                       \
    _Pragma("unroll") for (int q = 0; q < 2; ++q) {                                                                  \
      __builtin_amdgcn_raw_buffer_store_b128(sent, rs, ap[q], so_, AUX_SC1);                                         \
      ap[q] ^= dx_;                                                                                                  \
    }                                                                                                                \
    __builtin_amdgcn_raw_buffer_store_b128(sent, rs, as, so_, AUX_SC1);                                              \
    as ^= dx_;                                                                                                       \
    __builtin_amdgcn_raw_buffer_store_b128(sent, rs, ax1, so_, AUX_SC1);                                              \
    __builtin_amdgcn_raw_buffer_store_b128(sent, rs, ax2, so_, AUX_SC1);                                             \
  }
  if constexpr (PRE) {
    if (WTO == 3 && wsl)
      for (int k = 0; k < WTN; ++k) w_touch(wsl, (li >> 1) * 128 + 64 + (li & 1) * 32 + (k & 1) * 16 - (k >> 1) * 64, 160000 + w * 256);
    fft_in1(ax, awr, awi, c, w);
    if (w < NARM + (li & 1)) {
      if (w == 0) {
        const int row = lane >> 3, np = lane & 7;
#pragma unroll
        for (int r = 0; r < NXC; ++r)
          __builtin_amdgcn_raw_buffer_store_b32(min(m0v, 0xfffffffeu), rs,
                                                (int)OFF_STEP + R_MEM + (c >> 4) * 32768 + ((cls_of(perm, r) ^ xr0) << 12) + (c & 15) * 256 +
                                                    ((row & 1) + 2 * (np & 1) + 4 * (row >> 1)) * 16 + (np >> 1) * 4,
                                                0, AUX_SC1);
        am = OOB;
      }
      ARM_REC(w)
    }
#pragma unroll
    for (int k = 0; k < 4; ++k)
      __builtin_amdgcn_raw_buffer_store_b128(sent, rs, lane * 16 + k * 1024, ibs + vm_slot(w >> 1) + (w & 1) * 32768, AUX_SC1);
    if (c == 0 && w == 7) __builtin_amdgcn_raw_buffer_store_b32(perm, rs, lane << 28, OFF_PERM, AUX_SC1);
  }
  if constexpr (PRE) return perm;
  __syncthreads();
  if (w == 7) {
    if (lane < 32) fl[lane] = 0;
    arm_wait(rs, tmo);
  }
  __syncthreads();

  const int team = li & 1, c0 = c & ~1;
  const int l16 = lane * 16;
  const int cown = cls_of(perm, xg);
  if (w < 2) {
    const int eg = (c0 >> 2) & 1;
    typedef int v8i __attribute__((ext_vector_type(8)));
    v8i adr;
    adr[0] = l16;
    adr[1] = (lane >> 2) * 128 + (lane & 3) * 16;
    adr[2] = L_D + w * 8192 + eg * 4096 + l16;
    adr[3] = ((g & 1) + (l15 & 2) + 4 * ((g & 2) + (l15 & 1))) * 16 + (l15 >> 2) * 4;
    adr[4] = (l15 & 1) ? 0x03020706 : 0x05040100;
    adr[5] = ((l15 & 8) ? 0xeeee : 0x4444) | ((l15 & 2) ? 0xeeee0000 : 0x44440000);
    adr[6] = L_F + 48;
    adr[7] = adr[2] ^ 4096;
    const v4u rsv = {(unsigned)(uintptr_t)ws, (unsigned)((uintptr_t)ws >> 32) & 0xffffu, (unsigned)WS_USED, 0x00020000u};
    const int dtc_ = TN(12, DTC);
#pragma unroll 1
    for (int s = team; s < NSEG; s += 2) {
      EVDECL;
      const int so = (int)OFF_STEP + s * (int)UNIT;
      const int sp = so + R_PM + ((cown ^ xreg(r_s + s)) << 12);
      int sv = ibs + (w - team) * 65536 + vm_slot(s);
      int sv1 = sv + eg * 32768, sv2 = sv + (1 - eg) * 32768, sp1 = sp + eg * 32768, sp2 = sp + (1 - eg) * 32768;
      const int xn = xreg(r_s + s + 1);
      int sm[NXC];
#pragma unroll
      for (int q = 0; q < NXC; ++q) {
        sm[q] = so + (int)UNIT + R_MEM + (c0 >> 4) * 32768 + ((cls_of(perm, q) ^ xn) << 12) + ((c0 + w) & 15) * 256;
        asm volatile("" : "+s"(sm[q]));
      }
      asm volatile("" : "+s"(sv1), "+s"(sv2), "+s"(sp1), "+s"(sp2));
      if (s >= 2) lds_wait_ge<4>(20, 3, s - 1, tmo, 0x30017);
      v4u bl[8];
      u64 tp64 = 0;
      if (s < NSEG - 1) {
        v32u blv;
        unsigned cnt, bad;
        asm volatile(OW_F
                     : [cnt] "=&s"(cnt), [st] "=&s"(bad), [tp] "=&s"(tp64), "={v[0:31]}"(blv) EVOUT
                     : "{v[112:119]}"(adr), [rs] "s"(rsv), [sv1] "s"(sv1), [sv2] "s"(sv2), [sp1] "s"(sp1), [sp2] "s"(sp2),
                       [sm0] "s"(sm[0]), [sm1] "s"(sm[1]), [sm2] "s"(sm[2]), [sm3] "s"(sm[3])
                     : OF_CLOB, "vcc", "scc", "memory");
#pragma unroll
        for (int k = 0; k < 8; ++k) bl[k] = v4u{blv[4 * k], blv[4 * k + 1], blv[4 * k + 2], blv[4 * k + 3]};
        if (__builtin_expect(bad != 0u, 0)) {
          v4u pv[8];
          _Pragma("clang loop unroll(disable)") for (unsigned spins = 0;;) {
            OPQ(va, l16);
            OPQ(v2, adr[1]);
#pragma unroll
            for (int k = 0; k < 8; ++k) bl[k] = __builtin_amdgcn_raw_buffer_load_b128(rs, va + (k & 3) * 1024, k < 4 ? sv1 : sv2, AUX_SC1);
#pragma unroll
            for (int k = 0; k < 8; ++k)
              pv[k] = __builtin_amdgcn_raw_buffer_load_b128(rs, v2 + (k & 1) * 2048 + (k >> 2) * 64, (k & 2) ? sp2 : sp1, AUX_SC1);
            unsigned m = 0;
#pragma unroll
            for (int k = 0; k < 8; ++k) m = mx4(mx4(m, pv[k]), bl[k]);
            if (!anysent(m)) break;
            __builtin_amdgcn_s_sleep(RSO);
            if (spin_fail(spins, tmo, 0x20000 + s)) break;
          }
          f32x4 a0 = {0.f, 0.f, 0.f, 0.f};
#pragma unroll
          for (int k = 0; k < 4; ++k) {
            v8u b = cat8(bl[2 * k], bl[2 * k + 1]);
#pragma unroll
            for (int j = 0; j < 8; j += 2) {
              const auto q = __builtin_amdgcn_permlane16_swap(b[j], b[j + 1], false, false);
              b[j] = q[0];
              b[j + 1] = q[1];
            }
            a0 = smf(pv[k], b, a0, adr[5]);
          }
#pragma unroll
          for (int k = 0; k < 4; ++k) {
            LAS char* const pd = L + adr[k < 2 ? 2 : 7] + (k & 1) * 2048;
            a0 = smf1(pv[4 + k], cat8(*(const LAS v4u*)pd, *(const LAS v4u*)(pd + 1024)), a0, adr[5]);
          }
          const auto q0 = __builtin_amdgcn_permlane32_swap(__float_as_uint(a0[0]), __float_as_uint(a0[2]), false, false);
          const auto q1 = __builtin_amdgcn_permlane32_swap(__float_as_uint(a0[1]), __float_as_uint(a0[3]), false, false);
          const unsigned own = pk2(__uint_as_float(q0[0]) + __uint_as_float(q0[1]), __uint_as_float(q1[0]) + __uint_as_float(q1[1]));
          const unsigned nb = (unsigned)__builtin_amdgcn_mov_dpp((int)own, 0xb1, 0xf, 0xf, true);
          const unsigned o = __builtin_amdgcn_perm(nb, own, (unsigned)adr[4]);
#pragma unroll
          for (int q = 0; q < NXC; ++q) __builtin_amdgcn_raw_buffer_store_b32(o, rs, adr[3], sm[q], AUX_SC1);
          tp64 = cyc32();
          EV(3);
        }
      } else {
        BARE_BARRIER();
        wait_cyc(*flw(12, 0));
        _Pragma("clang loop unroll(disable)") for (unsigned spins = 0;;) {
          OPQ(va, l16);
          unsigned m = 0;
#pragma unroll
          for (int k = 0; k < 8; ++k) bl[k] = __builtin_amdgcn_raw_buffer_load_b128(rs, va + (k & 3) * 1024, k < 4 ? sv1 : sv2, AUX_SC1);
#pragma unroll
          for (int k = 0; k < 8; ++k) m = mx4(m, bl[k]);
          if (!anysent(m)) break;
          __builtin_amdgcn_s_sleep(RSO);
          if (spin_fail(spins, tmo, 0x20000 + s)) break;
        }
      }
      const unsigned tp = (unsigned)tp64;
      *flw(2 + w, 0) = s + 1;
      if (s < NSEG - 1) {
        const v4u tv = *(volatile LAS v4u*)flw(12, 0);
        const int lr = olane();
        __builtin_amdgcn_raw_buffer_store_b32(min(((tp - tv[1]) * tv[2]) >> 16, 60000u), rs,
                                              ((cls_of(perm, lr & 3) ^ xn) << 12) | ((lr & 4) << 9) | ((lr & 8) << 12) | ((lr >> 4) << 28),
                                              so + (int)UNIT + R_ST + (c0 + w) * 32 + 4, AUX_SC1);
      }
      OPQ(vs, l16);
#pragma unroll
      for (int k = 0; k < 8; ++k) *(LAS v4u*)(L + w * 8192 + ((k ^ (4 * eg)) * 1024) + vs) = bl[k];
      asm volatile("s_waitcnt lgkmcnt(0)" ::: "memory");
      *flw(4 + w, 0) = s + 1;
      if (s < NSEG - 4) {
        lds_wait_ge(2, 1, s + 1, tmo, 0x3001a);
        v4u se;
        asm volatile("v_mov_b32 %0, -1" : "=v"(se[0]));
        se[1] = se[2] = se[3] = se[0];
#pragma unroll
        for (int k = 0; k < 8; ++k) __builtin_amdgcn_raw_buffer_store_b128(se, rs, vs + (k & 3) * 1024, sv + (k >> 2) * 32768, AUX_SC1);
        asm volatile("s_nop 1" ::"v"(se));
      }
      if (s < NSEG - 2) {
        wait_cyc(tp + dtc_);
        const int lt = olane();
        const unsigned tc = __builtin_amdgcn_raw_buffer_load_b32(rs, lt < 40 ? lt * 128 : OOB, so + R_PX + ((li & ~1) + w) * 5120, AUX_SC1);
        asm volatile("" ::"v"(tc));
      }
      EVDUMP(s);
    }
  } else if (w < 6) {
    const int n = w - 2;
    const unsigned vpm_ = perm | (unsigned)((r_a + (int)(OFF_VWX / UNIT)) & 7) << 12;
    const int dox_ = TN(10, DOX2), pace_ = TN(11, PACE);
    v4u raw[2][8];
#define RAW_LD(dst, sg)                                                                                       \
  _Pragma("unroll") for (int t = 0; t < 2; ++t) _Pragma("unroll") for (int k = 0; k < 8; ++k) dst[t][k] =     \
      __builtin_amdgcn_raw_buffer_load_b128(rs, l16 + (k & 3) * 1024, vw_addr(vpm_, c0 + t, sg, 2 * n + (k >> 2)), AUX_SC1);
#pragma unroll
    for (int k = 0; k < 16; ++k) asm volatile("" : "=v"(raw[k >> 3][k & 7]));
    _Pragma("clang loop unroll(disable)") for (unsigned spins = 0, got = 0; got != 255u;) {
      OPQ(vfl, F_VW + flag_of(team, 8 * n + (olane() & 7)));
      const unsigned nw = (unsigned)__builtin_amdgcn_ballot_w64(__builtin_amdgcn_raw_buffer_load_b32(rs, vfl, 0, AUX_SC1) == 0u) & 255u & ~got;
      got |= nw;
#pragma unroll
      for (int k = 0; k < 8; ++k)
        if (nw >> k & 1u) {
#pragma unroll
          for (int t = 0; t < 2; ++t)
            raw[t][k] = __builtin_amdgcn_raw_buffer_load_b128(rs, l16 + (k & 3) * 1024, vw_addr(vpm_, c0 + t, team, 2 * n + (k >> 2)), AUX_SC1);
        }
      if (!nw) __builtin_amdgcn_s_sleep(OSL);
      if (spin_fail(spins, tmo, 0x30018)) break;
    }
    int nrdy = team;
#pragma unroll 1
    for (int s = team; s < NSEG; s += 2) {
      EVDECL;
      const int so = (int)OFF_STEP + s * (int)UNIT;
      if (s < NSEG - 1) {
        const int ln = olane();
        OPQ(eidx, (ln & 4) ? 0xeeee : 0x4444);
        OPQ(vea, (ln & 4) * 128 + (ln & 8) * 32 + (ln >> 4) * 64 + (ln & 3) * 16);
        int se = so + R_EX + n * 32768 + ((cown ^ xreg(r_s + s)) << 12);
        v8u bx[2][4];
#pragma unroll
        for (int t = 0; t < 2; ++t)
#pragma unroll
          for (int k = 0; k < 4; ++k) {
            const v4u u = raw[t][2 * k], v = raw[t][2 * k + 1];
            bx[t][k] = v8u{u[0], v[0], u[1], v[1], u[2], v[2], u[3], v[3]};
            asm volatile("" : "+v"(bx[t][k]));
          }
        OPQ(vd, L_D + n * 2048 + (ln & 31) * 16 + (ln >> 5) * 8);
        f32x4 d[2][4];
#pragma unroll
        for (int t = 0; t < 2; ++t)
#pragma unroll
          for (int k = 0; k < 4; ++k) d[t][k] = zero4();
        asm volatile("" : "+s"(se));
        EV(3);
        asm volatile(AL(A_OW_D));
        BARE_BARRIER();
        if (EDL) asm volatile("s_nop %0" ::"n"(EDL > 0 ? EDL - 1 : 0));
        EV(0);
#define E_PACK(t, k) v2u{pk2(d[t][k][0], d[t][k][1]), pk2(d[t][k][2], d[t][k][3])}
        _Pragma("clang loop unroll(disable)") for (unsigned spins = 0;;) {
          v4u pv[4];
          asm volatile("" : "+v"(vea));
#pragma unroll
          for (int k = 0; k < 4; ++k) pv[k] = __builtin_amdgcn_raw_buffer_load_b128(rs, vea + k * 1024, se, AUX_SC1);
          unsigned m = 0;
          v2u cv[2][4];
          SB();
#pragma unroll
          for (int k = 0; k < 4; ++k) {
            d[0][k] = smf(pv[k], bx[0][k], d[0][k], eidx);
            m = mx4(m, pv[k]);
            asm volatile("" : "+v"(m), "+v"(d[0][k]));
            d[1][k] = smf(pv[k], bx[1][k], d[1][k], eidx);
            asm volatile("" : "+v"(d[1][k]));
            if (k > 0) {
              const int j = k > 0 ? k - 1 : 0;
              asm volatile("" : "+v"(d[0][j]), "+v"(d[1][j]));
              cv[0][j] = E_PACK(0, j);
              cv[1][j] = E_PACK(1, j);
              *(LAS v2u*)(L + vd + j * 512) = cv[0][j];
              *(LAS v2u*)(L + vd + j * 512 + 8192) = cv[1][j];
            }
            SB();
          }
          EV(1);
          *(LAS v2u*)(L + vd + 1536) = E_PACK(0, 3);
          SB();
          *(LAS v2u*)(L + vd + 9728) = E_PACK(1, 3);
          if (__builtin_expect(!anysent(m), 1)) break;
          __builtin_amdgcn_s_sleep(RSO);
          if (spin_fail(spins, tmo, 0x10000 + s)) break;
#pragma unroll
          for (int t = 0; t < 2; ++t)
#pragma unroll
            for (int k = 0; k < 4; ++k) d[t][k] = zero4();
        }
        EV(2);
        RAW_BARRIER();
      } else BARE_BARRIER();
      v4u nx[2][8];
      const int sn = min(s + 2, NSEG - 1);
      unsigned tpb = 0;
      if (s < NSEG - 2) {
        __builtin_amdgcn_s_sleep(5);
        lds_wait_ge(2, 1, s + 1, tmo, 0x30014);
        tpb = cyc32();
        const unsigned tw = tpb + dox_;
        _Pragma("clang loop unroll(disable)") for (unsigned spins = 0; nrdy < s + 2;) {
          const int lf = olane();
          OPQ(vfl, F_VW + flag_of(min(s + 2 + 2 * (lf >> 3), NSEG - 2 + team), 8 * n + (lf & 7)));
          const u64 miss = __builtin_amdgcn_ballot_w64(__builtin_amdgcn_raw_buffer_load_b32(rs, vfl, 0, AUX_SC1) != 0u);
          nrdy = s + 2 * (miss ? __builtin_ctzll(miss) >> 3 : 8);
          if (nrdy >= s + 2) break;
          __builtin_amdgcn_s_sleep(8);
          if (spin_fail(spins, tmo, 0x30018)) break;
        }
        wait_cyc(tw);
      }
      EV(6);
      OPQ(va, s < NSEG - 2 ? l16 : OOB);
#define NX_LD(dst, q0, q1)                                                                              \
  _Pragma("unroll") for (int q = q0; q < q1; ++q) dst[q >> 3][q & 7] = __builtin_amdgcn_raw_buffer_load_b128( \
      rs, va + (q & 3) * 1024, vw_addr(vpm_, c0 + (q >> 3), sn, 2 * n + ((q >> 2) & 1)), AUX_SC1);
      NX_LD(nx, 0, NX1)
      SB();
      if (s < NSEG - 2) wait_cyc(cyc32() + ((n + (li >> 1)) & 3) * pace_);
      else if (s == NSEG - 2 || !TLW) lds_wait_ge(4, 1, s + 1, tmo, 0x30014);
      else wait_cyc(cyc32() + TLW);
      EV(4);
      f32x4 ac[2][2];
      const int sx = so + R_PX + n * 8192, sy = so + R_PX + 32768 + n * 2048;
      v4u pv[2][10], vm[2][2];
      OPQ(vb, l16);
#define P_LD                                                                                         \
  _Pragma("unroll") for (int k = 0; k < 10; ++k) _Pragma("unroll") for (int rt = 0; rt < 2; ++rt) pv[rt][k] = \
      __builtin_amdgcn_raw_buffer_load_b128(rs, vb + (k & 3) * 1024, (k < 8 ? sx + (k >> 2) * 4096 : sy) + rt * 40960, AUX_SC1);
#define P_MM(k0, k1)                                                                                 \
  _Pragma("unroll") for (int k = k0; k < k1; ++k) _Pragma("unroll") for (int rt = 0; rt < 2; ++rt) {  \
    _Pragma("unroll") for (int t = 0; t < 2; ++t)                                                    \
      ac[rt][t] = mma(k < 8 ? raw[t][k & 7] : vm[t][k & 1], pv[rt][k], k == 0 ? f32x4{0.f, 0.f, 0.f, 0.f} : ac[rt][t]); \
    asm volatile("" : "+v"(ac[rt][0]), "+v"(ac[rt][1]));                                             \
  }
      P_LD
      SB();
      NX_LD(nx, NX1, NX1 + NX2)
      SB();
      lds_wait_ge(4, 1, s + 1, tmo, 0x30014);
#pragma unroll
      for (int t = 0; t < 2; ++t)
#pragma unroll
        for (int k = 0; k < 2; ++k) vm[t][k] = *(const LAS v4u*)(L + vb + t * 8192 + n * 2048 + k * 1024);
      SB();
      P_MM(0, 10)
      if (__builtin_expect(__builtin_amdgcn_ballot_w64(__builtin_isunordered(ac[0][0][0], ac[1][0][0])) != 0ull, 0)) {
        _Pragma("clang loop unroll(disable)") for (unsigned spins = 0;;) {
          asm volatile("" : "+v"(vb));
          P_LD
          unsigned m = 0;
#pragma unroll
          for (int k = 0; k < 10; ++k) m = mx4(mx4(m, pv[0][k]), pv[1][k]);
          if (!anysent(m)) break;
          __builtin_amdgcn_s_sleep(8);
          if (spin_fail(spins, tmo, 0x30015)) break;
        }
        P_MM(0, 10)
      }
      EV(7);
      NX_LD(raw, NX1 + NX2, 16)
      SB();
      OPQ(vr, L_R + (s & 2) * 8192 + l16);
#pragma unroll
      for (int q = 0; q < 4; ++q) *(LAS f32x4*)(L + vr + q * 4096 + n * 1024) = ac[q >> 1][q & 1];
      asm volatile("s_waitcnt lgkmcnt(0)" ::: "memory");
      fl[20 + n] = s + 1;
      lds_wait_ge(20, 3, s + 1, tmo, 0x30016);
      {
        const LAS f32x4* pr = (const LAS f32x4*)(L + vr + n * 4096);
        const f32x4 o = (pr[0] + pr[64]) + (pr[128] + pr[192]);
        const int lo = olane();
        __builtin_amdgcn_raw_buffer_store_b64(v2u{pk2(o[0], o[1]), pk2(o[2], o[3])}, __builtin_amdgcn_make_buffer_rsrc(out, 0, S * 4096, 0x00020000),
                                              (lo & 15) * 8192 + (lo >> 4) * 8, (s * 32 + (n >> 1)) * 4096 + 2048 + 32 * (c0 + (n & 1)), OAUXX);
      }
      EV(5);
#pragma unroll
      for (int q = 0; q < NX1 + NX2; ++q) raw[q >> 3][q & 7] = nx[q >> 3][q & 7];
      EVDUMP(s);
    }
  } else if (w == 6) {
    if (c == 1) hs_lead<64>(rs, false, CNT_HS, 192u);
#pragma unroll 1
    for (int s = team; s + NARM - 2 < NSEG; s += 2) {
      VMCNT(0);
      BARE_BARRIER();
      if (s + NARM >= NSEG) break;
      BARE_BARRIER();
      __builtin_amdgcn_s_sleep(5);
      lds_wait_ge(2, 1, s + 1, tmo, 0x3001b);
      if (APR) __builtin_amdgcn_s_setprio(APR);
      ARM_REC(s + NARM)
      if (s + NARM + 1 < NSEG) ARM_REC(s + NARM + 1)
      if (APR) __builtin_amdgcn_s_setprio(0);
      if ((WBL & 4) && li == 0 && s % WBP == WBQ) asm volatile("buffer_wbl2 sc1" ::: "memory");
    }
  } else {
    const int sdk_ = NS2F(TN(2, SDK)), cv2_ = NS2F(TN(4, CV2));
    int cv1_ = NS2F(TN(3, CV1));
    bool clean = false;
    Clk ck;
    clk_init(ck);
    unsigned t1 = cyc32();
    const int mg_ = NS2F(TN(0, MARG)), tol_ = NS2F(TN(13, TOL)), xl_ = NS2F(TN(14, XLATE));
    Pace pc = {0u, NS2F(ED0), NS2F(EA0), 0};
    const int sti_ = TN(15, STI), ofd_ = NS2F(TN(9, OFD));
#pragma unroll 1
    for (int s = 0; s < NSEG; ++s) {
      EVDECL;
      const bool on = (s & 1) == team;
      asm volatile(AL(A_OW_T));
      EV(3);
      if (on ? (int)(t1 - cyc32()) > OCK : s < 4) {
        wait_cyc(t1 - OCK);
        clk_update<OTRY>(ck, s < 4 ? 12 : OGN);
      }
      int k1 = ((cv1_ * (int)ck.cq) >> 16) - OK1, k2 = (cv2_ * (int)ck.cq) >> 16;
      int sst = (int)OFF_STEP + s * (int)UNIT + R_ST + ((cown ^ xreg(r_s + s)) << 12);
      asm volatile("" : "+s"(k1), "+s"(k2), "+s"(sst));
      OPQ(va, lane * 32);
      OPQ(vt, L_F + 48);
      wait_cyc(t1);
      __builtin_amdgcn_s_setprio(3);
      EV(0);
      v2u st = __builtin_amdgcn_raw_buffer_load_b64(rs, va, sst, AUX_SC1);
      if (__builtin_expect(anysent(st[0]), 0)) _Pragma("clang loop unroll(disable)") for (unsigned spins = 0;;) {
        if (spins >= 2) __builtin_amdgcn_s_sleep(4);
        if (spin_fail(spins, tmo, 0x3000b)) break;
        asm volatile("" : "+v"(va));
        wait_cyc(t1 + spins * sti_);
        st = __builtin_amdgcn_raw_buffer_load_b64(rs, va, sst + (spins == 1 ? 2048 : (spins == 2 ? 32768 : 0)), AUX_SC1);
        if (spins > 2) clk_update(ck, 8);
        if (!anysent(st[0])) break;
      }
      EV(4);
      u64 tn;
      asm volatile("s_memtime %0" : "=s"(tn), "+v"(st));
      unsigned md;
      const int dm = stamp_max(ck, st, md);
      const unsigned tr = clk_cyc(ck, dm);
      if (on) {
        asm volatile("s_waitcnt lgkmcnt(0)" : "+s"(tn));
        *(volatile LAS v4u*)(L + vt) = v4u{tr + k2, tr, ck.inv, 0u};
        EV(1);
        int d = (int)(tr + k1 - (unsigned)tn);
        d = (unsigned)d > (unsigned)CMAX ? 0 : d;
        asm volatile(".p2align 4\n1: s_sub_u32 %0, %0, 28\n\ts_cbranch_scc0 1b\n\ts_barrier" : "+s"(d)::"scc", "memory");
        if (s < NSEG - 1) BARE_BARRIER();
      }
      __builtin_amdgcn_s_setprio(0);
      EV(5);
      const bool ol = md - (unsigned)(pc.ed + NS2F(COT)) < 60000u;
      if (s > 8) cv1_ = ol && clean ? min(cv1_ + CUP, NS2F(CHI)) : max(cv1_ - CDN, NS2F(CLO));
      pace_step(pc, s, (ck.r << 4) + dm, md, mg_, tol_, xl_);
      clean = !ol && pc.dq == pc.ed + mg_;
      t1 = clk_cyc(ck, dm + pc.dq + pc.ea - sdk_ + (on ? ofd_ : 0));
    }
  }
  return 0u;
}
#define CPE 0
#define FSL 32
__global__ __launch_bounds__(512, 2) void mega_kernel(const u16* __restrict__ x, const u16* __restrict__ mem0,
                                                      const u16* __restrict__ Wq, const u16* __restrict__ Wk,
                                                      const u16* __restrict__ Wv, const u16* __restrict__ Wo,
                                                      char* __restrict__ ws, u16* __restrict__ out,
                                                      const u16* __restrict__ ax, const u16* __restrict__ awr,
                                                      const u16* __restrict__ awi) {
  asm volatile(".Lkbeg:");
  if (CPE) {
    unsigned keep, t;
    const int w0 = __builtin_amdgcn_readfirstlane(threadIdx.x >> 6);
    asm volatile("s_getpc_b64 vcc\n.Lce:\n\ts_mov_b32 %0, m0\n\ts_mov_b32 m0, %4\n\ts_mov_b32 %1, .Lrole0-.Lce\n\t"
                 "s_cmp_eq_u32 %3, 1\n\ts_cselect_b32 %1, .Lrole1-.Lce, %1\n\ts_cmp_eq_u32 %3, 2\n\ts_cselect_b32 %1, .Lrole2-.Lce, %1\n\t"
                 "s_cmp_eq_u32 %3, 3\n\ts_cselect_b32 %1, .Lrole3-.Lce, %1\n\t"
                 "s_add_u32 vcc_lo, vcc_lo, %1\n\ts_addc_u32 vcc_hi, vcc_hi, 0\n\t"
                 "global_load_lds_dword %2, vcc\n\ts_mov_b32 m0, %0"
                 : "=&s"(keep), "=&s"(t)
                 : "v"(((threadIdx.x & 7) | (threadIdx.x >> 7) << 3) * 128), "s"((w0 & 1) | (blockIdx.x & 4) >> 1),
                   "s"((blockIdx.x & 132) == 128 ? 160000 + w0 * 256 : 32768 + w0 * 2048)
                 : "vcc", "scc", "memory");
  }
  ws += (size_t)(-(uintptr_t)ws) & (UNIT - 1);
  const int bx = blockIdx.x;
  const int w = __builtin_amdgcn_readfirstlane(threadIdx.x >> 6);
  const int xg = bx & 7, idx = bx >> 3;
#define TEND
#define TMID(k)
  const u64 nonce = hs_post(__builtin_amdgcn_make_buffer_rsrc(ws, 0, 65536, 0x00020000), w);
  TMID(0);
  int role, h, par = 0;
  if (xg < NXC) {
    role = idx >= 2 * PQ ? 4 : idx >= PQ;
    h = xg * PQ + idx % PQ;
  } else {
    role = 2 + (idx / PQ & 1);
    par = idx / (2 * PQ);
    h = (xg - NXC) * PQ + idx % PQ;
  }
  if (CPF && role == 4 && (w == 1 || w == 2)) code_touch(idx - 16 * (w - 1), 160000 + 256 * w);
  if (role == 0) wroles<0>(h, par, idx & 7, xg, w, nonce, x, Wq, Wk, Wv, Wo, ws);
  if (role == 1) wroles<1>(h, par, idx & 7, xg, w, nonce, x, Wq, Wk, Wv, Wo, ws);
  if (role == 2) wroles<2>(h, par, idx & 7, xg, w, nonce, x, Wq, Wk, Wv, Wo, ws);
  if (role == 3) wroles<3>(h, par, idx & 7, xg, w, nonce, x, Wq, Wk, Wv, Wo, ws);
  const int oc = xg * PO + idx - 2 * PQ;
  unsigned perm = 0;
  if (role == 4) perm = owner<true>(oc, w, nonce, mem0, ws, out, 0u, w_slow(Wq, Wk, Wv, w, xg), ax, awr, awi);
  TMID(2);
  char* lds = g_lds;
  const __amdgpu_buffer_rsrc_t rs = __builtin_amdgcn_make_buffer_rsrc(ws, 0, 65536, 0x00020000);
  gu32* tmo = (gu32*)(ws + OFF_TMO);
  volatile LAS int* sact = (volatile LAS int*)(g_lds + 163584);
  unsigned idle = 0;
#define TLOG(id)
  auto pick = [&]() __attribute__((always_inline)) {
    int off = CNT_FFT1 + (olane() & 7) * 128;
    asm volatile("" : "+v"(off));
    const unsigned v = __builtin_amdgcn_raw_buffer_load_b32(rs, off, 0, AUX_SC1) + 1u;
    const unsigned d1 = __builtin_amdgcn_readlane(v, 0), d2 = __builtin_amdgcn_readlane(v, 1);
    const unsigned q1 = __builtin_amdgcn_readlane(v, 4), q2 = __builtin_amdgcn_readlane(v, 5), q3 = __builtin_amdgcn_readlane(v, 6);
    int kind = -1, qoff = 0;
    unsigned lim = 0;
    if (q1 < 64u) kind = 1, qoff = Q_FFT1, lim = 64u;
    else if (d1 >= 128u && q2 < 65u) kind = 2, qoff = Q_FFT2, lim = 65u;
    else if (d2 >= 65u && q3 < 128u) kind = 3, qoff = Q_FFT3, lim = 128u;
    else if (q3 >= 128u) kind = -2;
    int act = kind;
    if (kind >= 0) {
      unsigned t = 0;
      if (olane() == 0) t = __hip_atomic_fetch_add((gu32*)(ws + qoff), 1u, RLX_AGENT) + 1u;
      t = __builtin_amdgcn_readfirstlane(t);
      act = t < lim ? (kind << 16) | (int)t : -3;
    }
    return act;
  };
  fft_table(lds, w);
  int done = role == 4 ? CNT_ARM : 0;
  for (;;) {
    if (w == 7) *sact = role == 4 ? (done == CNT_ARM ? 0x10040 | oc : -2) : pick();
    VMCNT(0);
    RAW_BARRIER();
    if (done && w == 0 && olane() == 0) __hip_atomic_fetch_add((gu32*)(ws + done), 1u, RLX_AGENT);
    done = 0;
    const int act = __builtin_amdgcn_readfirstlane(*sact);
    RAW_BARRIER();
    if (act == -2) break;
    if (act >= 0) {
      const int kind = act >> 16, t = act & 0xffff;
      TLOG(1000 + kind);
      if (kind == 1) {
        fft_pass1(ws, ax, awr, awi, t ^ 64, w, role == 4 ? x : nullptr);
        done = CNT_FFT1;
      } else if (kind == 2) {
        fft_pass2(ws, t == 0 ? 64 : t - 1, w);
        done = CNT_FFT2;
      } else fft_pass3(ws, out, t, w);
      TLOG(999);
    } else if (act == -1) {
      __builtin_amdgcn_s_sleep(FSL);
      if (spin_fail(idle, tmo, 0x30022)) break;
    }
  }
  if (role == 4) {
    owner<false>(oc, w, nonce, mem0, ws, out, perm, nullptr, nullptr, nullptr, nullptr);
    return;
  }
  if (WBL & 1)
    if (w == 0) asm volatile("buffer_wbl2 sc1\n\ts_waitcnt vmcnt(0)" ::: "memory");
  TEND;
}

extern "C" void kernel_launch(void* const* d_in, const int* in_sizes, int n_in, void* d_out, int out_size, void* d_ws,
                              size_t ws_size, hipStream_t stream) {
  const u16* A_x = (const u16*)d_in[0];
  const u16* A_wr = (const u16*)d_in[1];
  const u16* A_wi = (const u16*)d_in[2];
  const u16* B_x = (const u16*)d_in[3];
  const u16* B_mem0 = (const u16*)d_in[4];
  const u16* Wq = (const u16*)d_in[5];
  const u16* Wk = (const u16*)d_in[6];
  const u16* Wv = (const u16*)d_in[7];
  const u16* Wo = (const u16*)d_in[8];
  u16* out = (u16*)d_out;
  char* ws = (char*)d_ws;

  mega_kernel<<<256, 512, 0, stream>>>(B_x, B_mem0, Wq, Wk, Wv, Wo, ws, out, A_x, A_wr, A_wi);
}
```

```cpp
#include <hip/hip_runtime.h>
#include <hip/hip_bf16.h>
#include <stdint.h>
#include <type_traits>

typedef unsigned short u16;
typedef unsigned long long u64;
using bf16x8 = __attribute__((ext_vector_type(8))) __bf16;
using bf16x16 = __attribute__((ext_vector_type(16))) __bf16;
using f32x4 = __attribute__((ext_vector_type(4))) float;
typedef unsigned v32u __attribute__((ext_vector_type(32)));
typedef unsigned v16u __attribute__((ext_vector_type(16)));
typedef unsigned v8u __attribute__((ext_vector_type(8)));
typedef unsigned v4u __attribute__((ext_vector_type(4)));
typedef unsigned v2u __attribute__((ext_vector_type(2)));
typedef short s16x4 __attribute__((ext_vector_type(4)));
typedef float f32x2 __attribute__((ext_vector_type(2)));
#define GAS __attribute__((address_space(1)))
#define LAS __attribute__((address_space(3)))
typedef GAS unsigned gu32;
#define RLX_AGENT __ATOMIC_RELAXED, __HIP_MEMORY_SCOPE_AGENT

constexpr int S = 2048, D = 1024, H = 32, DH = 128, SEG = 32, NSEG = 64;
constexpr int DF = 513;

constexpr int NXC = 4;
constexpr int PQ = 32 / NXC, PO = 64 / NXC;

__shared__ __attribute__((aligned(16))) char g_lds[163840];
#define SPINCAP (1u << 18)
constexpr unsigned kSpinCap = SPINCAP;
constexpr int AUX_SC1 = 16;
constexpr int OOB = 0x7ff00000;
constexpr size_t UNIT = 524288;
constexpr int F_QK = 0, F_VW = 8192, CNT_FFT1 = 16384, CNT_FFT2 = 16512, CNT_ARM = 16640;
constexpr int Q_FFT1 = 16896, Q_FFT2 = 17024, Q_FFT3 = 17152;
constexpr size_t OFF_TMO = 20480;
#define CST 128
constexpr size_t OFF_CAL = 28672;
constexpr int OFF_PERM = 25600;
constexpr int OFF_PROG = 24576;
constexpr size_t ARM_HOST = 28672 + (64 * CST + 1023) / 1024 * 1024;
constexpr int OFF_REQ = 49152, OFF_ACK = 53248, CNT_HS = 17280, CNT_HSO = 17408;
constexpr size_t OFF_SCR = 65536;
constexpr size_t OFF_VM = UNIT;
constexpr int VM_SLOT = 4 << 20, VM_SLOT2 = 8 << 20;
constexpr size_t OFF_DBG = 3 * UNIT;
constexpr size_t OFF_STEP = 27 * UNIT;
constexpr int R_MEM = 0, R_EX = 131072, R_PM = 262144, R_ST = 327680, R_PX = 393216, R_DUM = 475136;
constexpr size_t OFF_QK = (size_t)48 << 20;
constexpr size_t OFF_X1 = (size_t)80 << 20;
constexpr size_t OFF_VWX = (size_t)89 << 20;
constexpr size_t WS_USED = (size_t)244 << 20;
constexpr size_t OFF_WT = (size_t)244 << 20;
constexpr int NARM = 4;

__device__ __forceinline__ int olane() {
  int t;
  asm volatile("v_mbcnt_lo_u32_b32 %0, -1, 0\n\tv_mbcnt_hi_u32_b32 %0, -1, %0" : "=v"(t));
  __builtin_assume(t >= 0 && t < 64);
  return t;
}
__device__ __forceinline__ float bf2f(u16 v) { return __uint_as_float((unsigned)v << 16); }
__device__ __forceinline__ unsigned pk2(float a, float b) {
  typedef __bf16 b2 __attribute__((ext_vector_type(2)));
  f32x2 f = {a, b};
  b2 r = __builtin_convertvector(f, b2);
  return __builtin_bit_cast(unsigned, r);
}
__device__ __forceinline__ unsigned pk2s(float a, float b) { return min(pk2(a, b), 0xfffffffeu); }
__device__ __forceinline__ u16 f2bf(float f) { return (u16)(pk2(f, 0.f) & 0xffffu); }
__device__ __forceinline__ float rbf(float f) { return __uint_as_float(pk2(0.f, f)); }
__device__ __forceinline__ float opq_one() {
  float o = 1.f;
  asm("" : "+s"(o));
  return o;
}
__device__ __forceinline__ unsigned fold_pk(f32x4 a, float one) { return pk2(a[0] + a[2], __builtin_fmaf(a[1], one, a[3])); }
__device__ __forceinline__ float mul1(float a, float c) {
  float r;
  asm("v_mul_f32 %0, %2, %1" : "=v"(r) : "v"(a), "s"(c));
  return r;
}
__device__ __forceinline__ float fma1(float a, float c, float b) {
  float r;
  asm("v_fma_f32 %0, %1, %2, %3" : "=v"(r) : "v"(a), "s"(c), "v"(b));
  return r;
}
__device__ __forceinline__ int flag_of(int seg, int h) { return h * 256 + (seg & 1) * 128 + (seg >> 1) * 4; }
__device__ __forceinline__ int xreg(int r) { return ((r & 1) << 2) | ((r >> 1) & 1) | ((r >> 1) & 2); }

__device__ __forceinline__ void tmo_report(gu32* tmo, unsigned code) {
  unsigned z = 0, c = code;
  asm volatile("" : "+v"(z), "+v"(c));
  asm volatile("global_store_dword %0, %1, %2 sc1" ::"v"(z), "v"(c), "s"(tmo) : "memory");
}
__device__ __forceinline__ bool spin_fail(unsigned& spins, gu32* tmo, unsigned code) {
  asm volatile("" ::: "memory");
  if (__builtin_expect(++spins > kSpinCap, 0)) {
    tmo_report(tmo, code);
    return true;
  }
  return false;
}
__device__ __forceinline__ unsigned wave_min(unsigned v) {
  v = min(v, (unsigned)__builtin_amdgcn_mov_dpp((int)v, 0x121, 0xf, 0xf, true));
  v = min(v, (unsigned)__builtin_amdgcn_mov_dpp((int)v, 0x122, 0xf, 0xf, true));
  v = min(v, (unsigned)__builtin_amdgcn_mov_dpp((int)v, 0x124, 0xf, 0xf, true));
  v = min(v, (unsigned)__builtin_amdgcn_mov_dpp((int)v, 0x128, 0xf, 0xf, true));
  auto r = __builtin_amdgcn_permlane32_swap(v, v, false, false);
  v = min(r[0], r[1]);
  auto q = __builtin_amdgcn_permlane16_swap(v, v, false, false);
  return min(q[0], q[1]);
}
__device__ __forceinline__ float xmax16_32(float x) {
  auto r = __builtin_amdgcn_permlane32_swap(__float_as_uint(x), __float_as_uint(x), false, false);
  x = __builtin_amdgcn_fmed3f(__uint_as_float(r[0]), __uint_as_float(r[1]), __builtin_inff());
  auto q = __builtin_amdgcn_permlane16_swap(__float_as_uint(x), __float_as_uint(x), false, false);
  return __builtin_amdgcn_fmed3f(__uint_as_float(q[0]), __uint_as_float(q[1]), __builtin_inff());
}
__device__ __forceinline__ float xsum16(float x) {
  auto q = __builtin_amdgcn_permlane16_swap(__float_as_uint(x), __float_as_uint(x), false, false);
  return __uint_as_float(q[0]) + __uint_as_float(q[1]);
}
__device__ __forceinline__ float xsum16_32(float x) {
  auto r = __builtin_amdgcn_permlane32_swap(__float_as_uint(x), __float_as_uint(x), false, false);
  return xsum16(__uint_as_float(r[0]) + __uint_as_float(r[1]));
}
__device__ __forceinline__ unsigned mx4(unsigned m, v4u v) { return max(max(max(max(m, v[0]), v[1]), v[2]), v[3]); }
__device__ __forceinline__ bool anysent(unsigned m) { return __builtin_amdgcn_ballot_w64(m == 0xffffffffu) != 0ull; }

__device__ __forceinline__ unsigned now32() { return (unsigned)__builtin_amdgcn_s_memrealtime(); }
__device__ __forceinline__ unsigned cyc32() { return (unsigned)__builtin_amdgcn_s_memtime(); }
#define CMAX 14000
__device__ __forceinline__ void wait_cyc(unsigned t) {
  t = __builtin_amdgcn_readfirstlane(t);
  int d = (int)(t - cyc32());
  if (d > CMAX) return;
  while (d > 260) {
    __builtin_amdgcn_s_sleep(1);
    d = (int)(t - cyc32());
  }
  d = __builtin_amdgcn_readfirstlane(d - 70);
  if (d > 0) asm volatile(".p2align 4\n1: s_sub_u32 %0, %0, 28\n\ts_cbranch_scc0 1b" : "+s"(d)::"scc", "memory");
}
#define CLKSPAN 124u
struct Clk {
  unsigned r, m;
  unsigned cq, inv;
};
__device__ __forceinline__ bool clk_edge(unsigned& r, unsigned& m) {
  u64 t[14], m0, m1;
  unsigned n;
#define CE_S(i) "s_memrealtime %" #i "\n\ts_nop 0\n\t"
  asm volatile("s_setprio 3\n\ts_memtime %14\n\t" CE_S(0) CE_S(1) CE_S(2) CE_S(3) CE_S(4) CE_S(5) CE_S(6) CE_S(7) CE_S(8) CE_S(9) CE_S(10)
               CE_S(11) CE_S(12) "s_memtime %15\n\ts_memrealtime %13\n\ts_setprio 0\n\ts_waitcnt lgkmcnt(0)"
               : "=s"(t[0]), "=s"(t[1]), "=s"(t[2]), "=s"(t[3]), "=s"(t[4]), "=s"(t[5]), "=s"(t[6]), "=s"(t[7]), "=s"(t[8]), "=s"(t[9]),
                 "=s"(t[10]), "=s"(t[11]), "=s"(t[12]), "=s"(t[13]), "=s"(m0), "=s"(m1));
#define CE_C(i) "s_cmp_eq_u32 %" #i ", %1\n\ts_addc_u32 %0, %0, 0\n\t"
#define CE_T(i) "s"((unsigned)t[i])
  asm("s_mov_b32 %0, 0\n\t" CE_C(2) CE_C(3) CE_C(4) CE_C(5) CE_C(6) CE_C(7) CE_C(8) CE_C(9) CE_C(10) CE_C(11) CE_C(12) CE_C(13) CE_C(14)
      : "=&s"(n)
      : CE_T(0), CE_T(1), CE_T(2), CE_T(3), CE_T(4), CE_T(5), CE_T(6), CE_T(7), CE_T(8), CE_T(9), CE_T(10), CE_T(11), CE_T(12), CE_T(13)
      : "scc");
  r = (unsigned)t[0] + 4;
  m = (unsigned)m0 + n * 8;
  return (unsigned)m1 - (unsigned)m0 <= CLKSPAN;
}
__device__ __forceinline__ void clk_init(Clk& c) {
#pragma unroll 1
  for (int k = 0; k < 3; ++k)
    if (clk_edge(c.r, c.m)) break;
  c.cq = 92500u;
  c.inv = 46432u;
}
template <int TRIES = 1>
__device__ __forceinline__ void clk_update(Clk& c, int gain) {
  unsigned r, m;
  bool ok = clk_edge(r, m);
#pragma unroll 1
  for (int k = 1; k < TRIES && !ok; ++k) ok = clk_edge(r, m);
  ok |= r - c.r >= 1500u;
  const unsigned dr = r - c.r;
  const int err = (int)(m - c.m - ((dr * c.cq) >> 12));
  unsigned q = c.cq + (unsigned)((ok & (dr * gain < 6000u)) ? err * gain : 0);
  asm("s_max_i32 %0, %0, 0x101d0\n\ts_min_i32 %0, %0, 0x1b580" : "+s"(q)::"scc");
  c.cq = q;
  c.inv -= (unsigned)__mulhi((int)c.inv, (int)(q * c.inv));
  c.r = ok ? r : c.r;
  c.m = ok ? m : c.m;
}
__device__ __forceinline__ int stamp_max(const Clk& c, v2u st, unsigned& md) {
  unsigned a = st[0] - (c.r << 4) + 0x40000000u, b = st[1], x;
#define SM_S(p) "v_max_u32_dpp %2, %2, %2 " p "\n\tv_max_u32_dpp %3, %3, %3 " p "\n\ts_nop 0\n\t"
  asm volatile("s_nop 1\n\t" SM_S("row_ror:1 row_mask:0xf bank_mask:0xf") SM_S("row_ror:2 row_mask:0xf bank_mask:0xf")
               SM_S("row_ror:4 row_mask:0xf bank_mask:0xf") SM_S("row_ror:8 row_mask:0xf bank_mask:0xf")
               SM_S("row_bcast:15 row_mask:0xa bank_mask:0xf") SM_S("row_bcast:31 row_mask:0xc bank_mask:0xf")
               "v_readlane_b32 %0, %2, 63\n\tv_readlane_b32 %1, %3, 63"
               : "=s"(x), "=s"(md), "+v"(a), "+v"(b));
  return (int)(x - 0x40000000u);
}
__device__ __forceinline__ unsigned clk_cyc(const Clk& c, int d) { return c.m + (unsigned)(((long long)d * (int)c.cq) >> 16); }
#define NS2F(ns) (((ns) * 8) / 5)
struct Pace {
  unsigned rf;
  int ed, ea, dq;
};
__device__ __forceinline__ void pace_step(Pace& p, int s, unsigned rf, unsigned md, int marg, int tol, int xlate) {
  const int arr = s ? (int)(rf - p.rf) - p.dq : 0x10000000;
  const bool late = arr > p.ea + tol;
  p.ea = min(arr, p.ea + 5);
  p.ed = (int)min(md, (unsigned)p.ed + 5u);
  p.rf = rf;
  p.dq = p.ed + marg + (late ? xlate : 0);
}

template <int SLP>
__device__ __forceinline__ void wait_flag(__amdgpu_buffer_rsrc_t rs, int off, gu32* tmo, unsigned code) {
  _Pragma("clang loop unroll(disable)") for (unsigned spins = 0;;) {
    asm volatile("" : "+v"(off));
    unsigned v = __builtin_amdgcn_raw_buffer_load_b32(rs, off, 0, AUX_SC1);
    if (__all(v == 0u)) break;
    __builtin_amdgcn_s_sleep(SLP);
    if (spin_fail(spins, tmo, code)) break;
  }
}
__device__ __forceinline__ void arm_wait(__amdgpu_buffer_rsrc_t rs, gu32* tmo) {
  _Pragma("clang loop unroll(disable)") for (unsigned spins = 0;;) {
    int off = CNT_ARM;
    asm volatile("" : "+v"(off));
    if (__all(__builtin_amdgcn_raw_buffer_load_b32(rs, off, 0, AUX_SC1) + 1u >= 64u)) break;
    __builtin_amdgcn_s_sleep(8);
    if (spin_fail(spins, tmo, 0x30007)) break;
  }
}
#define RAW_BARRIER()                                    \
  do {                                                   \
    asm volatile("" ::: "memory");                       \
    __builtin_amdgcn_s_waitcnt(0xc07f);                  \
    __builtin_amdgcn_s_barrier();                        \
    asm volatile("" ::: "memory");                       \
  } while (0)
#define BARE_BARRIER()                 \
  do {                                 \
    asm volatile("" ::: "memory");     \
    __builtin_amdgcn_s_barrier();      \
    asm volatile("" ::: "memory");     \
  } while (0)
#define VMCNT(n) asm volatile("s_waitcnt vmcnt(" #n ")" ::: "memory")
#define SB() __builtin_amdgcn_sched_barrier(0)
#define OPQ(v, e) int v = (e); asm volatile("" : "+v"(v))
template <bool NT = false>
__device__ __forceinline__ void glds16(const void* base, int voff, unsigned dst) {
  unsigned keep;
  if constexpr (NT)
    asm volatile("s_mov_b32 %0, m0\n\ts_mov_b32 m0, %3\n\ts_nop 0\n\tglobal_load_lds_dwordx4 %1, %2 nt\n\ts_mov_b32 m0, %0"
                 : "=&s"(keep)
                 : "v"(voff), "s"(base), "s"(dst)
                 : "memory");
  else
  asm volatile("s_mov_b32 %0, m0\n\ts_mov_b32 m0, %3\n\ts_nop 0\n\tglobal_load_lds_dwordx4 %1, %2\n\ts_mov_b32 m0, %0"
               : "=&s"(keep)
               : "v"(voff), "s"(base), "s"(dst)
               : "memory");
}
__device__ __forceinline__ const char* w_slow(const u16* Wq, const u16* Wk, const u16* Wv, int w, int xg) {
  const char* p = (const char*)(w < 2 || w == 6 ? Wq : (w < 4 || w == 7 ? Wk : Wv));
  const int sk = 3 - (int)((uintptr_t)p >> 7);
  const int h = w < 6 ? (xg & 3) * 8 + (w & 1) * 4 + ((sk & 7) >> 1) : ((sk + 60) & 63) >> 1;
  return (h >> 3) == (xg & 3) ? p + h * 256 : nullptr;
}
__device__ __forceinline__ void w_touch(const char* p, int row, unsigned dst) {
  unsigned keep;
  const int ln = olane();
  asm volatile("s_mov_b32 %0, m0\n\ts_mov_b32 m0, %3\n\ts_nop 0\n\tglobal_load_lds_dword %1, %2\n\ts_mov_b32 m0, %0"
               : "=&s"(keep)
               : "v"((row + (ln >> 2)) * 8192 + (ln & 3) * 64), "s"(p), "s"(dst)
               : "memory");
}
#define WBL 0
#define A_VO 4
#define A_QK 3
#define A_OW 0
#define A_VO_A A_VO
#define A_VO_B A_VO
#define A_QK_A0 A_QK
#define A_QK_A1 A_QK
#define A_QK_X A_QK
#define A_QK_K A_QK
#define A_OW_F 3
#define A_OW_D 4
#define A_OW_T 2
#define AL_(k) ".p2align 6\n.fill " #k ",4,0xbf800000\n"
#define AL(k) AL_(k)
#define NSL 8
#define CPF 1
constexpr int CODE_LEN = 168960;
__device__ __forceinline__ void code_touch(int part, unsigned dst) {
  unsigned keep;
  const int v = min(part * 8192 + olane() * 128, CODE_LEN - 128);
  asm volatile("s_getpc_b64 vcc\n.Lct%=:\n\ts_sub_u32 vcc_lo, vcc_lo, .Lct%=-.Lkbeg\n\ts_subb_u32 vcc_hi, vcc_hi, 0\n\t"
               "s_mov_b32 %0, m0\n\ts_mov_b32 m0, %2\n\ts_nop 0\n\t"
               "global_load_lds_dword %1, vcc\n\ts_mov_b32 m0, %0"
               : "=&s"(keep)
               : "v"(v), "s"(dst)
               : "vcc", "scc", "memory");
}
__device__ __forceinline__ void code_ahead(int part, unsigned dst) {
  unsigned keep;
  const int v = part * 2048 + (olane() & 15) * 128;
  asm volatile("s_getpc_b64 vcc\n\ts_mov_b32 %0, m0\n\ts_mov_b32 m0, %2\n\ts_nop 0\n\tglobal_load_lds_dword %1, vcc ; AHEAD\n\ts_mov_b32 m0, %0"
               : "=&s"(keep)
               : "v"(v), "s"(dst)
               : "vcc", "memory");
}
__device__ __forceinline__ f32x4 smf(v4u a, v8u b, f32x4 c, int idx) {
  return __builtin_amdgcn_smfmac_f32_16x16x64_bf16(__builtin_bit_cast(bf16x8, a), __builtin_bit_cast(bf16x16, b), c, idx, 0, 0);
}
__device__ __forceinline__ f32x4 smf1(v4u a, v8u b, f32x4 c, int idx) {
  return __builtin_amdgcn_smfmac_f32_16x16x64_bf16(__builtin_bit_cast(bf16x8, a), __builtin_bit_cast(bf16x16, b), c, idx, 0, 1);
}
__device__ __forceinline__ f32x4 mma(v4u a, v4u b, f32x4 c) {
  return __builtin_amdgcn_mfma_f32_16x16x32_bf16(__builtin_bit_cast(bf16x8, a), __builtin_bit_cast(bf16x8, b), c, 0, 0, 0);
}
__device__ __forceinline__ void st128(v4u d, __amdgpu_buffer_rsrc_t rs, int voff, int soff) {
  __builtin_amdgcn_raw_buffer_store_b128(d, rs, voff, soff, AUX_SC1);
  asm volatile("s_nop 1" ::"v"(d));
}
__device__ __forceinline__ f32x4 zero4() {
  f32x2 a, b;
  asm volatile("v_mov_b64 %0, 0" : "=v"(a));
  asm volatile("v_mov_b64 %0, 0" : "=v"(b));
  return f32x4{a[0], a[1], b[0], b[1]};
}
__device__ __forceinline__ f32x2 zero2() {
  f32x2 a;
  asm volatile("v_mov_b64 %0, 0" : "=v"(a));
  return a;
}
__device__ __forceinline__ v4u lo4(v8u v) { return v4u{v[0], v[1], v[2], v[3]}; }
__device__ __forceinline__ v4u hi4(v8u v) { return v4u{v[4], v[5], v[6], v[7]}; }
__device__ __forceinline__ v8u cat8(v4u a, v4u b) { return v8u{a[0], a[1], a[2], a[3], b[0], b[1], b[2], b[3]}; }
__device__ __forceinline__ v32u cat32(v8u a, v8u b, v8u c, v8u d) {
  const v16u x = __builtin_shufflevector(a, b, 0, 1, 2, 3, 4, 5, 6, 7, 8, 9, 10, 11, 12, 13, 14, 15);
  const v16u y = __builtin_shufflevector(c, d, 0, 1, 2, 3, 4, 5, 6, 7, 8, 9, 10, 11, 12, 13, 14, 15);
  return __builtin_shufflevector(x, y, 0, 1, 2, 3, 4, 5, 6, 7, 8, 9, 10, 11, 12, 13, 14, 15, 16, 17, 18, 19, 20, 21, 22, 23, 24, 25, 26,
                                 27, 28, 29, 30, 31);
}
__device__ __forceinline__ v2u trd(LAS char* p) {
  return __builtin_bit_cast(v2u, __builtin_amdgcn_ds_read_tr16_b64_v4i16((LAS s16x4*)p));
}
__device__ __forceinline__ int vm_slot(int s) { return (s & 1) * VM_SLOT + ((s >> 1) & 1) * VM_SLOT2; }
__device__ __forceinline__ int cls_of(unsigned perm, int x) { return (int)((perm >> (3 * x)) & 7u); }

__device__ __forceinline__ v2u numa_load(__amdgpu_buffer_rsrc_t rs, gu32* tmo) {
  unsigned c, xid;
  _Pragma("clang loop unroll(disable)") for (unsigned spins = 0;;) {
    OPQ(a, olane() * CST);
    c = __builtin_amdgcn_raw_buffer_load_b32(rs, a, (int)OFF_CAL, AUX_SC1);
    if (__all(c != 0xffffffffu)) break;
    __builtin_amdgcn_s_sleep(NSL);
    if (spin_fail(spins, tmo, 0x30008)) break;
  }
  xid = (unsigned)__builtin_amdgcn_ds_bpermute(((olane() >> 3) & (NXC - 1)) * PO * 4, (int)c) >> 19;
  return v2u{c, xid};
}
__device__ __forceinline__ unsigned numa_perm(v2u in, LAS unsigned* tab) {
  const int lane = olane();
  const unsigned c = in[0], xid = in[1];
  tab[lane] = 0xffffu;
  tab[64 + lane] = 0xffffu;
  tab[128 + lane] = 0u;
  __builtin_amdgcn_s_waitcnt(0xc07f);
  __hip_atomic_fetch_min(tab + ((c >> 16) & 127u), c & 0xffffu, __ATOMIC_RELAXED, __HIP_MEMORY_SCOPE_WORKGROUP);
  __hip_atomic_fetch_add(tab + 128 + ((c >> 21) & 3u) * 2 + ((c >> 17) & 1u), c & 0xffffu, __ATOMIC_RELAXED, __HIP_MEMORY_SCOPE_WORKGROUP);
  __builtin_amdgcn_s_waitcnt(0xc07f);
  asm volatile("" ::: "memory");
  const unsigned s0 = tab[128 + ((xid >> 2) & 3u) * 2], s1 = tab[129 + ((xid >> 2) & 3u) * 2];
  const bool far = ((lane >> 1) & 1) != (s1 < s0 ? 1 : 0);
  unsigned key = ((tab[(xid & 15u) * 8 + (lane & 7)] + (far ? 0x10000u : 0u)) << 6) | (unsigned)lane;
  if ((lane >> 3) >= NXC) key = 0xffffffffu;
  unsigned perm = 0;
#pragma unroll 1
  for (int r = 0; r < NXC; ++r) {
    const unsigned m = wave_min(key);
    const unsigned bx = (m >> 3) & 7u, bc = m & 7u;
    perm |= bc << (3 * bx);
    if ((unsigned)(lane >> 3) == bx || (unsigned)(lane & 7) == bc) key = 0xffffffffu;
  }
  return __builtin_amdgcn_readfirstlane(perm);
}
#define VRING 32
#define VWN 0
__device__ __forceinline__ int vw_addr(unsigned pm, int tile, int seg, int gq) {
  return (int)OFF_VWX + ((((tile >> 3) * VRING + (seg & (VRING - 1))) * 8 + (tile & 7)) * 8 + gq) * 4096;
}

#define QKN 0
constexpr int QK_RT = QKN ? 32768 : 4096;
__device__ __forceinline__ int qk_addr(int mat, int seg, int h, int cl, int ra) {
  return (int)OFF_QK + ((mat * 64 + seg) * 32 + h) * 8192;
}

__device__ __forceinline__ u64 hs_post(__amdgpu_buffer_rsrc_t rs, int w) {
  const u64 n = __builtin_amdgcn_s_memrealtime();
  if (w == 0) __builtin_amdgcn_raw_buffer_store_b64(v2u{(unsigned)n, (unsigned)(n >> 32)}, rs, olane() == 0 ? 0 : OOB,
                                                    OFF_REQ + (int)blockIdx.x * 8, AUX_SC1);
  return n;
}
__device__ __forceinline__ void hs_wait(__amdgpu_buffer_rsrc_t rs, char* ws, u64 n, int cnt) {
  _Pragma("clang loop unroll(disable)") for (unsigned spins = 0; spins < (kSpinCap << 2); ++spins) {
    int z = 0;
    asm volatile("" : "+v"(z));
    const v2u v = __builtin_amdgcn_raw_buffer_load_b64(rs, z, OFF_ACK + (int)blockIdx.x * 8, AUX_SC1);
    if (__all(v[0] == (unsigned)n && v[1] == (unsigned)(n >> 32))) break;
    __builtin_amdgcn_s_sleep(8);
  }
  if (olane() == 0) __hip_atomic_fetch_add((gu32*)(ws + cnt), 1u, RLX_AGENT);
}
template <int SLP>
__device__ __forceinline__ void hs_lead(__amdgpu_buffer_rsrc_t rs, bool fill, int cnt, unsigned want) {
  const int l16 = olane() * 16;
  if (fill) {
    for (int o = 0; o < (int)ARM_HOST; o += 1024) __builtin_amdgcn_raw_buffer_store_b128(v4u{~0u, ~0u, ~0u, ~0u}, rs, l16, o, AUX_SC1);
    VMCNT(0);
  }
  _Pragma("clang loop unroll(disable)") for (unsigned spins = 0; spins < (kSpinCap << 2); ++spins) {
    int va = l16;
    asm volatile("" : "+v"(va));
    const v4u a = __builtin_amdgcn_raw_buffer_load_b128(rs, va, OFF_REQ, AUX_SC1);
    const v4u b = __builtin_amdgcn_raw_buffer_load_b128(rs, va + 1024, OFF_REQ, AUX_SC1);
    const unsigned n = __builtin_amdgcn_raw_buffer_load_b32(rs, va & 0, cnt, AUX_SC1);
    if (__all(n + 1u >= want)) break;
    __builtin_amdgcn_raw_buffer_store_b128(a, rs, va, OFF_ACK, AUX_SC1);
    __builtin_amdgcn_raw_buffer_store_b128(b, rs, va + 1024, OFF_ACK, AUX_SC1);
    __builtin_amdgcn_s_sleep(SLP);
  }
}

#define TN(k, d) (d)
#define EVDECL
#define EV(k)
#define EVDUMP(s)
#define EVCLK(s, c)
#define EVOUT
#define PX 2
#define NOB 3
#define VO_A \
  "s_branch .Lgo_%=\n" \
  AL(A_VO_A) \
  ".Lr_%=:\n" \
  "s_sub_u32 %[rt], %[rt], 1\n" \
  "s_cbranch_scc1 .Lrb_%=\n" \
  "s_sleep 2\n" \
  "buffer_load_dwordx4 v[192:195], v248, %[rs], %[o0] offen sc1\n" \
  "buffer_load_dwordx4 v[196:199], v248, %[rs], %[o0] offen offset:1024 sc1\n" \
  "v_mov_b64 v[224:225], 0\n" \
  "v_mov_b64 v[226:227], 0\n" \
  "v_mov_b64 v[228:229], 0\n" \
  "v_mov_b64 v[230:231], 0\n" \
  "s_nop 1\n" \
  "s_branch .Lw_%=\n" \
  ".Lgo_%=:\n" \
  "v_add_u32 v252, 0x20000, v248\n" \
  "ds_read_b128 v[200:203], v248 offset:32768\n" \
  "ds_read_b128 v[204:207], v248 offset:33792\n" \
  "ds_read_b128 v[208:211], v248 offset:49152\n" \
  "ds_read_b128 v[212:215], v248 offset:50176\n" \
  "ds_read_b128 v[216:219], v252 offset:0\n" \
  "ds_read_b128 v[220:223], v252 offset:1024\n" \
  "ds_read_b128 v[240:243], v252 offset:16384\n" \
  "ds_read_b128 v[244:247], v252 offset:17408\n" \
  "v_mov_b64 v[224:225], 0\n" \
  "v_mov_b64 v[226:227], 0\n" \
  "v_mov_b64 v[228:229], 0\n" \
  "v_mov_b64 v[230:231], 0\n" \
  "v_xor_b32 v250, 0x800, v248\n" \
  "v_xor_b32 v251, 0x1000, v248\n" \
  "s_mov_b32 %[rt], 0x40000\n" \
  "s_setprio 2\n" \
  "s_waitcnt lgkmcnt(0)\n" \
  "s_barrier\n" \
  "buffer_load_dwordx4 v[192:195], v248, %[rs], %[o0] offen sc1\n" \
  "buffer_load_dwordx4 v[196:199], v248, %[rs], %[o0] offen offset:1024 sc1\n" \
  ".Lw_%=:\n" \
  "s_waitcnt vmcnt(1)\n" \
  "ds_write_b128 v248, v[192:195]\n" \
  "v_smfmac_f32_16x16x64_bf16 v[224:227], v[192:195], v[200:207], v249\n" \
  "v_max3_u32 v252, v192, v193, v194\n" \
  "s_waitcnt vmcnt(0)\n" \
  "ds_write_b128 v248, v[196:199] offset:1024\n" \
  "v_smfmac_f32_16x16x64_bf16 v[228:231], v[192:195], v[208:215], v249\n" \
  "v_max3_u32 v252, v252, v195, v196\n" \
  "v_smfmac_f32_16x16x64_bf16 v[224:227], v[196:199], v[216:223], v249\n" \
  "v_max3_u32 v252, v252, v197, v198\n" \
  "v_max_u32 v252, v252, v199\n" \
  "v_smfmac_f32_16x16x64_bf16 v[228:231], v[196:199], v[240:247], v249\n" \
  "v_cmp_eq_u32 vcc, -1, v252\n" \
  "v_xor_b32 v252, 0x1800, v248\n" \
  "s_cbranch_vccnz .Lr_%=\n" \
  ".Lrb_%=:\n" \
  "s_waitcnt lgkmcnt(0)\n" \
  "s_barrier\n" \
  "ds_read_b128 v[200:203], v250 offset:0\n" \
  "ds_read_b128 v[204:207], v250 offset:1024\n" \
  "ds_read_b128 v[208:211], v251 offset:0\n" \
  "ds_read_b128 v[212:215], v251 offset:1024\n" \
  "ds_read_b128 v[216:219], v252 offset:0\n" \
  "ds_read_b128 v[220:223], v252 offset:1024\n" \
  "s_waitcnt lgkmcnt(5)\n" \
  "v_smfmac_f32_16x16x64_bf16 v[224:227], v[200:203], v[0:7], v249\n" \
  "v_smfmac_f32_16x16x64_bf16 v[228:231], v[200:203], v[48:55], v249\n" \
  "s_waitcnt lgkmcnt(4)\n" \
  "v_smfmac_f32_16x16x64_bf16 v[224:227], v[204:207], v[8:15], v249\n" \
  "v_smfmac_f32_16x16x64_bf16 v[228:231], v[204:207], v[56:63], v249\n" \
  "s_waitcnt lgkmcnt(3)\n" \
  "v_smfmac_f32_16x16x64_bf16 v[224:227], v[208:211], v[16:23], v249\n" \
  "v_smfmac_f32_16x16x64_bf16 v[228:231], v[208:211], v[64:71], v249\n" \
  "s_waitcnt lgkmcnt(2)\n" \
  "v_smfmac_f32_16x16x64_bf16 v[224:227], v[212:215], v[24:31], v249\n" \
  "s_waitcnt lgkmcnt(1)\n" \
  "v_smfmac_f32_16x16x64_bf16 v[224:227], v[216:219], v[32:39], v249\n" \
  "s_waitcnt lgkmcnt(0)\n" \
  "v_smfmac_f32_16x16x64_bf16 v[224:227], v[220:223], v[40:47], v249\n" \
  "v_smfmac_f32_16x16x64_bf16 v[228:231], v[212:215], v[72:79], v249\n" \
  "v_smfmac_f32_16x16x64_bf16 v[228:231], v[216:219], v[80:87], v249\n" \
  "s_nop 0\n" \
  "v_add_f32 v224, v224, v226\n" \
  "v_smfmac_f32_16x16x64_bf16 v[228:231], v[220:223], v[88:95], v249\n" \
  "v_add_f32 v225, v225, v227\n" \
  "ds_write_b64 v248, v[224:225] offset:16384\n" \
  "s_nop 6\n" \
  "v_add_f32 v228, v228, v230\n" \
  "v_add_f32 v229, v229, v231\n" \
  "ds_write_b64 v248, v[228:229] offset:16392\n" \
  "s_setprio 0\n" \
  "ds_read_b128 v[240:243], v254 offset:0\n" \
  "ds_read_b128 v[244:247], v254 offset:1024\n" \
  "s_waitcnt lgkmcnt(2)\n" \
  "s_barrier\n" \
  "s_nop 1\n" \
  "ds_read_b128 v[224:227], v254 offset:16384\n" \
  "ds_read_b128 v[228:231], v254 offset:17408\n" \
  "v_mov_b64 v[192:193], 0\n" \
  "v_mov_b64 v[194:195], 0\n" \
  "v_mov_b64 v[196:197], 0\n" \
  "v_mov_b64 v[198:199], 0\n" \
  "v_mov_b64 v[200:201], 0\n" \
  "v_mov_b64 v[202:203], 0\n" \
  "v_mov_b64 v[204:205], 0\n" \
  "v_mov_b64 v[206:207], 0\n" \
  "s_setprio 2\n" \
  "s_barrier\n" \
  "ds_read_b64_tr_b16 v[208:209], v238 offset:16384\n" \
  "ds_read_b64_tr_b16 v[210:211], v238 offset:16576\n" \
  "ds_read_b64_tr_b16 v[212:213], v238 offset:17152\n" \
  "ds_read_b64_tr_b16 v[214:215], v238 offset:17344\n" \
  "ds_read_b64_tr_b16 v[216:217], v238 offset:17920\n" \
  "ds_read_b64_tr_b16 v[218:219], v238 offset:18112\n" \
  "ds_read_b64_tr_b16 v[220:221], v238 offset:18688\n" \
  "ds_read_b64_tr_b16 v[222:223], v238 offset:18880\n" \
  "s_waitcnt lgkmcnt(6)\n" \
  "v_smfmac_f32_16x16x64_bf16 v[200:203], v[208:211], v[240:247], v249 abid:1\n" \
  "ds_read_b128 v[240:243], v254 offset:32768\n" \
  "v_smfmac_f32_16x16x64_bf16 v[192:195], v[208:211], v[160:167], v249 abid:1\n" \
  "ds_read_b128 v[244:247], v254 offset:33792\n" \
  "v_smfmac_f32_16x16x64_bf16 v[196:199], v[208:211], v[96:103], v249 abid:1\n" \
  "v_smfmac_f32_16x16x64_bf16 v[204:207], v[208:211], v[128:135], v249 abid:1\n" \
  "s_waitcnt lgkmcnt(6)\n" \
  "v_smfmac_f32_16x16x64_bf16 v[192:195], v[212:215], v[168:175], v249 abid:1\n" \
  "v_smfmac_f32_16x16x64_bf16 v[200:203], v[212:215], v[224:231], v249 abid:1\n" \
  "ds_read_b128 v[224:227], v254 offset:49152\n" \
  "v_smfmac_f32_16x16x64_bf16 v[196:199], v[212:215], v[104:111], v249 abid:1\n" \
  "ds_read_b128 v[228:231], v254 offset:50176\n" \
  "v_smfmac_f32_16x16x64_bf16 v[204:207], v[212:215], v[136:143], v249 abid:1\n" \
  "s_waitcnt lgkmcnt(6)\n" \
  "v_smfmac_f32_16x16x64_bf16 v[192:195], v[216:219], v[176:183], v249 abid:1\n" \
  "v_smfmac_f32_16x16x64_bf16 v[196:199], v[216:219], v[112:119], v249 abid:1\n" \
  "s_waitcnt lgkmcnt(4)\n" \
  "v_smfmac_f32_16x16x64_bf16 v[192:195], v[220:223], v[184:191], v249 abid:1\n" \
  "v_smfmac_f32_16x16x64_bf16 v[196:199], v[220:223], v[120:127], v249 abid:1\n" \
  "v_smfmac_f32_16x16x64_bf16 v[204:207], v[216:219], v[144:151], v249 abid:1\n" \
  "s_nop 0\n" \
  "v_cvt_pk_bf16_f32 v192, v192, v193\n" \
  "v_smfmac_f32_16x16x64_bf16 v[204:207], v[220:223], v[152:159], v249 abid:1\n" \
  "v_cvt_pk_bf16_f32 v193, v194, v195\n" \
  "s_waitcnt lgkmcnt(2)\n" \
  "v_smfmac_f32_16x16x64_bf16 v[200:203], v[216:219], v[240:247], v249 abid:1\n" \
  "v_cvt_pk_bf16_f32 v194, v196, v197\n" \
  "s_waitcnt lgkmcnt(0)\n" \
  "v_smfmac_f32_16x16x64_bf16 v[200:203], v[220:223], v[224:231], v249 abid:1\n" \
  "v_cvt_pk_bf16_f32 v195, v198, v199\n" \
  "v_cvt_pk_bf16_f32 v204, v204, v205\n" \
  "v_permlane16_swap_b32 v192, v194\n" \
  "v_permlane16_swap_b32 v193, v195\n" \
  "buffer_store_dwordx4 v[192:195], v236, %[rs], %[vs] offen sc1\n" \
  "v_cvt_pk_bf16_f32 v205, v206, v207\n" \
  "s_nop 2\n" \
  "v_cvt_pk_bf16_f32 v203, v202, v203\n" \
  "v_cvt_pk_bf16_f32 v202, v200, v201\n" \
  "s_nop 0\n" \
  "v_permlane16_swap_b32 v203, v205\n" \
  "v_permlane16_swap_b32 v202, v204\n" \
  "buffer_store_dwordx4 v[202:205], v237, %[rs], %[vs] offen sc1\n" \
  "s_setprio 0\n" \
  "s_nop 1\n"
#define VO_B \
  "s_branch .Lgo_%=\n" \
  AL(A_VO_B) \
  ".Lr_%=:\n" \
  "s_sub_u32 %[rt], %[rt], 1\n" \
  "s_cbranch_scc1 .Lrb_%=\n" \
  "s_sleep 2\n" \
  "buffer_load_dwordx4 v[192:195], v248, %[rs], %[o0] offen sc1\n" \
  "buffer_load_dwordx4 v[196:199], v248, %[rs], %[o0] offen offset:1024 sc1\n" \
  "v_mov_b64 v[224:225], 0\n" \
  "v_mov_b64 v[226:227], 0\n" \
  "v_mov_b64 v[228:229], 0\n" \
  "v_mov_b64 v[230:231], 0\n" \
  "s_nop 1\n" \
  "s_branch .Lw_%=\n" \
  ".Lgo_%=:\n" \
  "v_add_u32 v252, 0x20000, v248\n" \
  "ds_read_b128 v[200:203], v248 offset:32768\n" \
  "ds_read_b128 v[204:207], v248 offset:33792\n" \
  "ds_read_b128 v[208:211], v248 offset:49152\n" \
  "ds_read_b128 v[212:215], v248 offset:50176\n" \
  "ds_read_b128 v[216:219], v252 offset:0\n" \
  "ds_read_b128 v[220:223], v252 offset:1024\n" \
  "ds_read_b128 v[240:243], v252 offset:16384\n" \
  "ds_read_b128 v[244:247], v252 offset:17408\n" \
  "v_mov_b64 v[224:225], 0\n" \
  "v_mov_b64 v[226:227], 0\n" \
  "v_mov_b64 v[228:229], 0\n" \
  "v_mov_b64 v[230:231], 0\n" \
  "v_xor_b32 v250, 0x800, v248\n" \
  "v_xor_b32 v251, 0x1000, v248\n" \
  "s_mov_b32 %[rt], 0x40000\n" \
  "s_setprio 1\n" \
  "s_waitcnt lgkmcnt(0)\n" \
  "s_cmp_eq_u32 %[tw], 0\n" \
  "s_cbranch_scc1 .Ltq_%=\n" \
  ".Ltl_%=:\n" \
  "s_memtime vcc\n" \
  "s_waitcnt lgkmcnt(0)\n" \
  "s_sub_i32 %[cnt], %[tg], vcc_lo\n" \
  "s_cmp_lt_i32 %[cnt], 260\n" \
  "s_cbranch_scc1 .Ltf_%=\n" \
  "s_sleep 1\n" \
  "s_branch .Ltl_%=\n" \
  ".Ltf_%=:\n" \
  "s_max_i32 %[cnt], %[cnt], 0\n" \
  ".Ltg_%=:\n" \
  "s_sub_u32 %[cnt], %[cnt], 28\n" \
  "s_cbranch_scc0 .Ltg_%=\n" \
  ".Ltq_%=:\n" \
  "s_barrier\n" \
  "buffer_load_dwordx4 v[192:195], v248, %[rs], %[o0] offen sc1\n" \
  "buffer_load_dwordx4 v[196:199], v248, %[rs], %[o0] offen offset:1024 sc1\n" \
  ".Lw_%=:\n" \
  "s_waitcnt vmcnt(1)\n" \
  "ds_write_b128 v248, v[192:195]\n" \
  "v_smfmac_f32_16x16x64_bf16 v[224:227], v[192:195], v[200:207], v249\n" \
  "v_max3_u32 v252, v192, v193, v194\n" \
  "s_waitcnt vmcnt(0)\n" \
  "ds_write_b128 v248, v[196:199] offset:1024\n" \
  "v_smfmac_f32_16x16x64_bf16 v[228:231], v[192:195], v[208:215], v249\n" \
  "v_max3_u32 v252, v252, v195, v196\n" \
  "v_smfmac_f32_16x16x64_bf16 v[224:227], v[196:199], v[216:223], v249\n" \
  "v_max3_u32 v252, v252, v197, v198\n" \
  "v_max_u32 v252, v252, v199\n" \
  "v_smfmac_f32_16x16x64_bf16 v[228:231], v[196:199], v[240:247], v249\n" \
  "v_cmp_eq_u32 vcc, -1, v252\n" \
  "v_xor_b32 v252, 0x1800, v248\n" \
  "s_cbranch_vccnz .Lr_%=\n" \
  ".Lrb_%=:\n" \
  "s_waitcnt lgkmcnt(0)\n" \
  "s_barrier\n" \
  "s_memtime vcc\n" \
  "s_setprio 3\n" \
  "v_mov_b64 v[192:193], 0\n" \
  "v_mov_b64 v[194:195], 0\n" \
  "v_mov_b64 v[196:197], 0\n" \
  "v_mov_b64 v[198:199], 0\n" \
  "s_waitcnt lgkmcnt(0)\n" \
  "s_sub_u32 %[cnt], vcc_lo, %[ckm]\n" \
  "s_mul_hi_u32 %[cnt], %[cnt], %[inv]\n" \
  "s_add_u32 %[cnt], %[cnt], %[r16]\n" \
  "s_and_b32 %[cnt], %[cnt], -2\n" \
  "v_mov_b32 v233, %[cnt]\n" \
  "buffer_store_dword v233, v239, %[rs], %[sst] offen sc1\n" \
  "s_nop 5\n" \
  "ds_read_b128 v[200:203], v250 offset:0\n" \
  "ds_read_b128 v[204:207], v250 offset:1024\n" \
  "ds_read_b128 v[208:211], v251 offset:0\n" \
  "ds_read_b128 v[212:215], v251 offset:1024\n" \
  "ds_read_b128 v[216:219], v252 offset:0\n" \
  "ds_read_b128 v[220:223], v252 offset:1024\n" \
  "s_setprio 1\n" \
  "s_waitcnt lgkmcnt(5)\n" \
  "v_smfmac_f32_16x16x64_bf16 v[224:227], v[200:203], v[0:7], v249\n" \
  "v_smfmac_f32_16x16x64_bf16 v[228:231], v[200:203], v[48:55], v249\n" \
  "s_waitcnt lgkmcnt(4)\n" \
  "v_smfmac_f32_16x16x64_bf16 v[224:227], v[204:207], v[8:15], v249\n" \
  "v_smfmac_f32_16x16x64_bf16 v[228:231], v[204:207], v[56:63], v249\n" \
  "s_waitcnt lgkmcnt(3)\n" \
  "v_smfmac_f32_16x16x64_bf16 v[224:227], v[208:211], v[16:23], v249\n" \
  "ds_read_b128 v[240:243], v254 offset:0\n" \
  "v_smfmac_f32_16x16x64_bf16 v[228:231], v[208:211], v[64:71], v249\n" \
  "ds_read_b128 v[244:247], v254 offset:1024\n" \
  "s_waitcnt lgkmcnt(4)\n" \
  "v_smfmac_f32_16x16x64_bf16 v[224:227], v[212:215], v[24:31], v249\n" \
  "s_waitcnt lgkmcnt(3)\n" \
  "v_smfmac_f32_16x16x64_bf16 v[224:227], v[216:219], v[32:39], v249\n" \
  "s_waitcnt lgkmcnt(2)\n" \
  "v_smfmac_f32_16x16x64_bf16 v[224:227], v[220:223], v[40:47], v249\n" \
  "v_smfmac_f32_16x16x64_bf16 v[228:231], v[212:215], v[72:79], v249\n" \
  "s_barrier\n" \
  "ds_read_b128 v[232:235], v248 offset:24576\n" \
  "v_smfmac_f32_16x16x64_bf16 v[228:231], v[216:219], v[80:87], v249\n" \
  "s_nop 0\n" \
  "v_add_f32 v224, v224, v226\n" \
  "v_smfmac_f32_16x16x64_bf16 v[228:231], v[220:223], v[88:95], v249\n" \
  "v_add_f32 v225, v225, v227\n" \
  "s_waitcnt lgkmcnt(0)\n" \
  "v_add_f32 v232, v232, v224\n" \
  "v_add_f32 v233, v233, v225\n" \
  "v_cvt_pk_bf16_f32 v232, v232, v233\n" \
  "ds_write_b32 v253, v232\n" \
  "s_nop 2\n" \
  "v_add_f32 v234, v234, v228\n" \
  "v_add_f32 v235, v235, v229\n" \
  "v_add_f32 v234, v234, v230\n" \
  "v_add_f32 v235, v235, v231\n" \
  "v_cvt_pk_bf16_f32 v234, v234, v235\n" \
  "ds_write_b32 v253, v234 offset:1536\n" \
  "v_mov_b64 v[200:201], 0\n" \
  "v_mov_b64 v[202:203], 0\n" \
  "v_mov_b64 v[204:205], 0\n" \
  "v_mov_b64 v[206:207], 0\n" \
  "s_waitcnt lgkmcnt(0)\n" \
  "s_barrier\n" \
  "s_nop 7\n" \
  "ds_read_b64_tr_b16 v[208:209], v238 offset:16384\n" \
  "ds_read_b64_tr_b16 v[210:211], v238 offset:16576\n" \
  "ds_read_b128 v[224:227], v254 offset:16384\n" \
  "ds_read_b128 v[228:231], v254 offset:17408\n" \
  "ds_read_b64_tr_b16 v[212:213], v238 offset:17152\n" \
  "ds_read_b64_tr_b16 v[214:215], v238 offset:17344\n" \
  "ds_read_b64_tr_b16 v[216:217], v238 offset:17920\n" \
  "ds_read_b64_tr_b16 v[218:219], v238 offset:18112\n" \
  "ds_read_b64_tr_b16 v[220:221], v238 offset:18688\n" \
  "ds_read_b64_tr_b16 v[222:223], v238 offset:18880\n" \
  "s_waitcnt lgkmcnt(8)\n" \
  "v_smfmac_f32_16x16x64_bf16 v[200:203], v[208:211], v[240:247], v249 abid:1\n" \
  "ds_read_b128 v[240:243], v254 offset:32768\n" \
  "v_smfmac_f32_16x16x64_bf16 v[192:195], v[208:211], v[160:167], v249 abid:1\n" \
  "ds_read_b128 v[244:247], v254 offset:33792\n" \
  "v_smfmac_f32_16x16x64_bf16 v[196:199], v[208:211], v[96:103], v249 abid:1\n" \
  "v_smfmac_f32_16x16x64_bf16 v[204:207], v[208:211], v[128:135], v249 abid:1\n" \
  "s_waitcnt lgkmcnt(6)\n" \
  "v_smfmac_f32_16x16x64_bf16 v[192:195], v[212:215], v[168:175], v249 abid:1\n" \
  "v_smfmac_f32_16x16x64_bf16 v[200:203], v[212:215], v[224:231], v249 abid:1\n" \
  "ds_read_b128 v[224:227], v254 offset:49152\n" \
  "v_smfmac_f32_16x16x64_bf16 v[196:199], v[212:215], v[104:111], v249 abid:1\n" \
  "ds_read_b128 v[228:231], v254 offset:50176\n" \
  "v_smfmac_f32_16x16x64_bf16 v[204:207], v[212:215], v[136:143], v249 abid:1\n" \
  "s_waitcnt lgkmcnt(6)\n" \
  "v_smfmac_f32_16x16x64_bf16 v[192:195], v[216:219], v[176:183], v249 abid:1\n" \
  "v_smfmac_f32_16x16x64_bf16 v[196:199], v[216:219], v[112:119], v249 abid:1\n" \
  "s_waitcnt lgkmcnt(4)\n" \
  "v_smfmac_f32_16x16x64_bf16 v[192:195], v[220:223], v[184:191], v249 abid:1\n" \
  "v_smfmac_f32_16x16x64_bf16 v[196:199], v[220:223], v[120:127], v249 abid:1\n" \
  "v_smfmac_f32_16x16x64_bf16 v[204:207], v[216:219], v[144:151], v249 abid:1\n" \
  "s_nop 0\n" \
  "v_cvt_pk_bf16_f32 v192, v192, v193\n" \
  "v_smfmac_f32_16x16x64_bf16 v[204:207], v[220:223], v[152:159], v249 abid:1\n" \
  "v_cvt_pk_bf16_f32 v193, v194, v195\n" \
  "s_waitcnt lgkmcnt(2)\n" \
  "v_smfmac_f32_16x16x64_bf16 v[200:203], v[216:219], v[240:247], v249 abid:1\n" \
  "v_cvt_pk_bf16_f32 v194, v196, v197\n" \
  "s_waitcnt lgkmcnt(0)\n" \
  "v_smfmac_f32_16x16x64_bf16 v[200:203], v[220:223], v[224:231], v249 abid:1\n" \
  "v_cvt_pk_bf16_f32 v195, v198, v199\n" \
  "v_cvt_pk_bf16_f32 v204, v204, v205\n" \
  "v_permlane16_swap_b32 v192, v194\n" \
  "v_permlane16_swap_b32 v193, v195\n" \
  "buffer_store_dwordx4 v[192:195], v236, %[rs], %[vs] offen sc1\n" \
  "v_cvt_pk_bf16_f32 v205, v206, v207\n" \
  "s_nop 2\n" \
  "v_cvt_pk_bf16_f32 v203, v202, v203\n" \
  "v_cvt_pk_bf16_f32 v202, v200, v201\n" \
  "s_nop 0\n" \
  "v_permlane16_swap_b32 v203, v205\n" \
  "v_permlane16_swap_b32 v202, v204\n" \
  "buffer_store_dwordx4 v[202:205], v237, %[rs], %[vs] offen sc1\n" \
  "s_setprio 0\n" \
  "s_nop 1\n"
#define QK_A0 \
  "s_branch .Lgo_%=\n" \
  AL(A_QK_A0) \
  ".Lr_%=:\n" \
  "s_sub_u32 %[rt], %[rt], 1\n" \
  "s_cbranch_scc1 .Lrb_%=\n" \
  "s_sleep 2\n" \
  "buffer_load_dwordx4 v[192:195], v248, %[rs], %[o0] offen sc1\n" \
  "buffer_load_dwordx4 v[196:199], v248, %[rs], %[o0] offen offset:1024 sc1\n" \
  "v_mov_b64 v[224:225], 0\n" \
  "v_mov_b64 v[226:227], 0\n" \
  "v_mov_b64 v[228:229], 0\n" \
  "v_mov_b64 v[230:231], 0\n" \
  "v_mov_b64 v[232:233], 0\n" \
  "v_mov_b64 v[234:235], 0\n" \
  "v_mov_b64 v[236:237], 0\n" \
  "v_mov_b64 v[238:239], 0\n" \
  "s_nop 1\n" \
  "s_branch .Lw_%=\n" \
  ".Lw1_%=:\n" \
  "v_max_f32 v200, v192, v193\n" \
  "s_nop 1\n" \
  "v_max_f32_dpp v200, v200, v200 row_ror:8 row_mask:0xf bank_mask:0xf\n" \
  "s_nop 1\n" \
  "v_max_f32_dpp v200, v200, v200 row_ror:4 row_mask:0xf bank_mask:0xf\n" \
  "s_nop 1\n" \
  "v_max_f32_dpp v200, v200, v200 row_ror:2 row_mask:0xf bank_mask:0xf\n" \
  "s_nop 1\n" \
  "v_max_f32_dpp v200, v200, v200 row_ror:1 row_mask:0xf bank_mask:0xf\n" \
  "v_sub_f32 v194, v192, v200\n" \
  "v_sub_f32 v195, v193, v200\n" \
  "v_exp_f32 v194, v194\n" \
  "v_exp_f32 v195, v195\n" \
  "s_mov_b32 %[rt], -1.0\n" \
  "s_nop 0\n" \
  "v_cvt_pk_bf16_f32 v196, v194, v195\n" \
  "v_and_b32 v196, 0x7fffffff, v196\n" \
  "s_branch .Lw1b_%=\n" \
  ".Lw2_%=:\n" \
  "s_cmp_lt_i32 %[rt], 0\n" \
  "s_cbranch_scc1 .Lw3_%=\n" \
  "v_mov_b32 v200, 0\n" \
  ".Lw3_%=:\n" \
  "s_nop 1\n" \
  "v_max_f32_dpp v197, v192, v200 quad_perm:[0,0,0,0] row_mask:0xf bank_mask:0xf\n" \
  "s_nop 1\n" \
  "v_max_f32_dpp v197, v192, v197 quad_perm:[1,1,1,1] row_mask:0xf bank_mask:0xf\n" \
  "s_nop 1\n" \
  "v_max_f32_dpp v197, v197, v197 row_ror:4 row_mask:0xf bank_mask:0xf\n" \
  "s_nop 1\n" \
  "v_max_f32_dpp v197, v197, v197 row_ror:8 row_mask:0xf bank_mask:0xf\n" \
  "v_sub_f32 v194, v192, v197\n" \
  "v_sub_f32 v201, v200, v197\n" \
  "v_exp_f32 v194, v194\n" \
  "v_exp_f32 v201, v201\n" \
  "s_nop 1\n" \
  "v_mul_f32 v199, v201, v199\n" \
  "s_nop 1\n" \
  "v_add_f32_dpp v198, v194, v199 quad_perm:[0,0,0,0] row_mask:0xf bank_mask:0xf\n" \
  "s_nop 1\n" \
  "v_mov_b32_dpp v193, v194 quad_perm:[1,1,1,1] row_mask:0xf bank_mask:0xf\n" \
  "s_nop 1\n" \
  "v_add_f32 v198, v198, v193\n" \
  "s_nop 1\n" \
  "v_mov_b32_dpp v195, v194 quad_perm:[0,0,0,0] row_mask:0xf bank_mask:0xf\n" \
  "s_nop 1\n" \
  "v_add_f32_dpp v198, v198, v198 row_ror:4 row_mask:0xf bank_mask:0xf\n" \
  "s_nop 1\n" \
  "v_add_f32_dpp v198, v198, v198 row_ror:8 row_mask:0xf bank_mask:0xf\n" \
  "s_nop 1\n" \
  "v_rcp_f32 v198, v198\n" \
  "s_nop 1\n" \
  "v_mul_f32 v195, v195, v198\n" \
  "s_nop 1\n" \
  "v_mul_f32 v193, v193, v198\n" \
  "s_nop 1\n" \
  "v_cvt_pk_bf16_f32 v196, v195, v193\n" \
  "s_nop 1\n" \
  "v_and_b32 v196, 0x7fffffff, v196\n" \
  "s_nop 1\n" \
  "v_mul_f32 v198, v201, v198\n" \
  "s_nop 1\n" \
  "buffer_store_dword v196, v251, %[rs], %[so] offen sc1\n" \
  "s_nop 1\n" \
  "v_cvt_pk_bf16_f32 v197, v198, v198\n" \
  "s_nop 1\n" \
  "v_and_b32 v197, v252, v197\n" \
  "s_nop 1\n" \
  "buffer_store_dword v197, v251, %[rs], %[so] offen offset:64 sc1\n" \
  "s_branch .Lw2b_%=\n" \
  ".Lgo_%=:\n" \
  "v_add_u32 v252, 0x20000, v248\n" \
  "ds_read_b128 v[200:203], v248 offset:32768\n" \
  "ds_read_b128 v[204:207], v248 offset:33792\n" \
  "ds_read_b128 v[208:211], v248 offset:49152\n" \
  "ds_read_b128 v[212:215], v248 offset:50176\n" \
  "ds_read_b128 v[216:219], v252 offset:0\n" \
  "ds_read_b128 v[220:223], v252 offset:1024\n" \
  "ds_read_b128 v[240:243], v252 offset:16384\n" \
  "ds_read_b128 v[244:247], v252 offset:17408\n" \
  "v_mov_b64 v[224:225], 0\n" \
  "v_mov_b64 v[226:227], 0\n" \
  "v_mov_b64 v[228:229], 0\n" \
  "v_mov_b64 v[230:231], 0\n" \
  "v_mov_b64 v[232:233], 0\n" \
  "v_mov_b64 v[234:235], 0\n" \
  "v_mov_b64 v[236:237], 0\n" \
  "v_mov_b64 v[238:239], 0\n" \
  "v_xor_b32 v250, 0x800, v248\n" \
  "v_xor_b32 v251, 0x1000, v248\n" \
  "s_mov_b32 %[rt], 0x40000\n" \
  "s_setprio 2\n" \
  "s_waitcnt lgkmcnt(0)\n" \
  "s_barrier\n" \
  "buffer_load_dwordx4 v[192:195], v248, %[rs], %[o0] offen sc1\n" \
  "buffer_load_dwordx4 v[196:199], v248, %[rs], %[o0] offen offset:1024 sc1\n" \
  ".Lw_%=:\n" \
  "s_waitcnt vmcnt(1)\n" \
  "ds_write_b128 v248, v[192:195]\n" \
  "v_smfmac_f32_16x16x64_bf16 v[224:227], v[192:195], v[200:207], v249\n" \
  "v_max3_u32 v252, v192, v193, v194\n" \
  "s_waitcnt vmcnt(0)\n" \
  "ds_write_b128 v248, v[196:199] offset:1024\n" \
  "v_smfmac_f32_16x16x64_bf16 v[228:231], v[192:195], v[208:215], v249\n" \
  "v_max3_u32 v252, v252, v195, v196\n" \
  "v_smfmac_f32_16x16x64_bf16 v[224:227], v[196:199], v[216:223], v249\n" \
  "v_max3_u32 v252, v252, v197, v198\n" \
  "v_smfmac_f32_16x16x64_bf16 v[228:231], v[196:199], v[240:247], v249\n" \
  "v_max_u32 v252, v252, v199\n" \
  "v_smfmac_f32_16x16x64_bf16 v[232:235], v[192:195], v[96:103], v249\n" \
  "v_cmp_eq_u32 vcc, -1, v252\n" \
  "v_smfmac_f32_16x16x64_bf16 v[236:239], v[192:195], v[144:151], v249\n" \
  "v_xor_b32 v252, 0x1800, v248\n" \
  "s_cbranch_vccnz .Lr_%=\n" \
  ".Lrb_%=:\n" \
  "v_smfmac_f32_16x16x64_bf16 v[232:235], v[196:199], v[104:111], v249\n" \
  "v_smfmac_f32_16x16x64_bf16 v[236:239], v[196:199], v[152:159], v249\n" \
  "s_waitcnt lgkmcnt(0)\n" \
  "s_barrier\n" \
  "ds_read_b128 v[200:203], v250 offset:0\n" \
  "ds_read_b128 v[204:207], v250 offset:1024\n" \
  "ds_read_b128 v[208:211], v251 offset:0\n" \
  "ds_read_b128 v[212:215], v251 offset:1024\n" \
  "ds_read_b128 v[216:219], v252 offset:0\n" \
  "ds_read_b128 v[220:223], v252 offset:1024\n" \
  "s_waitcnt lgkmcnt(5)\n" \
  "v_smfmac_f32_16x16x64_bf16 v[224:227], v[200:203], v[0:7], v249\n" \
  "v_smfmac_f32_16x16x64_bf16 v[228:231], v[200:203], v[48:55], v249\n" \
  "s_waitcnt lgkmcnt(4)\n" \
  "v_smfmac_f32_16x16x64_bf16 v[224:227], v[204:207], v[8:15], v249\n" \
  "v_smfmac_f32_16x16x64_bf16 v[228:231], v[204:207], v[56:63], v249\n" \
  "s_waitcnt lgkmcnt(3)\n" \
  "v_smfmac_f32_16x16x64_bf16 v[224:227], v[208:211], v[16:23], v249\n" \
  "v_smfmac_f32_16x16x64_bf16 v[228:231], v[208:211], v[64:71], v249\n" \
  "s_waitcnt lgkmcnt(2)\n" \
  "v_smfmac_f32_16x16x64_bf16 v[224:227], v[212:215], v[24:31], v249\n" \
  "s_waitcnt lgkmcnt(1)\n" \
  "v_smfmac_f32_16x16x64_bf16 v[224:227], v[216:219], v[32:39], v249\n" \
  "s_waitcnt lgkmcnt(0)\n" \
  "v_smfmac_f32_16x16x64_bf16 v[224:227], v[220:223], v[40:47], v249\n" \
  "v_smfmac_f32_16x16x64_bf16 v[228:231], v[212:215], v[72:79], v249\n" \
  "v_smfmac_f32_16x16x64_bf16 v[228:231], v[216:219], v[80:87], v249\n" \
  "s_nop 0\n" \
  "v_add_f32 v224, v224, v226\n" \
  "v_smfmac_f32_16x16x64_bf16 v[228:231], v[220:223], v[88:95], v249\n" \
  "v_add_f32 v225, v225, v227\n" \
  "ds_write_b64 v248, v[224:225] offset:8192\n" \
  "s_nop 6\n" \
  "v_add_f32 v228, v228, v230\n" \
  "v_add_f32 v229, v229, v231\n" \
  "ds_write_b64 v248, v[228:229] offset:8200\n" \
  "s_setprio 0\n" \
  "ds_read_b128 v[240:243], v254 offset:0\n" \
  "ds_read_b128 v[244:247], v254 offset:1024\n" \
  "s_waitcnt lgkmcnt(2)\n" \
  "s_barrier\n" \
  "s_nop 1\n" \
  "ds_read_b128 v[192:195], v254 offset:16384\n" \
  "ds_read_b128 v[196:199], v254 offset:17408\n" \
  "ds_read_b128 v[224:227], v254 offset:32768\n" \
  "ds_read_b128 v[228:231], v254 offset:33792\n" \
  "s_waitcnt lgkmcnt(4)\n" \
  "v_smfmac_f32_16x16x64_bf16 v[232:235], v[200:203], v[240:247], v249\n" \
  "ds_read_b128 v[240:243], v254 offset:49152\n" \
  "ds_read_b128 v[244:247], v254 offset:50176\n" \
  "s_waitcnt lgkmcnt(4)\n" \
  "v_smfmac_f32_16x16x64_bf16 v[236:239], v[200:203], v[192:199], v249\n" \
  "v_smfmac_f32_16x16x64_bf16 v[232:235], v[204:207], v[112:119], v249\n" \
  "ds_read_b128 v[192:195], v248 offset:16384\n" \
  "v_smfmac_f32_16x16x64_bf16 v[236:239], v[204:207], v[160:167], v249\n" \
  "ds_read_b128 v[196:199], v248 offset:17408\n" \
  "v_smfmac_f32_16x16x64_bf16 v[232:235], v[212:215], v[120:127], v249\n" \
  "ds_read_b128 v[200:203], v248 offset:20480\n" \
  "v_smfmac_f32_16x16x64_bf16 v[236:239], v[212:215], v[168:175], v249\n" \
  "ds_read_b128 v[204:207], v248 offset:21504\n" \
  "s_waitcnt lgkmcnt(6)\n" \
  "v_smfmac_f32_16x16x64_bf16 v[232:235], v[208:211], v[224:231], v249\n" \
  "ds_read_b128 v[212:215], v248 offset:23552\n" \
  "s_waitcnt lgkmcnt(5)\n" \
  "v_smfmac_f32_16x16x64_bf16 v[236:239], v[208:211], v[240:247], v249\n" \
  "ds_read_b128 v[240:243], v248 offset:18432\n" \
  "v_smfmac_f32_16x16x64_bf16 v[232:235], v[216:219], v[128:135], v249\n" \
  "ds_read_b128 v[244:247], v248 offset:19456\n" \
  "v_smfmac_f32_16x16x64_bf16 v[232:235], v[220:223], v[136:143], v249\n" \
  "ds_read_b128 v[208:211], v248 offset:22528\n" \
  "v_smfmac_f32_16x16x64_bf16 v[236:239], v[216:219], v[176:183], v249\n" \
  "ds_read_b96 v[250:252], v248 offset:13312\n" \
  "v_smfmac_f32_16x16x64_bf16 v[236:239], v[220:223], v[184:191], v249\n" \
  "s_setprio 3\n" \
  "s_barrier\n" \
  "ds_read_b64_tr_b16 v[224:225], v253 offset:17408\n" \
  "ds_read_b64_tr_b16 v[226:227], v253 offset:17664\n" \
  "ds_read_b64_tr_b16 v[228:229], v253 offset:19456\n" \
  "ds_read_b64_tr_b16 v[230:231], v253 offset:19712\n" \
  "v_mov_b64 v[216:217], 0\n" \
  "v_mov_b64 v[218:219], 0\n" \
  "v_add_f32 v232, v232, v234\n" \
  "v_add_f32 v233, v233, v235\n" \
  "ds_write_b64 v248, v[232:233] offset:0\n" \
  "v_mov_b64 v[220:221], 0\n" \
  "v_mov_b64 v[222:223], 0\n" \
  "v_add_f32 v236, v236, v238\n" \
  "v_add_f32 v237, v237, v239\n" \
  "ds_write_b64 v248, v[236:237] offset:8\n" \
  "s_waitcnt lgkmcnt(4)\n" \
  "v_smfmac_f32_16x16x64_bf16 v[216:219], v[224:227], v[192:199], v249\n" \
  "v_smfmac_f32_16x16x64_bf16 v[220:223], v[224:227], v[200:207], v249\n" \
  "s_waitcnt lgkmcnt(2)\n" \
  "v_smfmac_f32_16x16x64_bf16 v[216:219], v[228:231], v[240:247], v249\n" \
  "v_smfmac_f32_16x16x64_bf16 v[220:223], v[228:231], v[208:215], v249\n" \
  "s_mov_b32 %[rt], 0x422c0000\n" \
  "s_waitcnt lgkmcnt(0)\n" \
  "s_barrier\n" \
  "s_nop 1\n" \
  "v_add_f32 v192, v216, v218\n" \
  "s_nop 2\n" \
  "v_add_f32 v193, v220, v222\n" \
  "v_exp_f32 v194, v192\n" \
  "v_exp_f32 v195, v193\n" \
  "v_max_f32 v197, |v192|, |v193|\n" \
  "v_cvt_pk_bf16_f32 v196, v194, v195\n" \
  "v_cmp_nge_f32 vcc, %[rt], v197\n" \
  "s_cbranch_vccnz .Lw1_%=\n" \
  ".Lw1b_%=:\n" \
  "buffer_store_dword v196, v250, %[rs], %[c0] offen sc1\n" \
  "buffer_store_dword v196, v250, %[rs], %[c1] offen sc1\n" \
  "buffer_store_dword v196, v250, %[rs], %[c2] offen sc1\n" \
  "buffer_store_dword v196, v250, %[rs], %[c3] offen sc1\n" \
  "v_add_f32 v199, v194, v195\n" \
  "v_xor_b32 v251, %[xr], v251\n" \
  "s_nop 0\n" \
  "v_add_f32_dpp v199, v199, v199 quad_perm:[1,0,3,2] row_mask:0xf bank_mask:0xf\n" \
  "s_nop 1\n" \
  "v_add_f32_dpp v199, v199, v199 quad_perm:[2,3,0,1] row_mask:0xf bank_mask:0xf\n" \
  "s_barrier\n" \
  "ds_read_b64_tr_b16 v[232:233], v253 offset:9216\n" \
  "ds_read_b64_tr_b16 v[234:235], v253 offset:9472\n" \
  "ds_read_b64_tr_b16 v[236:237], v253 offset:11264\n" \
  "ds_read_b64_tr_b16 v[238:239], v253 offset:11520\n" \
  "s_waitcnt lgkmcnt(2)\n" \
  "v_mfma_f32_16x16x32_bf16 v[216:219], v[224:227], v[232:235], 0\n" \
  "s_waitcnt lgkmcnt(0)\n" \
  "v_mfma_f32_16x16x32_bf16 v[216:219], v[228:231], v[236:239], v[216:219]\n" \
  "s_nop 8\n" \
  "v_add_f32_dpp v192, v218, v216 quad_perm:[2,3,2,3] row_mask:0xf bank_mask:0xf\n" \
  "v_exp_f32 v194, v192\n" \
  "v_cmp_nge_f32 vcc, %[rt], |v192|\n" \
  "s_and_b64 vcc, vcc, %[lo]\n" \
  "v_add_f32_dpp v198, v194, v199 quad_perm:[0,0,0,0] row_mask:0xf bank_mask:0xf\n" \
  "v_mov_b32_dpp v193, v194 quad_perm:[1,1,1,1] row_mask:0xf bank_mask:0xf\n" \
  "v_add_f32 v198, v198, v193\n" \
  "v_mov_b32_dpp v195, v194 quad_perm:[0,0,0,0] row_mask:0xf bank_mask:0xf\n" \
  "s_cbranch_scc1 .Lw2_%=\n" \
  "v_add_f32_dpp v198, v198, v198 row_ror:4 row_mask:0xf bank_mask:0xf\n" \
  "s_nop 1\n" \
  "v_add_f32_dpp v198, v198, v198 row_ror:8 row_mask:0xf bank_mask:0xf\n" \
  "v_rcp_f32 v198, v198\n" \
  "s_nop 0\n" \
  "v_mul_f32 v195, v195, v198\n" \
  "v_mul_f32 v193, v193, v198\n" \
  "v_cvt_pk_bf16_f32 v196, v195, v193\n" \
  "buffer_store_dword v196, v251, %[rs], %[so] offen sc1\n" \
  "v_cvt_pk_bf16_f32 v197, v198, v198\n" \
  "v_and_b32 v197, v252, v197\n" \
  "buffer_store_dword v197, v251, %[rs], %[so] offen offset:64 sc1\n" \
  ".Lw2b_%=:\n" \
  "s_setprio 0\n"
#define QK_A1 \
  "s_branch .Lgo_%=\n" \
  AL(A_QK_A1) \
  ".Lr_%=:\n" \
  "s_sub_u32 %[rt], %[rt], 1\n" \
  "s_cbranch_scc1 .Lrb_%=\n" \
  "s_sleep 2\n" \
  "buffer_load_dwordx4 v[192:195], v248, %[rs], %[o0] offen sc1\n" \
  "buffer_load_dwordx4 v[196:199], v248, %[rs], %[o0] offen offset:1024 sc1\n" \
  "v_mov_b64 v[224:225], 0\n" \
  "v_mov_b64 v[226:227], 0\n" \
  "v_mov_b64 v[228:229], 0\n" \
  "v_mov_b64 v[230:231], 0\n" \
  "v_mov_b64 v[232:233], 0\n" \
  "v_mov_b64 v[234:235], 0\n" \
  "v_mov_b64 v[236:237], 0\n" \
  "v_mov_b64 v[238:239], 0\n" \
  "s_nop 1\n" \
  "s_branch .Lw_%=\n" \
  ".Lw1_%=:\n" \
  "v_max_f32 v200, v192, v193\n" \
  "s_nop 1\n" \
  "v_max_f32_dpp v200, v200, v200 row_ror:8 row_mask:0xf bank_mask:0xf\n" \
  "s_nop 1\n" \
  "v_max_f32_dpp v200, v200, v200 row_ror:4 row_mask:0xf bank_mask:0xf\n" \
  "s_nop 1\n" \
  "v_max_f32_dpp v200, v200, v200 row_ror:2 row_mask:0xf bank_mask:0xf\n" \
  "s_nop 1\n" \
  "v_max_f32_dpp v200, v200, v200 row_ror:1 row_mask:0xf bank_mask:0xf\n" \
  "v_sub_f32 v194, v192, v200\n" \
  "v_sub_f32 v195, v193, v200\n" \
  "v_exp_f32 v194, v194\n" \
  "v_exp_f32 v195, v195\n" \
  "s_mov_b32 %[rt], -1.0\n" \
  "s_nop 0\n" \
  "v_cvt_pk_bf16_f32 v196, v194, v195\n" \
  "v_and_b32 v196, 0x7fffffff, v196\n" \
  "s_branch .Lw1b_%=\n" \
  ".Lw2_%=:\n" \
  "s_cmp_lt_i32 %[rt], 0\n" \
  "s_cbranch_scc1 .Lw3_%=\n" \
  "v_mov_b32 v200, 0\n" \
  ".Lw3_%=:\n" \
  "s_nop 1\n" \
  "v_max_f32_dpp v197, v192, v200 quad_perm:[0,0,0,0] row_mask:0xf bank_mask:0xf\n" \
  "s_nop 1\n" \
  "v_max_f32_dpp v197, v192, v197 quad_perm:[1,1,1,1] row_mask:0xf bank_mask:0xf\n" \
  "s_nop 1\n" \
  "v_max_f32_dpp v197, v197, v197 row_ror:4 row_mask:0xf bank_mask:0xf\n" \
  "s_nop 1\n" \
  "v_max_f32_dpp v197, v197, v197 row_ror:8 row_mask:0xf bank_mask:0xf\n" \
  "v_sub_f32 v194, v192, v197\n" \
  "v_sub_f32 v201, v200, v197\n" \
  "v_exp_f32 v194, v194\n" \
  "v_exp_f32 v201, v201\n" \
  "s_nop 1\n" \
  "v_mul_f32 v199, v201, v199\n" \
  "s_nop 1\n" \
  "v_add_f32_dpp v198, v194, v199 quad_perm:[0,0,0,0] row_mask:0xf bank_mask:0xf\n" \
  "s_nop 1\n" \
  "v_mov_b32_dpp v193, v194 quad_perm:[1,1,1,1] row_mask:0xf bank_mask:0xf\n" \
  "s_nop 1\n" \
  "v_add_f32 v198, v198, v193\n" \
  "s_nop 1\n" \
  "v_mov_b32_dpp v195, v194 quad_perm:[0,0,0,0] row_mask:0xf bank_mask:0xf\n" \
  "s_nop 1\n" \
  "v_add_f32_dpp v198, v198, v198 row_ror:4 row_mask:0xf bank_mask:0xf\n" \
  "s_nop 1\n" \
  "v_add_f32_dpp v198, v198, v198 row_ror:8 row_mask:0xf bank_mask:0xf\n" \
  "s_nop 1\n" \
  "v_rcp_f32 v198, v198\n" \
  "s_nop 1\n" \
  "v_mul_f32 v195, v195, v198\n" \
  "s_nop 1\n" \
  "v_mul_f32 v193, v193, v198\n" \
  "s_nop 1\n" \
  "v_cvt_pk_bf16_f32 v196, v195, v193\n" \
  "s_nop 1\n" \
  "v_and_b32 v196, 0x7fffffff, v196\n" \
  "s_nop 1\n" \
  "v_mul_f32 v198, v201, v198\n" \
  "s_nop 1\n" \
  "buffer_store_dword v196, v251, %[rs], %[so] offen sc1\n" \
  "s_nop 1\n" \
  "v_cvt_pk_bf16_f32 v197, v198, v198\n" \
  "s_nop 1\n" \
  "v_and_b32 v197, v252, v197\n" \
  "s_nop 1\n" \
  "buffer_store_dword v197, v251, %[rs], %[so] offen offset:64 sc1\n" \
  "s_branch .Lw2b_%=\n" \
  ".Lgo_%=:\n" \
  "v_add_u32 v252, 0x20000, v248\n" \
  "ds_read_b128 v[200:203], v248 offset:32768\n" \
  "ds_read_b128 v[204:207], v248 offset:33792\n" \
  "ds_read_b128 v[208:211], v248 offset:49152\n" \
  "ds_read_b128 v[212:215], v248 offset:50176\n" \
  "ds_read_b128 v[216:219], v252 offset:0\n" \
  "ds_read_b128 v[220:223], v252 offset:1024\n" \
  "ds_read_b128 v[240:243], v252 offset:16384\n" \
  "ds_read_b128 v[244:247], v252 offset:17408\n" \
  "v_mov_b64 v[224:225], 0\n" \
  "v_mov_b64 v[226:227], 0\n" \
  "v_mov_b64 v[228:229], 0\n" \
  "v_mov_b64 v[230:231], 0\n" \
  "v_mov_b64 v[232:233], 0\n" \
  "v_mov_b64 v[234:235], 0\n" \
  "v_mov_b64 v[236:237], 0\n" \
  "v_mov_b64 v[238:239], 0\n" \
  "v_xor_b32 v250, 0x800, v248\n" \
  "v_xor_b32 v251, 0x1000, v248\n" \
  "s_mov_b32 %[rt], 0x40000\n" \
  "s_setprio 2\n" \
  "s_waitcnt lgkmcnt(0)\n" \
  "s_barrier\n" \
  "buffer_load_dwordx4 v[192:195], v248, %[rs], %[o0] offen sc1\n" \
  "buffer_load_dwordx4 v[196:199], v248, %[rs], %[o0] offen offset:1024 sc1\n" \
  ".Lw_%=:\n" \
  "s_waitcnt vmcnt(1)\n" \
  "ds_write_b128 v248, v[192:195]\n" \
  "v_smfmac_f32_16x16x64_bf16 v[224:227], v[192:195], v[200:207], v249\n" \
  "v_max3_u32 v252, v192, v193, v194\n" \
  "s_waitcnt vmcnt(0)\n" \
  "ds_write_b128 v248, v[196:199] offset:1024\n" \
  "v_smfmac_f32_16x16x64_bf16 v[228:231], v[192:195], v[208:215], v249\n" \
  "v_max3_u32 v252, v252, v195, v196\n" \
  "v_smfmac_f32_16x16x64_bf16 v[224:227], v[196:199], v[216:223], v249\n" \
  "v_max3_u32 v252, v252, v197, v198\n" \
  "v_smfmac_f32_16x16x64_bf16 v[228:231], v[196:199], v[240:247], v249\n" \
  "v_max_u32 v252, v252, v199\n" \
  "v_smfmac_f32_16x16x64_bf16 v[232:235], v[192:195], v[96:103], v249\n" \
  "v_cmp_eq_u32 vcc, -1, v252\n" \
  "v_smfmac_f32_16x16x64_bf16 v[236:239], v[192:195], v[144:151], v249\n" \
  "v_xor_b32 v252, 0x1800, v248\n" \
  "s_cbranch_vccnz .Lr_%=\n" \
  ".Lrb_%=:\n" \
  "v_smfmac_f32_16x16x64_bf16 v[232:235], v[196:199], v[104:111], v249\n" \
  "v_smfmac_f32_16x16x64_bf16 v[236:239], v[196:199], v[152:159], v249\n" \
  "s_waitcnt lgkmcnt(0)\n" \
  "s_barrier\n" \
  "ds_read_b128 v[200:203], v250 offset:0\n" \
  "ds_read_b128 v[204:207], v250 offset:1024\n" \
  "ds_read_b128 v[208:211], v251 offset:0\n" \
  "ds_read_b128 v[212:215], v251 offset:1024\n" \
  "ds_read_b128 v[216:219], v252 offset:0\n" \
  "ds_read_b128 v[220:223], v252 offset:1024\n" \
  "s_waitcnt lgkmcnt(5)\n" \
  "v_smfmac_f32_16x16x64_bf16 v[224:227], v[200:203], v[0:7], v249\n" \
  "v_smfmac_f32_16x16x64_bf16 v[228:231], v[200:203], v[48:55], v249\n" \
  "s_waitcnt lgkmcnt(4)\n" \
  "v_smfmac_f32_16x16x64_bf16 v[224:227], v[204:207], v[8:15], v249\n" \
  "v_smfmac_f32_16x16x64_bf16 v[228:231], v[204:207], v[56:63], v249\n" \
  "s_waitcnt lgkmcnt(3)\n" \
  "v_smfmac_f32_16x16x64_bf16 v[224:227], v[208:211], v[16:23], v249\n" \
  "v_smfmac_f32_16x16x64_bf16 v[228:231], v[208:211], v[64:71], v249\n" \
  "s_waitcnt lgkmcnt(2)\n" \
  "v_smfmac_f32_16x16x64_bf16 v[224:227], v[212:215], v[24:31], v249\n" \
  "s_waitcnt lgkmcnt(1)\n" \
  "v_smfmac_f32_16x16x64_bf16 v[224:227], v[216:219], v[32:39], v249\n" \
  "s_waitcnt lgkmcnt(0)\n" \
  "v_smfmac_f32_16x16x64_bf16 v[224:227], v[220:223], v[40:47], v249\n" \
  "v_smfmac_f32_16x16x64_bf16 v[228:231], v[212:215], v[72:79], v249\n" \
  "v_smfmac_f32_16x16x64_bf16 v[228:231], v[216:219], v[80:87], v249\n" \
  "s_nop 0\n" \
  "v_add_f32 v224, v224, v226\n" \
  "v_smfmac_f32_16x16x64_bf16 v[228:231], v[220:223], v[88:95], v249\n" \
  "v_add_f32 v225, v225, v227\n" \
  "ds_write_b64 v248, v[224:225] offset:8192\n" \
  "s_nop 6\n" \
  "v_add_f32 v228, v228, v230\n" \
  "v_add_f32 v229, v229, v231\n" \
  "ds_write_b64 v248, v[228:229] offset:8200\n" \
  "s_setprio 0\n" \
  "ds_read_b128 v[240:243], v254 offset:0\n" \
  "ds_read_b128 v[244:247], v254 offset:1024\n" \
  "s_waitcnt lgkmcnt(2)\n" \
  "s_barrier\n" \
  "s_nop 1\n" \
  "ds_read_b128 v[192:195], v254 offset:16384\n" \
  "ds_read_b128 v[196:199], v254 offset:17408\n" \
  "ds_read_b128 v[224:227], v254 offset:32768\n" \
  "ds_read_b128 v[228:231], v254 offset:33792\n" \
  "s_waitcnt lgkmcnt(4)\n" \
  "v_smfmac_f32_16x16x64_bf16 v[232:235], v[200:203], v[240:247], v249\n" \
  "ds_read_b128 v[240:243], v254 offset:49152\n" \
  "ds_read_b128 v[244:247], v254 offset:50176\n" \
  "s_waitcnt lgkmcnt(4)\n" \
  "v_smfmac_f32_16x16x64_bf16 v[236:239], v[200:203], v[192:199], v249\n" \
  "v_smfmac_f32_16x16x64_bf16 v[232:235], v[204:207], v[112:119], v249\n" \
  "ds_read_b128 v[192:195], v248 offset:14336\n" \
  "v_smfmac_f32_16x16x64_bf16 v[236:239], v[204:207], v[160:167], v249\n" \
  "ds_read_b128 v[196:199], v248 offset:15360\n" \
  "v_smfmac_f32_16x16x64_bf16 v[232:235], v[212:215], v[120:127], v249\n" \
  "ds_read_b128 v[200:203], v248 offset:18432\n" \
  "v_smfmac_f32_16x16x64_bf16 v[236:239], v[212:215], v[168:175], v249\n" \
  "ds_read_b128 v[204:207], v248 offset:19456\n" \
  "s_waitcnt lgkmcnt(6)\n" \
  "v_smfmac_f32_16x16x64_bf16 v[232:235], v[208:211], v[224:231], v249\n" \
  "ds_read_b128 v[212:215], v248 offset:21504\n" \
  "s_waitcnt lgkmcnt(5)\n" \
  "v_smfmac_f32_16x16x64_bf16 v[236:239], v[208:211], v[240:247], v249\n" \
  "ds_read_b128 v[240:243], v248 offset:16384\n" \
  "v_smfmac_f32_16x16x64_bf16 v[232:235], v[216:219], v[128:135], v249\n" \
  "ds_read_b128 v[244:247], v248 offset:17408\n" \
  "v_smfmac_f32_16x16x64_bf16 v[232:235], v[220:223], v[136:143], v249\n" \
  "ds_read_b128 v[208:211], v248 offset:20480\n" \
  "v_smfmac_f32_16x16x64_bf16 v[236:239], v[216:219], v[176:183], v249\n" \
  "ds_read_b96 v[250:252], v248 offset:13312\n" \
  "v_smfmac_f32_16x16x64_bf16 v[236:239], v[220:223], v[184:191], v249\n" \
  "s_setprio 3\n" \
  "s_barrier\n" \
  "ds_read_b64_tr_b16 v[224:225], v253 offset:17408\n" \
  "ds_read_b64_tr_b16 v[226:227], v253 offset:17664\n" \
  "ds_read_b64_tr_b16 v[228:229], v253 offset:19456\n" \
  "ds_read_b64_tr_b16 v[230:231], v253 offset:19712\n" \
  "v_mov_b64 v[216:217], 0\n" \
  "v_mov_b64 v[218:219], 0\n" \
  "v_add_f32 v232, v232, v234\n" \
  "v_add_f32 v233, v233, v235\n" \
  "ds_write_b64 v248, v[232:233] offset:0\n" \
  "v_mov_b64 v[220:221], 0\n" \
  "v_mov_b64 v[222:223], 0\n" \
  "v_add_f32 v236, v236, v238\n" \
  "v_add_f32 v237, v237, v239\n" \
  "ds_write_b64 v248, v[236:237] offset:8\n" \
  "s_waitcnt lgkmcnt(4)\n" \
  "v_smfmac_f32_16x16x64_bf16 v[216:219], v[224:227], v[192:199], v249\n" \
  "v_smfmac_f32_16x16x64_bf16 v[220:223], v[224:227], v[200:207], v249\n" \
  "s_waitcnt lgkmcnt(2)\n" \
  "v_smfmac_f32_16x16x64_bf16 v[216:219], v[228:231], v[240:247], v249\n" \
  "v_smfmac_f32_16x16x64_bf16 v[220:223], v[228:231], v[208:215], v249\n" \
  "s_mov_b32 %[rt], 0x422c0000\n" \
  "s_waitcnt lgkmcnt(0)\n" \
  "s_barrier\n" \
  "s_nop 1\n" \
  "v_add_f32 v192, v217, v219\n" \
  "s_nop 2\n" \
  "v_add_f32 v193, v221, v223\n" \
  "v_exp_f32 v194, v192\n" \
  "v_exp_f32 v195, v193\n" \
  "v_max_f32 v197, |v192|, |v193|\n" \
  "v_cvt_pk_bf16_f32 v196, v194, v195\n" \
  "v_cmp_nge_f32 vcc, %[rt], v197\n" \
  "s_cbranch_vccnz .Lw1_%=\n" \
  ".Lw1b_%=:\n" \
  "buffer_store_dword v196, v250, %[rs], %[c0] offen sc1\n" \
  "buffer_store_dword v196, v250, %[rs], %[c1] offen sc1\n" \
  "buffer_store_dword v196, v250, %[rs], %[c2] offen sc1\n" \
  "buffer_store_dword v196, v250, %[rs], %[c3] offen sc1\n" \
  "v_add_f32 v199, v194, v195\n" \
  "v_xor_b32 v251, %[xr], v251\n" \
  "s_nop 0\n" \
  "v_add_f32_dpp v199, v199, v199 quad_perm:[1,0,3,2] row_mask:0xf bank_mask:0xf\n" \
  "s_nop 1\n" \
  "v_add_f32_dpp v199, v199, v199 quad_perm:[2,3,0,1] row_mask:0xf bank_mask:0xf\n" \
  "s_barrier\n" \
  "ds_read_b64_tr_b16 v[232:233], v253 offset:9216\n" \
  "ds_read_b64_tr_b16 v[234:235], v253 offset:9472\n" \
  "ds_read_b64_tr_b16 v[236:237], v253 offset:11264\n" \
  "ds_read_b64_tr_b16 v[238:239], v253 offset:11520\n" \
  "s_waitcnt lgkmcnt(2)\n" \
  "v_mfma_f32_16x16x32_bf16 v[216:219], v[224:227], v[232:235], 0\n" \
  "s_waitcnt lgkmcnt(0)\n" \
  "v_mfma_f32_16x16x32_bf16 v[216:219], v[228:231], v[236:239], v[216:219]\n" \
  "s_nop 8\n" \
  "v_add_f32_dpp v192, v219, v217 quad_perm:[2,3,2,3] row_mask:0xf bank_mask:0xf\n" \
  "v_exp_f32 v194, v192\n" \
  "v_cmp_nge_f32 vcc, %[rt], |v192|\n" \
  "s_and_b64 vcc, vcc, %[lo]\n" \
  "v_add_f32_dpp v198, v194, v199 quad_perm:[0,0,0,0] row_mask:0xf bank_mask:0xf\n" \
  "v_mov_b32_dpp v193, v194 quad_perm:[1,1,1,1] row_mask:0xf bank_mask:0xf\n" \
  "v_add_f32 v198, v198, v193\n" \
  "v_mov_b32_dpp v195, v194 quad_perm:[0,0,0,0] row_mask:0xf bank_mask:0xf\n" \
  "s_cbranch_scc1 .Lw2_%=\n" \
  "v_add_f32_dpp v198, v198, v198 row_ror:4 row_mask:0xf bank_mask:0xf\n" \
  "s_nop 1\n" \
  "v_add_f32_dpp v198, v198, v198 row_ror:8 row_mask:0xf bank_mask:0xf\n" \
  "v_rcp_f32 v198, v198\n" \
  "s_nop 0\n" \
  "v_mul_f32 v195, v195, v198\n" \
  "v_mul_f32 v193, v193, v198\n" \
  "v_cvt_pk_bf16_f32 v196, v195, v193\n" \
  "buffer_store_dword v196, v251, %[rs], %[so] offen sc1\n" \
  "v_cvt_pk_bf16_f32 v197, v198, v198\n" \
  "v_and_b32 v197, v252, v197\n" \
  "buffer_store_dword v197, v251, %[rs], %[so] offen offset:64 sc1\n" \
  ".Lw2b_%=:\n" \
  "s_setprio 0\n"
#define QK_X \
  "s_branch .Lgo_%=\n" \
  AL(A_QK_X) \
  ".Lr_%=:\n" \
  "s_sub_u32 %[rt], %[rt], 1\n" \
  "s_cbranch_scc1 .Lrb_%=\n" \
  "s_sleep 2\n" \
  "buffer_load_dwordx4 v[192:195], v248, %[rs], %[o0] offen sc1\n" \
  "buffer_load_dwordx4 v[196:199], v248, %[rs], %[o0] offen offset:1024 sc1\n" \
  "v_mov_b64 v[224:225], 0\n" \
  "v_mov_b64 v[226:227], 0\n" \
  "v_mov_b64 v[228:229], 0\n" \
  "v_mov_b64 v[230:231], 0\n" \
  "v_mov_b64 v[232:233], 0\n" \
  "v_mov_b64 v[234:235], 0\n" \
  "v_mov_b64 v[236:237], 0\n" \
  "v_mov_b64 v[238:239], 0\n" \
  "s_nop 1\n" \
  "s_branch .Lw_%=\n" \
  ".Lgo_%=:\n" \
  "v_add_u32 v252, 0x20000, v248\n" \
  "ds_read_b128 v[200:203], v248 offset:32768\n" \
  "ds_read_b128 v[204:207], v248 offset:33792\n" \
  "ds_read_b128 v[208:211], v248 offset:49152\n" \
  "ds_read_b128 v[212:215], v248 offset:50176\n" \
  "ds_read_b128 v[216:219], v252 offset:0\n" \
  "ds_read_b128 v[220:223], v252 offset:1024\n" \
  "ds_read_b128 v[240:243], v252 offset:16384\n" \
  "ds_read_b128 v[244:247], v252 offset:17408\n" \
  "v_mov_b64 v[224:225], 0\n" \
  "v_mov_b64 v[226:227], 0\n" \
  "v_mov_b64 v[228:229], 0\n" \
  "v_mov_b64 v[230:231], 0\n" \
  "v_mov_b64 v[232:233], 0\n" \
  "v_mov_b64 v[234:235], 0\n" \
  "v_mov_b64 v[236:237], 0\n" \
  "v_mov_b64 v[238:239], 0\n" \
  "v_xor_b32 v250, 0x800, v248\n" \
  "v_xor_b32 v251, 0x1000, v248\n" \
  "s_mov_b32 %[rt], 0x40000\n" \
  "s_setprio 2\n" \
  "s_waitcnt lgkmcnt(0)\n" \
  "s_barrier\n" \
  "s_nop 3\n" \
  "buffer_load_dwordx4 v[192:195], v248, %[rs], %[o0] offen sc1\n" \
  "buffer_load_dwordx4 v[196:199], v248, %[rs], %[o0] offen offset:1024 sc1\n" \
  ".Lw_%=:\n" \
  "s_waitcnt vmcnt(1)\n" \
  "ds_write_b128 v248, v[192:195]\n" \
  "v_smfmac_f32_16x16x64_bf16 v[224:227], v[192:195], v[200:207], v249\n" \
  "v_max3_u32 v252, v192, v193, v194\n" \
  "s_waitcnt vmcnt(0)\n" \
  "ds_write_b128 v248, v[196:199] offset:1024\n" \
  "v_smfmac_f32_16x16x64_bf16 v[228:231], v[192:195], v[208:215], v249\n" \
  "v_max3_u32 v252, v252, v195, v196\n" \
  "v_smfmac_f32_16x16x64_bf16 v[224:227], v[196:199], v[216:223], v249\n" \
  "v_max3_u32 v252, v252, v197, v198\n" \
  "v_smfmac_f32_16x16x64_bf16 v[228:231], v[196:199], v[240:247], v249\n" \
  "v_max_u32 v252, v252, v199\n" \
  "v_smfmac_f32_16x16x64_bf16 v[232:235], v[192:195], v[96:103], v249\n" \
  "v_cmp_eq_u32 vcc, -1, v252\n" \
  "v_smfmac_f32_16x16x64_bf16 v[236:239], v[192:195], v[144:151], v249\n" \
  "v_xor_b32 v252, 0x1800, v248\n" \
  "s_cbranch_vccnz .Lr_%=\n" \
  ".Lrb_%=:\n" \
  "v_smfmac_f32_16x16x64_bf16 v[232:235], v[196:199], v[104:111], v249\n" \
  "v_smfmac_f32_16x16x64_bf16 v[236:239], v[196:199], v[152:159], v249\n" \
  "s_waitcnt lgkmcnt(0)\n" \
  "s_barrier\n" \
  "ds_read_b128 v[200:203], v250 offset:0\n" \
  "ds_read_b128 v[204:207], v250 offset:1024\n" \
  "ds_read_b128 v[208:211], v251 offset:0\n" \
  "ds_read_b128 v[212:215], v251 offset:1024\n" \
  "ds_read_b128 v[216:219], v252 offset:0\n" \
  "ds_read_b128 v[220:223], v252 offset:1024\n" \
  "s_waitcnt lgkmcnt(5)\n" \
  "v_smfmac_f32_16x16x64_bf16 v[224:227], v[200:203], v[0:7], v249\n" \
  "v_smfmac_f32_16x16x64_bf16 v[228:231], v[200:203], v[48:55], v249\n" \
  "s_waitcnt lgkmcnt(4)\n" \
  "v_smfmac_f32_16x16x64_bf16 v[224:227], v[204:207], v[8:15], v249\n" \
  "v_smfmac_f32_16x16x64_bf16 v[228:231], v[204:207], v[56:63], v249\n" \
  "s_waitcnt lgkmcnt(3)\n" \
  "v_smfmac_f32_16x16x64_bf16 v[224:227], v[208:211], v[16:23], v249\n" \
  "v_smfmac_f32_16x16x64_bf16 v[228:231], v[208:211], v[64:71], v249\n" \
  "s_waitcnt lgkmcnt(2)\n" \
  "v_smfmac_f32_16x16x64_bf16 v[224:227], v[212:215], v[24:31], v249\n" \
  "s_waitcnt lgkmcnt(1)\n" \
  "v_smfmac_f32_16x16x64_bf16 v[224:227], v[216:219], v[32:39], v249\n" \
  "s_waitcnt lgkmcnt(0)\n" \
  "v_smfmac_f32_16x16x64_bf16 v[224:227], v[220:223], v[40:47], v249\n" \
  "v_smfmac_f32_16x16x64_bf16 v[228:231], v[212:215], v[72:79], v249\n" \
  "v_and_b32 v252, 0x3ff, v248\n" \
  "v_smfmac_f32_16x16x64_bf16 v[228:231], v[216:219], v[80:87], v249\n" \
  "s_nop 0\n" \
  "v_add_f32 v224, v224, v226\n" \
  "v_smfmac_f32_16x16x64_bf16 v[228:231], v[220:223], v[88:95], v249\n" \
  "v_add_f32 v225, v225, v227\n" \
  "ds_write_b64 v248, v[224:225] offset:8192\n" \
  "s_nop 6\n" \
  "v_add_f32 v228, v228, v230\n" \
  "v_add_f32 v229, v229, v231\n" \
  "ds_write_b64 v248, v[228:229] offset:8200\n" \
  "s_setprio 0\n" \
  "ds_read_b128 v[240:243], v254 offset:0\n" \
  "ds_read_b128 v[244:247], v254 offset:1024\n" \
  "s_waitcnt lgkmcnt(2)\n" \
  "s_barrier\n" \
  "s_nop 1\n" \
  "ds_read_b128 v[192:195], v254 offset:16384\n" \
  "ds_read_b128 v[196:199], v254 offset:17408\n" \
  "ds_read_b128 v[224:227], v254 offset:32768\n" \
  "ds_read_b128 v[228:231], v254 offset:33792\n" \
  "s_waitcnt lgkmcnt(4)\n" \
  "v_smfmac_f32_16x16x64_bf16 v[232:235], v[200:203], v[240:247], v249\n" \
  "ds_read_b128 v[240:243], v254 offset:49152\n" \
  "ds_read_b128 v[244:247], v254 offset:50176\n" \
  "s_waitcnt lgkmcnt(4)\n" \
  "v_smfmac_f32_16x16x64_bf16 v[236:239], v[200:203], v[192:199], v249\n" \
  "v_smfmac_f32_16x16x64_bf16 v[232:235], v[204:207], v[112:119], v249\n" \
  "v_smfmac_f32_16x16x64_bf16 v[236:239], v[204:207], v[160:167], v249\n" \
  "v_smfmac_f32_16x16x64_bf16 v[232:235], v[212:215], v[120:127], v249\n" \
  "v_smfmac_f32_16x16x64_bf16 v[236:239], v[212:215], v[168:175], v249\n" \
  "s_waitcnt lgkmcnt(2)\n" \
  "v_smfmac_f32_16x16x64_bf16 v[232:235], v[208:211], v[224:231], v249\n" \
  "s_waitcnt lgkmcnt(0)\n" \
  "v_smfmac_f32_16x16x64_bf16 v[236:239], v[208:211], v[240:247], v249\n" \
  "v_smfmac_f32_16x16x64_bf16 v[232:235], v[216:219], v[128:135], v249\n" \
  "v_smfmac_f32_16x16x64_bf16 v[232:235], v[220:223], v[136:143], v249\n" \
  "v_smfmac_f32_16x16x64_bf16 v[236:239], v[216:219], v[176:183], v249\n" \
  "buffer_load_dwordx4 v[224:227], v252, %[rs], %[qo] offen offset:0 sc1\n" \
  "buffer_load_dwordx4 v[228:231], v252, %[rs], %[qo] offen offset:1024 sc1\n" \
  "buffer_load_dwordx4 v[240:243], v252, %[rs], %[qo] offen offset:2048 sc1\n" \
  "buffer_load_dwordx4 v[244:247], v252, %[rs], %[qo] offen offset:3072 sc1\n" \
  "v_smfmac_f32_16x16x64_bf16 v[236:239], v[220:223], v[184:191], v249\n" \
  "s_setprio 3\n" \
  "s_barrier\n" \
  "v_add_f32 v232, v232, v234\n" \
  "v_add_f32 v233, v233, v235\n" \
  "ds_write_b64 v248, v[232:233] offset:0\n" \
  "s_nop 3\n" \
  "v_add_f32 v236, v236, v238\n" \
  "v_add_f32 v237, v237, v239\n" \
  "ds_write_b64 v248, v[236:237] offset:8\n" \
  "s_waitcnt lgkmcnt(0)\n" \
  "s_barrier\n" \
  "s_setprio 0\n" \
  "s_nop 15\n" \
  "s_nop 13\n" \
  "ds_read_b128 v[192:195], v252 offset:24576\n" \
  "ds_read_b128 v[196:199], v252 offset:28672\n" \
  "ds_read_b128 v[200:203], v252 offset:25600\n" \
  "ds_read_b128 v[204:207], v252 offset:29696\n" \
  "ds_read_b128 v[208:211], v252 offset:26624\n" \
  "ds_read_b128 v[212:215], v252 offset:30720\n" \
  "ds_read_b128 v[216:219], v252 offset:27648\n" \
  "ds_read_b128 v[220:223], v252 offset:31744\n" \
  "s_waitcnt lgkmcnt(0)\n" \
  "s_barrier\n" \
  "s_nop 15\n" \
  "s_nop 3\n" \
  "ds_read_b64_tr_b16 v[232:233], v253 offset:9216\n" \
  "ds_read_b64_tr_b16 v[234:235], v253 offset:9472\n" \
  "ds_read_b64_tr_b16 v[236:237], v253 offset:11264\n" \
  "ds_read_b64_tr_b16 v[238:239], v253 offset:11520\n" \
  "s_waitcnt vmcnt(3)\n" \
  "v_mfma_f32_16x16x32_bf16 v[192:195], v[192:195], v[224:227], 0\n" \
  "v_mfma_f32_16x16x32_bf16 v[196:199], v[196:199], v[224:227], 0\n" \
  "s_waitcnt vmcnt(2)\n" \
  "v_mfma_f32_16x16x32_bf16 v[192:195], v[200:203], v[228:231], v[192:195]\n" \
  "v_mfma_f32_16x16x32_bf16 v[196:199], v[204:207], v[228:231], v[196:199]\n" \
  "s_waitcnt vmcnt(1)\n" \
  "v_mfma_f32_16x16x32_bf16 v[192:195], v[208:211], v[240:243], v[192:195]\n" \
  "v_mov_b64 v[200:201], 0\n" \
  "v_mfma_f32_16x16x32_bf16 v[196:199], v[212:215], v[240:243], v[196:199]\n" \
  "v_mov_b64 v[202:203], 0\n" \
  "s_waitcnt vmcnt(0)\n" \
  "v_mfma_f32_16x16x32_bf16 v[192:195], v[216:219], v[244:247], v[192:195]\n" \
  "v_mfma_f32_16x16x32_bf16 v[196:199], v[220:223], v[244:247], v[196:199]\n" \
  "s_waitcnt lgkmcnt(2)\n" \
  "v_smfmac_f32_16x16x64_bf16 v[200:203], v[232:235], v[224:231], v249\n" \
  "s_waitcnt lgkmcnt(0)\n" \
  "v_smfmac_f32_16x16x64_bf16 v[200:203], v[236:239], v[240:247], v249\n" \
  "s_nop 8\n"
#define QK_K \
  "s_branch .Lgo_%=\n" \
  AL(A_QK_K) \
  ".Lr_%=:\n" \
  "s_sub_u32 %[rt], %[rt], 1\n" \
  "s_cbranch_scc1 .Lrb_%=\n" \
  "s_sleep 2\n" \
  "buffer_load_dwordx4 v[192:195], v248, %[rs], %[o0] offen sc1\n" \
  "buffer_load_dwordx4 v[196:199], v248, %[rs], %[o0] offen offset:1024 sc1\n" \
  "v_mov_b64 v[224:225], 0\n" \
  "v_mov_b64 v[226:227], 0\n" \
  "v_mov_b64 v[228:229], 0\n" \
  "v_mov_b64 v[230:231], 0\n" \
  "s_nop 1\n" \
  "s_branch .Lw_%=\n" \
  ".Lgo_%=:\n" \
  "v_add_u32 v252, 0x20000, v248\n" \
  "ds_read_b128 v[200:203], v248 offset:32768\n" \
  "ds_read_b128 v[204:207], v248 offset:33792\n" \
  "ds_read_b128 v[208:211], v248 offset:49152\n" \
  "ds_read_b128 v[212:215], v248 offset:50176\n" \
  "ds_read_b128 v[216:219], v252 offset:0\n" \
  "ds_read_b128 v[220:223], v252 offset:1024\n" \
  "ds_read_b128 v[240:243], v252 offset:16384\n" \
  "ds_read_b128 v[244:247], v252 offset:17408\n" \
  "v_mov_b64 v[224:225], 0\n" \
  "v_mov_b64 v[226:227], 0\n" \
  "v_mov_b64 v[228:229], 0\n" \
  "v_mov_b64 v[230:231], 0\n" \
  "v_xor_b32 v250, 0x800, v248\n" \
  "v_xor_b32 v251, 0x1000, v248\n" \
  "s_mov_b32 %[rt], 0x40000\n" \
  "s_setprio 1\n" \
  "s_waitcnt lgkmcnt(0)\n" \
  "s_cmp_eq_u32 %[tw], 0\n" \
  "s_cbranch_scc1 .Ltq_%=\n" \
  ".Ltl_%=:\n" \
  "s_memtime vcc\n" \
  "s_waitcnt lgkmcnt(0)\n" \
  "s_sub_i32 %[cnt], %[tg], vcc_lo\n" \
  "s_cmp_lt_i32 %[cnt], 260\n" \
  "s_cbranch_scc1 .Ltf_%=\n" \
  "s_sleep 1\n" \
  "s_branch .Ltl_%=\n" \
  ".Ltf_%=:\n" \
  "s_max_i32 %[cnt], %[cnt], 0\n" \
  ".Ltg_%=:\n" \
  "s_sub_u32 %[cnt], %[cnt], 28\n" \
  "s_cbranch_scc0 .Ltg_%=\n" \
  ".Ltq_%=:\n" \
  "s_barrier\n" \
  "buffer_load_dwordx4 v[192:195], v248, %[rs], %[o0] offen sc1\n" \
  "buffer_load_dwordx4 v[196:199], v248, %[rs], %[o0] offen offset:1024 sc1\n" \
  ".Lw_%=:\n" \
  "s_waitcnt vmcnt(1)\n" \
  "ds_write_b128 v248, v[192:195]\n" \
  "v_smfmac_f32_16x16x64_bf16 v[224:227], v[192:195], v[200:207], v249\n" \
  "v_max3_u32 v252, v192, v193, v194\n" \
  "s_waitcnt vmcnt(0)\n" \
  "ds_write_b128 v248, v[196:199] offset:1024\n" \
  "v_smfmac_f32_16x16x64_bf16 v[228:231], v[192:195], v[208:215], v249\n" \
  "v_max3_u32 v252, v252, v195, v196\n" \
  "v_smfmac_f32_16x16x64_bf16 v[224:227], v[196:199], v[216:223], v249\n" \
  "v_max3_u32 v252, v252, v197, v198\n" \
  "v_max_u32 v252, v252, v199\n" \
  "v_smfmac_f32_16x16x64_bf16 v[228:231], v[196:199], v[240:247], v249\n" \
  "v_cmp_eq_u32 vcc, -1, v252\n" \
  "v_xor_b32 v252, 0x1800, v248\n" \
  "s_cbranch_vccnz .Lr_%=\n" \
  ".Lrb_%=:\n" \
  "s_waitcnt lgkmcnt(0)\n" \
  "s_barrier\n" \
  "s_memtime vcc\n" \
  "s_setprio 3\n" \
  "s_waitcnt lgkmcnt(0)\n" \
  "s_sub_u32 %[cnt], vcc_lo, %[ckm]\n" \
  "s_mul_hi_u32 %[cnt], %[cnt], %[inv]\n" \
  "s_add_u32 %[cnt], %[cnt], %[r16]\n" \
  "s_and_b32 %[cnt], %[cnt], -2\n" \
  "v_mov_b32 v235, %[cnt]\n" \
  "buffer_store_dword v235, v232, %[rs], %[sst] offen sc1\n" \
  "s_nop 5\n" \
  "ds_read_b128 v[200:203], v250 offset:0\n" \
  "ds_read_b128 v[204:207], v250 offset:1024\n" \
  "ds_read_b128 v[208:211], v251 offset:0\n" \
  "ds_read_b128 v[212:215], v251 offset:1024\n" \
  "ds_read_b128 v[216:219], v252 offset:0\n" \
  "ds_read_b128 v[220:223], v252 offset:1024\n" \
  "buffer_load_dword v250, v233, %[rs], %[t0] offen sc1\n" \
  "buffer_load_dword v251, v234, %[rs], %[t1] offen sc1\n" \
  "s_setprio 1\n" \
  "s_waitcnt lgkmcnt(5)\n" \
  "v_smfmac_f32_16x16x64_bf16 v[224:227], v[200:203], v[0:7], v249\n" \
  "v_smfmac_f32_16x16x64_bf16 v[228:231], v[200:203], v[48:55], v249\n" \
  "s_waitcnt lgkmcnt(4)\n" \
  "v_smfmac_f32_16x16x64_bf16 v[224:227], v[204:207], v[8:15], v249\n" \
  "v_mov_b64 v[232:233], 0\n" \
  "v_smfmac_f32_16x16x64_bf16 v[228:231], v[204:207], v[56:63], v249\n" \
  "v_mov_b64 v[234:235], 0\n" \
  "s_waitcnt lgkmcnt(3)\n" \
  "v_smfmac_f32_16x16x64_bf16 v[224:227], v[208:211], v[16:23], v249\n" \
  "ds_read_b128 v[240:243], v254 offset:0\n" \
  "v_smfmac_f32_16x16x64_bf16 v[228:231], v[208:211], v[64:71], v249\n" \
  "ds_read_b128 v[244:247], v254 offset:1024\n" \
  "s_waitcnt lgkmcnt(4)\n" \
  "v_smfmac_f32_16x16x64_bf16 v[224:227], v[212:215], v[24:31], v249\n" \
  "s_waitcnt lgkmcnt(3)\n" \
  "v_smfmac_f32_16x16x64_bf16 v[224:227], v[216:219], v[32:39], v249\n" \
  "s_waitcnt lgkmcnt(2)\n" \
  "v_smfmac_f32_16x16x64_bf16 v[224:227], v[220:223], v[40:47], v249\n" \
  "s_barrier\n" \
  "ds_read_b128 v[236:239], v248 offset:16384\n" \
  "v_smfmac_f32_16x16x64_bf16 v[228:231], v[212:215], v[72:79], v249\n" \
  "v_smfmac_f32_16x16x64_bf16 v[228:231], v[216:219], v[80:87], v249\n" \
  "s_nop 0\n" \
  "v_add_f32 v224, v224, v226\n" \
  "v_smfmac_f32_16x16x64_bf16 v[228:231], v[220:223], v[88:95], v249\n" \
  "v_add_f32 v225, v225, v227\n" \
  "s_waitcnt lgkmcnt(0)\n" \
  "v_add_f32 v236, v236, v224\n" \
  "v_add_f32 v237, v237, v225\n" \
  "v_cvt_pk_bf16_f32 v236, v236, v237\n" \
  "s_nop 3\n" \
  "v_add_f32 v238, v238, v228\n" \
  "v_add_f32 v239, v239, v229\n" \
  "v_add_f32 v238, v238, v230\n" \
  "v_add_f32 v239, v239, v231\n" \
  "v_cvt_pk_bf16_f32 v237, v238, v239\n" \
  "ds_write_b64 v253, v[236:237] offset:17408\n" \
  "s_setprio 0\n" \
  "ds_read_b128 v[224:227], v254 offset:16384\n" \
  "ds_read_b128 v[228:231], v254 offset:17408\n" \
  "v_mov_b64 v[236:237], 0\n" \
  "v_mov_b64 v[238:239], 0\n" \
  "v_smfmac_f32_16x16x64_bf16 v[232:235], v[192:195], v[96:103], v249\n" \
  "v_smfmac_f32_16x16x64_bf16 v[236:239], v[192:195], v[144:151], v249\n" \
  "s_waitcnt lgkmcnt(2)\n" \
  "s_barrier\n" \
  "v_smfmac_f32_16x16x64_bf16 v[232:235], v[196:199], v[104:111], v249\n" \
  "v_smfmac_f32_16x16x64_bf16 v[236:239], v[196:199], v[152:159], v249\n" \
  "v_smfmac_f32_16x16x64_bf16 v[232:235], v[200:203], v[240:247], v249\n" \
  "ds_read_b128 v[192:195], v254 offset:32768\n" \
  "ds_read_b128 v[196:199], v254 offset:33792\n" \
  "s_waitcnt lgkmcnt(2)\n" \
  "v_smfmac_f32_16x16x64_bf16 v[236:239], v[200:203], v[224:231], v249\n" \
  "ds_read_b128 v[240:243], v254 offset:49152\n" \
  "ds_read_b128 v[244:247], v254 offset:50176\n" \
  "v_smfmac_f32_16x16x64_bf16 v[232:235], v[204:207], v[112:119], v249\n" \
  "v_smfmac_f32_16x16x64_bf16 v[236:239], v[204:207], v[160:167], v249\n" \
  "s_barrier\n" \
  "ds_read_b128 v[200:203], v248 offset:8192\n" \
  "v_smfmac_f32_16x16x64_bf16 v[232:235], v[212:215], v[120:127], v249\n" \
  "v_smfmac_f32_16x16x64_bf16 v[236:239], v[212:215], v[168:175], v249\n" \
  "v_smfmac_f32_16x16x64_bf16 v[232:235], v[216:219], v[128:135], v249\n" \
  "v_smfmac_f32_16x16x64_bf16 v[232:235], v[220:223], v[136:143], v249\n" \
  "s_waitcnt lgkmcnt(0)\n" \
  "v_mul_f32 v200, %[cs], v200\n" \
  "v_smfmac_f32_16x16x64_bf16 v[232:235], v[208:211], v[192:199], v249\n" \
  "v_mul_f32 v201, %[cs], v201\n" \
  "v_smfmac_f32_16x16x64_bf16 v[236:239], v[216:219], v[176:183], v249\n" \
  "v_mul_f32 v202, %[cs], v202\n" \
  "v_smfmac_f32_16x16x64_bf16 v[236:239], v[220:223], v[184:191], v249\n" \
  "v_mul_f32 v203, %[cs], v203\n" \
  "v_smfmac_f32_16x16x64_bf16 v[236:239], v[208:211], v[240:247], v249\n" \
  "v_fma_f32 v200, v232, %[cs], v200\n" \
  "v_fma_f32 v201, v233, %[cs], v201\n" \
  "v_fma_f32 v200, v234, %[cs], v200\n" \
  "v_fma_f32 v201, v235, %[cs], v201\n" \
  "v_cvt_pk_bf16_f32 v200, v200, v201\n" \
  "s_nop 3\n" \
  "v_fma_f32 v202, v236, %[cs], v202\n" \
  "v_fma_f32 v203, v237, %[cs], v203\n" \
  "v_fma_f32 v202, v238, %[cs], v202\n" \
  "v_fma_f32 v203, v239, %[cs], v203\n" \
  "v_cvt_pk_bf16_f32 v201, v202, v203\n" \
  "ds_write_b64 v253, v[200:201] offset:9216\n" \
  "s_waitcnt lgkmcnt(0)\n" \
  "s_barrier\n" \
  "s_waitcnt vmcnt(0)\n"
#define OW_F \
  "s_branch .Lgo_%=\n" \
  AL(A_OW_F) \
  ".Lbad_%=:\n" \
  "s_mov_b32 %[st], 1\n" \
  "s_branch .Lend_%=\n" \
  ".Lgo_%=:\n" \
  "v_mov_b64 v[96:97], 0\n" \
  "v_mov_b64 v[98:99], 0\n" \
  "s_mov_b32 %[st], 0\n" \
  "s_barrier\n" \
  "s_nop 15\n" \
  "s_nop 15\n" \
  "s_nop 12\n" \
  "ds_read_b32 v100, v118\n" \
  "buffer_load_dwordx4 v[0:3], v112, %[rs], %[sv1] offen offset:0 sc1\n" \
  "buffer_load_dwordx4 v[4:7], v112, %[rs], %[sv1] offen offset:1024 sc1\n" \
  "buffer_load_dwordx4 v[8:11], v112, %[rs], %[sv1] offen offset:2048 sc1\n" \
  "buffer_load_dwordx4 v[12:15], v112, %[rs], %[sv1] offen offset:3072 sc1\n" \
  "s_memtime vcc\n" \
  "s_waitcnt lgkmcnt(0)\n" \
  "v_readfirstlane_b32 %[cnt], v100\n" \
  "s_nop 0\n" \
  "s_sub_i32 %[cnt], %[cnt], vcc_lo\n" \
  "s_max_i32 %[cnt], %[cnt], 0\n" \
  "s_min_i32 %[cnt], %[cnt], 2000\n" \
  ".Ltg_%=:\n" \
  "s_sub_u32 %[cnt], %[cnt], 28\n" \
  "s_cbranch_scc0 .Ltg_%=\n" \
  "buffer_load_dwordx4 v[16:19], v112, %[rs], %[sv2] offen offset:0 sc1\n" \
  "buffer_load_dwordx4 v[20:23], v112, %[rs], %[sv2] offen offset:1024 sc1\n" \
  "buffer_load_dwordx4 v[24:27], v112, %[rs], %[sv2] offen offset:2048 sc1\n" \
  "buffer_load_dwordx4 v[28:31], v112, %[rs], %[sv2] offen offset:3072 sc1\n" \
  "s_nop 3\n" \
  "buffer_load_dwordx4 v[64:67], v113, %[rs], %[sp1] offen offset:0 sc1\n" \
  "buffer_load_dwordx4 v[68:71], v113, %[rs], %[sp1] offen offset:2048 sc1\n" \
  "buffer_load_dwordx4 v[72:75], v113, %[rs], %[sp2] offen offset:0 sc1\n" \
  "buffer_load_dwordx4 v[76:79], v113, %[rs], %[sp2] offen offset:2048 sc1\n" \
  "buffer_load_dwordx4 v[80:83], v113, %[rs], %[sp1] offen offset:64 sc1\n" \
  "buffer_load_dwordx4 v[84:87], v113, %[rs], %[sp1] offen offset:2112 sc1\n" \
  "buffer_load_dwordx4 v[88:91], v113, %[rs], %[sp2] offen offset:64 sc1\n" \
  "buffer_load_dwordx4 v[92:95], v113, %[rs], %[sp2] offen offset:2112 sc1\n" \
  "s_waitcnt vmcnt(15)\n" \
  "v_permlane16_swap_b32 v0, v1\n" \
  "v_permlane16_swap_b32 v2, v3\n" \
  "s_waitcnt vmcnt(14)\n" \
  "v_permlane16_swap_b32 v4, v5\n" \
  "v_permlane16_swap_b32 v6, v7\n" \
  "s_waitcnt vmcnt(13)\n" \
  "v_permlane16_swap_b32 v8, v9\n" \
  "v_permlane16_swap_b32 v10, v11\n" \
  "s_waitcnt vmcnt(12)\n" \
  "v_permlane16_swap_b32 v12, v13\n" \
  "v_permlane16_swap_b32 v14, v15\n" \
  "s_waitcnt vmcnt(11)\n" \
  "v_permlane16_swap_b32 v16, v17\n" \
  "v_permlane16_swap_b32 v18, v19\n" \
  "s_waitcnt vmcnt(10)\n" \
  "v_permlane16_swap_b32 v20, v21\n" \
  "v_permlane16_swap_b32 v22, v23\n" \
  "s_waitcnt vmcnt(9)\n" \
  "v_permlane16_swap_b32 v24, v25\n" \
  "v_permlane16_swap_b32 v26, v27\n" \
  "s_waitcnt vmcnt(8)\n" \
  "v_permlane16_swap_b32 v28, v29\n" \
  "v_permlane16_swap_b32 v30, v31\n" \
  "s_barrier\n" \
  "ds_read_b128 v[32:35], v114 offset:0\n" \
  "ds_read_b128 v[36:39], v114 offset:1024\n" \
  "ds_read_b128 v[40:43], v114 offset:2048\n" \
  "ds_read_b128 v[44:47], v114 offset:3072\n" \
  "ds_read_b128 v[48:51], v119 offset:0\n" \
  "ds_read_b128 v[52:55], v119 offset:1024\n" \
  "ds_read_b128 v[56:59], v119 offset:2048\n" \
  "ds_read_b128 v[60:63], v119 offset:3072\n" \
  "s_waitcnt vmcnt(7)\n" \
  "v_smfmac_f32_16x16x64_bf16 v[96:99], v[64:67], v[0:7], v117\n" \
  "s_waitcnt vmcnt(6)\n" \
  "v_smfmac_f32_16x16x64_bf16 v[96:99], v[68:71], v[8:15], v117\n" \
  "s_waitcnt vmcnt(5)\n" \
  "v_smfmac_f32_16x16x64_bf16 v[96:99], v[72:75], v[16:23], v117\n" \
  "s_waitcnt vmcnt(4)\n" \
  "v_smfmac_f32_16x16x64_bf16 v[96:99], v[76:79], v[24:31], v117\n" \
  "s_waitcnt vmcnt(3) lgkmcnt(6)\n" \
  "v_smfmac_f32_16x16x64_bf16 v[96:99], v[80:83], v[32:39], v117 abid:1\n" \
  "s_waitcnt vmcnt(2) lgkmcnt(4)\n" \
  "v_smfmac_f32_16x16x64_bf16 v[96:99], v[84:87], v[40:47], v117 abid:1\n" \
  "s_waitcnt vmcnt(1) lgkmcnt(2)\n" \
  "v_smfmac_f32_16x16x64_bf16 v[96:99], v[88:91], v[48:55], v117 abid:1\n" \
  "s_waitcnt vmcnt(0) lgkmcnt(0)\n" \
  "v_smfmac_f32_16x16x64_bf16 v[96:99], v[92:95], v[56:63], v117 abid:1\n" \
  "s_nop 8\n" \
  "v_permlane32_swap_b32 v96, v98\n" \
  "v_permlane32_swap_b32 v97, v99\n" \
  "v_add_f32 v96, v96, v98\n" \
  "v_add_f32 v97, v97, v99\n" \
  "v_cvt_pk_bf16_f32 v100, v96, v97\n" \
  "v_cmp_u_f32 vcc, v96, v97\n" \
  "s_nop 0\n" \
  "v_mov_b32_dpp v101, v100 quad_perm:[1,0,3,2] row_mask:0xf bank_mask:0xf\n" \
  "v_perm_b32 v100, v101, v100, v116\n" \
  "s_cbranch_vccnz .Lbad_%=\n" \
  "buffer_store_dword v100, v115, %[rs], %[sm0] offen sc1\n" \
  "buffer_store_dword v100, v115, %[rs], %[sm1] offen sc1\n" \
  "buffer_store_dword v100, v115, %[rs], %[sm2] offen sc1\n" \
  "buffer_store_dword v100, v115, %[rs], %[sm3] offen sc1\n" \
  ".Lend_%=:\n" \
  "s_memtime %[tp]\n" \
  "v_permlane16_swap_b32 v0, v1\n" \
  "v_permlane16_swap_b32 v2, v3\n" \
  "v_permlane16_swap_b32 v4, v5\n" \
  "v_permlane16_swap_b32 v6, v7\n" \
  "v_permlane16_swap_b32 v8, v9\n" \
  "v_permlane16_swap_b32 v10, v11\n" \
  "v_permlane16_swap_b32 v12, v13\n" \
  "v_permlane16_swap_b32 v14, v15\n" \
  "v_permlane16_swap_b32 v16, v17\n" \
  "v_permlane16_swap_b32 v18, v19\n" \
  "v_permlane16_swap_b32 v20, v21\n" \
  "v_permlane16_swap_b32 v22, v23\n" \
  "v_permlane16_swap_b32 v24, v25\n" \
  "v_permlane16_swap_b32 v26, v27\n" \
  "v_permlane16_swap_b32 v28, v29\n" \
  "v_permlane16_swap_b32 v30, v31\n" \
  "s_waitcnt lgkmcnt(0)\n"
#define ST_Y \
  "s_mov_b32 %[keep], m0\n" \
  "ds_read_b128 v[240:243], v250 offset:0\n" \
  "ds_read_b128 v[244:247], v250 offset:1024\n" \
  "v_mfma_f32_16x16x32_bf16 v[192:195], v[0:3], v[208:211], 0\n" \
  "v_mfma_f32_16x16x32_bf16 v[196:199], v[64:67], v[208:211], 0\n" \
  "ds_read_b128 v[208:211], v252 offset:8192\n" \
  "v_mfma_f32_16x16x32_bf16 v[200:203], v[0:3], v[212:215], 0\n" \
  "v_mfma_f32_16x16x32_bf16 v[204:207], v[64:67], v[212:215], 0\n" \
  "ds_read_b128 v[212:215], v252 offset:9216\n" \
  "v_mfma_f32_16x16x32_bf16 v[192:195], v[128:131], v[216:219], v[192:195]\n" \
  "v_mfma_f32_16x16x32_bf16 v[196:199], v[132:135], v[216:219], v[196:199]\n" \
  "ds_read_b128 v[216:219], v252 offset:10240\n" \
  "v_mfma_f32_16x16x32_bf16 v[200:203], v[128:131], v[220:223], v[200:203]\n" \
  "v_mfma_f32_16x16x32_bf16 v[204:207], v[132:135], v[220:223], v[204:207]\n" \
  "ds_read_b128 v[220:223], v252 offset:11264\n" \
  "v_mfma_f32_16x16x32_bf16 v[192:195], v[4:7], v[224:227], v[192:195]\n" \
  "v_mfma_f32_16x16x32_bf16 v[196:199], v[68:71], v[224:227], v[196:199]\n" \
  "ds_read_b128 v[224:227], v252 offset:12288\n" \
  "v_mfma_f32_16x16x32_bf16 v[200:203], v[4:7], v[228:231], v[200:203]\n" \
  "v_mfma_f32_16x16x32_bf16 v[204:207], v[68:71], v[228:231], v[204:207]\n" \
  "ds_read_b128 v[228:231], v252 offset:13312\n" \
  "s_waitcnt lgkmcnt(7)\n" \
  "v_mfma_f32_16x16x32_bf16 v[192:195], v[240:243], v[232:235], v[192:195]\n" \
  "s_waitcnt lgkmcnt(6)\n" \
  "v_mfma_f32_16x16x32_bf16 v[196:199], v[244:247], v[232:235], v[196:199]\n" \
  "ds_read_b128 v[232:235], v252 offset:14336\n" \
  "v_mfma_f32_16x16x32_bf16 v[200:203], v[240:243], v[236:239], v[200:203]\n" \
  "v_mfma_f32_16x16x32_bf16 v[204:207], v[244:247], v[236:239], v[204:207]\n" \
  "ds_read_b128 v[236:239], v252 offset:15360\n" \
  "ds_read_b128 v[240:243], v250 offset:16384\n" \
  "ds_read_b128 v[244:247], v250 offset:17408\n" \
  "s_waitcnt lgkmcnt(9)\n" \
  "v_mfma_f32_16x16x32_bf16 v[192:195], v[8:11], v[208:211], v[192:195]\n" \
  "v_mfma_f32_16x16x32_bf16 v[196:199], v[72:75], v[208:211], v[196:199]\n" \
  "s_waitcnt lgkmcnt(8)\n" \
  "v_mfma_f32_16x16x32_bf16 v[200:203], v[8:11], v[212:215], v[200:203]\n" \
  "v_mfma_f32_16x16x32_bf16 v[204:207], v[72:75], v[212:215], v[204:207]\n" \
  "s_waitcnt vmcnt(2)\n" \
  "s_waitcnt lgkmcnt(0)\n" \
  "s_barrier\n" \
  "ds_read_b128 v[208:211], v252 offset:16384\n" \
  "ds_read_b128 v[212:215], v252 offset:17408\n" \
  "s_add_u32 m0, %[mw], 0\n" \
  "s_xor_b32 %[cnt], %[sc], 512\n" \
  "buffer_load_dwordx4 v248, %[rx], %[cnt] offen lds\n" \
  "s_add_u32 m0, %[mw], 8192\n" \
  "s_xor_b32 %[cnt], %[sc], 640\n" \
  "buffer_load_dwordx4 v248, %[rx], %[cnt] offen lds\n" \
  "v_mfma_f32_16x16x32_bf16 v[192:195], v[136:139], v[216:219], v[192:195]\n" \
  "v_mfma_f32_16x16x32_bf16 v[196:199], v[140:143], v[216:219], v[196:199]\n" \
  "ds_read_b128 v[216:219], v252 offset:18432\n" \
  "v_mfma_f32_16x16x32_bf16 v[200:203], v[136:139], v[220:223], v[200:203]\n" \
  "v_mfma_f32_16x16x32_bf16 v[204:207], v[140:143], v[220:223], v[204:207]\n" \
  "ds_read_b128 v[220:223], v252 offset:19456\n" \
  "v_mfma_f32_16x16x32_bf16 v[192:195], v[12:15], v[224:227], v[192:195]\n" \
  "v_mfma_f32_16x16x32_bf16 v[196:199], v[76:79], v[224:227], v[196:199]\n" \
  "ds_read_b128 v[224:227], v252 offset:20480\n" \
  "v_mfma_f32_16x16x32_bf16 v[200:203], v[12:15], v[228:231], v[200:203]\n" \
  "v_mfma_f32_16x16x32_bf16 v[204:207], v[76:79], v[228:231], v[204:207]\n" \
  "ds_read_b128 v[228:231], v252 offset:21504\n" \
  "v_mfma_f32_16x16x32_bf16 v[192:195], v[240:243], v[232:235], v[192:195]\n" \
  "v_mfma_f32_16x16x32_bf16 v[196:199], v[244:247], v[232:235], v[196:199]\n" \
  "ds_read_b128 v[232:235], v252 offset:22528\n" \
  "v_mfma_f32_16x16x32_bf16 v[200:203], v[240:243], v[236:239], v[200:203]\n" \
  "v_mfma_f32_16x16x32_bf16 v[204:207], v[244:247], v[236:239], v[204:207]\n" \
  "ds_read_b128 v[236:239], v252 offset:23552\n" \
  "ds_read_b128 v[240:243], v250 offset:32768\n" \
  "ds_read_b128 v[244:247], v250 offset:33792\n" \
  "s_waitcnt lgkmcnt(9)\n" \
  "v_mfma_f32_16x16x32_bf16 v[192:195], v[16:19], v[208:211], v[192:195]\n" \
  "v_mfma_f32_16x16x32_bf16 v[196:199], v[80:83], v[208:211], v[196:199]\n" \
  "ds_read_b128 v[208:211], v252 offset:24576\n" \
  "s_waitcnt lgkmcnt(9)\n" \
  "v_mfma_f32_16x16x32_bf16 v[200:203], v[16:19], v[212:215], v[200:203]\n" \
  "v_mfma_f32_16x16x32_bf16 v[204:207], v[80:83], v[212:215], v[204:207]\n" \
  "ds_read_b128 v[212:215], v252 offset:25600\n" \
  "s_waitcnt lgkmcnt(9)\n" \
  "v_mfma_f32_16x16x32_bf16 v[192:195], v[144:147], v[216:219], v[192:195]\n" \
  "v_mfma_f32_16x16x32_bf16 v[196:199], v[148:151], v[216:219], v[196:199]\n" \
  "ds_read_b128 v[216:219], v252 offset:26624\n" \
  "s_waitcnt lgkmcnt(9)\n" \
  "v_mfma_f32_16x16x32_bf16 v[200:203], v[144:147], v[220:223], v[200:203]\n" \
  "v_mfma_f32_16x16x32_bf16 v[204:207], v[148:151], v[220:223], v[204:207]\n" \
  "ds_read_b128 v[220:223], v252 offset:27648\n" \
  "s_waitcnt lgkmcnt(9)\n" \
  "v_mfma_f32_16x16x32_bf16 v[192:195], v[20:23], v[224:227], v[192:195]\n" \
  "v_mfma_f32_16x16x32_bf16 v[196:199], v[84:87], v[224:227], v[196:199]\n" \
  "ds_read_b128 v[224:227], v252 offset:28672\n" \
  "s_waitcnt lgkmcnt(9)\n" \
  "v_mfma_f32_16x16x32_bf16 v[200:203], v[20:23], v[228:231], v[200:203]\n" \
  "v_mfma_f32_16x16x32_bf16 v[204:207], v[84:87], v[228:231], v[204:207]\n" \
  "ds_read_b128 v[228:231], v252 offset:29696\n" \
  "s_waitcnt lgkmcnt(7)\n" \
  "v_mfma_f32_16x16x32_bf16 v[192:195], v[240:243], v[232:235], v[192:195]\n" \
  "s_waitcnt lgkmcnt(6)\n" \
  "v_mfma_f32_16x16x32_bf16 v[196:199], v[244:247], v[232:235], v[196:199]\n" \
  "ds_read_b128 v[232:235], v252 offset:30720\n" \
  "v_mfma_f32_16x16x32_bf16 v[200:203], v[240:243], v[236:239], v[200:203]\n" \
  "v_mfma_f32_16x16x32_bf16 v[204:207], v[244:247], v[236:239], v[204:207]\n" \
  "ds_read_b128 v[236:239], v252 offset:31744\n" \
  "ds_read_b128 v[240:243], v250 offset:49152\n" \
  "ds_read_b128 v[244:247], v250 offset:50176\n" \
  "s_waitcnt lgkmcnt(9)\n" \
  "v_mfma_f32_16x16x32_bf16 v[192:195], v[24:27], v[208:211], v[192:195]\n" \
  "v_mfma_f32_16x16x32_bf16 v[196:199], v[88:91], v[208:211], v[196:199]\n" \
  "s_waitcnt lgkmcnt(8)\n" \
  "v_mfma_f32_16x16x32_bf16 v[200:203], v[24:27], v[212:215], v[200:203]\n" \
  "v_mfma_f32_16x16x32_bf16 v[204:207], v[88:91], v[212:215], v[204:207]\n" \
  "s_waitcnt vmcnt(0)\n" \
  "s_waitcnt lgkmcnt(0)\n" \
  "s_barrier\n" \
  "ds_read_b128 v[208:211], v252 offset:0\n" \
  "ds_read_b128 v[212:215], v252 offset:1024\n" \
  "s_add_u32 m0, %[mw], 16384\n" \
  "s_xor_b32 %[cnt], %[sc], 768\n" \
  "buffer_load_dwordx4 v248, %[rx], %[cnt] offen lds\n" \
  "s_add_u32 m0, %[mw], 24576\n" \
  "s_xor_b32 %[cnt], %[sc], 896\n" \
  "buffer_load_dwordx4 v248, %[rx], %[cnt] offen lds\n" \
  "v_lshl_or_b32 v253, v251, 24, %[fm]\n" \
  "buffer_store_dword v253, v253, %[rs], %[fo] offen sc1\n" \
  "v_mfma_f32_16x16x32_bf16 v[192:195], v[152:155], v[216:219], v[192:195]\n" \
  "v_mfma_f32_16x16x32_bf16 v[196:199], v[156:159], v[216:219], v[196:199]\n" \
  "ds_read_b128 v[216:219], v252 offset:2048\n" \
  "v_mfma_f32_16x16x32_bf16 v[200:203], v[152:155], v[220:223], v[200:203]\n" \
  "v_mfma_f32_16x16x32_bf16 v[204:207], v[156:159], v[220:223], v[204:207]\n" \
  "ds_read_b128 v[220:223], v252 offset:3072\n" \
  "v_mfma_f32_16x16x32_bf16 v[192:195], v[28:31], v[224:227], v[192:195]\n" \
  "v_mfma_f32_16x16x32_bf16 v[196:199], v[92:95], v[224:227], v[196:199]\n" \
  "ds_read_b128 v[224:227], v252 offset:4096\n" \
  "v_mfma_f32_16x16x32_bf16 v[200:203], v[28:31], v[228:231], v[200:203]\n" \
  "v_mfma_f32_16x16x32_bf16 v[204:207], v[92:95], v[228:231], v[204:207]\n" \
  "ds_read_b128 v[228:231], v252 offset:5120\n" \
  "v_mfma_f32_16x16x32_bf16 v[192:195], v[240:243], v[232:235], v[192:195]\n" \
  "v_mfma_f32_16x16x32_bf16 v[196:199], v[244:247], v[232:235], v[196:199]\n" \
  "ds_read_b128 v[232:235], v252 offset:6144\n" \
  "v_mfma_f32_16x16x32_bf16 v[200:203], v[240:243], v[236:239], v[200:203]\n" \
  "v_mfma_f32_16x16x32_bf16 v[204:207], v[244:247], v[236:239], v[204:207]\n" \
  "ds_read_b128 v[236:239], v252 offset:7168\n" \
  "ds_read_b128 v[240:243], v249 offset:0\n" \
  "ds_read_b128 v[244:247], v249 offset:1024\n" \
  "s_waitcnt lgkmcnt(9)\n" \
  "v_mfma_f32_16x16x32_bf16 v[192:195], v[32:35], v[208:211], v[192:195]\n" \
  "v_mfma_f32_16x16x32_bf16 v[196:199], v[96:99], v[208:211], v[196:199]\n" \
  "ds_read_b128 v[208:211], v252 offset:8192\n" \
  "s_waitcnt lgkmcnt(9)\n" \
  "v_mfma_f32_16x16x32_bf16 v[200:203], v[32:35], v[212:215], v[200:203]\n" \
  "v_mfma_f32_16x16x32_bf16 v[204:207], v[96:99], v[212:215], v[204:207]\n" \
  "ds_read_b128 v[212:215], v252 offset:9216\n" \
  "s_waitcnt lgkmcnt(9)\n" \
  "v_mfma_f32_16x16x32_bf16 v[192:195], v[160:163], v[216:219], v[192:195]\n" \
  "v_mfma_f32_16x16x32_bf16 v[196:199], v[164:167], v[216:219], v[196:199]\n" \
  "ds_read_b128 v[216:219], v252 offset:10240\n" \
  "s_waitcnt lgkmcnt(9)\n" \
  "v_mfma_f32_16x16x32_bf16 v[200:203], v[160:163], v[220:223], v[200:203]\n" \
  "v_mfma_f32_16x16x32_bf16 v[204:207], v[164:167], v[220:223], v[204:207]\n" \
  "ds_read_b128 v[220:223], v252 offset:11264\n" \
  "s_waitcnt lgkmcnt(9)\n" \
  "v_mfma_f32_16x16x32_bf16 v[192:195], v[36:39], v[224:227], v[192:195]\n" \
  "v_mfma_f32_16x16x32_bf16 v[196:199], v[100:103], v[224:227], v[196:199]\n" \
  "ds_read_b128 v[224:227], v252 offset:12288\n" \
  "s_waitcnt lgkmcnt(9)\n" \
  "v_mfma_f32_16x16x32_bf16 v[200:203], v[36:39], v[228:231], v[200:203]\n" \
  "v_mfma_f32_16x16x32_bf16 v[204:207], v[100:103], v[228:231], v[204:207]\n" \
  "ds_read_b128 v[228:231], v252 offset:13312\n" \
  "s_waitcnt lgkmcnt(7)\n" \
  "v_mfma_f32_16x16x32_bf16 v[192:195], v[240:243], v[232:235], v[192:195]\n" \
  "s_waitcnt lgkmcnt(6)\n" \
  "v_mfma_f32_16x16x32_bf16 v[196:199], v[244:247], v[232:235], v[196:199]\n" \
  "ds_read_b128 v[232:235], v252 offset:14336\n" \
  "v_mfma_f32_16x16x32_bf16 v[200:203], v[240:243], v[236:239], v[200:203]\n" \
  "v_mfma_f32_16x16x32_bf16 v[204:207], v[244:247], v[236:239], v[204:207]\n" \
  "ds_read_b128 v[236:239], v252 offset:15360\n" \
  "ds_read_b128 v[240:243], v249 offset:16384\n" \
  "ds_read_b128 v[244:247], v249 offset:17408\n" \
  "s_waitcnt lgkmcnt(9)\n" \
  "v_mfma_f32_16x16x32_bf16 v[192:195], v[40:43], v[208:211], v[192:195]\n" \
  "v_mfma_f32_16x16x32_bf16 v[196:199], v[104:107], v[208:211], v[196:199]\n" \
  "s_waitcnt lgkmcnt(8)\n" \
  "v_mfma_f32_16x16x32_bf16 v[200:203], v[40:43], v[212:215], v[200:203]\n" \
  "v_mfma_f32_16x16x32_bf16 v[204:207], v[104:107], v[212:215], v[204:207]\n" \
  "s_waitcnt vmcnt(1)\n" \
  "s_waitcnt lgkmcnt(0)\n" \
  "s_barrier\n" \
  "ds_read_b128 v[208:211], v252 offset:16384\n" \
  "ds_read_b128 v[212:215], v252 offset:17408\n" \
  "s_add_u32 m0, %[mw], 0\n" \
  "s_xor_b32 %[cnt], %[sn], 0\n" \
  "buffer_load_dwordx4 v248, %[rx], %[cnt] offen lds\n" \
  "s_add_u32 m0, %[mw], 8192\n" \
  "s_xor_b32 %[cnt], %[sn], 128\n" \
  "buffer_load_dwordx4 v248, %[rx], %[cnt] offen lds\n" \
  "v_lshl_or_b32 v253, v251, 3, %[tm]\n" \
  "buffer_load_dword v253, v253, %[rx], %[tc] offen\n" \
  "v_mfma_f32_16x16x32_bf16 v[192:195], v[168:171], v[216:219], v[192:195]\n" \
  "v_mfma_f32_16x16x32_bf16 v[196:199], v[172:175], v[216:219], v[196:199]\n" \
  "ds_read_b128 v[216:219], v252 offset:18432\n" \
  "v_mfma_f32_16x16x32_bf16 v[200:203], v[168:171], v[220:223], v[200:203]\n" \
  "v_mfma_f32_16x16x32_bf16 v[204:207], v[172:175], v[220:223], v[204:207]\n" \
  "ds_read_b128 v[220:223], v252 offset:19456\n" \
  "v_mfma_f32_16x16x32_bf16 v[192:195], v[44:47], v[224:227], v[192:195]\n" \
  "v_mfma_f32_16x16x32_bf16 v[196:199], v[108:111], v[224:227], v[196:199]\n" \
  "ds_read_b128 v[224:227], v252 offset:20480\n" \
  "v_mfma_f32_16x16x32_bf16 v[200:203], v[44:47], v[228:231], v[200:203]\n" \
  "v_mfma_f32_16x16x32_bf16 v[204:207], v[108:111], v[228:231], v[204:207]\n" \
  "ds_read_b128 v[228:231], v252 offset:21504\n" \
  "v_mfma_f32_16x16x32_bf16 v[192:195], v[240:243], v[232:235], v[192:195]\n" \
  "v_mfma_f32_16x16x32_bf16 v[196:199], v[244:247], v[232:235], v[196:199]\n" \
  "ds_read_b128 v[232:235], v252 offset:22528\n" \
  "v_mfma_f32_16x16x32_bf16 v[200:203], v[240:243], v[236:239], v[200:203]\n" \
  "v_mfma_f32_16x16x32_bf16 v[204:207], v[244:247], v[236:239], v[204:207]\n" \
  "ds_read_b128 v[236:239], v252 offset:23552\n" \
  "ds_read_b128 v[240:243], v249 offset:32768\n" \
  "ds_read_b128 v[244:247], v249 offset:33792\n" \
  "s_waitcnt lgkmcnt(9)\n" \
  "v_mfma_f32_16x16x32_bf16 v[192:195], v[48:51], v[208:211], v[192:195]\n" \
  "v_mfma_f32_16x16x32_bf16 v[196:199], v[112:115], v[208:211], v[196:199]\n" \
  "ds_read_b128 v[208:211], v252 offset:24576\n" \
  "s_waitcnt lgkmcnt(9)\n" \
  "v_mfma_f32_16x16x32_bf16 v[200:203], v[48:51], v[212:215], v[200:203]\n" \
  "v_mfma_f32_16x16x32_bf16 v[204:207], v[112:115], v[212:215], v[204:207]\n" \
  "ds_read_b128 v[212:215], v252 offset:25600\n" \
  "s_waitcnt lgkmcnt(9)\n" \
  "v_mfma_f32_16x16x32_bf16 v[192:195], v[176:179], v[216:219], v[192:195]\n" \
  "v_mfma_f32_16x16x32_bf16 v[196:199], v[180:183], v[216:219], v[196:199]\n" \
  "ds_read_b128 v[216:219], v252 offset:26624\n" \
  "s_waitcnt lgkmcnt(9)\n" \
  "v_mfma_f32_16x16x32_bf16 v[200:203], v[176:179], v[220:223], v[200:203]\n" \
  "v_mfma_f32_16x16x32_bf16 v[204:207], v[180:183], v[220:223], v[204:207]\n" \
  "ds_read_b128 v[220:223], v252 offset:27648\n" \
  "s_waitcnt lgkmcnt(9)\n" \
  "v_mfma_f32_16x16x32_bf16 v[192:195], v[52:55], v[224:227], v[192:195]\n" \
  "v_mfma_f32_16x16x32_bf16 v[196:199], v[116:119], v[224:227], v[196:199]\n" \
  "ds_read_b128 v[224:227], v252 offset:28672\n" \
  "s_waitcnt lgkmcnt(9)\n" \
  "v_mfma_f32_16x16x32_bf16 v[200:203], v[52:55], v[228:231], v[200:203]\n" \
  "v_mfma_f32_16x16x32_bf16 v[204:207], v[116:119], v[228:231], v[204:207]\n" \
  "ds_read_b128 v[228:231], v252 offset:29696\n" \
  "s_waitcnt lgkmcnt(7)\n" \
  "v_mfma_f32_16x16x32_bf16 v[192:195], v[240:243], v[232:235], v[192:195]\n" \
  "s_waitcnt lgkmcnt(6)\n" \
  "v_mfma_f32_16x16x32_bf16 v[196:199], v[244:247], v[232:235], v[196:199]\n" \
  "ds_read_b128 v[232:235], v252 offset:30720\n" \
  "v_mfma_f32_16x16x32_bf16 v[200:203], v[240:243], v[236:239], v[200:203]\n" \
  "v_mfma_f32_16x16x32_bf16 v[204:207], v[244:247], v[236:239], v[204:207]\n" \
  "ds_read_b128 v[236:239], v252 offset:31744\n" \
  "ds_read_b128 v[240:243], v249 offset:49152\n" \
  "ds_read_b128 v[244:247], v249 offset:50176\n" \
  "s_waitcnt lgkmcnt(9)\n" \
  "v_mfma_f32_16x16x32_bf16 v[192:195], v[56:59], v[208:211], v[192:195]\n" \
  "v_mfma_f32_16x16x32_bf16 v[196:199], v[120:123], v[208:211], v[196:199]\n" \
  "s_waitcnt lgkmcnt(8)\n" \
  "v_mfma_f32_16x16x32_bf16 v[200:203], v[56:59], v[212:215], v[200:203]\n" \
  "v_mfma_f32_16x16x32_bf16 v[204:207], v[120:123], v[212:215], v[204:207]\n" \
  "s_waitcnt vmcnt(1)\n" \
  "s_waitcnt lgkmcnt(0)\n" \
  "s_barrier\n" \
  "ds_read_b128 v[208:211], v252 offset:0\n" \
  "ds_read_b128 v[212:215], v252 offset:1024\n" \
  "s_add_u32 m0, %[mw], 16384\n" \
  "s_xor_b32 %[cnt], %[sn], 256\n" \
  "buffer_load_dwordx4 v248, %[rx], %[cnt] offen lds\n" \
  "s_add_u32 m0, %[mw], 24576\n" \
  "s_xor_b32 %[cnt], %[sn], 384\n" \
  "buffer_load_dwordx4 v248, %[rx], %[cnt] offen lds\n" \
  "v_mfma_f32_16x16x32_bf16 v[192:195], v[184:187], v[216:219], v[192:195]\n" \
  "v_mfma_f32_16x16x32_bf16 v[196:199], v[188:191], v[216:219], v[196:199]\n" \
  "ds_read_b128 v[216:219], v252 offset:2048\n" \
  "v_mfma_f32_16x16x32_bf16 v[200:203], v[184:187], v[220:223], v[200:203]\n" \
  "v_mfma_f32_16x16x32_bf16 v[204:207], v[188:191], v[220:223], v[204:207]\n" \
  "ds_read_b128 v[220:223], v252 offset:3072\n" \
  "v_mfma_f32_16x16x32_bf16 v[192:195], v[60:63], v[224:227], v[192:195]\n" \
  "v_mfma_f32_16x16x32_bf16 v[196:199], v[124:127], v[224:227], v[196:199]\n" \
  "ds_read_b128 v[224:227], v252 offset:4096\n" \
  "v_mfma_f32_16x16x32_bf16 v[200:203], v[60:63], v[228:231], v[200:203]\n" \
  "v_mfma_f32_16x16x32_bf16 v[204:207], v[124:127], v[228:231], v[204:207]\n" \
  "ds_read_b128 v[228:231], v252 offset:5120\n" \
  "v_mfma_f32_16x16x32_bf16 v[192:195], v[240:243], v[232:235], v[192:195]\n" \
  "v_mfma_f32_16x16x32_bf16 v[196:199], v[244:247], v[232:235], v[196:199]\n" \
  "ds_read_b128 v[232:235], v252 offset:6144\n" \
  "v_mfma_f32_16x16x32_bf16 v[200:203], v[240:243], v[236:239], v[200:203]\n" \
  "v_mfma_f32_16x16x32_bf16 v[204:207], v[244:247], v[236:239], v[204:207]\n" \
  "ds_read_b128 v[236:239], v252 offset:7168\n" \
  "s_cmp_eq_u32 %[cs], 0\n" \
  "s_cbranch_scc1 .Lq_%=\n" \
  "v_mul_f32 v192, %[cs], v192\n" \
  "v_mul_f32 v193, %[cs], v193\n" \
  "v_mul_f32 v194, %[cs], v194\n" \
  "v_mul_f32 v195, %[cs], v195\n" \
  "v_mul_f32 v196, %[cs], v196\n" \
  "v_mul_f32 v197, %[cs], v197\n" \
  "v_mul_f32 v198, %[cs], v198\n" \
  "v_mul_f32 v199, %[cs], v199\n" \
  "v_cvt_pk_bf16_f32 v192, v192, v193\n" \
  "v_cvt_pk_bf16_f32 v193, v196, v197\n" \
  "v_cvt_pk_bf16_f32 v194, v194, v195\n" \
  "v_cvt_pk_bf16_f32 v195, v198, v199\n" \
  "buffer_store_dwordx4 v[192:195], v251, %[rs], %[so] offen sc1\n" \
  "v_mul_f32 v200, %[cs], v200\n" \
  "v_mul_f32 v201, %[cs], v201\n" \
  "v_mul_f32 v202, %[cs], v202\n" \
  "v_mul_f32 v203, %[cs], v203\n" \
  "v_mul_f32 v204, %[cs], v204\n" \
  "v_mul_f32 v205, %[cs], v205\n" \
  "v_mul_f32 v206, %[cs], v206\n" \
  "v_mul_f32 v207, %[cs], v207\n" \
  "v_cvt_pk_bf16_f32 v200, v200, v201\n" \
  "v_cvt_pk_bf16_f32 v201, v204, v205\n" \
  "v_cvt_pk_bf16_f32 v202, v202, v203\n" \
  "v_cvt_pk_bf16_f32 v203, v206, v207\n" \
  "s_add_u32 %[cnt], %[so], %c[rt]\n" \
  "buffer_store_dwordx4 v[200:203], v251, %[rs], %[cnt] offen sc1\n" \
  "s_branch .Lqe_%=\n" \
  ".Lq_%=:\n" \
  "v_cvt_pk_bf16_f32 v192, v192, v193\n" \
  "v_cvt_pk_bf16_f32 v193, v196, v197\n" \
  "v_cvt_pk_bf16_f32 v194, v194, v195\n" \
  "v_cvt_pk_bf16_f32 v195, v198, v199\n" \
  "buffer_store_dwordx4 v[192:195], v251, %[rs], %[so] offen sc1\n" \
  "v_cvt_pk_bf16_f32 v200, v200, v201\n" \
  "v_cvt_pk_bf16_f32 v201, v204, v205\n" \
  "v_cvt_pk_bf16_f32 v202, v202, v203\n" \
  "v_cvt_pk_bf16_f32 v203, v206, v207\n" \
  "s_add_u32 %[cnt], %[so], %c[rt]\n" \
  "buffer_store_dwordx4 v[200:203], v251, %[rs], %[cnt] offen sc1\n" \
  ".Lqe_%=:\n" \
  "s_mov_b32 m0, %[keep]\n" \
  "s_waitcnt lgkmcnt(0)\n"
#define ST_XA \
  "s_mov_b32 %[keep], m0\n" \
  "ds_read_b128 v[240:243], v250 offset:0\n" \
  "ds_read_b128 v[244:247], v250 offset:1024\n" \
  "v_mfma_f32_16x16x32_bf16 v[192:195], v[0:3], v[208:211], 0\n" \
  "v_mfma_f32_16x16x32_bf16 v[196:199], v[64:67], v[208:211], 0\n" \
  "ds_read_b128 v[208:211], v252 offset:8192\n" \
  "v_mfma_f32_16x16x32_bf16 v[200:203], v[0:3], v[212:215], 0\n" \
  "v_mfma_f32_16x16x32_bf16 v[204:207], v[64:67], v[212:215], 0\n" \
  "ds_read_b128 v[212:215], v252 offset:9216\n" \
  "v_mfma_f32_16x16x32_bf16 v[192:195], v[128:131], v[216:219], v[192:195]\n" \
  "v_mfma_f32_16x16x32_bf16 v[196:199], v[132:135], v[216:219], v[196:199]\n" \
  "ds_read_b128 v[216:219], v252 offset:10240\n" \
  "v_mfma_f32_16x16x32_bf16 v[200:203], v[128:131], v[220:223], v[200:203]\n" \
  "v_mfma_f32_16x16x32_bf16 v[204:207], v[132:135], v[220:223], v[204:207]\n" \
  "ds_read_b128 v[220:223], v252 offset:11264\n" \
  "v_mfma_f32_16x16x32_bf16 v[192:195], v[4:7], v[224:227], v[192:195]\n" \
  "v_mfma_f32_16x16x32_bf16 v[196:199], v[68:71], v[224:227], v[196:199]\n" \
  "v_mfma_f32_16x16x32_bf16 v[200:203], v[4:7], v[228:231], v[200:203]\n" \
  "v_mfma_f32_16x16x32_bf16 v[204:207], v[68:71], v[228:231], v[204:207]\n" \
  "s_waitcnt vmcnt(0)\n" \
  "s_waitcnt lgkmcnt(0)\n" \
  "s_barrier\n" \
  "ds_read_b128 v[224:227], v252 offset:12288\n" \
  "ds_read_b128 v[228:231], v252 offset:13312\n" \
  "s_add_u32 m0, %[mw], 0\n" \
  "s_xor_b32 %[cnt], %[sc], 384\n" \
  "buffer_load_dwordx4 v248, %[rx], %[cnt] offen lds\n" \
  "s_add_u32 m0, %[mw], 4096\n" \
  "s_xor_b32 %[cnt], %[sc], 448\n" \
  "buffer_load_dwordx4 v248, %[rx], %[cnt] offen lds\n" \
  "v_mfma_f32_16x16x32_bf16 v[192:195], v[240:243], v[232:235], v[192:195]\n" \
  "v_mfma_f32_16x16x32_bf16 v[196:199], v[244:247], v[232:235], v[196:199]\n" \
  "ds_read_b128 v[232:235], v252 offset:14336\n" \
  "v_mfma_f32_16x16x32_bf16 v[200:203], v[240:243], v[236:239], v[200:203]\n" \
  "v_mfma_f32_16x16x32_bf16 v[204:207], v[244:247], v[236:239], v[204:207]\n" \
  "ds_read_b128 v[236:239], v252 offset:15360\n" \
  "ds_read_b128 v[240:243], v250 offset:16384\n" \
  "ds_read_b128 v[244:247], v250 offset:17408\n" \
  "v_mfma_f32_16x16x32_bf16 v[192:195], v[8:11], v[208:211], v[192:195]\n" \
  "v_mfma_f32_16x16x32_bf16 v[196:199], v[72:75], v[208:211], v[196:199]\n" \
  "ds_read_b128 v[208:211], v252 offset:16384\n" \
  "v_mfma_f32_16x16x32_bf16 v[200:203], v[8:11], v[212:215], v[200:203]\n" \
  "v_mfma_f32_16x16x32_bf16 v[204:207], v[72:75], v[212:215], v[204:207]\n" \
  "ds_read_b128 v[212:215], v252 offset:17408\n" \
  "v_mfma_f32_16x16x32_bf16 v[192:195], v[136:139], v[216:219], v[192:195]\n" \
  "v_mfma_f32_16x16x32_bf16 v[196:199], v[140:143], v[216:219], v[196:199]\n" \
  "ds_read_b128 v[216:219], v252 offset:18432\n" \
  "v_mfma_f32_16x16x32_bf16 v[200:203], v[136:139], v[220:223], v[200:203]\n" \
  "v_mfma_f32_16x16x32_bf16 v[204:207], v[140:143], v[220:223], v[204:207]\n" \
  "ds_read_b128 v[220:223], v252 offset:19456\n" \
  "s_waitcnt lgkmcnt(9)\n" \
  "v_mfma_f32_16x16x32_bf16 v[192:195], v[12:15], v[224:227], v[192:195]\n" \
  "v_mfma_f32_16x16x32_bf16 v[196:199], v[76:79], v[224:227], v[196:199]\n" \
  "ds_read_b128 v[224:227], v252 offset:20480\n" \
  "s_waitcnt lgkmcnt(9)\n" \
  "v_mfma_f32_16x16x32_bf16 v[200:203], v[12:15], v[228:231], v[200:203]\n" \
  "v_mfma_f32_16x16x32_bf16 v[204:207], v[76:79], v[228:231], v[204:207]\n" \
  "ds_read_b128 v[228:231], v252 offset:21504\n" \
  "s_waitcnt lgkmcnt(7)\n" \
  "v_mfma_f32_16x16x32_bf16 v[192:195], v[240:243], v[232:235], v[192:195]\n" \
  "s_waitcnt lgkmcnt(6)\n" \
  "v_mfma_f32_16x16x32_bf16 v[196:199], v[244:247], v[232:235], v[196:199]\n" \
  "ds_read_b128 v[232:235], v252 offset:22528\n" \
  "v_mfma_f32_16x16x32_bf16 v[200:203], v[240:243], v[236:239], v[200:203]\n" \
  "v_mfma_f32_16x16x32_bf16 v[204:207], v[244:247], v[236:239], v[204:207]\n" \
  "ds_read_b128 v[236:239], v252 offset:23552\n" \
  "ds_read_b128 v[240:243], v250 offset:32768\n" \
  "ds_read_b128 v[244:247], v250 offset:33792\n" \
  "s_waitcnt lgkmcnt(9)\n" \
  "v_mfma_f32_16x16x32_bf16 v[192:195], v[16:19], v[208:211], v[192:195]\n" \
  "v_mfma_f32_16x16x32_bf16 v[196:199], v[80:83], v[208:211], v[196:199]\n" \
  "s_waitcnt lgkmcnt(8)\n" \
  "v_mfma_f32_16x16x32_bf16 v[200:203], v[16:19], v[212:215], v[200:203]\n" \
  "v_mfma_f32_16x16x32_bf16 v[204:207], v[80:83], v[212:215], v[204:207]\n" \
  "s_waitcnt vmcnt(0)\n" \
  "s_waitcnt lgkmcnt(0)\n" \
  "s_barrier\n" \
  "ds_read_b128 v[208:211], v252 offset:0\n" \
  "ds_read_b128 v[212:215], v252 offset:1024\n" \
  "s_add_u32 m0, %[mw], 12288\n" \
  "s_xor_b32 %[cnt], %[sc], 512\n" \
  "buffer_load_dwordx4 v248, %[rx], %[cnt] offen lds\n" \
  "s_add_u32 m0, %[mw], 16384\n" \
  "s_xor_b32 %[cnt], %[sc], 576\n" \
  "buffer_load_dwordx4 v248, %[rx], %[cnt] offen lds\n" \
  "s_add_u32 m0, %[mw], 20480\n" \
  "s_xor_b32 %[cnt], %[sc], 640\n" \
  "buffer_load_dwordx4 v248, %[rx], %[cnt] offen lds\n" \
  "v_lshl_or_b32 v253, v251, 24, %[fm]\n" \
  "buffer_store_dword v253, v253, %[rs], %[fo] offen sc1\n" \
  "v_mfma_f32_16x16x32_bf16 v[192:195], v[144:147], v[216:219], v[192:195]\n" \
  "v_mfma_f32_16x16x32_bf16 v[196:199], v[148:151], v[216:219], v[196:199]\n" \
  "ds_read_b128 v[216:219], v252 offset:2048\n" \
  "v_mfma_f32_16x16x32_bf16 v[200:203], v[144:147], v[220:223], v[200:203]\n" \
  "v_mfma_f32_16x16x32_bf16 v[204:207], v[148:151], v[220:223], v[204:207]\n" \
  "ds_read_b128 v[220:223], v252 offset:3072\n" \
  "v_mfma_f32_16x16x32_bf16 v[192:195], v[20:23], v[224:227], v[192:195]\n" \
  "v_mfma_f32_16x16x32_bf16 v[196:199], v[84:87], v[224:227], v[196:199]\n" \
  "ds_read_b128 v[224:227], v252 offset:4096\n" \
  "v_mfma_f32_16x16x32_bf16 v[200:203], v[20:23], v[228:231], v[200:203]\n" \
  "v_mfma_f32_16x16x32_bf16 v[204:207], v[84:87], v[228:231], v[204:207]\n" \
  "ds_read_b128 v[228:231], v252 offset:5120\n" \
  "v_mfma_f32_16x16x32_bf16 v[192:195], v[240:243], v[232:235], v[192:195]\n" \
  "v_mfma_f32_16x16x32_bf16 v[196:199], v[244:247], v[232:235], v[196:199]\n" \
  "ds_read_b128 v[232:235], v252 offset:6144\n" \
  "v_mfma_f32_16x16x32_bf16 v[200:203], v[240:243], v[236:239], v[200:203]\n" \
  "v_mfma_f32_16x16x32_bf16 v[204:207], v[244:247], v[236:239], v[204:207]\n" \
  "ds_read_b128 v[236:239], v252 offset:7168\n" \
  "ds_read_b128 v[240:243], v250 offset:49152\n" \
  "ds_read_b128 v[244:247], v250 offset:50176\n" \
  "s_waitcnt lgkmcnt(9)\n" \
  "v_mfma_f32_16x16x32_bf16 v[192:195], v[24:27], v[208:211], v[192:195]\n" \
  "v_mfma_f32_16x16x32_bf16 v[196:199], v[88:91], v[208:211], v[196:199]\n" \
  "s_waitcnt lgkmcnt(8)\n" \
  "v_mfma_f32_16x16x32_bf16 v[200:203], v[24:27], v[212:215], v[200:203]\n" \
  "v_mfma_f32_16x16x32_bf16 v[204:207], v[88:91], v[212:215], v[204:207]\n" \
  "s_waitcnt vmcnt(1)\n" \
  "s_waitcnt lgkmcnt(0)\n" \
  "s_barrier\n" \
  "ds_read_b128 v[208:211], v252 offset:12288\n" \
  "ds_read_b128 v[212:215], v252 offset:13312\n" \
  "s_add_u32 m0, %[mw], 0\n" \
  "s_xor_b32 %[cnt], %[sc], 704\n" \
  "buffer_load_dwordx4 v248, %[rx], %[cnt] offen lds\n" \
  "s_add_u32 m0, %[mw], 4096\n" \
  "s_xor_b32 %[cnt], %[sc], 768\n" \
  "buffer_load_dwordx4 v248, %[rx], %[cnt] offen lds\n" \
  "s_add_u32 m0, %[mw], 8192\n" \
  "s_xor_b32 %[cnt], %[sc], 832\n" \
  "buffer_load_dwordx4 v248, %[rx], %[cnt] offen lds\n" \
  "v_lshl_or_b32 v253, v251, 3, %[tm]\n" \
  "buffer_load_dword v253, v253, %[rx], %[tc] offen\n" \
  "v_mfma_f32_16x16x32_bf16 v[192:195], v[152:155], v[216:219], v[192:195]\n" \
  "v_mfma_f32_16x16x32_bf16 v[196:199], v[156:159], v[216:219], v[196:199]\n" \
  "ds_read_b128 v[216:219], v252 offset:14336\n" \
  "v_mfma_f32_16x16x32_bf16 v[200:203], v[152:155], v[220:223], v[200:203]\n" \
  "v_mfma_f32_16x16x32_bf16 v[204:207], v[156:159], v[220:223], v[204:207]\n" \
  "ds_read_b128 v[220:223], v252 offset:15360\n" \
  "v_mfma_f32_16x16x32_bf16 v[192:195], v[28:31], v[224:227], v[192:195]\n" \
  "v_mfma_f32_16x16x32_bf16 v[196:199], v[92:95], v[224:227], v[196:199]\n" \
  "ds_read_b128 v[224:227], v252 offset:16384\n" \
  "v_mfma_f32_16x16x32_bf16 v[200:203], v[28:31], v[228:231], v[200:203]\n" \
  "v_mfma_f32_16x16x32_bf16 v[204:207], v[92:95], v[228:231], v[204:207]\n" \
  "ds_read_b128 v[228:231], v252 offset:17408\n" \
  "v_mfma_f32_16x16x32_bf16 v[192:195], v[240:243], v[232:235], v[192:195]\n" \
  "v_mfma_f32_16x16x32_bf16 v[196:199], v[244:247], v[232:235], v[196:199]\n" \
  "ds_read_b128 v[232:235], v252 offset:18432\n" \
  "v_mfma_f32_16x16x32_bf16 v[200:203], v[240:243], v[236:239], v[200:203]\n" \
  "v_mfma_f32_16x16x32_bf16 v[204:207], v[244:247], v[236:239], v[204:207]\n" \
  "ds_read_b128 v[236:239], v252 offset:19456\n" \
  "ds_read_b128 v[240:243], v249 offset:0\n" \
  "ds_read_b128 v[244:247], v249 offset:1024\n" \
  "s_waitcnt lgkmcnt(9)\n" \
  "v_mfma_f32_16x16x32_bf16 v[192:195], v[32:35], v[208:211], v[192:195]\n" \
  "v_mfma_f32_16x16x32_bf16 v[196:199], v[96:99], v[208:211], v[196:199]\n" \
  "ds_read_b128 v[208:211], v252 offset:20480\n" \
  "s_waitcnt lgkmcnt(9)\n" \
  "v_mfma_f32_16x16x32_bf16 v[200:203], v[32:35], v[212:215], v[200:203]\n" \
  "v_mfma_f32_16x16x32_bf16 v[204:207], v[96:99], v[212:215], v[204:207]\n" \
  "ds_read_b128 v[212:215], v252 offset:21504\n" \
  "s_waitcnt lgkmcnt(9)\n" \
  "v_mfma_f32_16x16x32_bf16 v[192:195], v[160:163], v[216:219], v[192:195]\n" \
  "v_mfma_f32_16x16x32_bf16 v[196:199], v[164:167], v[216:219], v[196:199]\n" \
  "ds_read_b128 v[216:219], v252 offset:22528\n" \
  "s_waitcnt lgkmcnt(9)\n" \
  "v_mfma_f32_16x16x32_bf16 v[200:203], v[160:163], v[220:223], v[200:203]\n" \
  "v_mfma_f32_16x16x32_bf16 v[204:207], v[164:167], v[220:223], v[204:207]\n" \
  "ds_read_b128 v[220:223], v252 offset:23552\n" \
  "s_waitcnt lgkmcnt(9)\n" \
  "v_mfma_f32_16x16x32_bf16 v[192:195], v[36:39], v[224:227], v[192:195]\n" \
  "v_mfma_f32_16x16x32_bf16 v[196:199], v[100:103], v[224:227], v[196:199]\n" \
  "s_waitcnt lgkmcnt(8)\n" \
  "v_mfma_f32_16x16x32_bf16 v[200:203], v[36:39], v[228:231], v[200:203]\n" \
  "v_mfma_f32_16x16x32_bf16 v[204:207], v[100:103], v[228:231], v[204:207]\n" \
  "s_waitcnt vmcnt(1)\n" \
  "s_waitcnt lgkmcnt(0)\n" \
  "s_barrier\n" \
  "ds_read_b128 v[224:227], v252 offset:0\n" \
  "ds_read_b128 v[228:231], v252 offset:1024\n" \
  "s_add_u32 m0, %[mw], 12288\n" \
  "s_xor_b32 %[cnt], %[sc], 896\n" \
  "buffer_load_dwordx4 v248, %[rx], %[cnt] offen lds\n" \
  "s_add_u32 m0, %[mw], 16384\n" \
  "s_xor_b32 %[cnt], %[sc], 960\n" \
  "buffer_load_dwordx4 v248, %[rx], %[cnt] offen lds\n" \
  "v_mfma_f32_16x16x32_bf16 v[192:195], v[240:243], v[232:235], v[192:195]\n" \
  "v_mfma_f32_16x16x32_bf16 v[196:199], v[244:247], v[232:235], v[196:199]\n" \
  "ds_read_b128 v[232:235], v252 offset:2048\n" \
  "v_mfma_f32_16x16x32_bf16 v[200:203], v[240:243], v[236:239], v[200:203]\n" \
  "v_mfma_f32_16x16x32_bf16 v[204:207], v[244:247], v[236:239], v[204:207]\n" \
  "ds_read_b128 v[236:239], v252 offset:3072\n" \
  "ds_read_b128 v[240:243], v249 offset:16384\n" \
  "ds_read_b128 v[244:247], v249 offset:17408\n" \
  "v_mfma_f32_16x16x32_bf16 v[192:195], v[40:43], v[208:211], v[192:195]\n" \
  "v_mfma_f32_16x16x32_bf16 v[196:199], v[104:107], v[208:211], v[196:199]\n" \
  "ds_read_b128 v[208:211], v252 offset:4096\n" \
  "v_mfma_f32_16x16x32_bf16 v[200:203], v[40:43], v[212:215], v[200:203]\n" \
  "v_mfma_f32_16x16x32_bf16 v[204:207], v[104:107], v[212:215], v[204:207]\n" \
  "ds_read_b128 v[212:215], v252 offset:5120\n" \
  "v_mfma_f32_16x16x32_bf16 v[192:195], v[168:171], v[216:219], v[192:195]\n" \
  "v_mfma_f32_16x16x32_bf16 v[196:199], v[172:175], v[216:219], v[196:199]\n" \
  "ds_read_b128 v[216:219], v252 offset:6144\n" \
  "v_mfma_f32_16x16x32_bf16 v[200:203], v[168:171], v[220:223], v[200:203]\n" \
  "v_mfma_f32_16x16x32_bf16 v[204:207], v[172:175], v[220:223], v[204:207]\n" \
  "ds_read_b128 v[220:223], v252 offset:7168\n" \
  "s_waitcnt lgkmcnt(9)\n" \
  "v_mfma_f32_16x16x32_bf16 v[192:195], v[44:47], v[224:227], v[192:195]\n" \
  "v_mfma_f32_16x16x32_bf16 v[196:199], v[108:111], v[224:227], v[196:199]\n" \
  "ds_read_b128 v[224:227], v252 offset:8192\n" \
  "s_waitcnt lgkmcnt(9)\n" \
  "v_mfma_f32_16x16x32_bf16 v[200:203], v[44:47], v[228:231], v[200:203]\n" \
  "v_mfma_f32_16x16x32_bf16 v[204:207], v[108:111], v[228:231], v[204:207]\n" \
  "ds_read_b128 v[228:231], v252 offset:9216\n" \
  "s_waitcnt lgkmcnt(7)\n" \
  "v_mfma_f32_16x16x32_bf16 v[192:195], v[240:243], v[232:235], v[192:195]\n" \
  "s_waitcnt lgkmcnt(6)\n" \
  "v_mfma_f32_16x16x32_bf16 v[196:199], v[244:247], v[232:235], v[196:199]\n" \
  "ds_read_b128 v[232:235], v252 offset:10240\n" \
  "v_mfma_f32_16x16x32_bf16 v[200:203], v[240:243], v[236:239], v[200:203]\n" \
  "v_mfma_f32_16x16x32_bf16 v[204:207], v[244:247], v[236:239], v[204:207]\n" \
  "ds_read_b128 v[236:239], v252 offset:11264\n" \
  "ds_read_b128 v[240:243], v249 offset:32768\n" \
  "ds_read_b128 v[244:247], v249 offset:33792\n" \
  "s_waitcnt lgkmcnt(9)\n" \
  "v_mfma_f32_16x16x32_bf16 v[192:195], v[48:51], v[208:211], v[192:195]\n" \
  "v_mfma_f32_16x16x32_bf16 v[196:199], v[112:115], v[208:211], v[196:199]\n" \
  "s_waitcnt lgkmcnt(8)\n" \
  "v_mfma_f32_16x16x32_bf16 v[200:203], v[48:51], v[212:215], v[200:203]\n" \
  "v_mfma_f32_16x16x32_bf16 v[204:207], v[112:115], v[212:215], v[204:207]\n" \
  "s_waitcnt vmcnt(0)\n" \
  "s_waitcnt lgkmcnt(0)\n" \
  "s_barrier\n" \
  "ds_read_b128 v[208:211], v252 offset:12288\n" \
  "ds_read_b128 v[212:215], v252 offset:13312\n" \
  "s_add_u32 m0, %[mw], 0\n" \
  "s_xor_b32 %[cnt], %[sn], 0\n" \
  "buffer_load_dwordx4 v248, %[rx], %[cnt] offen lds\n" \
  "s_add_u32 m0, %[mw], 4096\n" \
  "s_xor_b32 %[cnt], %[sn], 64\n" \
  "buffer_load_dwordx4 v248, %[rx], %[cnt] offen lds\n" \
  "s_add_u32 m0, %[mw], 8192\n" \
  "s_xor_b32 %[cnt], %[sn], 128\n" \
  "buffer_load_dwordx4 v248, %[rx], %[cnt] offen lds\n" \
  "v_mfma_f32_16x16x32_bf16 v[192:195], v[176:179], v[216:219], v[192:195]\n" \
  "v_mfma_f32_16x16x32_bf16 v[196:199], v[180:183], v[216:219], v[196:199]\n" \
  "ds_read_b128 v[216:219], v252 offset:14336\n" \
  "v_mfma_f32_16x16x32_bf16 v[200:203], v[176:179], v[220:223], v[200:203]\n" \
  "v_mfma_f32_16x16x32_bf16 v[204:207], v[180:183], v[220:223], v[204:207]\n" \
  "ds_read_b128 v[220:223], v252 offset:15360\n" \
  "v_mfma_f32_16x16x32_bf16 v[192:195], v[52:55], v[224:227], v[192:195]\n" \
  "v_mfma_f32_16x16x32_bf16 v[196:199], v[116:119], v[224:227], v[196:199]\n" \
  "ds_read_b128 v[224:227], v252 offset:16384\n" \
  "v_mfma_f32_16x16x32_bf16 v[200:203], v[52:55], v[228:231], v[200:203]\n" \
  "v_mfma_f32_16x16x32_bf16 v[204:207], v[116:119], v[228:231], v[204:207]\n" \
  "ds_read_b128 v[228:231], v252 offset:17408\n" \
  "v_mfma_f32_16x16x32_bf16 v[192:195], v[240:243], v[232:235], v[192:195]\n" \
  "v_mfma_f32_16x16x32_bf16 v[196:199], v[244:247], v[232:235], v[196:199]\n" \
  "ds_read_b128 v[232:235], v252 offset:18432\n" \
  "v_mfma_f32_16x16x32_bf16 v[200:203], v[240:243], v[236:239], v[200:203]\n" \
  "v_mfma_f32_16x16x32_bf16 v[204:207], v[244:247], v[236:239], v[204:207]\n" \
  "ds_read_b128 v[236:239], v252 offset:19456\n" \
  "ds_read_b128 v[240:243], v249 offset:49152\n" \
  "ds_read_b128 v[244:247], v249 offset:50176\n" \
  "s_waitcnt lgkmcnt(9)\n" \
  "v_mfma_f32_16x16x32_bf16 v[192:195], v[56:59], v[208:211], v[192:195]\n" \
  "v_mfma_f32_16x16x32_bf16 v[196:199], v[120:123], v[208:211], v[196:199]\n" \
  "s_waitcnt lgkmcnt(8)\n" \
  "v_mfma_f32_16x16x32_bf16 v[200:203], v[56:59], v[212:215], v[200:203]\n" \
  "v_mfma_f32_16x16x32_bf16 v[204:207], v[120:123], v[212:215], v[204:207]\n" \
  "s_waitcnt vmcnt(0)\n" \
  "s_waitcnt lgkmcnt(0)\n" \
  "s_barrier\n" \
  "ds_read_b128 v[208:211], v252 offset:0\n" \
  "ds_read_b128 v[212:215], v252 offset:1024\n" \
  "s_add_u32 m0, %[mw], 12288\n" \
  "s_xor_b32 %[cnt], %[sn], 192\n" \
  "buffer_load_dwordx4 v248, %[rx], %[cnt] offen lds\n" \
  "s_add_u32 m0, %[mw], 16384\n" \
  "s_xor_b32 %[cnt], %[sn], 256\n" \
  "buffer_load_dwordx4 v248, %[rx], %[cnt] offen lds\n" \
  "s_add_u32 m0, %[mw], 20480\n" \
  "s_xor_b32 %[cnt], %[sn], 320\n" \
  "buffer_load_dwordx4 v248, %[rx], %[cnt] offen lds\n" \
  "v_mfma_f32_16x16x32_bf16 v[192:195], v[184:187], v[216:219], v[192:195]\n" \
  "v_mfma_f32_16x16x32_bf16 v[196:199], v[188:191], v[216:219], v[196:199]\n" \
  "ds_read_b128 v[216:219], v252 offset:2048\n" \
  "v_mfma_f32_16x16x32_bf16 v[200:203], v[184:187], v[220:223], v[200:203]\n" \
  "v_mfma_f32_16x16x32_bf16 v[204:207], v[188:191], v[220:223], v[204:207]\n" \
  "ds_read_b128 v[220:223], v252 offset:3072\n" \
  "v_mfma_f32_16x16x32_bf16 v[192:195], v[60:63], v[224:227], v[192:195]\n" \
  "v_mfma_f32_16x16x32_bf16 v[196:199], v[124:127], v[224:227], v[196:199]\n" \
  "ds_read_b128 v[224:227], v252 offset:4096\n" \
  "v_mfma_f32_16x16x32_bf16 v[200:203], v[60:63], v[228:231], v[200:203]\n" \
  "v_mfma_f32_16x16x32_bf16 v[204:207], v[124:127], v[228:231], v[204:207]\n" \
  "ds_read_b128 v[228:231], v252 offset:5120\n" \
  "v_mfma_f32_16x16x32_bf16 v[192:195], v[240:243], v[232:235], v[192:195]\n" \
  "v_mfma_f32_16x16x32_bf16 v[196:199], v[244:247], v[232:235], v[196:199]\n" \
  "ds_read_b128 v[232:235], v252 offset:6144\n" \
  "v_mfma_f32_16x16x32_bf16 v[200:203], v[240:243], v[236:239], v[200:203]\n" \
  "v_mfma_f32_16x16x32_bf16 v[204:207], v[244:247], v[236:239], v[204:207]\n" \
  "ds_read_b128 v[236:239], v252 offset:7168\n" \
  "v_cvt_pk_bf16_f32 v192, v192, v193\n" \
  "v_cvt_pk_bf16_f32 v193, v194, v195\n" \
  "v_cvt_pk_bf16_f32 v194, v196, v197\n" \
  "v_cvt_pk_bf16_f32 v195, v198, v199\n" \
  "v_add_u32 v196, %[so], v251\n" \
  "ds_write_b128 v196, v[192:195] offset:24576\n" \
  "v_cvt_pk_bf16_f32 v200, v200, v201\n" \
  "v_cvt_pk_bf16_f32 v201, v202, v203\n" \
  "v_cvt_pk_bf16_f32 v202, v204, v205\n" \
  "v_cvt_pk_bf16_f32 v203, v206, v207\n" \
  "ds_write_b128 v196, v[200:203] offset:28672\n" \
  ".Lqe_%=:\n" \
  "s_mov_b32 m0, %[keep]\n" \
  "s_waitcnt lgkmcnt(0)\n"
#define ST_XB \
  "s_barrier\n" \
  "ds_read_b128 v[192:195], v251 offset:24576\n" \
  "ds_read_b128 v[208:211], v251 offset:28672\n" \
  "ds_read_b128 v[196:199], v251 offset:25600\n" \
  "ds_read_b128 v[212:215], v251 offset:29696\n" \
  "ds_read_b128 v[200:203], v251 offset:26624\n" \
  "ds_read_b128 v[216:219], v251 offset:30720\n" \
  "ds_read_b128 v[204:207], v251 offset:27648\n" \
  "ds_read_b128 v[220:223], v251 offset:31744\n" \
  "ds_read_b128 v[240:243], v250 offset:0\n" \
  "ds_read_b128 v[244:247], v250 offset:16384\n" \
  "s_waitcnt lgkmcnt(9)\n" \
  "v_mfma_f32_16x16x32_bf16 v[224:227], v[192:195], v[0:3], 0\n" \
  "s_waitcnt lgkmcnt(8)\n" \
  "v_mfma_f32_16x16x32_bf16 v[228:231], v[208:211], v[0:3], 0\n" \
  "s_waitcnt lgkmcnt(7)\n" \
  "v_mfma_f32_16x16x32_bf16 v[224:227], v[196:199], v[4:7], v[224:227]\n" \
  "s_waitcnt lgkmcnt(6)\n" \
  "v_mfma_f32_16x16x32_bf16 v[228:231], v[212:215], v[4:7], v[228:231]\n" \
  "s_waitcnt lgkmcnt(5)\n" \
  "v_mfma_f32_16x16x32_bf16 v[224:227], v[200:203], v[8:11], v[224:227]\n" \
  "s_waitcnt lgkmcnt(4)\n" \
  "v_mfma_f32_16x16x32_bf16 v[228:231], v[216:219], v[8:11], v[228:231]\n" \
  "s_waitcnt lgkmcnt(3)\n" \
  "v_mfma_f32_16x16x32_bf16 v[224:227], v[204:207], v[12:15], v[224:227]\n" \
  "s_waitcnt lgkmcnt(2)\n" \
  "v_mfma_f32_16x16x32_bf16 v[228:231], v[220:223], v[12:15], v[228:231]\n" \
  "v_mfma_f32_16x16x32_bf16 v[232:235], v[192:195], v[128:131], 0\n" \
  "v_mfma_f32_16x16x32_bf16 v[236:239], v[208:211], v[128:131], 0\n" \
  "v_mfma_f32_16x16x32_bf16 v[232:235], v[200:203], v[136:139], v[232:235]\n" \
  "v_cvt_pk_bf16_f32 v224, v224, v228\n" \
  "v_cvt_pk_bf16_f32 v225, v225, v229\n" \
  "v_mfma_f32_16x16x32_bf16 v[236:239], v[216:219], v[136:139], v[236:239]\n" \
  "v_cvt_pk_bf16_f32 v226, v226, v230\n" \
  "v_cvt_pk_bf16_f32 v227, v227, v231\n" \
  "s_waitcnt lgkmcnt(1)\n" \
  "v_mfma_f32_16x16x32_bf16 v[232:235], v[196:199], v[240:243], v[232:235]\n" \
  "s_add_u32 %[cnt], %[so], 0*%c[js]+0*%c[ss]\n" \
  "buffer_store_dwordx4 v[224:227], v251, %[rs], %[cnt] offen sc1\n" \
  "v_mfma_f32_16x16x32_bf16 v[236:239], v[212:215], v[240:243], v[236:239]\n" \
  "s_waitcnt lgkmcnt(0)\n" \
  "v_mfma_f32_16x16x32_bf16 v[232:235], v[204:207], v[244:247], v[232:235]\n" \
  "v_mfma_f32_16x16x32_bf16 v[236:239], v[220:223], v[244:247], v[236:239]\n" \
  "ds_read_b128 v[240:243], v250 offset:1024\n" \
  "ds_read_b128 v[244:247], v250 offset:17408\n" \
  "v_mfma_f32_16x16x32_bf16 v[224:227], v[192:195], v[64:67], 0\n" \
  "v_mfma_f32_16x16x32_bf16 v[228:231], v[208:211], v[64:67], 0\n" \
  "v_mfma_f32_16x16x32_bf16 v[224:227], v[196:199], v[68:71], v[224:227]\n" \
  "v_cvt_pk_bf16_f32 v232, v232, v236\n" \
  "v_cvt_pk_bf16_f32 v233, v233, v237\n" \
  "v_mfma_f32_16x16x32_bf16 v[228:231], v[212:215], v[68:71], v[228:231]\n" \
  "v_cvt_pk_bf16_f32 v234, v234, v238\n" \
  "v_cvt_pk_bf16_f32 v235, v235, v239\n" \
  "v_mfma_f32_16x16x32_bf16 v[224:227], v[200:203], v[72:75], v[224:227]\n" \
  "s_add_u32 %[cnt], %[so], 4*%c[js]+0*%c[ss]\n" \
  "buffer_store_dwordx4 v[232:235], v251, %[rs], %[cnt] offen sc1\n" \
  "v_mfma_f32_16x16x32_bf16 v[228:231], v[216:219], v[72:75], v[228:231]\n" \
  "v_mfma_f32_16x16x32_bf16 v[224:227], v[204:207], v[76:79], v[224:227]\n" \
  "v_mfma_f32_16x16x32_bf16 v[228:231], v[220:223], v[76:79], v[228:231]\n" \
  "s_waitcnt vmcnt(2)\n" \
  "s_barrier\n" \
  "v_mfma_f32_16x16x32_bf16 v[232:235], v[192:195], v[132:135], 0\n" \
  "v_mfma_f32_16x16x32_bf16 v[236:239], v[208:211], v[132:135], 0\n" \
  "v_mfma_f32_16x16x32_bf16 v[232:235], v[200:203], v[140:143], v[232:235]\n" \
  "v_cvt_pk_bf16_f32 v224, v224, v228\n" \
  "v_cvt_pk_bf16_f32 v225, v225, v229\n" \
  "v_mfma_f32_16x16x32_bf16 v[236:239], v[216:219], v[140:143], v[236:239]\n" \
  "v_cvt_pk_bf16_f32 v226, v226, v230\n" \
  "v_cvt_pk_bf16_f32 v227, v227, v231\n" \
  "s_waitcnt lgkmcnt(1)\n" \
  "v_mfma_f32_16x16x32_bf16 v[232:235], v[196:199], v[240:243], v[232:235]\n" \
  "s_add_u32 %[cnt], %[so], 0*%c[js]+1*%c[ss]\n" \
  "buffer_store_dwordx4 v[224:227], v251, %[rs], %[cnt] offen sc1\n" \
  "v_mfma_f32_16x16x32_bf16 v[236:239], v[212:215], v[240:243], v[236:239]\n" \
  "s_waitcnt lgkmcnt(0)\n" \
  "v_mfma_f32_16x16x32_bf16 v[232:235], v[204:207], v[244:247], v[232:235]\n" \
  "v_mfma_f32_16x16x32_bf16 v[236:239], v[220:223], v[244:247], v[236:239]\n" \
  "ds_read_b128 v[240:243], v250 offset:32768\n" \
  "ds_read_b128 v[244:247], v250 offset:49152\n" \
  "v_mfma_f32_16x16x32_bf16 v[224:227], v[192:195], v[16:19], 0\n" \
  "v_mfma_f32_16x16x32_bf16 v[228:231], v[208:211], v[16:19], 0\n" \
  "v_mfma_f32_16x16x32_bf16 v[224:227], v[196:199], v[20:23], v[224:227]\n" \
  "v_cvt_pk_bf16_f32 v232, v232, v236\n" \
  "v_cvt_pk_bf16_f32 v233, v233, v237\n" \
  "v_mfma_f32_16x16x32_bf16 v[228:231], v[212:215], v[20:23], v[228:231]\n" \
  "v_cvt_pk_bf16_f32 v234, v234, v238\n" \
  "v_cvt_pk_bf16_f32 v235, v235, v239\n" \
  "v_mfma_f32_16x16x32_bf16 v[224:227], v[200:203], v[24:27], v[224:227]\n" \
  "s_add_u32 %[cnt], %[so], 4*%c[js]+1*%c[ss]\n" \
  "buffer_store_dwordx4 v[232:235], v251, %[rs], %[cnt] offen sc1\n" \
  "v_mfma_f32_16x16x32_bf16 v[228:231], v[216:219], v[24:27], v[228:231]\n" \
  "v_mfma_f32_16x16x32_bf16 v[224:227], v[204:207], v[28:31], v[224:227]\n" \
  "v_mfma_f32_16x16x32_bf16 v[228:231], v[220:223], v[28:31], v[228:231]\n" \
  "s_barrier\n" \
  "v_mfma_f32_16x16x32_bf16 v[232:235], v[192:195], v[144:147], 0\n" \
  "v_mfma_f32_16x16x32_bf16 v[236:239], v[208:211], v[144:147], 0\n" \
  "v_mfma_f32_16x16x32_bf16 v[232:235], v[200:203], v[152:155], v[232:235]\n" \
  "v_cvt_pk_bf16_f32 v224, v224, v228\n" \
  "v_cvt_pk_bf16_f32 v225, v225, v229\n" \
  "v_mfma_f32_16x16x32_bf16 v[236:239], v[216:219], v[152:155], v[236:239]\n" \
  "v_cvt_pk_bf16_f32 v226, v226, v230\n" \
  "v_cvt_pk_bf16_f32 v227, v227, v231\n" \
  "s_waitcnt lgkmcnt(1)\n" \
  "v_mfma_f32_16x16x32_bf16 v[232:235], v[196:199], v[240:243], v[232:235]\n" \
  "s_add_u32 %[cnt], %[so2], 1*%c[js]+0*%c[ss]\n" \
  "buffer_store_dwordx4 v[224:227], v251, %[rs], %[cnt] offen sc1\n" \
  "v_mfma_f32_16x16x32_bf16 v[236:239], v[212:215], v[240:243], v[236:239]\n" \
  "s_waitcnt lgkmcnt(0)\n" \
  "v_mfma_f32_16x16x32_bf16 v[232:235], v[204:207], v[244:247], v[232:235]\n" \
  "v_mfma_f32_16x16x32_bf16 v[236:239], v[220:223], v[244:247], v[236:239]\n" \
  "ds_read_b128 v[240:243], v250 offset:33792\n" \
  "ds_read_b128 v[244:247], v250 offset:50176\n" \
  "v_mfma_f32_16x16x32_bf16 v[224:227], v[192:195], v[80:83], 0\n" \
  "v_mfma_f32_16x16x32_bf16 v[228:231], v[208:211], v[80:83], 0\n" \
  "v_mfma_f32_16x16x32_bf16 v[224:227], v[196:199], v[84:87], v[224:227]\n" \
  "v_cvt_pk_bf16_f32 v232, v232, v236\n" \
  "v_cvt_pk_bf16_f32 v233, v233, v237\n" \
  "v_mfma_f32_16x16x32_bf16 v[228:231], v[212:215], v[84:87], v[228:231]\n" \
  "v_cvt_pk_bf16_f32 v234, v234, v238\n" \
  "v_cvt_pk_bf16_f32 v235, v235, v239\n" \
  "v_mfma_f32_16x16x32_bf16 v[224:227], v[200:203], v[88:91], v[224:227]\n" \
  "s_add_u32 %[cnt], %[so2], 5*%c[js]+0*%c[ss]\n" \
  "buffer_store_dwordx4 v[232:235], v251, %[rs], %[cnt] offen sc1\n" \
  "v_mfma_f32_16x16x32_bf16 v[228:231], v[216:219], v[88:91], v[228:231]\n" \
  "v_mfma_f32_16x16x32_bf16 v[224:227], v[204:207], v[92:95], v[224:227]\n" \
  "v_mfma_f32_16x16x32_bf16 v[228:231], v[220:223], v[92:95], v[228:231]\n" \
  "v_mfma_f32_16x16x32_bf16 v[232:235], v[192:195], v[148:151], 0\n" \
  "v_mfma_f32_16x16x32_bf16 v[236:239], v[208:211], v[148:151], 0\n" \
  "v_mfma_f32_16x16x32_bf16 v[232:235], v[200:203], v[156:159], v[232:235]\n" \
  "v_cvt_pk_bf16_f32 v224, v224, v228\n" \
  "v_cvt_pk_bf16_f32 v225, v225, v229\n" \
  "v_mfma_f32_16x16x32_bf16 v[236:239], v[216:219], v[156:159], v[236:239]\n" \
  "v_cvt_pk_bf16_f32 v226, v226, v230\n" \
  "v_cvt_pk_bf16_f32 v227, v227, v231\n" \
  "s_waitcnt lgkmcnt(1)\n" \
  "v_mfma_f32_16x16x32_bf16 v[232:235], v[196:199], v[240:243], v[232:235]\n" \
  "s_add_u32 %[cnt], %[so2], 1*%c[js]+1*%c[ss]\n" \
  "buffer_store_dwordx4 v[224:227], v251, %[rs], %[cnt] offen sc1\n" \
  "v_mfma_f32_16x16x32_bf16 v[236:239], v[212:215], v[240:243], v[236:239]\n" \
  "s_waitcnt lgkmcnt(0)\n" \
  "v_mfma_f32_16x16x32_bf16 v[232:235], v[204:207], v[244:247], v[232:235]\n" \
  "v_mfma_f32_16x16x32_bf16 v[236:239], v[220:223], v[244:247], v[236:239]\n" \
  "ds_read_b128 v[240:243], v249 offset:0\n" \
  "ds_read_b128 v[244:247], v249 offset:16384\n" \
  "s_barrier\n" \
  "v_mfma_f32_16x16x32_bf16 v[224:227], v[192:195], v[32:35], 0\n" \
  "v_mfma_f32_16x16x32_bf16 v[228:231], v[208:211], v[32:35], 0\n" \
  "v_mfma_f32_16x16x32_bf16 v[224:227], v[196:199], v[36:39], v[224:227]\n" \
  "v_cvt_pk_bf16_f32 v232, v232, v236\n" \
  "v_cvt_pk_bf16_f32 v233, v233, v237\n" \
  "v_mfma_f32_16x16x32_bf16 v[228:231], v[212:215], v[36:39], v[228:231]\n" \
  "v_cvt_pk_bf16_f32 v234, v234, v238\n" \
  "v_cvt_pk_bf16_f32 v235, v235, v239\n" \
  "v_mfma_f32_16x16x32_bf16 v[224:227], v[200:203], v[40:43], v[224:227]\n" \
  "s_add_u32 %[cnt], %[so2], 5*%c[js]+1*%c[ss]\n" \
  "buffer_store_dwordx4 v[232:235], v251, %[rs], %[cnt] offen sc1\n" \
  "v_mfma_f32_16x16x32_bf16 v[228:231], v[216:219], v[40:43], v[228:231]\n" \
  "v_mfma_f32_16x16x32_bf16 v[224:227], v[204:207], v[44:47], v[224:227]\n" \
  "v_mfma_f32_16x16x32_bf16 v[228:231], v[220:223], v[44:47], v[228:231]\n" \
  "v_mfma_f32_16x16x32_bf16 v[232:235], v[192:195], v[160:163], 0\n" \
  "v_mfma_f32_16x16x32_bf16 v[236:239], v[208:211], v[160:163], 0\n" \
  "v_mfma_f32_16x16x32_bf16 v[232:235], v[200:203], v[168:171], v[232:235]\n" \
  "v_cvt_pk_bf16_f32 v224, v224, v228\n" \
  "v_cvt_pk_bf16_f32 v225, v225, v229\n" \
  "v_mfma_f32_16x16x32_bf16 v[236:239], v[216:219], v[168:171], v[236:239]\n" \
  "v_cvt_pk_bf16_f32 v226, v226, v230\n" \
  "v_cvt_pk_bf16_f32 v227, v227, v231\n" \
  "s_waitcnt lgkmcnt(1)\n" \
  "v_mfma_f32_16x16x32_bf16 v[232:235], v[196:199], v[240:243], v[232:235]\n" \
  "s_add_u32 %[cnt], %[so], 2*%c[js]+0*%c[ss]\n" \
  "buffer_store_dwordx4 v[224:227], v251, %[rs], %[cnt] offen sc1\n" \
  "v_mfma_f32_16x16x32_bf16 v[236:239], v[212:215], v[240:243], v[236:239]\n" \
  "s_waitcnt lgkmcnt(0)\n" \
  "v_mfma_f32_16x16x32_bf16 v[232:235], v[204:207], v[244:247], v[232:235]\n" \
  "v_mfma_f32_16x16x32_bf16 v[236:239], v[220:223], v[244:247], v[236:239]\n" \
  "ds_read_b128 v[240:243], v249 offset:1024\n" \
  "ds_read_b128 v[244:247], v249 offset:17408\n" \
  "v_mfma_f32_16x16x32_bf16 v[224:227], v[192:195], v[96:99], 0\n" \
  "v_mfma_f32_16x16x32_bf16 v[228:231], v[208:211], v[96:99], 0\n" \
  "v_mfma_f32_16x16x32_bf16 v[224:227], v[196:199], v[100:103], v[224:227]\n" \
  "v_cvt_pk_bf16_f32 v232, v232, v236\n" \
  "v_cvt_pk_bf16_f32 v233, v233, v237\n" \
  "v_mfma_f32_16x16x32_bf16 v[228:231], v[212:215], v[100:103], v[228:231]\n" \
  "v_cvt_pk_bf16_f32 v234, v234, v238\n" \
  "v_cvt_pk_bf16_f32 v235, v235, v239\n" \
  "v_mfma_f32_16x16x32_bf16 v[224:227], v[200:203], v[104:107], v[224:227]\n" \
  "s_add_u32 %[cnt], %[so], 6*%c[js]+0*%c[ss]\n" \
  "buffer_store_dwordx4 v[232:235], v251, %[rs], %[cnt] offen sc1\n" \
  "v_mfma_f32_16x16x32_bf16 v[228:231], v[216:219], v[104:107], v[228:231]\n" \
  "v_mfma_f32_16x16x32_bf16 v[224:227], v[204:207], v[108:111], v[224:227]\n" \
  "v_mfma_f32_16x16x32_bf16 v[228:231], v[220:223], v[108:111], v[228:231]\n" \
  "s_barrier\n" \
  "v_mfma_f32_16x16x32_bf16 v[232:235], v[192:195], v[164:167], 0\n" \
  "v_mfma_f32_16x16x32_bf16 v[236:239], v[208:211], v[164:167], 0\n" \
  "v_mfma_f32_16x16x32_bf16 v[232:235], v[200:203], v[172:175], v[232:235]\n" \
  "v_cvt_pk_bf16_f32 v224, v224, v228\n" \
  "v_cvt_pk_bf16_f32 v225, v225, v229\n" \
  "v_mfma_f32_16x16x32_bf16 v[236:239], v[216:219], v[172:175], v[236:239]\n" \
  "v_cvt_pk_bf16_f32 v226, v226, v230\n" \
  "v_cvt_pk_bf16_f32 v227, v227, v231\n" \
  "s_waitcnt lgkmcnt(1)\n" \
  "v_mfma_f32_16x16x32_bf16 v[232:235], v[196:199], v[240:243], v[232:235]\n" \
  "s_add_u32 %[cnt], %[so], 2*%c[js]+1*%c[ss]\n" \
  "buffer_store_dwordx4 v[224:227], v251, %[rs], %[cnt] offen sc1\n" \
  "v_mfma_f32_16x16x32_bf16 v[236:239], v[212:215], v[240:243], v[236:239]\n" \
  "s_waitcnt lgkmcnt(0)\n" \
  "v_mfma_f32_16x16x32_bf16 v[232:235], v[204:207], v[244:247], v[232:235]\n" \
  "v_mfma_f32_16x16x32_bf16 v[236:239], v[220:223], v[244:247], v[236:239]\n" \
  "ds_read_b128 v[240:243], v249 offset:32768\n" \
  "ds_read_b128 v[244:247], v249 offset:49152\n" \
  "v_mfma_f32_16x16x32_bf16 v[224:227], v[192:195], v[48:51], 0\n" \
  "v_mfma_f32_16x16x32_bf16 v[228:231], v[208:211], v[48:51], 0\n" \
  "v_mfma_f32_16x16x32_bf16 v[224:227], v[196:199], v[52:55], v[224:227]\n" \
  "v_cvt_pk_bf16_f32 v232, v232, v236\n" \
  "v_cvt_pk_bf16_f32 v233, v233, v237\n" \
  "v_mfma_f32_16x16x32_bf16 v[228:231], v[212:215], v[52:55], v[228:231]\n" \
  "v_cvt_pk_bf16_f32 v234, v234, v238\n" \
  "v_cvt_pk_bf16_f32 v235, v235, v239\n" \
  "v_mfma_f32_16x16x32_bf16 v[224:227], v[200:203], v[56:59], v[224:227]\n" \
  "s_add_u32 %[cnt], %[so], 6*%c[js]+1*%c[ss]\n" \
  "buffer_store_dwordx4 v[232:235], v251, %[rs], %[cnt] offen sc1\n" \
  "v_mfma_f32_16x16x32_bf16 v[228:231], v[216:219], v[56:59], v[228:231]\n" \
  "v_mfma_f32_16x16x32_bf16 v[224:227], v[204:207], v[60:63], v[224:227]\n" \
  "v_mfma_f32_16x16x32_bf16 v[228:231], v[220:223], v[60:63], v[228:231]\n" \
  "s_barrier\n" \
  "v_mfma_f32_16x16x32_bf16 v[232:235], v[192:195], v[176:179], 0\n" \
  "v_mfma_f32_16x16x32_bf16 v[236:239], v[208:211], v[176:179], 0\n" \
  "v_mfma_f32_16x16x32_bf16 v[232:235], v[200:203], v[184:187], v[232:235]\n" \
  "v_cvt_pk_bf16_f32 v224, v224, v228\n" \
  "v_cvt_pk_bf16_f32 v225, v225, v229\n" \
  "v_mfma_f32_16x16x32_bf16 v[236:239], v[216:219], v[184:187], v[236:239]\n" \
  "v_cvt_pk_bf16_f32 v226, v226, v230\n" \
  "v_cvt_pk_bf16_f32 v227, v227, v231\n" \
  "s_waitcnt lgkmcnt(1)\n" \
  "v_mfma_f32_16x16x32_bf16 v[232:235], v[196:199], v[240:243], v[232:235]\n" \
  "s_add_u32 %[cnt], %[so2], 3*%c[js]+0*%c[ss]\n" \
  "buffer_store_dwordx4 v[224:227], v251, %[rs], %[cnt] offen sc1\n" \
  "v_mfma_f32_16x16x32_bf16 v[236:239], v[212:215], v[240:243], v[236:239]\n" \
  "s_waitcnt lgkmcnt(0)\n" \
  "v_mfma_f32_16x16x32_bf16 v[232:235], v[204:207], v[244:247], v[232:235]\n" \
  "v_mfma_f32_16x16x32_bf16 v[236:239], v[220:223], v[244:247], v[236:239]\n" \
  "ds_read_b128 v[240:243], v249 offset:33792\n" \
  "ds_read_b128 v[244:247], v249 offset:50176\n" \
  "v_mfma_f32_16x16x32_bf16 v[224:227], v[192:195], v[112:115], 0\n" \
  "v_mfma_f32_16x16x32_bf16 v[228:231], v[208:211], v[112:115], 0\n" \
  "v_mfma_f32_16x16x32_bf16 v[224:227], v[196:199], v[116:119], v[224:227]\n" \
  "v_cvt_pk_bf16_f32 v232, v232, v236\n" \
  "v_cvt_pk_bf16_f32 v233, v233, v237\n" \
  "v_mfma_f32_16x16x32_bf16 v[228:231], v[212:215], v[116:119], v[228:231]\n" \
  "v_cvt_pk_bf16_f32 v234, v234, v238\n" \
  "v_cvt_pk_bf16_f32 v235, v235, v239\n" \
  "v_mfma_f32_16x16x32_bf16 v[224:227], v[200:203], v[120:123], v[224:227]\n" \
  "s_add_u32 %[cnt], %[so2], 7*%c[js]+0*%c[ss]\n" \
  "buffer_store_dwordx4 v[232:235], v251, %[rs], %[cnt] offen sc1\n" \
  "v_mfma_f32_16x16x32_bf16 v[228:231], v[216:219], v[120:123], v[228:231]\n" \
  "v_mfma_f32_16x16x32_bf16 v[224:227], v[204:207], v[124:127], v[224:227]\n" \
  "v_mfma_f32_16x16x32_bf16 v[228:231], v[220:223], v[124:127], v[228:231]\n" \
  "v_mfma_f32_16x16x32_bf16 v[232:235], v[192:195], v[180:183], 0\n" \
  "v_mfma_f32_16x16x32_bf16 v[236:239], v[208:211], v[180:183], 0\n" \
  "v_mfma_f32_16x16x32_bf16 v[232:235], v[200:203], v[188:191], v[232:235]\n" \
  "v_cvt_pk_bf16_f32 v224, v224, v228\n" \
  "v_cvt_pk_bf16_f32 v225, v225, v229\n" \
  "v_mfma_f32_16x16x32_bf16 v[236:239], v[216:219], v[188:191], v[236:239]\n" \
  "v_cvt_pk_bf16_f32 v226, v226, v230\n" \
  "v_cvt_pk_bf16_f32 v227, v227, v231\n" \
  "s_waitcnt lgkmcnt(1)\n" \
  "v_mfma_f32_16x16x32_bf16 v[232:235], v[196:199], v[240:243], v[232:235]\n" \
  "s_add_u32 %[cnt], %[so2], 3*%c[js]+1*%c[ss]\n" \
  "buffer_store_dwordx4 v[224:227], v251, %[rs], %[cnt] offen sc1\n" \
  "v_mfma_f32_16x16x32_bf16 v[236:239], v[212:215], v[240:243], v[236:239]\n" \
  "s_waitcnt lgkmcnt(0)\n" \
  "v_mfma_f32_16x16x32_bf16 v[232:235], v[204:207], v[244:247], v[232:235]\n" \
  "v_mfma_f32_16x16x32_bf16 v[236:239], v[220:223], v[244:247], v[236:239]\n" \
  "s_nop 8\n" \
  "v_cvt_pk_bf16_f32 v232, v232, v236\n" \
  "v_cvt_pk_bf16_f32 v233, v233, v237\n" \
  "v_cvt_pk_bf16_f32 v234, v234, v238\n" \
  "v_cvt_pk_bf16_f32 v235, v235, v239\n" \
  "s_add_u32 %[cnt], %[so2], 7*%c[js]+1*%c[ss]\n" \
  "buffer_store_dwordx4 v[232:235], v251, %[rs], %[cnt] offen sc1\n" \
  "s_nop 1\n"
#define ST_XB0 \
  "s_setprio 1\n" \
  "ds_read_b128 v[192:195], v251 offset:24576\n" \
  "ds_read_b128 v[208:211], v251 offset:28672\n" \
  "ds_read_b128 v[196:199], v251 offset:25600\n" \
  "ds_read_b128 v[212:215], v251 offset:29696\n" \
  "ds_read_b128 v[200:203], v251 offset:26624\n" \
  "ds_read_b128 v[216:219], v251 offset:30720\n" \
  "ds_read_b128 v[204:207], v251 offset:27648\n" \
  "ds_read_b128 v[220:223], v251 offset:31744\n" \
  "ds_read_b128 v[240:243], v250 offset:0\n" \
  "ds_read_b128 v[244:247], v250 offset:16384\n" \
  "s_waitcnt lgkmcnt(9)\n" \
  "v_mfma_f32_16x16x32_bf16 v[224:227], v[192:195], v[0:3], 0\n" \
  "s_waitcnt lgkmcnt(8)\n" \
  "v_mfma_f32_16x16x32_bf16 v[228:231], v[208:211], v[0:3], 0\n" \
  "s_waitcnt lgkmcnt(7)\n" \
  "v_mfma_f32_16x16x32_bf16 v[224:227], v[196:199], v[4:7], v[224:227]\n" \
  "s_waitcnt lgkmcnt(6)\n" \
  "v_mfma_f32_16x16x32_bf16 v[228:231], v[212:215], v[4:7], v[228:231]\n" \
  "s_waitcnt lgkmcnt(5)\n" \
  "v_mfma_f32_16x16x32_bf16 v[224:227], v[200:203], v[8:11], v[224:227]\n" \
  "s_waitcnt lgkmcnt(4)\n" \
  "v_mfma_f32_16x16x32_bf16 v[228:231], v[216:219], v[8:11], v[228:231]\n" \
  "s_waitcnt lgkmcnt(3)\n" \
  "v_mfma_f32_16x16x32_bf16 v[224:227], v[204:207], v[12:15], v[224:227]\n" \
  "s_waitcnt lgkmcnt(2)\n" \
  "v_mfma_f32_16x16x32_bf16 v[228:231], v[220:223], v[12:15], v[228:231]\n" \
  "v_mfma_f32_16x16x32_bf16 v[232:235], v[192:195], v[128:131], 0\n" \
  "v_mfma_f32_16x16x32_bf16 v[236:239], v[208:211], v[128:131], 0\n" \
  "v_mfma_f32_16x16x32_bf16 v[232:235], v[200:203], v[136:139], v[232:235]\n" \
  "v_cvt_pk_bf16_f32 v224, v224, v228\n" \
  "v_cvt_pk_bf16_f32 v225, v225, v229\n" \
  "v_mfma_f32_16x16x32_bf16 v[236:239], v[216:219], v[136:139], v[236:239]\n" \
  "v_cvt_pk_bf16_f32 v226, v226, v230\n" \
  "v_cvt_pk_bf16_f32 v227, v227, v231\n" \
  "s_waitcnt lgkmcnt(1)\n" \
  "v_mfma_f32_16x16x32_bf16 v[232:235], v[196:199], v[240:243], v[232:235]\n" \
  "s_add_u32 %[cnt], %[so], 0*%c[js]+0*%c[ss]\n" \
  "buffer_store_dwordx4 v[224:227], v251, %[rs], %[cnt] offen sc1\n" \
  "v_mfma_f32_16x16x32_bf16 v[236:239], v[212:215], v[240:243], v[236:239]\n" \
  "s_waitcnt lgkmcnt(0)\n" \
  "v_mfma_f32_16x16x32_bf16 v[232:235], v[204:207], v[244:247], v[232:235]\n" \
  "v_mfma_f32_16x16x32_bf16 v[236:239], v[220:223], v[244:247], v[236:239]\n" \
  "ds_read_b128 v[240:243], v250 offset:1024\n" \
  "ds_read_b128 v[244:247], v250 offset:17408\n" \
  "v_mfma_f32_16x16x32_bf16 v[224:227], v[192:195], v[64:67], 0\n" \
  "v_mfma_f32_16x16x32_bf16 v[228:231], v[208:211], v[64:67], 0\n" \
  "v_mfma_f32_16x16x32_bf16 v[224:227], v[196:199], v[68:71], v[224:227]\n" \
  "v_cvt_pk_bf16_f32 v232, v232, v236\n" \
  "v_cvt_pk_bf16_f32 v233, v233, v237\n" \
  "v_mfma_f32_16x16x32_bf16 v[228:231], v[212:215], v[68:71], v[228:231]\n" \
  "v_cvt_pk_bf16_f32 v234, v234, v238\n" \
  "v_cvt_pk_bf16_f32 v235, v235, v239\n" \
  "v_mfma_f32_16x16x32_bf16 v[224:227], v[200:203], v[72:75], v[224:227]\n" \
  "s_add_u32 %[cnt], %[so], 4*%c[js]+0*%c[ss]\n" \
  "buffer_store_dwordx4 v[232:235], v251, %[rs], %[cnt] offen sc1\n" \
  "v_mfma_f32_16x16x32_bf16 v[228:231], v[216:219], v[72:75], v[228:231]\n" \
  "v_mfma_f32_16x16x32_bf16 v[224:227], v[204:207], v[76:79], v[224:227]\n" \
  "v_mfma_f32_16x16x32_bf16 v[228:231], v[220:223], v[76:79], v[228:231]\n" \
  "v_mfma_f32_16x16x32_bf16 v[232:235], v[192:195], v[132:135], 0\n" \
  "v_mfma_f32_16x16x32_bf16 v[236:239], v[208:211], v[132:135], 0\n" \
  "v_mfma_f32_16x16x32_bf16 v[232:235], v[200:203], v[140:143], v[232:235]\n" \
  "v_cvt_pk_bf16_f32 v224, v224, v228\n" \
  "v_cvt_pk_bf16_f32 v225, v225, v229\n" \
  "v_mfma_f32_16x16x32_bf16 v[236:239], v[216:219], v[140:143], v[236:239]\n" \
  "v_cvt_pk_bf16_f32 v226, v226, v230\n" \
  "v_cvt_pk_bf16_f32 v227, v227, v231\n" \
  "s_waitcnt lgkmcnt(1)\n" \
  "v_mfma_f32_16x16x32_bf16 v[232:235], v[196:199], v[240:243], v[232:235]\n" \
  "s_add_u32 %[cnt], %[so], 0*%c[js]+1*%c[ss]\n" \
  "buffer_store_dwordx4 v[224:227], v251, %[rs], %[cnt] offen sc1\n" \
  "v_mfma_f32_16x16x32_bf16 v[236:239], v[212:215], v[240:243], v[236:239]\n" \
  "s_waitcnt lgkmcnt(0)\n" \
  "v_mfma_f32_16x16x32_bf16 v[232:235], v[204:207], v[244:247], v[232:235]\n" \
  "v_mfma_f32_16x16x32_bf16 v[236:239], v[220:223], v[244:247], v[236:239]\n" \
  "ds_read_b128 v[240:243], v250 offset:32768\n" \
  "ds_read_b128 v[244:247], v250 offset:49152\n" \
  "v_mfma_f32_16x16x32_bf16 v[224:227], v[192:195], v[16:19], 0\n" \
  "v_mfma_f32_16x16x32_bf16 v[228:231], v[208:211], v[16:19], 0\n" \
  "v_mfma_f32_16x16x32_bf16 v[224:227], v[196:199], v[20:23], v[224:227]\n" \
  "v_cvt_pk_bf16_f32 v232, v232, v236\n" \
  "v_cvt_pk_bf16_f32 v233, v233, v237\n" \
  "v_mfma_f32_16x16x32_bf16 v[228:231], v[212:215], v[20:23], v[228:231]\n" \
  "v_cvt_pk_bf16_f32 v234, v234, v238\n" \
  "v_cvt_pk_bf16_f32 v235, v235, v239\n" \
  "v_mfma_f32_16x16x32_bf16 v[224:227], v[200:203], v[24:27], v[224:227]\n" \
  "s_add_u32 %[cnt], %[so], 4*%c[js]+1*%c[ss]\n" \
  "buffer_store_dwordx4 v[232:235], v251, %[rs], %[cnt] offen sc1\n" \
  "v_mfma_f32_16x16x32_bf16 v[228:231], v[216:219], v[24:27], v[228:231]\n" \
  "v_mfma_f32_16x16x32_bf16 v[224:227], v[204:207], v[28:31], v[224:227]\n" \
  "v_mfma_f32_16x16x32_bf16 v[228:231], v[220:223], v[28:31], v[228:231]\n" \
  "v_mfma_f32_16x16x32_bf16 v[232:235], v[192:195], v[144:147], 0\n" \
  "v_mfma_f32_16x16x32_bf16 v[236:239], v[208:211], v[144:147], 0\n" \
  "v_mfma_f32_16x16x32_bf16 v[232:235], v[200:203], v[152:155], v[232:235]\n" \
  "v_cvt_pk_bf16_f32 v224, v224, v228\n" \
  "v_cvt_pk_bf16_f32 v225, v225, v229\n" \
  "v_mfma_f32_16x16x32_bf16 v[236:239], v[216:219], v[152:155], v[236:239]\n" \
  "v_cvt_pk_bf16_f32 v226, v226, v230\n" \
  "v_cvt_pk_bf16_f32 v227, v227, v231\n" \
  "s_waitcnt lgkmcnt(1)\n" \
  "v_mfma_f32_16x16x32_bf16 v[232:235], v[196:199], v[240:243], v[232:235]\n" \
  "s_add_u32 %[cnt], %[so2], 1*%c[js]+0*%c[ss]\n" \
  "buffer_store_dwordx4 v[224:227], v251, %[rs], %[cnt] offen sc1\n" \
  "v_mfma_f32_16x16x32_bf16 v[236:239], v[212:215], v[240:243], v[236:239]\n" \
  "s_waitcnt lgkmcnt(0)\n" \
  "v_mfma_f32_16x16x32_bf16 v[232:235], v[204:207], v[244:247], v[232:235]\n" \
  "v_mfma_f32_16x16x32_bf16 v[236:239], v[220:223], v[244:247], v[236:239]\n" \
  "ds_read_b128 v[240:243], v250 offset:33792\n" \
  "ds_read_b128 v[244:247], v250 offset:50176\n" \
  "s_cmp_eq_u32 %[ob], 0\n" \
  "s_cbranch_scc1 .Lo6_%=\n" \
  "s_barrier\n" \
  ".Lo6_%=:\n" \
  "v_mfma_f32_16x16x32_bf16 v[224:227], v[192:195], v[80:83], 0\n" \
  "v_mfma_f32_16x16x32_bf16 v[228:231], v[208:211], v[80:83], 0\n" \
  "v_mfma_f32_16x16x32_bf16 v[224:227], v[196:199], v[84:87], v[224:227]\n" \
  "v_cvt_pk_bf16_f32 v232, v232, v236\n" \
  "v_cvt_pk_bf16_f32 v233, v233, v237\n" \
  "v_mfma_f32_16x16x32_bf16 v[228:231], v[212:215], v[84:87], v[228:231]\n" \
  "v_cvt_pk_bf16_f32 v234, v234, v238\n" \
  "v_cvt_pk_bf16_f32 v235, v235, v239\n" \
  "v_mfma_f32_16x16x32_bf16 v[224:227], v[200:203], v[88:91], v[224:227]\n" \
  "s_add_u32 %[cnt], %[so2], 5*%c[js]+0*%c[ss]\n" \
  "buffer_store_dwordx4 v[232:235], v251, %[rs], %[cnt] offen sc1\n" \
  "v_mfma_f32_16x16x32_bf16 v[228:231], v[216:219], v[88:91], v[228:231]\n" \
  "v_mfma_f32_16x16x32_bf16 v[224:227], v[204:207], v[92:95], v[224:227]\n" \
  "v_mfma_f32_16x16x32_bf16 v[228:231], v[220:223], v[92:95], v[228:231]\n" \
  "v_mfma_f32_16x16x32_bf16 v[232:235], v[192:195], v[148:151], 0\n" \
  "v_mfma_f32_16x16x32_bf16 v[236:239], v[208:211], v[148:151], 0\n" \
  "v_mfma_f32_16x16x32_bf16 v[232:235], v[200:203], v[156:159], v[232:235]\n" \
  "v_cvt_pk_bf16_f32 v224, v224, v228\n" \
  "v_cvt_pk_bf16_f32 v225, v225, v229\n" \
  "v_mfma_f32_16x16x32_bf16 v[236:239], v[216:219], v[156:159], v[236:239]\n" \
  "v_cvt_pk_bf16_f32 v226, v226, v230\n" \
  "v_cvt_pk_bf16_f32 v227, v227, v231\n" \
  "s_waitcnt lgkmcnt(1)\n" \
  "v_mfma_f32_16x16x32_bf16 v[232:235], v[196:199], v[240:243], v[232:235]\n" \
  "s_add_u32 %[cnt], %[so2], 1*%c[js]+1*%c[ss]\n" \
  "buffer_store_dwordx4 v[224:227], v251, %[rs], %[cnt] offen sc1\n" \
  "v_mfma_f32_16x16x32_bf16 v[236:239], v[212:215], v[240:243], v[236:239]\n" \
  "s_waitcnt lgkmcnt(0)\n" \
  "v_mfma_f32_16x16x32_bf16 v[232:235], v[204:207], v[244:247], v[232:235]\n" \
  "v_mfma_f32_16x16x32_bf16 v[236:239], v[220:223], v[244:247], v[236:239]\n" \
  "ds_read_b128 v[240:243], v249 offset:0\n" \
  "ds_read_b128 v[244:247], v249 offset:16384\n" \
  "s_cmp_eq_u32 %[ob], 0\n" \
  "s_cbranch_scc1 .Lo8_%=\n" \
  "s_barrier\n" \
  ".Lo8_%=:\n" \
  "v_mfma_f32_16x16x32_bf16 v[224:227], v[192:195], v[32:35], 0\n" \
  "v_mfma_f32_16x16x32_bf16 v[228:231], v[208:211], v[32:35], 0\n" \
  "v_mfma_f32_16x16x32_bf16 v[224:227], v[196:199], v[36:39], v[224:227]\n" \
  "v_cvt_pk_bf16_f32 v232, v232, v236\n" \
  "v_cvt_pk_bf16_f32 v233, v233, v237\n" \
  "v_mfma_f32_16x16x32_bf16 v[228:231], v[212:215], v[36:39], v[228:231]\n" \
  "v_cvt_pk_bf16_f32 v234, v234, v238\n" \
  "v_cvt_pk_bf16_f32 v235, v235, v239\n" \
  "v_mfma_f32_16x16x32_bf16 v[224:227], v[200:203], v[40:43], v[224:227]\n" \
  "s_add_u32 %[cnt], %[so2], 5*%c[js]+1*%c[ss]\n" \
  "buffer_store_dwordx4 v[232:235], v251, %[rs], %[cnt] offen sc1\n" \
  "v_mfma_f32_16x16x32_bf16 v[228:231], v[216:219], v[40:43], v[228:231]\n" \
  "v_mfma_f32_16x16x32_bf16 v[224:227], v[204:207], v[44:47], v[224:227]\n" \
  "v_mfma_f32_16x16x32_bf16 v[228:231], v[220:223], v[44:47], v[228:231]\n" \
  "v_mfma_f32_16x16x32_bf16 v[232:235], v[192:195], v[160:163], 0\n" \
  "v_mfma_f32_16x16x32_bf16 v[236:239], v[208:211], v[160:163], 0\n" \
  "v_mfma_f32_16x16x32_bf16 v[232:235], v[200:203], v[168:171], v[232:235]\n" \
  "v_cvt_pk_bf16_f32 v224, v224, v228\n" \
  "v_cvt_pk_bf16_f32 v225, v225, v229\n" \
  "v_mfma_f32_16x16x32_bf16 v[236:239], v[216:219], v[168:171], v[236:239]\n" \
  "v_cvt_pk_bf16_f32 v226, v226, v230\n" \
  "v_cvt_pk_bf16_f32 v227, v227, v231\n" \
  "s_waitcnt lgkmcnt(1)\n" \
  "v_mfma_f32_16x16x32_bf16 v[232:235], v[196:199], v[240:243], v[232:235]\n" \
  "s_add_u32 %[cnt], %[so], 2*%c[js]+0*%c[ss]\n" \
  "buffer_store_dwordx4 v[224:227], v251, %[rs], %[cnt] offen sc1\n" \
  "v_mfma_f32_16x16x32_bf16 v[236:239], v[212:215], v[240:243], v[236:239]\n" \
  "s_waitcnt lgkmcnt(0)\n" \
  "v_mfma_f32_16x16x32_bf16 v[232:235], v[204:207], v[244:247], v[232:235]\n" \
  "v_mfma_f32_16x16x32_bf16 v[236:239], v[220:223], v[244:247], v[236:239]\n" \
  "ds_read_b128 v[240:243], v249 offset:1024\n" \
  "ds_read_b128 v[244:247], v249 offset:17408\n" \
  "v_mfma_f32_16x16x32_bf16 v[224:227], v[192:195], v[96:99], 0\n" \
  "v_mfma_f32_16x16x32_bf16 v[228:231], v[208:211], v[96:99], 0\n" \
  "v_mfma_f32_16x16x32_bf16 v[224:227], v[196:199], v[100:103], v[224:227]\n" \
  "v_cvt_pk_bf16_f32 v232, v232, v236\n" \
  "v_cvt_pk_bf16_f32 v233, v233, v237\n" \
  "v_mfma_f32_16x16x32_bf16 v[228:231], v[212:215], v[100:103], v[228:231]\n" \
  "v_cvt_pk_bf16_f32 v234, v234, v238\n" \
  "v_cvt_pk_bf16_f32 v235, v235, v239\n" \
  "v_mfma_f32_16x16x32_bf16 v[224:227], v[200:203], v[104:107], v[224:227]\n" \
  "s_add_u32 %[cnt], %[so], 6*%c[js]+0*%c[ss]\n" \
  "buffer_store_dwordx4 v[232:235], v251, %[rs], %[cnt] offen sc1\n" \
  "v_mfma_f32_16x16x32_bf16 v[228:231], v[216:219], v[104:107], v[228:231]\n" \
  "v_mfma_f32_16x16x32_bf16 v[224:227], v[204:207], v[108:111], v[224:227]\n" \
  "v_mfma_f32_16x16x32_bf16 v[228:231], v[220:223], v[108:111], v[228:231]\n" \
  "v_mfma_f32_16x16x32_bf16 v[232:235], v[192:195], v[164:167], 0\n" \
  "v_mfma_f32_16x16x32_bf16 v[236:239], v[208:211], v[164:167], 0\n" \
  "v_mfma_f32_16x16x32_bf16 v[232:235], v[200:203], v[172:175], v[232:235]\n" \
  "v_cvt_pk_bf16_f32 v224, v224, v228\n" \
  "v_cvt_pk_bf16_f32 v225, v225, v229\n" \
  "v_mfma_f32_16x16x32_bf16 v[236:239], v[216:219], v[172:175], v[236:239]\n" \
  "v_cvt_pk_bf16_f32 v226, v226, v230\n" \
  "v_cvt_pk_bf16_f32 v227, v227, v231\n" \
  "s_waitcnt lgkmcnt(1)\n" \
  "v_mfma_f32_16x16x32_bf16 v[232:235], v[196:199], v[240:243], v[232:235]\n" \
  "s_add_u32 %[cnt], %[so], 2*%c[js]+1*%c[ss]\n" \
  "buffer_store_dwordx4 v[224:227], v251, %[rs], %[cnt] offen sc1\n" \
  "v_mfma_f32_16x16x32_bf16 v[236:239], v[212:215], v[240:243], v[236:239]\n" \
  "s_waitcnt lgkmcnt(0)\n" \
  "v_mfma_f32_16x16x32_bf16 v[232:235], v[204:207], v[244:247], v[232:235]\n" \
  "v_mfma_f32_16x16x32_bf16 v[236:239], v[220:223], v[244:247], v[236:239]\n" \
  "ds_read_b128 v[240:243], v249 offset:32768\n" \
  "ds_read_b128 v[244:247], v249 offset:49152\n" \
  "s_cmp_eq_u32 %[ob], 0\n" \
  "s_cbranch_scc1 .Lo12_%=\n" \
  "s_barrier\n" \
  ".Lo12_%=:\n" \
  "v_mfma_f32_16x16x32_bf16 v[224:227], v[192:195], v[48:51], 0\n" \
  "v_mfma_f32_16x16x32_bf16 v[228:231], v[208:211], v[48:51], 0\n" \
  "v_mfma_f32_16x16x32_bf16 v[224:227], v[196:199], v[52:55], v[224:227]\n" \
  "v_cvt_pk_bf16_f32 v232, v232, v236\n" \
  "v_cvt_pk_bf16_f32 v233, v233, v237\n" \
  "v_mfma_f32_16x16x32_bf16 v[228:231], v[212:215], v[52:55], v[228:231]\n" \
  "v_cvt_pk_bf16_f32 v234, v234, v238\n" \
  "v_cvt_pk_bf16_f32 v235, v235, v239\n" \
  "v_mfma_f32_16x16x32_bf16 v[224:227], v[200:203], v[56:59], v[224:227]\n" \
  "s_add_u32 %[cnt], %[so], 6*%c[js]+1*%c[ss]\n" \
  "buffer_store_dwordx4 v[232:235], v251, %[rs], %[cnt] offen sc1\n" \
  "v_mfma_f32_16x16x32_bf16 v[228:231], v[216:219], v[56:59], v[228:231]\n" \
  "v_mfma_f32_16x16x32_bf16 v[224:227], v[204:207], v[60:63], v[224:227]\n" \
  "v_mfma_f32_16x16x32_bf16 v[228:231], v[220:223], v[60:63], v[228:231]\n" \
  "v_mfma_f32_16x16x32_bf16 v[232:235], v[192:195], v[176:179], 0\n" \
  "v_mfma_f32_16x16x32_bf16 v[236:239], v[208:211], v[176:179], 0\n" \
  "v_mfma_f32_16x16x32_bf16 v[232:235], v[200:203], v[184:187], v[232:235]\n" \
  "v_cvt_pk_bf16_f32 v224, v224, v228\n" \
  "v_cvt_pk_bf16_f32 v225, v225, v229\n" \
  "v_mfma_f32_16x16x32_bf16 v[236:239], v[216:219], v[184:187], v[236:239]\n" \
  "v_cvt_pk_bf16_f32 v226, v226, v230\n" \
  "v_cvt_pk_bf16_f32 v227, v227, v231\n" \
  "s_waitcnt lgkmcnt(1)\n" \
  "v_mfma_f32_16x16x32_bf16 v[232:235], v[196:199], v[240:243], v[232:235]\n" \
  "s_add_u32 %[cnt], %[so2], 3*%c[js]+0*%c[ss]\n" \
  "buffer_store_dwordx4 v[224:227], v251, %[rs], %[cnt] offen sc1\n" \
  "v_mfma_f32_16x16x32_bf16 v[236:239], v[212:215], v[240:243], v[236:239]\n" \
  "s_waitcnt lgkmcnt(0)\n" \
  "v_mfma_f32_16x16x32_bf16 v[232:235], v[204:207], v[244:247], v[232:235]\n" \
  "v_mfma_f32_16x16x32_bf16 v[236:239], v[220:223], v[244:247], v[236:239]\n" \
  "ds_read_b128 v[240:243], v249 offset:33792\n" \
  "ds_read_b128 v[244:247], v249 offset:50176\n" \
  "v_mfma_f32_16x16x32_bf16 v[224:227], v[192:195], v[112:115], 0\n" \
  "v_mfma_f32_16x16x32_bf16 v[228:231], v[208:211], v[112:115], 0\n" \
  "v_mfma_f32_16x16x32_bf16 v[224:227], v[196:199], v[116:119], v[224:227]\n" \
  "v_cvt_pk_bf16_f32 v232, v232, v236\n" \
  "v_cvt_pk_bf16_f32 v233, v233, v237\n" \
  "v_mfma_f32_16x16x32_bf16 v[228:231], v[212:215], v[116:119], v[228:231]\n" \
  "v_cvt_pk_bf16_f32 v234, v234, v238\n" \
  "v_cvt_pk_bf16_f32 v235, v235, v239\n" \
  "v_mfma_f32_16x16x32_bf16 v[224:227], v[200:203], v[120:123], v[224:227]\n" \
  "s_add_u32 %[cnt], %[so2], 7*%c[js]+0*%c[ss]\n" \
  "buffer_store_dwordx4 v[232:235], v251, %[rs], %[cnt] offen sc1\n" \
  "v_mfma_f32_16x16x32_bf16 v[228:231], v[216:219], v[120:123], v[228:231]\n" \
  "v_mfma_f32_16x16x32_bf16 v[224:227], v[204:207], v[124:127], v[224:227]\n" \
  "v_mfma_f32_16x16x32_bf16 v[228:231], v[220:223], v[124:127], v[228:231]\n" \
  "v_mfma_f32_16x16x32_bf16 v[232:235], v[192:195], v[180:183], 0\n" \
  "v_mfma_f32_16x16x32_bf16 v[236:239], v[208:211], v[180:183], 0\n" \
  "v_mfma_f32_16x16x32_bf16 v[232:235], v[200:203], v[188:191], v[232:235]\n" \
  "v_cvt_pk_bf16_f32 v224, v224, v228\n" \
  "v_cvt_pk_bf16_f32 v225, v225, v229\n" \
  "v_mfma_f32_16x16x32_bf16 v[236:239], v[216:219], v[188:191], v[236:239]\n" \
  "v_cvt_pk_bf16_f32 v226, v226, v230\n" \
  "v_cvt_pk_bf16_f32 v227, v227, v231\n" \
  "s_waitcnt lgkmcnt(1)\n" \
  "v_mfma_f32_16x16x32_bf16 v[232:235], v[196:199], v[240:243], v[232:235]\n" \
  "s_add_u32 %[cnt], %[so2], 3*%c[js]+1*%c[ss]\n" \
  "buffer_store_dwordx4 v[224:227], v251, %[rs], %[cnt] offen sc1\n" \
  "v_mfma_f32_16x16x32_bf16 v[236:239], v[212:215], v[240:243], v[236:239]\n" \
  "s_waitcnt lgkmcnt(0)\n" \
  "v_mfma_f32_16x16x32_bf16 v[232:235], v[204:207], v[244:247], v[232:235]\n" \
  "v_mfma_f32_16x16x32_bf16 v[236:239], v[220:223], v[244:247], v[236:239]\n" \
  "s_nop 8\n" \
  "v_cvt_pk_bf16_f32 v232, v232, v236\n" \
  "v_cvt_pk_bf16_f32 v233, v233, v237\n" \
  "v_cvt_pk_bf16_f32 v234, v234, v238\n" \
  "v_cvt_pk_bf16_f32 v235, v235, v239\n" \
  "s_add_u32 %[cnt], %[so2], 7*%c[js]+1*%c[ss]\n" \
  "buffer_store_dwordx4 v[232:235], v251, %[rs], %[cnt] offen sc1\n" \
  "s_setprio 0\n" \
  "s_nop 1\n"
#define PRO0_0 \
  "s_cmp_lg_u32 %[hb], 0\n" \
  "s_cbranch_scc1 .Ls_%=\n" \
  "v_xor_b32 %[t0], 0x0, %[pb]\n" \
  "ds_read_b64_tr_b16 v[12:13], %[t0] offset:0\n" \
  "ds_read_b64_tr_b16 v[96:97], %[t0] offset:16384\n" \
  "v_xor_b32 %[t1], 0x20, %[pb]\n" \
  "ds_read_b64_tr_b16 v[28:29], %[t1] offset:0\n" \
  "ds_read_b64_tr_b16 v[144:145], %[t1] offset:16384\n" \
  "v_xor_b32 %[t0], 0x1000, %[pb]\n" \
  "ds_read_b64_tr_b16 v[0:1], %[t0] offset:0\n" \
  "ds_read_b64_tr_b16 v[192:193], %[t0] offset:16384\n" \
  "v_xor_b32 %[t1], 0x1020, %[pb]\n" \
  "ds_read_b64_tr_b16 v[48:49], %[t1] offset:0\n" \
  "ds_read_b64_tr_b16 v[200:201], %[t1] offset:16384\n" \
  "v_xor_b32 %[t0], 0x2000, %[pb]\n" \
  "ds_read_b64_tr_b16 v[16:17], %[t0] offset:0\n" \
  "ds_read_b64_tr_b16 v[208:209], %[t0] offset:16384\n" \
  "v_xor_b32 %[t1], 0x2020, %[pb]\n" \
  "ds_read_b64_tr_b16 v[64:65], %[t1] offset:0\n" \
  "ds_read_b64_tr_b16 v[216:217], %[t1] offset:16384\n" \
  "v_xor_b32 %[t0], 0x3000, %[pb]\n" \
  "ds_read_b64_tr_b16 v[32:33], %[t0] offset:0\n" \
  "ds_read_b64_tr_b16 v[128:129], %[t0] offset:16384\n" \
  "v_xor_b32 %[t1], 0x3020, %[pb]\n" \
  "ds_read_b64_tr_b16 v[80:81], %[t1] offset:0\n" \
  "ds_read_b64_tr_b16 v[176:177], %[t1] offset:16384\n" \
  ".Ls_%=:\n" \
  "s_waitcnt lgkmcnt(0)\n"
#define PRO0_1 \
  "s_cmp_lg_u32 %[hb], 1\n" \
  "s_cbranch_scc1 .Ls_%=\n" \
  "v_xor_b32 %[t0], 0x8000, %[pb]\n" \
  "ds_read_b64_tr_b16 v[12:13], %[t0] offset:0\n" \
  "ds_read_b64_tr_b16 v[96:97], %[t0] offset:16384\n" \
  "v_xor_b32 %[t1], 0x8020, %[pb]\n" \
  "ds_read_b64_tr_b16 v[28:29], %[t1] offset:0\n" \
  "ds_read_b64_tr_b16 v[144:145], %[t1] offset:16384\n" \
  "v_xor_b32 %[t0], 0x9000, %[pb]\n" \
  "ds_read_b64_tr_b16 v[0:1], %[t0] offset:0\n" \
  "ds_read_b64_tr_b16 v[192:193], %[t0] offset:16384\n" \
  "v_xor_b32 %[t1], 0x9020, %[pb]\n" \
  "ds_read_b64_tr_b16 v[48:49], %[t1] offset:0\n" \
  "ds_read_b64_tr_b16 v[200:201], %[t1] offset:16384\n" \
  "v_xor_b32 %[t0], 0xa000, %[pb]\n" \
  "ds_read_b64_tr_b16 v[16:17], %[t0] offset:0\n" \
  "ds_read_b64_tr_b16 v[208:209], %[t0] offset:16384\n" \
  "v_xor_b32 %[t1], 0xa020, %[pb]\n" \
  "ds_read_b64_tr_b16 v[64:65], %[t1] offset:0\n" \
  "ds_read_b64_tr_b16 v[216:217], %[t1] offset:16384\n" \
  "v_xor_b32 %[t0], 0xb000, %[pb]\n" \
  "ds_read_b64_tr_b16 v[32:33], %[t0] offset:0\n" \
  "ds_read_b64_tr_b16 v[128:129], %[t0] offset:16384\n" \
  "v_xor_b32 %[t1], 0xb020, %[pb]\n" \
  "ds_read_b64_tr_b16 v[80:81], %[t1] offset:0\n" \
  "ds_read_b64_tr_b16 v[176:177], %[t1] offset:16384\n" \
  ".Ls_%=:\n" \
  "s_waitcnt lgkmcnt(0)\n"
#define PRO0_2 \
  "s_cmp_lg_u32 %[hb], 0\n" \
  "s_cbranch_scc1 .Ls_%=\n" \
  "v_xor_b32 %[t0], 0x10000, %[pb]\n" \
  "ds_read_b64_tr_b16 v[14:15], %[t0] offset:0\n" \
  "ds_read_b64_tr_b16 v[98:99], %[t0] offset:16384\n" \
  "v_xor_b32 %[t1], 0x10020, %[pb]\n" \
  "ds_read_b64_tr_b16 v[30:31], %[t1] offset:0\n" \
  "ds_read_b64_tr_b16 v[146:147], %[t1] offset:16384\n" \
  "v_xor_b32 %[t0], 0x11000, %[pb]\n" \
  "ds_read_b64_tr_b16 v[2:3], %[t0] offset:0\n" \
  "ds_read_b64_tr_b16 v[194:195], %[t0] offset:16384\n" \
  "v_xor_b32 %[t1], 0x11020, %[pb]\n" \
  "ds_read_b64_tr_b16 v[50:51], %[t1] offset:0\n" \
  "ds_read_b64_tr_b16 v[202:203], %[t1] offset:16384\n" \
  "v_xor_b32 %[t0], 0x12000, %[pb]\n" \
  "ds_read_b64_tr_b16 v[18:19], %[t0] offset:0\n" \
  "ds_read_b64_tr_b16 v[210:211], %[t0] offset:16384\n" \
  "v_xor_b32 %[t1], 0x12020, %[pb]\n" \
  "ds_read_b64_tr_b16 v[66:67], %[t1] offset:0\n" \
  "ds_read_b64_tr_b16 v[218:219], %[t1] offset:16384\n" \
  "v_xor_b32 %[t0], 0x13000, %[pb]\n" \
  "ds_read_b64_tr_b16 v[34:35], %[t0] offset:0\n" \
  "ds_read_b64_tr_b16 v[130:131], %[t0] offset:16384\n" \
  "v_xor_b32 %[t1], 0x13020, %[pb]\n" \
  "ds_read_b64_tr_b16 v[82:83], %[t1] offset:0\n" \
  "ds_read_b64_tr_b16 v[178:179], %[t1] offset:16384\n" \
  ".Ls_%=:\n" \
  "s_waitcnt lgkmcnt(0)\n"
#define PRO0_3 \
  "s_cmp_lg_u32 %[hb], 1\n" \
  "s_cbranch_scc1 .Ls_%=\n" \
  "v_xor_b32 %[t0], 0x18000, %[pb]\n" \
  "ds_read_b64_tr_b16 v[14:15], %[t0] offset:0\n" \
  "ds_read_b64_tr_b16 v[98:99], %[t0] offset:16384\n" \
  "v_xor_b32 %[t1], 0x18020, %[pb]\n" \
  "ds_read_b64_tr_b16 v[30:31], %[t1] offset:0\n" \
  "ds_read_b64_tr_b16 v[146:147], %[t1] offset:16384\n" \
  "v_xor_b32 %[t0], 0x19000, %[pb]\n" \
  "ds_read_b64_tr_b16 v[2:3], %[t0] offset:0\n" \
  "ds_read_b64_tr_b16 v[194:195], %[t0] offset:16384\n" \
  "v_xor_b32 %[t1], 0x19020, %[pb]\n" \
  "ds_read_b64_tr_b16 v[50:51], %[t1] offset:0\n" \
  "ds_read_b64_tr_b16 v[202:203], %[t1] offset:16384\n" \
  "v_xor_b32 %[t0], 0x1a000, %[pb]\n" \
  "ds_read_b64_tr_b16 v[18:19], %[t0] offset:0\n" \
  "ds_read_b64_tr_b16 v[210:211], %[t0] offset:16384\n" \
  "v_xor_b32 %[t1], 0x1a020, %[pb]\n" \
  "ds_read_b64_tr_b16 v[66:67], %[t1] offset:0\n" \
  "ds_read_b64_tr_b16 v[218:219], %[t1] offset:16384\n" \
  "v_xor_b32 %[t0], 0x1b000, %[pb]\n" \
  "ds_read_b64_tr_b16 v[34:35], %[t0] offset:0\n" \
  "ds_read_b64_tr_b16 v[130:131], %[t0] offset:16384\n" \
  "v_xor_b32 %[t1], 0x1b020, %[pb]\n" \
  "ds_read_b64_tr_b16 v[82:83], %[t1] offset:0\n" \
  "ds_read_b64_tr_b16 v[178:179], %[t1] offset:16384\n" \
  ".Ls_%=:\n" \
  "s_waitcnt lgkmcnt(0)\n"
#define PRO0_4 \
  "s_cmp_lg_u32 %[hb], 0\n" \
  "s_cbranch_scc1 .Ls_%=\n" \
  "v_xor_b32 %[t0], 0x20000, %[pb]\n" \
  "ds_read_b64_tr_b16 v[60:61], %[t0] offset:0\n" \
  "ds_read_b64_tr_b16 v[100:101], %[t0] offset:16384\n" \
  "v_xor_b32 %[t1], 0x20020, %[pb]\n" \
  "ds_read_b64_tr_b16 v[76:77], %[t1] offset:0\n" \
  "ds_read_b64_tr_b16 v[148:149], %[t1] offset:16384\n" \
  "v_xor_b32 %[t0], 0x21000, %[pb]\n" \
  "ds_read_b64_tr_b16 v[4:5], %[t0] offset:0\n" \
  "ds_read_b64_tr_b16 v[196:197], %[t0] offset:16384\n" \
  "v_xor_b32 %[t1], 0x21020, %[pb]\n" \
  "ds_read_b64_tr_b16 v[52:53], %[t1] offset:0\n" \
  "ds_read_b64_tr_b16 v[204:205], %[t1] offset:16384\n" \
  "v_xor_b32 %[t0], 0x22000, %[pb]\n" \
  "ds_read_b64_tr_b16 v[20:21], %[t0] offset:0\n" \
  "ds_read_b64_tr_b16 v[212:213], %[t0] offset:16384\n" \
  "v_xor_b32 %[t1], 0x22020, %[pb]\n" \
  "ds_read_b64_tr_b16 v[68:69], %[t1] offset:0\n" \
  "ds_read_b64_tr_b16 v[220:221], %[t1] offset:16384\n" \
  "v_xor_b32 %[t0], 0x23000, %[pb]\n" \
  "ds_read_b64_tr_b16 v[36:37], %[t0] offset:0\n" \
  "ds_read_b64_tr_b16 v[132:133], %[t0] offset:16384\n" \
  "v_xor_b32 %[t1], 0x23020, %[pb]\n" \
  "ds_read_b64_tr_b16 v[84:85], %[t1] offset:0\n" \
  "ds_read_b64_tr_b16 v[180:181], %[t1] offset:16384\n" \
  ".Ls_%=:\n" \
  "s_waitcnt lgkmcnt(0)\n"
#define PRO0_5 \
  "s_cmp_lg_u32 %[hb], 1\n" \
  "s_cbranch_scc1 .Ls_%=\n" \
  "v_xor_b32 %[t0], 0x0, %[pb]\n" \
  "ds_read_b64_tr_b16 v[60:61], %[t0] offset:0\n" \
  "ds_read_b64_tr_b16 v[100:101], %[t0] offset:16384\n" \
  "v_xor_b32 %[t1], 0x20, %[pb]\n" \
  "ds_read_b64_tr_b16 v[76:77], %[t1] offset:0\n" \
  "ds_read_b64_tr_b16 v[148:149], %[t1] offset:16384\n" \
  "v_xor_b32 %[t0], 0x1000, %[pb]\n" \
  "ds_read_b64_tr_b16 v[4:5], %[t0] offset:0\n" \
  "ds_read_b64_tr_b16 v[196:197], %[t0] offset:16384\n" \
  "v_xor_b32 %[t1], 0x1020, %[pb]\n" \
  "ds_read_b64_tr_b16 v[52:53], %[t1] offset:0\n" \
  "ds_read_b64_tr_b16 v[204:205], %[t1] offset:16384\n" \
  "v_xor_b32 %[t0], 0x2000, %[pb]\n" \
  "ds_read_b64_tr_b16 v[20:21], %[t0] offset:0\n" \
  "ds_read_b64_tr_b16 v[212:213], %[t0] offset:16384\n" \
  "v_xor_b32 %[t1], 0x2020, %[pb]\n" \
  "ds_read_b64_tr_b16 v[68:69], %[t1] offset:0\n" \
  "ds_read_b64_tr_b16 v[220:221], %[t1] offset:16384\n" \
  "v_xor_b32 %[t0], 0x3000, %[pb]\n" \
  "ds_read_b64_tr_b16 v[36:37], %[t0] offset:0\n" \
  "ds_read_b64_tr_b16 v[132:133], %[t0] offset:16384\n" \
  "v_xor_b32 %[t1], 0x3020, %[pb]\n" \
  "ds_read_b64_tr_b16 v[84:85], %[t1] offset:0\n" \
  "ds_read_b64_tr_b16 v[180:181], %[t1] offset:16384\n" \
  ".Ls_%=:\n" \
  "s_waitcnt lgkmcnt(0)\n"
#define PRO0_6 \
  "s_cmp_lg_u32 %[hb], 0\n" \
  "s_cbranch_scc1 .Ls_%=\n" \
  "v_xor_b32 %[t0], 0x8000, %[pb]\n" \
  "ds_read_b64_tr_b16 v[62:63], %[t0] offset:0\n" \
  "ds_read_b64_tr_b16 v[102:103], %[t0] offset:16384\n" \
  "v_xor_b32 %[t1], 0x8020, %[pb]\n" \
  "ds_read_b64_tr_b16 v[78:79], %[t1] offset:0\n" \
  "ds_read_b64_tr_b16 v[150:151], %[t1] offset:16384\n" \
  "v_xor_b32 %[t0], 0x9000, %[pb]\n" \
  "ds_read_b64_tr_b16 v[6:7], %[t0] offset:0\n" \
  "ds_read_b64_tr_b16 v[198:199], %[t0] offset:16384\n" \
  "v_xor_b32 %[t1], 0x9020, %[pb]\n" \
  "ds_read_b64_tr_b16 v[54:55], %[t1] offset:0\n" \
  "ds_read_b64_tr_b16 v[206:207], %[t1] offset:16384\n" \
  "v_xor_b32 %[t0], 0xa000, %[pb]\n" \
  "ds_read_b64_tr_b16 v[22:23], %[t0] offset:0\n" \
  "ds_read_b64_tr_b16 v[214:215], %[t0] offset:16384\n" \
  "v_xor_b32 %[t1], 0xa020, %[pb]\n" \
  "ds_read_b64_tr_b16 v[70:71], %[t1] offset:0\n" \
  "ds_read_b64_tr_b16 v[222:223], %[t1] offset:16384\n" \
  "v_xor_b32 %[t0], 0xb000, %[pb]\n" \
  "ds_read_b64_tr_b16 v[38:39], %[t0] offset:0\n" \
  "ds_read_b64_tr_b16 v[134:135], %[t0] offset:16384\n" \
  "v_xor_b32 %[t1], 0xb020, %[pb]\n" \
  "ds_read_b64_tr_b16 v[86:87], %[t1] offset:0\n" \
  "ds_read_b64_tr_b16 v[182:183], %[t1] offset:16384\n" \
  ".Ls_%=:\n" \
  "s_waitcnt lgkmcnt(0)\n"
#define PRO0_7 \
  "s_cmp_lg_u32 %[hb], 1\n" \
  "s_cbranch_scc1 .Ls_%=\n" \
  "v_xor_b32 %[t0], 0x10000, %[pb]\n" \
  "ds_read_b64_tr_b16 v[62:63], %[t0] offset:0\n" \
  "ds_read_b64_tr_b16 v[102:103], %[t0] offset:16384\n" \
  "v_xor_b32 %[t1], 0x10020, %[pb]\n" \
  "ds_read_b64_tr_b16 v[78:79], %[t1] offset:0\n" \
  "ds_read_b64_tr_b16 v[150:151], %[t1] offset:16384\n" \
  "v_xor_b32 %[t0], 0x11000, %[pb]\n" \
  "ds_read_b64_tr_b16 v[6:7], %[t0] offset:0\n" \
  "ds_read_b64_tr_b16 v[198:199], %[t0] offset:16384\n" \
  "v_xor_b32 %[t1], 0x11020, %[pb]\n" \
  "ds_read_b64_tr_b16 v[54:55], %[t1] offset:0\n" \
  "ds_read_b64_tr_b16 v[206:207], %[t1] offset:16384\n" \
  "v_xor_b32 %[t0], 0x12000, %[pb]\n" \
  "ds_read_b64_tr_b16 v[22:23], %[t0] offset:0\n" \
  "ds_read_b64_tr_b16 v[214:215], %[t0] offset:16384\n" \
  "v_xor_b32 %[t1], 0x12020, %[pb]\n" \
  "ds_read_b64_tr_b16 v[70:71], %[t1] offset:0\n" \
  "ds_read_b64_tr_b16 v[222:223], %[t1] offset:16384\n" \
  "v_xor_b32 %[t0], 0x13000, %[pb]\n" \
  "ds_read_b64_tr_b16 v[38:39], %[t0] offset:0\n" \
  "ds_read_b64_tr_b16 v[134:135], %[t0] offset:16384\n" \
  "v_xor_b32 %[t1], 0x13020, %[pb]\n" \
  "ds_read_b64_tr_b16 v[86:87], %[t1] offset:0\n" \
  "ds_read_b64_tr_b16 v[182:183], %[t1] offset:16384\n" \
  ".Ls_%=:\n" \
  "s_waitcnt lgkmcnt(0)\n"
#define PRO0_8 \
  "s_cmp_lg_u32 %[hb], 0\n" \
  "s_cbranch_scc1 .Ls_%=\n" \
  "v_xor_b32 %[t0], 0x18000, %[pb]\n" \
  "ds_read_b64_tr_b16 v[224:225], %[t0] offset:0\n" \
  "ds_read_b64_tr_b16 v[104:105], %[t0] offset:16384\n" \
  "v_xor_b32 %[t1], 0x18020, %[pb]\n" \
  "ds_read_b64_tr_b16 v[232:233], %[t1] offset:0\n" \
  "ds_read_b64_tr_b16 v[152:153], %[t1] offset:16384\n" \
  "v_xor_b32 %[t0], 0x19000, %[pb]\n" \
  "ds_read_b64_tr_b16 v[8:9], %[t0] offset:0\n" \
  "ds_read_b64_tr_b16 v[112:113], %[t0] offset:16384\n" \
  "v_xor_b32 %[t1], 0x19020, %[pb]\n" \
  "ds_read_b64_tr_b16 v[56:57], %[t1] offset:0\n" \
  "ds_read_b64_tr_b16 v[160:161], %[t1] offset:16384\n" \
  "v_xor_b32 %[t0], 0x1a000, %[pb]\n" \
  "ds_read_b64_tr_b16 v[24:25], %[t0] offset:0\n" \
  "ds_read_b64_tr_b16 v[120:121], %[t0] offset:16384\n" \
  "v_xor_b32 %[t1], 0x1a020, %[pb]\n" \
  "ds_read_b64_tr_b16 v[72:73], %[t1] offset:0\n" \
  "ds_read_b64_tr_b16 v[168:169], %[t1] offset:16384\n" \
  "v_xor_b32 %[t0], 0x1b000, %[pb]\n" \
  "ds_read_b64_tr_b16 v[40:41], %[t0] offset:0\n" \
  "ds_read_b64_tr_b16 v[136:137], %[t0] offset:16384\n" \
  "v_xor_b32 %[t1], 0x1b020, %[pb]\n" \
  "ds_read_b64_tr_b16 v[88:89], %[t1] offset:0\n" \
  "ds_read_b64_tr_b16 v[184:185], %[t1] offset:16384\n" \
  ".Ls_%=:\n" \
  "s_waitcnt lgkmcnt(0)\n"
#define PRO0_9 \
  "s_cmp_lg_u32 %[hb], 1\n" \
  "s_cbranch_scc1 .Ls_%=\n" \
  "v_xor_b32 %[t0], 0x20000, %[pb]\n" \
  "ds_read_b64_tr_b16 v[224:225], %[t0] offset:0\n" \
  "ds_read_b64_tr_b16 v[104:105], %[t0] offset:16384\n" \
  "v_xor_b32 %[t1], 0x20020, %[pb]\n" \
  "ds_read_b64_tr_b16 v[232:233], %[t1] offset:0\n" \
  "ds_read_b64_tr_b16 v[152:153], %[t1] offset:16384\n" \
  "v_xor_b32 %[t0], 0x21000, %[pb]\n" \
  "ds_read_b64_tr_b16 v[8:9], %[t0] offset:0\n" \
  "ds_read_b64_tr_b16 v[112:113], %[t0] offset:16384\n" \
  "v_xor_b32 %[t1], 0x21020, %[pb]\n" \
  "ds_read_b64_tr_b16 v[56:57], %[t1] offset:0\n" \
  "ds_read_b64_tr_b16 v[160:161], %[t1] offset:16384\n" \
  "v_xor_b32 %[t0], 0x22000, %[pb]\n" \
  "ds_read_b64_tr_b16 v[24:25], %[t0] offset:0\n" \
  "ds_read_b64_tr_b16 v[120:121], %[t0] offset:16384\n" \
  "v_xor_b32 %[t1], 0x22020, %[pb]\n" \
  "ds_read_b64_tr_b16 v[72:73], %[t1] offset:0\n" \
  "ds_read_b64_tr_b16 v[168:169], %[t1] offset:16384\n" \
  "v_xor_b32 %[t0], 0x23000, %[pb]\n" \
  "ds_read_b64_tr_b16 v[40:41], %[t0] offset:0\n" \
  "ds_read_b64_tr_b16 v[136:137], %[t0] offset:16384\n" \
  "v_xor_b32 %[t1], 0x23020, %[pb]\n" \
  "ds_read_b64_tr_b16 v[88:89], %[t1] offset:0\n" \
  "ds_read_b64_tr_b16 v[184:185], %[t1] offset:16384\n" \
  ".Ls_%=:\n" \
  "s_waitcnt lgkmcnt(0)\n"
#define PRO0_10 \
  "s_cmp_lg_u32 %[hb], 0\n" \
  "s_cbranch_scc1 .Ls_%=\n" \
  "v_xor_b32 %[t0], 0x0, %[pb]\n" \
  "ds_read_b64_tr_b16 v[226:227], %[t0] offset:0\n" \
  "ds_read_b64_tr_b16 v[106:107], %[t0] offset:16384\n" \
  "v_xor_b32 %[t1], 0x20, %[pb]\n" \
  "ds_read_b64_tr_b16 v[234:235], %[t1] offset:0\n" \
  "ds_read_b64_tr_b16 v[154:155], %[t1] offset:16384\n" \
  "v_xor_b32 %[t0], 0x1000, %[pb]\n" \
  "ds_read_b64_tr_b16 v[10:11], %[t0] offset:0\n" \
  "ds_read_b64_tr_b16 v[114:115], %[t0] offset:16384\n" \
  "v_xor_b32 %[t1], 0x1020, %[pb]\n" \
  "ds_read_b64_tr_b16 v[58:59], %[t1] offset:0\n" \
  "ds_read_b64_tr_b16 v[162:163], %[t1] offset:16384\n" \
  "v_xor_b32 %[t0], 0x2000, %[pb]\n" \
  "ds_read_b64_tr_b16 v[26:27], %[t0] offset:0\n" \
  "ds_read_b64_tr_b16 v[122:123], %[t0] offset:16384\n" \
  "v_xor_b32 %[t1], 0x2020, %[pb]\n" \
  "ds_read_b64_tr_b16 v[74:75], %[t1] offset:0\n" \
  "ds_read_b64_tr_b16 v[170:171], %[t1] offset:16384\n" \
  "v_xor_b32 %[t0], 0x3000, %[pb]\n" \
  "ds_read_b64_tr_b16 v[42:43], %[t0] offset:0\n" \
  "ds_read_b64_tr_b16 v[138:139], %[t0] offset:16384\n" \
  "v_xor_b32 %[t1], 0x3020, %[pb]\n" \
  "ds_read_b64_tr_b16 v[90:91], %[t1] offset:0\n" \
  "ds_read_b64_tr_b16 v[186:187], %[t1] offset:16384\n" \
  ".Ls_%=:\n" \
  "s_waitcnt lgkmcnt(0)\n"
#define PRO0_11 \
  "s_cmp_lg_u32 %[hb], 1\n" \
  "s_cbranch_scc1 .Ls_%=\n" \
  "v_xor_b32 %[t0], 0x8000, %[pb]\n" \
  "ds_read_b64_tr_b16 v[226:227], %[t0] offset:0\n" \
  "ds_read_b64_tr_b16 v[106:107], %[t0] offset:16384\n" \
  "v_xor_b32 %[t1], 0x8020, %[pb]\n" \
  "ds_read_b64_tr_b16 v[234:235], %[t1] offset:0\n" \
  "ds_read_b64_tr_b16 v[154:155], %[t1] offset:16384\n" \
  "v_xor_b32 %[t0], 0x9000, %[pb]\n" \
  "ds_read_b64_tr_b16 v[10:11], %[t0] offset:0\n" \
  "ds_read_b64_tr_b16 v[114:115], %[t0] offset:16384\n" \
  "v_xor_b32 %[t1], 0x9020, %[pb]\n" \
  "ds_read_b64_tr_b16 v[58:59], %[t1] offset:0\n" \
  "ds_read_b64_tr_b16 v[162:163], %[t1] offset:16384\n" \
  "v_xor_b32 %[t0], 0xa000, %[pb]\n" \
  "ds_read_b64_tr_b16 v[26:27], %[t0] offset:0\n" \
  "ds_read_b64_tr_b16 v[122:123], %[t0] offset:16384\n" \
  "v_xor_b32 %[t1], 0xa020, %[pb]\n" \
  "ds_read_b64_tr_b16 v[74:75], %[t1] offset:0\n" \
  "ds_read_b64_tr_b16 v[170:171], %[t1] offset:16384\n" \
  "v_xor_b32 %[t0], 0xb000, %[pb]\n" \
  "ds_read_b64_tr_b16 v[42:43], %[t0] offset:0\n" \
  "ds_read_b64_tr_b16 v[138:139], %[t0] offset:16384\n" \
  "v_xor_b32 %[t1], 0xb020, %[pb]\n" \
  "ds_read_b64_tr_b16 v[90:91], %[t1] offset:0\n" \
  "ds_read_b64_tr_b16 v[186:187], %[t1] offset:16384\n" \
  ".Ls_%=:\n" \
  "s_waitcnt lgkmcnt(0)\n"
#define PRO0_12 \
  "v_add_u32 %[t0], 0x8000, %[b0]\n" \
  "ds_write_b128 %[t0], v[12:15]\n" \
  "ds_write_b128 %[t0], v[60:63] offset:1024\n" \
  "v_add_u32 %[t0], 0xc000, %[b0]\n" \
  "ds_write_b128 %[t0], v[28:31]\n" \
  "ds_write_b128 %[t0], v[76:79] offset:1024\n" \
  "s_cmp_lg_u32 %[hb], 0\n" \
  "s_cbranch_scc1 .Ls_%=\n" \
  "v_xor_b32 %[t0], 0x10000, %[pb]\n" \
  "ds_read_b64_tr_b16 v[228:229], %[t0] offset:0\n" \
  "ds_read_b64_tr_b16 v[108:109], %[t0] offset:16384\n" \
  "v_xor_b32 %[t1], 0x10020, %[pb]\n" \
  "ds_read_b64_tr_b16 v[236:237], %[t1] offset:0\n" \
  "ds_read_b64_tr_b16 v[156:157], %[t1] offset:16384\n" \
  "v_xor_b32 %[t0], 0x11000, %[pb]\n" \
  "ds_read_b64_tr_b16 v[12:13], %[t0] offset:0\n" \
  "ds_read_b64_tr_b16 v[116:117], %[t0] offset:16384\n" \
  "v_xor_b32 %[t1], 0x11020, %[pb]\n" \
  "ds_read_b64_tr_b16 v[60:61], %[t1] offset:0\n" \
  "ds_read_b64_tr_b16 v[164:165], %[t1] offset:16384\n" \
  "v_xor_b32 %[t0], 0x12000, %[pb]\n" \
  "ds_read_b64_tr_b16 v[28:29], %[t0] offset:0\n" \
  "ds_read_b64_tr_b16 v[124:125], %[t0] offset:16384\n" \
  "v_xor_b32 %[t1], 0x12020, %[pb]\n" \
  "ds_read_b64_tr_b16 v[76:77], %[t1] offset:0\n" \
  "ds_read_b64_tr_b16 v[172:173], %[t1] offset:16384\n" \
  "v_xor_b32 %[t0], 0x13000, %[pb]\n" \
  "ds_read_b64_tr_b16 v[44:45], %[t0] offset:0\n" \
  "ds_read_b64_tr_b16 v[140:141], %[t0] offset:16384\n" \
  "v_xor_b32 %[t1], 0x13020, %[pb]\n" \
  "ds_read_b64_tr_b16 v[92:93], %[t1] offset:0\n" \
  "ds_read_b64_tr_b16 v[188:189], %[t1] offset:16384\n" \
  ".Ls_%=:\n" \
  "s_waitcnt lgkmcnt(0)\n"
#define PRO0_13 \
  "v_add_u32 %[t0], 0x10000, %[b0]\n" \
  "ds_write_b128 %[t0], v[192:195]\n" \
  "ds_write_b128 %[t0], v[196:199] offset:1024\n" \
  "v_add_u32 %[t0], 0x14000, %[b0]\n" \
  "ds_write_b128 %[t0], v[200:203]\n" \
  "ds_write_b128 %[t0], v[204:207] offset:1024\n" \
  "s_cmp_lg_u32 %[hb], 1\n" \
  "s_cbranch_scc1 .Ls_%=\n" \
  "v_xor_b32 %[t0], 0x18000, %[pb]\n" \
  "ds_read_b64_tr_b16 v[228:229], %[t0] offset:0\n" \
  "ds_read_b64_tr_b16 v[108:109], %[t0] offset:16384\n" \
  "v_xor_b32 %[t1], 0x18020, %[pb]\n" \
  "ds_read_b64_tr_b16 v[236:237], %[t1] offset:0\n" \
  "ds_read_b64_tr_b16 v[156:157], %[t1] offset:16384\n" \
  "v_xor_b32 %[t0], 0x19000, %[pb]\n" \
  "ds_read_b64_tr_b16 v[12:13], %[t0] offset:0\n" \
  "ds_read_b64_tr_b16 v[116:117], %[t0] offset:16384\n" \
  "v_xor_b32 %[t1], 0x19020, %[pb]\n" \
  "ds_read_b64_tr_b16 v[60:61], %[t1] offset:0\n" \
  "ds_read_b64_tr_b16 v[164:165], %[t1] offset:16384\n" \
  "v_xor_b32 %[t0], 0x1a000, %[pb]\n" \
  "ds_read_b64_tr_b16 v[28:29], %[t0] offset:0\n" \
  "ds_read_b64_tr_b16 v[124:125], %[t0] offset:16384\n" \
  "v_xor_b32 %[t1], 0x1a020, %[pb]\n" \
  "ds_read_b64_tr_b16 v[76:77], %[t1] offset:0\n" \
  "ds_read_b64_tr_b16 v[172:173], %[t1] offset:16384\n" \
  "v_xor_b32 %[t0], 0x1b000, %[pb]\n" \
  "ds_read_b64_tr_b16 v[44:45], %[t0] offset:0\n" \
  "ds_read_b64_tr_b16 v[140:141], %[t0] offset:16384\n" \
  "v_xor_b32 %[t1], 0x1b020, %[pb]\n" \
  "ds_read_b64_tr_b16 v[92:93], %[t1] offset:0\n" \
  "ds_read_b64_tr_b16 v[188:189], %[t1] offset:16384\n" \
  ".Ls_%=:\n" \
  "s_waitcnt lgkmcnt(0)\n"
#define PRO0_14 \
  "v_add_u32 %[t0], 0x18000, %[b0]\n" \
  "ds_write_b128 %[t0], v[208:211]\n" \
  "ds_write_b128 %[t0], v[212:215] offset:1024\n" \
  "v_add_u32 %[t0], 0x1c000, %[b0]\n" \
  "ds_write_b128 %[t0], v[216:219]\n" \
  "ds_write_b128 %[t0], v[220:223] offset:1024\n" \
  "s_cmp_lg_u32 %[hb], 0\n" \
  "s_cbranch_scc1 .Ls_%=\n" \
  "v_xor_b32 %[t0], 0x20000, %[pb]\n" \
  "ds_read_b64_tr_b16 v[230:231], %[t0] offset:0\n" \
  "ds_read_b64_tr_b16 v[110:111], %[t0] offset:16384\n" \
  "v_xor_b32 %[t1], 0x20020, %[pb]\n" \
  "ds_read_b64_tr_b16 v[238:239], %[t1] offset:0\n" \
  "ds_read_b64_tr_b16 v[158:159], %[t1] offset:16384\n" \
  "v_xor_b32 %[t0], 0x21000, %[pb]\n" \
  "ds_read_b64_tr_b16 v[14:15], %[t0] offset:0\n" \
  "ds_read_b64_tr_b16 v[118:119], %[t0] offset:16384\n" \
  "v_xor_b32 %[t1], 0x21020, %[pb]\n" \
  "ds_read_b64_tr_b16 v[62:63], %[t1] offset:0\n" \
  "ds_read_b64_tr_b16 v[166:167], %[t1] offset:16384\n" \
  "v_xor_b32 %[t0], 0x22000, %[pb]\n" \
  "ds_read_b64_tr_b16 v[30:31], %[t0] offset:0\n" \
  "ds_read_b64_tr_b16 v[126:127], %[t0] offset:16384\n" \
  "v_xor_b32 %[t1], 0x22020, %[pb]\n" \
  "ds_read_b64_tr_b16 v[78:79], %[t1] offset:0\n" \
  "ds_read_b64_tr_b16 v[174:175], %[t1] offset:16384\n" \
  "v_xor_b32 %[t0], 0x23000, %[pb]\n" \
  "ds_read_b64_tr_b16 v[46:47], %[t0] offset:0\n" \
  "ds_read_b64_tr_b16 v[142:143], %[t0] offset:16384\n" \
  "v_xor_b32 %[t1], 0x23020, %[pb]\n" \
  "ds_read_b64_tr_b16 v[94:95], %[t1] offset:0\n" \
  "ds_read_b64_tr_b16 v[190:191], %[t1] offset:16384\n" \
  ".Ls_%=:\n" \
  "s_waitcnt lgkmcnt(0)\n"
#define PRO0_15 \
  "s_cmp_lg_u32 %[hb], 1\n" \
  "s_cbranch_scc1 .Ls_%=\n" \
  "v_xor_b32 %[t0], 0x0, %[pb]\n" \
  "ds_read_b64_tr_b16 v[230:231], %[t0] offset:0\n" \
  "ds_read_b64_tr_b16 v[110:111], %[t0] offset:16384\n" \
  "v_xor_b32 %[t1], 0x20, %[pb]\n" \
  "ds_read_b64_tr_b16 v[238:239], %[t1] offset:0\n" \
  "ds_read_b64_tr_b16 v[158:159], %[t1] offset:16384\n" \
  "v_xor_b32 %[t0], 0x1000, %[pb]\n" \
  "ds_read_b64_tr_b16 v[14:15], %[t0] offset:0\n" \
  "ds_read_b64_tr_b16 v[118:119], %[t0] offset:16384\n" \
  "v_xor_b32 %[t1], 0x1020, %[pb]\n" \
  "ds_read_b64_tr_b16 v[62:63], %[t1] offset:0\n" \
  "ds_read_b64_tr_b16 v[166:167], %[t1] offset:16384\n" \
  "v_xor_b32 %[t0], 0x2000, %[pb]\n" \
  "ds_read_b64_tr_b16 v[30:31], %[t0] offset:0\n" \
  "ds_read_b64_tr_b16 v[126:127], %[t0] offset:16384\n" \
  "v_xor_b32 %[t1], 0x2020, %[pb]\n" \
  "ds_read_b64_tr_b16 v[78:79], %[t1] offset:0\n" \
  "ds_read_b64_tr_b16 v[174:175], %[t1] offset:16384\n" \
  "v_xor_b32 %[t0], 0x3000, %[pb]\n" \
  "ds_read_b64_tr_b16 v[46:47], %[t0] offset:0\n" \
  "ds_read_b64_tr_b16 v[142:143], %[t0] offset:16384\n" \
  "v_xor_b32 %[t1], 0x3020, %[pb]\n" \
  "ds_read_b64_tr_b16 v[94:95], %[t1] offset:0\n" \
  "ds_read_b64_tr_b16 v[190:191], %[t1] offset:16384\n" \
  ".Ls_%=:\n" \
  "s_waitcnt lgkmcnt(0)\n" \
  "v_add_u32 %[t0], 0x20000, %[b0]\n" \
  "ds_write_b128 %[t0], v[224:227]\n" \
  "ds_write_b128 %[t0], v[228:231] offset:1024\n" \
  "v_add_u32 %[t0], 0x24000, %[b0]\n" \
  "ds_write_b128 %[t0], v[232:235]\n" \
  "ds_write_b128 %[t0], v[236:239] offset:1024\n"
#define PRO1_0 \
  "v_add_u32 %[t0], 0x0, %[pa]\n" \
  "ds_read_b64_tr_b16 v[96:97], %[t0] offset:0\n" \
  "ds_read_b64_tr_b16 v[100:101], %[t0] offset:1024\n" \
  "ds_read_b64_tr_b16 v[104:105], %[t0] offset:8192\n" \
  "ds_read_b64_tr_b16 v[108:109], %[t0] offset:9216\n" \
  "s_cmp_lg_u32 %[hb], 0\n" \
  "s_cbranch_scc1 .Ls_%=\n" \
  "v_xor_b32 %[t0], 0x0, %[pb]\n" \
  "ds_read_b64_tr_b16 v[192:193], %[t0] offset:0\n" \
  "v_xor_b32 %[t1], 0x80, %[pb]\n" \
  "ds_read_b64_tr_b16 v[200:201], %[t1] offset:0\n" \
  "v_xor_b32 %[t0], 0x1000, %[pb]\n" \
  "ds_read_b64_tr_b16 v[0:1], %[t0] offset:0\n" \
  "v_xor_b32 %[t1], 0x1080, %[pb]\n" \
  "ds_read_b64_tr_b16 v[48:49], %[t1] offset:0\n" \
  "v_xor_b32 %[t0], 0x2000, %[pb]\n" \
  "ds_read_b64_tr_b16 v[16:17], %[t0] offset:0\n" \
  "v_xor_b32 %[t1], 0x2080, %[pb]\n" \
  "ds_read_b64_tr_b16 v[64:65], %[t1] offset:0\n" \
  "v_xor_b32 %[t0], 0x3000, %[pb]\n" \
  "ds_read_b64_tr_b16 v[32:33], %[t0] offset:0\n" \
  "v_xor_b32 %[t1], 0x3080, %[pb]\n" \
  "ds_read_b64_tr_b16 v[80:81], %[t1] offset:0\n" \
  ".Ls_%=:\n" \
  "s_waitcnt lgkmcnt(0)\n"
#define PRO1_1 \
  "v_add_u32 %[t0], 0x8000, %[pa]\n" \
  "ds_read_b64_tr_b16 v[98:99], %[t0] offset:0\n" \
  "ds_read_b64_tr_b16 v[102:103], %[t0] offset:1024\n" \
  "ds_read_b64_tr_b16 v[106:107], %[t0] offset:8192\n" \
  "ds_read_b64_tr_b16 v[110:111], %[t0] offset:9216\n" \
  "s_cmp_lg_u32 %[hb], 1\n" \
  "s_cbranch_scc1 .Ls_%=\n" \
  "v_xor_b32 %[t0], 0x8000, %[pb]\n" \
  "ds_read_b64_tr_b16 v[192:193], %[t0] offset:0\n" \
  "v_xor_b32 %[t1], 0x8080, %[pb]\n" \
  "ds_read_b64_tr_b16 v[200:201], %[t1] offset:0\n" \
  "v_xor_b32 %[t0], 0x9000, %[pb]\n" \
  "ds_read_b64_tr_b16 v[0:1], %[t0] offset:0\n" \
  "v_xor_b32 %[t1], 0x9080, %[pb]\n" \
  "ds_read_b64_tr_b16 v[48:49], %[t1] offset:0\n" \
  "v_xor_b32 %[t0], 0xa000, %[pb]\n" \
  "ds_read_b64_tr_b16 v[16:17], %[t0] offset:0\n" \
  "v_xor_b32 %[t1], 0xa080, %[pb]\n" \
  "ds_read_b64_tr_b16 v[64:65], %[t1] offset:0\n" \
  "v_xor_b32 %[t0], 0xb000, %[pb]\n" \
  "ds_read_b64_tr_b16 v[32:33], %[t0] offset:0\n" \
  "v_xor_b32 %[t1], 0xb080, %[pb]\n" \
  "ds_read_b64_tr_b16 v[80:81], %[t1] offset:0\n" \
  ".Ls_%=:\n" \
  "s_waitcnt lgkmcnt(0)\n" \
  "v_swap_b32 v97, v98\n" \
  "v_swap_b32 v101, v102\n" \
  "v_swap_b32 v105, v106\n" \
  "v_swap_b32 v109, v110\n"
#define PRO1_2 \
  "v_add_u32 %[t0], 0x10000, %[pa]\n" \
  "ds_read_b64_tr_b16 v[112:113], %[t0] offset:0\n" \
  "ds_read_b64_tr_b16 v[116:117], %[t0] offset:1024\n" \
  "ds_read_b64_tr_b16 v[120:121], %[t0] offset:8192\n" \
  "ds_read_b64_tr_b16 v[124:125], %[t0] offset:9216\n" \
  "s_cmp_lg_u32 %[hb], 0\n" \
  "s_cbranch_scc1 .Ls_%=\n" \
  "v_xor_b32 %[t0], 0x10000, %[pb]\n" \
  "ds_read_b64_tr_b16 v[194:195], %[t0] offset:0\n" \
  "v_xor_b32 %[t1], 0x10080, %[pb]\n" \
  "ds_read_b64_tr_b16 v[202:203], %[t1] offset:0\n" \
  "v_xor_b32 %[t0], 0x11000, %[pb]\n" \
  "ds_read_b64_tr_b16 v[2:3], %[t0] offset:0\n" \
  "v_xor_b32 %[t1], 0x11080, %[pb]\n" \
  "ds_read_b64_tr_b16 v[50:51], %[t1] offset:0\n" \
  "v_xor_b32 %[t0], 0x12000, %[pb]\n" \
  "ds_read_b64_tr_b16 v[18:19], %[t0] offset:0\n" \
  "v_xor_b32 %[t1], 0x12080, %[pb]\n" \
  "ds_read_b64_tr_b16 v[66:67], %[t1] offset:0\n" \
  "v_xor_b32 %[t0], 0x13000, %[pb]\n" \
  "ds_read_b64_tr_b16 v[34:35], %[t0] offset:0\n" \
  "v_xor_b32 %[t1], 0x13080, %[pb]\n" \
  "ds_read_b64_tr_b16 v[82:83], %[t1] offset:0\n" \
  ".Ls_%=:\n" \
  "s_waitcnt lgkmcnt(0)\n"
#define PRO1_3 \
  "v_add_u32 %[t0], 0x18000, %[pa]\n" \
  "ds_read_b64_tr_b16 v[114:115], %[t0] offset:0\n" \
  "ds_read_b64_tr_b16 v[118:119], %[t0] offset:1024\n" \
  "ds_read_b64_tr_b16 v[122:123], %[t0] offset:8192\n" \
  "ds_read_b64_tr_b16 v[126:127], %[t0] offset:9216\n" \
  "s_cmp_lg_u32 %[hb], 1\n" \
  "s_cbranch_scc1 .Ls_%=\n" \
  "v_xor_b32 %[t0], 0x18000, %[pb]\n" \
  "ds_read_b64_tr_b16 v[194:195], %[t0] offset:0\n" \
  "v_xor_b32 %[t1], 0x18080, %[pb]\n" \
  "ds_read_b64_tr_b16 v[202:203], %[t1] offset:0\n" \
  "v_xor_b32 %[t0], 0x19000, %[pb]\n" \
  "ds_read_b64_tr_b16 v[2:3], %[t0] offset:0\n" \
  "v_xor_b32 %[t1], 0x19080, %[pb]\n" \
  "ds_read_b64_tr_b16 v[50:51], %[t1] offset:0\n" \
  "v_xor_b32 %[t0], 0x1a000, %[pb]\n" \
  "ds_read_b64_tr_b16 v[18:19], %[t0] offset:0\n" \
  "v_xor_b32 %[t1], 0x1a080, %[pb]\n" \
  "ds_read_b64_tr_b16 v[66:67], %[t1] offset:0\n" \
  "v_xor_b32 %[t0], 0x1b000, %[pb]\n" \
  "ds_read_b64_tr_b16 v[34:35], %[t0] offset:0\n" \
  "v_xor_b32 %[t1], 0x1b080, %[pb]\n" \
  "ds_read_b64_tr_b16 v[82:83], %[t1] offset:0\n" \
  ".Ls_%=:\n" \
  "s_waitcnt lgkmcnt(0)\n" \
  "v_swap_b32 v113, v114\n" \
  "v_swap_b32 v117, v118\n" \
  "v_swap_b32 v121, v122\n" \
  "v_swap_b32 v125, v126\n"
#define PRO1_4 \
  "v_add_u32 %[t0], 0x20000, %[pa]\n" \
  "ds_read_b64_tr_b16 v[128:129], %[t0] offset:0\n" \
  "ds_read_b64_tr_b16 v[132:133], %[t0] offset:1024\n" \
  "ds_read_b64_tr_b16 v[136:137], %[t0] offset:8192\n" \
  "ds_read_b64_tr_b16 v[140:141], %[t0] offset:9216\n" \
  "s_cmp_lg_u32 %[hb], 0\n" \
  "s_cbranch_scc1 .Ls_%=\n" \
  "v_xor_b32 %[t0], 0x20000, %[pb]\n" \
  "ds_read_b64_tr_b16 v[196:197], %[t0] offset:0\n" \
  "v_xor_b32 %[t1], 0x20080, %[pb]\n" \
  "ds_read_b64_tr_b16 v[204:205], %[t1] offset:0\n" \
  "v_xor_b32 %[t0], 0x21000, %[pb]\n" \
  "ds_read_b64_tr_b16 v[4:5], %[t0] offset:0\n" \
  "v_xor_b32 %[t1], 0x21080, %[pb]\n" \
  "ds_read_b64_tr_b16 v[52:53], %[t1] offset:0\n" \
  "v_xor_b32 %[t0], 0x22000, %[pb]\n" \
  "ds_read_b64_tr_b16 v[20:21], %[t0] offset:0\n" \
  "v_xor_b32 %[t1], 0x22080, %[pb]\n" \
  "ds_read_b64_tr_b16 v[68:69], %[t1] offset:0\n" \
  "v_xor_b32 %[t0], 0x23000, %[pb]\n" \
  "ds_read_b64_tr_b16 v[36:37], %[t0] offset:0\n" \
  "v_xor_b32 %[t1], 0x23080, %[pb]\n" \
  "ds_read_b64_tr_b16 v[84:85], %[t1] offset:0\n" \
  ".Ls_%=:\n" \
  "s_waitcnt lgkmcnt(0)\n"
#define PRO1_5 \
  "v_add_u32 %[t0], 0x0, %[pa]\n" \
  "ds_read_b64_tr_b16 v[130:131], %[t0] offset:0\n" \
  "ds_read_b64_tr_b16 v[134:135], %[t0] offset:1024\n" \
  "ds_read_b64_tr_b16 v[138:139], %[t0] offset:8192\n" \
  "ds_read_b64_tr_b16 v[142:143], %[t0] offset:9216\n" \
  "s_cmp_lg_u32 %[hb], 1\n" \
  "s_cbranch_scc1 .Ls_%=\n" \
  "v_xor_b32 %[t0], 0x0, %[pb]\n" \
  "ds_read_b64_tr_b16 v[196:197], %[t0] offset:0\n" \
  "v_xor_b32 %[t1], 0x80, %[pb]\n" \
  "ds_read_b64_tr_b16 v[204:205], %[t1] offset:0\n" \
  "v_xor_b32 %[t0], 0x1000, %[pb]\n" \
  "ds_read_b64_tr_b16 v[4:5], %[t0] offset:0\n" \
  "v_xor_b32 %[t1], 0x1080, %[pb]\n" \
  "ds_read_b64_tr_b16 v[52:53], %[t1] offset:0\n" \
  "v_xor_b32 %[t0], 0x2000, %[pb]\n" \
  "ds_read_b64_tr_b16 v[20:21], %[t0] offset:0\n" \
  "v_xor_b32 %[t1], 0x2080, %[pb]\n" \
  "ds_read_b64_tr_b16 v[68:69], %[t1] offset:0\n" \
  "v_xor_b32 %[t0], 0x3000, %[pb]\n" \
  "ds_read_b64_tr_b16 v[36:37], %[t0] offset:0\n" \
  "v_xor_b32 %[t1], 0x3080, %[pb]\n" \
  "ds_read_b64_tr_b16 v[84:85], %[t1] offset:0\n" \
  ".Ls_%=:\n" \
  "s_waitcnt lgkmcnt(0)\n" \
  "v_swap_b32 v129, v130\n" \
  "v_swap_b32 v133, v134\n" \
  "v_swap_b32 v137, v138\n" \
  "v_swap_b32 v141, v142\n"
#define PRO1_6 \
  "v_add_u32 %[t0], 0x8000, %[pa]\n" \
  "ds_read_b64_tr_b16 v[144:145], %[t0] offset:0\n" \
  "ds_read_b64_tr_b16 v[148:149], %[t0] offset:1024\n" \
  "ds_read_b64_tr_b16 v[152:153], %[t0] offset:8192\n" \
  "ds_read_b64_tr_b16 v[156:157], %[t0] offset:9216\n" \
  "s_cmp_lg_u32 %[hb], 0\n" \
  "s_cbranch_scc1 .Ls_%=\n" \
  "v_xor_b32 %[t0], 0x8000, %[pb]\n" \
  "ds_read_b64_tr_b16 v[198:199], %[t0] offset:0\n" \
  "v_xor_b32 %[t1], 0x8080, %[pb]\n" \
  "ds_read_b64_tr_b16 v[206:207], %[t1] offset:0\n" \
  "v_xor_b32 %[t0], 0x9000, %[pb]\n" \
  "ds_read_b64_tr_b16 v[6:7], %[t0] offset:0\n" \
  "v_xor_b32 %[t1], 0x9080, %[pb]\n" \
  "ds_read_b64_tr_b16 v[54:55], %[t1] offset:0\n" \
  "v_xor_b32 %[t0], 0xa000, %[pb]\n" \
  "ds_read_b64_tr_b16 v[22:23], %[t0] offset:0\n" \
  "v_xor_b32 %[t1], 0xa080, %[pb]\n" \
  "ds_read_b64_tr_b16 v[70:71], %[t1] offset:0\n" \
  "v_xor_b32 %[t0], 0xb000, %[pb]\n" \
  "ds_read_b64_tr_b16 v[38:39], %[t0] offset:0\n" \
  "v_xor_b32 %[t1], 0xb080, %[pb]\n" \
  "ds_read_b64_tr_b16 v[86:87], %[t1] offset:0\n" \
  ".Ls_%=:\n" \
  "s_waitcnt lgkmcnt(0)\n"
#define PRO1_7 \
  "v_add_u32 %[t0], 0x10000, %[pa]\n" \
  "ds_read_b64_tr_b16 v[146:147], %[t0] offset:0\n" \
  "ds_read_b64_tr_b16 v[150:151], %[t0] offset:1024\n" \
  "ds_read_b64_tr_b16 v[154:155], %[t0] offset:8192\n" \
  "ds_read_b64_tr_b16 v[158:159], %[t0] offset:9216\n" \
  "s_cmp_lg_u32 %[hb], 1\n" \
  "s_cbranch_scc1 .Ls_%=\n" \
  "v_xor_b32 %[t0], 0x10000, %[pb]\n" \
  "ds_read_b64_tr_b16 v[198:199], %[t0] offset:0\n" \
  "v_xor_b32 %[t1], 0x10080, %[pb]\n" \
  "ds_read_b64_tr_b16 v[206:207], %[t1] offset:0\n" \
  "v_xor_b32 %[t0], 0x11000, %[pb]\n" \
  "ds_read_b64_tr_b16 v[6:7], %[t0] offset:0\n" \
  "v_xor_b32 %[t1], 0x11080, %[pb]\n" \
  "ds_read_b64_tr_b16 v[54:55], %[t1] offset:0\n" \
  "v_xor_b32 %[t0], 0x12000, %[pb]\n" \
  "ds_read_b64_tr_b16 v[22:23], %[t0] offset:0\n" \
  "v_xor_b32 %[t1], 0x12080, %[pb]\n" \
  "ds_read_b64_tr_b16 v[70:71], %[t1] offset:0\n" \
  "v_xor_b32 %[t0], 0x13000, %[pb]\n" \
  "ds_read_b64_tr_b16 v[38:39], %[t0] offset:0\n" \
  "v_xor_b32 %[t1], 0x13080, %[pb]\n" \
  "ds_read_b64_tr_b16 v[86:87], %[t1] offset:0\n" \
  ".Ls_%=:\n" \
  "s_waitcnt lgkmcnt(0)\n" \
  "v_swap_b32 v145, v146\n" \
  "v_swap_b32 v149, v150\n" \
  "v_swap_b32 v153, v154\n" \
  "v_swap_b32 v157, v158\n"
#define PRO1_8 \
  "v_add_u32 %[t0], 0x18000, %[pa]\n" \
  "ds_read_b64_tr_b16 v[160:161], %[t0] offset:0\n" \
  "ds_read_b64_tr_b16 v[164:165], %[t0] offset:1024\n" \
  "ds_read_b64_tr_b16 v[168:169], %[t0] offset:8192\n" \
  "ds_read_b64_tr_b16 v[172:173], %[t0] offset:9216\n" \
  "s_cmp_lg_u32 %[hb], 0\n" \
  "s_cbranch_scc1 .Ls_%=\n" \
  "v_xor_b32 %[t0], 0x18000, %[pb]\n" \
  "ds_read_b64_tr_b16 v[208:209], %[t0] offset:0\n" \
  "v_xor_b32 %[t1], 0x18080, %[pb]\n" \
  "ds_read_b64_tr_b16 v[216:217], %[t1] offset:0\n" \
  "v_xor_b32 %[t0], 0x19000, %[pb]\n" \
  "ds_read_b64_tr_b16 v[8:9], %[t0] offset:0\n" \
  "v_xor_b32 %[t1], 0x19080, %[pb]\n" \
  "ds_read_b64_tr_b16 v[56:57], %[t1] offset:0\n" \
  "v_xor_b32 %[t0], 0x1a000, %[pb]\n" \
  "ds_read_b64_tr_b16 v[24:25], %[t0] offset:0\n" \
  "v_xor_b32 %[t1], 0x1a080, %[pb]\n" \
  "ds_read_b64_tr_b16 v[72:73], %[t1] offset:0\n" \
  "v_xor_b32 %[t0], 0x1b000, %[pb]\n" \
  "ds_read_b64_tr_b16 v[40:41], %[t0] offset:0\n" \
  "v_xor_b32 %[t1], 0x1b080, %[pb]\n" \
  "ds_read_b64_tr_b16 v[88:89], %[t1] offset:0\n" \
  ".Ls_%=:\n" \
  "s_waitcnt lgkmcnt(0)\n"
#define PRO1_9 \
  "v_add_u32 %[t0], 0x20000, %[pa]\n" \
  "ds_read_b64_tr_b16 v[162:163], %[t0] offset:0\n" \
  "ds_read_b64_tr_b16 v[166:167], %[t0] offset:1024\n" \
  "ds_read_b64_tr_b16 v[170:171], %[t0] offset:8192\n" \
  "ds_read_b64_tr_b16 v[174:175], %[t0] offset:9216\n" \
  "s_cmp_lg_u32 %[hb], 1\n" \
  "s_cbranch_scc1 .Ls_%=\n" \
  "v_xor_b32 %[t0], 0x20000, %[pb]\n" \
  "ds_read_b64_tr_b16 v[208:209], %[t0] offset:0\n" \
  "v_xor_b32 %[t1], 0x20080, %[pb]\n" \
  "ds_read_b64_tr_b16 v[216:217], %[t1] offset:0\n" \
  "v_xor_b32 %[t0], 0x21000, %[pb]\n" \
  "ds_read_b64_tr_b16 v[8:9], %[t0] offset:0\n" \
  "v_xor_b32 %[t1], 0x21080, %[pb]\n" \
  "ds_read_b64_tr_b16 v[56:57], %[t1] offset:0\n" \
  "v_xor_b32 %[t0], 0x22000, %[pb]\n" \
  "ds_read_b64_tr_b16 v[24:25], %[t0] offset:0\n" \
  "v_xor_b32 %[t1], 0x22080, %[pb]\n" \
  "ds_read_b64_tr_b16 v[72:73], %[t1] offset:0\n" \
  "v_xor_b32 %[t0], 0x23000, %[pb]\n" \
  "ds_read_b64_tr_b16 v[40:41], %[t0] offset:0\n" \
  "v_xor_b32 %[t1], 0x23080, %[pb]\n" \
  "ds_read_b64_tr_b16 v[88:89], %[t1] offset:0\n" \
  ".Ls_%=:\n" \
  "s_waitcnt lgkmcnt(0)\n" \
  "v_swap_b32 v161, v162\n" \
  "v_swap_b32 v165, v166\n" \
  "v_swap_b32 v169, v170\n" \
  "v_swap_b32 v173, v174\n"
#define PRO1_10 \
  "v_add_u32 %[t0], 0x0, %[pa]\n" \
  "ds_read_b64_tr_b16 v[176:177], %[t0] offset:0\n" \
  "ds_read_b64_tr_b16 v[180:181], %[t0] offset:1024\n" \
  "ds_read_b64_tr_b16 v[184:185], %[t0] offset:8192\n" \
  "ds_read_b64_tr_b16 v[188:189], %[t0] offset:9216\n" \
  "s_cmp_lg_u32 %[hb], 0\n" \
  "s_cbranch_scc1 .Ls_%=\n" \
  "v_xor_b32 %[t0], 0x0, %[pb]\n" \
  "ds_read_b64_tr_b16 v[210:211], %[t0] offset:0\n" \
  "v_xor_b32 %[t1], 0x80, %[pb]\n" \
  "ds_read_b64_tr_b16 v[218:219], %[t1] offset:0\n" \
  "v_xor_b32 %[t0], 0x1000, %[pb]\n" \
  "ds_read_b64_tr_b16 v[10:11], %[t0] offset:0\n" \
  "v_xor_b32 %[t1], 0x1080, %[pb]\n" \
  "ds_read_b64_tr_b16 v[58:59], %[t1] offset:0\n" \
  "v_xor_b32 %[t0], 0x2000, %[pb]\n" \
  "ds_read_b64_tr_b16 v[26:27], %[t0] offset:0\n" \
  "v_xor_b32 %[t1], 0x2080, %[pb]\n" \
  "ds_read_b64_tr_b16 v[74:75], %[t1] offset:0\n" \
  "v_xor_b32 %[t0], 0x3000, %[pb]\n" \
  "ds_read_b64_tr_b16 v[42:43], %[t0] offset:0\n" \
  "v_xor_b32 %[t1], 0x3080, %[pb]\n" \
  "ds_read_b64_tr_b16 v[90:91], %[t1] offset:0\n" \
  ".Ls_%=:\n" \
  "s_waitcnt lgkmcnt(0)\n"
#define PRO1_11 \
  "v_add_u32 %[t0], 0x8000, %[pa]\n" \
  "ds_read_b64_tr_b16 v[178:179], %[t0] offset:0\n" \
  "ds_read_b64_tr_b16 v[182:183], %[t0] offset:1024\n" \
  "ds_read_b64_tr_b16 v[186:187], %[t0] offset:8192\n" \
  "ds_read_b64_tr_b16 v[190:191], %[t0] offset:9216\n" \
  "s_cmp_lg_u32 %[hb], 1\n" \
  "s_cbranch_scc1 .Ls_%=\n" \
  "v_xor_b32 %[t0], 0x8000, %[pb]\n" \
  "ds_read_b64_tr_b16 v[210:211], %[t0] offset:0\n" \
  "v_xor_b32 %[t1], 0x8080, %[pb]\n" \
  "ds_read_b64_tr_b16 v[218:219], %[t1] offset:0\n" \
  "v_xor_b32 %[t0], 0x9000, %[pb]\n" \
  "ds_read_b64_tr_b16 v[10:11], %[t0] offset:0\n" \
  "v_xor_b32 %[t1], 0x9080, %[pb]\n" \
  "ds_read_b64_tr_b16 v[58:59], %[t1] offset:0\n" \
  "v_xor_b32 %[t0], 0xa000, %[pb]\n" \
  "ds_read_b64_tr_b16 v[26:27], %[t0] offset:0\n" \
  "v_xor_b32 %[t1], 0xa080, %[pb]\n" \
  "ds_read_b64_tr_b16 v[74:75], %[t1] offset:0\n" \
  "v_xor_b32 %[t0], 0xb000, %[pb]\n" \
  "ds_read_b64_tr_b16 v[42:43], %[t0] offset:0\n" \
  "v_xor_b32 %[t1], 0xb080, %[pb]\n" \
  "ds_read_b64_tr_b16 v[90:91], %[t1] offset:0\n" \
  ".Ls_%=:\n" \
  "s_waitcnt lgkmcnt(0)\n" \
  "v_swap_b32 v177, v178\n" \
  "v_swap_b32 v181, v182\n" \
  "v_swap_b32 v185, v186\n" \
  "v_swap_b32 v189, v190\n"
#define PRO1_12 \
  "v_add_u32 %[t0], 0x8000, %[b0]\n" \
  "ds_write_b128 %[t0], v[192:195]\n" \
  "ds_write_b128 %[t0], v[196:199] offset:1024\n" \
  "v_add_u32 %[t0], 0xc000, %[b0]\n" \
  "ds_write_b128 %[t0], v[200:203]\n" \
  "ds_write_b128 %[t0], v[204:207] offset:1024\n" \
  "v_add_u32 %[t0], 0x10000, %[pa]\n" \
  "ds_read_b64_tr_b16 v[224:225], %[t0] offset:0\n" \
  "ds_read_b64_tr_b16 v[228:229], %[t0] offset:1024\n" \
  "ds_read_b64_tr_b16 v[232:233], %[t0] offset:8192\n" \
  "ds_read_b64_tr_b16 v[236:237], %[t0] offset:9216\n" \
  "s_cmp_lg_u32 %[hb], 0\n" \
  "s_cbranch_scc1 .Ls_%=\n" \
  "v_xor_b32 %[t0], 0x10000, %[pb]\n" \
  "ds_read_b64_tr_b16 v[212:213], %[t0] offset:0\n" \
  "v_xor_b32 %[t1], 0x10080, %[pb]\n" \
  "ds_read_b64_tr_b16 v[220:221], %[t1] offset:0\n" \
  "v_xor_b32 %[t0], 0x11000, %[pb]\n" \
  "ds_read_b64_tr_b16 v[12:13], %[t0] offset:0\n" \
  "v_xor_b32 %[t1], 0x11080, %[pb]\n" \
  "ds_read_b64_tr_b16 v[60:61], %[t1] offset:0\n" \
  "v_xor_b32 %[t0], 0x12000, %[pb]\n" \
  "ds_read_b64_tr_b16 v[28:29], %[t0] offset:0\n" \
  "v_xor_b32 %[t1], 0x12080, %[pb]\n" \
  "ds_read_b64_tr_b16 v[76:77], %[t1] offset:0\n" \
  "v_xor_b32 %[t0], 0x13000, %[pb]\n" \
  "ds_read_b64_tr_b16 v[44:45], %[t0] offset:0\n" \
  "v_xor_b32 %[t1], 0x13080, %[pb]\n" \
  "ds_read_b64_tr_b16 v[92:93], %[t1] offset:0\n" \
  ".Ls_%=:\n" \
  "s_waitcnt lgkmcnt(0)\n"
#define PRO1_13 \
  "v_add_u32 %[t0], 0x18000, %[pa]\n" \
  "ds_read_b64_tr_b16 v[226:227], %[t0] offset:0\n" \
  "ds_read_b64_tr_b16 v[230:231], %[t0] offset:1024\n" \
  "ds_read_b64_tr_b16 v[234:235], %[t0] offset:8192\n" \
  "ds_read_b64_tr_b16 v[238:239], %[t0] offset:9216\n" \
  "s_cmp_lg_u32 %[hb], 1\n" \
  "s_cbranch_scc1 .Ls_%=\n" \
  "v_xor_b32 %[t0], 0x18000, %[pb]\n" \
  "ds_read_b64_tr_b16 v[212:213], %[t0] offset:0\n" \
  "v_xor_b32 %[t1], 0x18080, %[pb]\n" \
  "ds_read_b64_tr_b16 v[220:221], %[t1] offset:0\n" \
  "v_xor_b32 %[t0], 0x19000, %[pb]\n" \
  "ds_read_b64_tr_b16 v[12:13], %[t0] offset:0\n" \
  "v_xor_b32 %[t1], 0x19080, %[pb]\n" \
  "ds_read_b64_tr_b16 v[60:61], %[t1] offset:0\n" \
  "v_xor_b32 %[t0], 0x1a000, %[pb]\n" \
  "ds_read_b64_tr_b16 v[28:29], %[t0] offset:0\n" \
  "v_xor_b32 %[t1], 0x1a080, %[pb]\n" \
  "ds_read_b64_tr_b16 v[76:77], %[t1] offset:0\n" \
  "v_xor_b32 %[t0], 0x1b000, %[pb]\n" \
  "ds_read_b64_tr_b16 v[44:45], %[t0] offset:0\n" \
  "v_xor_b32 %[t1], 0x1b080, %[pb]\n" \
  "ds_read_b64_tr_b16 v[92:93], %[t1] offset:0\n" \
  ".Ls_%=:\n" \
  "s_waitcnt lgkmcnt(0)\n" \
  "v_swap_b32 v225, v226\n" \
  "v_swap_b32 v229, v230\n" \
  "v_swap_b32 v233, v234\n" \
  "v_swap_b32 v237, v238\n" \
  "v_add_u32 %[t0], 0x10000, %[b0]\n" \
  "ds_write_b128 %[t0], v[224:227]\n" \
  "ds_write_b128 %[t0], v[228:231] offset:1024\n" \
  "v_add_u32 %[t0], 0x14000, %[b0]\n" \
  "ds_write_b128 %[t0], v[232:235]\n" \
  "ds_write_b128 %[t0], v[236:239] offset:1024\n"
#define PRO1_14 \
  "v_add_u32 %[t0], 0x20000, %[pa]\n" \
  "ds_read_b64_tr_b16 v[224:225], %[t0] offset:0\n" \
  "ds_read_b64_tr_b16 v[228:229], %[t0] offset:1024\n" \
  "ds_read_b64_tr_b16 v[232:233], %[t0] offset:8192\n" \
  "ds_read_b64_tr_b16 v[236:237], %[t0] offset:9216\n" \
  "s_cmp_lg_u32 %[hb], 0\n" \
  "s_cbranch_scc1 .Ls_%=\n" \
  "v_xor_b32 %[t0], 0x20000, %[pb]\n" \
  "ds_read_b64_tr_b16 v[214:215], %[t0] offset:0\n" \
  "v_xor_b32 %[t1], 0x20080, %[pb]\n" \
  "ds_read_b64_tr_b16 v[222:223], %[t1] offset:0\n" \
  "v_xor_b32 %[t0], 0x21000, %[pb]\n" \
  "ds_read_b64_tr_b16 v[14:15], %[t0] offset:0\n" \
  "v_xor_b32 %[t1], 0x21080, %[pb]\n" \
  "ds_read_b64_tr_b16 v[62:63], %[t1] offset:0\n" \
  "v_xor_b32 %[t0], 0x22000, %[pb]\n" \
  "ds_read_b64_tr_b16 v[30:31], %[t0] offset:0\n" \
  "v_xor_b32 %[t1], 0x22080, %[pb]\n" \
  "ds_read_b64_tr_b16 v[78:79], %[t1] offset:0\n" \
  "v_xor_b32 %[t0], 0x23000, %[pb]\n" \
  "ds_read_b64_tr_b16 v[46:47], %[t0] offset:0\n" \
  "v_xor_b32 %[t1], 0x23080, %[pb]\n" \
  "ds_read_b64_tr_b16 v[94:95], %[t1] offset:0\n" \
  ".Ls_%=:\n" \
  "s_waitcnt lgkmcnt(0)\n"
#define PRO1_15 \
  "v_add_u32 %[t0], 0x0, %[pa]\n" \
  "ds_read_b64_tr_b16 v[226:227], %[t0] offset:0\n" \
  "ds_read_b64_tr_b16 v[230:231], %[t0] offset:1024\n" \
  "ds_read_b64_tr_b16 v[234:235], %[t0] offset:8192\n" \
  "ds_read_b64_tr_b16 v[238:239], %[t0] offset:9216\n" \
  "s_cmp_lg_u32 %[hb], 1\n" \
  "s_cbranch_scc1 .Ls_%=\n" \
  "v_xor_b32 %[t0], 0x0, %[pb]\n" \
  "ds_read_b64_tr_b16 v[214:215], %[t0] offset:0\n" \
  "v_xor_b32 %[t1], 0x80, %[pb]\n" \
  "ds_read_b64_tr_b16 v[222:223], %[t1] offset:0\n" \
  "v_xor_b32 %[t0], 0x1000, %[pb]\n" \
  "ds_read_b64_tr_b16 v[14:15], %[t0] offset:0\n" \
  "v_xor_b32 %[t1], 0x1080, %[pb]\n" \
  "ds_read_b64_tr_b16 v[62:63], %[t1] offset:0\n" \
  "v_xor_b32 %[t0], 0x2000, %[pb]\n" \
  "ds_read_b64_tr_b16 v[30:31], %[t0] offset:0\n" \
  "v_xor_b32 %[t1], 0x2080, %[pb]\n" \
  "ds_read_b64_tr_b16 v[78:79], %[t1] offset:0\n" \
  "v_xor_b32 %[t0], 0x3000, %[pb]\n" \
  "ds_read_b64_tr_b16 v[46:47], %[t0] offset:0\n" \
  "v_xor_b32 %[t1], 0x3080, %[pb]\n" \
  "ds_read_b64_tr_b16 v[94:95], %[t1] offset:0\n" \
  ".Ls_%=:\n" \
  "s_waitcnt lgkmcnt(0)\n" \
  "v_swap_b32 v225, v226\n" \
  "v_swap_b32 v229, v230\n" \
  "v_swap_b32 v233, v234\n" \
  "v_swap_b32 v237, v238\n" \
  "v_add_u32 %[t0], 0x18000, %[b0]\n" \
  "ds_write_b128 %[t0], v[224:227]\n" \
  "ds_write_b128 %[t0], v[228:231] offset:1024\n" \
  "v_add_u32 %[t0], 0x1c000, %[b0]\n" \
  "ds_write_b128 %[t0], v[232:235]\n" \
  "ds_write_b128 %[t0], v[236:239] offset:1024\n" \
  "v_add_u32 %[t0], 0x20000, %[b0]\n" \
  "ds_write_b128 %[t0], v[208:211]\n" \
  "ds_write_b128 %[t0], v[212:215] offset:1024\n" \
  "v_add_u32 %[t0], 0x24000, %[b0]\n" \
  "ds_write_b128 %[t0], v[216:219]\n" \
  "ds_write_b128 %[t0], v[220:223] offset:1024\n"
#define VO_CLOB "v192", "v193", "v194", "v195", "v196", "v197", "v198", "v199", "v200", "v201", "v202", "v203", "v204", "v205", "v206", "v207", "v208", "v209", "v210", "v211", "v212", "v213", "v214", "v215", "v216", "v217", "v218", "v219", "v220", "v221", "v222", "v223", "v224", "v225", "v226", "v227", "v228", "v229", "v230", "v231", "v232", "v233", "v234", "v235", "v240", "v241", "v242", "v243", "v244", "v245", "v246", "v247", "v250", "v251", "v252"
#define QK_CLOB "v204", "v205", "v206", "v207", "v208", "v209", "v210", "v211", "v212", "v213", "v214", "v215", "v216", "v217", "v218", "v219", "v220", "v221", "v222", "v223", "v224", "v225", "v226", "v227", "v228", "v229", "v230", "v231", "v236", "v237", "v238", "v239", "v240", "v241", "v242", "v243", "v244", "v245", "v246", "v247"
#define QL_CLOB "v192", "v193", "v194", "v195", "v196", "v197", "v198", "v199", "v200", "v201", "v202", "v203"
#define QH_CLOB "v232", "v233", "v234", "v235"
#define ST_CLOB "v192", "v193", "v194", "v195", "v196", "v197", "v198", "v199", "v200", "v201", "v202", "v203", "v204", "v205", "v206", "v207", "v240", "v241", "v242", "v243", "v244", "v245", "v246", "v247", "v253"
#define XB_CLOB "v192", "v193", "v194", "v195", "v196", "v197", "v198", "v199", "v200", "v201", "v202", "v203", "v204", "v205", "v206", "v207", "v208", "v209", "v210", "v211", "v212", "v213", "v214", "v215", "v216", "v217", "v218", "v219", "v220", "v221", "v222", "v223", "v224", "v225", "v226", "v227", "v228", "v229", "v230", "v231", "v232", "v233", "v234", "v235", "v236", "v237", "v238", "v239", "v240", "v241", "v242", "v243", "v244", "v245", "v246", "v247"
#define OF_CLOB "v32", "v33", "v34", "v35", "v36", "v37", "v38", "v39", "v40", "v41", "v42", "v43", "v44", "v45", "v46", "v47", "v48", "v49", "v50", "v51", "v52", "v53", "v54", "v55", "v56", "v57", "v58", "v59", "v60", "v61", "v62", "v63", "v64", "v65", "v66", "v67", "v68", "v69", "v70", "v71", "v72", "v73", "v74", "v75", "v76", "v77", "v78", "v79", "v80", "v81", "v82", "v83", "v84", "v85", "v86", "v87", "v88", "v89", "v90", "v91", "v92", "v93", "v94", "v95", "v96", "v97", "v98", "v99", "v100", "v101", "v102", "v103"
constexpr int L_W = 32768;
constexpr int L_HI = 16384;
constexpr float CSC = 0.08838834764831845f * 1.4426950408889634f;
#define TWY 3
#define MARG 115
#define ED0 700
#define EA0 1110
#define SDK 360
#define TOL 145
#define XLATE 130
#define TFR 600
#define QT 0
#define HSU 11
#define WNT 1
#define KROT 7
#define VLATE 0
#define VSL 2
#define CG1 18
#define CG2 6
#define KSL2 0
#define KSL 4
#define NCY 1600
#define NCYV NCY
#define NRY 24
#define NRX 31
#define PDU 1
#define PDY 4
#define PDC 6
#define NF1O 64
#define NPUB 2
#define NSER 4
#define NSB 6
#define NSB1 4
#define XTU 20
#define CPU_ 9
#define CPR 12
#define TLP 0
#define CP0 0
#define RSL 2
#define PFL 4
#define EVT2R
template <int role>
__device__ __forceinline__ void wroles(const int h, const int par, const int sl, const int xg, const int w, const u64 nonce, const u16* __restrict__ x,
                                       const u16* __restrict__ Wq, const u16* __restrict__ Wk, const u16* __restrict__ Wv,
                                       const u16* __restrict__ Wo, char* ws) {
  asm volatile(".Lrole%c0:" ::"n"(role));
  LAS char* const L = (LAS char*)g_lds;
  gu32* tmo = (gu32*)(ws + OFF_TMO);
  const __amdgpu_buffer_rsrc_t rs = __builtin_amdgcn_make_buffer_rsrc(ws, 0, (int)WS_USED, 0x00020000);
  constexpr bool vo = role & 1;
  v8u wa[8], wb[16];
  v32u W0, W1, W2, W3, W4, W5;
  constexpr bool fuse = role >= 2;
  constexpr int RG = fuse ? 4 : 5, RB = fuse ? 32768 : 0;
  f32x4 fac[2][2];
  v2u nin = {0u, 0u};
  constexpr int TWV = 5;
  Clk ck = {0u, 0u, 92500u, 46432u};
  const int krot = role >= 2 ? (h & KROT) ^ (par * PX) : 0;
  {
    const int lane = olane();
    const int l15 = lane & 15, g = lane >> 4, trq = l15 >> 2, trp = lane & 3;
    const char* bl = (const char*)((vo ? Wv : Wq) + h * DH);
    const char* br = (const char*)(vo ? Wo + (size_t)h * DH * D : Wk + h * DH);
    const int rot = role == 1 ? (h >> 4) * 4 : 0;
    int vl[2], vr[2];
#pragma unroll
    for (int q = 0; q < 2; ++q) {
      const int row = 4 * (2 * w + q) + g;
      const int cb = ((((l15 >> 1) ^ ((row & 3) | ((row >> 1) & 4))) << 5) | ((l15 & 1) << 4));
      const int i = row & 7;
      const int prow = (row & 32) + ((i & 4) << 2) + ((row >> 1) & 12) + (i & 3);
      vl[q] = role < 2 ? (128 * (w >> 1) + 16 * (w & 1) + 8 * q + g) * 8192 + ((((l15 >> 1) ^ (4 * q + g)) << 5) | ((l15 & 1) << 4))
                       : row * 8192 + cb;
      vr[q] = vo ? (role == 1 ? row : prow) * 2048 + cb : vl[q];
    }
#define W_ISSUE(u)                                                                                   \
  {                                                                                                  \
    const size_t ro = (size_t)(role < 2 ? (8 * ((u) & 1) + ((u) >> 3)) * 64 + (((u) >> 2) & 1) * 32 + (((u) >> 1) & 1) * 4 : ((u) ^ krot) * 64) * 8192; \
    const char* pl = bl + ro;                                                                        \
    const char* pr = br + (role == 1 ? (size_t)(((u) >> 1) & 1) * 64 * 2048 + (2 * ((u) >> 2) + ((u) & 1)) * 256          \
                                     : (vo ? (size_t)((u) & 1) * 64 * 2048 + (((u) ^ (par * PX)) >> 1) * 256 : ro)); \
    _Pragma("unroll") for (int q = 0; q < 2; ++q) {                                                  \
      glds16<((WNT >> (role >> 1)) & 1) != 0>(pl, vl[q], RB + ((u) % RG) * 32768 + (2 * w + q) * 1024);                                \
      glds16<((WNT >> (role >> 1)) & 1) != 0>(pr, vr[q], RB + ((u) % RG) * 32768 + 16384 + (2 * w + q) * 1024);                        \
    }                                                                                                \
  }
    const int ta = role == 1 ? 8 + (w ^ rot) : 2 * w, tb = role < 2 ? (2 - role) * (w & 3) : 2 * w + 1;
    const int sw = trq | ((g & 1) << 2);
    int pa = (ta >> 3) * 16384 + (8 * g + trq) * 256 + (((ta & 7) ^ sw) << 5) + trp * 8;
    int pb = (role < 2 ? (w & 3) * 4096 + (4 * g + trq) * 256 : (tb >> 3) * 16384 + (8 * g + trq) * 256) + (((tb & 7) ^ sw) << 5) + trp * 8;
    v16u K0, K1, K2;
    if constexpr (role < 2)
      asm volatile("" : "={v[0:31]}"(W0), "={v[32:63]}"(W1), "={v[64:95]}"(W2), "={v[96:127]}"(W3), "={v[128:159]}"(W4), "={v[160:191]}"(W5),
                   "={v[192:207]}"(K0), "={v[208:223]}"(K1), "={v[224:239]}"(K2));
    const int b0 = lane * 16 + (w ^ 4) * 2048;
    const int pw = L_W + w * 2048 + lane * 16;
#define W_FRAG(dst, p, u)                                                                            \
  {                                                                                                  \
    int p_ = p + RB + ((u) % RG) * 32768;                                                             \
    asm volatile("" : "+v"(p_));                                                                     \
    const v2u t0 = trd(L + p_), t1 = trd(L + p_ + 1024), t2 = trd(L + p_ + 8192), t3 = trd(L + p_ + 9216); \
    dst = v8u{t0[0], t0[1], t1[0], t1[1], t2[0], t2[1], t3[0], t3[1]};                               \
  }
#define W_PUT(c)                                           \
  {                                                        \
    int p_ = pw + ((c) - 8) * 16384;                       \
    asm volatile("" : "+v"(p_));                           \
    *(LAS v4u*)(L + p_) = lo4(tmp[(c) - 8]);               \
    *(LAS v4u*)(L + p_ + 1024) = hi4(tmp[(c) - 8]);        \
  }
#define PT(i)
#define PT4(i)
    PT(0);
    const int fxv = ((w & 1) + 2 * (lane >> 2)) * 2048 + (((lane ^ -g) & 3) << 4) + ((w >> 1) & 1) * 64;
    const int fxr = l15 * 64 + (((g ^ -trq) & 3) << 4);
    constexpr int XS = role == 3;
#define X_ISSUE(j) glds16((const char*)x + par * 65536 + ((2 * (j) + (w >> 2)) ^ krot) * 128, fxv, (((j) + XS) & 3) * 8192 + w * 1024)
    if (TLP && w >= 6 + vo) {
      unsigned keep;
      asm volatile("s_mov_b32 %0, m0\n\ts_mov_b32 m0, %3\n\ts_nop 0\n\tglobal_load_lds_dword %1, %2\n\ts_mov_b32 m0, %0"
                   : "=&s"(keep) : "v"((olane() & (TLP - 1)) * (8388608 / (TLP ? TLP : 1))), "s"(w == 7 ? bl : br), "s"(RB + w * 2048) : "memory");
    }
    W_ISSUE(0);
    if (((CP0 >> role) & 1) && w == 1) code_ahead(sl + 8 * par, RB + 32768 + 2048);
    if (fuse) {
#pragma unroll
      for (int j = 0; j < 4; ++j) X_ISSUE(j);
    }
#pragma unroll
    for (int u = 1; u < RG; ++u) {
      if (u == PDU && role < 3)
        for (int k = role == 2 ? TN(6, PDY) : TN(5, PDC); k > 0; --k) __builtin_amdgcn_s_sleep(8);
      W_ISSUE(u);
    }
    PT(1);
    v8u tmp[8];
#pragma unroll
    for (int u = 0; u < 16; ++u) {
      {
        int young = 4 * (u + RG - 1 < 16 ? RG - 1 : 15 - u);
        if (fuse)
          for (int i = u - RG; i < u; ++i) young += i >= 0 && i < 8 && (i & 1);
        asm volatile("s_waitcnt vmcnt(%0)" ::"n"(young) : "memory");
      }
      RAW_BARRIER();
      if ((u & 3) == 0 || u == 15) PT(2 + (u + 1) / 4);
      if constexpr (role < 2) {
        unsigned t0, t1;
#define PRO_OPS                                                                                                                    \
  : "+{v[0:31]}"(W0), "+{v[32:63]}"(W1), "+{v[64:95]}"(W2), "+{v[96:127]}"(W3), "+{v[128:159]}"(W4), "+{v[160:191]}"(W5),          \
    "+{v[192:207]}"(K0), "+{v[208:223]}"(K1), "+{v[224:239]}"(K2), [t0] "=&v"(t0), [t1] "=&v"(t1)                                  \
  : [b0] "v"(b0), [pa] "v"(pa), [pb] "v"(pb), [hb] "s"(w >> 2)                                                                     \
  : "scc", "memory"
#define PRO_U(n)                                         \
  if (u == n) {                                          \
    if constexpr (role == 0) asm volatile(PRO0_##n PRO_OPS); \
    else asm volatile(PRO1_##n PRO_OPS);                 \
  }
        PRO_U(0) PRO_U(1) PRO_U(2) PRO_U(3) PRO_U(4) PRO_U(5) PRO_U(6) PRO_U(7)
        PRO_U(8) PRO_U(9) PRO_U(10) PRO_U(11) PRO_U(12) PRO_U(13) PRO_U(14) PRO_U(15)
      } else {
        if (u >= 13) { W_PUT(u >= 13 ? 2 * u - 18 : 8); W_PUT(u >= 13 ? 2 * u - 17 : 8); }
        v8u fa, fb;
        W_FRAG(fa, pa, u)
        W_FRAG(fb, pb, u)
        if (u < 8) {
          wa[u & 7] = fa;
          wb[u] = fb;
        } else {
          wb[u] = cat8(lo4(fa), lo4(fb));
          tmp[u & 7] = cat8(hi4(fa), hi4(fb));
        }
#pragma unroll
        for (int hf = 0; hf < 2; ++hf)
#pragma unroll
          for (int rt = 0; rt < 2; ++rt) {
            const v4u xq = *(const LAS v4u*)(L + fxr + ((u + 2 * XS) & 7) * 4096 + hf * 2048 + rt * 1024);
            fac[0][rt] = mma(hf ? hi4(fa) : lo4(fa), xq, u + hf == 0 ? f32x4{0.f, 0.f, 0.f, 0.f} : fac[0][rt]);
            fac[1][rt] = mma(hf ? hi4(fb) : lo4(fb), xq, u + hf == 0 ? f32x4{0.f, 0.f, 0.f, 0.f} : fac[1][rt]);
          }
        asm volatile("" : "+v"(fac[0][0]), "+v"(fac[0][1]), "+v"(fac[1][0]), "+v"(fac[1][1]));
      }
      if (u == HSU && w == 0) hs_wait(rs, ws, nonce, CNT_HS);
      if (((VWN && role == 3) || (QKN && role == 2)) && u == HSU + 2 && w == 7) nin = numa_load(rs, tmo);
      if (role < 2 && u == HSU + 2 && w == 7) {
        arm_wait(rs, tmo);
        OPQ(off, OFF_PERM);
        nin[0] = __builtin_amdgcn_raw_buffer_load_b32(rs, off, 0, AUX_SC1);
      }
      if (role < 2 && u == 14 && w == TWV) clk_init(ck);
      if (u + RG < 16) {
        RAW_BARRIER();
        if (fuse && u == XTU && w < 2) {
          unsigned keep;
          asm volatile("s_mov_b32 %0, m0\n\ts_mov_b32 m0, %3\n\ts_nop 0\n\tglobal_load_lds_dword %1, %2\n\ts_mov_b32 m0, %0"
                       : "=&s"(keep) : "v"(olane() * 128), "s"((const char*)x + (2 * w + 2 + par) * 65536 + sl * 8192), "s"(RB + (u % RG) * 32768 + w * 2048) : "memory");
        }
        if (u == CPU_ && ((CPR >> role) & 1) && w == 1) code_ahead(sl + 8 * par, RB + (u % RG) * 32768 + 2048);
        W_ISSUE(u + RG);
        if (fuse && u < 8 && (u & 1)) X_ISSUE(4 + (u >> 1));
      }
    }
    PT(7);
    static_assert(!QKN, "");
    if (role == 2 || (role == 3 && w < 4)) {
      const int l16 = olane() * 16;
#pragma unroll
      for (int rt = 0; rt < 2; ++rt) {
        const float ks = role == 2 && w >= 4 ? CSC : 1.f;
        const f32x4 f0 = fac[0][rt] * ks, f1 = fac[1][rt] * ks;
        const v4u o = {pk2(f0[0], f0[1]), pk2(f0[2], f0[3]), pk2(f1[0], f1[1]), pk2(f1[2], f1[3])};
        if (role == 3) *(LAS v4u*)(L + 24576 + rt * 4096 + w * 1024 + l16) = o;
        else st128(v4u{o[0], o[2], o[1], o[3]}, rs, l16, qk_addr(w >> 2, par, h, 0, 0) + rt * QK_RT + (w & 3) * 1024);
      }
    }
    RAW_BARRIER();
    PT(8);
    if (fuse) { W_PUT(14); W_PUT(15); }
  }
  const int lane = olane();
  const int l15 = lane & 15, g = lane >> 4, trq = l15 >> 2, trp = lane & 3;
  int pw0 = L_W + w * 2048 + lane * 16, pw1 = pw0 + 65536;
  asm volatile("" : "+v"(pw0), "+v"(pw1));
#define WA_P(c) (L + ((c) < 12 ? pw0 : pw1) + ((c) & 3) * 16384)
#define WA_LDS(c) cat8(*(const LAS v4u*)WA_P(c), *(const LAS v4u*)(WA_P(c) + 1024))
  const int r_a = (int)(((uintptr_t)ws >> 19) & 7);
  const int r_s = r_a + (int)(OFF_STEP / UNIT);

  typedef int v4i __attribute__((ext_vector_type(4)));
#define WPIN "{v[0:31]}"(W0), "{v[32:63]}"(W1), "{v[64:95]}"(W2), "{v[96:127]}"(W3), "{v[128:159]}"(W4), "{v[160:191]}"(W5)
  const v4u rsv = {(unsigned)(uintptr_t)ws, (unsigned)((uintptr_t)ws >> 32) & 0xffffu, (unsigned)WS_USED, 0x00020000u};
  if constexpr (role >= 2) {
    constexpr bool isx = role == 3;
    W0 = cat32(wa[0], wa[1], wa[2], wa[3]), W1 = cat32(wa[4], wa[5], wa[6], wa[7]), W2 = cat32(wb[0], wb[1], wb[2], wb[3]);
    W3 = cat32(wb[4], wb[5], wb[6], wb[7]), W4 = cat32(wb[8], wb[9], wb[10], wb[11]), W5 = cat32(wb[12], wb[13], wb[14], wb[15]);
    const v4u rxv = {(unsigned)(uintptr_t)x, (unsigned)((uintptr_t)x >> 32) & 0xffffu, (unsigned)(S * D * 2), 0x00020000u};
    v4i adr;
    adr[3] = lane * 16;
    adr[2] = pw0;
    adr[1] = pw1;
    adr[0] = ((w & 1) + 2 * (lane >> 2)) * 2048 + (((lane ^ -g) & 3) << 4) + ((w >> 1) & 1) * 1024 + (isx ? 0 : (w >> 2) * 64);
    const int xr = l15 * 64 + (((g ^ -trq) & 3) << 4);
    constexpr int fo = isx ? F_VW : F_QK;
    const int nrel = isx ? TN(8, NRX) : TN(7, NRY);
    const int nit = par ? nrel : NSEG - nrel;
#define SEGOF(i) ((i) < nrel ? 2 * (i) + par : (i) + nrel)
#define ST_FLAG(sg)                                                                                                   \
  {                                                                                                                   \
    VMCNT(0);                                                                                                         \
    RAW_BARRIER();                                                                                                    \
    if (w == 0) __builtin_amdgcn_raw_buffer_store_b32(0u, rs, olane() << 28, fo + flag_of(sg, h), AUX_SC1);               \
  }
    unsigned vpm_ = 0;
    if ((VWN && isx) || (QKN && !isx)) {
      if (w == 7) *(volatile LAS unsigned*)L = numa_perm(nin, (LAS unsigned*)(L + 512));
      RAW_BARRIER();
      vpm_ = __builtin_amdgcn_readfirstlane(*(volatile LAS unsigned*)L) | (unsigned)((r_a + (int)(OFF_VWX / UNIT)) & 7) << 12;
      RAW_BARRIER();
    }
    const int qcl = cls_of(vpm_, h >> 3) ^ (QKN == 2 ? 2 : 0);
#define XB_OPS(sg)                                                                                                      \
  : [cnt] "=&s"(cnt) EVOUT                                                                                              \
  : WPIN, "{v[248:251]}"(adr), [rs] "s"(rsv), [so] "s"(vw_addr(vpm_, 2 * (w - 4), sg, h >> 2) + (h & 3) * 1024 + par * (PX / 2 * VRING * 262144)), \
    [so2] "s"(vw_addr(vpm_, 2 * (w - 4), sg, h >> 2) + (h & 3) * 1024 - par * (PX / 2 * VRING * 262144)),               \
    [js] "i"(VWN ? 32 * 524288 : VRING * 262144), [ss] "i"(VWN ? 65536 : 32768), [ob] "s"(nit - it)                         \
  : XB_CLOB, "scc", "memory"
    if (!isx || w < 4) {
      constexpr int B0 = isx ? 6 : 8, PER = isx ? 2 : 4, SL = isx ? 12288 : 16384;
#pragma unroll
      for (int k = 0; k < 2; ++k)
#pragma unroll
        for (int j = 0; j < B0 / PER; ++j)
          glds16((const char*)x + SEGOF(1) * 65536 + (((k * B0 + j * PER) / 2 * 64) ^ (krot * 128)), adr[0], k * SL + j * PER * 2048 + w * 1024);
    }
    PT(9);
    if (!isx) {
      VMCNT(4);
      RAW_BARRIER();
    }
    PT(10);
    if (!isx && w == 0) __builtin_amdgcn_raw_buffer_store_b32(0u, rs, olane() << 28, fo + flag_of(par, h), AUX_SC1);
    if (!isx || w < 4) {
      if (isx) VMCNT(3);
      else VMCNT(0);
      RAW_BARRIER();
    }
    PT(11);
    if (!isx || w < 4) {
      v32u xf;
#pragma unroll
      for (int q = 0; q < 8; ++q) {
        const v4u t = *(const LAS v4u*)(L + xr + q * 1024);
#pragma unroll
        for (int j = 0; j < 4; ++j) xf[4 * q + j] = t[j];
      }
#pragma unroll 1
      for (int it = 1; it < nit; ++it) {
        EVDECL;
        const int seg = SEGOF(it), ip = it - (isx ? 2 : 1);
        unsigned cnt, keep;
#define ST_OPS                                                                                                                 \
  : [cnt] "=&s"(cnt), [keep] "=&s"(keep), "+{v[208:239]}"(xf) EVOUT                                                            \
  : WPIN, "{v[248:251]}"(adr), "{v252}"(xr), [rs] "s"(rsv), [rx] "s"(rxv), [sc] "s"(seg * 65536 + krot * 128),                    \
    [sn] "s"(SEGOF(min(it + 1, nit - 1)) * 65536 + krot * 128),                                                                  \
    [mw] "s"(w * 1024), [so] "s"(isx ? w * 1024 : qk_addr(w >> 2, seg, h, qcl, r_a) + (w & 3) * 1024), [rt] "i"(QK_RT), \
    [fo] "s"(fo + flag_of(SEGOF(max(ip, 0)), h)), [fm] "s"(w == 0 && ip >= 0 ? 0 : 1 << 30), [tm] "s"(w == (isx ? 3 : TWY) ? 0 : 1 << 30),            \
    [tc] "s"(min(SEGOF(it + 2), NSEG - 1) * 65536 + sl * 8192), [cs] "s"((w >> 2) * __float_as_uint(CSC))                                                                   \
  : ST_CLOB, "scc", "memory"
        if constexpr (isx) asm volatile(ST_XA ST_OPS);
        else asm volatile(ST_Y ST_OPS);
        EVT2R
        if (!isx && it < NPUB) ST_FLAG(seg)
      }
      if (isx) RAW_BARRIER();
    } else {
      int prog = -1;
#define RING_WAIT(sg)                                                                                     \
  _Pragma("clang loop unroll(disable)") for (unsigned spins = 0; prog <= (sg) - VRING && (sg) >= VRING;) { \
    if (spins) __builtin_amdgcn_s_sleep(16);                                                              \
    OPQ(vp, OFF_PROG);                                                                                    \
    prog = __builtin_amdgcn_readfirstlane(__builtin_amdgcn_raw_buffer_load_b32(rs, vp, 0, AUX_SC1));      \
    if (spin_fail(spins, tmo, 0x30019)) break;                                                            \
  }
#pragma unroll 1
      for (int it = 1;;) {
        RING_WAIT(SEGOF(it - 1))
        {
          EVDECL;
          unsigned cnt;
          asm volatile(ST_XB0 XB_OPS(SEGOF(it - 1)));
        }
        if (it == nit) break;
        if (it <= 2) PT4(8 + 4 * it);
        const int nsb = it == 1 ? NSB1 : NSB;
#pragma unroll 1
        for (int k = (it == 1 ? 0 : 2) + NOB; k < nsb; ++k) BARE_BARRIER();
        if (it <= 2) PT4(9 + 4 * it);
        VMCNT(0);
        if (it <= 2) PT4(10 + 4 * it);
        BARE_BARRIER();
        if (it <= 2) PT4(11 + 4 * it);
        if (w == 4) __builtin_amdgcn_raw_buffer_store_b32(0u, rs, olane() << 28, fo + flag_of(SEGOF(it - 1), h), AUX_SC1);
#pragma unroll 1
        for (int k = nsb; k < 6; ++k) BARE_BARRIER();
        {
          EVDECL;
          EVT2R
        }
        if (++it > NSER) {
#pragma unroll 1
          for (; it < nit; ++it) {
            RING_WAIT(SEGOF(it - 1))
            EVDECL;
            unsigned cnt;
            asm volatile(ST_XB XB_OPS(SEGOF(it - 1)));
            EVT2R
          }
        }
        BARE_BARRIER();
      }
    }
    ST_FLAG(SEGOF(nit - 1))
    if (isx && w == 0) __builtin_amdgcn_raw_buffer_store_b32(0u, rs, olane() << 28, fo + flag_of(SEGOF(nit - 2), h), AUX_SC1);
    return;
  }

  volatile LAS unsigned* sword = (volatile LAS unsigned*)(L + L_HI + 6400);
  if (w == 7) sword[0] = nin[0];
  RAW_BARRIER();
  const unsigned perm = __builtin_amdgcn_readfirstlane(sword[0]);
  PT(9);
  const int cown = cls_of(perm, xg);
  const int sidx = ((l15 & 2) ? 0xeeee : 0x4444) | ((l15 & 8) ? 0xeeee0000 : 0x44440000);
  int ntw = min(w ^ TWV, 1) << 30;
  asm volatile("" : "+s"(ntw));
  v2u bs = {(unsigned)(lane * 16 + (w ^ 4) * 2048), (unsigned)sidx};
  unsigned pl1 = bs[0] + 65536;
  asm volatile("" : "+v"(bs), "+v"(pl1));
  const int mg_ = NS2F(TN(0, MARG)), tol_ = NS2F(TN(13, TOL)), xl_ = NS2F(TN(14, XLATE));
  const int ncy_ = TN(1, NCY), vd_ = role ? NS2F(VLATE) : 0;
  Pace pc = {0u, NS2F(ED0), NS2F(EA0), 0};
  unsigned tg = 0, tw = 0;
  const int mo0 = R_MEM + (w >> 1) * 32768 + (w & 1) * 2048 - (w ^ 4) * 2048;
#define CH_IN WPIN, "{v[248:249]}"(bs), "{v253}"(pk), "{v254}"(pl1), [rs] "s"(rsv)
#define TLB_TOUCH                    \
  if (((r_s + s + 2) & 3) == 0)      \
    tch = __builtin_amdgcn_raw_buffer_load_b32(rs, 0, so + 2 * (int)UNIT + R_DUM + (role * 32 + h) * 128, AUX_SC1);
#define ST_ADR (lst * 32 + 32768 + 2048)
#define TIMING_WAVE(slp)                                                                             \
  {                                                                                                  \
    v2u st;                                                                                          \
    {                                                                                                \
      const int lst = olane();                                                                       \
      st = __builtin_amdgcn_raw_buffer_load_b64(rs, ST_ADR, so + R_ST + ((cown ^ xr) << 12), AUX_SC1); \
    }                                                                                                \
    SB();                                                                                            \
    if (slp) __builtin_amdgcn_s_sleep(slp);                             \
    clk_update(ck, s < 2 ? CG1 : CG2);                                                \
    if (role == 0) EV(7);                                                                            \
    SB();                                                                                            \
    if (__builtin_expect(anysent(st[0]), 0)) _Pragma("clang loop unroll(disable)") for (unsigned spins = 0;;) { \
      if (spins) __builtin_amdgcn_s_sleep(4);                                                        \
      if (spin_fail(spins, tmo, 0x30009)) break;                                                     \
      const int lst = olane();                                                                       \
      OPQ(va, ST_ADR - 2048);                                                                        \
      st = __builtin_amdgcn_raw_buffer_load_b64(rs, va, so + R_ST + ((cown ^ xr) << 12), AUX_SC1);   \
      if (!anysent(st[0])) break;                                                                       \
    }                                                                                                \
    unsigned md;                                                                                     \
    const int dm = stamp_max(ck, st, md);                                                            \
    const unsigned rf = (ck.r << 4) + dm;                                                            \
    pace_step(pc, s, rf, md, mg_, tol_, xl_);                                                     \
    tg = clk_cyc(ck, dm + pc.dq + vd_);                                                                    \
    tw = (unsigned)max(CMAX - (int)(tg - ck.m), 0);                                                  \
    EV(6);                                                                                           \
    EVDUMP(s);                                                                                       \
    EVCLK(s, ck);                                                                                    \
  }

  if constexpr (role == 0) {
    unsigned pk = w < 4 ? (g >> 1) * 512 + (g & 1) * 128 + trq * 32 + 8 * (trp ^ (2 * (g & 1)))
                        : (w & 2) * 1024 + (w & 1) * 512 + trp * 32 + (l15 >> 3) * 128 + (trq & 1) * 256 + 8 * (g ^ (trq & 2));
    asm volatile("" : "+v"(pk));
#define K_STAGE(sn)                                                                                                   \
  {                                                                                                                   \
    v4u kn[4];                                                                                                        \
    _Pragma("clang loop unroll(disable)") for (unsigned spins = 0; krdy <= (sn);) {    \
      OPQ(vfl, F_QK + flag_of(min((sn) + (olane() & 15), NSEG - 1), h));                                           \
      const u64 miss = __builtin_amdgcn_ballot_w64(__builtin_amdgcn_raw_buffer_load_b32(rs, vfl, 0, AUX_SC1) != 0u);  \
      krdy = (sn) + (miss ? __builtin_ctzll(miss) : 16);                                                              \
      if (krdy > (sn)) break;                                                                                         \
      __builtin_amdgcn_s_sleep(8);                                                                                    \
      if (spin_fail(spins, tmo, 0x30000)) break;                                                                      \
    }                                                                                                                 \
    const int lk = olane() * 16;                                                                                      \
    _Pragma("unroll") for (int q = 0; q < 4; ++q) kn[q] = __builtin_amdgcn_raw_buffer_load_b128(                      \
        rs, lk + q * 1024, qk_addr(1, sn, h, cown, r_a) + (w & 1) * QK_RT, AUX_SC1);                 \
    _Pragma("unroll") for (int q = 0; q < 4; ++q) *(LAS v4u*)(L + L_HI + 8192 + (w & 1) * 4096 + q * 1024 + lk) = kn[q]; \
  }
    int krdy = 0;
    const bool kst = w >= 4 && ((w ^ TWV) & 2);
    PT(10);
    if (kst) K_STAGE(0)
    RAW_BARRIER();
    PT(11);
    if (w < 2) {
      {
        const int lane = olane(), l15 = lane & 15, g = lane >> 4, d = ((l15 >> 1) & 2) | (l15 >> 3);
        *(LAS v4u*)(L + L_HI + 5120 + w * 2048 + lane * 16) =
            v4u{(unsigned)(R_EX + (h >> 3) * 32768 + (h & 7) * 512 + w * 256 + g * 16 + ((l15 >> 3) | (l15 & 2)) * 64 + (l15 & 4) * 2 + (l15 & 1) * 4),
                (unsigned)(R_PM + (cls_of(perm, l15 & 3) << 12) + (h >> 4) * 32768 + (h & 15) * 256 + w * 128 + g * 16 + d * 4),
                d == 2 * (h & 1) + w ? 0xffffu << (16 * (g & 1)) : 0u, 0u};
      }
#pragma unroll 1
      for (int s = 0; s < NSEG; ++s) {
        EVDECL;
        const int so = (int)OFF_STEP + s * (int)UNIT, xr = xreg(r_s + s);
#define CG(r) (so + ((cls_of(perm, (r) ^ (2 * w)) ^ xr) << 12))
        unsigned rt;
#define QA_OPS                                                                                                                    \
  : [rt] "=&s"(rt) EVOUT                                                                                                          \
  : CH_IN, [o0] "s"(so + mo0 + ((cown ^ xr) << 12)), [c0] "s"(CG(0)), [c1] "s"(CG(1)), [c2] "s"(CG(2)), [c3] "s"(CG(3)),          \
    [lo] "s"(0x3333333333333333ull), [so] "s"(so), [xr] "s"(xr << 12)                                                                                              \
  : QL_CLOB, QK_CLOB, QH_CLOB, "v250", "v251", "v252", "vcc", "scc", "memory"
        if (w == 0) asm volatile(QK_A0 QA_OPS);
        else asm volatile(QK_A1 QA_OPS);
        EVDUMP(s);
      }
    } else if (w < 4) {
#pragma unroll 1
      for (int s = 0; s < NSEG; ++s) {
        {
          EVDECL;
          const int so = (int)OFF_STEP + s * (int)UNIT, xr = xreg(r_s + s);
          unsigned rt;
          f32x4 sc[3];
          asm volatile(QK_X
                       : [rt] "=&s"(rt), "={v[192:195]}"(sc[0]), "={v[196:199]}"(sc[1]), "={v[200:203]}"(sc[2]) EVOUT
                       : CH_IN, [o0] "s"(so + mo0 + ((cown ^ xr) << 12)), [qo] "s"(qk_addr(0, s, h, cown, r_a) + (w - 2) * QK_RT)
                       : QK_CLOB, QH_CLOB, "v250", "v251", "v252", "vcc", "scc", "memory");
          const int ln = olane(), l15 = ln & 15, g = ln >> 4, l16 = ln * 16;
          EV(4);
          const int tok = 2 * l15 + w - 2 - 8 * g;
#pragma unroll
          for (int t = 0; t < 2; ++t)
#pragma unroll
            for (int j = 0; j < 4; ++j)
              if (2 * j + t > tok) sc[t][j] = -1000000000.0f * 1.4426950408889634f;
          float m0 = sc[2][0] + sc[2][2], m1 = sc[2][1] + sc[2][3];
          float mx;
          asm("v_max3_f32 %0, %1, %2, %3\n\tv_max3_f32 %0, %0, %4, %5\n\tv_max3_f32 %0, %0, %6, %7\n\tv_max3_f32 %0, %0, %8, %9\n\t"
              "v_max_f32 %0, %0, %10\n\ts_nop 1"
              : "=&v"(mx)
              : "v"(sc[0][0]), "v"(sc[0][1]), "v"(sc[0][2]), "v"(sc[0][3]), "v"(sc[1][0]), "v"(sc[1][1]), "v"(sc[1][2]), "v"(sc[1][3]),
                "v"(m0), "v"(m1));
          mx = xmax16_32(mx);
#pragma unroll
          for (int t = 0; t < 2; ++t)
#pragma unroll
            for (int j = 0; j < 4; ++j) sc[t][j] = __builtin_amdgcn_exp2f(sc[t][j] - mx);
          m0 = __builtin_amdgcn_exp2f(m0 - mx);
          m1 = __builtin_amdgcn_exp2f(m1 - mx);
          const f32x4 s4 = sc[0] + sc[1];
          const float inv = __builtin_amdgcn_rcpf(xsum16_32(((s4[0] + s4[1]) + (s4[2] + s4[3])) + (m0 + m1)));
          sc[0] *= inv;
          sc[1] *= inv;
          const v4u o = {pk2(sc[0][0], sc[1][0]), pk2(sc[0][1], sc[1][1]), pk2(sc[0][2], sc[1][2]), pk2(sc[0][3], sc[1][3])};
          const int pb = so + R_PX + (w - 2) * 40960;
          st128(o, rs, l16, pb + h * 1024);
          __builtin_amdgcn_raw_buffer_store_b32(pk2(m0 * inv, m1 * inv), rs, l15 * 16 + g * 4, pb + (32 + (h >> 2)) * 1024 + (h & 3) * 256, AUX_SC1);
          EV(6);
          EVDUMP(s);
        }
      }
    } else {
#pragma unroll 1
      for (int s = 0; s < NSEG; ++s) {
        EVDECL;
        const int so = (int)OFF_STEP + s * (int)UNIT, xr = xreg(r_s + s), sc_ = so + ((cown ^ xr) << 12);
        const int lst = olane();
        const bool fq = kst && krdy <= s + 2;
        int i0 = ((cls_of(perm, lst & 3) ^ xr) << 12) | ((lst & 4) << 9) | ((lst & 8) << 12) | ((lst >> 4) << 28) | ntw;
        int i2 = ((lst & 31) * 128 + (lst >> 5) * QK_RT) | (krdy > s + 1 ? 0 : 1 << 30);
        int i1 = (F_QK + flag_of(min(s + 1 + (lst & 15), NSEG - 1), h)) | (fq ? 0 : 1 << 30);
        v2u st;
        unsigned cnt, rt;
        int o0 = sc_ + mo0, sst = so + R_ST + role * 1024 + h * 32;
        int t1 = qk_addr(w & 1, min(s + 1, NSEG - 1), h, cown, r_a), t0 = 0;
        asm volatile("" : "+{v232}"(i0), "+{v233}"(i1), "+{v234}"(i2));
        asm volatile("" : "+s"(o0), "+s"(sst), "+s"(t0), "+s"(t1));
        asm volatile(QK_K
                     : [cnt] "=&s"(cnt), [rt] "=&s"(rt), "+{v232}"(i0), "+{v233}"(i1), "+{v234}"(i2), "={v[250:251]}"(st) EVOUT
                     : CH_IN, [o0] "s"(o0), [ckm] "s"(ck.m - ncy_), [inv] "s"(ck.inv << 16), [r16] "s"(ck.r << 4), [sst] "s"(sst), [tg] "s"(tg),
                       [tw] "s"(tw), [t0] "s"(t0), [t1] "s"(t1), [cs] "s"(CSC)
                     : QL_CLOB, QK_CLOB, "v235", "v252", "vcc", "scc", "memory");
        if (s < NSEG - 1 && w == TWV) {
          TIMING_WAVE(KSL)
        } else if (s < NSEG - 1 && kst) {
          if (fq) {
            const u64 miss = __builtin_amdgcn_ballot_w64(st[0] != 0u);
            krdy = s + 1 + (miss ? __builtin_ctzll(miss) : 16);
          }
          if (KSL2) __builtin_amdgcn_s_sleep(KSL2);
          EV(7);
          K_STAGE(s + 1)
        } else {
          __builtin_amdgcn_s_sleep(KSL);
          unsigned tch = 0;
          TLB_TOUCH
          asm volatile("" ::"v"(tch));
        }
        if (w != TWV) EV(6);
        if (w != TWV) EVDUMP(s);
      }
    }
  } else {
    int adr[4];
#pragma unroll
    for (int q = 0; q < 2; ++q) {
      const int c = 8 * (2 * q + (g >> 1) + ((g & 1) ? 0 : 4)) + (w ^ ((h >> 4) * 4));
      const int blk = (c % PO) * 2 + (h >> 4);
      adr[q] = (int)OFF_VM + blk * 32768 + ((cls_of(perm, c / PO) ^ xreg(r_a + (int)(OFF_VM / UNIT) + (blk >> 4))) << 12) +
                   (h & 15) * 256 + l15 * 16;
    }
    adr[2] = (16 * (g & 1) + 4 * (g >> 1) + trq) * 24 + (trp & 1) * 8;
    unsigned pk = L_HI + (16 * (w & 3) + l15) * 24 + 4 * g;
    asm volatile("" : "+v"(pk), "+v"(adr[0]), "+v"(adr[1]), "+v"(adr[2]));
    PT(10);
    PT(11);
    if (w < 4) {
#pragma unroll 1
      for (int s = 0; s < NSEG; ++s) {
        EVDECL;
        const int so = (int)OFF_STEP + s * (int)UNIT, xr = xreg(r_s + s);
        unsigned rt;
        asm volatile(VO_A
                     : [rt] "=&s"(rt) EVOUT
                     : CH_IN, "{v236}"(adr[0]), "{v237}"(adr[1]), "{v238}"(adr[2]), [o0] "s"(so + mo0 + ((cown ^ xr) << 12)), [vs] "s"(vm_slot(s))
                     : VO_CLOB, "vcc", "scc", "memory");
        if (w == 0) {
          unsigned tch = 0;
          TLB_TOUCH
          asm volatile("" ::"v"(tch));
        }
        EVDUMP(s);
      }
    } else {
#pragma unroll 1
      for (int s = 0; s < NSEG; ++s) {
        EVDECL;
        const int so = (int)OFF_STEP + s * (int)UNIT, xr = xreg(r_s + s);
        const int lst = olane();
        adr[3] = ((cls_of(perm, lst & 3) ^ xr) << 12) | ((lst & 4) << 9) | ((lst & 8) << 12) | ((lst >> 4) << 28) | ntw;
        unsigned cnt, rt;
        int o0 = so + mo0 + ((cown ^ xr) << 12), vs = vm_slot(s), sst = so + R_ST + role * 1024 + h * 32;
        asm volatile("" : "+{v239}"(adr[3]));
        asm volatile("" : "+s"(o0), "+s"(vs), "+s"(sst));
        asm volatile(VO_B
                     : [cnt] "=&s"(cnt), [rt] "=&s"(rt) EVOUT
                     : CH_IN, "{v236}"(adr[0]), "{v237}"(adr[1]), "{v238}"(adr[2]), "{v239}"(adr[3]), [o0] "s"(o0), [vs] "s"(vs),
                       [ckm] "s"(ck.m - ncy_), [inv] "s"(ck.inv << 16), [r16] "s"(ck.r << 4), [sst] "s"(sst), [tg] "s"(tg), [tw] "s"(tw)
                     : VO_CLOB, "vcc", "scc", "memory");
        if (w == TWV && s < NSEG - 1) TIMING_WAVE(VSL)
        if (w != TWV) EVDUMP(s);
      }
    }
  }
  if ((WBL & 2) && role == 1)
    if (w == 0 && sl == 0) asm volatile("buffer_wbl2 sc1\n\ts_waitcnt vmcnt(0)" ::: "memory");
  __builtin_amdgcn_endpgm();
}
typedef f32x2 c32;
template <bool CJ>
__device__ __forceinline__ c32 cmul(c32 a, c32 b) {
  c32 d;
  if (CJ)
    asm("v_pk_mul_f32 %0, %1, %2 op_sel:[1,1] op_sel_hi:[1,0]\n\tv_pk_fma_f32 %0, %1, %2, %0 op_sel_hi:[0,1,1] neg_hi:[0,1,0]"
        : "=&v"(d) : "v"(a), "v"(b));
  else
    asm("v_pk_mul_f32 %0, %1, %2 op_sel:[1,1] op_sel_hi:[1,0] neg_lo:[0,1]\n\tv_pk_fma_f32 %0, %1, %2, %0 op_sel_hi:[0,1,1]"
        : "=&v"(d) : "v"(a), "v"(b));
  return d;
}
__device__ __forceinline__ c32 cmuls(c32 a, c32 b) {
  c32 d;
  asm("v_pk_mul_f32 %0, %1, %2 op_sel:[1,1] op_sel_hi:[1,0] neg_lo:[0,1]\n\tv_pk_fma_f32 %0, %1, %2, %0 op_sel_hi:[0,1,1]"
      : "=&v"(d) : "v"(a), "s"(b));
  return d;
}
template <int SG>
__device__ __forceinline__ c32 addi(c32 a, c32 b) {
  c32 d;
  if (SG > 0) asm("v_pk_add_f32 %0, %1, %2 op_sel:[0,1] op_sel_hi:[1,0] neg_lo:[0,1]" : "=v"(d) : "v"(a), "v"(b));
  else asm("v_pk_add_f32 %0, %1, %2 op_sel:[0,1] op_sel_hi:[1,0] neg_hi:[0,1]" : "=v"(d) : "v"(a), "v"(b));
  return d;
}
template <int SG, bool I2 = false>
__device__ __forceinline__ void dft4(c32& x0, c32& x1, c32& x2, c32& x3) {
  const c32 a = I2 ? addi<SG>(x0, x2) : x0 + x2, b = I2 ? addi<-SG>(x0, x2) : x0 - x2, c = x1 + x3, d = x1 - x3;
  x0 = a + c;
  x1 = addi<SG>(b, d);
  x2 = a - c;
  x3 = addi<-SG>(b, d);
}
template <int SG>
__device__ __forceinline__ c32 w16(int m) {
  constexpr float C[16] = {1.f, 0.92387953251128674f, 0.70710678118654752f, 0.38268343236508977f, 0.f, -0.38268343236508977f,
                           -0.70710678118654752f, -0.92387953251128674f, -1.f, -0.92387953251128674f, -0.70710678118654752f,
                           -0.38268343236508977f, 0.f, 0.38268343236508977f, 0.70710678118654752f, 0.92387953251128674f};
  return c32{C[m & 15], (float)SG * C[(m + 12) & 15]};
}
template <int SG>
__device__ __forceinline__ void dft16(c32 (&v)[16]) {
#pragma unroll
  for (int n2 = 0; n2 < 4; ++n2) dft4<SG>(v[n2], v[4 + n2], v[8 + n2], v[12 + n2]);
#pragma unroll
  for (int k1 = 1; k1 < 4; ++k1)
#pragma unroll
    for (int n2 = 1; n2 < 4; ++n2)
      if (n2 * k1 != 4) v[4 * k1 + n2] = cmuls(v[4 * k1 + n2], w16<SG>(n2 * k1));
#pragma unroll
  for (int k1 = 0; k1 < 4; ++k1)
    if (k1 == 2) dft4<SG, true>(v[8], v[9], v[10], v[11]);
    else dft4<SG>(v[4 * k1], v[4 * k1 + 1], v[4 * k1 + 2], v[4 * k1 + 3]);
  c32 t[16];
#pragma unroll
  for (int k1 = 0; k1 < 4; ++k1)
#pragma unroll
    for (int k2 = 0; k2 < 4; ++k2) t[k1 + 4 * k2] = v[4 * k1 + k2];
#pragma unroll
  for (int i = 0; i < 16; ++i) v[i] = t[i];
}
template <int SG>
__device__ __forceinline__ void dft8(c32 (&v)[8]) {
#pragma unroll
  for (int n2 = 0; n2 < 2; ++n2) dft4<SG>(v[n2], v[2 + n2], v[4 + n2], v[6 + n2]);
#pragma unroll
  for (int k1 = 1; k1 < 4; k1 += 2) v[2 * k1 + 1] = cmuls(v[2 * k1 + 1], w16<SG>(2 * k1));
  c32 t[8];
#pragma unroll
  for (int k1 = 0; k1 < 4; ++k1) {
    t[k1] = k1 == 2 ? addi<SG>(v[4], v[5]) : v[2 * k1] + v[2 * k1 + 1];
    t[k1 + 4] = k1 == 2 ? addi<-SG>(v[4], v[5]) : v[2 * k1] - v[2 * k1 + 1];
  }
#pragma unroll
  for (int i = 0; i < 8; ++i) v[i] = t[i];
}
__device__ __forceinline__ void lds_fence_wave() {
  __builtin_amdgcn_s_waitcnt(0xc07f);
  asm volatile("" ::: "memory");
}
template <int SG>
__device__ __forceinline__ void twiddle16(c32 (&u)[16], c32 w1, c32 w4) {
  const c32 w2 = cmul<false>(w1, w1), w3 = cmul<false>(w2, w1), w8 = cmul<false>(w4, w4), w12 = cmul<false>(w8, w4);
  const c32 lo[4] = {w1, w1, w2, w3}, hi[4] = {w4, w4, w8, w12};
#pragma unroll
  for (int k = 1; k < 16; ++k)
    u[k] = cmul<(SG > 0)>(u[k], (k & 3) ? ((k >> 2) ? cmul<false>(hi[k >> 2], lo[k & 3]) : lo[k & 3]) : hi[k >> 2]);
}
template <int N, int SG>
__device__ __forceinline__ void wave_fft(c32 (&v)[N / 64], c32* eb, const c32* tw, int lane) {
  constexpr int P = N / 64, B = P / 16, R3 = N / 256, M = N / 16, S1 = M + R3, S2 = 256 + 16 / R3, TS = 2048 / N;
  asm volatile("" : "+v"(lane));
#pragma unroll
  for (int b = 0; b < B; ++b) {
    c32 u[16];
#pragma unroll
    for (int n1 = 0; n1 < 16; ++n1) u[n1] = v[b + B * n1];
    dft16<SG>(u);
    const int n2 = lane + 64 * b;
    twiddle16<SG>(u, tw[n2 * TS], tw[n2 * TS * 4]);
#pragma unroll
    for (int k1 = 0; k1 < 16; ++k1) eb[k1 * S1 + n2] = u[k1];
    __builtin_amdgcn_sched_barrier(0);
  }
  lds_fence_wave();
  {
    c32 u[B][16];
#pragma unroll
    for (int b = 0; b < B; ++b) {
      const int q = lane + 64 * b, k1 = q / R3, m2 = q % R3;
#pragma unroll
      for (int m1 = 0; m1 < 16; ++m1) u[b][m1] = eb[k1 * S1 + m1 * R3 + m2];
    }
    const c32 w1 = tw[16 * (lane % R3) * TS], w4 = tw[64 * (lane % R3) * TS];
    lds_fence_wave();
#pragma unroll
    for (int b = 0; b < B; ++b) {
      const int q = lane + 64 * b, k1 = q / R3, m2 = q % R3;
      dft16<SG>(u[b]);
      twiddle16<SG>(u[b], w1, w4);
#pragma unroll
      for (int j1 = 0; j1 < 16; ++j1) eb[m2 * S2 + k1 + 16 * j1] = u[b][j1];
      __builtin_amdgcn_sched_barrier(0);
    }
  }
  lds_fence_wave();
#pragma unroll
  for (int c = 0; c < 4; ++c) {
    c32 u[R3];
#pragma unroll
    for (int m2 = 0; m2 < R3; ++m2) u[m2] = eb[m2 * S2 + lane + 64 * c];
    if constexpr (R3 == 4) dft4<SG>(u[0], u[1], u[2], u[3]);
    else dft8<SG>(u);
#pragma unroll
    for (int j2 = 0; j2 < R3; ++j2) v[c + 4 * j2] = u[j2];
  }
  lds_fence_wave();
}

constexpr int FFT_EB = 2176;
constexpr int LDS_TW = 0;
constexpr int LDS_EB = 16384;
constexpr int LDS_STG = 16384 + 8 * 1088 * 8;

__device__ __forceinline__ void fft_table(char* lds, int w) {
  c32* tw = (c32*)(lds + LDS_TW);
  {
    const int m = w * 64 + olane();
    float sn, cs;
    sincospif(-(float)m * (1.f / 1024.f), &sn, &cs);
    tw[m] = c32{cs, sn};
  }
}
#define XSK 12
__device__ __forceinline__ int x1_chunk(int kb, int rb) { return kb * 128 + ((rb + XSK * kb) & 127); }
#define F1NT true
__device__ __forceinline__ void fft_in1(const u16* x, const u16* wr, const u16* wi, int task, int w) {
  const int lane = olane(), r0 = task * 16 + 2 * w;
  const int eo = LDS_EB + w * 8704, fo = r0 * DF * 2;
#pragma unroll
  for (int q = 0; q < 4; ++q) glds16<F1NT>(x, r0 * D * 2 + q * 1024 + lane * 16, eo + q * 1024);
#pragma unroll
  for (int q = 0; q < 3; ++q)
    if (q < 2 || lane < 2) {
      const int o = min((fo & ~15) + q * 1024 + lane * 16, S * DF * 2 - 16);
      glds16<F1NT>(wr, o, eo + 4096 + q * 1024);
      glds16<F1NT>(wi, o, eo + 6176 + q * 1024);
    }
}
__device__ __forceinline__ void fft_pass1(char* ws, const u16* __restrict__ x, const u16* __restrict__ wr,
                                          const u16* __restrict__ wi, int task, int w, const u16* xb) {
  char* lds = g_lds;
  const __amdgpu_buffer_rsrc_t rx = __builtin_amdgcn_make_buffer_rsrc(ws + OFF_X1, 0, 65 * 131072, 0x00020000);
  const int lane = olane(), tid_ = w * 64 + lane;
  const c32* tw = (const c32*)(lds + LDS_TW);
  c32* eb = (c32*)(lds + LDS_EB) + w * 1088;
  c32(*stg)[16] = (c32(*)[16])(lds + LDS_STG);
  const int r0 = task * 16 + 2 * w;
#define F1T(i)
  F1T(0);
  const int fo = r0 * DF * 2;
  if (xb) {
    unsigned keep;
    asm volatile("s_mov_b32 %0, m0\n\ts_mov_b32 m0, %3\n\ts_nop 0\n\tglobal_load_lds_dword %1, %2\n\ts_mov_b32 m0, %0"
                 : "=&s"(keep) : "v"(task * 65536 + tid_ * 128), "s"(xb), "s"(160000 + w * 256) : "memory");
  } else {
    fft_in1(x, wr, wi, task, w);
    VMCNT(0);
  }
  F1T(1);
  c32 v[16];
  const u16* in = (const u16*)eb;
#pragma unroll
  for (int t = 0; t < 16; ++t) v[t] = c32{bf2f(in[lane + 64 * t]), bf2f(in[D + lane + 64 * t])};
  unsigned fw[2][9];
#pragma unroll
  for (int r = 0; r < 2; ++r)
#pragma unroll
    for (int t = 0; t < 9; ++t) {
      const int o = 2048 + ((fo & 15) >> 1) + r * DF + min(lane + 64 * t, 512);
      fw[r][t] = (unsigned)in[o] | ((unsigned)in[o + 1040] << 16);
    }
  lds_fence_wave();
  F1T(2);
  wave_fft<1024, -1>(v, eb, tw, lane);
  F1T(3);
#pragma unroll
  for (int t = 0; t < 16; ++t) eb[lane + 64 * t] = v[t];
  lds_fence_wave();
#pragma unroll
  for (int t = 0; t < 9; ++t) {
    const int k = lane + 64 * t;
    if (k <= 512) {
      c32 z = eb[k], zc = eb[(1024 - k) & 1023];
      *(f32x4*)&stg[k][2 * (w ^ (k & 7))] = f32x4{0.5f * (z.x + zc.x), 0.5f * (z.y - zc.y), 0.5f * (z.y + zc.y), -0.5f * (z.x - zc.x)};
    }
  }
  __syncthreads();
  F1T(4);
#pragma unroll
  for (int i = 0; i < 9; ++i) {
    const int idx = tid_ + 512 * i, k = min(idx >> 3, 512), p = idx & 7;
    v4u d = *(const v4u*)&stg[k][2 * (p ^ (k & 7))];
    st128(d, rx, idx < 513 * 8 ? lane * 16 : OOB, x1_chunk(min(w + 8 * i, 64), task) * 1024);
  }
  {
    const __amdgpu_buffer_rsrc_t rt = __builtin_amdgcn_make_buffer_rsrc(ws + OFF_WT, 0, 65 * 65536, 0x00020000);
    v2u* s2 = (v2u*)(lds + LDS_EB);
#pragma unroll
    for (int t = 0; t < 9; ++t) {
      const int k = lane + 64 * t;
      if (k <= 512) s2[k * 8 + (w ^ (k & 6))] = v2u{fw[0][t], fw[1][t]};
    }
    __syncthreads();
#pragma unroll
    for (int i = 0; i < 5; ++i) {
      const int idx = tid_ + 512 * i, k = min(idx >> 2, 512), p = idx & 3;
      const v4u d = *(const v4u*)&s2[k * 8 + ((2 * p) ^ (k & 6))];
      __builtin_amdgcn_raw_buffer_store_b128(d, rt, idx < 514 * 4 ? x1_chunk(idx >> 5, task) * 512 + (idx & 31) * 16 : OOB, 0, AUX_SC1);
    }
  }
}
__device__ __forceinline__ void fft_pass2(char* ws, int task, int w) {
  char* lds = g_lds;
  const __amdgpu_buffer_rsrc_t rx = __builtin_amdgcn_make_buffer_rsrc(ws + OFF_X1, 0, 65 * 131072, 0x00020000);
  const int lane = olane();
  const int k = task * 8 + w;
  const c32* tw = (const c32*)(lds + LDS_TW);
  c32* eb = (c32*)(lds + LDS_EB) + w * FFT_EB;
#define F2T(i)
  if (k > 512) return;
  F2T(0);
  c32 v[32];
  const int xoff = (lane >> 4) * 1024 + w * 128 + (lane & 15) * 8;
#pragma unroll
  for (int t = 0; t < 32; ++t) {
    v2u d = __builtin_amdgcn_raw_buffer_load_b64(rx, xoff, x1_chunk(task, 4 * t) * 1024, AUX_SC1);
    v[t] = c32{__uint_as_float(d[0]), __uint_as_float(d[1])};
  }
  unsigned wv[32];
  asm volatile("" : "+s"(task));
  {
    const __amdgpu_buffer_rsrc_t rt = __builtin_amdgcn_make_buffer_rsrc(ws + OFF_WT, 0, 65 * 65536, 0x00020000);
#pragma unroll
    for (int t = 0; t < 32; ++t)
      wv[t] = __builtin_amdgcn_raw_buffer_load_b32(rt, xoff >> 1, x1_chunk(task, 4 * t) * 512, AUX_SC1);
  }
  F2T(1);
  wave_fft<2048, -1>(v, eb, tw, lane);
  F2T(2);
#pragma unroll
  for (int t = 0; t < 32; ++t) v[t] = cmul<false>(v[t], c32{__uint_as_float(wv[t] << 16), __uint_as_float(wv[t] & 0xffff0000u)});
  F2T(3);
  wave_fft<2048, 1>(v, eb, tw, lane);
  F2T(4);
  asm volatile("" : "+s"(task));
#pragma unroll
  for (int t = 0; t < 32; ++t) {
    v2u d = {__float_as_uint(v[t].x), __float_as_uint(v[t].y)};
    __builtin_amdgcn_raw_buffer_store_b64(d, rx, xoff, x1_chunk(task, 4 * t) * 1024, AUX_SC1);
  }
  F2T(5);
  VMCNT(0);
  F2T(6);
}
#define OAUX3 16
#define OAUXX 0
__device__ __forceinline__ void fft_pass3(const char* ws, u16* __restrict__ out, int task, int w) {
  char* lds = g_lds;
  const __amdgpu_buffer_rsrc_t rx = __builtin_amdgcn_make_buffer_rsrc((void*)(ws + OFF_X1), 0, 65 * 131072, 0x00020000);
  const int lane = olane(), tid_ = w * 64 + lane;
  const c32* tw = (const c32*)(lds + LDS_TW);
  c32* eb = (c32*)(lds + LDS_EB) + w * 1088;
  c32(*stg)[16] = (c32(*)[16])(lds + LDS_STG);
  {
    v4u d[9];
#pragma unroll
    for (int i = 0; i < 9; ++i) {
      d[i] = __builtin_amdgcn_raw_buffer_load_b128(rx, (i < 8 ? lane : lane & 7) * 16, x1_chunk(min(w + 8 * i, 64), task) * 1024, AUX_SC1);
    }
#pragma unroll
    for (int i = 0; i < 9; ++i) {
      const int idx = tid_ + 512 * i, k = min(idx >> 3, 512), p = idx & 7;
      *(v4u*)&stg[k][2 * (p ^ (k & 7))] = d[i];
    }
  }
  __syncthreads();
  c32 v[16];
#pragma unroll
  for (int t = 0; t < 16; ++t) {
    const int n = lane + 64 * t;
    const int k = n <= 512 ? n : 1024 - n;
    const f32x4 xx = *(const f32x4*)&stg[k][2 * (w ^ (k & 7))];
    c32 xa = {xx[0], xx[1]}, xb = {xx[2], xx[3]};
    if (k == 0 || k == 512) {
      xa.y = 0.f;
      xb.y = 0.f;
    }
    v[t] = n <= 512 ? c32{xa.x - xb.y, xa.y + xb.x} : c32{xa.x + xb.y, -xa.y + xb.x};
  }
  wave_fft<1024, 1>(v, eb, tw, lane);
  const float sc = 1.f / (2048.f * 1024.f);
  const int r0 = task * 16 + 2 * w;
  const __amdgpu_buffer_rsrc_t ro = __builtin_amdgcn_make_buffer_rsrc(out, 0, S * 4096, 0x00020000);
#pragma unroll
  for (int t = 0; t < 16; ++t) {
    __builtin_amdgcn_raw_buffer_store_b16(f2bf(v[t].x * sc), ro, lane * 2 + t * 128, r0 * 4096, OAUX3);
    __builtin_amdgcn_raw_buffer_store_b16(f2bf(v[t].y * sc), ro, lane * 2 + t * 128, r0 * 4096 + 4096, OAUX3);
  }
}

#define OB2 0
#define FIE 0
#define FLC 0
#define WBP 8
#define WBQ 2
#define COT 100
#define CUP 24
#define CDN 1
#define CLO -40
#define CHI 30
#define TLW 900
#define OSL 4

#define OCK 800
#define OTRY 3
#define APR 0
#define OGN 5
#define OK1 150
#define NX2 0
#define WTO 2
#define WTS 30
#define WTN 2
#define RSO 5
#define NCAL 6
#define DOX2 200
#define DTC 100
#define NX1 4
#define NXG 2
#define NXK 5
#define OFD 200
#define STI 600
#define TNX 0
#define NXP 0
#define PACE 150
#define PFB 4
#define CV1 -20
#define CV2 125
#define EDL 0
constexpr int L_D = 16384, L_R = 32768, L_F = 122880;
__device__ __forceinline__ volatile LAS unsigned* flw(int i, int m) {
  return (volatile LAS unsigned*)((LAS char*)g_lds + L_F + i * 4 + (olane() & m) * 4);
}
template <int SLP = 0>
__device__ __forceinline__ void lds_wait_ge(int i, int m, unsigned v, gu32* tmo, unsigned code) {
  volatile LAS unsigned* p = flw(i, m);
  _Pragma("clang loop unroll(disable)") for (unsigned spins = 0;;) {
    if (__all(*p >= v)) break;
    if (SLP) __builtin_amdgcn_s_sleep(SLP);
    if (++spins > (kSpinCap << 5)) {
      tmo_report(tmo, code);
      break;
    }
  }
  asm volatile("" ::: "memory");
}
template <bool PRE>
__device__ __forceinline__ unsigned owner(const int c, const int w, const u64 nonce, const u16* __restrict__ mem0, char* ws,
                                          u16* __restrict__ out, unsigned perm, const char* wsl, const u16* ax, const u16* awr,
                                          const u16* awi) {
  LAS char* const L = (LAS char*)g_lds;
  gu32* tmo = (gu32*)(ws + OFF_TMO);
  const int lane = olane();
  const int l15 = lane & 15, g = lane >> 4;
  const __amdgpu_buffer_rsrc_t rs = __builtin_amdgcn_make_buffer_rsrc(ws, 0, (int)WS_USED, 0x00020000);
  const int xg = c / PO, li = c % PO;
  const int r_a = (int)(((uintptr_t)ws >> 19) & 7);
  const int r_s = r_a + (int)(OFF_STEP / UNIT);
  volatile LAS unsigned* fl = (volatile LAS unsigned*)(L + L_F);
  unsigned m0v = 0;
  if constexpr (PRE) {
  if (c == 0 && w == 6) hs_lead<4>(rs, true, CNT_HSO, 64u);
  if (WTO == 1 && wsl) {
    __builtin_amdgcn_s_sleep(WTS);
    for (int k = 0; k < WTN; ++k) w_touch(wsl, (li >> 1) * 128 + 64 + (li & 1) * 32 + (k & 1) * 16 - (k >> 1) * 64, 160000 + w * 256);
  }
  unsigned cal = 0;
  if (w == 0)
    st128(v4u{(unsigned)lane, 0u, 0u, 0u}, rs, (lane & 31) * 16 + (lane >> 5) * 32768, (int)OFF_SCR + (((li & 7) ^ xreg(r_a)) << 12) + (c >> 3) * 512);
  if (w == 0 && lane == 0) {
    const int cl = li & 7;
    int off = (int)OFF_SCR + ((cl ^ xreg(r_a)) << 12) + (c >> 3) * 512;
    unsigned best = 0xfffeu;
#pragma unroll 1
    for (int r = 0; r < NCAL; ++r) {
      VMCNT(0);
      const u64 t1 = __builtin_amdgcn_s_memtime();
      asm volatile("s_waitcnt lgkmcnt(0)" : "+v"(off)::"memory");
      const unsigned v = __builtin_amdgcn_raw_buffer_load_b32(rs, off, (r & 3) * 128 + (r >> 2) * 32768, AUX_SC1);
      asm volatile("s_waitcnt vmcnt(0)" ::"v"(v) : "memory");
      const u64 t2 = __builtin_amdgcn_s_memtime();
      best = min(best, (unsigned)(t2 - t1));
    }
    cal = ((unsigned)cl << 16) | best;
  }
  if (w == 0) {
    m0v = *(const unsigned*)(mem0 + (lane >> 3) * D + 16 * c + 2 * (lane & 7));
    hs_wait(rs, ws, nonce, CNT_HSO);
    unsigned id;
    asm volatile("s_getreg_b32 %0, hwreg(HW_REG_XCC_ID)" : "=s"(id));
    if (lane == 0) {
      __builtin_amdgcn_raw_buffer_store_b32(((id & 15u) << 19) | cal, rs, c * CST, (int)OFF_CAL, AUX_SC1);
    }
  }
  __syncthreads();
  if (FIE == 1) fft_in1(ax, awr, awi, c, w);
  if (WTO == 2 && wsl)
    for (int k = 0; k < WTN; ++k) w_touch(wsl, (li >> 1) * 128 + 64 + (li & 1) * 32 + (k & 1) * 16 - (k >> 1) * 64, 160000 + w * 256);
  if (FIE == 2) fft_in1(ax, awr, awi, c, w);
  if (w == 7) {
    const unsigned pm = numa_perm(numa_load(rs, tmo), (LAS unsigned*)(L + L_F + 512));
    fl[10] = pm;
  }
  RAW_BARRIER();
  perm = __builtin_amdgcn_readfirstlane(fl[10]);
  }
  const int xr0 = xreg(r_s + (PRE ? w : NARM + (li & 1)));
  const v4u sent = {0xffffffffu, 0xffffffffu, 0xffffffffu, 0xffffffffu};
  const int ibs = (int)OFF_VM + li * 65536 + ((cls_of(perm, xg) ^ xreg(r_a + (int)(OFF_VM / UNIT) + (li >> 3))) << 12);

  int am = (int)OFF_STEP + R_MEM + (c >> 4) * 32768 + ((cls_of(perm, g) ^ xr0) << 12) + (c & 15) * 256 + l15 * 16;
  int ap[2];
#pragma unroll
  for (int q = 0; q < 2; ++q) {
    const int o = c * 384 + (lane & 31) * 16;
    ap[q] = (lane & 31) < 24 ? (int)OFF_STEP + R_EX + (o >> 12) * 32768 + ((cls_of(perm, 2 * q + (lane >> 5)) ^ xr0) << 12) + (o & 4095)
                             : OOB;
  }
  int as = lane < 8 * NXC ? (int)OFF_STEP + R_ST + ((cls_of(perm, lane >> 3) ^ xr0) << 12) + (c >> 5) * 32768 + (c & 31) * 128 + (lane & 7) * 16
                          : OOB;
  const int ax1 = (int)OFF_STEP + R_PX + c * 1280 + lane * 16;
  const int ax2 = lane < 16 ? ax1 + 1024 : (lane < 24 ? (int)OFF_STEP + R_DUM + c * 128 + (lane - 16) * 16 : OOB);
#define ARM_REC(r)                                                                                                   \
  {                                                                                                                  \
    const int so_ = (r) * (int)UNIT, dx_ = (xreg(r_s + (r)) ^ xreg(r_s + (r) + 1)) << 12;                            \
    __builtin_amdgcn_raw_buffer_store_b128(sent, rs, am, so_, AUX_SC1);                                              \
    am ^= dx_;                                                                                                       \
    _Pragma("unroll") for (int q = 0; q < 2; ++q) {                                                                  \
      __builtin_amdgcn_raw_buffer_store_b128(sent, rs, ap[q], so_, AUX_SC1);                                         \
      ap[q] ^= dx_;                                                                                                  \
    }                                                                                                                \
    __builtin_amdgcn_raw_buffer_store_b128(sent, rs, as, so_, AUX_SC1);                                              \
    as ^= dx_;                                                                                                       \
    __builtin_amdgcn_raw_buffer_store_b128(sent, rs, ax1, so_, AUX_SC1);                                              \
    __builtin_amdgcn_raw_buffer_store_b128(sent, rs, ax2, so_, AUX_SC1);                                             \
  }
  if constexpr (PRE) {
    if (WTO == 3 && wsl)
      for (int k = 0; k < WTN; ++k) w_touch(wsl, (li >> 1) * 128 + 64 + (li & 1) * 32 + (k & 1) * 16 - (k >> 1) * 64, 160000 + w * 256);
    if (!FIE) fft_in1(ax, awr, awi, c, w);
    if (w < NARM + (li & 1)) {
      if (w == 0) {
        const int row = lane >> 3, np = lane & 7;
#pragma unroll
        for (int r = 0; r < NXC; ++r)
          __builtin_amdgcn_raw_buffer_store_b32(min(m0v, 0xfffffffeu), rs,
                                                (int)OFF_STEP + R_MEM + (c >> 4) * 32768 + ((cls_of(perm, r) ^ xr0) << 12) + (c & 15) * 256 +
                                                    ((row & 1) + 2 * (np & 1) + 4 * (row >> 1)) * 16 + (np >> 1) * 4,
                                                0, AUX_SC1);
        am = OOB;
      }
      ARM_REC(w)
    }
#pragma unroll
    for (int k = 0; k < 4; ++k)
      __builtin_amdgcn_raw_buffer_store_b128(sent, rs, lane * 16 + k * 1024, ibs + vm_slot(w >> 1) + (w & 1) * 32768, AUX_SC1);
    if (c == 0 && w == 7) __builtin_amdgcn_raw_buffer_store_b32(perm, rs, lane << 28, OFF_PERM, AUX_SC1);
  }
  if constexpr (PRE) return perm;
  if (FLC || OB2) RAW_BARRIER();
  if (w == 7) {
    if (lane < 32) fl[lane] = 0;
    arm_wait(rs, tmo);
  }
  if (OB2) RAW_BARRIER();
  if (FLC && (w < 2 || w == 7)) VMCNT(0);

  const int team = li & 1, c0 = c & ~1;
  const int l16 = lane * 16;
  const int cown = cls_of(perm, xg);
  if (w < 2) {
    const int eg = (c0 >> 2) & 1;
    typedef int v8i __attribute__((ext_vector_type(8)));
    v8i adr;
    adr[0] = l16;
    adr[1] = (lane >> 2) * 128 + (lane & 3) * 16;
    adr[2] = L_D + w * 8192 + eg * 4096 + l16;
    adr[3] = ((g & 1) + (l15 & 2) + 4 * ((g & 2) + (l15 & 1))) * 16 + (l15 >> 2) * 4;
    adr[4] = (l15 & 1) ? 0x03020706 : 0x05040100;
    adr[5] = ((l15 & 8) ? 0xeeee : 0x4444) | ((l15 & 2) ? 0xeeee0000 : 0x44440000);
    adr[6] = L_F + 48;
    adr[7] = adr[2] ^ 4096;
    const v4u rsv = {(unsigned)(uintptr_t)ws, (unsigned)((uintptr_t)ws >> 32) & 0xffffu, (unsigned)WS_USED, 0x00020000u};
    const int dtc_ = TN(12, DTC);
#pragma unroll 1
    for (int s = team; s < NSEG; s += 2) {
      EVDECL;
      const int so = (int)OFF_STEP + s * (int)UNIT;
      const int sp = so + R_PM + ((cown ^ xreg(r_s + s)) << 12);
      int sv = ibs + (w - team) * 65536 + vm_slot(s);
      int sv1 = sv + eg * 32768, sv2 = sv + (1 - eg) * 32768, sp1 = sp + eg * 32768, sp2 = sp + (1 - eg) * 32768;
      const int xn = xreg(r_s + s + 1);
      int sm[NXC];
#pragma unroll
      for (int q = 0; q < NXC; ++q) {
        sm[q] = so + (int)UNIT + R_MEM + (c0 >> 4) * 32768 + ((cls_of(perm, q) ^ xn) << 12) + ((c0 + w) & 15) * 256;
        asm volatile("" : "+s"(sm[q]));
      }
      asm volatile("" : "+s"(sv1), "+s"(sv2), "+s"(sp1), "+s"(sp2));
      if (s >= 2) lds_wait_ge<4>(20, 3, s - 1, tmo, 0x30017);
      v4u bl[8];
      u64 tp64 = 0;
      if (s < NSEG - 1) {
        v32u blv;
        unsigned cnt, bad;
        asm volatile(OW_F
                     : [cnt] "=&s"(cnt), [st] "=&s"(bad), [tp] "=&s"(tp64), "={v[0:31]}"(blv) EVOUT
                     : "{v[112:119]}"(adr), [rs] "s"(rsv), [sv1] "s"(sv1), [sv2] "s"(sv2), [sp1] "s"(sp1), [sp2] "s"(sp2),
                       [sm0] "s"(sm[0]), [sm1] "s"(sm[1]), [sm2] "s"(sm[2]), [sm3] "s"(sm[3])
                     : OF_CLOB, "vcc", "scc", "memory");
#pragma unroll
        for (int k = 0; k < 8; ++k) bl[k] = v4u{blv[4 * k], blv[4 * k + 1], blv[4 * k + 2], blv[4 * k + 3]};
        if (__builtin_expect(bad != 0u, 0)) {
          v4u pv[8];
          _Pragma("clang loop unroll(disable)") for (unsigned spins = 0;;) {
            OPQ(va, l16);
            OPQ(v2, adr[1]);
#pragma unroll
            for (int k = 0; k < 8; ++k) bl[k] = __builtin_amdgcn_raw_buffer_load_b128(rs, va + (k & 3) * 1024, k < 4 ? sv1 : sv2, AUX_SC1);
#pragma unroll
            for (int k = 0; k < 8; ++k)
              pv[k] = __builtin_amdgcn_raw_buffer_load_b128(rs, v2 + (k & 1) * 2048 + (k >> 2) * 64, (k & 2) ? sp2 : sp1, AUX_SC1);
            unsigned m = 0;
#pragma unroll
            for (int k = 0; k < 8; ++k) m = mx4(mx4(m, pv[k]), bl[k]);
            if (!anysent(m)) break;
            __builtin_amdgcn_s_sleep(RSO);
            if (spin_fail(spins, tmo, 0x20000 + s)) break;
          }
          f32x4 a0 = {0.f, 0.f, 0.f, 0.f};
#pragma unroll
          for (int k = 0; k < 4; ++k) {
            v8u b = cat8(bl[2 * k], bl[2 * k + 1]);
#pragma unroll
            for (int j = 0; j < 8; j += 2) {
              const auto q = __builtin_amdgcn_permlane16_swap(b[j], b[j + 1], false, false);
              b[j] = q[0];
              b[j + 1] = q[1];
            }
            a0 = smf(pv[k], b, a0, adr[5]);
          }
#pragma unroll
          for (int k = 0; k < 4; ++k) {
            LAS char* const pd = L + adr[k < 2 ? 2 : 7] + (k & 1) * 2048;
            a0 = smf1(pv[4 + k], cat8(*(const LAS v4u*)pd, *(const LAS v4u*)(pd + 1024)), a0, adr[5]);
          }
          const auto q0 = __builtin_amdgcn_permlane32_swap(__float_as_uint(a0[0]), __float_as_uint(a0[2]), false, false);
          const auto q1 = __builtin_amdgcn_permlane32_swap(__float_as_uint(a0[1]), __float_as_uint(a0[3]), false, false);
          const unsigned own = pk2(__uint_as_float(q0[0]) + __uint_as_float(q0[1]), __uint_as_float(q1[0]) + __uint_as_float(q1[1]));
          const unsigned nb = (unsigned)__builtin_amdgcn_mov_dpp((int)own, 0xb1, 0xf, 0xf, true);
          const unsigned o = __builtin_amdgcn_perm(nb, own, (unsigned)adr[4]);
#pragma unroll
          for (int q = 0; q < NXC; ++q) __builtin_amdgcn_raw_buffer_store_b32(o, rs, adr[3], sm[q], AUX_SC1);
          tp64 = cyc32();
          EV(3);
        }
      } else {
        BARE_BARRIER();
        wait_cyc(*flw(12, 0));
        _Pragma("clang loop unroll(disable)") for (unsigned spins = 0;;) {
          OPQ(va, l16);
          unsigned m = 0;
#pragma unroll
          for (int k = 0; k < 8; ++k) bl[k] = __builtin_amdgcn_raw_buffer_load_b128(rs, va + (k & 3) * 1024, k < 4 ? sv1 : sv2, AUX_SC1);
#pragma unroll
          for (int k = 0; k < 8; ++k) m = mx4(m, bl[k]);
          if (!anysent(m)) break;
          __builtin_amdgcn_s_sleep(RSO);
          if (spin_fail(spins, tmo, 0x20000 + s)) break;
        }
      }
      const unsigned tp = (unsigned)tp64;
      *flw(2 + w, 0) = s + 1;
      if (s < NSEG - 1) {
        const v4u tv = *(volatile LAS v4u*)flw(12, 0);
        const int lr = olane();
        __builtin_amdgcn_raw_buffer_store_b32(min(((tp - tv[1]) * tv[2]) >> 16, 60000u), rs,
                                              ((cls_of(perm, lr & 3) ^ xn) << 12) | ((lr & 4) << 9) | ((lr & 8) << 12) | ((lr >> 4) << 28),
                                              so + (int)UNIT + R_ST + (c0 + w) * 32 + 4, AUX_SC1);
      }
      OPQ(vs, l16);
#pragma unroll
      for (int k = 0; k < 8; ++k) *(LAS v4u*)(L + w * 8192 + ((k ^ (4 * eg)) * 1024) + vs) = bl[k];
      asm volatile("s_waitcnt lgkmcnt(0)" ::: "memory");
      *flw(4 + w, 0) = s + 1;
      if (s < NSEG - 4) {
        lds_wait_ge(2, 1, s + 1, tmo, 0x3001a);
        v4u se;
        asm volatile("v_mov_b32 %0, -1" : "=v"(se[0]));
        se[1] = se[2] = se[3] = se[0];
#pragma unroll
        for (int k = 0; k < 8; ++k) __builtin_amdgcn_raw_buffer_store_b128(se, rs, vs + (k & 3) * 1024, sv + (k >> 2) * 32768, AUX_SC1);
        asm volatile("s_nop 1" ::"v"(se));
      }
      if (s < NSEG - 2) {
        wait_cyc(tp + dtc_);
        const int lt = olane();
        const unsigned tc = __builtin_amdgcn_raw_buffer_load_b32(rs, lt < 40 ? lt * 128 : OOB, so + R_PX + ((li & ~1) + w) * 5120, AUX_SC1);
        asm volatile("" ::"v"(tc));
      }
      EVDUMP(s);
    }
  } else if (w < 6) {
    const int n = w - 2;
    const unsigned vpm_ = perm | (unsigned)((r_a + (int)(OFF_VWX / UNIT)) & 7) << 12;
    const int dox_ = TN(10, DOX2), pace_ = TN(11, PACE);
    v4u raw[2][8];
#define RAW_LD(dst, sg)                                                                                       \
  _Pragma("unroll") for (int t = 0; t < 2; ++t) _Pragma("unroll") for (int k = 0; k < 8; ++k) dst[t][k] =     \
      __builtin_amdgcn_raw_buffer_load_b128(rs, l16 + (k & 3) * 1024, vw_addr(vpm_, c0 + t, sg, 2 * n + (k >> 2)), AUX_SC1);
#pragma unroll
    for (int k = 0; k < 16; ++k) asm volatile("" : "=v"(raw[k >> 3][k & 7]));
    _Pragma("clang loop unroll(disable)") for (unsigned spins = 0, got = 0; got != 255u;) {
      OPQ(vfl, F_VW + flag_of(team, 8 * n + (olane() & 7)));
      const unsigned nw = (unsigned)__builtin_amdgcn_ballot_w64(__builtin_amdgcn_raw_buffer_load_b32(rs, vfl, 0, AUX_SC1) == 0u) & 255u & ~got;
      got |= nw;
#pragma unroll
      for (int k = 0; k < 8; ++k)
        if (nw >> k & 1u) {
#pragma unroll
          for (int t = 0; t < 2; ++t)
            raw[t][k] = __builtin_amdgcn_raw_buffer_load_b128(rs, l16 + (k & 3) * 1024, vw_addr(vpm_, c0 + t, team, 2 * n + (k >> 2)), AUX_SC1);
        }
      if (!nw) __builtin_amdgcn_s_sleep(OSL);
      if (spin_fail(spins, tmo, 0x30018)) break;
    }
    if (FLC) VMCNT(0);
    int nrdy = team;
#pragma unroll 1
    for (int s = team; s < NSEG; s += 2) {
      EVDECL;
      const int so = (int)OFF_STEP + s * (int)UNIT;
      if (s < NSEG - 1) {
        const int ln = olane();
        OPQ(eidx, (ln & 4) ? 0xeeee : 0x4444);
        OPQ(vea, (ln & 4) * 128 + (ln & 8) * 32 + (ln >> 4) * 64 + (ln & 3) * 16);
        int se = so + R_EX + n * 32768 + ((cown ^ xreg(r_s + s)) << 12);
        v8u bx[2][4];
#pragma unroll
        for (int t = 0; t < 2; ++t)
#pragma unroll
          for (int k = 0; k < 4; ++k) {
            const v4u u = raw[t][2 * k], v = raw[t][2 * k + 1];
            bx[t][k] = v8u{u[0], v[0], u[1], v[1], u[2], v[2], u[3], v[3]};
            asm volatile("" : "+v"(bx[t][k]));
          }
        OPQ(vd, L_D + n * 2048 + (ln & 31) * 16 + (ln >> 5) * 8);
        f32x4 d[2][4];
#pragma unroll
        for (int t = 0; t < 2; ++t)
#pragma unroll
          for (int k = 0; k < 4; ++k) d[t][k] = zero4();
        asm volatile("" : "+s"(se));
        EV(3);
        asm volatile(AL(A_OW_D));
        BARE_BARRIER();
        if (EDL) asm volatile("s_nop %0" ::"n"(EDL > 0 ? EDL - 1 : 0));
        EV(0);
#define E_PACK(t, k) v2u{pk2(d[t][k][0], d[t][k][1]), pk2(d[t][k][2], d[t][k][3])}
        _Pragma("clang loop unroll(disable)") for (unsigned spins = 0;;) {
          v4u pv[4];
          asm volatile("" : "+v"(vea));
#pragma unroll
          for (int k = 0; k < 4; ++k) pv[k] = __builtin_amdgcn_raw_buffer_load_b128(rs, vea + k * 1024, se, AUX_SC1);
          unsigned m = 0;
          v2u cv[2][4];
          SB();
#pragma unroll
          for (int k = 0; k < 4; ++k) {
            d[0][k] = smf(pv[k], bx[0][k], d[0][k], eidx);
            m = mx4(m, pv[k]);
            asm volatile("" : "+v"(m), "+v"(d[0][k]));
            d[1][k] = smf(pv[k], bx[1][k], d[1][k], eidx);
            asm volatile("" : "+v"(d[1][k]));
            if (k > 0) {
              const int j = k > 0 ? k - 1 : 0;
              asm volatile("" : "+v"(d[0][j]), "+v"(d[1][j]));
              cv[0][j] = E_PACK(0, j);
              cv[1][j] = E_PACK(1, j);
              *(LAS v2u*)(L + vd + j * 512) = cv[0][j];
              *(LAS v2u*)(L + vd + j * 512 + 8192) = cv[1][j];
            }
            SB();
          }
          EV(1);
          *(LAS v2u*)(L + vd + 1536) = E_PACK(0, 3);
          SB();
          *(LAS v2u*)(L + vd + 9728) = E_PACK(1, 3);
          if (__builtin_expect(!anysent(m), 1)) break;
          __builtin_amdgcn_s_sleep(RSO);
          if (spin_fail(spins, tmo, 0x10000 + s)) break;
#pragma unroll
          for (int t = 0; t < 2; ++t)
#pragma unroll
            for (int k = 0; k < 4; ++k) d[t][k] = zero4();
        }
        EV(2);
        RAW_BARRIER();
      } else BARE_BARRIER();
      v4u nx[2][8];
      const int sn = min(s + 2, NSEG - 1);
      unsigned tpb = 0;
      if (s < NSEG - 2) {
        __builtin_amdgcn_s_sleep(5);
        lds_wait_ge(2, 1, s + 1, tmo, 0x30014);
        tpb = cyc32();
        const unsigned tw = tpb + dox_;
        _Pragma("clang loop unroll(disable)") for (unsigned spins = 0; nrdy < s + 2;) {
          const int lf = olane();
          OPQ(vfl, F_VW + flag_of(min(s + 2 + 2 * (lf >> 3), NSEG - 2 + team), 8 * n + (lf & 7)));
          const u64 miss = __builtin_amdgcn_ballot_w64(__builtin_amdgcn_raw_buffer_load_b32(rs, vfl, 0, AUX_SC1) != 0u);
          nrdy = s + 2 * (miss ? __builtin_ctzll(miss) >> 3 : 8);
          if (nrdy >= s + 2) break;
          __builtin_amdgcn_s_sleep(8);
          if (spin_fail(spins, tmo, 0x30018)) break;
        }
        wait_cyc(tw);
      }
      EV(6);
      OPQ(va, s < NSEG - 2 ? l16 : OOB);
#define NX_LD(dst, q0, q1)                                                                              \
  _Pragma("unroll") for (int q = q0; q < q1; ++q) dst[q >> 3][q & 7] = __builtin_amdgcn_raw_buffer_load_b128( \
      rs, va + (q & 3) * 1024, vw_addr(vpm_, c0 + (q >> 3), sn, 2 * n + ((q >> 2) & 1)), AUX_SC1);
      NX_LD(nx, 0, NX1)
      SB();
      if (s < NSEG - 2) wait_cyc(cyc32() + ((n + (li >> 1)) & 3) * pace_);
      else if (s == NSEG - 2 || !TLW) lds_wait_ge(4, 1, s + 1, tmo, 0x30014);
      else wait_cyc(cyc32() + TLW);
      EV(4);
      f32x4 ac[2][2];
      const int sx = so + R_PX + n * 8192, sy = so + R_PX + 32768 + n * 2048;
      v4u pv[2][10], vm[2][2];
      OPQ(vb, l16);
#define P_LD                                                                                         \
  _Pragma("unroll") for (int k = 0; k < 10; ++k) _Pragma("unroll") for (int rt = 0; rt < 2; ++rt) pv[rt][k] = \
      __builtin_amdgcn_raw_buffer_load_b128(rs, vb + (k & 3) * 1024, (k < 8 ? sx + (k >> 2) * 4096 : sy) + rt * 40960, AUX_SC1);
#define P_MM(k0, k1)                                                                                 \
  _Pragma("unroll") for (int k = k0; k < k1; ++k) _Pragma("unroll") for (int rt = 0; rt < 2; ++rt) {  \
    _Pragma("unroll") for (int t = 0; t < 2; ++t)                                                    \
      ac[rt][t] = mma(k < 8 ? raw[t][k & 7] : vm[t][k & 1], pv[rt][k], k == 0 ? f32x4{0.f, 0.f, 0.f, 0.f} : ac[rt][t]); \
    asm volatile("" : "+v"(ac[rt][0]), "+v"(ac[rt][1]));                                             \
  }
      P_LD
      SB();
      NX_LD(nx, NX1, NX1 + NX2)
      SB();
      lds_wait_ge(4, 1, s + 1, tmo, 0x30014);
#pragma unroll
      for (int t = 0; t < 2; ++t)
#pragma unroll
        for (int k = 0; k < 2; ++k) vm[t][k] = *(const LAS v4u*)(L + vb + t * 8192 + n * 2048 + k * 1024);
      SB();
      P_MM(0, 10)
      if (__builtin_expect(__builtin_amdgcn_ballot_w64(__builtin_isunordered(ac[0][0][0], ac[1][0][0])) != 0ull, 0)) {
        _Pragma("clang loop unroll(disable)") for (unsigned spins = 0;;) {
          asm volatile("" : "+v"(vb));
          P_LD
          unsigned m = 0;
#pragma unroll
          for (int k = 0; k < 10; ++k) m = mx4(mx4(m, pv[0][k]), pv[1][k]);
          if (!anysent(m)) break;
          __builtin_amdgcn_s_sleep(8);
          if (spin_fail(spins, tmo, 0x30015)) break;
        }
        P_MM(0, 10)
      }
      EV(7);
      NX_LD(raw, NX1 + NX2, 16)
      SB();
      OPQ(vr, L_R + (s & 2) * 8192 + l16);
#pragma unroll
      for (int q = 0; q < 4; ++q) *(LAS f32x4*)(L + vr + q * 4096 + n * 1024) = ac[q >> 1][q & 1];
      asm volatile("s_waitcnt lgkmcnt(0)" ::: "memory");
      fl[20 + n] = s + 1;
      lds_wait_ge(20, 3, s + 1, tmo, 0x30016);
      {
        const LAS f32x4* pr = (const LAS f32x4*)(L + vr + n * 4096);
        const f32x4 o = (pr[0] + pr[64]) + (pr[128] + pr[192]);
        const int lo = olane();
        __builtin_amdgcn_raw_buffer_store_b64(v2u{pk2(o[0], o[1]), pk2(o[2], o[3])}, __builtin_amdgcn_make_buffer_rsrc(out, 0, S * 4096, 0x00020000),
                                              (lo & 15) * 8192 + (lo >> 4) * 8, (s * 32 + (n >> 1)) * 4096 + 2048 + 32 * (c0 + (n & 1)), OAUXX);
      }
      EV(5);
#pragma unroll
      for (int q = 0; q < NX1 + NX2; ++q) raw[q >> 3][q & 7] = nx[q >> 3][q & 7];
      EVDUMP(s);
    }
  } else if (w == 6) {
    if (c == 1) hs_lead<64>(rs, false, CNT_HS, 192u);
#pragma unroll 1
    for (int s = team; s + NARM - 2 < NSEG; s += 2) {
      VMCNT(0);
      BARE_BARRIER();
      if (FLC && s == team && olane() == 0) __hip_atomic_fetch_add((gu32*)(ws + CNT_FFT1), 1u, RLX_AGENT);
      if (s + NARM >= NSEG) break;
      BARE_BARRIER();
      __builtin_amdgcn_s_sleep(5);
      lds_wait_ge(2, 1, s + 1, tmo, 0x3001b);
      if (APR) __builtin_amdgcn_s_setprio(APR);
      ARM_REC(s + NARM)
      if (s + NARM + 1 < NSEG) ARM_REC(s + NARM + 1)
      if (APR) __builtin_amdgcn_s_setprio(0);
      if ((WBL & 4) && li == 0 && s % WBP == WBQ) asm volatile("buffer_wbl2 sc1" ::: "memory");
    }
  } else {
    const int sdk_ = NS2F(TN(2, SDK)), cv2_ = NS2F(TN(4, CV2));
    int cv1_ = NS2F(TN(3, CV1));
    bool clean = false;
    Clk ck;
    clk_init(ck);
    unsigned t1 = cyc32();
    const int mg_ = NS2F(TN(0, MARG)), tol_ = NS2F(TN(13, TOL)), xl_ = NS2F(TN(14, XLATE));
    Pace pc = {0u, NS2F(ED0), NS2F(EA0), 0};
    const int sti_ = TN(15, STI), ofd_ = NS2F(TN(9, OFD));
#pragma unroll 1
    for (int s = 0; s < NSEG; ++s) {
      EVDECL;
      const bool on = (s & 1) == team;
      asm volatile(AL(A_OW_T));
      EV(3);
      if (on ? (int)(t1 - cyc32()) > OCK : s < 4) {
        wait_cyc(t1 - OCK);
        clk_update<OTRY>(ck, s < 4 ? 12 : OGN);
      }
      int k1 = ((cv1_ * (int)ck.cq) >> 16) - OK1, k2 = (cv2_ * (int)ck.cq) >> 16;
      int sst = (int)OFF_STEP + s * (int)UNIT + R_ST + ((cown ^ xreg(r_s + s)) << 12);
      asm volatile("" : "+s"(k1), "+s"(k2), "+s"(sst));
      OPQ(va, lane * 32);
      OPQ(vt, L_F + 48);
      wait_cyc(t1);
      __builtin_amdgcn_s_setprio(3);
      EV(0);
      v2u st = __builtin_amdgcn_raw_buffer_load_b64(rs, va, sst, AUX_SC1);
      if (__builtin_expect(anysent(st[0]), 0)) _Pragma("clang loop unroll(disable)") for (unsigned spins = 0;;) {
        if (spins >= 2) __builtin_amdgcn_s_sleep(4);
        if (spin_fail(spins, tmo, 0x3000b)) break;
        asm volatile("" : "+v"(va));
        wait_cyc(t1 + spins * sti_);
        st = __builtin_amdgcn_raw_buffer_load_b64(rs, va, sst + (spins == 1 ? 2048 : (spins == 2 ? 32768 : 0)), AUX_SC1);
        if (spins > 2) clk_update(ck, 8);
        if (!anysent(st[0])) break;
      }
      EV(4);
      u64 tn;
      asm volatile("s_memtime %0" : "=s"(tn), "+v"(st));
      unsigned md;
      const int dm = stamp_max(ck, st, md);
      const unsigned tr = clk_cyc(ck, dm);
      if (on) {
        asm volatile("s_waitcnt lgkmcnt(0)" : "+s"(tn));
        *(volatile LAS v4u*)(L + vt) = v4u{tr + k2, tr, ck.inv, 0u};
        EV(1);
        int d = (int)(tr + k1 - (unsigned)tn);
        d = (unsigned)d > (unsigned)CMAX ? 0 : d;
        asm volatile(".p2align 4\n1: s_sub_u32 %0, %0, 28\n\ts_cbranch_scc0 1b\n\ts_barrier" : "+s"(d)::"scc", "memory");
        if (s < NSEG - 1) BARE_BARRIER();
      }
      __builtin_amdgcn_s_setprio(0);
      EV(5);
      const bool ol = md - (unsigned)(pc.ed + NS2F(COT)) < 60000u;
      if (s > 8) cv1_ = ol && clean ? min(cv1_ + CUP, NS2F(CHI)) : max(cv1_ - CDN, NS2F(CLO));
      pace_step(pc, s, (ck.r << 4) + dm, md, mg_, tol_, xl_);
      clean = !ol && pc.dq == pc.ed + mg_;
      if (c == 0 && (s & 3) == 1) __builtin_amdgcn_raw_buffer_store_b32((unsigned)s, rs, olane() << 28, OFF_PROG, AUX_SC1);
      t1 = clk_cyc(ck, dm + pc.dq + pc.ea - sdk_ + (on ? ofd_ : 0));
    }
  }
  return 0u;
}
#define STG2 0
#define STG3 0
#define FTL 0
#define CPE 0
#define FSL 32
__global__ __launch_bounds__(512, 2) void mega_kernel(const u16* __restrict__ x, const u16* __restrict__ mem0,
                                                      const u16* __restrict__ Wq, const u16* __restrict__ Wk,
                                                      const u16* __restrict__ Wv, const u16* __restrict__ Wo,
                                                      char* __restrict__ ws, u16* __restrict__ out,
                                                      const u16* __restrict__ ax, const u16* __restrict__ awr,
                                                      const u16* __restrict__ awi) {
  asm volatile(".Lkbeg:");
  if (CPE) {
    unsigned keep, t;
    const int w0 = __builtin_amdgcn_readfirstlane(threadIdx.x >> 6);
    asm volatile("s_getpc_b64 vcc\n.Lce:\n\ts_mov_b32 %0, m0\n\ts_mov_b32 m0, %4\n\ts_mov_b32 %1, .Lrole0-.Lce\n\t"
                 "s_cmp_eq_u32 %3, 1\n\ts_cselect_b32 %1, .Lrole1-.Lce, %1\n\ts_cmp_eq_u32 %3, 2\n\ts_cselect_b32 %1, .Lrole2-.Lce, %1\n\t"
                 "s_cmp_eq_u32 %3, 3\n\ts_cselect_b32 %1, .Lrole3-.Lce, %1\n\t"
                 "s_add_u32 vcc_lo, vcc_lo, %1\n\ts_addc_u32 vcc_hi, vcc_hi, 0\n\t"
                 "global_load_lds_dword %2, vcc\n\ts_mov_b32 m0, %0"
                 : "=&s"(keep), "=&s"(t)
                 : "v"(((threadIdx.x & 7) | (threadIdx.x >> 7) << 3) * 128), "s"((w0 & 1) | (blockIdx.x & 4) >> 1),
                   "s"((blockIdx.x & 132) == 128 ? 160000 + w0 * 256 : 32768 + w0 * 2048)
                 : "vcc", "scc", "memory");
  }
  ws += (size_t)(-(uintptr_t)ws) & (UNIT - 1);
  const int bx = blockIdx.x;
  const int w = __builtin_amdgcn_readfirstlane(threadIdx.x >> 6);
  const int xg = bx & 7, idx = bx >> 3;
#define TEND
#define TMID(k)
  const u64 nonce = hs_post(__builtin_amdgcn_make_buffer_rsrc(ws, 0, 65536, 0x00020000), w);
  TMID(0);
  int role, h, par = 0;
  if (xg < NXC) {
    role = idx >= 2 * PQ ? 4 : idx >= PQ;
    h = xg * PQ + idx % PQ;
  } else {
    role = 2 + (idx / PQ & 1);
    par = idx / (2 * PQ);
    h = (xg - NXC) * PQ + idx % PQ;
  }
  if (CPF && role == 4 && (w == 1 || w == 2)) code_touch(idx - 16 * (w - 1), 160000 + 256 * w);
  if (FTL && role == 4 && w >= 3 && w < 6) {
    unsigned keep;
    asm volatile("s_mov_b32 %0, m0\n\ts_mov_b32 m0, %3\n\ts_nop 0\n\tglobal_load_lds_dword %1, %2\n\ts_mov_b32 m0, %0"
                 : "=&s"(keep)
                 : "v"((xg * PO + idx - 2 * PQ) * (w == 3 ? 32768 : 16416) + (olane() & (FTL - 1)) * (16384 / (FTL ? FTL : 1))),
                   "s"(w == 3 ? ax : (w == 4 ? awr : awi)), "s"(160000 + w * 256)
                 : "memory");
  }
  if (role == 0) wroles<0>(h, par, idx & 7, xg, w, nonce, x, Wq, Wk, Wv, Wo, ws);
  if (role == 1) wroles<1>(h, par, idx & 7, xg, w, nonce, x, Wq, Wk, Wv, Wo, ws);
  if (role == 2) wroles<2>(h, par, idx & 7, xg, w, nonce, x, Wq, Wk, Wv, Wo, ws);
  if (role == 3) wroles<3>(h, par, idx & 7, xg, w, nonce, x, Wq, Wk, Wv, Wo, ws);
  const int oc = xg * PO + idx - 2 * PQ;
  unsigned perm = 0;
  if (role == 4) perm = owner<true>(oc, w, nonce, mem0, ws, out, 0u, w_slow(Wq, Wk, Wv, w, xg), ax, awr, awi);
  TMID(2);
  char* lds = g_lds;
  const __amdgpu_buffer_rsrc_t rs = __builtin_amdgcn_make_buffer_rsrc(ws, 0, 65536, 0x00020000);
  gu32* tmo = (gu32*)(ws + OFF_TMO);
  volatile LAS int* sact = (volatile LAS int*)(g_lds + 163584);
  unsigned idle = 0;
#define TLOG(id)
  auto pick = [&]() __attribute__((always_inline)) {
    int off = CNT_FFT1 + (olane() & 7) * 128;
    asm volatile("" : "+v"(off));
    const unsigned v = __builtin_amdgcn_raw_buffer_load_b32(rs, off, 0, AUX_SC1) + 1u;
    const unsigned d1 = __builtin_amdgcn_readlane(v, 0), d2 = __builtin_amdgcn_readlane(v, 1);
    const unsigned q1 = __builtin_amdgcn_readlane(v, 4), q2 = __builtin_amdgcn_readlane(v, 5), q3 = __builtin_amdgcn_readlane(v, 6);
    int kind = -1, qoff = 0;
    unsigned lim = 0;
    if (q1 < 64u) kind = 1, qoff = Q_FFT1, lim = 64u;
    else if (d1 >= 128u && q2 < 65u) kind = 2, qoff = Q_FFT2, lim = 65u;
    else if (d2 >= 65u && q3 < 128u) kind = 3, qoff = Q_FFT3, lim = 128u;
    else if (q3 >= 128u) kind = -2;
    int act = kind;
    if (kind >= 0) {
      unsigned t = 0;
      if (olane() == 0) t = __hip_atomic_fetch_add((gu32*)(ws + qoff), 1u, RLX_AGENT) + 1u;
      t = __builtin_amdgcn_readfirstlane(t);
      act = t < lim ? (kind << 16) | (int)t : -3;
    }
    return act;
  };
  fft_table(lds, w);
  int done = role == 4 ? CNT_ARM : 0;
  for (;;) {
    if (w == 7) *sact = role == 4 ? (done == CNT_ARM ? 0x10040 | oc : -2) : pick();
    VMCNT(0);
    RAW_BARRIER();
    if (done && w == 0 && olane() == 0) __hip_atomic_fetch_add((gu32*)(ws + done), 1u, RLX_AGENT);
    done = 0;
    const int act = __builtin_amdgcn_readfirstlane(*sact);
    RAW_BARRIER();
    if (act == -2) break;
    if (act >= 0) {
      const int kind = act >> 16, t = act & 0xffff;
      for (int k = kind == 3 ? t * STG3 >> 1 : (kind == 2 && t < 32 ? t * STG2 : 0); k > 0; --k) __builtin_amdgcn_s_sleep(1);
      TLOG(1000 + kind);
      if (kind == 1) {
        fft_pass1(ws, ax, awr, awi, t ^ 64, w, role == 4 ? x : nullptr);
        done = CNT_FFT1;
        if (FLC && role == 4) break;
      } else if (kind == 2) {
        fft_pass2(ws, t == 0 ? 64 : t - 1, w);
        done = CNT_FFT2;
      } else fft_pass3(ws, out, t, w);
      TLOG(999);
    } else if (act == -1) {
      __builtin_amdgcn_s_sleep(FSL);
      if (spin_fail(idle, tmo, 0x30022)) break;
    }
  }
  if (role == 4) {
    owner<false>(oc, w, nonce, mem0, ws, out, perm, nullptr, nullptr, nullptr, nullptr);
    return;
  }
  if (WBL & 1)
    if (w == 0) asm volatile("buffer_wbl2 sc1\n\ts_waitcnt vmcnt(0)" ::: "memory");
  TEND;
}

extern "C" void kernel_launch(void* const* d_in, const int* in_sizes, int n_in, void* d_out, int out_size, void* d_ws,
                              size_t ws_size, hipStream_t stream) {
  const u16* A_x = (const u16*)d_in[0];
  const u16* A_wr = (const u16*)d_in[1];
  const u16* A_wi = (const u16*)d_in[2];
  const u16* B_x = (const u16*)d_in[3];
  const u16* B_mem0 = (const u16*)d_in[4];
  const u16* Wq = (const u16*)d_in[5];
  const u16* Wk = (const u16*)d_in[6];
  const u16* Wv = (const u16*)d_in[7];
  const u16* Wo = (const u16*)d_in[8];
  u16* out = (u16*)d_out;
  char* ws = (char*)d_ws;

  mega_kernel<<<256, 512, 0, stream>>>(B_x, B_mem0, Wq, Wk, Wv, Wo, ws, out, A_x, A_wr, A_wi);
}
```

```cpp
#include <hip/hip_runtime.h>
#include <hip/hip_bf16.h>
#include <stdint.h>
#include <type_traits>

typedef unsigned short u16;
typedef unsigned long long u64;
using bf16x8 = __attribute__((ext_vector_type(8))) __bf16;
using bf16x16 = __attribute__((ext_vector_type(16))) __bf16;
using f32x4 = __attribute__((ext_vector_type(4))) float;
typedef unsigned v32u __attribute__((ext_vector_type(32)));
typedef unsigned v16u __attribute__((ext_vector_type(16)));
typedef unsigned v8u __attribute__((ext_vector_type(8)));
typedef unsigned v4u __attribute__((ext_vector_type(4)));
typedef unsigned v2u __attribute__((ext_vector_type(2)));
typedef short s16x4 __attribute__((ext_vector_type(4)));
typedef float f32x2 __attribute__((ext_vector_type(2)));
#define GAS __attribute__((address_space(1)))
#define LAS __attribute__((address_space(3)))
typedef GAS unsigned gu32;
#define RLX_AGENT __ATOMIC_RELAXED, __HIP_MEMORY_SCOPE_AGENT

constexpr int S = 2048, D = 1024, H = 32, DH = 128, SEG = 32, NSEG = 64;
constexpr int DF = 513;

constexpr int NXC = 4;
constexpr int PQ = 32 / NXC, PO = 64 / NXC;

__shared__ __attribute__((aligned(16))) char g_lds[163840];
#define SPINCAP (1u << 18)
constexpr unsigned kSpinCap = SPINCAP;
constexpr int AUX_SC1 = 16;
constexpr int OOB = 0x7ff00000;
constexpr size_t UNIT = 524288;
constexpr int F_QK = 0, F_VW = 8192, CNT_FFT1 = 16384, CNT_FFT2 = 16512, CNT_ARM = 16640;
constexpr int Q_FFT1 = 16896, Q_FFT2 = 17024, Q_FFT3 = 17152;
constexpr size_t OFF_TMO = 20480;
#define CST 128
constexpr size_t OFF_CAL = 28672;
constexpr int OFF_PERM = 25600;
constexpr int OFF_PROG = 24576;
constexpr size_t ARM_HOST = 28672 + (64 * CST + 1023) / 1024 * 1024;
constexpr int OFF_REQ = 49152, OFF_ACK = 53248, CNT_HS = 17280, CNT_HSO = 17408;
constexpr size_t OFF_SCR = 65536;
constexpr size_t OFF_VM = UNIT;
constexpr int VM_SLOT = 4 << 20, VM_SLOT2 = 8 << 20;
constexpr size_t OFF_DBG = 3 * UNIT;
constexpr size_t OFF_STEP = 27 * UNIT;
constexpr int R_MEM = 0, R_EX = 131072, R_PM = 262144, R_ST = 327680, R_PX = 393216, R_DUM = 475136;
constexpr size_t OFF_QK = (size_t)48 << 20;
constexpr size_t OFF_X1 = (size_t)80 << 20;
constexpr size_t OFF_VWX = (size_t)89 << 20;
constexpr size_t WS_USED = (size_t)154 << 20;
constexpr size_t OFF_WT = (size_t)154 << 20;
constexpr int NARM = 4;

__device__ __forceinline__ int olane() {
  int t;
  asm volatile("v_mbcnt_lo_u32_b32 %0, -1, 0\n\tv_mbcnt_hi_u32_b32 %0, -1, %0" : "=v"(t));
  __builtin_assume(t >= 0 && t < 64);
  return t;
}
__device__ __forceinline__ float bf2f(u16 v) { return __uint_as_float((unsigned)v << 16); }
__device__ __forceinline__ unsigned pk2(float a, float b) {
  typedef __bf16 b2 __attribute__((ext_vector_type(2)));
  f32x2 f = {a, b};
  b2 r = __builtin_convertvector(f, b2);
  return __builtin_bit_cast(unsigned, r);
}
__device__ __forceinline__ unsigned pk2s(float a, float b) { return min(pk2(a, b), 0xfffffffeu); }
__device__ __forceinline__ u16 f2bf(float f) { return (u16)(pk2(f, 0.f) & 0xffffu); }
__device__ __forceinline__ float rbf(float f) { return __uint_as_float(pk2(0.f, f)); }
__device__ __forceinline__ float opq_one() {
  float o = 1.f;
  asm("" : "+s"(o));
  return o;
}
__device__ __forceinline__ unsigned fold_pk(f32x4 a, float one) { return pk2(a[0] + a[2], __builtin_fmaf(a[1], one, a[3])); }
__device__ __forceinline__ float mul1(float a, float c) {
  float r;
  asm("v_mul_f32 %0, %2, %1" : "=v"(r) : "v"(a), "s"(c));
  return r;
}
__device__ __forceinline__ float fma1(float a, float c, float b) {
  float r;
  asm("v_fma_f32 %0, %1, %2, %3" : "=v"(r) : "v"(a), "s"(c), "v"(b));
  return r;
}
__device__ __forceinline__ int flag_of(int seg, int h) { return h * 256 + (seg & 1) * 128 + (seg >> 1) * 4; }
__device__ __forceinline__ int xreg(int r) { return ((r & 1) << 2) | ((r >> 1) & 1) | ((r >> 1) & 2); }

__device__ __forceinline__ void tmo_report(gu32* tmo, unsigned code) {
  unsigned z = 0, c = code;
  asm volatile("" : "+v"(z), "+v"(c));
  asm volatile("global_store_dword %0, %1, %2 sc1" ::"v"(z), "v"(c), "s"(tmo) : "memory");
}
__device__ __forceinline__ bool spin_fail(unsigned& spins, gu32* tmo, unsigned code) {
  asm volatile("" ::: "memory");
  if (__builtin_expect(++spins > kSpinCap, 0)) {
    tmo_report(tmo, code);
    return true;
  }
  return false;
}
__device__ __forceinline__ unsigned wave_min(unsigned v) {
  v = min(v, (unsigned)__builtin_amdgcn_mov_dpp((int)v, 0x121, 0xf, 0xf, true));
  v = min(v, (unsigned)__builtin_amdgcn_mov_dpp((int)v, 0x122, 0xf, 0xf, true));
  v = min(v, (unsigned)__builtin_amdgcn_mov_dpp((int)v, 0x124, 0xf, 0xf, true));
  v = min(v, (unsigned)__builtin_amdgcn_mov_dpp((int)v, 0x128, 0xf, 0xf, true));
  auto r = __builtin_amdgcn_permlane32_swap(v, v, false, false);
  v = min(r[0], r[1]);
  auto q = __builtin_amdgcn_permlane16_swap(v, v, false, false);
  return min(q[0], q[1]);
}
__device__ __forceinline__ float xmax16_32(float x) {
  auto r = __builtin_amdgcn_permlane32_swap(__float_as_uint(x), __float_as_uint(x), false, false);
  x = __builtin_amdgcn_fmed3f(__uint_as_float(r[0]), __uint_as_float(r[1]), __builtin_inff());
  auto q = __builtin_amdgcn_permlane16_swap(__float_as_uint(x), __float_as_uint(x), false, false);
  return __builtin_amdgcn_fmed3f(__uint_as_float(q[0]), __uint_as_float(q[1]), __builtin_inff());
}
__device__ __forceinline__ float xsum16(float x) {
  auto q = __builtin_amdgcn_permlane16_swap(__float_as_uint(x), __float_as_uint(x), false, false);
  return __uint_as_float(q[0]) + __uint_as_float(q[1]);
}
__device__ __forceinline__ float xsum16_32(float x) {
  auto r = __builtin_amdgcn_permlane32_swap(__float_as_uint(x), __float_as_uint(x), false, false);
  return xsum16(__uint_as_float(r[0]) + __uint_as_float(r[1]));
}
__device__ __forceinline__ unsigned mx4(unsigned m, v4u v) { return max(max(max(max(m, v[0]), v[1]), v[2]), v[3]); }
__device__ __forceinline__ bool anysent(unsigned m) { return __builtin_amdgcn_ballot_w64(m == 0xffffffffu) != 0ull; }

__device__ __forceinline__ unsigned now32() { return (unsigned)__builtin_amdgcn_s_memrealtime(); }
__device__ __forceinline__ unsigned cyc32() { return (unsigned)__builtin_amdgcn_s_memtime(); }
#define CMAX 14000
__device__ __forceinline__ void wait_cyc(unsigned t) {
  t = __builtin_amdgcn_readfirstlane(t);
  int d = (int)(t - cyc32());
  if (d > CMAX) return;
  while (d > 260) {
    __builtin_amdgcn_s_sleep(1);
    d = (int)(t - cyc32());
  }
  d = __builtin_amdgcn_readfirstlane(d - 70);
  if (d > 0) asm volatile(".p2align 4\n1: s_sub_u32 %0, %0, 28\n\ts_cbranch_scc0 1b" : "+s"(d)::"scc", "memory");
}
#define CLKSPAN 124u
struct Clk {
  unsigned r, m;
  unsigned cq, inv;
};
__device__ __forceinline__ bool clk_edge(unsigned& r, unsigned& m) {
  u64 t[14], m0, m1;
  unsigned n;
#define CE_S(i) "s_memrealtime %" #i "\n\ts_nop 0\n\t"
  asm volatile("s_setprio 3\n\ts_memtime %14\n\t" CE_S(0) CE_S(1) CE_S(2) CE_S(3) CE_S(4) CE_S(5) CE_S(6) CE_S(7) CE_S(8) CE_S(9) CE_S(10)
               CE_S(11) CE_S(12) "s_memtime %15\n\ts_memrealtime %13\n\ts_setprio 0\n\ts_waitcnt lgkmcnt(0)"
               : "=s"(t[0]), "=s"(t[1]), "=s"(t[2]), "=s"(t[3]), "=s"(t[4]), "=s"(t[5]), "=s"(t[6]), "=s"(t[7]), "=s"(t[8]), "=s"(t[9]),
                 "=s"(t[10]), "=s"(t[11]), "=s"(t[12]), "=s"(t[13]), "=s"(m0), "=s"(m1));
#define CE_C(i) "s_cmp_eq_u32 %" #i ", %1\n\ts_addc_u32 %0, %0, 0\n\t"
#define CE_T(i) "s"((unsigned)t[i])
  asm("s_mov_b32 %0, 0\n\t" CE_C(2) CE_C(3) CE_C(4) CE_C(5) CE_C(6) CE_C(7) CE_C(8) CE_C(9) CE_C(10) CE_C(11) CE_C(12) CE_C(13) CE_C(14)
      : "=&s"(n)
      : CE_T(0), CE_T(1), CE_T(2), CE_T(3), CE_T(4), CE_T(5), CE_T(6), CE_T(7), CE_T(8), CE_T(9), CE_T(10), CE_T(11), CE_T(12), CE_T(13)
      : "scc");
  r = (unsigned)t[0] + 4;
  m = (unsigned)m0 + n * 8;
  return (unsigned)m1 - (unsigned)m0 <= CLKSPAN;
}
__device__ __forceinline__ void clk_init(Clk& c) {
#pragma unroll 1
  for (int k = 0; k < 3; ++k)
    if (clk_edge(c.r, c.m)) break;
  c.cq = 92500u;
  c.inv = 46432u;
}
template <int TRIES = 1>
__device__ __forceinline__ void clk_update(Clk& c, int gain) {
  unsigned r, m;
  bool ok = clk_edge(r, m);
#pragma unroll 1
  for (int k = 1; k < TRIES && !ok; ++k) ok = clk_edge(r, m);
  ok |= r - c.r >= 1500u;
  const unsigned dr = r - c.r;
  const int err = (int)(m - c.m - ((dr * c.cq) >> 12));
  unsigned q = c.cq + (unsigned)((ok & (dr * gain < 6000u)) ? err * gain : 0);
  asm("s_max_i32 %0, %0, 0x101d0\n\ts_min_i32 %0, %0, 0x1b580" : "+s"(q)::"scc");
  c.cq = q;
  c.inv -= (unsigned)__mulhi((int)c.inv, (int)(q * c.inv));
  c.r = ok ? r : c.r;
  c.m = ok ? m : c.m;
}
__device__ __forceinline__ int stamp_max(const Clk& c, v2u st, unsigned& md) {
  unsigned a = st[0] - (c.r << 4) + 0x40000000u, b = st[1], x;
#define SM_S(p) "v_max_u32_dpp %2, %2, %2 " p "\n\tv_max_u32_dpp %3, %3, %3 " p "\n\ts_nop 0\n\t"
  asm volatile("s_nop 1\n\t" SM_S("row_ror:1 row_mask:0xf bank_mask:0xf") SM_S("row_ror:2 row_mask:0xf bank_mask:0xf")
               SM_S("row_ror:4 row_mask:0xf bank_mask:0xf") SM_S("row_ror:8 row_mask:0xf bank_mask:0xf")
               SM_S("row_bcast:15 row_mask:0xa bank_mask:0xf") SM_S("row_bcast:31 row_mask:0xc bank_mask:0xf")
               "v_readlane_b32 %0, %2, 63\n\tv_readlane_b32 %1, %3, 63"
               : "=s"(x), "=s"(md), "+v"(a), "+v"(b));
  return (int)(x - 0x40000000u);
}
__device__ __forceinline__ unsigned clk_cyc(const Clk& c, int d) { return c.m + (unsigned)(((long long)d * (int)c.cq) >> 16); }
#define NS2F(ns) (((ns) * 8) / 5)
struct Pace {
  unsigned rf;
  int ed, ea, dq;
};
__device__ __forceinline__ void pace_step(Pace& p, int s, unsigned rf, unsigned md, int marg, int tol, int xlate) {
  const int arr = s ? (int)(rf - p.rf) - p.dq : 0x10000000;
  const bool late = arr > p.ea + tol;
  p.ea = min(arr, p.ea + 5);
  p.ed = (int)min(md, (unsigned)p.ed + 5u);
  p.rf = rf;
  p.dq = p.ed + marg + (late ? xlate : 0);
}

template <int SLP>
__device__ __forceinline__ void wait_flag(__amdgpu_buffer_rsrc_t rs, int off, gu32* tmo, unsigned code) {
  _Pragma("clang loop unroll(disable)") for (unsigned spins = 0;;) {
    asm volatile("" : "+v"(off));
    unsigned v = __builtin_amdgcn_raw_buffer_load_b32(rs, off, 0, AUX_SC1);
    if (__all(v == 0u)) break;
    __builtin_amdgcn_s_sleep(SLP);
    if (spin_fail(spins, tmo, code)) break;
  }
}
__device__ __forceinline__ void arm_wait(__amdgpu_buffer_rsrc_t rs, gu32* tmo) {
  _Pragma("clang loop unroll(disable)") for (unsigned spins = 0;;) {
    int off = CNT_ARM;
    asm volatile("" : "+v"(off));
    if (__all(__builtin_amdgcn_raw_buffer_load_b32(rs, off, 0, AUX_SC1) + 1u >= 64u)) break;
    __builtin_amdgcn_s_sleep(8);
    if (spin_fail(spins, tmo, 0x30007)) break;
  }
}
#define RAW_BARRIER()                                    \
  do {                                                   \
    asm volatile("" ::: "memory");                       \
    __builtin_amdgcn_s_waitcnt(0xc07f);                  \
    __builtin_amdgcn_s_barrier();                        \
    asm volatile("" ::: "memory");                       \
  } while (0)
#define BARE_BARRIER()                 \
  do {                                 \
    asm volatile("" ::: "memory");     \
    __builtin_amdgcn_s_barrier();      \
    asm volatile("" ::: "memory");     \
  } while (0)
#define VMCNT(n) asm volatile("s_waitcnt vmcnt(" #n ")" ::: "memory")
#define SB() __builtin_amdgcn_sched_barrier(0)
#define OPQ(v, e) int v = (e); asm volatile("" : "+v"(v))
template <bool NT = false>
__device__ __forceinline__ void glds16(const void* base, int voff, unsigned dst) {
  unsigned keep;
  if constexpr (NT)
    asm volatile("s_mov_b32 %0, m0\n\ts_mov_b32 m0, %3\n\ts_nop 0\n\tglobal_load_lds_dwordx4 %1, %2 nt\n\ts_mov_b32 m0, %0"
                 : "=&s"(keep)
                 : "v"(voff), "s"(base), "s"(dst)
                 : "memory");
  else
  asm volatile("s_mov_b32 %0, m0\n\ts_mov_b32 m0, %3\n\ts_nop 0\n\tglobal_load_lds_dwordx4 %1, %2\n\ts_mov_b32 m0, %0"
               : "=&s"(keep)
               : "v"(voff), "s"(base), "s"(dst)
               : "memory");
}
__device__ __forceinline__ const char* w_slow(const u16* Wq, const u16* Wk, const u16* Wv, int w, int xg) {
  const char* p = (const char*)(w < 2 || w == 6 ? Wq : (w < 4 || w == 7 ? Wk : Wv));
  const int sk = 3 - (int)((uintptr_t)p >> 7);
  const int h = w < 6 ? (xg & 3) * 8 + (w & 1) * 4 + ((sk & 7) >> 1) : ((sk + 60) & 63) >> 1;
  return (h >> 3) == (xg & 3) ? p + h * 256 : nullptr;
}
__device__ __forceinline__ void w_touch(const char* p, int row, unsigned dst) {
  unsigned keep;
  const int ln = olane();
  asm volatile("s_mov_b32 %0, m0\n\ts_mov_b32 m0, %3\n\ts_nop 0\n\tglobal_load_lds_dword %1, %2\n\ts_mov_b32 m0, %0"
               : "=&s"(keep)
               : "v"((row + (ln >> 2)) * 8192 + (ln & 3) * 64), "s"(p), "s"(dst)
               : "memory");
}
#define WBL 0
#define A_VO 4
#define A_QK 3
#define A_OW 0
#define A_VO_A A_VO
#define A_VO_B A_VO
#define A_QK_A0 A_QK
#define A_QK_A1 A_QK
#define A_QK_X A_QK
#define A_QK_K A_QK
#define A_OW_F 3
#define A_OW_D 4
#define A_OW_T 2
#define AL_(k) ".p2align 6\n.fill " #k ",4,0xbf800000\n"
#define AL(k) AL_(k)
#define NSL 8
#define CPF 1
constexpr int CODE_LEN = 168960;
__device__ __forceinline__ void code_touch(int part, unsigned dst) {
  unsigned keep;
  const int v = min(part * 8192 + olane() * 128, CODE_LEN - 128);
  asm volatile("s_getpc_b64 vcc\n.Lct%=:\n\ts_sub_u32 vcc_lo, vcc_lo, .Lct%=-.Lkbeg\n\ts_subb_u32 vcc_hi, vcc_hi, 0\n\t"
               "s_mov_b32 %0, m0\n\ts_mov_b32 m0, %2\n\ts_nop 0\n\t"
               "global_load_lds_dword %1, vcc\n\ts_mov_b32 m0, %0"
               : "=&s"(keep)
               : "v"(v), "s"(dst)
               : "vcc", "scc", "memory");
}
__device__ __forceinline__ void code_ahead(int part, unsigned dst) {
  unsigned keep;
  const int v = part * 2048 + (olane() & 15) * 128;
  asm volatile("s_getpc_b64 vcc\n\ts_mov_b32 %0, m0\n\ts_mov_b32 m0, %2\n\ts_nop 0\n\tglobal_load_lds_dword %1, vcc ; AHEAD\n\ts_mov_b32 m0, %0"
               : "=&s"(keep)
               : "v"(v), "s"(dst)
               : "vcc", "memory");
}
__device__ __forceinline__ f32x4 smf(v4u a, v8u b, f32x4 c, int idx) {
  return __builtin_amdgcn_smfmac_f32_16x16x64_bf16(__builtin_bit_cast(bf16x8, a), __builtin_bit_cast(bf16x16, b), c, idx, 0, 0);
}
__device__ __forceinline__ f32x4 smf1(v4u a, v8u b, f32x4 c, int idx) {
  return __builtin_amdgcn_smfmac_f32_16x16x64_bf16(__builtin_bit_cast(bf16x8, a), __builtin_bit_cast(bf16x16, b), c, idx, 0, 1);
}
__device__ __forceinline__ f32x4 mma(v4u a, v4u b, f32x4 c) {
  return __builtin_amdgcn_mfma_f32_16x16x32_bf16(__builtin_bit_cast(bf16x8, a), __builtin_bit_cast(bf16x8, b), c, 0, 0, 0);
}
__device__ __forceinline__ void st128(v4u d, __amdgpu_buffer_rsrc_t rs, int voff, int soff) {
  __builtin_amdgcn_raw_buffer_store_b128(d, rs, voff, soff, AUX_SC1);
  asm volatile("s_nop 1" ::"v"(d));
}
__device__ __forceinline__ f32x4 zero4() {
  f32x2 a, b;
  asm volatile("v_mov_b64 %0, 0" : "=v"(a));
  asm volatile("v_mov_b64 %0, 0" : "=v"(b));
  return f32x4{a[0], a[1], b[0], b[1]};
}
__device__ __forceinline__ f32x2 zero2() {
  f32x2 a;
  asm volatile("v_mov_b64 %0, 0" : "=v"(a));
  return a;
}
__device__ __forceinline__ v4u lo4(v8u v) { return v4u{v[0], v[1], v[2], v[3]}; }
__device__ __forceinline__ v4u hi4(v8u v) { return v4u{v[4], v[5], v[6], v[7]}; }
__device__ __forceinline__ v8u cat8(v4u a, v4u b) { return v8u{a[0], a[1], a[2], a[3], b[0], b[1], b[2], b[3]}; }
__device__ __forceinline__ v32u cat32(v8u a, v8u b, v8u c, v8u d) {
  const v16u x = __builtin_shufflevector(a, b, 0, 1, 2, 3, 4, 5, 6, 7, 8, 9, 10, 11, 12, 13, 14, 15);
  const v16u y = __builtin_shufflevector(c, d, 0, 1, 2, 3, 4, 5, 6, 7, 8, 9, 10, 11, 12, 13, 14, 15);
  return __builtin_shufflevector(x, y, 0, 1, 2, 3, 4, 5, 6, 7, 8, 9, 10, 11, 12, 13, 14, 15, 16, 17, 18, 19, 20, 21, 22, 23, 24, 25, 26,
                                 27, 28, 29, 30, 31);
}
__device__ __forceinline__ v2u trd(LAS char* p) {
  return __builtin_bit_cast(v2u, __builtin_amdgcn_ds_read_tr16_b64_v4i16((LAS s16x4*)p));
}
__device__ __forceinline__ int vm_slot(int s) { return (s & 1) * VM_SLOT + ((s >> 1) & 1) * VM_SLOT2; }
__device__ __forceinline__ int cls_of(unsigned perm, int x) { return (int)((perm >> (3 * x)) & 7u); }

__device__ __forceinline__ v2u numa_load(__amdgpu_buffer_rsrc_t rs, gu32* tmo) {
  unsigned c, xid;
  _Pragma("clang loop unroll(disable)") for (unsigned spins = 0;;) {
    OPQ(a, olane() * CST);
    c = __builtin_amdgcn_raw_buffer_load_b32(rs, a, (int)OFF_CAL, AUX_SC1);
    if (__all(c != 0xffffffffu)) break;
    __builtin_amdgcn_s_sleep(NSL);
    if (spin_fail(spins, tmo, 0x30008)) break;
  }
  xid = (unsigned)__builtin_amdgcn_ds_bpermute(((olane() >> 3) & (NXC - 1)) * PO * 4, (int)c) >> 19;
  return v2u{c, xid};
}
__device__ __forceinline__ unsigned numa_perm(v2u in, LAS unsigned* tab) {
  const int lane = olane();
  const unsigned c = in[0], xid = in[1];
  tab[lane] = 0xffffu;
  tab[64 + lane] = 0xffffu;
  tab[128 + lane] = 0u;
  __builtin_amdgcn_s_waitcnt(0xc07f);
  __hip_atomic_fetch_min(tab + ((c >> 16) & 127u), c & 0xffffu, __ATOMIC_RELAXED, __HIP_MEMORY_SCOPE_WORKGROUP);
  __hip_atomic_fetch_add(tab + 128 + ((c >> 21) & 3u) * 2 + ((c >> 17) & 1u), c & 0xffffu, __ATOMIC_RELAXED, __HIP_MEMORY_SCOPE_WORKGROUP);
  __builtin_amdgcn_s_waitcnt(0xc07f);
  asm volatile("" ::: "memory");
  const unsigned s0 = tab[128 + ((xid >> 2) & 3u) * 2], s1 = tab[129 + ((xid >> 2) & 3u) * 2];
  const bool far = ((lane >> 1) & 1) != (s1 < s0 ? 1 : 0);
  unsigned key = ((tab[(xid & 15u) * 8 + (lane & 7)] + (far ? 0x10000u : 0u)) << 6) | (unsigned)lane;
  if ((lane >> 3) >= NXC) key = 0xffffffffu;
  unsigned perm = 0;
#pragma unroll 1
  for (int r = 0; r < NXC; ++r) {
    const unsigned m = wave_min(key);
    const unsigned bx = (m >> 3) & 7u, bc = m & 7u;
    perm |= bc << (3 * bx);
    if ((unsigned)(lane >> 3) == bx || (unsigned)(lane & 7) == bc) key = 0xffffffffu;
  }
  return __builtin_amdgcn_readfirstlane(perm);
}
#define VRING 32
static_assert(OFF_VWX + (size_t)VRING * 2097152 <= WS_USED, "");
#define VWN 0
__device__ __forceinline__ int vw_addr(unsigned pm, int tile, int seg, int gq) {
  return (int)OFF_VWX + ((((tile >> 3) * VRING + (seg & (VRING - 1))) * 8 + (tile & 7)) * 8 + gq) * 4096;
}

#define QKN 0
constexpr int QK_RT = QKN ? 32768 : 4096;
__device__ __forceinline__ int qk_addr(int mat, int seg, int h, int cl, int ra) {
  return (int)OFF_QK + ((mat * 64 + seg) * 32 + h) * 8192;
}

__device__ __forceinline__ u64 hs_post(__amdgpu_buffer_rsrc_t rs, int w) {
  const u64 n = __builtin_amdgcn_s_memrealtime();
  if (w == 0) __builtin_amdgcn_raw_buffer_store_b64(v2u{(unsigned)n, (unsigned)(n >> 32)}, rs, olane() == 0 ? 0 : OOB,
                                                    OFF_REQ + (int)blockIdx.x * 8, AUX_SC1);
  return n;
}
__device__ __forceinline__ void hs_wait(__amdgpu_buffer_rsrc_t rs, char* ws, u64 n, int cnt) {
  _Pragma("clang loop unroll(disable)") for (unsigned spins = 0; spins < (kSpinCap << 2); ++spins) {
    int z = 0;
    asm volatile("" : "+v"(z));
    const v2u v = __builtin_amdgcn_raw_buffer_load_b64(rs, z, OFF_ACK + (int)blockIdx.x * 8, AUX_SC1);
    if (__all(v[0] == (unsigned)n && v[1] == (unsigned)(n >> 32))) break;
    __builtin_amdgcn_s_sleep(8);
  }
  if (olane() == 0) __hip_atomic_fetch_add((gu32*)(ws + cnt), 1u, RLX_AGENT);
}
template <int SLP>
__device__ __forceinline__ void hs_lead(__amdgpu_buffer_rsrc_t rs, bool fill, int cnt, unsigned want) {
  const int l16 = olane() * 16;
  if (fill) {
    for (int o = 0; o < (int)ARM_HOST; o += 1024) __builtin_amdgcn_raw_buffer_store_b128(v4u{~0u, ~0u, ~0u, ~0u}, rs, l16, o, AUX_SC1);
    VMCNT(0);
  }
  _Pragma("clang loop unroll(disable)") for (unsigned spins = 0; spins < (kSpinCap << 2); ++spins) {
    int va = l16;
    asm volatile("" : "+v"(va));
    const v4u a = __builtin_amdgcn_raw_buffer_load_b128(rs, va, OFF_REQ, AUX_SC1);
    const v4u b = __builtin_amdgcn_raw_buffer_load_b128(rs, va + 1024, OFF_REQ, AUX_SC1);
    const unsigned n = __builtin_amdgcn_raw_buffer_load_b32(rs, va & 0, cnt, AUX_SC1);
    if (__all(n + 1u >= want)) break;
    __builtin_amdgcn_raw_buffer_store_b128(a, rs, va, OFF_ACK, AUX_SC1);
    __builtin_amdgcn_raw_buffer_store_b128(b, rs, va + 1024, OFF_ACK, AUX_SC1);
    __builtin_amdgcn_s_sleep(SLP);
  }
}

#define TN(k, d) (d)
#define EVDECL
#define EV(k)
#define EVDUMP(s)
#define EVCLK(s, c)
#define EVOUT
#define PX 2
#define NOB 3
#define VO_A \
  "s_branch .Lgo_%=\n" \
  AL(A_VO_A) \
  ".Lr_%=:\n" \
  "s_sub_u32 %[rt], %[rt], 1\n" \
  "s_cbranch_scc1 .Lrb_%=\n" \
  "s_sleep 2\n" \
  "buffer_load_dwordx4 v[192:195], v248, %[rs], %[o0] offen sc1\n" \
  "buffer_load_dwordx4 v[196:199], v248, %[rs], %[o0] offen offset:1024 sc1\n" \
  "v_mov_b64 v[224:225], 0\n" \
  "v_mov_b64 v[226:227], 0\n" \
  "v_mov_b64 v[228:229], 0\n" \
  "v_mov_b64 v[230:231], 0\n" \
  "s_nop 1\n" \
  "s_branch .Lw_%=\n" \
  ".Lgo_%=:\n" \
  "v_add_u32 v252, 0x20000, v248\n" \
  "ds_read_b128 v[200:203], v248 offset:32768\n" \
  "ds_read_b128 v[204:207], v248 offset:33792\n" \
  "ds_read_b128 v[208:211], v248 offset:49152\n" \
  "ds_read_b128 v[212:215], v248 offset:50176\n" \
  "ds_read_b128 v[216:219], v252 offset:0\n" \
  "ds_read_b128 v[220:223], v252 offset:1024\n" \
  "ds_read_b128 v[240:243], v252 offset:16384\n" \
  "ds_read_b128 v[244:247], v252 offset:17408\n" \
  "v_mov_b64 v[224:225], 0\n" \
  "v_mov_b64 v[226:227], 0\n" \
  "v_mov_b64 v[228:229], 0\n" \
  "v_mov_b64 v[230:231], 0\n" \
  "v_xor_b32 v250, 0x800, v248\n" \
  "v_xor_b32 v251, 0x1000, v248\n" \
  "s_mov_b32 %[rt], 0x40000\n" \
  "s_setprio 2\n" \
  "s_waitcnt lgkmcnt(0)\n" \
  "s_barrier\n" \
  "buffer_load_dwordx4 v[192:195], v248, %[rs], %[o0] offen sc1\n" \
  "buffer_load_dwordx4 v[196:199], v248, %[rs], %[o0] offen offset:1024 sc1\n" \
  ".Lw_%=:\n" \
  "s_waitcnt vmcnt(1)\n" \
  "ds_write_b128 v248, v[192:195]\n" \
  "v_smfmac_f32_16x16x64_bf16 v[224:227], v[192:195], v[200:207], v249\n" \
  "v_max3_u32 v252, v192, v193, v194\n" \
  "s_waitcnt vmcnt(0)\n" \
  "ds_write_b128 v248, v[196:199] offset:1024\n" \
  "v_smfmac_f32_16x16x64_bf16 v[228:231], v[192:195], v[208:215], v249\n" \
  "v_max3_u32 v252, v252, v195, v196\n" \
  "v_smfmac_f32_16x16x64_bf16 v[224:227], v[196:199], v[216:223], v249\n" \
  "v_max3_u32 v252, v252, v197, v198\n" \
  "v_max_u32 v252, v252, v199\n" \
  "v_smfmac_f32_16x16x64_bf16 v[228:231], v[196:199], v[240:247], v249\n" \
  "v_cmp_eq_u32 vcc, -1, v252\n" \
  "v_xor_b32 v252, 0x1800, v248\n" \
  "s_cbranch_vccnz .Lr_%=\n" \
  ".Lrb_%=:\n" \
  "s_waitcnt lgkmcnt(0)\n" \
  "s_barrier\n" \
  "ds_read_b128 v[200:203], v250 offset:0\n" \
  "ds_read_b128 v[204:207], v250 offset:1024\n" \
  "ds_read_b128 v[208:211], v251 offset:0\n" \
  "ds_read_b128 v[212:215], v251 offset:1024\n" \
  "ds_read_b128 v[216:219], v252 offset:0\n" \
  "ds_read_b128 v[220:223], v252 offset:1024\n" \
  "s_waitcnt lgkmcnt(5)\n" \
  "v_smfmac_f32_16x16x64_bf16 v[224:227], v[200:203], v[0:7], v249\n" \
  "v_smfmac_f32_16x16x64_bf16 v[228:231], v[200:203], v[48:55], v249\n" \
  "s_waitcnt lgkmcnt(4)\n" \
  "v_smfmac_f32_16x16x64_bf16 v[224:227], v[204:207], v[8:15], v249\n" \
  "v_smfmac_f32_16x16x64_bf16 v[228:231], v[204:207], v[56:63], v249\n" \
  "s_waitcnt lgkmcnt(3)\n" \
  "v_smfmac_f32_16x16x64_bf16 v[224:227], v[208:211], v[16:23], v249\n" \
  "v_smfmac_f32_16x16x64_bf16 v[228:231], v[208:211], v[64:71], v249\n" \
  "s_waitcnt lgkmcnt(2)\n" \
  "v_smfmac_f32_16x16x64_bf16 v[224:227], v[212:215], v[24:31], v249\n" \
  "s_waitcnt lgkmcnt(1)\n" \
  "v_smfmac_f32_16x16x64_bf16 v[224:227], v[216:219], v[32:39], v249\n" \
  "s_waitcnt lgkmcnt(0)\n" \
  "v_smfmac_f32_16x16x64_bf16 v[224:227], v[220:223], v[40:47], v249\n" \
  "v_smfmac_f32_16x16x64_bf16 v[228:231], v[212:215], v[72:79], v249\n" \
  "v_smfmac_f32_16x16x64_bf16 v[228:231], v[216:219], v[80:87], v249\n" \
  "s_nop 0\n" \
  "v_add_f32 v224, v224, v226\n" \
  "v_smfmac_f32_16x16x64_bf16 v[228:231], v[220:223], v[88:95], v249\n" \
  "v_add_f32 v225, v225, v227\n" \
  "ds_write_b64 v248, v[224:225] offset:16384\n" \
  "s_nop 6\n" \
  "v_add_f32 v228, v228, v230\n" \
  "v_add_f32 v229, v229, v231\n" \
  "ds_write_b64 v248, v[228:229] offset:16392\n" \
  "s_setprio 0\n" \
  "ds_read_b128 v[240:243], v254 offset:0\n" \
  "ds_read_b128 v[244:247], v254 offset:1024\n" \
  "s_waitcnt lgkmcnt(2)\n" \
  "s_barrier\n" \
  "s_nop 1\n" \
  "ds_read_b128 v[224:227], v254 offset:16384\n" \
  "ds_read_b128 v[228:231], v254 offset:17408\n" \
  "v_mov_b64 v[192:193], 0\n" \
  "v_mov_b64 v[194:195], 0\n" \
  "v_mov_b64 v[196:197], 0\n" \
  "v_mov_b64 v[198:199], 0\n" \
  "v_mov_b64 v[200:201], 0\n" \
  "v_mov_b64 v[202:203], 0\n" \
  "v_mov_b64 v[204:205], 0\n" \
  "v_mov_b64 v[206:207], 0\n" \
  "s_setprio 2\n" \
  "s_barrier\n" \
  "ds_read_b64_tr_b16 v[208:209], v238 offset:16384\n" \
  "ds_read_b64_tr_b16 v[210:211], v238 offset:16576\n" \
  "ds_read_b64_tr_b16 v[212:213], v238 offset:17152\n" \
  "ds_read_b64_tr_b16 v[214:215], v238 offset:17344\n" \
  "ds_read_b64_tr_b16 v[216:217], v238 offset:17920\n" \
  "ds_read_b64_tr_b16 v[218:219], v238 offset:18112\n" \
  "ds_read_b64_tr_b16 v[220:221], v238 offset:18688\n" \
  "ds_read_b64_tr_b16 v[222:223], v238 offset:18880\n" \
  "s_waitcnt lgkmcnt(6)\n" \
  "v_smfmac_f32_16x16x64_bf16 v[200:203], v[208:211], v[240:247], v249 abid:1\n" \
  "ds_read_b128 v[240:243], v254 offset:32768\n" \
  "v_smfmac_f32_16x16x64_bf16 v[192:195], v[208:211], v[160:167], v249 abid:1\n" \
  "ds_read_b128 v[244:247], v254 offset:33792\n" \
  "v_smfmac_f32_16x16x64_bf16 v[196:199], v[208:211], v[96:103], v249 abid:1\n" \
  "v_smfmac_f32_16x16x64_bf16 v[204:207], v[208:211], v[128:135], v249 abid:1\n" \
  "s_waitcnt lgkmcnt(6)\n" \
  "v_smfmac_f32_16x16x64_bf16 v[192:195], v[212:215], v[168:175], v249 abid:1\n" \
  "v_smfmac_f32_16x16x64_bf16 v[200:203], v[212:215], v[224:231], v249 abid:1\n" \
  "ds_read_b128 v[224:227], v254 offset:49152\n" \
  "v_smfmac_f32_16x16x64_bf16 v[196:199], v[212:215], v[104:111], v249 abid:1\n" \
  "ds_read_b128 v[228:231], v254 offset:50176\n" \
  "v_smfmac_f32_16x16x64_bf16 v[204:207], v[212:215], v[136:143], v249 abid:1\n" \
  "s_waitcnt lgkmcnt(6)\n" \
  "v_smfmac_f32_16x16x64_bf16 v[192:195], v[216:219], v[176:183], v249 abid:1\n" \
  "v_smfmac_f32_16x16x64_bf16 v[196:199], v[216:219], v[112:119], v249 abid:1\n" \
  "s_waitcnt lgkmcnt(4)\n" \
  "v_smfmac_f32_16x16x64_bf16 v[192:195], v[220:223], v[184:191], v249 abid:1\n" \
  "v_smfmac_f32_16x16x64_bf16 v[196:199], v[220:223], v[120:127], v249 abid:1\n" \
  "v_smfmac_f32_16x16x64_bf16 v[204:207], v[216:219], v[144:151], v249 abid:1\n" \
  "s_nop 0\n" \
  "v_cvt_pk_bf16_f32 v192, v192, v193\n" \
  "v_smfmac_f32_16x16x64_bf16 v[204:207], v[220:223], v[152:159], v249 abid:1\n" \
  "v_cvt_pk_bf16_f32 v193, v194, v195\n" \
  "s_waitcnt lgkmcnt(2)\n" \
  "v_smfmac_f32_16x16x64_bf16 v[200:203], v[216:219], v[240:247], v249 abid:1\n" \
  "v_cvt_pk_bf16_f32 v194, v196, v197\n" \
  "s_waitcnt lgkmcnt(0)\n" \
  "v_smfmac_f32_16x16x64_bf16 v[200:203], v[220:223], v[224:231], v249 abid:1\n" \
  "v_cvt_pk_bf16_f32 v195, v198, v199\n" \
  "v_cvt_pk_bf16_f32 v204, v204, v205\n" \
  "v_permlane16_swap_b32 v192, v194\n" \
  "v_permlane16_swap_b32 v193, v195\n" \
  "buffer_store_dwordx4 v[192:195], v236, %[rs], %[vs] offen sc1\n" \
  "v_cvt_pk_bf16_f32 v205, v206, v207\n" \
  "s_nop 2\n" \
  "v_cvt_pk_bf16_f32 v203, v202, v203\n" \
  "v_cvt_pk_bf16_f32 v202, v200, v201\n" \
  "s_nop 0\n" \
  "v_permlane16_swap_b32 v203, v205\n" \
  "v_permlane16_swap_b32 v202, v204\n" \
  "buffer_store_dwordx4 v[202:205], v237, %[rs], %[vs] offen sc1\n" \
  "s_setprio 0\n" \
  "s_nop 1\n"
#define VO_B \
  "s_branch .Lgo_%=\n" \
  AL(A_VO_B) \
  ".Lr_%=:\n" \
  "s_sub_u32 %[rt], %[rt], 1\n" \
  "s_cbranch_scc1 .Lrb_%=\n" \
  "s_sleep 2\n" \
  "buffer_load_dwordx4 v[192:195], v248, %[rs], %[o0] offen sc1\n" \
  "buffer_load_dwordx4 v[196:199], v248, %[rs], %[o0] offen offset:1024 sc1\n" \
  "v_mov_b64 v[224:225], 0\n" \
  "v_mov_b64 v[226:227], 0\n" \
  "v_mov_b64 v[228:229], 0\n" \
  "v_mov_b64 v[230:231], 0\n" \
  "s_nop 1\n" \
  "s_branch .Lw_%=\n" \
  ".Lgo_%=:\n" \
  "v_add_u32 v252, 0x20000, v248\n" \
  "ds_read_b128 v[200:203], v248 offset:32768\n" \
  "ds_read_b128 v[204:207], v248 offset:33792\n" \
  "ds_read_b128 v[208:211], v248 offset:49152\n" \
  "ds_read_b128 v[212:215], v248 offset:50176\n" \
  "ds_read_b128 v[216:219], v252 offset:0\n" \
  "ds_read_b128 v[220:223], v252 offset:1024\n" \
  "ds_read_b128 v[240:243], v252 offset:16384\n" \
  "ds_read_b128 v[244:247], v252 offset:17408\n" \
  "v_mov_b64 v[224:225], 0\n" \
  "v_mov_b64 v[226:227], 0\n" \
  "v_mov_b64 v[228:229], 0\n" \
  "v_mov_b64 v[230:231], 0\n" \
  "v_xor_b32 v250, 0x800, v248\n" \
  "v_xor_b32 v251, 0x1000, v248\n" \
  "s_mov_b32 %[rt], 0x40000\n" \
  "s_setprio 1\n" \
  "s_waitcnt lgkmcnt(0)\n" \
  "s_cmp_eq_u32 %[tw], 0\n" \
  "s_cbranch_scc1 .Ltq_%=\n" \
  ".Ltl_%=:\n" \
  "s_memtime vcc\n" \
  "s_waitcnt lgkmcnt(0)\n" \
  "s_sub_i32 %[cnt], %[tg], vcc_lo\n" \
  "s_cmp_lt_i32 %[cnt], 260\n" \
  "s_cbranch_scc1 .Ltf_%=\n" \
  "s_sleep 1\n" \
  "s_branch .Ltl_%=\n" \
  ".Ltf_%=:\n" \
  "s_max_i32 %[cnt], %[cnt], 0\n" \
  ".Ltg_%=:\n" \
  "s_sub_u32 %[cnt], %[cnt], 28\n" \
  "s_cbranch_scc0 .Ltg_%=\n" \
  ".Ltq_%=:\n" \
  "s_barrier\n" \
  "buffer_load_dwordx4 v[192:195], v248, %[rs], %[o0] offen sc1\n" \
  "buffer_load_dwordx4 v[196:199], v248, %[rs], %[o0] offen offset:1024 sc1\n" \
  ".Lw_%=:\n" \
  "s_waitcnt vmcnt(1)\n" \
  "ds_write_b128 v248, v[192:195]\n" \
  "v_smfmac_f32_16x16x64_bf16 v[224:227], v[192:195], v[200:207], v249\n" \
  "v_max3_u32 v252, v192, v193, v194\n" \
  "s_waitcnt vmcnt(0)\n" \
  "ds_write_b128 v248, v[196:199] offset:1024\n" \
  "v_smfmac_f32_16x16x64_bf16 v[228:231], v[192:195], v[208:215], v249\n" \
  "v_max3_u32 v252, v252, v195, v196\n" \
  "v_smfmac_f32_16x16x64_bf16 v[224:227], v[196:199], v[216:223], v249\n" \
  "v_max3_u32 v252, v252, v197, v198\n" \
  "v_max_u32 v252, v252, v199\n" \
  "v_smfmac_f32_16x16x64_bf16 v[228:231], v[196:199], v[240:247], v249\n" \
  "v_cmp_eq_u32 vcc, -1, v252\n" \
  "v_xor_b32 v252, 0x1800, v248\n" \
  "s_cbranch_vccnz .Lr_%=\n" \
  ".Lrb_%=:\n" \
  "s_waitcnt lgkmcnt(0)\n" \
  "s_barrier\n" \
  "s_memtime vcc\n" \
  "s_setprio 3\n" \
  "v_mov_b64 v[192:193], 0\n" \
  "v_mov_b64 v[194:195], 0\n" \
  "v_mov_b64 v[196:197], 0\n" \
  "v_mov_b64 v[198:199], 0\n" \
  "s_waitcnt lgkmcnt(0)\n" \
  "s_sub_u32 %[cnt], vcc_lo, %[ckm]\n" \
  "s_mul_hi_u32 %[cnt], %[cnt], %[inv]\n" \
  "s_add_u32 %[cnt], %[cnt], %[r16]\n" \
  "s_and_b32 %[cnt], %[cnt], -2\n" \
  "v_mov_b32 v233, %[cnt]\n" \
  "buffer_store_dword v233, v239, %[rs], %[sst] offen sc1\n" \
  "s_nop 5\n" \
  "ds_read_b128 v[200:203], v250 offset:0\n" \
  "ds_read_b128 v[204:207], v250 offset:1024\n" \
  "ds_read_b128 v[208:211], v251 offset:0\n" \
  "ds_read_b128 v[212:215], v251 offset:1024\n" \
  "ds_read_b128 v[216:219], v252 offset:0\n" \
  "ds_read_b128 v[220:223], v252 offset:1024\n" \
  "s_setprio 1\n" \
  "s_waitcnt lgkmcnt(5)\n" \
  "v_smfmac_f32_16x16x64_bf16 v[224:227], v[200:203], v[0:7], v249\n" \
  "v_smfmac_f32_16x16x64_bf16 v[228:231], v[200:203], v[48:55], v249\n" \
  "s_waitcnt lgkmcnt(4)\n" \
  "v_smfmac_f32_16x16x64_bf16 v[224:227], v[204:207], v[8:15], v249\n" \
  "v_smfmac_f32_16x16x64_bf16 v[228:231], v[204:207], v[56:63], v249\n" \
  "s_waitcnt lgkmcnt(3)\n" \
  "v_smfmac_f32_16x16x64_bf16 v[224:227], v[208:211], v[16:23], v249\n" \
  "ds_read_b128 v[240:243], v254 offset:0\n" \
  "v_smfmac_f32_16x16x64_bf16 v[228:231], v[208:211], v[64:71], v249\n" \
  "ds_read_b128 v[244:247], v254 offset:1024\n" \
  "s_waitcnt lgkmcnt(4)\n" \
  "v_smfmac_f32_16x16x64_bf16 v[224:227], v[212:215], v[24:31], v249\n" \
  "s_waitcnt lgkmcnt(3)\n" \
  "v_smfmac_f32_16x16x64_bf16 v[224:227], v[216:219], v[32:39], v249\n" \
  "s_waitcnt lgkmcnt(2)\n" \
  "v_smfmac_f32_16x16x64_bf16 v[224:227], v[220:223], v[40:47], v249\n" \
  "v_smfmac_f32_16x16x64_bf16 v[228:231], v[212:215], v[72:79], v249\n" \
  "s_barrier\n" \
  "ds_read_b128 v[232:235], v248 offset:24576\n" \
  "v_smfmac_f32_16x16x64_bf16 v[228:231], v[216:219], v[80:87], v249\n" \
  "s_nop 0\n" \
  "v_add_f32 v224, v224, v226\n" \
  "v_smfmac_f32_16x16x64_bf16 v[228:231], v[220:223], v[88:95], v249\n" \
  "v_add_f32 v225, v225, v227\n" \
  "s_waitcnt lgkmcnt(0)\n" \
  "v_add_f32 v232, v232, v224\n" \
  "v_add_f32 v233, v233, v225\n" \
  "v_cvt_pk_bf16_f32 v232, v232, v233\n" \
  "ds_write_b32 v253, v232\n" \
  "s_nop 2\n" \
  "v_add_f32 v234, v234, v228\n" \
  "v_add_f32 v235, v235, v229\n" \
  "v_add_f32 v234, v234, v230\n" \
  "v_add_f32 v235, v235, v231\n" \
  "v_cvt_pk_bf16_f32 v234, v234, v235\n" \
  "ds_write_b32 v253, v234 offset:1536\n" \
  "v_mov_b64 v[200:201], 0\n" \
  "v_mov_b64 v[202:203], 0\n" \
  "v_mov_b64 v[204:205], 0\n" \
  "v_mov_b64 v[206:207], 0\n" \
  "s_waitcnt lgkmcnt(0)\n" \
  "s_barrier\n" \
  "s_nop 7\n" \
  "ds_read_b64_tr_b16 v[208:209], v238 offset:16384\n" \
  "ds_read_b64_tr_b16 v[210:211], v238 offset:16576\n" \
  "ds_read_b128 v[224:227], v254 offset:16384\n" \
  "ds_read_b128 v[228:231], v254 offset:17408\n" \
  "ds_read_b64_tr_b16 v[212:213], v238 offset:17152\n" \
  "ds_read_b64_tr_b16 v[214:215], v238 offset:17344\n" \
  "ds_read_b64_tr_b16 v[216:217], v238 offset:17920\n" \
  "ds_read_b64_tr_b16 v[218:219], v238 offset:18112\n" \
  "ds_read_b64_tr_b16 v[220:221], v238 offset:18688\n" \
  "ds_read_b64_tr_b16 v[222:223], v238 offset:18880\n" \
  "s_waitcnt lgkmcnt(8)\n" \
  "v_smfmac_f32_16x16x64_bf16 v[200:203], v[208:211], v[240:247], v249 abid:1\n" \
  "ds_read_b128 v[240:243], v254 offset:32768\n" \
  "v_smfmac_f32_16x16x64_bf16 v[192:195], v[208:211], v[160:167], v249 abid:1\n" \
  "ds_read_b128 v[244:247], v254 offset:33792\n" \
  "v_smfmac_f32_16x16x64_bf16 v[196:199], v[208:211], v[96:103], v249 abid:1\n" \
  "v_smfmac_f32_16x16x64_bf16 v[204:207], v[208:211], v[128:135], v249 abid:1\n" \
  "s_waitcnt lgkmcnt(6)\n" \
  "v_smfmac_f32_16x16x64_bf16 v[192:195], v[212:215], v[168:175], v249 abid:1\n" \
  "v_smfmac_f32_16x16x64_bf16 v[200:203], v[212:215], v[224:231], v249 abid:1\n" \
  "ds_read_b128 v[224:227], v254 offset:49152\n" \
  "v_smfmac_f32_16x16x64_bf16 v[196:199], v[212:215], v[104:111], v249 abid:1\n" \
  "ds_read_b128 v[228:231], v254 offset:50176\n" \
  "v_smfmac_f32_16x16x64_bf16 v[204:207], v[212:215], v[136:143], v249 abid:1\n" \
  "s_waitcnt lgkmcnt(6)\n" \
  "v_smfmac_f32_16x16x64_bf16 v[192:195], v[216:219], v[176:183], v249 abid:1\n" \
  "v_smfmac_f32_16x16x64_bf16 v[196:199], v[216:219], v[112:119], v249 abid:1\n" \
  "s_waitcnt lgkmcnt(4)\n" \
  "v_smfmac_f32_16x16x64_bf16 v[192:195], v[220:223], v[184:191], v249 abid:1\n" \
  "v_smfmac_f32_16x16x64_bf16 v[196:199], v[220:223], v[120:127], v249 abid:1\n" \
  "v_smfmac_f32_16x16x64_bf16 v[204:207], v[216:219], v[144:151], v249 abid:1\n" \
  "s_nop 0\n" \
  "v_cvt_pk_bf16_f32 v192, v192, v193\n" \
  "v_smfmac_f32_16x16x64_bf16 v[204:207], v[220:223], v[152:159], v249 abid:1\n" \
  "v_cvt_pk_bf16_f32 v193, v194, v195\n" \
  "s_waitcnt lgkmcnt(2)\n" \
  "v_smfmac_f32_16x16x64_bf16 v[200:203], v[216:219], v[240:247], v249 abid:1\n" \
  "v_cvt_pk_bf16_f32 v194, v196, v197\n" \
  "s_waitcnt lgkmcnt(0)\n" \
  "v_smfmac_f32_16x16x64_bf16 v[200:203], v[220:223], v[224:231], v249 abid:1\n" \
  "v_cvt_pk_bf16_f32 v195, v198, v199\n" \
  "v_cvt_pk_bf16_f32 v204, v204, v205\n" \
  "v_permlane16_swap_b32 v192, v194\n" \
  "v_permlane16_swap_b32 v193, v195\n" \
  "buffer_store_dwordx4 v[192:195], v236, %[rs], %[vs] offen sc1\n" \
  "v_cvt_pk_bf16_f32 v205, v206, v207\n" \
  "s_nop 2\n" \
  "v_cvt_pk_bf16_f32 v203, v202, v203\n" \
  "v_cvt_pk_bf16_f32 v202, v200, v201\n" \
  "s_nop 0\n" \
  "v_permlane16_swap_b32 v203, v205\n" \
  "v_permlane16_swap_b32 v202, v204\n" \
  "buffer_store_dwordx4 v[202:205], v237, %[rs], %[vs] offen sc1\n" \
  "s_setprio 0\n" \
  "s_nop 1\n"
#define QK_A0 \
  "s_branch .Lgo_%=\n" \
  AL(A_QK_A0) \
  ".Lr_%=:\n" \
  "s_sub_u32 %[rt], %[rt], 1\n" \
  "s_cbranch_scc1 .Lrb_%=\n" \
  "s_sleep 2\n" \
  "buffer_load_dwordx4 v[192:195], v248, %[rs], %[o0] offen sc1\n" \
  "buffer_load_dwordx4 v[196:199], v248, %[rs], %[o0] offen offset:1024 sc1\n" \
  "v_mov_b64 v[224:225], 0\n" \
  "v_mov_b64 v[226:227], 0\n" \
  "v_mov_b64 v[228:229], 0\n" \
  "v_mov_b64 v[230:231], 0\n" \
  "v_mov_b64 v[232:233], 0\n" \
  "v_mov_b64 v[234:235], 0\n" \
  "v_mov_b64 v[236:237], 0\n" \
  "v_mov_b64 v[238:239], 0\n" \
  "s_nop 1\n" \
  "s_branch .Lw_%=\n" \
  ".Lw1_%=:\n" \
  "v_max_f32 v200, v192, v193\n" \
  "s_nop 1\n" \
  "v_max_f32_dpp v200, v200, v200 row_ror:8 row_mask:0xf bank_mask:0xf\n" \
  "s_nop 1\n" \
  "v_max_f32_dpp v200, v200, v200 row_ror:4 row_mask:0xf bank_mask:0xf\n" \
  "s_nop 1\n" \
  "v_max_f32_dpp v200, v200, v200 row_ror:2 row_mask:0xf bank_mask:0xf\n" \
  "s_nop 1\n" \
  "v_max_f32_dpp v200, v200, v200 row_ror:1 row_mask:0xf bank_mask:0xf\n" \
  "v_sub_f32 v194, v192, v200\n" \
  "v_sub_f32 v195, v193, v200\n" \
  "v_exp_f32 v194, v194\n" \
  "v_exp_f32 v195, v195\n" \
  "s_mov_b32 %[rt], -1.0\n" \
  "s_nop 0\n" \
  "v_cvt_pk_bf16_f32 v196, v194, v195\n" \
  "v_and_b32 v196, 0x7fffffff, v196\n" \
  "s_branch .Lw1b_%=\n" \
  ".Lw2_%=:\n" \
  "s_cmp_lt_i32 %[rt], 0\n" \
  "s_cbranch_scc1 .Lw3_%=\n" \
  "v_mov_b32 v200, 0\n" \
  ".Lw3_%=:\n" \
  "s_nop 1\n" \
  "v_max_f32_dpp v197, v192, v200 quad_perm:[0,0,0,0] row_mask:0xf bank_mask:0xf\n" \
  "s_nop 1\n" \
  "v_max_f32_dpp v197, v192, v197 quad_perm:[1,1,1,1] row_mask:0xf bank_mask:0xf\n" \
  "s_nop 1\n" \
  "v_max_f32_dpp v197, v197, v197 row_ror:4 row_mask:0xf bank_mask:0xf\n" \
  "s_nop 1\n" \
  "v_max_f32_dpp v197, v197, v197 row_ror:8 row_mask:0xf bank_mask:0xf\n" \
  "v_sub_f32 v194, v192, v197\n" \
  "v_sub_f32 v201, v200, v197\n" \
  "v_exp_f32 v194, v194\n" \
  "v_exp_f32 v201, v201\n" \
  "s_nop 1\n" \
  "v_mul_f32 v199, v201, v199\n" \
  "s_nop 1\n" \
  "v_add_f32_dpp v198, v194, v199 quad_perm:[0,0,0,0] row_mask:0xf bank_mask:0xf\n" \
  "s_nop 1\n" \
  "v_mov_b32_dpp v193, v194 quad_perm:[1,1,1,1] row_mask:0xf bank_mask:0xf\n" \
  "s_nop 1\n" \
  "v_add_f32 v198, v198, v193\n" \
  "s_nop 1\n" \
  "v_mov_b32_dpp v195, v194 quad_perm:[0,0,0,0] row_mask:0xf bank_mask:0xf\n" \
  "s_nop 1\n" \
  "v_add_f32_dpp v198, v198, v198 row_ror:4 row_mask:0xf bank_mask:0xf\n" \
  "s_nop 1\n" \
  "v_add_f32_dpp v198, v198, v198 row_ror:8 row_mask:0xf bank_mask:0xf\n" \
  "s_nop 1\n" \
  "v_rcp_f32 v198, v198\n" \
  "s_nop 1\n" \
  "v_mul_f32 v195, v195, v198\n" \
  "s_nop 1\n" \
  "v_mul_f32 v193, v193, v198\n" \
  "s_nop 1\n" \
  "v_cvt_pk_bf16_f32 v196, v195, v193\n" \
  "s_nop 1\n" \
  "v_and_b32 v196, 0x7fffffff, v196\n" \
  "s_nop 1\n" \
  "v_mul_f32 v198, v201, v198\n" \
  "s_nop 1\n" \
  "buffer_store_dword v196, v251, %[rs], %[so] offen sc1\n" \
  "s_nop 1\n" \
  "v_cvt_pk_bf16_f32 v197, v198, v198\n" \
  "s_nop 1\n" \
  "v_and_b32 v197, v252, v197\n" \
  "s_nop 1\n" \
  "buffer_store_dword v197, v251, %[rs], %[so] offen offset:64 sc1\n" \
  "s_branch .Lw2b_%=\n" \
  ".Lgo_%=:\n" \
  "v_add_u32 v252, 0x20000, v248\n" \
  "ds_read_b128 v[200:203], v248 offset:32768\n" \
  "ds_read_b128 v[204:207], v248 offset:33792\n" \
  "ds_read_b128 v[208:211], v248 offset:49152\n" \
  "ds_read_b128 v[212:215], v248 offset:50176\n" \
  "ds_read_b128 v[216:219], v252 offset:0\n" \
  "ds_read_b128 v[220:223], v252 offset:1024\n" \
  "ds_read_b128 v[240:243], v252 offset:16384\n" \
  "ds_read_b128 v[244:247], v252 offset:17408\n" \
  "v_mov_b64 v[224:225], 0\n" \
  "v_mov_b64 v[226:227], 0\n" \
  "v_mov_b64 v[228:229], 0\n" \
  "v_mov_b64 v[230:231], 0\n" \
  "v_mov_b64 v[232:233], 0\n" \
  "v_mov_b64 v[234:235], 0\n" \
  "v_mov_b64 v[236:237], 0\n" \
  "v_mov_b64 v[238:239], 0\n" \
  "v_xor_b32 v250, 0x800, v248\n" \
  "v_xor_b32 v251, 0x1000, v248\n" \
  "s_mov_b32 %[rt], 0x40000\n" \
  "s_setprio 2\n" \
  "s_waitcnt lgkmcnt(0)\n" \
  "s_barrier\n" \
  "buffer_load_dwordx4 v[192:195], v248, %[rs], %[o0] offen sc1\n" \
  "buffer_load_dwordx4 v[196:199], v248, %[rs], %[o0] offen offset:1024 sc1\n" \
  ".Lw_%=:\n" \
  "s_waitcnt vmcnt(1)\n" \
  "ds_write_b128 v248, v[192:195]\n" \
  "v_smfmac_f32_16x16x64_bf16 v[224:227], v[192:195], v[200:207], v249\n" \
  "v_max3_u32 v252, v192, v193, v194\n" \
  "s_waitcnt vmcnt(0)\n" \
  "ds_write_b128 v248, v[196:199] offset:1024\n" \
  "v_smfmac_f32_16x16x64_bf16 v[228:231], v[192:195], v[208:215], v249\n" \
  "v_max3_u32 v252, v252, v195, v196\n" \
  "v_smfmac_f32_16x16x64_bf16 v[224:227], v[196:199], v[216:223], v249\n" \
  "v_max3_u32 v252, v252, v197, v198\n" \
  "v_smfmac_f32_16x16x64_bf16 v[228:231], v[196:199], v[240:247], v249\n" \
  "v_max_u32 v252, v252, v199\n" \
  "v_smfmac_f32_16x16x64_bf16 v[232:235], v[192:195], v[96:103], v249\n" \
  "v_cmp_eq_u32 vcc, -1, v252\n" \
  "v_smfmac_f32_16x16x64_bf16 v[236:239], v[192:195], v[144:151], v249\n" \
  "v_xor_b32 v252, 0x1800, v248\n" \
  "s_cbranch_vccnz .Lr_%=\n" \
  ".Lrb_%=:\n" \
  "v_smfmac_f32_16x16x64_bf16 v[232:235], v[196:199], v[104:111], v249\n" \
  "v_smfmac_f32_16x16x64_bf16 v[236:239], v[196:199], v[152:159], v249\n" \
  "s_waitcnt lgkmcnt(0)\n" \
  "s_barrier\n" \
  "ds_read_b128 v[200:203], v250 offset:0\n" \
  "ds_read_b128 v[204:207], v250 offset:1024\n" \
  "ds_read_b128 v[208:211], v251 offset:0\n" \
  "ds_read_b128 v[212:215], v251 offset:1024\n" \
  "ds_read_b128 v[216:219], v252 offset:0\n" \
  "ds_read_b128 v[220:223], v252 offset:1024\n" \
  "s_waitcnt lgkmcnt(5)\n" \
  "v_smfmac_f32_16x16x64_bf16 v[224:227], v[200:203], v[0:7], v249\n" \
  "v_smfmac_f32_16x16x64_bf16 v[228:231], v[200:203], v[48:55], v249\n" \
  "s_waitcnt lgkmcnt(4)\n" \
  "v_smfmac_f32_16x16x64_bf16 v[224:227], v[204:207], v[8:15], v249\n" \
  "v_smfmac_f32_16x16x64_bf16 v[228:231], v[204:207], v[56:63], v249\n" \
  "s_waitcnt lgkmcnt(3)\n" \
  "v_smfmac_f32_16x16x64_bf16 v[224:227], v[208:211], v[16:23], v249\n" \
  "v_smfmac_f32_16x16x64_bf16 v[228:231], v[208:211], v[64:71], v249\n" \
  "s_waitcnt lgkmcnt(2)\n" \
  "v_smfmac_f32_16x16x64_bf16 v[224:227], v[212:215], v[24:31], v249\n" \
  "s_waitcnt lgkmcnt(1)\n" \
  "v_smfmac_f32_16x16x64_bf16 v[224:227], v[216:219], v[32:39], v249\n" \
  "s_waitcnt lgkmcnt(0)\n" \
  "v_smfmac_f32_16x16x64_bf16 v[224:227], v[220:223], v[40:47], v249\n" \
  "v_smfmac_f32_16x16x64_bf16 v[228:231], v[212:215], v[72:79], v249\n" \
  "v_smfmac_f32_16x16x64_bf16 v[228:231], v[216:219], v[80:87], v249\n" \
  "s_nop 0\n" \
  "v_add_f32 v224, v224, v226\n" \
  "v_smfmac_f32_16x16x64_bf16 v[228:231], v[220:223], v[88:95], v249\n" \
  "v_add_f32 v225, v225, v227\n" \
  "ds_write_b64 v248, v[224:225] offset:8192\n" \
  "s_nop 6\n" \
  "v_add_f32 v228, v228, v230\n" \
  "v_add_f32 v229, v229, v231\n" \
  "ds_write_b64 v248, v[228:229] offset:8200\n" \
  "s_setprio 0\n" \
  "ds_read_b128 v[240:243], v254 offset:0\n" \
  "ds_read_b128 v[244:247], v254 offset:1024\n" \
  "s_waitcnt lgkmcnt(2)\n" \
  "s_barrier\n" \
  "s_nop 1\n" \
  "ds_read_b128 v[192:195], v254 offset:16384\n" \
  "ds_read_b128 v[196:199], v254 offset:17408\n" \
  "ds_read_b128 v[224:227], v254 offset:32768\n" \
  "ds_read_b128 v[228:231], v254 offset:33792\n" \
  "s_waitcnt lgkmcnt(4)\n" \
  "v_smfmac_f32_16x16x64_bf16 v[232:235], v[200:203], v[240:247], v249\n" \
  "ds_read_b128 v[240:243], v254 offset:49152\n" \
  "ds_read_b128 v[244:247], v254 offset:50176\n" \
  "s_waitcnt lgkmcnt(4)\n" \
  "v_smfmac_f32_16x16x64_bf16 v[236:239], v[200:203], v[192:199], v249\n" \
  "v_smfmac_f32_16x16x64_bf16 v[232:235], v[204:207], v[112:119], v249\n" \
  "ds_read_b128 v[192:195], v248 offset:16384\n" \
  "v_smfmac_f32_16x16x64_bf16 v[236:239], v[204:207], v[160:167], v249\n" \
  "ds_read_b128 v[196:199], v248 offset:17408\n" \
  "v_smfmac_f32_16x16x64_bf16 v[232:235], v[212:215], v[120:127], v249\n" \
  "ds_read_b128 v[200:203], v248 offset:20480\n" \
  "v_smfmac_f32_16x16x64_bf16 v[236:239], v[212:215], v[168:175], v249\n" \
  "ds_read_b128 v[204:207], v248 offset:21504\n" \
  "s_waitcnt lgkmcnt(6)\n" \
  "v_smfmac_f32_16x16x64_bf16 v[232:235], v[208:211], v[224:231], v249\n" \
  "ds_read_b128 v[212:215], v248 offset:23552\n" \
  "s_waitcnt lgkmcnt(5)\n" \
  "v_smfmac_f32_16x16x64_bf16 v[236:239], v[208:211], v[240:247], v249\n" \
  "ds_read_b128 v[240:243], v248 offset:18432\n" \
  "v_smfmac_f32_16x16x64_bf16 v[232:235], v[216:219], v[128:135], v249\n" \
  "ds_read_b128 v[244:247], v248 offset:19456\n" \
  "v_smfmac_f32_16x16x64_bf16 v[232:235], v[220:223], v[136:143], v249\n" \
  "ds_read_b128 v[208:211], v248 offset:22528\n" \
  "v_smfmac_f32_16x16x64_bf16 v[236:239], v[216:219], v[176:183], v249\n" \
  "ds_read_b96 v[250:252], v248 offset:13312\n" \
  "v_smfmac_f32_16x16x64_bf16 v[236:239], v[220:223], v[184:191], v249\n" \
  "s_setprio 3\n" \
  "s_barrier\n" \
  "ds_read_b64_tr_b16 v[224:225], v253 offset:17408\n" \
  "ds_read_b64_tr_b16 v[226:227], v253 offset:17664\n" \
  "ds_read_b64_tr_b16 v[228:229], v253 offset:19456\n" \
  "ds_read_b64_tr_b16 v[230:231], v253 offset:19712\n" \
  "v_mov_b64 v[216:217], 0\n" \
  "v_mov_b64 v[218:219], 0\n" \
  "v_add_f32 v232, v232, v234\n" \
  "v_add_f32 v233, v233, v235\n" \
  "ds_write_b64 v248, v[232:233] offset:0\n" \
  "v_mov_b64 v[220:221], 0\n" \
  "v_mov_b64 v[222:223], 0\n" \
  "v_add_f32 v236, v236, v238\n" \
  "v_add_f32 v237, v237, v239\n" \
  "ds_write_b64 v248, v[236:237] offset:8\n" \
  "s_waitcnt lgkmcnt(4)\n" \
  "v_smfmac_f32_16x16x64_bf16 v[216:219], v[224:227], v[192:199], v249\n" \
  "v_smfmac_f32_16x16x64_bf16 v[220:223], v[224:227], v[200:207], v249\n" \
  "s_waitcnt lgkmcnt(2)\n" \
  "v_smfmac_f32_16x16x64_bf16 v[216:219], v[228:231], v[240:247], v249\n" \
  "v_smfmac_f32_16x16x64_bf16 v[220:223], v[228:231], v[208:215], v249\n" \
  "s_mov_b32 %[rt], 0x422c0000\n" \
  "s_waitcnt lgkmcnt(0)\n" \
  "s_barrier\n" \
  "s_nop 1\n" \
  "v_add_f32 v192, v216, v218\n" \
  "s_nop 2\n" \
  "v_add_f32 v193, v220, v222\n" \
  "v_exp_f32 v194, v192\n" \
  "v_exp_f32 v195, v193\n" \
  "v_max_f32 v197, |v192|, |v193|\n" \
  "v_cvt_pk_bf16_f32 v196, v194, v195\n" \
  "v_cmp_nge_f32 vcc, %[rt], v197\n" \
  "s_cbranch_vccnz .Lw1_%=\n" \
  ".Lw1b_%=:\n" \
  "buffer_store_dword v196, v250, %[rs], %[c0] offen sc1\n" \
  "buffer_store_dword v196, v250, %[rs], %[c1] offen sc1\n" \
  "buffer_store_dword v196, v250, %[rs], %[c2] offen sc1\n" \
  "buffer_store_dword v196, v250, %[rs], %[c3] offen sc1\n" \
  "v_add_f32 v199, v194, v195\n" \
  "v_xor_b32 v251, %[xr], v251\n" \
  "s_nop 0\n" \
  "v_add_f32_dpp v199, v199, v199 quad_perm:[1,0,3,2] row_mask:0xf bank_mask:0xf\n" \
  "s_nop 1\n" \
  "v_add_f32_dpp v199, v199, v199 quad_perm:[2,3,0,1] row_mask:0xf bank_mask:0xf\n" \
  "s_barrier\n" \
  "ds_read_b64_tr_b16 v[232:233], v253 offset:9216\n" \
  "ds_read_b64_tr_b16 v[234:235], v253 offset:9472\n" \
  "ds_read_b64_tr_b16 v[236:237], v253 offset:11264\n" \
  "ds_read_b64_tr_b16 v[238:239], v253 offset:11520\n" \
  "s_waitcnt lgkmcnt(2)\n" \
  "v_mfma_f32_16x16x32_bf16 v[216:219], v[224:227], v[232:235], 0\n" \
  "s_waitcnt lgkmcnt(0)\n" \
  "v_mfma_f32_16x16x32_bf16 v[216:219], v[228:231], v[236:239], v[216:219]\n" \
  "s_nop 8\n" \
  "v_add_f32_dpp v192, v218, v216 quad_perm:[2,3,2,3] row_mask:0xf bank_mask:0xf\n" \
  "v_exp_f32 v194, v192\n" \
  "v_cmp_nge_f32 vcc, %[rt], |v192|\n" \
  "s_and_b64 vcc, vcc, %[lo]\n" \
  "v_add_f32_dpp v198, v194, v199 quad_perm:[0,0,0,0] row_mask:0xf bank_mask:0xf\n" \
  "v_mov_b32_dpp v193, v194 quad_perm:[1,1,1,1] row_mask:0xf bank_mask:0xf\n" \
  "v_add_f32 v198, v198, v193\n" \
  "v_mov_b32_dpp v195, v194 quad_perm:[0,0,0,0] row_mask:0xf bank_mask:0xf\n" \
  "s_cbranch_scc1 .Lw2_%=\n" \
  "v_add_f32_dpp v198, v198, v198 row_ror:4 row_mask:0xf bank_mask:0xf\n" \
  "s_nop 1\n" \
  "v_add_f32_dpp v198, v198, v198 row_ror:8 row_mask:0xf bank_mask:0xf\n" \
  "v_rcp_f32 v198, v198\n" \
  "s_nop 0\n" \
  "v_mul_f32 v195, v195, v198\n" \
  "v_mul_f32 v193, v193, v198\n" \
  "v_cvt_pk_bf16_f32 v196, v195, v193\n" \
  "buffer_store_dword v196, v251, %[rs], %[so] offen sc1\n" \
  "v_cvt_pk_bf16_f32 v197, v198, v198\n" \
  "v_and_b32 v197, v252, v197\n" \
  "buffer_store_dword v197, v251, %[rs], %[so] offen offset:64 sc1\n" \
  ".Lw2b_%=:\n" \
  "s_setprio 0\n"
#define QK_A1 \
  "s_branch .Lgo_%=\n" \
  AL(A_QK_A1) \
  ".Lr_%=:\n" \
  "s_sub_u32 %[rt], %[rt], 1\n" \
  "s_cbranch_scc1 .Lrb_%=\n" \
  "s_sleep 2\n" \
  "buffer_load_dwordx4 v[192:195], v248, %[rs], %[o0] offen sc1\n" \
  "buffer_load_dwordx4 v[196:199], v248, %[rs], %[o0] offen offset:1024 sc1\n" \
  "v_mov_b64 v[224:225], 0\n" \
  "v_mov_b64 v[226:227], 0\n" \
  "v_mov_b64 v[228:229], 0\n" \
  "v_mov_b64 v[230:231], 0\n" \
  "v_mov_b64 v[232:233], 0\n" \
  "v_mov_b64 v[234:235], 0\n" \
  "v_mov_b64 v[236:237], 0\n" \
  "v_mov_b64 v[238:239], 0\n" \
  "s_nop 1\n" \
  "s_branch .Lw_%=\n" \
  ".Lw1_%=:\n" \
  "v_max_f32 v200, v192, v193\n" \
  "s_nop 1\n" \
  "v_max_f32_dpp v200, v200, v200 row_ror:8 row_mask:0xf bank_mask:0xf\n" \
  "s_nop 1\n" \
  "v_max_f32_dpp v200, v200, v200 row_ror:4 row_mask:0xf bank_mask:0xf\n" \
  "s_nop 1\n" \
  "v_max_f32_dpp v200, v200, v200 row_ror:2 row_mask:0xf bank_mask:0xf\n" \
  "s_nop 1\n" \
  "v_max_f32_dpp v200, v200, v200 row_ror:1 row_mask:0xf bank_mask:0xf\n" \
  "v_sub_f32 v194, v192, v200\n" \
  "v_sub_f32 v195, v193, v200\n" \
  "v_exp_f32 v194, v194\n" \
  "v_exp_f32 v195, v195\n" \
  "s_mov_b32 %[rt], -1.0\n" \
  "s_nop 0\n" \
  "v_cvt_pk_bf16_f32 v196, v194, v195\n" \
  "v_and_b32 v196, 0x7fffffff, v196\n" \
  "s_branch .Lw1b_%=\n" \
  ".Lw2_%=:\n" \
  "s_cmp_lt_i32 %[rt], 0\n" \
  "s_cbranch_scc1 .Lw3_%=\n" \
  "v_mov_b32 v200, 0\n" \
  ".Lw3_%=:\n" \
  "s_nop 1\n" \
  "v_max_f32_dpp v197, v192, v200 quad_perm:[0,0,0,0] row_mask:0xf bank_mask:0xf\n" \
  "s_nop 1\n" \
  "v_max_f32_dpp v197, v192, v197 quad_perm:[1,1,1,1] row_mask:0xf bank_mask:0xf\n" \
  "s_nop 1\n" \
  "v_max_f32_dpp v197, v197, v197 row_ror:4 row_mask:0xf bank_mask:0xf\n" \
  "s_nop 1\n" \
  "v_max_f32_dpp v197, v197, v197 row_ror:8 row_mask:0xf bank_mask:0xf\n" \
  "v_sub_f32 v194, v192, v197\n" \
  "v_sub_f32 v201, v200, v197\n" \
  "v_exp_f32 v194, v194\n" \
  "v_exp_f32 v201, v201\n" \
  "s_nop 1\n" \
  "v_mul_f32 v199, v201, v199\n" \
  "s_nop 1\n" \
  "v_add_f32_dpp v198, v194, v199 quad_perm:[0,0,0,0] row_mask:0xf bank_mask:0xf\n" \
  "s_nop 1\n" \
  "v_mov_b32_dpp v193, v194 quad_perm:[1,1,1,1] row_mask:0xf bank_mask:0xf\n" \
  "s_nop 1\n" \
  "v_add_f32 v198, v198, v193\n" \
  "s_nop 1\n" \
  "v_mov_b32_dpp v195, v194 quad_perm:[0,0,0,0] row_mask:0xf bank_mask:0xf\n" \
  "s_nop 1\n" \
  "v_add_f32_dpp v198, v198, v198 row_ror:4 row_mask:0xf bank_mask:0xf\n" \
  "s_nop 1\n" \
  "v_add_f32_dpp v198, v198, v198 row_ror:8 row_mask:0xf bank_mask:0xf\n" \
  "s_nop 1\n" \
  "v_rcp_f32 v198, v198\n" \
  "s_nop 1\n" \
  "v_mul_f32 v195, v195, v198\n" \
  "s_nop 1\n" \
  "v_mul_f32 v193, v193, v198\n" \
  "s_nop 1\n" \
  "v_cvt_pk_bf16_f32 v196, v195, v193\n" \
  "s_nop 1\n" \
  "v_and_b32 v196, 0x7fffffff, v196\n" \
  "s_nop 1\n" \
  "v_mul_f32 v198, v201, v198\n" \
  "s_nop 1\n" \
  "buffer_store_dword v196, v251, %[rs], %[so] offen sc1\n" \
  "s_nop 1\n" \
  "v_cvt_pk_bf16_f32 v197, v198, v198\n" \
  "s_nop 1\n" \
  "v_and_b32 v197, v252, v197\n" \
  "s_nop 1\n" \
  "buffer_store_dword v197, v251, %[rs], %[so] offen offset:64 sc1\n" \
  "s_branch .Lw2b_%=\n" \
  ".Lgo_%=:\n" \
  "v_add_u32 v252, 0x20000, v248\n" \
  "ds_read_b128 v[200:203], v248 offset:32768\n" \
  "ds_read_b128 v[204:207], v248 offset:33792\n" \
  "ds_read_b128 v[208:211], v248 offset:49152\n" \
  "ds_read_b128 v[212:215], v248 offset:50176\n" \
  "ds_read_b128 v[216:219], v252 offset:0\n" \
  "ds_read_b128 v[220:223], v252 offset:1024\n" \
  "ds_read_b128 v[240:243], v252 offset:16384\n" \
  "ds_read_b128 v[244:247], v252 offset:17408\n" \
  "v_mov_b64 v[224:225], 0\n" \
  "v_mov_b64 v[226:227], 0\n" \
  "v_mov_b64 v[228:229], 0\n" \
  "v_mov_b64 v[230:231], 0\n" \
  "v_mov_b64 v[232:233], 0\n" \
  "v_mov_b64 v[234:235], 0\n" \
  "v_mov_b64 v[236:237], 0\n" \
  "v_mov_b64 v[238:239], 0\n" \
  "v_xor_b32 v250, 0x800, v248\n" \
  "v_xor_b32 v251, 0x1000, v248\n" \
  "s_mov_b32 %[rt], 0x40000\n" \
  "s_setprio 2\n" \
  "s_waitcnt lgkmcnt(0)\n" \
  "s_barrier\n" \
  "buffer_load_dwordx4 v[192:195], v248, %[rs], %[o0] offen sc1\n" \
  "buffer_load_dwordx4 v[196:199], v248, %[rs], %[o0] offen offset:1024 sc1\n" \
  ".Lw_%=:\n" \
  "s_waitcnt vmcnt(1)\n" \
  "ds_write_b128 v248, v[192:195]\n" \
  "v_smfmac_f32_16x16x64_bf16 v[224:227], v[192:195], v[200:207], v249\n" \
  "v_max3_u32 v252, v192, v193, v194\n" \
  "s_waitcnt vmcnt(0)\n" \
  "ds_write_b128 v248, v[196:199] offset:1024\n" \
  "v_smfmac_f32_16x16x64_bf16 v[228:231], v[192:195], v[208:215], v249\n" \
  "v_max3_u32 v252, v252, v195, v196\n" \
  "v_smfmac_f32_16x16x64_bf16 v[224:227], v[196:199], v[216:223], v249\n" \
  "v_max3_u32 v252, v252, v197, v198\n" \
  "v_smfmac_f32_16x16x64_bf16 v[228:231], v[196:199], v[240:247], v249\n" \
  "v_max_u32 v252, v252, v199\n" \
  "v_smfmac_f32_16x16x64_bf16 v[232:235], v[192:195], v[96:103], v249\n" \
  "v_cmp_eq_u32 vcc, -1, v252\n" \
  "v_smfmac_f32_16x16x64_bf16 v[236:239], v[192:195], v[144:151], v249\n" \
  "v_xor_b32 v252, 0x1800, v248\n" \
  "s_cbranch_vccnz .Lr_%=\n" \
  ".Lrb_%=:\n" \
  "v_smfmac_f32_16x16x64_bf16 v[232:235], v[196:199], v[104:111], v249\n" \
  "v_smfmac_f32_16x16x64_bf16 v[236:239], v[196:199], v[152:159], v249\n" \
  "s_waitcnt lgkmcnt(0)\n" \
  "s_barrier\n" \
  "ds_read_b128 v[200:203], v250 offset:0\n" \
  "ds_read_b128 v[204:207], v250 offset:1024\n" \
  "ds_read_b128 v[208:211], v251 offset:0\n" \
  "ds_read_b128 v[212:215], v251 offset:1024\n" \
  "ds_read_b128 v[216:219], v252 offset:0\n" \
  "ds_read_b128 v[220:223], v252 offset:1024\n" \
  "s_waitcnt lgkmcnt(5)\n" \
  "v_smfmac_f32_16x16x64_bf16 v[224:227], v[200:203], v[0:7], v249\n" \
  "v_smfmac_f32_16x16x64_bf16 v[228:231], v[200:203], v[48:55], v249\n" \
  "s_waitcnt lgkmcnt(4)\n" \
  "v_smfmac_f32_16x16x64_bf16 v[224:227], v[204:207], v[8:15], v249\n" \
  "v_smfmac_f32_16x16x64_bf16 v[228:231], v[204:207], v[56:63], v249\n" \
  "s_waitcnt lgkmcnt(3)\n" \
  "v_smfmac_f32_16x16x64_bf16 v[224:227], v[208:211], v[16:23], v249\n" \
  "v_smfmac_f32_16x16x64_bf16 v[228:231], v[208:211], v[64:71], v249\n" \
  "s_waitcnt lgkmcnt(2)\n" \
  "v_smfmac_f32_16x16x64_bf16 v[224:227], v[212:215], v[24:31], v249\n" \
  "s_waitcnt lgkmcnt(1)\n" \
  "v_smfmac_f32_16x16x64_bf16 v[224:227], v[216:219], v[32:39], v249\n" \
  "s_waitcnt lgkmcnt(0)\n" \
  "v_smfmac_f32_16x16x64_bf16 v[224:227], v[220:223], v[40:47], v249\n" \
  "v_smfmac_f32_16x16x64_bf16 v[228:231], v[212:215], v[72:79], v249\n" \
  "v_smfmac_f32_16x16x64_bf16 v[228:231], v[216:219], v[80:87], v249\n" \
  "s_nop 0\n" \
  "v_add_f32 v224, v224, v226\n" \
  "v_smfmac_f32_16x16x64_bf16 v[228:231], v[220:223], v[88:95], v249\n" \
  "v_add_f32 v225, v225, v227\n" \
  "ds_write_b64 v248, v[224:225] offset:8192\n" \
  "s_nop 6\n" \
  "v_add_f32 v228, v228, v230\n" \
  "v_add_f32 v229, v229, v231\n" \
  "ds_write_b64 v248, v[228:229] offset:8200\n" \
  "s_setprio 0\n" \
  "ds_read_b128 v[240:243], v254 offset:0\n" \
  "ds_read_b128 v[244:247], v254 offset:1024\n" \
  "s_waitcnt lgkmcnt(2)\n" \
  "s_barrier\n" \
  "s_nop 1\n" \
  "ds_read_b128 v[192:195], v254 offset:16384\n" \
  "ds_read_b128 v[196:199], v254 offset:17408\n" \
  "ds_read_b128 v[224:227], v254 offset:32768\n" \
  "ds_read_b128 v[228:231], v254 offset:33792\n" \
  "s_waitcnt lgkmcnt(4)\n" \
  "v_smfmac_f32_16x16x64_bf16 v[232:235], v[200:203], v[240:247], v249\n" \
  "ds_read_b128 v[240:243], v254 offset:49152\n" \
  "ds_read_b128 v[244:247], v254 offset:50176\n" \
  "s_waitcnt lgkmcnt(4)\n" \
  "v_smfmac_f32_16x16x64_bf16 v[236:239], v[200:203], v[192:199], v249\n" \
  "v_smfmac_f32_16x16x64_bf16 v[232:235], v[204:207], v[112:119], v249\n" \
  "ds_read_b128 v[192:195], v248 offset:14336\n" \
  "v_smfmac_f32_16x16x64_bf16 v[236:239], v[204:207], v[160:167], v249\n" \
  "ds_read_b128 v[196:199], v248 offset:15360\n" \
  "v_smfmac_f32_16x16x64_bf16 v[232:235], v[212:215], v[120:127], v249\n" \
  "ds_read_b128 v[200:203], v248 offset:18432\n" \
  "v_smfmac_f32_16x16x64_bf16 v[236:239], v[212:215], v[168:175], v249\n" \
  "ds_read_b128 v[204:207], v248 offset:19456\n" \
  "s_waitcnt lgkmcnt(6)\n" \
  "v_smfmac_f32_16x16x64_bf16 v[232:235], v[208:211], v[224:231], v249\n" \
  "ds_read_b128 v[212:215], v248 offset:21504\n" \
  "s_waitcnt lgkmcnt(5)\n" \
  "v_smfmac_f32_16x16x64_bf16 v[236:239], v[208:211], v[240:247], v249\n" \
  "ds_read_b128 v[240:243], v248 offset:16384\n" \
  "v_smfmac_f32_16x16x64_bf16 v[232:235], v[216:219], v[128:135], v249\n" \
  "ds_read_b128 v[244:247], v248 offset:17408\n" \
  "v_smfmac_f32_16x16x64_bf16 v[232:235], v[220:223], v[136:143], v249\n" \
  "ds_read_b128 v[208:211], v248 offset:20480\n" \
  "v_smfmac_f32_16x16x64_bf16 v[236:239], v[216:219], v[176:183], v249\n" \
  "ds_read_b96 v[250:252], v248 offset:13312\n" \
  "v_smfmac_f32_16x16x64_bf16 v[236:239], v[220:223], v[184:191], v249\n" \
  "s_setprio 3\n" \
  "s_barrier\n" \
  "ds_read_b64_tr_b16 v[224:225], v253 offset:17408\n" \
  "ds_read_b64_tr_b16 v[226:227], v253 offset:17664\n" \
  "ds_read_b64_tr_b16 v[228:229], v253 offset:19456\n" \
  "ds_read_b64_tr_b16 v[230:231], v253 offset:19712\n" \
  "v_mov_b64 v[216:217], 0\n" \
  "v_mov_b64 v[218:219], 0\n" \
  "v_add_f32 v232, v232, v234\n" \
  "v_add_f32 v233, v233, v235\n" \
  "ds_write_b64 v248, v[232:233] offset:0\n" \
  "v_mov_b64 v[220:221], 0\n" \
  "v_mov_b64 v[222:223], 0\n" \
  "v_add_f32 v236, v236, v238\n" \
  "v_add_f32 v237, v237, v239\n" \
  "ds_write_b64 v248, v[236:237] offset:8\n" \
  "s_waitcnt lgkmcnt(4)\n" \
  "v_smfmac_f32_16x16x64_bf16 v[216:219], v[224:227], v[192:199], v249\n" \
  "v_smfmac_f32_16x16x64_bf16 v[220:223], v[224:227], v[200:207], v249\n" \
  "s_waitcnt lgkmcnt(2)\n" \
  "v_smfmac_f32_16x16x64_bf16 v[216:219], v[228:231], v[240:247], v249\n" \
  "v_smfmac_f32_16x16x64_bf16 v[220:223], v[228:231], v[208:215], v249\n" \
  "s_mov_b32 %[rt], 0x422c0000\n" \
  "s_waitcnt lgkmcnt(0)\n" \
  "s_barrier\n" \
  "s_nop 1\n" \
  "v_add_f32 v192, v217, v219\n" \
  "s_nop 2\n" \
  "v_add_f32 v193, v221, v223\n" \
  "v_exp_f32 v194, v192\n" \
  "v_exp_f32 v195, v193\n" \
  "v_max_f32 v197, |v192|, |v193|\n" \
  "v_cvt_pk_bf16_f32 v196, v194, v195\n" \
  "v_cmp_nge_f32 vcc, %[rt], v197\n" \
  "s_cbranch_vccnz .Lw1_%=\n" \
  ".Lw1b_%=:\n" \
  "buffer_store_dword v196, v250, %[rs], %[c0] offen sc1\n" \
  "buffer_store_dword v196, v250, %[rs], %[c1] offen sc1\n" \
  "buffer_store_dword v196, v250, %[rs], %[c2] offen sc1\n" \
  "buffer_store_dword v196, v250, %[rs], %[c3] offen sc1\n" \
  "v_add_f32 v199, v194, v195\n" \
  "v_xor_b32 v251, %[xr], v251\n" \
  "s_nop 0\n" \
  "v_add_f32_dpp v199, v199, v199 quad_perm:[1,0,3,2] row_mask:0xf bank_mask:0xf\n" \
  "s_nop 1\n" \
  "v_add_f32_dpp v199, v199, v199 quad_perm:[2,3,0,1] row_mask:0xf bank_mask:0xf\n" \
  "s_barrier\n" \
  "ds_read_b64_tr_b16 v[232:233], v253 offset:9216\n" \
  "ds_read_b64_tr_b16 v[234:235], v253 offset:9472\n" \
  "ds_read_b64_tr_b16 v[236:237], v253 offset:11264\n" \
  "ds_read_b64_tr_b16 v[238:239], v253 offset:11520\n" \
  "s_waitcnt lgkmcnt(2)\n" \
  "v_mfma_f32_16x16x32_bf16 v[216:219], v[224:227], v[232:235], 0\n" \
  "s_waitcnt lgkmcnt(0)\n" \
  "v_mfma_f32_16x16x32_bf16 v[216:219], v[228:231], v[236:239], v[216:219]\n" \
  "s_nop 8\n" \
  "v_add_f32_dpp v192, v219, v217 quad_perm:[2,3,2,3] row_mask:0xf bank_mask:0xf\n" \
  "v_exp_f32 v194, v192\n" \
  "v_cmp_nge_f32 vcc, %[rt], |v192|\n" \
  "s_and_b64 vcc, vcc, %[lo]\n" \
  "v_add_f32_dpp v198, v194, v199 quad_perm:[0,0,0,0] row_mask:0xf bank_mask:0xf\n" \
  "v_mov_b32_dpp v193, v194 quad_perm:[1,1,1,1] row_mask:0xf bank_mask:0xf\n" \
  "v_add_f32 v198, v198, v193\n" \
  "v_mov_b32_dpp v195, v194 quad_perm:[0,0,0,0] row_mask:0xf bank_mask:0xf\n" \
  "s_cbranch_scc1 .Lw2_%=\n" \
  "v_add_f32_dpp v198, v198, v198 row_ror:4 row_mask:0xf bank_mask:0xf\n" \
  "s_nop 1\n" \
  "v_add_f32_dpp v198, v198, v198 row_ror:8 row_mask:0xf bank_mask:0xf\n" \
  "v_rcp_f32 v198, v198\n" \
  "s_nop 0\n" \
  "v_mul_f32 v195, v195, v198\n" \
  "v_mul_f32 v193, v193, v198\n" \
  "v_cvt_pk_bf16_f32 v196, v195, v193\n" \
  "buffer_store_dword v196, v251, %[rs], %[so] offen sc1\n" \
  "v_cvt_pk_bf16_f32 v197, v198, v198\n" \
  "v_and_b32 v197, v252, v197\n" \
  "buffer_store_dword v197, v251, %[rs], %[so] offen offset:64 sc1\n" \
  ".Lw2b_%=:\n" \
  "s_setprio 0\n"
#define QK_X \
  "s_branch .Lgo_%=\n" \
  AL(A_QK_X) \
  ".Lr_%=:\n" \
  "s_sub_u32 %[rt], %[rt], 1\n" \
  "s_cbranch_scc1 .Lrb_%=\n" \
  "s_sleep 2\n" \
  "buffer_load_dwordx4 v[192:195], v248, %[rs], %[o0] offen sc1\n" \
  "buffer_load_dwordx4 v[196:199], v248, %[rs], %[o0] offen offset:1024 sc1\n" \
  "v_mov_b64 v[224:225], 0\n" \
  "v_mov_b64 v[226:227], 0\n" \
  "v_mov_b64 v[228:229], 0\n" \
  "v_mov_b64 v[230:231], 0\n" \
  "v_mov_b64 v[232:233], 0\n" \
  "v_mov_b64 v[234:235], 0\n" \
  "v_mov_b64 v[236:237], 0\n" \
  "v_mov_b64 v[238:239], 0\n" \
  "s_nop 1\n" \
  "s_branch .Lw_%=\n" \
  ".Lgo_%=:\n" \
  "v_add_u32 v252, 0x20000, v248\n" \
  "ds_read_b128 v[200:203], v248 offset:32768\n" \
  "ds_read_b128 v[204:207], v248 offset:33792\n" \
  "ds_read_b128 v[208:211], v248 offset:49152\n" \
  "ds_read_b128 v[212:215], v248 offset:50176\n" \
  "ds_read_b128 v[216:219], v252 offset:0\n" \
  "ds_read_b128 v[220:223], v252 offset:1024\n" \
  "ds_read_b128 v[240:243], v252 offset:16384\n" \
  "ds_read_b128 v[244:247], v252 offset:17408\n" \
  "v_mov_b64 v[224:225], 0\n" \
  "v_mov_b64 v[226:227], 0\n" \
  "v_mov_b64 v[228:229], 0\n" \
  "v_mov_b64 v[230:231], 0\n" \
  "v_mov_b64 v[232:233], 0\n" \
  "v_mov_b64 v[234:235], 0\n" \
  "v_mov_b64 v[236:237], 0\n" \
  "v_mov_b64 v[238:239], 0\n" \
  "v_xor_b32 v250, 0x800, v248\n" \
  "v_xor_b32 v251, 0x1000, v248\n" \
  "s_mov_b32 %[rt], 0x40000\n" \
  "s_setprio 2\n" \
  "s_waitcnt lgkmcnt(0)\n" \
  "s_barrier\n" \
  "s_nop 3\n" \
  "buffer_load_dwordx4 v[192:195], v248, %[rs], %[o0] offen sc1\n" \
  "buffer_load_dwordx4 v[196:199], v248, %[rs], %[o0] offen offset:1024 sc1\n" \
  ".Lw_%=:\n" \
  "s_waitcnt vmcnt(1)\n" \
  "ds_write_b128 v248, v[192:195]\n" \
  "v_smfmac_f32_16x16x64_bf16 v[224:227], v[192:195], v[200:207], v249\n" \
  "v_max3_u32 v252, v192, v193, v194\n" \
  "s_waitcnt vmcnt(0)\n" \
  "ds_write_b128 v248, v[196:199] offset:1024\n" \
  "v_smfmac_f32_16x16x64_bf16 v[228:231], v[192:195], v[208:215], v249\n" \
  "v_max3_u32 v252, v252, v195, v196\n" \
  "v_smfmac_f32_16x16x64_bf16 v[224:227], v[196:199], v[216:223], v249\n" \
  "v_max3_u32 v252, v252, v197, v198\n" \
  "v_smfmac_f32_16x16x64_bf16 v[228:231], v[196:199], v[240:247], v249\n" \
  "v_max_u32 v252, v252, v199\n" \
  "v_smfmac_f32_16x16x64_bf16 v[232:235], v[192:195], v[96:103], v249\n" \
  "v_cmp_eq_u32 vcc, -1, v252\n" \
  "v_smfmac_f32_16x16x64_bf16 v[236:239], v[192:195], v[144:151], v249\n" \
  "v_xor_b32 v252, 0x1800, v248\n" \
  "s_cbranch_vccnz .Lr_%=\n" \
  ".Lrb_%=:\n" \
  "v_smfmac_f32_16x16x64_bf16 v[232:235], v[196:199], v[104:111], v249\n" \
  "v_smfmac_f32_16x16x64_bf16 v[236:239], v[196:199], v[152:159], v249\n" \
  "s_waitcnt lgkmcnt(0)\n" \
  "s_barrier\n" \
  "ds_read_b128 v[200:203], v250 offset:0\n" \
  "ds_read_b128 v[204:207], v250 offset:1024\n" \
  "ds_read_b128 v[208:211], v251 offset:0\n" \
  "ds_read_b128 v[212:215], v251 offset:1024\n" \
  "ds_read_b128 v[216:219], v252 offset:0\n" \
  "ds_read_b128 v[220:223], v252 offset:1024\n" \
  "s_waitcnt lgkmcnt(5)\n" \
  "v_smfmac_f32_16x16x64_bf16 v[224:227], v[200:203], v[0:7], v249\n" \
  "v_smfmac_f32_16x16x64_bf16 v[228:231], v[200:203], v[48:55], v249\n" \
  "s_waitcnt lgkmcnt(4)\n" \
  "v_smfmac_f32_16x16x64_bf16 v[224:227], v[204:207], v[8:15], v249\n" \
  "v_smfmac_f32_16x16x64_bf16 v[228:231], v[204:207], v[56:63], v249\n" \
  "s_waitcnt lgkmcnt(3)\n" \
  "v_smfmac_f32_16x16x64_bf16 v[224:227], v[208:211], v[16:23], v249\n" \
  "v_smfmac_f32_16x16x64_bf16 v[228:231], v[208:211], v[64:71], v249\n" \
  "s_waitcnt lgkmcnt(2)\n" \
  "v_smfmac_f32_16x16x64_bf16 v[224:227], v[212:215], v[24:31], v249\n" \
  "s_waitcnt lgkmcnt(1)\n" \
  "v_smfmac_f32_16x16x64_bf16 v[224:227], v[216:219], v[32:39], v249\n" \
  "s_waitcnt lgkmcnt(0)\n" \
  "v_smfmac_f32_16x16x64_bf16 v[224:227], v[220:223], v[40:47], v249\n" \
  "v_smfmac_f32_16x16x64_bf16 v[228:231], v[212:215], v[72:79], v249\n" \
  "v_and_b32 v252, 0x3ff, v248\n" \
  "v_smfmac_f32_16x16x64_bf16 v[228:231], v[216:219], v[80:87], v249\n" \
  "s_nop 0\n" \
  "v_add_f32 v224, v224, v226\n" \
  "v_smfmac_f32_16x16x64_bf16 v[228:231], v[220:223], v[88:95], v249\n" \
  "v_add_f32 v225, v225, v227\n" \
  "ds_write_b64 v248, v[224:225] offset:8192\n" \
  "s_nop 6\n" \
  "v_add_f32 v228, v228, v230\n" \
  "v_add_f32 v229, v229, v231\n" \
  "ds_write_b64 v248, v[228:229] offset:8200\n" \
  "s_setprio 0\n" \
  "ds_read_b128 v[240:243], v254 offset:0\n" \
  "ds_read_b128 v[244:247], v254 offset:1024\n" \
  "s_waitcnt lgkmcnt(2)\n" \
  "s_barrier\n" \
  "s_nop 1\n" \
  "ds_read_b128 v[192:195], v254 offset:16384\n" \
  "ds_read_b128 v[196:199], v254 offset:17408\n" \
  "ds_read_b128 v[224:227], v254 offset:32768\n" \
  "ds_read_b128 v[228:231], v254 offset:33792\n" \
  "s_waitcnt lgkmcnt(4)\n" \
  "v_smfmac_f32_16x16x64_bf16 v[232:235], v[200:203], v[240:247], v249\n" \
  "ds_read_b128 v[240:243], v254 offset:49152\n" \
  "ds_read_b128 v[244:247], v254 offset:50176\n" \
  "s_waitcnt lgkmcnt(4)\n" \
  "v_smfmac_f32_16x16x64_bf16 v[236:239], v[200:203], v[192:199], v249\n" \
  "v_smfmac_f32_16x16x64_bf16 v[232:235], v[204:207], v[112:119], v249\n" \
  "v_smfmac_f32_16x16x64_bf16 v[236:239], v[204:207], v[160:167], v249\n" \
  "v_smfmac_f32_16x16x64_bf16 v[232:235], v[212:215], v[120:127], v249\n" \
  "v_smfmac_f32_16x16x64_bf16 v[236:239], v[212:215], v[168:175], v249\n" \
  "s_waitcnt lgkmcnt(2)\n" \
  "v_smfmac_f32_16x16x64_bf16 v[232:235], v[208:211], v[224:231], v249\n" \
  "s_waitcnt lgkmcnt(0)\n" \
  "v_smfmac_f32_16x16x64_bf16 v[236:239], v[208:211], v[240:247], v249\n" \
  "v_smfmac_f32_16x16x64_bf16 v[232:235], v[216:219], v[128:135], v249\n" \
  "v_smfmac_f32_16x16x64_bf16 v[232:235], v[220:223], v[136:143], v249\n" \
  "v_smfmac_f32_16x16x64_bf16 v[236:239], v[216:219], v[176:183], v249\n" \
  "buffer_load_dwordx4 v[224:227], v252, %[rs], %[qo] offen offset:0 sc1\n" \
  "buffer_load_dwordx4 v[228:231], v252, %[rs], %[qo] offen offset:1024 sc1\n" \
  "buffer_load_dwordx4 v[240:243], v252, %[rs], %[qo] offen offset:2048 sc1\n" \
  "buffer_load_dwordx4 v[244:247], v252, %[rs], %[qo] offen offset:3072 sc1\n" \
  "v_smfmac_f32_16x16x64_bf16 v[236:239], v[220:223], v[184:191], v249\n" \
  "s_setprio 3\n" \
  "s_barrier\n" \
  "v_add_f32 v232, v232, v234\n" \
  "v_add_f32 v233, v233, v235\n" \
  "ds_write_b64 v248, v[232:233] offset:0\n" \
  "s_nop 3\n" \
  "v_add_f32 v236, v236, v238\n" \
  "v_add_f32 v237, v237, v239\n" \
  "ds_write_b64 v248, v[236:237] offset:8\n" \
  "s_waitcnt lgkmcnt(0)\n" \
  "s_barrier\n" \
  "s_setprio 0\n" \
  "s_nop 15\n" \
  "s_nop 13\n" \
  "ds_read_b128 v[192:195], v252 offset:24576\n" \
  "ds_read_b128 v[196:199], v252 offset:28672\n" \
  "ds_read_b128 v[200:203], v252 offset:25600\n" \
  "ds_read_b128 v[204:207], v252 offset:29696\n" \
  "ds_read_b128 v[208:211], v252 offset:26624\n" \
  "ds_read_b128 v[212:215], v252 offset:30720\n" \
  "ds_read_b128 v[216:219], v252 offset:27648\n" \
  "ds_read_b128 v[220:223], v252 offset:31744\n" \
  "s_waitcnt lgkmcnt(0)\n" \
  "s_barrier\n" \
  "s_nop 15\n" \
  "s_nop 3\n" \
  "ds_read_b64_tr_b16 v[232:233], v253 offset:9216\n" \
  "ds_read_b64_tr_b16 v[234:235], v253 offset:9472\n" \
  "ds_read_b64_tr_b16 v[236:237], v253 offset:11264\n" \
  "ds_read_b64_tr_b16 v[238:239], v253 offset:11520\n" \
  "s_waitcnt vmcnt(3)\n" \
  "v_mfma_f32_16x16x32_bf16 v[192:195], v[192:195], v[224:227], 0\n" \
  "v_mfma_f32_16x16x32_bf16 v[196:199], v[196:199], v[224:227], 0\n" \
  "s_waitcnt vmcnt(2)\n" \
  "v_mfma_f32_16x16x32_bf16 v[192:195], v[200:203], v[228:231], v[192:195]\n" \
  "v_mfma_f32_16x16x32_bf16 v[196:199], v[204:207], v[228:231], v[196:199]\n" \
  "s_waitcnt vmcnt(1)\n" \
  "v_mfma_f32_16x16x32_bf16 v[192:195], v[208:211], v[240:243], v[192:195]\n" \
  "v_mov_b64 v[200:201], 0\n" \
  "v_mfma_f32_16x16x32_bf16 v[196:199], v[212:215], v[240:243], v[196:199]\n" \
  "v_mov_b64 v[202:203], 0\n" \
  "s_waitcnt vmcnt(0)\n" \
  "v_mfma_f32_16x16x32_bf16 v[192:195], v[216:219], v[244:247], v[192:195]\n" \
  "v_mfma_f32_16x16x32_bf16 v[196:199], v[220:223], v[244:247], v[196:199]\n" \
  "s_waitcnt lgkmcnt(2)\n" \
  "v_smfmac_f32_16x16x64_bf16 v[200:203], v[232:235], v[224:231], v249\n" \
  "s_waitcnt lgkmcnt(0)\n" \
  "v_smfmac_f32_16x16x64_bf16 v[200:203], v[236:239], v[240:247], v249\n" \
  "s_nop 8\n"
#define QK_K \
  "s_branch .Lgo_%=\n" \
  AL(A_QK_K) \
  ".Lr_%=:\n" \
  "s_sub_u32 %[rt], %[rt], 1\n" \
  "s_cbranch_scc1 .Lrb_%=\n" \
  "s_sleep 2\n" \
  "buffer_load_dwordx4 v[192:195], v248, %[rs], %[o0] offen sc1\n" \
  "buffer_load_dwordx4 v[196:199], v248, %[rs], %[o0] offen offset:1024 sc1\n" \
  "v_mov_b64 v[224:225], 0\n" \
  "v_mov_b64 v[226:227], 0\n" \
  "v_mov_b64 v[228:229], 0\n" \
  "v_mov_b64 v[230:231], 0\n" \
  "s_nop 1\n" \
  "s_branch .Lw_%=\n" \
  ".Lgo_%=:\n" \
  "v_add_u32 v252, 0x20000, v248\n" \
  "ds_read_b128 v[200:203], v248 offset:32768\n" \
  "ds_read_b128 v[204:207], v248 offset:33792\n" \
  "ds_read_b128 v[208:211], v248 offset:49152\n" \
  "ds_read_b128 v[212:215], v248 offset:50176\n" \
  "ds_read_b128 v[216:219], v252 offset:0\n" \
  "ds_read_b128 v[220:223], v252 offset:1024\n" \
  "ds_read_b128 v[240:243], v252 offset:16384\n" \
  "ds_read_b128 v[244:247], v252 offset:17408\n" \
  "v_mov_b64 v[224:225], 0\n" \
  "v_mov_b64 v[226:227], 0\n" \
  "v_mov_b64 v[228:229], 0\n" \
  "v_mov_b64 v[230:231], 0\n" \
  "v_xor_b32 v250, 0x800, v248\n" \
  "v_xor_b32 v251, 0x1000, v248\n" \
  "s_mov_b32 %[rt], 0x40000\n" \
  "s_setprio 1\n" \
  "s_waitcnt lgkmcnt(0)\n" \
  "s_cmp_eq_u32 %[tw], 0\n" \
  "s_cbranch_scc1 .Ltq_%=\n" \
  ".Ltl_%=:\n" \
  "s_memtime vcc\n" \
  "s_waitcnt lgkmcnt(0)\n" \
  "s_sub_i32 %[cnt], %[tg], vcc_lo\n" \
  "s_cmp_lt_i32 %[cnt], 260\n" \
  "s_cbranch_scc1 .Ltf_%=\n" \
  "s_sleep 1\n" \
  "s_branch .Ltl_%=\n" \
  ".Ltf_%=:\n" \
  "s_max_i32 %[cnt], %[cnt], 0\n" \
  ".Ltg_%=:\n" \
  "s_sub_u32 %[cnt], %[cnt], 28\n" \
  "s_cbranch_scc0 .Ltg_%=\n" \
  ".Ltq_%=:\n" \
  "s_barrier\n" \
  "buffer_load_dwordx4 v[192:195], v248, %[rs], %[o0] offen sc1\n" \
  "buffer_load_dwordx4 v[196:199], v248, %[rs], %[o0] offen offset:1024 sc1\n" \
  ".Lw_%=:\n" \
  "s_waitcnt vmcnt(1)\n" \
  "ds_write_b128 v248, v[192:195]\n" \
  "v_smfmac_f32_16x16x64_bf16 v[224:227], v[192:195], v[200:207], v249\n" \
  "v_max3_u32 v252, v192, v193, v194\n" \
  "s_waitcnt vmcnt(0)\n" \
  "ds_write_b128 v248, v[196:199] offset:1024\n" \
  "v_smfmac_f32_16x16x64_bf16 v[228:231], v[192:195], v[208:215], v249\n" \
  "v_max3_u32 v252, v252, v195, v196\n" \
  "v_smfmac_f32_16x16x64_bf16 v[224:227], v[196:199], v[216:223], v249\n" \
  "v_max3_u32 v252, v252, v197, v198\n" \
  "v_max_u32 v252, v252, v199\n" \
  "v_smfmac_f32_16x16x64_bf16 v[228:231], v[196:199], v[240:247], v249\n" \
  "v_cmp_eq_u32 vcc, -1, v252\n" \
  "v_xor_b32 v252, 0x1800, v248\n" \
  "s_cbranch_vccnz .Lr_%=\n" \
  ".Lrb_%=:\n" \
  "s_waitcnt lgkmcnt(0)\n" \
  "s_barrier\n" \
  "s_memtime vcc\n" \
  "s_setprio 3\n" \
  "s_waitcnt lgkmcnt(0)\n" \
  "s_sub_u32 %[cnt], vcc_lo, %[ckm]\n" \
  "s_mul_hi_u32 %[cnt], %[cnt], %[inv]\n" \
  "s_add_u32 %[cnt], %[cnt], %[r16]\n" \
  "s_and_b32 %[cnt], %[cnt], -2\n" \
  "v_mov_b32 v235, %[cnt]\n" \
  "buffer_store_dword v235, v232, %[rs], %[sst] offen sc1\n" \
  "s_nop 5\n" \
  "ds_read_b128 v[200:203], v250 offset:0\n" \
  "ds_read_b128 v[204:207], v250 offset:1024\n" \
  "ds_read_b128 v[208:211], v251 offset:0\n" \
  "ds_read_b128 v[212:215], v251 offset:1024\n" \
  "ds_read_b128 v[216:219], v252 offset:0\n" \
  "ds_read_b128 v[220:223], v252 offset:1024\n" \
  "buffer_load_dword v250, v233, %[rs], %[t0] offen sc1\n" \
  "buffer_load_dword v251, v234, %[rs], %[t1] offen sc1\n" \
  "s_setprio 1\n" \
  "s_waitcnt lgkmcnt(5)\n" \
  "v_smfmac_f32_16x16x64_bf16 v[224:227], v[200:203], v[0:7], v249\n" \
  "v_smfmac_f32_16x16x64_bf16 v[228:231], v[200:203], v[48:55], v249\n" \
  "s_waitcnt lgkmcnt(4)\n" \
  "v_smfmac_f32_16x16x64_bf16 v[224:227], v[204:207], v[8:15], v249\n" \
  "v_mov_b64 v[232:233], 0\n" \
  "v_smfmac_f32_16x16x64_bf16 v[228:231], v[204:207], v[56:63], v249\n" \
  "v_mov_b64 v[234:235], 0\n" \
  "s_waitcnt lgkmcnt(3)\n" \
  "v_smfmac_f32_16x16x64_bf16 v[224:227], v[208:211], v[16:23], v249\n" \
  "ds_read_b128 v[240:243], v254 offset:0\n" \
  "v_smfmac_f32_16x16x64_bf16 v[228:231], v[208:211], v[64:71], v249\n" \
  "ds_read_b128 v[244:247], v254 offset:1024\n" \
  "s_waitcnt lgkmcnt(4)\n" \
  "v_smfmac_f32_16x16x64_bf16 v[224:227], v[212:215], v[24:31], v249\n" \
  "s_waitcnt lgkmcnt(3)\n" \
  "v_smfmac_f32_16x16x64_bf16 v[224:227], v[216:219], v[32:39], v249\n" \
  "s_waitcnt lgkmcnt(2)\n" \
  "v_smfmac_f32_16x16x64_bf16 v[224:227], v[220:223], v[40:47], v249\n" \
  "s_barrier\n" \
  "ds_read_b128 v[236:239], v248 offset:16384\n" \
  "v_smfmac_f32_16x16x64_bf16 v[228:231], v[212:215], v[72:79], v249\n" \
  "v_smfmac_f32_16x16x64_bf16 v[228:231], v[216:219], v[80:87], v249\n" \
  "s_nop 0\n" \
  "v_add_f32 v224, v224, v226\n" \
  "v_smfmac_f32_16x16x64_bf16 v[228:231], v[220:223], v[88:95], v249\n" \
  "v_add_f32 v225, v225, v227\n" \
  "s_waitcnt lgkmcnt(0)\n" \
  "v_add_f32 v236, v236, v224\n" \
  "v_add_f32 v237, v237, v225\n" \
  "v_cvt_pk_bf16_f32 v236, v236, v237\n" \
  "s_nop 3\n" \
  "v_add_f32 v238, v238, v228\n" \
  "v_add_f32 v239, v239, v229\n" \
  "v_add_f32 v238, v238, v230\n" \
  "v_add_f32 v239, v239, v231\n" \
  "v_cvt_pk_bf16_f32 v237, v238, v239\n" \
  "ds_write_b64 v253, v[236:237] offset:17408\n" \
  "s_setprio 0\n" \
  "ds_read_b128 v[224:227], v254 offset:16384\n" \
  "ds_read_b128 v[228:231], v254 offset:17408\n" \
  "v_mov_b64 v[236:237], 0\n" \
  "v_mov_b64 v[238:239], 0\n" \
  "v_smfmac_f32_16x16x64_bf16 v[232:235], v[192:195], v[96:103], v249\n" \
  "v_smfmac_f32_16x16x64_bf16 v[236:239], v[192:195], v[144:151], v249\n" \
  "s_waitcnt lgkmcnt(2)\n" \
  "s_barrier\n" \
  "v_smfmac_f32_16x16x64_bf16 v[232:235], v[196:199], v[104:111], v249\n" \
  "v_smfmac_f32_16x16x64_bf16 v[236:239], v[196:199], v[152:159], v249\n" \
  "v_smfmac_f32_16x16x64_bf16 v[232:235], v[200:203], v[240:247], v249\n" \
  "ds_read_b128 v[192:195], v254 offset:32768\n" \
  "ds_read_b128 v[196:199], v254 offset:33792\n" \
  "s_waitcnt lgkmcnt(2)\n" \
  "v_smfmac_f32_16x16x64_bf16 v[236:239], v[200:203], v[224:231], v249\n" \
  "ds_read_b128 v[240:243], v254 offset:49152\n" \
  "ds_read_b128 v[244:247], v254 offset:50176\n" \
  "v_smfmac_f32_16x16x64_bf16 v[232:235], v[204:207], v[112:119], v249\n" \
  "v_smfmac_f32_16x16x64_bf16 v[236:239], v[204:207], v[160:167], v249\n" \
  "s_barrier\n" \
  "ds_read_b128 v[200:203], v248 offset:8192\n" \
  "v_smfmac_f32_16x16x64_bf16 v[232:235], v[212:215], v[120:127], v249\n" \
  "v_smfmac_f32_16x16x64_bf16 v[236:239], v[212:215], v[168:175], v249\n" \
  "v_smfmac_f32_16x16x64_bf16 v[232:235], v[216:219], v[128:135], v249\n" \
  "v_smfmac_f32_16x16x64_bf16 v[232:235], v[220:223], v[136:143], v249\n" \
  "s_waitcnt lgkmcnt(0)\n" \
  "v_mul_f32 v200, %[cs], v200\n" \
  "v_smfmac_f32_16x16x64_bf16 v[232:235], v[208:211], v[192:199], v249\n" \
  "v_mul_f32 v201, %[cs], v201\n" \
  "v_smfmac_f32_16x16x64_bf16 v[236:239], v[216:219], v[176:183], v249\n" \
  "v_mul_f32 v202, %[cs], v202\n" \
  "v_smfmac_f32_16x16x64_bf16 v[236:239], v[220:223], v[184:191], v249\n" \
  "v_mul_f32 v203, %[cs], v203\n" \
  "v_smfmac_f32_16x16x64_bf16 v[236:239], v[208:211], v[240:247], v249\n" \
  "v_fma_f32 v200, v232, %[cs], v200\n" \
  "v_fma_f32 v201, v233, %[cs], v201\n" \
  "v_fma_f32 v200, v234, %[cs], v200\n" \
  "v_fma_f32 v201, v235, %[cs], v201\n" \
  "v_cvt_pk_bf16_f32 v200, v200, v201\n" \
  "s_nop 3\n" \
  "v_fma_f32 v202, v236, %[cs], v202\n" \
  "v_fma_f32 v203, v237, %[cs], v203\n" \
  "v_fma_f32 v202, v238, %[cs], v202\n" \
  "v_fma_f32 v203, v239, %[cs], v203\n" \
  "v_cvt_pk_bf16_f32 v201, v202, v203\n" \
  "ds_write_b64 v253, v[200:201] offset:9216\n" \
  "s_waitcnt lgkmcnt(0)\n" \
  "s_barrier\n" \
  "s_waitcnt vmcnt(0)\n"
#define OW_F \
  "s_branch .Lgo_%=\n" \
  AL(A_OW_F) \
  ".Lbad_%=:\n" \
  "s_mov_b32 %[st], 1\n" \
  "s_branch .Lend_%=\n" \
  ".Lgo_%=:\n" \
  "v_mov_b64 v[96:97], 0\n" \
  "v_mov_b64 v[98:99], 0\n" \
  "s_mov_b32 %[st], 0\n" \
  "s_barrier\n" \
  "s_nop 15\n" \
  "s_nop 15\n" \
  "s_nop 12\n" \
  "ds_read_b32 v100, v118\n" \
  "buffer_load_dwordx4 v[0:3], v112, %[rs], %[sv1] offen offset:0 sc1\n" \
  "buffer_load_dwordx4 v[4:7], v112, %[rs], %[sv1] offen offset:1024 sc1\n" \
  "buffer_load_dwordx4 v[8:11], v112, %[rs], %[sv1] offen offset:2048 sc1\n" \
  "buffer_load_dwordx4 v[12:15], v112, %[rs], %[sv1] offen offset:3072 sc1\n" \
  "s_memtime vcc\n" \
  "s_waitcnt lgkmcnt(0)\n" \
  "v_readfirstlane_b32 %[cnt], v100\n" \
  "s_nop 0\n" \
  "s_sub_i32 %[cnt], %[cnt], vcc_lo\n" \
  "s_max_i32 %[cnt], %[cnt], 0\n" \
  "s_min_i32 %[cnt], %[cnt], 2000\n" \
  ".Ltg_%=:\n" \
  "s_sub_u32 %[cnt], %[cnt], 28\n" \
  "s_cbranch_scc0 .Ltg_%=\n" \
  "buffer_load_dwordx4 v[16:19], v112, %[rs], %[sv2] offen offset:0 sc1\n" \
  "buffer_load_dwordx4 v[20:23], v112, %[rs], %[sv2] offen offset:1024 sc1\n" \
  "buffer_load_dwordx4 v[24:27], v112, %[rs], %[sv2] offen offset:2048 sc1\n" \
  "buffer_load_dwordx4 v[28:31], v112, %[rs], %[sv2] offen offset:3072 sc1\n" \
  "s_nop 3\n" \
  "buffer_load_dwordx4 v[64:67], v113, %[rs], %[sp1] offen offset:0 sc1\n" \
  "buffer_load_dwordx4 v[68:71], v113, %[rs], %[sp1] offen offset:2048 sc1\n" \
  "buffer_load_dwordx4 v[72:75], v113, %[rs], %[sp2] offen offset:0 sc1\n" \
  "buffer_load_dwordx4 v[76:79], v113, %[rs], %[sp2] offen offset:2048 sc1\n" \
  "buffer_load_dwordx4 v[80:83], v113, %[rs], %[sp1] offen offset:64 sc1\n" \
  "buffer_load_dwordx4 v[84:87], v113, %[rs], %[sp1] offen offset:2112 sc1\n" \
  "buffer_load_dwordx4 v[88:91], v113, %[rs], %[sp2] offen offset:64 sc1\n" \
  "buffer_load_dwordx4 v[92:95], v113, %[rs], %[sp2] offen offset:2112 sc1\n" \
  "s_waitcnt vmcnt(15)\n" \
  "v_permlane16_swap_b32 v0, v1\n" \
  "v_permlane16_swap_b32 v2, v3\n" \
  "s_waitcnt vmcnt(14)\n" \
  "v_permlane16_swap_b32 v4, v5\n" \
  "v_permlane16_swap_b32 v6, v7\n" \
  "s_waitcnt vmcnt(13)\n" \
  "v_permlane16_swap_b32 v8, v9\n" \
  "v_permlane16_swap_b32 v10, v11\n" \
  "s_waitcnt vmcnt(12)\n" \
  "v_permlane16_swap_b32 v12, v13\n" \
  "v_permlane16_swap_b32 v14, v15\n" \
  "s_waitcnt vmcnt(11)\n" \
  "v_permlane16_swap_b32 v16, v17\n" \
  "v_permlane16_swap_b32 v18, v19\n" \
  "s_waitcnt vmcnt(10)\n" \
  "v_permlane16_swap_b32 v20, v21\n" \
  "v_permlane16_swap_b32 v22, v23\n" \
  "s_waitcnt vmcnt(9)\n" \
  "v_permlane16_swap_b32 v24, v25\n" \
  "v_permlane16_swap_b32 v26, v27\n" \
  "s_waitcnt vmcnt(8)\n" \
  "v_permlane16_swap_b32 v28, v29\n" \
  "v_permlane16_swap_b32 v30, v31\n" \
  "s_barrier\n" \
  "ds_read_b128 v[32:35], v114 offset:0\n" \
  "ds_read_b128 v[36:39], v114 offset:1024\n" \
  "ds_read_b128 v[40:43], v114 offset:2048\n" \
  "ds_read_b128 v[44:47], v114 offset:3072\n" \
  "ds_read_b128 v[48:51], v119 offset:0\n" \
  "ds_read_b128 v[52:55], v119 offset:1024\n" \
  "ds_read_b128 v[56:59], v119 offset:2048\n" \
  "ds_read_b128 v[60:63], v119 offset:3072\n" \
  "s_waitcnt vmcnt(7)\n" \
  "v_smfmac_f32_16x16x64_bf16 v[96:99], v[64:67], v[0:7], v117\n" \
  "s_waitcnt vmcnt(6)\n" \
  "v_smfmac_f32_16x16x64_bf16 v[96:99], v[68:71], v[8:15], v117\n" \
  "s_waitcnt vmcnt(5)\n" \
  "v_smfmac_f32_16x16x64_bf16 v[96:99], v[72:75], v[16:23], v117\n" \
  "s_waitcnt vmcnt(4)\n" \
  "v_smfmac_f32_16x16x64_bf16 v[96:99], v[76:79], v[24:31], v117\n" \
  "s_waitcnt vmcnt(3) lgkmcnt(6)\n" \
  "v_smfmac_f32_16x16x64_bf16 v[96:99], v[80:83], v[32:39], v117 abid:1\n" \
  "s_waitcnt vmcnt(2) lgkmcnt(4)\n" \
  "v_smfmac_f32_16x16x64_bf16 v[96:99], v[84:87], v[40:47], v117 abid:1\n" \
  "s_waitcnt vmcnt(1) lgkmcnt(2)\n" \
  "v_smfmac_f32_16x16x64_bf16 v[96:99], v[88:91], v[48:55], v117 abid:1\n" \
  "s_waitcnt vmcnt(0) lgkmcnt(0)\n" \
  "v_smfmac_f32_16x16x64_bf16 v[96:99], v[92:95], v[56:63], v117 abid:1\n" \
  "s_nop 8\n" \
  "v_permlane32_swap_b32 v96, v98\n" \
  "v_permlane32_swap_b32 v97, v99\n" \
  "v_add_f32 v96, v96, v98\n" \
  "v_add_f32 v97, v97, v99\n" \
  "v_cvt_pk_bf16_f32 v100, v96, v97\n" \
  "v_cmp_u_f32 vcc, v96, v97\n" \
  "s_nop 0\n" \
  "v_mov_b32_dpp v101, v100 quad_perm:[1,0,3,2] row_mask:0xf bank_mask:0xf\n" \
  "v_perm_b32 v100, v101, v100, v116\n" \
  "s_cbranch_vccnz .Lbad_%=\n" \
  "buffer_store_dword v100, v115, %[rs], %[sm0] offen sc1\n" \
  "buffer_store_dword v100, v115, %[rs], %[sm1] offen sc1\n" \
  "buffer_store_dword v100, v115, %[rs], %[sm2] offen sc1\n" \
  "buffer_store_dword v100, v115, %[rs], %[sm3] offen sc1\n" \
  ".Lend_%=:\n" \
  "s_memtime %[tp]\n" \
  "v_permlane16_swap_b32 v0, v1\n" \
  "v_permlane16_swap_b32 v2, v3\n" \
  "v_permlane16_swap_b32 v4, v5\n" \
  "v_permlane16_swap_b32 v6, v7\n" \
  "v_permlane16_swap_b32 v8, v9\n" \
  "v_permlane16_swap_b32 v10, v11\n" \
  "v_permlane16_swap_b32 v12, v13\n" \
  "v_permlane16_swap_b32 v14, v15\n" \
  "v_permlane16_swap_b32 v16, v17\n" \
  "v_permlane16_swap_b32 v18, v19\n" \
  "v_permlane16_swap_b32 v20, v21\n" \
  "v_permlane16_swap_b32 v22, v23\n" \
  "v_permlane16_swap_b32 v24, v25\n" \
  "v_permlane16_swap_b32 v26, v27\n" \
  "v_permlane16_swap_b32 v28, v29\n" \
  "v_permlane16_swap_b32 v30, v31\n" \
  "s_waitcnt lgkmcnt(0)\n"
#define ST_Y \
  "s_mov_b32 %[keep], m0\n" \
  "ds_read_b128 v[240:243], v250 offset:0\n" \
  "ds_read_b128 v[244:247], v250 offset:1024\n" \
  "v_mfma_f32_16x16x32_bf16 v[192:195], v[0:3], v[208:211], 0\n" \
  "v_mfma_f32_16x16x32_bf16 v[196:199], v[64:67], v[208:211], 0\n" \
  "ds_read_b128 v[208:211], v252 offset:8192\n" \
  "v_mfma_f32_16x16x32_bf16 v[200:203], v[0:3], v[212:215], 0\n" \
  "v_mfma_f32_16x16x32_bf16 v[204:207], v[64:67], v[212:215], 0\n" \
  "ds_read_b128 v[212:215], v252 offset:9216\n" \
  "v_mfma_f32_16x16x32_bf16 v[192:195], v[128:131], v[216:219], v[192:195]\n" \
  "v_mfma_f32_16x16x32_bf16 v[196:199], v[132:135], v[216:219], v[196:199]\n" \
  "ds_read_b128 v[216:219], v252 offset:10240\n" \
  "v_mfma_f32_16x16x32_bf16 v[200:203], v[128:131], v[220:223], v[200:203]\n" \
  "v_mfma_f32_16x16x32_bf16 v[204:207], v[132:135], v[220:223], v[204:207]\n" \
  "ds_read_b128 v[220:223], v252 offset:11264\n" \
  "v_mfma_f32_16x16x32_bf16 v[192:195], v[4:7], v[224:227], v[192:195]\n" \
  "v_mfma_f32_16x16x32_bf16 v[196:199], v[68:71], v[224:227], v[196:199]\n" \
  "ds_read_b128 v[224:227], v252 offset:12288\n" \
  "v_mfma_f32_16x16x32_bf16 v[200:203], v[4:7], v[228:231], v[200:203]\n" \
  "v_mfma_f32_16x16x32_bf16 v[204:207], v[68:71], v[228:231], v[204:207]\n" \
  "ds_read_b128 v[228:231], v252 offset:13312\n" \
  "s_waitcnt lgkmcnt(7)\n" \
  "v_mfma_f32_16x16x32_bf16 v[192:195], v[240:243], v[232:235], v[192:195]\n" \
  "s_waitcnt lgkmcnt(6)\n" \
  "v_mfma_f32_16x16x32_bf16 v[196:199], v[244:247], v[232:235], v[196:199]\n" \
  "ds_read_b128 v[232:235], v252 offset:14336\n" \
  "v_mfma_f32_16x16x32_bf16 v[200:203], v[240:243], v[236:239], v[200:203]\n" \
  "v_mfma_f32_16x16x32_bf16 v[204:207], v[244:247], v[236:239], v[204:207]\n" \
  "ds_read_b128 v[236:239], v252 offset:15360\n" \
  "ds_read_b128 v[240:243], v250 offset:16384\n" \
  "ds_read_b128 v[244:247], v250 offset:17408\n" \
  "s_waitcnt lgkmcnt(9)\n" \
  "v_mfma_f32_16x16x32_bf16 v[192:195], v[8:11], v[208:211], v[192:195]\n" \
  "v_mfma_f32_16x16x32_bf16 v[196:199], v[72:75], v[208:211], v[196:199]\n" \
  "s_waitcnt lgkmcnt(8)\n" \
  "v_mfma_f32_16x16x32_bf16 v[200:203], v[8:11], v[212:215], v[200:203]\n" \
  "v_mfma_f32_16x16x32_bf16 v[204:207], v[72:75], v[212:215], v[204:207]\n" \
  "s_waitcnt vmcnt(2)\n" \
  "s_waitcnt lgkmcnt(0)\n" \
  "s_barrier\n" \
  "ds_read_b128 v[208:211], v252 offset:16384\n" \
  "ds_read_b128 v[212:215], v252 offset:17408\n" \
  "s_add_u32 m0, %[mw], 0\n" \
  "s_xor_b32 %[cnt], %[sc], 512\n" \
  "buffer_load_dwordx4 v248, %[rx], %[cnt] offen lds\n" \
  "s_add_u32 m0, %[mw], 8192\n" \
  "s_xor_b32 %[cnt], %[sc], 640\n" \
  "buffer_load_dwordx4 v248, %[rx], %[cnt] offen lds\n" \
  "v_mfma_f32_16x16x32_bf16 v[192:195], v[136:139], v[216:219], v[192:195]\n" \
  "v_mfma_f32_16x16x32_bf16 v[196:199], v[140:143], v[216:219], v[196:199]\n" \
  "ds_read_b128 v[216:219], v252 offset:18432\n" \
  "v_mfma_f32_16x16x32_bf16 v[200:203], v[136:139], v[220:223], v[200:203]\n" \
  "v_mfma_f32_16x16x32_bf16 v[204:207], v[140:143], v[220:223], v[204:207]\n" \
  "ds_read_b128 v[220:223], v252 offset:19456\n" \
  "v_mfma_f32_16x16x32_bf16 v[192:195], v[12:15], v[224:227], v[192:195]\n" \
  "v_mfma_f32_16x16x32_bf16 v[196:199], v[76:79], v[224:227], v[196:199]\n" \
  "ds_read_b128 v[224:227], v252 offset:20480\n" \
  "v_mfma_f32_16x16x32_bf16 v[200:203], v[12:15], v[228:231], v[200:203]\n" \
  "v_mfma_f32_16x16x32_bf16 v[204:207], v[76:79], v[228:231], v[204:207]\n" \
  "ds_read_b128 v[228:231], v252 offset:21504\n" \
  "v_mfma_f32_16x16x32_bf16 v[192:195], v[240:243], v[232:235], v[192:195]\n" \
  "v_mfma_f32_16x16x32_bf16 v[196:199], v[244:247], v[232:235], v[196:199]\n" \
  "ds_read_b128 v[232:235], v252 offset:22528\n" \
  "v_mfma_f32_16x16x32_bf16 v[200:203], v[240:243], v[236:239], v[200:203]\n" \
  "v_mfma_f32_16x16x32_bf16 v[204:207], v[244:247], v[236:239], v[204:207]\n" \
  "ds_read_b128 v[236:239], v252 offset:23552\n" \
  "ds_read_b128 v[240:243], v250 offset:32768\n" \
  "ds_read_b128 v[244:247], v250 offset:33792\n" \
  "s_waitcnt lgkmcnt(9)\n" \
  "v_mfma_f32_16x16x32_bf16 v[192:195], v[16:19], v[208:211], v[192:195]\n" \
  "v_mfma_f32_16x16x32_bf16 v[196:199], v[80:83], v[208:211], v[196:199]\n" \
  "ds_read_b128 v[208:211], v252 offset:24576\n" \
  "s_waitcnt lgkmcnt(9)\n" \
  "v_mfma_f32_16x16x32_bf16 v[200:203], v[16:19], v[212:215], v[200:203]\n" \
  "v_mfma_f32_16x16x32_bf16 v[204:207], v[80:83], v[212:215], v[204:207]\n" \
  "ds_read_b128 v[212:215], v252 offset:25600\n" \
  "s_waitcnt lgkmcnt(9)\n" \
  "v_mfma_f32_16x16x32_bf16 v[192:195], v[144:147], v[216:219], v[192:195]\n" \
  "v_mfma_f32_16x16x32_bf16 v[196:199], v[148:151], v[216:219], v[196:199]\n" \
  "ds_read_b128 v[216:219], v252 offset:26624\n" \
  "s_waitcnt lgkmcnt(9)\n" \
  "v_mfma_f32_16x16x32_bf16 v[200:203], v[144:147], v[220:223], v[200:203]\n" \
  "v_mfma_f32_16x16x32_bf16 v[204:207], v[148:151], v[220:223], v[204:207]\n" \
  "ds_read_b128 v[220:223], v252 offset:27648\n" \
  "s_waitcnt lgkmcnt(9)\n" \
  "v_mfma_f32_16x16x32_bf16 v[192:195], v[20:23], v[224:227], v[192:195]\n" \
  "v_mfma_f32_16x16x32_bf16 v[196:199], v[84:87], v[224:227], v[196:199]\n" \
  "ds_read_b128 v[224:227], v252 offset:28672\n" \
  "s_waitcnt lgkmcnt(9)\n" \
  "v_mfma_f32_16x16x32_bf16 v[200:203], v[20:23], v[228:231], v[200:203]\n" \
  "v_mfma_f32_16x16x32_bf16 v[204:207], v[84:87], v[228:231], v[204:207]\n" \
  "ds_read_b128 v[228:231], v252 offset:29696\n" \
  "s_waitcnt lgkmcnt(7)\n" \
  "v_mfma_f32_16x16x32_bf16 v[192:195], v[240:243], v[232:235], v[192:195]\n" \
  "s_waitcnt lgkmcnt(6)\n" \
  "v_mfma_f32_16x16x32_bf16 v[196:199], v[244:247], v[232:235], v[196:199]\n" \
  "ds_read_b128 v[232:235], v252 offset:30720\n" \
  "v_mfma_f32_16x16x32_bf16 v[200:203], v[240:243], v[236:239], v[200:203]\n" \
  "v_mfma_f32_16x16x32_bf16 v[204:207], v[244:247], v[236:239], v[204:207]\n" \
  "ds_read_b128 v[236:239], v252 offset:31744\n" \
  "ds_read_b128 v[240:243], v250 offset:49152\n" \
  "ds_read_b128 v[244:247], v250 offset:50176\n" \
  "s_waitcnt lgkmcnt(9)\n" \
  "v_mfma_f32_16x16x32_bf16 v[192:195], v[24:27], v[208:211], v[192:195]\n" \
  "v_mfma_f32_16x16x32_bf16 v[196:199], v[88:91], v[208:211], v[196:199]\n" \
  "s_waitcnt lgkmcnt(8)\n" \
  "v_mfma_f32_16x16x32_bf16 v[200:203], v[24:27], v[212:215], v[200:203]\n" \
  "v_mfma_f32_16x16x32_bf16 v[204:207], v[88:91], v[212:215], v[204:207]\n" \
  "s_waitcnt vmcnt(0)\n" \
  "s_waitcnt lgkmcnt(0)\n" \
  "s_barrier\n" \
  "ds_read_b128 v[208:211], v252 offset:0\n" \
  "ds_read_b128 v[212:215], v252 offset:1024\n" \
  "s_add_u32 m0, %[mw], 16384\n" \
  "s_xor_b32 %[cnt], %[sc], 768\n" \
  "buffer_load_dwordx4 v248, %[rx], %[cnt] offen lds\n" \
  "s_add_u32 m0, %[mw], 24576\n" \
  "s_xor_b32 %[cnt], %[sc], 896\n" \
  "buffer_load_dwordx4 v248, %[rx], %[cnt] offen lds\n" \
  "v_lshl_or_b32 v253, v251, 24, %[fm]\n" \
  "buffer_store_dword v253, v253, %[rs], %[fo] offen sc1\n" \
  "v_mfma_f32_16x16x32_bf16 v[192:195], v[152:155], v[216:219], v[192:195]\n" \
  "v_mfma_f32_16x16x32_bf16 v[196:199], v[156:159], v[216:219], v[196:199]\n" \
  "ds_read_b128 v[216:219], v252 offset:2048\n" \
  "v_mfma_f32_16x16x32_bf16 v[200:203], v[152:155], v[220:223], v[200:203]\n" \
  "v_mfma_f32_16x16x32_bf16 v[204:207], v[156:159], v[220:223], v[204:207]\n" \
  "ds_read_b128 v[220:223], v252 offset:3072\n" \
  "v_mfma_f32_16x16x32_bf16 v[192:195], v[28:31], v[224:227], v[192:195]\n" \
  "v_mfma_f32_16x16x32_bf16 v[196:199], v[92:95], v[224:227], v[196:199]\n" \
  "ds_read_b128 v[224:227], v252 offset:4096\n" \
  "v_mfma_f32_16x16x32_bf16 v[200:203], v[28:31], v[228:231], v[200:203]\n" \
  "v_mfma_f32_16x16x32_bf16 v[204:207], v[92:95], v[228:231], v[204:207]\n" \
  "ds_read_b128 v[228:231], v252 offset:5120\n" \
  "v_mfma_f32_16x16x32_bf16 v[192:195], v[240:243], v[232:235], v[192:195]\n" \
  "v_mfma_f32_16x16x32_bf16 v[196:199], v[244:247], v[232:235], v[196:199]\n" \
  "ds_read_b128 v[232:235], v252 offset:6144\n" \
  "v_mfma_f32_16x16x32_bf16 v[200:203], v[240:243], v[236:239], v[200:203]\n" \
  "v_mfma_f32_16x16x32_bf16 v[204:207], v[244:247], v[236:239], v[204:207]\n" \
  "ds_read_b128 v[236:239], v252 offset:7168\n" \
  "ds_read_b128 v[240:243], v249 offset:0\n" \
  "ds_read_b128 v[244:247], v249 offset:1024\n" \
  "s_waitcnt lgkmcnt(9)\n" \
  "v_mfma_f32_16x16x32_bf16 v[192:195], v[32:35], v[208:211], v[192:195]\n" \
  "v_mfma_f32_16x16x32_bf16 v[196:199], v[96:99], v[208:211], v[196:199]\n" \
  "ds_read_b128 v[208:211], v252 offset:8192\n" \
  "s_waitcnt lgkmcnt(9)\n" \
  "v_mfma_f32_16x16x32_bf16 v[200:203], v[32:35], v[212:215], v[200:203]\n" \
  "v_mfma_f32_16x16x32_bf16 v[204:207], v[96:99], v[212:215], v[204:207]\n" \
  "ds_read_b128 v[212:215], v252 offset:9216\n" \
  "s_waitcnt lgkmcnt(9)\n" \
  "v_mfma_f32_16x16x32_bf16 v[192:195], v[160:163], v[216:219], v[192:195]\n" \
  "v_mfma_f32_16x16x32_bf16 v[196:199], v[164:167], v[216:219], v[196:199]\n" \
  "ds_read_b128 v[216:219], v252 offset:10240\n" \
  "s_waitcnt lgkmcnt(9)\n" \
  "v_mfma_f32_16x16x32_bf16 v[200:203], v[160:163], v[220:223], v[200:203]\n" \
  "v_mfma_f32_16x16x32_bf16 v[204:207], v[164:167], v[220:223], v[204:207]\n" \
  "ds_read_b128 v[220:223], v252 offset:11264\n" \
  "s_waitcnt lgkmcnt(9)\n" \
  "v_mfma_f32_16x16x32_bf16 v[192:195], v[36:39], v[224:227], v[192:195]\n" \
  "v_mfma_f32_16x16x32_bf16 v[196:199], v[100:103], v[224:227], v[196:199]\n" \
  "ds_read_b128 v[224:227], v252 offset:12288\n" \
  "s_waitcnt lgkmcnt(9)\n" \
  "v_mfma_f32_16x16x32_bf16 v[200:203], v[36:39], v[228:231], v[200:203]\n" \
  "v_mfma_f32_16x16x32_bf16 v[204:207], v[100:103], v[228:231], v[204:207]\n" \
  "ds_read_b128 v[228:231], v252 offset:13312\n" \
  "s_waitcnt lgkmcnt(7)\n" \
  "v_mfma_f32_16x16x32_bf16 v[192:195], v[240:243], v[232:235], v[192:195]\n" \
  "s_waitcnt lgkmcnt(6)\n" \
  "v_mfma_f32_16x16x32_bf16 v[196:199], v[244:247], v[232:235], v[196:199]\n" \
  "ds_read_b128 v[232:235], v252 offset:14336\n" \
  "v_mfma_f32_16x16x32_bf16 v[200:203], v[240:243], v[236:239], v[200:203]\n" \
  "v_mfma_f32_16x16x32_bf16 v[204:207], v[244:247], v[236:239], v[204:207]\n" \
  "ds_read_b128 v[236:239], v252 offset:15360\n" \
  "ds_read_b128 v[240:243], v249 offset:16384\n" \
  "ds_read_b128 v[244:247], v249 offset:17408\n" \
  "s_waitcnt lgkmcnt(9)\n" \
  "v_mfma_f32_16x16x32_bf16 v[192:195], v[40:43], v[208:211], v[192:195]\n" \
  "v_mfma_f32_16x16x32_bf16 v[196:199], v[104:107], v[208:211], v[196:199]\n" \
  "s_waitcnt lgkmcnt(8)\n" \
  "v_mfma_f32_16x16x32_bf16 v[200:203], v[40:43], v[212:215], v[200:203]\n" \
  "v_mfma_f32_16x16x32_bf16 v[204:207], v[104:107], v[212:215], v[204:207]\n" \
  "s_waitcnt vmcnt(1)\n" \
  "s_waitcnt lgkmcnt(0)\n" \
  "s_barrier\n" \
  "ds_read_b128 v[208:211], v252 offset:16384\n" \
  "ds_read_b128 v[212:215], v252 offset:17408\n" \
  "s_add_u32 m0, %[mw], 0\n" \
  "s_xor_b32 %[cnt], %[sn], 0\n" \
  "buffer_load_dwordx4 v248, %[rx], %[cnt] offen lds\n" \
  "s_add_u32 m0, %[mw], 8192\n" \
  "s_xor_b32 %[cnt], %[sn], 128\n" \
  "buffer_load_dwordx4 v248, %[rx], %[cnt] offen lds\n" \
  "v_lshl_or_b32 v253, v251, 3, %[tm]\n" \
  "buffer_load_dword v253, v253, %[rx], %[tc] offen\n" \
  "v_mfma_f32_16x16x32_bf16 v[192:195], v[168:171], v[216:219], v[192:195]\n" \
  "v_mfma_f32_16x16x32_bf16 v[196:199], v[172:175], v[216:219], v[196:199]\n" \
  "ds_read_b128 v[216:219], v252 offset:18432\n" \
  "v_mfma_f32_16x16x32_bf16 v[200:203], v[168:171], v[220:223], v[200:203]\n" \
  "v_mfma_f32_16x16x32_bf16 v[204:207], v[172:175], v[220:223], v[204:207]\n" \
  "ds_read_b128 v[220:223], v252 offset:19456\n" \
  "v_mfma_f32_16x16x32_bf16 v[192:195], v[44:47], v[224:227], v[192:195]\n" \
  "v_mfma_f32_16x16x32_bf16 v[196:199], v[108:111], v[224:227], v[196:199]\n" \
  "ds_read_b128 v[224:227], v252 offset:20480\n" \
  "v_mfma_f32_16x16x32_bf16 v[200:203], v[44:47], v[228:231], v[200:203]\n" \
  "v_mfma_f32_16x16x32_bf16 v[204:207], v[108:111], v[228:231], v[204:207]\n" \
  "ds_read_b128 v[228:231], v252 offset:21504\n" \
  "v_mfma_f32_16x16x32_bf16 v[192:195], v[240:243], v[232:235], v[192:195]\n" \
  "v_mfma_f32_16x16x32_bf16 v[196:199], v[244:247], v[232:235], v[196:199]\n" \
  "ds_read_b128 v[232:235], v252 offset:22528\n" \
  "v_mfma_f32_16x16x32_bf16 v[200:203], v[240:243], v[236:239], v[200:203]\n" \
  "v_mfma_f32_16x16x32_bf16 v[204:207], v[244:247], v[236:239], v[204:207]\n" \
  "ds_read_b128 v[236:239], v252 offset:23552\n" \
  "ds_read_b128 v[240:243], v249 offset:32768\n" \
  "ds_read_b128 v[244:247], v249 offset:33792\n" \
  "s_waitcnt lgkmcnt(9)\n" \
  "v_mfma_f32_16x16x32_bf16 v[192:195], v[48:51], v[208:211], v[192:195]\n" \
  "v_mfma_f32_16x16x32_bf16 v[196:199], v[112:115], v[208:211], v[196:199]\n" \
  "ds_read_b128 v[208:211], v252 offset:24576\n" \
  "s_waitcnt lgkmcnt(9)\n" \
  "v_mfma_f32_16x16x32_bf16 v[200:203], v[48:51], v[212:215], v[200:203]\n" \
  "v_mfma_f32_16x16x32_bf16 v[204:207], v[112:115], v[212:215], v[204:207]\n" \
  "ds_read_b128 v[212:215], v252 offset:25600\n" \
  "s_waitcnt lgkmcnt(9)\n" \
  "v_mfma_f32_16x16x32_bf16 v[192:195], v[176:179], v[216:219], v[192:195]\n" \
  "v_mfma_f32_16x16x32_bf16 v[196:199], v[180:183], v[216:219], v[196:199]\n" \
  "ds_read_b128 v[216:219], v252 offset:26624\n" \
  "s_waitcnt lgkmcnt(9)\n" \
  "v_mfma_f32_16x16x32_bf16 v[200:203], v[176:179], v[220:223], v[200:203]\n" \
  "v_mfma_f32_16x16x32_bf16 v[204:207], v[180:183], v[220:223], v[204:207]\n" \
  "ds_read_b128 v[220:223], v252 offset:27648\n" \
  "s_waitcnt lgkmcnt(9)\n" \
  "v_mfma_f32_16x16x32_bf16 v[192:195], v[52:55], v[224:227], v[192:195]\n" \
  "v_mfma_f32_16x16x32_bf16 v[196:199], v[116:119], v[224:227], v[196:199]\n" \
  "ds_read_b128 v[224:227], v252 offset:28672\n" \
  "s_waitcnt lgkmcnt(9)\n" \
  "v_mfma_f32_16x16x32_bf16 v[200:203], v[52:55], v[228:231], v[200:203]\n" \
  "v_mfma_f32_16x16x32_bf16 v[204:207], v[116:119], v[228:231], v[204:207]\n" \
  "ds_read_b128 v[228:231], v252 offset:29696\n" \
  "s_waitcnt lgkmcnt(7)\n" \
  "v_mfma_f32_16x16x32_bf16 v[192:195], v[240:243], v[232:235], v[192:195]\n" \
  "s_waitcnt lgkmcnt(6)\n" \
  "v_mfma_f32_16x16x32_bf16 v[196:199], v[244:247], v[232:235], v[196:199]\n" \
  "ds_read_b128 v[232:235], v252 offset:30720\n" \
  "v_mfma_f32_16x16x32_bf16 v[200:203], v[240:243], v[236:239], v[200:203]\n" \
  "v_mfma_f32_16x16x32_bf16 v[204:207], v[244:247], v[236:239], v[204:207]\n" \
  "ds_read_b128 v[236:239], v252 offset:31744\n" \
  "ds_read_b128 v[240:243], v249 offset:49152\n" \
  "ds_read_b128 v[244:247], v249 offset:50176\n" \
  "s_waitcnt lgkmcnt(9)\n" \
  "v_mfma_f32_16x16x32_bf16 v[192:195], v[56:59], v[208:211], v[192:195]\n" \
  "v_mfma_f32_16x16x32_bf16 v[196:199], v[120:123], v[208:211], v[196:199]\n" \
  "s_waitcnt lgkmcnt(8)\n" \
  "v_mfma_f32_16x16x32_bf16 v[200:203], v[56:59], v[212:215], v[200:203]\n" \
  "v_mfma_f32_16x16x32_bf16 v[204:207], v[120:123], v[212:215], v[204:207]\n" \
  "s_waitcnt vmcnt(1)\n" \
  "s_waitcnt lgkmcnt(0)\n" \
  "s_barrier\n" \
  "ds_read_b128 v[208:211], v252 offset:0\n" \
  "ds_read_b128 v[212:215], v252 offset:1024\n" \
  "s_add_u32 m0, %[mw], 16384\n" \
  "s_xor_b32 %[cnt], %[sn], 256\n" \
  "buffer_load_dwordx4 v248, %[rx], %[cnt] offen lds\n" \
  "s_add_u32 m0, %[mw], 24576\n" \
  "s_xor_b32 %[cnt], %[sn], 384\n" \
  "buffer_load_dwordx4 v248, %[rx], %[cnt] offen lds\n" \
  "v_mfma_f32_16x16x32_bf16 v[192:195], v[184:187], v[216:219], v[192:195]\n" \
  "v_mfma_f32_16x16x32_bf16 v[196:199], v[188:191], v[216:219], v[196:199]\n" \
  "ds_read_b128 v[216:219], v252 offset:2048\n" \
  "v_mfma_f32_16x16x32_bf16 v[200:203], v[184:187], v[220:223], v[200:203]\n" \
  "v_mfma_f32_16x16x32_bf16 v[204:207], v[188:191], v[220:223], v[204:207]\n" \
  "ds_read_b128 v[220:223], v252 offset:3072\n" \
  "v_mfma_f32_16x16x32_bf16 v[192:195], v[60:63], v[224:227], v[192:195]\n" \
  "v_mfma_f32_16x16x32_bf16 v[196:199], v[124:127], v[224:227], v[196:199]\n" \
  "ds_read_b128 v[224:227], v252 offset:4096\n" \
  "v_mfma_f32_16x16x32_bf16 v[200:203], v[60:63], v[228:231], v[200:203]\n" \
  "v_mfma_f32_16x16x32_bf16 v[204:207], v[124:127], v[228:231], v[204:207]\n" \
  "ds_read_b128 v[228:231], v252 offset:5120\n" \
  "v_mfma_f32_16x16x32_bf16 v[192:195], v[240:243], v[232:235], v[192:195]\n" \
  "v_mfma_f32_16x16x32_bf16 v[196:199], v[244:247], v[232:235], v[196:199]\n" \
  "ds_read_b128 v[232:235], v252 offset:6144\n" \
  "v_mfma_f32_16x16x32_bf16 v[200:203], v[240:243], v[236:239], v[200:203]\n" \
  "v_mfma_f32_16x16x32_bf16 v[204:207], v[244:247], v[236:239], v[204:207]\n" \
  "ds_read_b128 v[236:239], v252 offset:7168\n" \
  "s_cmp_eq_u32 %[cs], 0\n" \
  "s_cbranch_scc1 .Lq_%=\n" \
  "v_mul_f32 v192, %[cs], v192\n" \
  "v_mul_f32 v193, %[cs], v193\n" \
  "v_mul_f32 v194, %[cs], v194\n" \
  "v_mul_f32 v195, %[cs], v195\n" \
  "v_mul_f32 v196, %[cs], v196\n" \
  "v_mul_f32 v197, %[cs], v197\n" \
  "v_mul_f32 v198, %[cs], v198\n" \
  "v_mul_f32 v199, %[cs], v199\n" \
  "v_cvt_pk_bf16_f32 v192, v192, v193\n" \
  "v_cvt_pk_bf16_f32 v193, v196, v197\n" \
  "v_cvt_pk_bf16_f32 v194, v194, v195\n" \
  "v_cvt_pk_bf16_f32 v195, v198, v199\n" \
  "buffer_store_dwordx4 v[192:195], v251, %[rs], %[so] offen sc1\n" \
  "v_mul_f32 v200, %[cs], v200\n" \
  "v_mul_f32 v201, %[cs], v201\n" \
  "v_mul_f32 v202, %[cs], v202\n" \
  "v_mul_f32 v203, %[cs], v203\n" \
  "v_mul_f32 v204, %[cs], v204\n" \
  "v_mul_f32 v205, %[cs], v205\n" \
  "v_mul_f32 v206, %[cs], v206\n" \
  "v_mul_f32 v207, %[cs], v207\n" \
  "v_cvt_pk_bf16_f32 v200, v200, v201\n" \
  "v_cvt_pk_bf16_f32 v201, v204, v205\n" \
  "v_cvt_pk_bf16_f32 v202, v202, v203\n" \
  "v_cvt_pk_bf16_f32 v203, v206, v207\n" \
  "s_add_u32 %[cnt], %[so], %c[rt]\n" \
  "buffer_store_dwordx4 v[200:203], v251, %[rs], %[cnt] offen sc1\n" \
  "s_branch .Lqe_%=\n" \
  ".Lq_%=:\n" \
  "v_cvt_pk_bf16_f32 v192, v192, v193\n" \
  "v_cvt_pk_bf16_f32 v193, v196, v197\n" \
  "v_cvt_pk_bf16_f32 v194, v194, v195\n" \
  "v_cvt_pk_bf16_f32 v195, v198, v199\n" \
  "buffer_store_dwordx4 v[192:195], v251, %[rs], %[so] offen sc1\n" \
  "v_cvt_pk_bf16_f32 v200, v200, v201\n" \
  "v_cvt_pk_bf16_f32 v201, v204, v205\n" \
  "v_cvt_pk_bf16_f32 v202, v202, v203\n" \
  "v_cvt_pk_bf16_f32 v203, v206, v207\n" \
  "s_add_u32 %[cnt], %[so], %c[rt]\n" \
  "buffer_store_dwordx4 v[200:203], v251, %[rs], %[cnt] offen sc1\n" \
  ".Lqe_%=:\n" \
  "s_mov_b32 m0, %[keep]\n" \
  "s_waitcnt lgkmcnt(0)\n"
#define ST_XA \
  "s_mov_b32 %[keep], m0\n" \
  "ds_read_b128 v[240:243], v250 offset:0\n" \
  "ds_read_b128 v[244:247], v250 offset:1024\n" \
  "v_mfma_f32_16x16x32_bf16 v[192:195], v[0:3], v[208:211], 0\n" \
  "v_mfma_f32_16x16x32_bf16 v[196:199], v[64:67], v[208:211], 0\n" \
  "ds_read_b128 v[208:211], v252 offset:8192\n" \
  "v_mfma_f32_16x16x32_bf16 v[200:203], v[0:3], v[212:215], 0\n" \
  "v_mfma_f32_16x16x32_bf16 v[204:207], v[64:67], v[212:215], 0\n" \
  "ds_read_b128 v[212:215], v252 offset:9216\n" \
  "v_mfma_f32_16x16x32_bf16 v[192:195], v[128:131], v[216:219], v[192:195]\n" \
  "v_mfma_f32_16x16x32_bf16 v[196:199], v[132:135], v[216:219], v[196:199]\n" \
  "ds_read_b128 v[216:219], v252 offset:10240\n" \
  "v_mfma_f32_16x16x32_bf16 v[200:203], v[128:131], v[220:223], v[200:203]\n" \
  "v_mfma_f32_16x16x32_bf16 v[204:207], v[132:135], v[220:223], v[204:207]\n" \
  "ds_read_b128 v[220:223], v252 offset:11264\n" \
  "v_mfma_f32_16x16x32_bf16 v[192:195], v[4:7], v[224:227], v[192:195]\n" \
  "v_mfma_f32_16x16x32_bf16 v[196:199], v[68:71], v[224:227], v[196:199]\n" \
  "v_mfma_f32_16x16x32_bf16 v[200:203], v[4:7], v[228:231], v[200:203]\n" \
  "v_mfma_f32_16x16x32_bf16 v[204:207], v[68:71], v[228:231], v[204:207]\n" \
  "s_waitcnt vmcnt(0)\n" \
  "s_waitcnt lgkmcnt(0)\n" \
  "s_barrier\n" \
  "ds_read_b128 v[224:227], v252 offset:12288\n" \
  "ds_read_b128 v[228:231], v252 offset:13312\n" \
  "s_add_u32 m0, %[mw], 0\n" \
  "s_xor_b32 %[cnt], %[sc], 384\n" \
  "buffer_load_dwordx4 v248, %[rx], %[cnt] offen lds\n" \
  "s_add_u32 m0, %[mw], 4096\n" \
  "s_xor_b32 %[cnt], %[sc], 448\n" \
  "buffer_load_dwordx4 v248, %[rx], %[cnt] offen lds\n" \
  "v_mfma_f32_16x16x32_bf16 v[192:195], v[240:243], v[232:235], v[192:195]\n" \
  "v_mfma_f32_16x16x32_bf16 v[196:199], v[244:247], v[232:235], v[196:199]\n" \
  "ds_read_b128 v[232:235], v252 offset:14336\n" \
  "v_mfma_f32_16x16x32_bf16 v[200:203], v[240:243], v[236:239], v[200:203]\n" \
  "v_mfma_f32_16x16x32_bf16 v[204:207], v[244:247], v[236:239], v[204:207]\n" \
  "ds_read_b128 v[236:239], v252 offset:15360\n" \
  "ds_read_b128 v[240:243], v250 offset:16384\n" \
  "ds_read_b128 v[244:247], v250 offset:17408\n" \
  "v_mfma_f32_16x16x32_bf16 v[192:195], v[8:11], v[208:211], v[192:195]\n" \
  "v_mfma_f32_16x16x32_bf16 v[196:199], v[72:75], v[208:211], v[196:199]\n" \
  "ds_read_b128 v[208:211], v252 offset:16384\n" \
  "v_mfma_f32_16x16x32_bf16 v[200:203], v[8:11], v[212:215], v[200:203]\n" \
  "v_mfma_f32_16x16x32_bf16 v[204:207], v[72:75], v[212:215], v[204:207]\n" \
  "ds_read_b128 v[212:215], v252 offset:17408\n" \
  "v_mfma_f32_16x16x32_bf16 v[192:195], v[136:139], v[216:219], v[192:195]\n" \
  "v_mfma_f32_16x16x32_bf16 v[196:199], v[140:143], v[216:219], v[196:199]\n" \
  "ds_read_b128 v[216:219], v252 offset:18432\n" \
  "v_mfma_f32_16x16x32_bf16 v[200:203], v[136:139], v[220:223], v[200:203]\n" \
  "v_mfma_f32_16x16x32_bf16 v[204:207], v[140:143], v[220:223], v[204:207]\n" \
  "ds_read_b128 v[220:223], v252 offset:19456\n" \
  "s_waitcnt lgkmcnt(9)\n" \
  "v_mfma_f32_16x16x32_bf16 v[192:195], v[12:15], v[224:227], v[192:195]\n" \
  "v_mfma_f32_16x16x32_bf16 v[196:199], v[76:79], v[224:227], v[196:199]\n" \
  "ds_read_b128 v[224:227], v252 offset:20480\n" \
  "s_waitcnt lgkmcnt(9)\n" \
  "v_mfma_f32_16x16x32_bf16 v[200:203], v[12:15], v[228:231], v[200:203]\n" \
  "v_mfma_f32_16x16x32_bf16 v[204:207], v[76:79], v[228:231], v[204:207]\n" \
  "ds_read_b128 v[228:231], v252 offset:21504\n" \
  "s_waitcnt lgkmcnt(7)\n" \
  "v_mfma_f32_16x16x32_bf16 v[192:195], v[240:243], v[232:235], v[192:195]\n" \
  "s_waitcnt lgkmcnt(6)\n" \
  "v_mfma_f32_16x16x32_bf16 v[196:199], v[244:247], v[232:235], v[196:199]\n" \
  "ds_read_b128 v[232:235], v252 offset:22528\n" \
  "v_mfma_f32_16x16x32_bf16 v[200:203], v[240:243], v[236:239], v[200:203]\n" \
  "v_mfma_f32_16x16x32_bf16 v[204:207], v[244:247], v[236:239], v[204:207]\n" \
  "ds_read_b128 v[236:239], v252 offset:23552\n" \
  "ds_read_b128 v[240:243], v250 offset:32768\n" \
  "ds_read_b128 v[244:247], v250 offset:33792\n" \
  "s_waitcnt lgkmcnt(9)\n" \
  "v_mfma_f32_16x16x32_bf16 v[192:195], v[16:19], v[208:211], v[192:195]\n" \
  "v_mfma_f32_16x16x32_bf16 v[196:199], v[80:83], v[208:211], v[196:199]\n" \
  "s_waitcnt lgkmcnt(8)\n" \
  "v_mfma_f32_16x16x32_bf16 v[200:203], v[16:19], v[212:215], v[200:203]\n" \
  "v_mfma_f32_16x16x32_bf16 v[204:207], v[80:83], v[212:215], v[204:207]\n" \
  "s_waitcnt vmcnt(0)\n" \
  "s_waitcnt lgkmcnt(0)\n" \
  "s_barrier\n" \
  "ds_read_b128 v[208:211], v252 offset:0\n" \
  "ds_read_b128 v[212:215], v252 offset:1024\n" \
  "s_add_u32 m0, %[mw], 12288\n" \
  "s_xor_b32 %[cnt], %[sc], 512\n" \
  "buffer_load_dwordx4 v248, %[rx], %[cnt] offen lds\n" \
  "s_add_u32 m0, %[mw], 16384\n" \
  "s_xor_b32 %[cnt], %[sc], 576\n" \
  "buffer_load_dwordx4 v248, %[rx], %[cnt] offen lds\n" \
  "s_add_u32 m0, %[mw], 20480\n" \
  "s_xor_b32 %[cnt], %[sc], 640\n" \
  "buffer_load_dwordx4 v248, %[rx], %[cnt] offen lds\n" \
  "v_lshl_or_b32 v253, v251, 24, %[fm]\n" \
  "buffer_store_dword v253, v253, %[rs], %[fo] offen sc1\n" \
  "v_mfma_f32_16x16x32_bf16 v[192:195], v[144:147], v[216:219], v[192:195]\n" \
  "v_mfma_f32_16x16x32_bf16 v[196:199], v[148:151], v[216:219], v[196:199]\n" \
  "ds_read_b128 v[216:219], v252 offset:2048\n" \
  "v_mfma_f32_16x16x32_bf16 v[200:203], v[144:147], v[220:223], v[200:203]\n" \
  "v_mfma_f32_16x16x32_bf16 v[204:207], v[148:151], v[220:223], v[204:207]\n" \
  "ds_read_b128 v[220:223], v252 offset:3072\n" \
  "v_mfma_f32_16x16x32_bf16 v[192:195], v[20:23], v[224:227], v[192:195]\n" \
  "v_mfma_f32_16x16x32_bf16 v[196:199], v[84:87], v[224:227], v[196:199]\n" \
  "ds_read_b128 v[224:227], v252 offset:4096\n" \
  "v_mfma_f32_16x16x32_bf16 v[200:203], v[20:23], v[228:231], v[200:203]\n" \
  "v_mfma_f32_16x16x32_bf16 v[204:207], v[84:87], v[228:231], v[204:207]\n" \
  "ds_read_b128 v[228:231], v252 offset:5120\n" \
  "v_mfma_f32_16x16x32_bf16 v[192:195], v[240:243], v[232:235], v[192:195]\n" \
  "v_mfma_f32_16x16x32_bf16 v[196:199], v[244:247], v[232:235], v[196:199]\n" \
  "ds_read_b128 v[232:235], v252 offset:6144\n" \
  "v_mfma_f32_16x16x32_bf16 v[200:203], v[240:243], v[236:239], v[200:203]\n" \
  "v_mfma_f32_16x16x32_bf16 v[204:207], v[244:247], v[236:239], v[204:207]\n" \
  "ds_read_b128 v[236:239], v252 offset:7168\n" \
  "ds_read_b128 v[240:243], v250 offset:49152\n" \
  "ds_read_b128 v[244:247], v250 offset:50176\n" \
  "s_waitcnt lgkmcnt(9)\n" \
  "v_mfma_f32_16x16x32_bf16 v[192:195], v[24:27], v[208:211], v[192:195]\n" \
  "v_mfma_f32_16x16x32_bf16 v[196:199], v[88:91], v[208:211], v[196:199]\n" \
  "s_waitcnt lgkmcnt(8)\n" \
  "v_mfma_f32_16x16x32_bf16 v[200:203], v[24:27], v[212:215], v[200:203]\n" \
  "v_mfma_f32_16x16x32_bf16 v[204:207], v[88:91], v[212:215], v[204:207]\n" \
  "s_waitcnt vmcnt(1)\n" \
  "s_waitcnt lgkmcnt(0)\n" \
  "s_barrier\n" \
  "ds_read_b128 v[208:211], v252 offset:12288\n" \
  "ds_read_b128 v[212:215], v252 offset:13312\n" \
  "s_add_u32 m0, %[mw], 0\n" \
  "s_xor_b32 %[cnt], %[sc], 704\n" \
  "buffer_load_dwordx4 v248, %[rx], %[cnt] offen lds\n" \
  "s_add_u32 m0, %[mw], 4096\n" \
  "s_xor_b32 %[cnt], %[sc], 768\n" \
  "buffer_load_dwordx4 v248, %[rx], %[cnt] offen lds\n" \
  "s_add_u32 m0, %[mw], 8192\n" \
  "s_xor_b32 %[cnt], %[sc], 832\n" \
  "buffer_load_dwordx4 v248, %[rx], %[cnt] offen lds\n" \
  "v_lshl_or_b32 v253, v251, 3, %[tm]\n" \
  "buffer_load_dword v253, v253, %[rx], %[tc] offen\n" \
  "v_mfma_f32_16x16x32_bf16 v[192:195], v[152:155], v[216:219], v[192:195]\n" \
  "v_mfma_f32_16x16x32_bf16 v[196:199], v[156:159], v[216:219], v[196:199]\n" \
  "ds_read_b128 v[216:219], v252 offset:14336\n" \
  "v_mfma_f32_16x16x32_bf16 v[200:203], v[152:155], v[220:223], v[200:203]\n" \
  "v_mfma_f32_16x16x32_bf16 v[204:207], v[156:159], v[220:223], v[204:207]\n" \
  "ds_read_b128 v[220:223], v252 offset:15360\n" \
  "v_mfma_f32_16x16x32_bf16 v[192:195], v[28:31], v[224:227], v[192:195]\n" \
  "v_mfma_f32_16x16x32_bf16 v[196:199], v[92:95], v[224:227], v[196:199]\n" \
  "ds_read_b128 v[224:227], v252 offset:16384\n" \
  "v_mfma_f32_16x16x32_bf16 v[200:203], v[28:31], v[228:231], v[200:203]\n" \
  "v_mfma_f32_16x16x32_bf16 v[204:207], v[92:95], v[228:231], v[204:207]\n" \
  "ds_read_b128 v[228:231], v252 offset:17408\n" \
  "v_mfma_f32_16x16x32_bf16 v[192:195], v[240:243], v[232:235], v[192:195]\n" \
  "v_mfma_f32_16x16x32_bf16 v[196:199], v[244:247], v[232:235], v[196:199]\n" \
  "ds_read_b128 v[232:235], v252 offset:18432\n" \
  "v_mfma_f32_16x16x32_bf16 v[200:203], v[240:243], v[236:239], v[200:203]\n" \
  "v_mfma_f32_16x16x32_bf16 v[204:207], v[244:247], v[236:239], v[204:207]\n" \
  "ds_read_b128 v[236:239], v252 offset:19456\n" \
  "ds_read_b128 v[240:243], v249 offset:0\n" \
  "ds_read_b128 v[244:247], v249 offset:1024\n" \
  "s_waitcnt lgkmcnt(9)\n" \
  "v_mfma_f32_16x16x32_bf16 v[192:195], v[32:35], v[208:211], v[192:195]\n" \
  "v_mfma_f32_16x16x32_bf16 v[196:199], v[96:99], v[208:211], v[196:199]\n" \
  "ds_read_b128 v[208:211], v252 offset:20480\n" \
  "s_waitcnt lgkmcnt(9)\n" \
  "v_mfma_f32_16x16x32_bf16 v[200:203], v[32:35], v[212:215], v[200:203]\n" \
  "v_mfma_f32_16x16x32_bf16 v[204:207], v[96:99], v[212:215], v[204:207]\n" \
  "ds_read_b128 v[212:215], v252 offset:21504\n" \
  "s_waitcnt lgkmcnt(9)\n" \
  "v_mfma_f32_16x16x32_bf16 v[192:195], v[160:163], v[216:219], v[192:195]\n" \
  "v_mfma_f32_16x16x32_bf16 v[196:199], v[164:167], v[216:219], v[196:199]\n" \
  "ds_read_b128 v[216:219], v252 offset:22528\n" \
  "s_waitcnt lgkmcnt(9)\n" \
  "v_mfma_f32_16x16x32_bf16 v[200:203], v[160:163], v[220:223], v[200:203]\n" \
  "v_mfma_f32_16x16x32_bf16 v[204:207], v[164:167], v[220:223], v[204:207]\n" \
  "ds_read_b128 v[220:223], v252 offset:23552\n" \
  "s_waitcnt lgkmcnt(9)\n" \
  "v_mfma_f32_16x16x32_bf16 v[192:195], v[36:39], v[224:227], v[192:195]\n" \
  "v_mfma_f32_16x16x32_bf16 v[196:199], v[100:103], v[224:227], v[196:199]\n" \
  "s_waitcnt lgkmcnt(8)\n" \
  "v_mfma_f32_16x16x32_bf16 v[200:203], v[36:39], v[228:231], v[200:203]\n" \
  "v_mfma_f32_16x16x32_bf16 v[204:207], v[100:103], v[228:231], v[204:207]\n" \
  "s_waitcnt vmcnt(1)\n" \
  "s_waitcnt lgkmcnt(0)\n" \
  "s_barrier\n" \
  "ds_read_b128 v[224:227], v252 offset:0\n" \
  "ds_read_b128 v[228:231], v252 offset:1024\n" \
  "s_add_u32 m0, %[mw], 12288\n" \
  "s_xor_b32 %[cnt], %[sc], 896\n" \
  "buffer_load_dwordx4 v248, %[rx], %[cnt] offen lds\n" \
  "s_add_u32 m0, %[mw], 16384\n" \
  "s_xor_b32 %[cnt], %[sc], 960\n" \
  "buffer_load_dwordx4 v248, %[rx], %[cnt] offen lds\n" \
  "v_mfma_f32_16x16x32_bf16 v[192:195], v[240:243], v[232:235], v[192:195]\n" \
  "v_mfma_f32_16x16x32_bf16 v[196:199], v[244:247], v[232:235], v[196:199]\n" \
  "ds_read_b128 v[232:235], v252 offset:2048\n" \
  "v_mfma_f32_16x16x32_bf16 v[200:203], v[240:243], v[236:239], v[200:203]\n" \
  "v_mfma_f32_16x16x32_bf16 v[204:207], v[244:247], v[236:239], v[204:207]\n" \
  "ds_read_b128 v[236:239], v252 offset:3072\n" \
  "ds_read_b128 v[240:243], v249 offset:16384\n" \
  "ds_read_b128 v[244:247], v249 offset:17408\n" \
  "v_mfma_f32_16x16x32_bf16 v[192:195], v[40:43], v[208:211], v[192:195]\n" \
  "v_mfma_f32_16x16x32_bf16 v[196:199], v[104:107], v[208:211], v[196:199]\n" \
  "ds_read_b128 v[208:211], v252 offset:4096\n" \
  "v_mfma_f32_16x16x32_bf16 v[200:203], v[40:43], v[212:215], v[200:203]\n" \
  "v_mfma_f32_16x16x32_bf16 v[204:207], v[104:107], v[212:215], v[204:207]\n" \
  "ds_read_b128 v[212:215], v252 offset:5120\n" \
  "v_mfma_f32_16x16x32_bf16 v[192:195], v[168:171], v[216:219], v[192:195]\n" \
  "v_mfma_f32_16x16x32_bf16 v[196:199], v[172:175], v[216:219], v[196:199]\n" \
  "ds_read_b128 v[216:219], v252 offset:6144\n" \
  "v_mfma_f32_16x16x32_bf16 v[200:203], v[168:171], v[220:223], v[200:203]\n" \
  "v_mfma_f32_16x16x32_bf16 v[204:207], v[172:175], v[220:223], v[204:207]\n" \
  "ds_read_b128 v[220:223], v252 offset:7168\n" \
  "s_waitcnt lgkmcnt(9)\n" \
  "v_mfma_f32_16x16x32_bf16 v[192:195], v[44:47], v[224:227], v[192:195]\n" \
  "v_mfma_f32_16x16x32_bf16 v[196:199], v[108:111], v[224:227], v[196:199]\n" \
  "ds_read_b128 v[224:227], v252 offset:8192\n" \
  "s_waitcnt lgkmcnt(9)\n" \
  "v_mfma_f32_16x16x32_bf16 v[200:203], v[44:47], v[228:231], v[200:203]\n" \
  "v_mfma_f32_16x16x32_bf16 v[204:207], v[108:111], v[228:231], v[204:207]\n" \
  "ds_read_b128 v[228:231], v252 offset:9216\n" \
  "s_waitcnt lgkmcnt(7)\n" \
  "v_mfma_f32_16x16x32_bf16 v[192:195], v[240:243], v[232:235], v[192:195]\n" \
  "s_waitcnt lgkmcnt(6)\n" \
  "v_mfma_f32_16x16x32_bf16 v[196:199], v[244:247], v[232:235], v[196:199]\n" \
  "ds_read_b128 v[232:235], v252 offset:10240\n" \
  "v_mfma_f32_16x16x32_bf16 v[200:203], v[240:243], v[236:239], v[200:203]\n" \
  "v_mfma_f32_16x16x32_bf16 v[204:207], v[244:247], v[236:239], v[204:207]\n" \
  "ds_read_b128 v[236:239], v252 offset:11264\n" \
  "ds_read_b128 v[240:243], v249 offset:32768\n" \
  "ds_read_b128 v[244:247], v249 offset:33792\n" \
  "s_waitcnt lgkmcnt(9)\n" \
  "v_mfma_f32_16x16x32_bf16 v[192:195], v[48:51], v[208:211], v[192:195]\n" \
  "v_mfma_f32_16x16x32_bf16 v[196:199], v[112:115], v[208:211], v[196:199]\n" \
  "s_waitcnt lgkmcnt(8)\n" \
  "v_mfma_f32_16x16x32_bf16 v[200:203], v[48:51], v[212:215], v[200:203]\n" \
  "v_mfma_f32_16x16x32_bf16 v[204:207], v[112:115], v[212:215], v[204:207]\n" \
  "s_waitcnt vmcnt(0)\n" \
  "s_waitcnt lgkmcnt(0)\n" \
  "s_barrier\n" \
  "ds_read_b128 v[208:211], v252 offset:12288\n" \
  "ds_read_b128 v[212:215], v252 offset:13312\n" \
  "s_add_u32 m0, %[mw], 0\n" \
  "s_xor_b32 %[cnt], %[sn], 0\n" \
  "buffer_load_dwordx4 v248, %[rx], %[cnt] offen lds\n" \
  "s_add_u32 m0, %[mw], 4096\n" \
  "s_xor_b32 %[cnt], %[sn], 64\n" \
  "buffer_load_dwordx4 v248, %[rx], %[cnt] offen lds\n" \
  "s_add_u32 m0, %[mw], 8192\n" \
  "s_xor_b32 %[cnt], %[sn], 128\n" \
  "buffer_load_dwordx4 v248, %[rx], %[cnt] offen lds\n" \
  "v_mfma_f32_16x16x32_bf16 v[192:195], v[176:179], v[216:219], v[192:195]\n" \
  "v_mfma_f32_16x16x32_bf16 v[196:199], v[180:183], v[216:219], v[196:199]\n" \
  "ds_read_b128 v[216:219], v252 offset:14336\n" \
  "v_mfma_f32_16x16x32_bf16 v[200:203], v[176:179], v[220:223], v[200:203]\n" \
  "v_mfma_f32_16x16x32_bf16 v[204:207], v[180:183], v[220:223], v[204:207]\n" \
  "ds_read_b128 v[220:223], v252 offset:15360\n" \
  "v_mfma_f32_16x16x32_bf16 v[192:195], v[52:55], v[224:227], v[192:195]\n" \
  "v_mfma_f32_16x16x32_bf16 v[196:199], v[116:119], v[224:227], v[196:199]\n" \
  "ds_read_b128 v[224:227], v252 offset:16384\n" \
  "v_mfma_f32_16x16x32_bf16 v[200:203], v[52:55], v[228:231], v[200:203]\n" \
  "v_mfma_f32_16x16x32_bf16 v[204:207], v[116:119], v[228:231], v[204:207]\n" \
  "ds_read_b128 v[228:231], v252 offset:17408\n" \
  "v_mfma_f32_16x16x32_bf16 v[192:195], v[240:243], v[232:235], v[192:195]\n" \
  "v_mfma_f32_16x16x32_bf16 v[196:199], v[244:247], v[232:235], v[196:199]\n" \
  "ds_read_b128 v[232:235], v252 offset:18432\n" \
  "v_mfma_f32_16x16x32_bf16 v[200:203], v[240:243], v[236:239], v[200:203]\n" \
  "v_mfma_f32_16x16x32_bf16 v[204:207], v[244:247], v[236:239], v[204:207]\n" \
  "ds_read_b128 v[236:239], v252 offset:19456\n" \
  "ds_read_b128 v[240:243], v249 offset:49152\n" \
  "ds_read_b128 v[244:247], v249 offset:50176\n" \
  "s_waitcnt lgkmcnt(9)\n" \
  "v_mfma_f32_16x16x32_bf16 v[192:195], v[56:59], v[208:211], v[192:195]\n" \
  "v_mfma_f32_16x16x32_bf16 v[196:199], v[120:123], v[208:211], v[196:199]\n" \
  "s_waitcnt lgkmcnt(8)\n" \
  "v_mfma_f32_16x16x32_bf16 v[200:203], v[56:59], v[212:215], v[200:203]\n" \
  "v_mfma_f32_16x16x32_bf16 v[204:207], v[120:123], v[212:215], v[204:207]\n" \
  "s_waitcnt vmcnt(0)\n" \
  "s_waitcnt lgkmcnt(0)\n" \
  "s_barrier\n" \
  "ds_read_b128 v[208:211], v252 offset:0\n" \
  "ds_read_b128 v[212:215], v252 offset:1024\n" \
  "s_add_u32 m0, %[mw], 12288\n" \
  "s_xor_b32 %[cnt], %[sn], 192\n" \
  "buffer_load_dwordx4 v248, %[rx], %[cnt] offen lds\n" \
  "s_add_u32 m0, %[mw], 16384\n" \
  "s_xor_b32 %[cnt], %[sn], 256\n" \
  "buffer_load_dwordx4 v248, %[rx], %[cnt] offen lds\n" \
  "s_add_u32 m0, %[mw], 20480\n" \
  "s_xor_b32 %[cnt], %[sn], 320\n" \
  "buffer_load_dwordx4 v248, %[rx], %[cnt] offen lds\n" \
  "v_mfma_f32_16x16x32_bf16 v[192:195], v[184:187], v[216:219], v[192:195]\n" \
  "v_mfma_f32_16x16x32_bf16 v[196:199], v[188:191], v[216:219], v[196:199]\n" \
  "ds_read_b128 v[216:219], v252 offset:2048\n" \
  "v_mfma_f32_16x16x32_bf16 v[200:203], v[184:187], v[220:223], v[200:203]\n" \
  "v_mfma_f32_16x16x32_bf16 v[204:207], v[188:191], v[220:223], v[204:207]\n" \
  "ds_read_b128 v[220:223], v252 offset:3072\n" \
  "v_mfma_f32_16x16x32_bf16 v[192:195], v[60:63], v[224:227], v[192:195]\n" \
  "v_mfma_f32_16x16x32_bf16 v[196:199], v[124:127], v[224:227], v[196:199]\n" \
  "ds_read_b128 v[224:227], v252 offset:4096\n" \
  "v_mfma_f32_16x16x32_bf16 v[200:203], v[60:63], v[228:231], v[200:203]\n" \
  "v_mfma_f32_16x16x32_bf16 v[204:207], v[124:127], v[228:231], v[204:207]\n" \
  "ds_read_b128 v[228:231], v252 offset:5120\n" \
  "v_mfma_f32_16x16x32_bf16 v[192:195], v[240:243], v[232:235], v[192:195]\n" \
  "v_mfma_f32_16x16x32_bf16 v[196:199], v[244:247], v[232:235], v[196:199]\n" \
  "ds_read_b128 v[232:235], v252 offset:6144\n" \
  "v_mfma_f32_16x16x32_bf16 v[200:203], v[240:243], v[236:239], v[200:203]\n" \
  "v_mfma_f32_16x16x32_bf16 v[204:207], v[244:247], v[236:239], v[204:207]\n" \
  "ds_read_b128 v[236:239], v252 offset:7168\n" \
  "v_cvt_pk_bf16_f32 v192, v192, v193\n" \
  "v_cvt_pk_bf16_f32 v193, v194, v195\n" \
  "v_cvt_pk_bf16_f32 v194, v196, v197\n" \
  "v_cvt_pk_bf16_f32 v195, v198, v199\n" \
  "v_add_u32 v196, %[so], v251\n" \
  "ds_write_b128 v196, v[192:195] offset:24576\n" \
  "v_cvt_pk_bf16_f32 v200, v200, v201\n" \
  "v_cvt_pk_bf16_f32 v201, v202, v203\n" \
  "v_cvt_pk_bf16_f32 v202, v204, v205\n" \
  "v_cvt_pk_bf16_f32 v203, v206, v207\n" \
  "ds_write_b128 v196, v[200:203] offset:28672\n" \
  ".Lqe_%=:\n" \
  "s_mov_b32 m0, %[keep]\n" \
  "s_waitcnt lgkmcnt(0)\n"
#define ST_XB \
  "s_barrier\n" \
  "ds_read_b128 v[192:195], v251 offset:24576\n" \
  "ds_read_b128 v[208:211], v251 offset:28672\n" \
  "ds_read_b128 v[196:199], v251 offset:25600\n" \
  "ds_read_b128 v[212:215], v251 offset:29696\n" \
  "ds_read_b128 v[200:203], v251 offset:26624\n" \
  "ds_read_b128 v[216:219], v251 offset:30720\n" \
  "ds_read_b128 v[204:207], v251 offset:27648\n" \
  "ds_read_b128 v[220:223], v251 offset:31744\n" \
  "ds_read_b128 v[240:243], v250 offset:0\n" \
  "ds_read_b128 v[244:247], v250 offset:16384\n" \
  "s_waitcnt lgkmcnt(9)\n" \
  "v_mfma_f32_16x16x32_bf16 v[224:227], v[192:195], v[0:3], 0\n" \
  "s_waitcnt lgkmcnt(8)\n" \
  "v_mfma_f32_16x16x32_bf16 v[228:231], v[208:211], v[0:3], 0\n" \
  "s_waitcnt lgkmcnt(7)\n" \
  "v_mfma_f32_16x16x32_bf16 v[224:227], v[196:199], v[4:7], v[224:227]\n" \
  "s_waitcnt lgkmcnt(6)\n" \
  "v_mfma_f32_16x16x32_bf16 v[228:231], v[212:215], v[4:7], v[228:231]\n" \
  "s_waitcnt lgkmcnt(5)\n" \
  "v_mfma_f32_16x16x32_bf16 v[224:227], v[200:203], v[8:11], v[224:227]\n" \
  "s_waitcnt lgkmcnt(4)\n" \
  "v_mfma_f32_16x16x32_bf16 v[228:231], v[216:219], v[8:11], v[228:231]\n" \
  "s_waitcnt lgkmcnt(3)\n" \
  "v_mfma_f32_16x16x32_bf16 v[224:227], v[204:207], v[12:15], v[224:227]\n" \
  "s_waitcnt lgkmcnt(2)\n" \
  "v_mfma_f32_16x16x32_bf16 v[228:231], v[220:223], v[12:15], v[228:231]\n" \
  "v_mfma_f32_16x16x32_bf16 v[232:235], v[192:195], v[128:131], 0\n" \
  "v_mfma_f32_16x16x32_bf16 v[236:239], v[208:211], v[128:131], 0\n" \
  "v_mfma_f32_16x16x32_bf16 v[232:235], v[200:203], v[136:139], v[232:235]\n" \
  "v_cvt_pk_bf16_f32 v224, v224, v228\n" \
  "v_cvt_pk_bf16_f32 v225, v225, v229\n" \
  "v_mfma_f32_16x16x32_bf16 v[236:239], v[216:219], v[136:139], v[236:239]\n" \
  "v_cvt_pk_bf16_f32 v226, v226, v230\n" \
  "v_cvt_pk_bf16_f32 v227, v227, v231\n" \
  "s_waitcnt lgkmcnt(1)\n" \
  "v_mfma_f32_16x16x32_bf16 v[232:235], v[196:199], v[240:243], v[232:235]\n" \
  "s_add_u32 %[cnt], %[so], 0*%c[js]+0*%c[ss]\n" \
  "buffer_store_dwordx4 v[224:227], v251, %[rs], %[cnt] offen sc1\n" \
  "v_mfma_f32_16x16x32_bf16 v[236:239], v[212:215], v[240:243], v[236:239]\n" \
  "s_waitcnt lgkmcnt(0)\n" \
  "v_mfma_f32_16x16x32_bf16 v[232:235], v[204:207], v[244:247], v[232:235]\n" \
  "v_mfma_f32_16x16x32_bf16 v[236:239], v[220:223], v[244:247], v[236:239]\n" \
  "ds_read_b128 v[240:243], v250 offset:1024\n" \
  "ds_read_b128 v[244:247], v250 offset:17408\n" \
  "v_mfma_f32_16x16x32_bf16 v[224:227], v[192:195], v[64:67], 0\n" \
  "v_mfma_f32_16x16x32_bf16 v[228:231], v[208:211], v[64:67], 0\n" \
  "v_mfma_f32_16x16x32_bf16 v[224:227], v[196:199], v[68:71], v[224:227]\n" \
  "v_cvt_pk_bf16_f32 v232, v232, v236\n" \
  "v_cvt_pk_bf16_f32 v233, v233, v237\n" \
  "v_mfma_f32_16x16x32_bf16 v[228:231], v[212:215], v[68:71], v[228:231]\n" \
  "v_cvt_pk_bf16_f32 v234, v234, v238\n" \
  "v_cvt_pk_bf16_f32 v235, v235, v239\n" \
  "v_mfma_f32_16x16x32_bf16 v[224:227], v[200:203], v[72:75], v[224:227]\n" \
  "s_add_u32 %[cnt], %[so], 4*%c[js]+0*%c[ss]\n" \
  "buffer_store_dwordx4 v[232:235], v251, %[rs], %[cnt] offen sc1\n" \
  "v_mfma_f32_16x16x32_bf16 v[228:231], v[216:219], v[72:75], v[228:231]\n" \
  "v_mfma_f32_16x16x32_bf16 v[224:227], v[204:207], v[76:79], v[224:227]\n" \
  "v_mfma_f32_16x16x32_bf16 v[228:231], v[220:223], v[76:79], v[228:231]\n" \
  "s_waitcnt vmcnt(2)\n" \
  "s_barrier\n" \
  "v_mfma_f32_16x16x32_bf16 v[232:235], v[192:195], v[132:135], 0\n" \
  "v_mfma_f32_16x16x32_bf16 v[236:239], v[208:211], v[132:135], 0\n" \
  "v_mfma_f32_16x16x32_bf16 v[232:235], v[200:203], v[140:143], v[232:235]\n" \
  "v_cvt_pk_bf16_f32 v224, v224, v228\n" \
  "v_cvt_pk_bf16_f32 v225, v225, v229\n" \
  "v_mfma_f32_16x16x32_bf16 v[236:239], v[216:219], v[140:143], v[236:239]\n" \
  "v_cvt_pk_bf16_f32 v226, v226, v230\n" \
  "v_cvt_pk_bf16_f32 v227, v227, v231\n" \
  "s_waitcnt lgkmcnt(1)\n" \
  "v_mfma_f32_16x16x32_bf16 v[232:235], v[196:199], v[240:243], v[232:235]\n" \
  "s_add_u32 %[cnt], %[so], 0*%c[js]+1*%c[ss]\n" \
  "buffer_store_dwordx4 v[224:227], v251, %[rs], %[cnt] offen sc1\n" \
  "v_mfma_f32_16x16x32_bf16 v[236:239], v[212:215], v[240:243], v[236:239]\n" \
  "s_waitcnt lgkmcnt(0)\n" \
  "v_mfma_f32_16x16x32_bf16 v[232:235], v[204:207], v[244:247], v[232:235]\n" \
  "v_mfma_f32_16x16x32_bf16 v[236:239], v[220:223], v[244:247], v[236:239]\n" \
  "ds_read_b128 v[240:243], v250 offset:32768\n" \
  "ds_read_b128 v[244:247], v250 offset:49152\n" \
  "v_mfma_f32_16x16x32_bf16 v[224:227], v[192:195], v[16:19], 0\n" \
  "v_mfma_f32_16x16x32_bf16 v[228:231], v[208:211], v[16:19], 0\n" \
  "v_mfma_f32_16x16x32_bf16 v[224:227], v[196:199], v[20:23], v[224:227]\n" \
  "v_cvt_pk_bf16_f32 v232, v232, v236\n" \
  "v_cvt_pk_bf16_f32 v233, v233, v237\n" \
  "v_mfma_f32_16x16x32_bf16 v[228:231], v[212:215], v[20:23], v[228:231]\n" \
  "v_cvt_pk_bf16_f32 v234, v234, v238\n" \
  "v_cvt_pk_bf16_f32 v235, v235, v239\n" \
  "v_mfma_f32_16x16x32_bf16 v[224:227], v[200:203], v[24:27], v[224:227]\n" \
  "s_add_u32 %[cnt], %[so], 4*%c[js]+1*%c[ss]\n" \
  "buffer_store_dwordx4 v[232:235], v251, %[rs], %[cnt] offen sc1\n" \
  "v_mfma_f32_16x16x32_bf16 v[228:231], v[216:219], v[24:27], v[228:231]\n" \
  "v_mfma_f32_16x16x32_bf16 v[224:227], v[204:207], v[28:31], v[224:227]\n" \
  "v_mfma_f32_16x16x32_bf16 v[228:231], v[220:223], v[28:31], v[228:231]\n" \
  "s_barrier\n" \
  "v_mfma_f32_16x16x32_bf16 v[232:235], v[192:195], v[144:147], 0\n" \
  "v_mfma_f32_16x16x32_bf16 v[236:239], v[208:211], v[144:147], 0\n" \
  "v_mfma_f32_16x16x32_bf16 v[232:235], v[200:203], v[152:155], v[232:235]\n" \
  "v_cvt_pk_bf16_f32 v224, v224, v228\n" \
  "v_cvt_pk_bf16_f32 v225, v225, v229\n" \
  "v_mfma_f32_16x16x32_bf16 v[236:239], v[216:219], v[152:155], v[236:239]\n" \
  "v_cvt_pk_bf16_f32 v226, v226, v230\n" \
  "v_cvt_pk_bf16_f32 v227, v227, v231\n" \
  "s_waitcnt lgkmcnt(1)\n" \
  "v_mfma_f32_16x16x32_bf16 v[232:235], v[196:199], v[240:243], v[232:235]\n" \
  "s_add_u32 %[cnt], %[so2], 1*%c[js]+0*%c[ss]\n" \
  "buffer_store_dwordx4 v[224:227], v251, %[rs], %[cnt] offen sc1\n" \
  "v_mfma_f32_16x16x32_bf16 v[236:239], v[212:215], v[240:243], v[236:239]\n" \
  "s_waitcnt lgkmcnt(0)\n" \
  "v_mfma_f32_16x16x32_bf16 v[232:235], v[204:207], v[244:247], v[232:235]\n" \
  "v_mfma_f32_16x16x32_bf16 v[236:239], v[220:223], v[244:247], v[236:239]\n" \
  "ds_read_b128 v[240:243], v250 offset:33792\n" \
  "ds_read_b128 v[244:247], v250 offset:50176\n" \
  "v_mfma_f32_16x16x32_bf16 v[224:227], v[192:195], v[80:83], 0\n" \
  "v_mfma_f32_16x16x32_bf16 v[228:231], v[208:211], v[80:83], 0\n" \
  "v_mfma_f32_16x16x32_bf16 v[224:227], v[196:199], v[84:87], v[224:227]\n" \
  "v_cvt_pk_bf16_f32 v232, v232, v236\n" \
  "v_cvt_pk_bf16_f32 v233, v233, v237\n" \
  "v_mfma_f32_16x16x32_bf16 v[228:231], v[212:215], v[84:87], v[228:231]\n" \
  "v_cvt_pk_bf16_f32 v234, v234, v238\n" \
  "v_cvt_pk_bf16_f32 v235, v235, v239\n" \
  "v_mfma_f32_16x16x32_bf16 v[224:227], v[200:203], v[88:91], v[224:227]\n" \
  "s_add_u32 %[cnt], %[so2], 5*%c[js]+0*%c[ss]\n" \
  "buffer_store_dwordx4 v[232:235], v251, %[rs], %[cnt] offen sc1\n" \
  "v_mfma_f32_16x16x32_bf16 v[228:231], v[216:219], v[88:91], v[228:231]\n" \
  "v_mfma_f32_16x16x32_bf16 v[224:227], v[204:207], v[92:95], v[224:227]\n" \
  "v_mfma_f32_16x16x32_bf16 v[228:231], v[220:223], v[92:95], v[228:231]\n" \
  "v_mfma_f32_16x16x32_bf16 v[232:235], v[192:195], v[148:151], 0\n" \
  "v_mfma_f32_16x16x32_bf16 v[236:239], v[208:211], v[148:151], 0\n" \
  "v_mfma_f32_16x16x32_bf16 v[232:235], v[200:203], v[156:159], v[232:235]\n" \
  "v_cvt_pk_bf16_f32 v224, v224, v228\n" \
  "v_cvt_pk_bf16_f32 v225, v225, v229\n" \
  "v_mfma_f32_16x16x32_bf16 v[236:239], v[216:219], v[156:159], v[236:239]\n" \
  "v_cvt_pk_bf16_f32 v226, v226, v230\n" \
  "v_cvt_pk_bf16_f32 v227, v227, v231\n" \
  "s_waitcnt lgkmcnt(1)\n" \
  "v_mfma_f32_16x16x32_bf16 v[232:235], v[196:199], v[240:243], v[232:235]\n" \
  "s_add_u32 %[cnt], %[so2], 1*%c[js]+1*%c[ss]\n" \
  "buffer_store_dwordx4 v[224:227], v251, %[rs], %[cnt] offen sc1\n" \
  "v_mfma_f32_16x16x32_bf16 v[236:239], v[212:215], v[240:243], v[236:239]\n" \
  "s_waitcnt lgkmcnt(0)\n" \
  "v_mfma_f32_16x16x32_bf16 v[232:235], v[204:207], v[244:247], v[232:235]\n" \
  "v_mfma_f32_16x16x32_bf16 v[236:239], v[220:223], v[244:247], v[236:239]\n" \
  "ds_read_b128 v[240:243], v249 offset:0\n" \
  "ds_read_b128 v[244:247], v249 offset:16384\n" \
  "s_barrier\n" \
  "v_mfma_f32_16x16x32_bf16 v[224:227], v[192:195], v[32:35], 0\n" \
  "v_mfma_f32_16x16x32_bf16 v[228:231], v[208:211], v[32:35], 0\n" \
  "v_mfma_f32_16x16x32_bf16 v[224:227], v[196:199], v[36:39], v[224:227]\n" \
  "v_cvt_pk_bf16_f32 v232, v232, v236\n" \
  "v_cvt_pk_bf16_f32 v233, v233, v237\n" \
  "v_mfma_f32_16x16x32_bf16 v[228:231], v[212:215], v[36:39], v[228:231]\n" \
  "v_cvt_pk_bf16_f32 v234, v234, v238\n" \
  "v_cvt_pk_bf16_f32 v235, v235, v239\n" \
  "v_mfma_f32_16x16x32_bf16 v[224:227], v[200:203], v[40:43], v[224:227]\n" \
  "s_add_u32 %[cnt], %[so2], 5*%c[js]+1*%c[ss]\n" \
  "buffer_store_dwordx4 v[232:235], v251, %[rs], %[cnt] offen sc1\n" \
  "v_mfma_f32_16x16x32_bf16 v[228:231], v[216:219], v[40:43], v[228:231]\n" \
  "v_mfma_f32_16x16x32_bf16 v[224:227], v[204:207], v[44:47], v[224:227]\n" \
  "v_mfma_f32_16x16x32_bf16 v[228:231], v[220:223], v[44:47], v[228:231]\n" \
  "v_mfma_f32_16x16x32_bf16 v[232:235], v[192:195], v[160:163], 0\n" \
  "v_mfma_f32_16x16x32_bf16 v[236:239], v[208:211], v[160:163], 0\n" \
  "v_mfma_f32_16x16x32_bf16 v[232:235], v[200:203], v[168:171], v[232:235]\n" \
  "v_cvt_pk_bf16_f32 v224, v224, v228\n" \
  "v_cvt_pk_bf16_f32 v225, v225, v229\n" \
  "v_mfma_f32_16x16x32_bf16 v[236:239], v[216:219], v[168:171], v[236:239]\n" \
  "v_cvt_pk_bf16_f32 v226, v226, v230\n" \
  "v_cvt_pk_bf16_f32 v227, v227, v231\n" \
  "s_waitcnt lgkmcnt(1)\n" \
  "v_mfma_f32_16x16x32_bf16 v[232:235], v[196:199], v[240:243], v[232:235]\n" \
  "s_add_u32 %[cnt], %[so], 2*%c[js]+0*%c[ss]\n" \
  "buffer_store_dwordx4 v[224:227], v251, %[rs], %[cnt] offen sc1\n" \
  "v_mfma_f32_16x16x32_bf16 v[236:239], v[212:215], v[240:243], v[236:239]\n" \
  "s_waitcnt lgkmcnt(0)\n" \
  "v_mfma_f32_16x16x32_bf16 v[232:235], v[204:207], v[244:247], v[232:235]\n" \
  "v_mfma_f32_16x16x32_bf16 v[236:239], v[220:223], v[244:247], v[236:239]\n" \
  "ds_read_b128 v[240:243], v249 offset:1024\n" \
  "ds_read_b128 v[244:247], v249 offset:17408\n" \
  "v_mfma_f32_16x16x32_bf16 v[224:227], v[192:195], v[96:99], 0\n" \
  "v_mfma_f32_16x16x32_bf16 v[228:231], v[208:211], v[96:99], 0\n" \
  "v_mfma_f32_16x16x32_bf16 v[224:227], v[196:199], v[100:103], v[224:227]\n" \
  "v_cvt_pk_bf16_f32 v232, v232, v236\n" \
  "v_cvt_pk_bf16_f32 v233, v233, v237\n" \
  "v_mfma_f32_16x16x32_bf16 v[228:231], v[212:215], v[100:103], v[228:231]\n" \
  "v_cvt_pk_bf16_f32 v234, v234, v238\n" \
  "v_cvt_pk_bf16_f32 v235, v235, v239\n" \
  "v_mfma_f32_16x16x32_bf16 v[224:227], v[200:203], v[104:107], v[224:227]\n" \
  "s_add_u32 %[cnt], %[so], 6*%c[js]+0*%c[ss]\n" \
  "buffer_store_dwordx4 v[232:235], v251, %[rs], %[cnt] offen sc1\n" \
  "v_mfma_f32_16x16x32_bf16 v[228:231], v[216:219], v[104:107], v[228:231]\n" \
  "v_mfma_f32_16x16x32_bf16 v[224:227], v[204:207], v[108:111], v[224:227]\n" \
  "v_mfma_f32_16x16x32_bf16 v[228:231], v[220:223], v[108:111], v[228:231]\n" \
  "s_barrier\n" \
  "v_mfma_f32_16x16x32_bf16 v[232:235], v[192:195], v[164:167], 0\n" \
  "v_mfma_f32_16x16x32_bf16 v[236:239], v[208:211], v[164:167], 0\n" \
  "v_mfma_f32_16x16x32_bf16 v[232:235], v[200:203], v[172:175], v[232:235]\n" \
  "v_cvt_pk_bf16_f32 v224, v224, v228\n" \
  "v_cvt_pk_bf16_f32 v225, v225, v229\n" \
  "v_mfma_f32_16x16x32_bf16 v[236:239], v[216:219], v[172:175], v[236:239]\n" \
  "v_cvt_pk_bf16_f32 v226, v226, v230\n" \
  "v_cvt_pk_bf16_f32 v227, v227, v231\n" \
  "s_waitcnt lgkmcnt(1)\n" \
  "v_mfma_f32_16x16x32_bf16 v[232:235], v[196:199], v[240:243], v[232:235]\n" \
  "s_add_u32 %[cnt], %[so], 2*%c[js]+1*%c[ss]\n" \
  "buffer_store_dwordx4 v[224:227], v251, %[rs], %[cnt] offen sc1\n" \
  "v_mfma_f32_16x16x32_bf16 v[236:239], v[212:215], v[240:243], v[236:239]\n" \
  "s_waitcnt lgkmcnt(0)\n" \
  "v_mfma_f32_16x16x32_bf16 v[232:235], v[204:207], v[244:247], v[232:235]\n" \
  "v_mfma_f32_16x16x32_bf16 v[236:239], v[220:223], v[244:247], v[236:239]\n" \
  "ds_read_b128 v[240:243], v249 offset:32768\n" \
  "ds_read_b128 v[244:247], v249 offset:49152\n" \
  "v_mfma_f32_16x16x32_bf16 v[224:227], v[192:195], v[48:51], 0\n" \
  "v_mfma_f32_16x16x32_bf16 v[228:231], v[208:211], v[48:51], 0\n" \
  "v_mfma_f32_16x16x32_bf16 v[224:227], v[196:199], v[52:55], v[224:227]\n" \
  "v_cvt_pk_bf16_f32 v232, v232, v236\n" \
  "v_cvt_pk_bf16_f32 v233, v233, v237\n" \
  "v_mfma_f32_16x16x32_bf16 v[228:231], v[212:215], v[52:55], v[228:231]\n" \
  "v_cvt_pk_bf16_f32 v234, v234, v238\n" \
  "v_cvt_pk_bf16_f32 v235, v235, v239\n" \
  "v_mfma_f32_16x16x32_bf16 v[224:227], v[200:203], v[56:59], v[224:227]\n" \
  "s_add_u32 %[cnt], %[so], 6*%c[js]+1*%c[ss]\n" \
  "buffer_store_dwordx4 v[232:235], v251, %[rs], %[cnt] offen sc1\n" \
  "v_mfma_f32_16x16x32_bf16 v[228:231], v[216:219], v[56:59], v[228:231]\n" \
  "v_mfma_f32_16x16x32_bf16 v[224:227], v[204:207], v[60:63], v[224:227]\n" \
  "v_mfma_f32_16x16x32_bf16 v[228:231], v[220:223], v[60:63], v[228:231]\n" \
  "s_barrier\n" \
  "v_mfma_f32_16x16x32_bf16 v[232:235], v[192:195], v[176:179], 0\n" \
  "v_mfma_f32_16x16x32_bf16 v[236:239], v[208:211], v[176:179], 0\n" \
  "v_mfma_f32_16x16x32_bf16 v[232:235], v[200:203], v[184:187], v[232:235]\n" \
  "v_cvt_pk_bf16_f32 v224, v224, v228\n" \
  "v_cvt_pk_bf16_f32 v225, v225, v229\n" \
  "v_mfma_f32_16x16x32_bf16 v[236:239], v[216:219], v[184:187], v[236:239]\n" \
  "v_cvt_pk_bf16_f32 v226, v226, v230\n" \
  "v_cvt_pk_bf16_f32 v227, v227, v231\n" \
  "s_waitcnt lgkmcnt(1)\n" \
  "v_mfma_f32_16x16x32_bf16 v[232:235], v[196:199], v[240:243], v[232:235]\n" \
  "s_add_u32 %[cnt], %[so2], 3*%c[js]+0*%c[ss]\n" \
  "buffer_store_dwordx4 v[224:227], v251, %[rs], %[cnt] offen sc1\n" \
  "v_mfma_f32_16x16x32_bf16 v[236:239], v[212:215], v[240:243], v[236:239]\n" \
  "s_waitcnt lgkmcnt(0)\n" \
  "v_mfma_f32_16x16x32_bf16 v[232:235], v[204:207], v[244:247], v[232:235]\n" \
  "v_mfma_f32_16x16x32_bf16 v[236:239], v[220:223], v[244:247], v[236:239]\n" \
  "ds_read_b128 v[240:243], v249 offset:33792\n" \
  "ds_read_b128 v[244:247], v249 offset:50176\n" \
  "v_mfma_f32_16x16x32_bf16 v[224:227], v[192:195], v[112:115], 0\n" \
  "v_mfma_f32_16x16x32_bf16 v[228:231], v[208:211], v[112:115], 0\n" \
  "v_mfma_f32_16x16x32_bf16 v[224:227], v[196:199], v[116:119], v[224:227]\n" \
  "v_cvt_pk_bf16_f32 v232, v232, v236\n" \
  "v_cvt_pk_bf16_f32 v233, v233, v237\n" \
  "v_mfma_f32_16x16x32_bf16 v[228:231], v[212:215], v[116:119], v[228:231]\n" \
  "v_cvt_pk_bf16_f32 v234, v234, v238\n" \
  "v_cvt_pk_bf16_f32 v235, v235, v239\n" \
  "v_mfma_f32_16x16x32_bf16 v[224:227], v[200:203], v[120:123], v[224:227]\n" \
  "s_add_u32 %[cnt], %[so2], 7*%c[js]+0*%c[ss]\n" \
  "buffer_store_dwordx4 v[232:235], v251, %[rs], %[cnt] offen sc1\n" \
  "v_mfma_f32_16x16x32_bf16 v[228:231], v[216:219], v[120:123], v[228:231]\n" \
  "v_mfma_f32_16x16x32_bf16 v[224:227], v[204:207], v[124:127], v[224:227]\n" \
  "v_mfma_f32_16x16x32_bf16 v[228:231], v[220:223], v[124:127], v[228:231]\n" \
  "v_mfma_f32_16x16x32_bf16 v[232:235], v[192:195], v[180:183], 0\n" \
  "v_mfma_f32_16x16x32_bf16 v[236:239], v[208:211], v[180:183], 0\n" \
  "v_mfma_f32_16x16x32_bf16 v[232:235], v[200:203], v[188:191], v[232:235]\n" \
  "v_cvt_pk_bf16_f32 v224, v224, v228\n" \
  "v_cvt_pk_bf16_f32 v225, v225, v229\n" \
  "v_mfma_f32_16x16x32_bf16 v[236:239], v[216:219], v[188:191], v[236:239]\n" \
  "v_cvt_pk_bf16_f32 v226, v226, v230\n" \
  "v_cvt_pk_bf16_f32 v227, v227, v231\n" \
  "s_waitcnt lgkmcnt(1)\n" \
  "v_mfma_f32_16x16x32_bf16 v[232:235], v[196:199], v[240:243], v[232:235]\n" \
  "s_add_u32 %[cnt], %[so2], 3*%c[js]+1*%c[ss]\n" \
  "buffer_store_dwordx4 v[224:227], v251, %[rs], %[cnt] offen sc1\n" \
  "v_mfma_f32_16x16x32_bf16 v[236:239], v[212:215], v[240:243], v[236:239]\n" \
  "s_waitcnt lgkmcnt(0)\n" \
  "v_mfma_f32_16x16x32_bf16 v[232:235], v[204:207], v[244:247], v[232:235]\n" \
  "v_mfma_f32_16x16x32_bf16 v[236:239], v[220:223], v[244:247], v[236:239]\n" \
  "s_nop 8\n" \
  "v_cvt_pk_bf16_f32 v232, v232, v236\n" \
  "v_cvt_pk_bf16_f32 v233, v233, v237\n" \
  "v_cvt_pk_bf16_f32 v234, v234, v238\n" \
  "v_cvt_pk_bf16_f32 v235, v235, v239\n" \
  "s_add_u32 %[cnt], %[so2], 7*%c[js]+1*%c[ss]\n" \
  "buffer_store_dwordx4 v[232:235], v251, %[rs], %[cnt] offen sc1\n" \
  "s_nop 1\n"
#define ST_XB0 \
  "s_setprio 1\n" \
  "ds_read_b128 v[192:195], v251 offset:24576\n" \
  "ds_read_b128 v[208:211], v251 offset:28672\n" \
  "ds_read_b128 v[196:199], v251 offset:25600\n" \
  "ds_read_b128 v[212:215], v251 offset:29696\n" \
  "ds_read_b128 v[200:203], v251 offset:26624\n" \
  "ds_read_b128 v[216:219], v251 offset:30720\n" \
  "ds_read_b128 v[204:207], v251 offset:27648\n" \
  "ds_read_b128 v[220:223], v251 offset:31744\n" \
  "ds_read_b128 v[240:243], v250 offset:0\n" \
  "ds_read_b128 v[244:247], v250 offset:16384\n" \
  "s_waitcnt lgkmcnt(9)\n" \
  "v_mfma_f32_16x16x32_bf16 v[224:227], v[192:195], v[0:3], 0\n" \
  "s_waitcnt lgkmcnt(8)\n" \
  "v_mfma_f32_16x16x32_bf16 v[228:231], v[208:211], v[0:3], 0\n" \
  "s_waitcnt lgkmcnt(7)\n" \
  "v_mfma_f32_16x16x32_bf16 v[224:227], v[196:199], v[4:7], v[224:227]\n" \
  "s_waitcnt lgkmcnt(6)\n" \
  "v_mfma_f32_16x16x32_bf16 v[228:231], v[212:215], v[4:7], v[228:231]\n" \
  "s_waitcnt lgkmcnt(5)\n" \
  "v_mfma_f32_16x16x32_bf16 v[224:227], v[200:203], v[8:11], v[224:227]\n" \
  "s_waitcnt lgkmcnt(4)\n" \
  "v_mfma_f32_16x16x32_bf16 v[228:231], v[216:219], v[8:11], v[228:231]\n" \
  "s_waitcnt lgkmcnt(3)\n" \
  "v_mfma_f32_16x16x32_bf16 v[224:227], v[204:207], v[12:15], v[224:227]\n" \
  "s_waitcnt lgkmcnt(2)\n" \
  "v_mfma_f32_16x16x32_bf16 v[228:231], v[220:223], v[12:15], v[228:231]\n" \
  "v_mfma_f32_16x16x32_bf16 v[232:235], v[192:195], v[128:131], 0\n" \
  "v_mfma_f32_16x16x32_bf16 v[236:239], v[208:211], v[128:131], 0\n" \
  "v_mfma_f32_16x16x32_bf16 v[232:235], v[200:203], v[136:139], v[232:235]\n" \
  "v_cvt_pk_bf16_f32 v224, v224, v228\n" \
  "v_cvt_pk_bf16_f32 v225, v225, v229\n" \
  "v_mfma_f32_16x16x32_bf16 v[236:239], v[216:219], v[136:139], v[236:239]\n" \
  "v_cvt_pk_bf16_f32 v226, v226, v230\n" \
  "v_cvt_pk_bf16_f32 v227, v227, v231\n" \
  "s_waitcnt lgkmcnt(1)\n" \
  "v_mfma_f32_16x16x32_bf16 v[232:235], v[196:199], v[240:243], v[232:235]\n" \
  "s_add_u32 %[cnt], %[so], 0*%c[js]+0*%c[ss]\n" \
  "buffer_store_dwordx4 v[224:227], v251, %[rs], %[cnt] offen sc1\n" \
  "v_mfma_f32_16x16x32_bf16 v[236:239], v[212:215], v[240:243], v[236:239]\n" \
  "s_waitcnt lgkmcnt(0)\n" \
  "v_mfma_f32_16x16x32_bf16 v[232:235], v[204:207], v[244:247], v[232:235]\n" \
  "v_mfma_f32_16x16x32_bf16 v[236:239], v[220:223], v[244:247], v[236:239]\n" \
  "ds_read_b128 v[240:243], v250 offset:1024\n" \
  "ds_read_b128 v[244:247], v250 offset:17408\n" \
  "v_mfma_f32_16x16x32_bf16 v[224:227], v[192:195], v[64:67], 0\n" \
  "v_mfma_f32_16x16x32_bf16 v[228:231], v[208:211], v[64:67], 0\n" \
  "v_mfma_f32_16x16x32_bf16 v[224:227], v[196:199], v[68:71], v[224:227]\n" \
  "v_cvt_pk_bf16_f32 v232, v232, v236\n" \
  "v_cvt_pk_bf16_f32 v233, v233, v237\n" \
  "v_mfma_f32_16x16x32_bf16 v[228:231], v[212:215], v[68:71], v[228:231]\n" \
  "v_cvt_pk_bf16_f32 v234, v234, v238\n" \
  "v_cvt_pk_bf16_f32 v235, v235, v239\n" \
  "v_mfma_f32_16x16x32_bf16 v[224:227], v[200:203], v[72:75], v[224:227]\n" \
  "s_add_u32 %[cnt], %[so], 4*%c[js]+0*%c[ss]\n" \
  "buffer_store_dwordx4 v[232:235], v251, %[rs], %[cnt] offen sc1\n" \
  "v_mfma_f32_16x16x32_bf16 v[228:231], v[216:219], v[72:75], v[228:231]\n" \
  "v_mfma_f32_16x16x32_bf16 v[224:227], v[204:207], v[76:79], v[224:227]\n" \
  "v_mfma_f32_16x16x32_bf16 v[228:231], v[220:223], v[76:79], v[228:231]\n" \
  "v_mfma_f32_16x16x32_bf16 v[232:235], v[192:195], v[132:135], 0\n" \
  "v_mfma_f32_16x16x32_bf16 v[236:239], v[208:211], v[132:135], 0\n" \
  "v_mfma_f32_16x16x32_bf16 v[232:235], v[200:203], v[140:143], v[232:235]\n" \
  "v_cvt_pk_bf16_f32 v224, v224, v228\n" \
  "v_cvt_pk_bf16_f32 v225, v225, v229\n" \
  "v_mfma_f32_16x16x32_bf16 v[236:239], v[216:219], v[140:143], v[236:239]\n" \
  "v_cvt_pk_bf16_f32 v226, v226, v230\n" \
  "v_cvt_pk_bf16_f32 v227, v227, v231\n" \
  "s_waitcnt lgkmcnt(1)\n" \
  "v_mfma_f32_16x16x32_bf16 v[232:235], v[196:199], v[240:243], v[232:235]\n" \
  "s_add_u32 %[cnt], %[so], 0*%c[js]+1*%c[ss]\n" \
  "buffer_store_dwordx4 v[224:227], v251, %[rs], %[cnt] offen sc1\n" \
  "v_mfma_f32_16x16x32_bf16 v[236:239], v[212:215], v[240:243], v[236:239]\n" \
  "s_waitcnt lgkmcnt(0)\n" \
  "v_mfma_f32_16x16x32_bf16 v[232:235], v[204:207], v[244:247], v[232:235]\n" \
  "v_mfma_f32_16x16x32_bf16 v[236:239], v[220:223], v[244:247], v[236:239]\n" \
  "ds_read_b128 v[240:243], v250 offset:32768\n" \
  "ds_read_b128 v[244:247], v250 offset:49152\n" \
  "v_mfma_f32_16x16x32_bf16 v[224:227], v[192:195], v[16:19], 0\n" \
  "v_mfma_f32_16x16x32_bf16 v[228:231], v[208:211], v[16:19], 0\n" \
  "v_mfma_f32_16x16x32_bf16 v[224:227], v[196:199], v[20:23], v[224:227]\n" \
  "v_cvt_pk_bf16_f32 v232, v232, v236\n" \
  "v_cvt_pk_bf16_f32 v233, v233, v237\n" \
  "v_mfma_f32_16x16x32_bf16 v[228:231], v[212:215], v[20:23], v[228:231]\n" \
  "v_cvt_pk_bf16_f32 v234, v234, v238\n" \
  "v_cvt_pk_bf16_f32 v235, v235, v239\n" \
  "v_mfma_f32_16x16x32_bf16 v[224:227], v[200:203], v[24:27], v[224:227]\n" \
  "s_add_u32 %[cnt], %[so], 4*%c[js]+1*%c[ss]\n" \
  "buffer_store_dwordx4 v[232:235], v251, %[rs], %[cnt] offen sc1\n" \
  "v_mfma_f32_16x16x32_bf16 v[228:231], v[216:219], v[24:27], v[228:231]\n" \
  "v_mfma_f32_16x16x32_bf16 v[224:227], v[204:207], v[28:31], v[224:227]\n" \
  "v_mfma_f32_16x16x32_bf16 v[228:231], v[220:223], v[28:31], v[228:231]\n" \
  "v_mfma_f32_16x16x32_bf16 v[232:235], v[192:195], v[144:147], 0\n" \
  "v_mfma_f32_16x16x32_bf16 v[236:239], v[208:211], v[144:147], 0\n" \
  "v_mfma_f32_16x16x32_bf16 v[232:235], v[200:203], v[152:155], v[232:235]\n" \
  "v_cvt_pk_bf16_f32 v224, v224, v228\n" \
  "v_cvt_pk_bf16_f32 v225, v225, v229\n" \
  "v_mfma_f32_16x16x32_bf16 v[236:239], v[216:219], v[152:155], v[236:239]\n" \
  "v_cvt_pk_bf16_f32 v226, v226, v230\n" \
  "v_cvt_pk_bf16_f32 v227, v227, v231\n" \
  "s_waitcnt lgkmcnt(1)\n" \
  "v_mfma_f32_16x16x32_bf16 v[232:235], v[196:199], v[240:243], v[232:235]\n" \
  "s_add_u32 %[cnt], %[so2], 1*%c[js]+0*%c[ss]\n" \
  "buffer_store_dwordx4 v[224:227], v251, %[rs], %[cnt] offen sc1\n" \
  "v_mfma_f32_16x16x32_bf16 v[236:239], v[212:215], v[240:243], v[236:239]\n" \
  "s_waitcnt lgkmcnt(0)\n" \
  "v_mfma_f32_16x16x32_bf16 v[232:235], v[204:207], v[244:247], v[232:235]\n" \
  "v_mfma_f32_16x16x32_bf16 v[236:239], v[220:223], v[244:247], v[236:239]\n" \
  "ds_read_b128 v[240:243], v250 offset:33792\n" \
  "ds_read_b128 v[244:247], v250 offset:50176\n" \
  "s_cmp_eq_u32 %[ob], 0\n" \
  "s_cbranch_scc1 .Lo6_%=\n" \
  "s_barrier\n" \
  ".Lo6_%=:\n" \
  "v_mfma_f32_16x16x32_bf16 v[224:227], v[192:195], v[80:83], 0\n" \
  "v_mfma_f32_16x16x32_bf16 v[228:231], v[208:211], v[80:83], 0\n" \
  "v_mfma_f32_16x16x32_bf16 v[224:227], v[196:199], v[84:87], v[224:227]\n" \
  "v_cvt_pk_bf16_f32 v232, v232, v236\n" \
  "v_cvt_pk_bf16_f32 v233, v233, v237\n" \
  "v_mfma_f32_16x16x32_bf16 v[228:231], v[212:215], v[84:87], v[228:231]\n" \
  "v_cvt_pk_bf16_f32 v234, v234, v238\n" \
  "v_cvt_pk_bf16_f32 v235, v235, v239\n" \
  "v_mfma_f32_16x16x32_bf16 v[224:227], v[200:203], v[88:91], v[224:227]\n" \
  "s_add_u32 %[cnt], %[so2], 5*%c[js]+0*%c[ss]\n" \
  "buffer_store_dwordx4 v[232:235], v251, %[rs], %[cnt] offen sc1\n" \
  "v_mfma_f32_16x16x32_bf16 v[228:231], v[216:219], v[88:91], v[228:231]\n" \
  "v_mfma_f32_16x16x32_bf16 v[224:227], v[204:207], v[92:95], v[224:227]\n" \
  "v_mfma_f32_16x16x32_bf16 v[228:231], v[220:223], v[92:95], v[228:231]\n" \
  "v_mfma_f32_16x16x32_bf16 v[232:235], v[192:195], v[148:151], 0\n" \
  "v_mfma_f32_16x16x32_bf16 v[236:239], v[208:211], v[148:151], 0\n" \
  "v_mfma_f32_16x16x32_bf16 v[232:235], v[200:203], v[156:159], v[232:235]\n" \
  "v_cvt_pk_bf16_f32 v224, v224, v228\n" \
  "v_cvt_pk_bf16_f32 v225, v225, v229\n" \
  "v_mfma_f32_16x16x32_bf16 v[236:239], v[216:219], v[156:159], v[236:239]\n" \
  "v_cvt_pk_bf16_f32 v226, v226, v230\n" \
  "v_cvt_pk_bf16_f32 v227, v227, v231\n" \
  "s_waitcnt lgkmcnt(1)\n" \
  "v_mfma_f32_16x16x32_bf16 v[232:235], v[196:199], v[240:243], v[232:235]\n" \
  "s_add_u32 %[cnt], %[so2], 1*%c[js]+1*%c[ss]\n" \
  "buffer_store_dwordx4 v[224:227], v251, %[rs], %[cnt] offen sc1\n" \
  "v_mfma_f32_16x16x32_bf16 v[236:239], v[212:215], v[240:243], v[236:239]\n" \
  "s_waitcnt lgkmcnt(0)\n" \
  "v_mfma_f32_16x16x32_bf16 v[232:235], v[204:207], v[244:247], v[232:235]\n" \
  "v_mfma_f32_16x16x32_bf16 v[236:239], v[220:223], v[244:247], v[236:239]\n" \
  "ds_read_b128 v[240:243], v249 offset:0\n" \
  "ds_read_b128 v[244:247], v249 offset:16384\n" \
  "s_cmp_eq_u32 %[ob], 0\n" \
  "s_cbranch_scc1 .Lo8_%=\n" \
  "s_barrier\n" \
  ".Lo8_%=:\n" \
  "v_mfma_f32_16x16x32_bf16 v[224:227], v[192:195], v[32:35], 0\n" \
  "v_mfma_f32_16x16x32_bf16 v[228:231], v[208:211], v[32:35], 0\n" \
  "v_mfma_f32_16x16x32_bf16 v[224:227], v[196:199], v[36:39], v[224:227]\n" \
  "v_cvt_pk_bf16_f32 v232, v232, v236\n" \
  "v_cvt_pk_bf16_f32 v233, v233, v237\n" \
  "v_mfma_f32_16x16x32_bf16 v[228:231], v[212:215], v[36:39], v[228:231]\n" \
  "v_cvt_pk_bf16_f32 v234, v234, v238\n" \
  "v_cvt_pk_bf16_f32 v235, v235, v239\n" \
  "v_mfma_f32_16x16x32_bf16 v[224:227], v[200:203], v[40:43], v[224:227]\n" \
  "s_add_u32 %[cnt], %[so2], 5*%c[js]+1*%c[ss]\n" \
  "buffer_store_dwordx4 v[232:235], v251, %[rs], %[cnt] offen sc1\n" \
  "v_mfma_f32_16x16x32_bf16 v[228:231], v[216:219], v[40:43], v[228:231]\n" \
  "v_mfma_f32_16x16x32_bf16 v[224:227], v[204:207], v[44:47], v[224:227]\n" \
  "v_mfma_f32_16x16x32_bf16 v[228:231], v[220:223], v[44:47], v[228:231]\n" \
  "v_mfma_f32_16x16x32_bf16 v[232:235], v[192:195], v[160:163], 0\n" \
  "v_mfma_f32_16x16x32_bf16 v[236:239], v[208:211], v[160:163], 0\n" \
  "v_mfma_f32_16x16x32_bf16 v[232:235], v[200:203], v[168:171], v[232:235]\n" \
  "v_cvt_pk_bf16_f32 v224, v224, v228\n" \
  "v_cvt_pk_bf16_f32 v225, v225, v229\n" \
  "v_mfma_f32_16x16x32_bf16 v[236:239], v[216:219], v[168:171], v[236:239]\n" \
  "v_cvt_pk_bf16_f32 v226, v226, v230\n" \
  "v_cvt_pk_bf16_f32 v227, v227, v231\n" \
  "s_waitcnt lgkmcnt(1)\n" \
  "v_mfma_f32_16x16x32_bf16 v[232:235], v[196:199], v[240:243], v[232:235]\n" \
  "s_add_u32 %[cnt], %[so], 2*%c[js]+0*%c[ss]\n" \
  "buffer_store_dwordx4 v[224:227], v251, %[rs], %[cnt] offen sc1\n" \
  "v_mfma_f32_16x16x32_bf16 v[236:239], v[212:215], v[240:243], v[236:239]\n" \
  "s_waitcnt lgkmcnt(0)\n" \
  "v_mfma_f32_16x16x32_bf16 v[232:235], v[204:207], v[244:247], v[232:235]\n" \
  "v_mfma_f32_16x16x32_bf16 v[236:239], v[220:223], v[244:247], v[236:239]\n" \
  "ds_read_b128 v[240:243], v249 offset:1024\n" \
  "ds_read_b128 v[244:247], v249 offset:17408\n" \
  "v_mfma_f32_16x16x32_bf16 v[224:227], v[192:195], v[96:99], 0\n" \
  "v_mfma_f32_16x16x32_bf16 v[228:231], v[208:211], v[96:99], 0\n" \
  "v_mfma_f32_16x16x32_bf16 v[224:227], v[196:199], v[100:103], v[224:227]\n" \
  "v_cvt_pk_bf16_f32 v232, v232, v236\n" \
  "v_cvt_pk_bf16_f32 v233, v233, v237\n" \
  "v_mfma_f32_16x16x32_bf16 v[228:231], v[212:215], v[100:103], v[228:231]\n" \
  "v_cvt_pk_bf16_f32 v234, v234, v238\n" \
  "v_cvt_pk_bf16_f32 v235, v235, v239\n" \
  "v_mfma_f32_16x16x32_bf16 v[224:227], v[200:203], v[104:107], v[224:227]\n" \
  "s_add_u32 %[cnt], %[so], 6*%c[js]+0*%c[ss]\n" \
  "buffer_store_dwordx4 v[232:235], v251, %[rs], %[cnt] offen sc1\n" \
  "v_mfma_f32_16x16x32_bf16 v[228:231], v[216:219], v[104:107], v[228:231]\n" \
  "v_mfma_f32_16x16x32_bf16 v[224:227], v[204:207], v[108:111], v[224:227]\n" \
  "v_mfma_f32_16x16x32_bf16 v[228:231], v[220:223], v[108:111], v[228:231]\n" \
  "v_mfma_f32_16x16x32_bf16 v[232:235], v[192:195], v[164:167], 0\n" \
  "v_mfma_f32_16x16x32_bf16 v[236:239], v[208:211], v[164:167], 0\n" \
  "v_mfma_f32_16x16x32_bf16 v[232:235], v[200:203], v[172:175], v[232:235]\n" \
  "v_cvt_pk_bf16_f32 v224, v224, v228\n" \
  "v_cvt_pk_bf16_f32 v225, v225, v229\n" \
  "v_mfma_f32_16x16x32_bf16 v[236:239], v[216:219], v[172:175], v[236:239]\n" \
  "v_cvt_pk_bf16_f32 v226, v226, v230\n" \
  "v_cvt_pk_bf16_f32 v227, v227, v231\n" \
  "s_waitcnt lgkmcnt(1)\n" \
  "v_mfma_f32_16x16x32_bf16 v[232:235], v[196:199], v[240:243], v[232:235]\n" \
  "s_add_u32 %[cnt], %[so], 2*%c[js]+1*%c[ss]\n" \
  "buffer_store_dwordx4 v[224:227], v251, %[rs], %[cnt] offen sc1\n" \
  "v_mfma_f32_16x16x32_bf16 v[236:239], v[212:215], v[240:243], v[236:239]\n" \
  "s_waitcnt lgkmcnt(0)\n" \
  "v_mfma_f32_16x16x32_bf16 v[232:235], v[204:207], v[244:247], v[232:235]\n" \
  "v_mfma_f32_16x16x32_bf16 v[236:239], v[220:223], v[244:247], v[236:239]\n" \
  "ds_read_b128 v[240:243], v249 offset:32768\n" \
  "ds_read_b128 v[244:247], v249 offset:49152\n" \
  "s_cmp_eq_u32 %[ob], 0\n" \
  "s_cbranch_scc1 .Lo12_%=\n" \
  "s_barrier\n" \
  ".Lo12_%=:\n" \
  "v_mfma_f32_16x16x32_bf16 v[224:227], v[192:195], v[48:51], 0\n" \
  "v_mfma_f32_16x16x32_bf16 v[228:231], v[208:211], v[48:51], 0\n" \
  "v_mfma_f32_16x16x32_bf16 v[224:227], v[196:199], v[52:55], v[224:227]\n" \
  "v_cvt_pk_bf16_f32 v232, v232, v236\n" \
  "v_cvt_pk_bf16_f32 v233, v233, v237\n" \
  "v_mfma_f32_16x16x32_bf16 v[228:231], v[212:215], v[52:55], v[228:231]\n" \
  "v_cvt_pk_bf16_f32 v234, v234, v238\n" \
  "v_cvt_pk_bf16_f32 v235, v235, v239\n" \
  "v_mfma_f32_16x16x32_bf16 v[224:227], v[200:203], v[56:59], v[224:227]\n" \
  "s_add_u32 %[cnt], %[so], 6*%c[js]+1*%c[ss]\n" \
  "buffer_store_dwordx4 v[232:235], v251, %[rs], %[cnt] offen sc1\n" \
  "v_mfma_f32_16x16x32_bf16 v[228:231], v[216:219], v[56:59], v[228:231]\n" \
  "v_mfma_f32_16x16x32_bf16 v[224:227], v[204:207], v[60:63], v[224:227]\n" \
  "v_mfma_f32_16x16x32_bf16 v[228:231], v[220:223], v[60:63], v[228:231]\n" \
  "v_mfma_f32_16x16x32_bf16 v[232:235], v[192:195], v[176:179], 0\n" \
  "v_mfma_f32_16x16x32_bf16 v[236:239], v[208:211], v[176:179], 0\n" \
  "v_mfma_f32_16x16x32_bf16 v[232:235], v[200:203], v[184:187], v[232:235]\n" \
  "v_cvt_pk_bf16_f32 v224, v224, v228\n" \
  "v_cvt_pk_bf16_f32 v225, v225, v229\n" \
  "v_mfma_f32_16x16x32_bf16 v[236:239], v[216:219], v[184:187], v[236:239]\n" \
  "v_cvt_pk_bf16_f32 v226, v226, v230\n" \
  "v_cvt_pk_bf16_f32 v227, v227, v231\n" \
  "s_waitcnt lgkmcnt(1)\n" \
  "v_mfma_f32_16x16x32_bf16 v[232:235], v[196:199], v[240:243], v[232:235]\n" \
  "s_add_u32 %[cnt], %[so2], 3*%c[js]+0*%c[ss]\n" \
  "buffer_store_dwordx4 v[224:227], v251, %[rs], %[cnt] offen sc1\n" \
  "v_mfma_f32_16x16x32_bf16 v[236:239], v[212:215], v[240:243], v[236:239]\n" \
  "s_waitcnt lgkmcnt(0)\n" \
  "v_mfma_f32_16x16x32_bf16 v[232:235], v[204:207], v[244:247], v[232:235]\n" \
  "v_mfma_f32_16x16x32_bf16 v[236:239], v[220:223], v[244:247], v[236:239]\n" \
  "ds_read_b128 v[240:243], v249 offset:33792\n" \
  "ds_read_b128 v[244:247], v249 offset:50176\n" \
  "v_mfma_f32_16x16x32_bf16 v[224:227], v[192:195], v[112:115], 0\n" \
  "v_mfma_f32_16x16x32_bf16 v[228:231], v[208:211], v[112:115], 0\n" \
  "v_mfma_f32_16x16x32_bf16 v[224:227], v[196:199], v[116:119], v[224:227]\n" \
  "v_cvt_pk_bf16_f32 v232, v232, v236\n" \
  "v_cvt_pk_bf16_f32 v233, v233, v237\n" \
  "v_mfma_f32_16x16x32_bf16 v[228:231], v[212:215], v[116:119], v[228:231]\n" \
  "v_cvt_pk_bf16_f32 v234, v234, v238\n" \
  "v_cvt_pk_bf16_f32 v235, v235, v239\n" \
  "v_mfma_f32_16x16x32_bf16 v[224:227], v[200:203], v[120:123], v[224:227]\n" \
  "s_add_u32 %[cnt], %[so2], 7*%c[js]+0*%c[ss]\n" \
  "buffer_store_dwordx4 v[232:235], v251, %[rs], %[cnt] offen sc1\n" \
  "v_mfma_f32_16x16x32_bf16 v[228:231], v[216:219], v[120:123], v[228:231]\n" \
  "v_mfma_f32_16x16x32_bf16 v[224:227], v[204:207], v[124:127], v[224:227]\n" \
  "v_mfma_f32_16x16x32_bf16 v[228:231], v[220:223], v[124:127], v[228:231]\n" \
  "v_mfma_f32_16x16x32_bf16 v[232:235], v[192:195], v[180:183], 0\n" \
  "v_mfma_f32_16x16x32_bf16 v[236:239], v[208:211], v[180:183], 0\n" \
  "v_mfma_f32_16x16x32_bf16 v[232:235], v[200:203], v[188:191], v[232:235]\n" \
  "v_cvt_pk_bf16_f32 v224, v224, v228\n" \
  "v_cvt_pk_bf16_f32 v225, v225, v229\n" \
  "v_mfma_f32_16x16x32_bf16 v[236:239], v[216:219], v[188:191], v[236:239]\n" \
  "v_cvt_pk_bf16_f32 v226, v226, v230\n" \
  "v_cvt_pk_bf16_f32 v227, v227, v231\n" \
  "s_waitcnt lgkmcnt(1)\n" \
  "v_mfma_f32_16x16x32_bf16 v[232:235], v[196:199], v[240:243], v[232:235]\n" \
  "s_add_u32 %[cnt], %[so2], 3*%c[js]+1*%c[ss]\n" \
  "buffer_store_dwordx4 v[224:227], v251, %[rs], %[cnt] offen sc1\n" \
  "v_mfma_f32_16x16x32_bf16 v[236:239], v[212:215], v[240:243], v[236:239]\n" \
  "s_waitcnt lgkmcnt(0)\n" \
  "v_mfma_f32_16x16x32_bf16 v[232:235], v[204:207], v[244:247], v[232:235]\n" \
  "v_mfma_f32_16x16x32_bf16 v[236:239], v[220:223], v[244:247], v[236:239]\n" \
  "s_nop 8\n" \
  "v_cvt_pk_bf16_f32 v232, v232, v236\n" \
  "v_cvt_pk_bf16_f32 v233, v233, v237\n" \
  "v_cvt_pk_bf16_f32 v234, v234, v238\n" \
  "v_cvt_pk_bf16_f32 v235, v235, v239\n" \
  "s_add_u32 %[cnt], %[so2], 7*%c[js]+1*%c[ss]\n" \
  "buffer_store_dwordx4 v[232:235], v251, %[rs], %[cnt] offen sc1\n" \
  "s_setprio 0\n" \
  "s_nop 1\n"
#define PRO0_0 \
  "s_cmp_lg_u32 %[hb], 0\n" \
  "s_cbranch_scc1 .Ls_%=\n" \
  "v_xor_b32 %[t0], 0x0, %[pb]\n" \
  "ds_read_b64_tr_b16 v[12:13], %[t0] offset:0\n" \
  "ds_read_b64_tr_b16 v[96:97], %[t0] offset:16384\n" \
  "v_xor_b32 %[t1], 0x20, %[pb]\n" \
  "ds_read_b64_tr_b16 v[28:29], %[t1] offset:0\n" \
  "ds_read_b64_tr_b16 v[144:145], %[t1] offset:16384\n" \
  "v_xor_b32 %[t0], 0x1000, %[pb]\n" \
  "ds_read_b64_tr_b16 v[0:1], %[t0] offset:0\n" \
  "ds_read_b64_tr_b16 v[192:193], %[t0] offset:16384\n" \
  "v_xor_b32 %[t1], 0x1020, %[pb]\n" \
  "ds_read_b64_tr_b16 v[48:49], %[t1] offset:0\n" \
  "ds_read_b64_tr_b16 v[200:201], %[t1] offset:16384\n" \
  "v_xor_b32 %[t0], 0x2000, %[pb]\n" \
  "ds_read_b64_tr_b16 v[16:17], %[t0] offset:0\n" \
  "ds_read_b64_tr_b16 v[208:209], %[t0] offset:16384\n" \
  "v_xor_b32 %[t1], 0x2020, %[pb]\n" \
  "ds_read_b64_tr_b16 v[64:65], %[t1] offset:0\n" \
  "ds_read_b64_tr_b16 v[216:217], %[t1] offset:16384\n" \
  "v_xor_b32 %[t0], 0x3000, %[pb]\n" \
  "ds_read_b64_tr_b16 v[32:33], %[t0] offset:0\n" \
  "ds_read_b64_tr_b16 v[128:129], %[t0] offset:16384\n" \
  "v_xor_b32 %[t1], 0x3020, %[pb]\n" \
  "ds_read_b64_tr_b16 v[80:81], %[t1] offset:0\n" \
  "ds_read_b64_tr_b16 v[176:177], %[t1] offset:16384\n" \
  ".Ls_%=:\n" \
  "s_waitcnt lgkmcnt(0)\n"
#define PRO0_1 \
  "s_cmp_lg_u32 %[hb], 1\n" \
  "s_cbranch_scc1 .Ls_%=\n" \
  "v_xor_b32 %[t0], 0x8000, %[pb]\n" \
  "ds_read_b64_tr_b16 v[12:13], %[t0] offset:0\n" \
  "ds_read_b64_tr_b16 v[96:97], %[t0] offset:16384\n" \
  "v_xor_b32 %[t1], 0x8020, %[pb]\n" \
  "ds_read_b64_tr_b16 v[28:29], %[t1] offset:0\n" \
  "ds_read_b64_tr_b16 v[144:145], %[t1] offset:16384\n" \
  "v_xor_b32 %[t0], 0x9000, %[pb]\n" \
  "ds_read_b64_tr_b16 v[0:1], %[t0] offset:0\n" \
  "ds_read_b64_tr_b16 v[192:193], %[t0] offset:16384\n" \
  "v_xor_b32 %[t1], 0x9020, %[pb]\n" \
  "ds_read_b64_tr_b16 v[48:49], %[t1] offset:0\n" \
  "ds_read_b64_tr_b16 v[200:201], %[t1] offset:16384\n" \
  "v_xor_b32 %[t0], 0xa000, %[pb]\n" \
  "ds_read_b64_tr_b16 v[16:17], %[t0] offset:0\n" \
  "ds_read_b64_tr_b16 v[208:209], %[t0] offset:16384\n" \
  "v_xor_b32 %[t1], 0xa020, %[pb]\n" \
  "ds_read_b64_tr_b16 v[64:65], %[t1] offset:0\n" \
  "ds_read_b64_tr_b16 v[216:217], %[t1] offset:16384\n" \
  "v_xor_b32 %[t0], 0xb000, %[pb]\n" \
  "ds_read_b64_tr_b16 v[32:33], %[t0] offset:0\n" \
  "ds_read_b64_tr_b16 v[128:129], %[t0] offset:16384\n" \
  "v_xor_b32 %[t1], 0xb020, %[pb]\n" \
  "ds_read_b64_tr_b16 v[80:81], %[t1] offset:0\n" \
  "ds_read_b64_tr_b16 v[176:177], %[t1] offset:16384\n" \
  ".Ls_%=:\n" \
  "s_waitcnt lgkmcnt(0)\n"
#define PRO0_2 \
  "s_cmp_lg_u32 %[hb], 0\n" \
  "s_cbranch_scc1 .Ls_%=\n" \
  "v_xor_b32 %[t0], 0x10000, %[pb]\n" \
  "ds_read_b64_tr_b16 v[14:15], %[t0] offset:0\n" \
  "ds_read_b64_tr_b16 v[98:99], %[t0] offset:16384\n" \
  "v_xor_b32 %[t1], 0x10020, %[pb]\n" \
  "ds_read_b64_tr_b16 v[30:31], %[t1] offset:0\n" \
  "ds_read_b64_tr_b16 v[146:147], %[t1] offset:16384\n" \
  "v_xor_b32 %[t0], 0x11000, %[pb]\n" \
  "ds_read_b64_tr_b16 v[2:3], %[t0] offset:0\n" \
  "ds_read_b64_tr_b16 v[194:195], %[t0] offset:16384\n" \
  "v_xor_b32 %[t1], 0x11020, %[pb]\n" \
  "ds_read_b64_tr_b16 v[50:51], %[t1] offset:0\n" \
  "ds_read_b64_tr_b16 v[202:203], %[t1] offset:16384\n" \
  "v_xor_b32 %[t0], 0x12000, %[pb]\n" \
  "ds_read_b64_tr_b16 v[18:19], %[t0] offset:0\n" \
  "ds_read_b64_tr_b16 v[210:211], %[t0] offset:16384\n" \
  "v_xor_b32 %[t1], 0x12020, %[pb]\n" \
  "ds_read_b64_tr_b16 v[66:67], %[t1] offset:0\n" \
  "ds_read_b64_tr_b16 v[218:219], %[t1] offset:16384\n" \
  "v_xor_b32 %[t0], 0x13000, %[pb]\n" \
  "ds_read_b64_tr_b16 v[34:35], %[t0] offset:0\n" \
  "ds_read_b64_tr_b16 v[130:131], %[t0] offset:16384\n" \
  "v_xor_b32 %[t1], 0x13020, %[pb]\n" \
  "ds_read_b64_tr_b16 v[82:83], %[t1] offset:0\n" \
  "ds_read_b64_tr_b16 v[178:179], %[t1] offset:16384\n" \
  ".Ls_%=:\n" \
  "s_waitcnt lgkmcnt(0)\n"
#define PRO0_3 \
  "s_cmp_lg_u32 %[hb], 1\n" \
  "s_cbranch_scc1 .Ls_%=\n" \
  "v_xor_b32 %[t0], 0x18000, %[pb]\n" \
  "ds_read_b64_tr_b16 v[14:15], %[t0] offset:0\n" \
  "ds_read_b64_tr_b16 v[98:99], %[t0] offset:16384\n" \
  "v_xor_b32 %[t1], 0x18020, %[pb]\n" \
  "ds_read_b64_tr_b16 v[30:31], %[t1] offset:0\n" \
  "ds_read_b64_tr_b16 v[146:147], %[t1] offset:16384\n" \
  "v_xor_b32 %[t0], 0x19000, %[pb]\n" \
  "ds_read_b64_tr_b16 v[2:3], %[t0] offset:0\n" \
  "ds_read_b64_tr_b16 v[194:195], %[t0] offset:16384\n" \
  "v_xor_b32 %[t1], 0x19020, %[pb]\n" \
  "ds_read_b64_tr_b16 v[50:51], %[t1] offset:0\n" \
  "ds_read_b64_tr_b16 v[202:203], %[t1] offset:16384\n" \
  "v_xor_b32 %[t0], 0x1a000, %[pb]\n" \
  "ds_read_b64_tr_b16 v[18:19], %[t0] offset:0\n" \
  "ds_read_b64_tr_b16 v[210:211], %[t0] offset:16384\n" \
  "v_xor_b32 %[t1], 0x1a020, %[pb]\n" \
  "ds_read_b64_tr_b16 v[66:67], %[t1] offset:0\n" \
  "ds_read_b64_tr_b16 v[218:219], %[t1] offset:16384\n" \
  "v_xor_b32 %[t0], 0x1b000, %[pb]\n" \
  "ds_read_b64_tr_b16 v[34:35], %[t0] offset:0\n" \
  "ds_read_b64_tr_b16 v[130:131], %[t0] offset:16384\n" \
  "v_xor_b32 %[t1], 0x1b020, %[pb]\n" \
  "ds_read_b64_tr_b16 v[82:83], %[t1] offset:0\n" \
  "ds_read_b64_tr_b16 v[178:179], %[t1] offset:16384\n" \
  ".Ls_%=:\n" \
  "s_waitcnt lgkmcnt(0)\n"
#define PRO0_4 \
  "s_cmp_lg_u32 %[hb], 0\n" \
  "s_cbranch_scc1 .Ls_%=\n" \
  "v_xor_b32 %[t0], 0x20000, %[pb]\n" \
  "ds_read_b64_tr_b16 v[60:61], %[t0] offset:0\n" \
  "ds_read_b64_tr_b16 v[100:101], %[t0] offset:16384\n" \
  "v_xor_b32 %[t1], 0x20020, %[pb]\n" \
  "ds_read_b64_tr_b16 v[76:77], %[t1] offset:0\n" \
  "ds_read_b64_tr_b16 v[148:149], %[t1] offset:16384\n" \
  "v_xor_b32 %[t0], 0x21000, %[pb]\n" \
  "ds_read_b64_tr_b16 v[4:5], %[t0] offset:0\n" \
  "ds_read_b64_tr_b16 v[196:197], %[t0] offset:16384\n" \
  "v_xor_b32 %[t1], 0x21020, %[pb]\n" \
  "ds_read_b64_tr_b16 v[52:53], %[t1] offset:0\n" \
  "ds_read_b64_tr_b16 v[204:205], %[t1] offset:16384\n" \
  "v_xor_b32 %[t0], 0x22000, %[pb]\n" \
  "ds_read_b64_tr_b16 v[20:21], %[t0] offset:0\n" \
  "ds_read_b64_tr_b16 v[212:213], %[t0] offset:16384\n" \
  "v_xor_b32 %[t1], 0x22020, %[pb]\n" \
  "ds_read_b64_tr_b16 v[68:69], %[t1] offset:0\n" \
  "ds_read_b64_tr_b16 v[220:221], %[t1] offset:16384\n" \
  "v_xor_b32 %[t0], 0x23000, %[pb]\n" \
  "ds_read_b64_tr_b16 v[36:37], %[t0] offset:0\n" \
  "ds_read_b64_tr_b16 v[132:133], %[t0] offset:16384\n" \
  "v_xor_b32 %[t1], 0x23020, %[pb]\n" \
  "ds_read_b64_tr_b16 v[84:85], %[t1] offset:0\n" \
  "ds_read_b64_tr_b16 v[180:181], %[t1] offset:16384\n" \
  ".Ls_%=:\n" \
  "s_waitcnt lgkmcnt(0)\n"
#define PRO0_5 \
  "s_cmp_lg_u32 %[hb], 1\n" \
  "s_cbranch_scc1 .Ls_%=\n" \
  "v_xor_b32 %[t0], 0x0, %[pb]\n" \
  "ds_read_b64_tr_b16 v[60:61], %[t0] offset:0\n" \
  "ds_read_b64_tr_b16 v[100:101], %[t0] offset:16384\n" \
  "v_xor_b32 %[t1], 0x20, %[pb]\n" \
  "ds_read_b64_tr_b16 v[76:77], %[t1] offset:0\n" \
  "ds_read_b64_tr_b16 v[148:149], %[t1] offset:16384\n" \
  "v_xor_b32 %[t0], 0x1000, %[pb]\n" \
  "ds_read_b64_tr_b16 v[4:5], %[t0] offset:0\n" \
  "ds_read_b64_tr_b16 v[196:197], %[t0] offset:16384\n" \
  "v_xor_b32 %[t1], 0x1020, %[pb]\n" \
  "ds_read_b64_tr_b16 v[52:53], %[t1] offset:0\n" \
  "ds_read_b64_tr_b16 v[204:205], %[t1] offset:16384\n" \
  "v_xor_b32 %[t0], 0x2000, %[pb]\n" \
  "ds_read_b64_tr_b16 v[20:21], %[t0] offset:0\n" \
  "ds_read_b64_tr_b16 v[212:213], %[t0] offset:16384\n" \
  "v_xor_b32 %[t1], 0x2020, %[pb]\n" \
  "ds_read_b64_tr_b16 v[68:69], %[t1] offset:0\n" \
  "ds_read_b64_tr_b16 v[220:221], %[t1] offset:16384\n" \
  "v_xor_b32 %[t0], 0x3000, %[pb]\n" \
  "ds_read_b64_tr_b16 v[36:37], %[t0] offset:0\n" \
  "ds_read_b64_tr_b16 v[132:133], %[t0] offset:16384\n" \
  "v_xor_b32 %[t1], 0x3020, %[pb]\n" \
  "ds_read_b64_tr_b16 v[84:85], %[t1] offset:0\n" \
  "ds_read_b64_tr_b16 v[180:181], %[t1] offset:16384\n" \
  ".Ls_%=:\n" \
  "s_waitcnt lgkmcnt(0)\n"
#define PRO0_6 \
  "s_cmp_lg_u32 %[hb], 0\n" \
  "s_cbranch_scc1 .Ls_%=\n" \
  "v_xor_b32 %[t0], 0x8000, %[pb]\n" \
  "ds_read_b64_tr_b16 v[62:63], %[t0] offset:0\n" \
  "ds_read_b64_tr_b16 v[102:103], %[t0] offset:16384\n" \
  "v_xor_b32 %[t1], 0x8020, %[pb]\n" \
  "ds_read_b64_tr_b16 v[78:79], %[t1] offset:0\n" \
  "ds_read_b64_tr_b16 v[150:151], %[t1] offset:16384\n" \
  "v_xor_b32 %[t0], 0x9000, %[pb]\n" \
  "ds_read_b64_tr_b16 v[6:7], %[t0] offset:0\n" \
  "ds_read_b64_tr_b16 v[198:199], %[t0] offset:16384\n" \
  "v_xor_b32 %[t1], 0x9020, %[pb]\n" \
  "ds_read_b64_tr_b16 v[54:55], %[t1] offset:0\n" \
  "ds_read_b64_tr_b16 v[206:207], %[t1] offset:16384\n" \
  "v_xor_b32 %[t0], 0xa000, %[pb]\n" \
  "ds_read_b64_tr_b16 v[22:23], %[t0] offset:0\n" \
  "ds_read_b64_tr_b16 v[214:215], %[t0] offset:16384\n" \
  "v_xor_b32 %[t1], 0xa020, %[pb]\n" \
  "ds_read_b64_tr_b16 v[70:71], %[t1] offset:0\n" \
  "ds_read_b64_tr_b16 v[222:223], %[t1] offset:16384\n" \
  "v_xor_b32 %[t0], 0xb000, %[pb]\n" \
  "ds_read_b64_tr_b16 v[38:39], %[t0] offset:0\n" \
  "ds_read_b64_tr_b16 v[134:135], %[t0] offset:16384\n" \
  "v_xor_b32 %[t1], 0xb020, %[pb]\n" \
  "ds_read_b64_tr_b16 v[86:87], %[t1] offset:0\n" \
  "ds_read_b64_tr_b16 v[182:183], %[t1] offset:16384\n" \
  ".Ls_%=:\n" \
  "s_waitcnt lgkmcnt(0)\n"
#define PRO0_7 \
  "s_cmp_lg_u32 %[hb], 1\n" \
  "s_cbranch_scc1 .Ls_%=\n" \
  "v_xor_b32 %[t0], 0x10000, %[pb]\n" \
  "ds_read_b64_tr_b16 v[62:63], %[t0] offset:0\n" \
  "ds_read_b64_tr_b16 v[102:103], %[t0] offset:16384\n" \
  "v_xor_b32 %[t1], 0x10020, %[pb]\n" \
  "ds_read_b64_tr_b16 v[78:79], %[t1] offset:0\n" \
  "ds_read_b64_tr_b16 v[150:151], %[t1] offset:16384\n" \
  "v_xor_b32 %[t0], 0x11000, %[pb]\n" \
  "ds_read_b64_tr_b16 v[6:7], %[t0] offset:0\n" \
  "ds_read_b64_tr_b16 v[198:199], %[t0] offset:16384\n" \
  "v_xor_b32 %[t1], 0x11020, %[pb]\n" \
  "ds_read_b64_tr_b16 v[54:55], %[t1] offset:0\n" \
  "ds_read_b64_tr_b16 v[206:207], %[t1] offset:16384\n" \
  "v_xor_b32 %[t0], 0x12000, %[pb]\n" \
  "ds_read_b64_tr_b16 v[22:23], %[t0] offset:0\n" \
  "ds_read_b64_tr_b16 v[214:215], %[t0] offset:16384\n" \
  "v_xor_b32 %[t1], 0x12020, %[pb]\n" \
  "ds_read_b64_tr_b16 v[70:71], %[t1] offset:0\n" \
  "ds_read_b64_tr_b16 v[222:223], %[t1] offset:16384\n" \
  "v_xor_b32 %[t0], 0x13000, %[pb]\n" \
  "ds_read_b64_tr_b16 v[38:39], %[t0] offset:0\n" \
  "ds_read_b64_tr_b16 v[134:135], %[t0] offset:16384\n" \
  "v_xor_b32 %[t1], 0x13020, %[pb]\n" \
  "ds_read_b64_tr_b16 v[86:87], %[t1] offset:0\n" \
  "ds_read_b64_tr_b16 v[182:183], %[t1] offset:16384\n" \
  ".Ls_%=:\n" \
  "s_waitcnt lgkmcnt(0)\n"
#define PRO0_8 \
  "s_cmp_lg_u32 %[hb], 0\n" \
  "s_cbranch_scc1 .Ls_%=\n" \
  "v_xor_b32 %[t0], 0x18000, %[pb]\n" \
  "ds_read_b64_tr_b16 v[224:225], %[t0] offset:0\n" \
  "ds_read_b64_tr_b16 v[104:105], %[t0] offset:16384\n" \
  "v_xor_b32 %[t1], 0x18020, %[pb]\n" \
  "ds_read_b64_tr_b16 v[232:233], %[t1] offset:0\n" \
  "ds_read_b64_tr_b16 v[152:153], %[t1] offset:16384\n" \
  "v_xor_b32 %[t0], 0x19000, %[pb]\n" \
  "ds_read_b64_tr_b16 v[8:9], %[t0] offset:0\n" \
  "ds_read_b64_tr_b16 v[112:113], %[t0] offset:16384\n" \
  "v_xor_b32 %[t1], 0x19020, %[pb]\n" \
  "ds_read_b64_tr_b16 v[56:57], %[t1] offset:0\n" \
  "ds_read_b64_tr_b16 v[160:161], %[t1] offset:16384\n" \
  "v_xor_b32 %[t0], 0x1a000, %[pb]\n" \
  "ds_read_b64_tr_b16 v[24:25], %[t0] offset:0\n" \
  "ds_read_b64_tr_b16 v[120:121], %[t0] offset:16384\n" \
  "v_xor_b32 %[t1], 0x1a020, %[pb]\n" \
  "ds_read_b64_tr_b16 v[72:73], %[t1] offset:0\n" \
  "ds_read_b64_tr_b16 v[168:169], %[t1] offset:16384\n" \
  "v_xor_b32 %[t0], 0x1b000, %[pb]\n" \
  "ds_read_b64_tr_b16 v[40:41], %[t0] offset:0\n" \
  "ds_read_b64_tr_b16 v[136:137], %[t0] offset:16384\n" \
  "v_xor_b32 %[t1], 0x1b020, %[pb]\n" \
  "ds_read_b64_tr_b16 v[88:89], %[t1] offset:0\n" \
  "ds_read_b64_tr_b16 v[184:185], %[t1] offset:16384\n" \
  ".Ls_%=:\n" \
  "s_waitcnt lgkmcnt(0)\n"
#define PRO0_9 \
  "s_cmp_lg_u32 %[hb], 1\n" \
  "s_cbranch_scc1 .Ls_%=\n" \
  "v_xor_b32 %[t0], 0x20000, %[pb]\n" \
  "ds_read_b64_tr_b16 v[224:225], %[t0] offset:0\n" \
  "ds_read_b64_tr_b16 v[104:105], %[t0] offset:16384\n" \
  "v_xor_b32 %[t1], 0x20020, %[pb]\n" \
  "ds_read_b64_tr_b16 v[232:233], %[t1] offset:0\n" \
  "ds_read_b64_tr_b16 v[152:153], %[t1] offset:16384\n" \
  "v_xor_b32 %[t0], 0x21000, %[pb]\n" \
  "ds_read_b64_tr_b16 v[8:9], %[t0] offset:0\n" \
  "ds_read_b64_tr_b16 v[112:113], %[t0] offset:16384\n" \
  "v_xor_b32 %[t1], 0x21020, %[pb]\n" \
  "ds_read_b64_tr_b16 v[56:57], %[t1] offset:0\n" \
  "ds_read_b64_tr_b16 v[160:161], %[t1] offset:16384\n" \
  "v_xor_b32 %[t0], 0x22000, %[pb]\n" \
  "ds_read_b64_tr_b16 v[24:25], %[t0] offset:0\n" \
  "ds_read_b64_tr_b16 v[120:121], %[t0] offset:16384\n" \
  "v_xor_b32 %[t1], 0x22020, %[pb]\n" \
  "ds_read_b64_tr_b16 v[72:73], %[t1] offset:0\n" \
  "ds_read_b64_tr_b16 v[168:169], %[t1] offset:16384\n" \
  "v_xor_b32 %[t0], 0x23000, %[pb]\n" \
  "ds_read_b64_tr_b16 v[40:41], %[t0] offset:0\n" \
  "ds_read_b64_tr_b16 v[136:137], %[t0] offset:16384\n" \
  "v_xor_b32 %[t1], 0x23020, %[pb]\n" \
  "ds_read_b64_tr_b16 v[88:89], %[t1] offset:0\n" \
  "ds_read_b64_tr_b16 v[184:185], %[t1] offset:16384\n" \
  ".Ls_%=:\n" \
  "s_waitcnt lgkmcnt(0)\n"
#define PRO0_10 \
  "s_cmp_lg_u32 %[hb], 0\n" \
  "s_cbranch_scc1 .Ls_%=\n" \
  "v_xor_b32 %[t0], 0x0, %[pb]\n" \
  "ds_read_b64_tr_b16 v[226:227], %[t0] offset:0\n" \
  "ds_read_b64_tr_b16 v[106:107], %[t0] offset:16384\n" \
  "v_xor_b32 %[t1], 0x20, %[pb]\n" \
  "ds_read_b64_tr_b16 v[234:235], %[t1] offset:0\n" \
  "ds_read_b64_tr_b16 v[154:155], %[t1] offset:16384\n" \
  "v_xor_b32 %[t0], 0x1000, %[pb]\n" \
  "ds_read_b64_tr_b16 v[10:11], %[t0] offset:0\n" \
  "ds_read_b64_tr_b16 v[114:115], %[t0] offset:16384\n" \
  "v_xor_b32 %[t1], 0x1020, %[pb]\n" \
  "ds_read_b64_tr_b16 v[58:59], %[t1] offset:0\n" \
  "ds_read_b64_tr_b16 v[162:163], %[t1] offset:16384\n" \
  "v_xor_b32 %[t0], 0x2000, %[pb]\n" \
  "ds_read_b64_tr_b16 v[26:27], %[t0] offset:0\n" \
  "ds_read_b64_tr_b16 v[122:123], %[t0] offset:16384\n" \
  "v_xor_b32 %[t1], 0x2020, %[pb]\n" \
  "ds_read_b64_tr_b16 v[74:75], %[t1] offset:0\n" \
  "ds_read_b64_tr_b16 v[170:171], %[t1] offset:16384\n" \
  "v_xor_b32 %[t0], 0x3000, %[pb]\n" \
  "ds_read_b64_tr_b16 v[42:43], %[t0] offset:0\n" \
  "ds_read_b64_tr_b16 v[138:139], %[t0] offset:16384\n" \
  "v_xor_b32 %[t1], 0x3020, %[pb]\n" \
  "ds_read_b64_tr_b16 v[90:91], %[t1] offset:0\n" \
  "ds_read_b64_tr_b16 v[186:187], %[t1] offset:16384\n" \
  ".Ls_%=:\n" \
  "s_waitcnt lgkmcnt(0)\n"
#define PRO0_11 \
  "s_cmp_lg_u32 %[hb], 1\n" \
  "s_cbranch_scc1 .Ls_%=\n" \
  "v_xor_b32 %[t0], 0x8000, %[pb]\n" \
  "ds_read_b64_tr_b16 v[226:227], %[t0] offset:0\n" \
  "ds_read_b64_tr_b16 v[106:107], %[t0] offset:16384\n" \
  "v_xor_b32 %[t1], 0x8020, %[pb]\n" \
  "ds_read_b64_tr_b16 v[234:235], %[t1] offset:0\n" \
  "ds_read_b64_tr_b16 v[154:155], %[t1] offset:16384\n" \
  "v_xor_b32 %[t0], 0x9000, %[pb]\n" \
  "ds_read_b64_tr_b16 v[10:11], %[t0] offset:0\n" \
  "ds_read_b64_tr_b16 v[114:115], %[t0] offset:16384\n" \
  "v_xor_b32 %[t1], 0x9020, %[pb]\n" \
  "ds_read_b64_tr_b16 v[58:59], %[t1] offset:0\n" \
  "ds_read_b64_tr_b16 v[162:163], %[t1] offset:16384\n" \
  "v_xor_b32 %[t0], 0xa000, %[pb]\n" \
  "ds_read_b64_tr_b16 v[26:27], %[t0] offset:0\n" \
  "ds_read_b64_tr_b16 v[122:123], %[t0] offset:16384\n" \
  "v_xor_b32 %[t1], 0xa020, %[pb]\n" \
  "ds_read_b64_tr_b16 v[74:75], %[t1] offset:0\n" \
  "ds_read_b64_tr_b16 v[170:171], %[t1] offset:16384\n" \
  "v_xor_b32 %[t0], 0xb000, %[pb]\n" \
  "ds_read_b64_tr_b16 v[42:43], %[t0] offset:0\n" \
  "ds_read_b64_tr_b16 v[138:139], %[t0] offset:16384\n" \
  "v_xor_b32 %[t1], 0xb020, %[pb]\n" \
  "ds_read_b64_tr_b16 v[90:91], %[t1] offset:0\n" \
  "ds_read_b64_tr_b16 v[186:187], %[t1] offset:16384\n" \
  ".Ls_%=:\n" \
  "s_waitcnt lgkmcnt(0)\n"
#define PRO0_12 \
  "v_add_u32 %[t0], 0x8000, %[b0]\n" \
  "ds_write_b128 %[t0], v[12:15]\n" \
  "ds_write_b128 %[t0], v[60:63] offset:1024\n" \
  "v_add_u32 %[t0], 0xc000, %[b0]\n" \
  "ds_write_b128 %[t0], v[28:31]\n" \
  "ds_write_b128 %[t0], v[76:79] offset:1024\n" \
  "s_cmp_lg_u32 %[hb], 0\n" \
  "s_cbranch_scc1 .Ls_%=\n" \
  "v_xor_b32 %[t0], 0x10000, %[pb]\n" \
  "ds_read_b64_tr_b16 v[228:229], %[t0] offset:0\n" \
  "ds_read_b64_tr_b16 v[108:109], %[t0] offset:16384\n" \
  "v_xor_b32 %[t1], 0x10020, %[pb]\n" \
  "ds_read_b64_tr_b16 v[236:237], %[t1] offset:0\n" \
  "ds_read_b64_tr_b16 v[156:157], %[t1] offset:16384\n" \
  "v_xor_b32 %[t0], 0x11000, %[pb]\n" \
  "ds_read_b64_tr_b16 v[12:13], %[t0] offset:0\n" \
  "ds_read_b64_tr_b16 v[116:117], %[t0] offset:16384\n" \
  "v_xor_b32 %[t1], 0x11020, %[pb]\n" \
  "ds_read_b64_tr_b16 v[60:61], %[t1] offset:0\n" \
  "ds_read_b64_tr_b16 v[164:165], %[t1] offset:16384\n" \
  "v_xor_b32 %[t0], 0x12000, %[pb]\n" \
  "ds_read_b64_tr_b16 v[28:29], %[t0] offset:0\n" \
  "ds_read_b64_tr_b16 v[124:125], %[t0] offset:16384\n" \
  "v_xor_b32 %[t1], 0x12020, %[pb]\n" \
  "ds_read_b64_tr_b16 v[76:77], %[t1] offset:0\n" \
  "ds_read_b64_tr_b16 v[172:173], %[t1] offset:16384\n" \
  "v_xor_b32 %[t0], 0x13000, %[pb]\n" \
  "ds_read_b64_tr_b16 v[44:45], %[t0] offset:0\n" \
  "ds_read_b64_tr_b16 v[140:141], %[t0] offset:16384\n" \
  "v_xor_b32 %[t1], 0x13020, %[pb]\n" \
  "ds_read_b64_tr_b16 v[92:93], %[t1] offset:0\n" \
  "ds_read_b64_tr_b16 v[188:189], %[t1] offset:16384\n" \
  ".Ls_%=:\n" \
  "s_waitcnt lgkmcnt(0)\n"
#define PRO0_13 \
  "v_add_u32 %[t0], 0x10000, %[b0]\n" \
  "ds_write_b128 %[t0], v[192:195]\n" \
  "ds_write_b128 %[t0], v[196:199] offset:1024\n" \
  "v_add_u32 %[t0], 0x14000, %[b0]\n" \
  "ds_write_b128 %[t0], v[200:203]\n" \
  "ds_write_b128 %[t0], v[204:207] offset:1024\n" \
  "s_cmp_lg_u32 %[hb], 1\n" \
  "s_cbranch_scc1 .Ls_%=\n" \
  "v_xor_b32 %[t0], 0x18000, %[pb]\n" \
  "ds_read_b64_tr_b16 v[228:229], %[t0] offset:0\n" \
  "ds_read_b64_tr_b16 v[108:109], %[t0] offset:16384\n" \
  "v_xor_b32 %[t1], 0x18020, %[pb]\n" \
  "ds_read_b64_tr_b16 v[236:237], %[t1] offset:0\n" \
  "ds_read_b64_tr_b16 v[156:157], %[t1] offset:16384\n" \
  "v_xor_b32 %[t0], 0x19000, %[pb]\n" \
  "ds_read_b64_tr_b16 v[12:13], %[t0] offset:0\n" \
  "ds_read_b64_tr_b16 v[116:117], %[t0] offset:16384\n" \
  "v_xor_b32 %[t1], 0x19020, %[pb]\n" \
  "ds_read_b64_tr_b16 v[60:61], %[t1] offset:0\n" \
  "ds_read_b64_tr_b16 v[164:165], %[t1] offset:16384\n" \
  "v_xor_b32 %[t0], 0x1a000, %[pb]\n" \
  "ds_read_b64_tr_b16 v[28:29], %[t0] offset:0\n" \
  "ds_read_b64_tr_b16 v[124:125], %[t0] offset:16384\n" \
  "v_xor_b32 %[t1], 0x1a020, %[pb]\n" \
  "ds_read_b64_tr_b16 v[76:77], %[t1] offset:0\n" \
  "ds_read_b64_tr_b16 v[172:173], %[t1] offset:16384\n" \
  "v_xor_b32 %[t0], 0x1b000, %[pb]\n" \
  "ds_read_b64_tr_b16 v[44:45], %[t0] offset:0\n" \
  "ds_read_b64_tr_b16 v[140:141], %[t0] offset:16384\n" \
  "v_xor_b32 %[t1], 0x1b020, %[pb]\n" \
  "ds_read_b64_tr_b16 v[92:93], %[t1] offset:0\n" \
  "ds_read_b64_tr_b16 v[188:189], %[t1] offset:16384\n" \
  ".Ls_%=:\n" \
  "s_waitcnt lgkmcnt(0)\n"
#define PRO0_14 \
  "v_add_u32 %[t0], 0x18000, %[b0]\n" \
  "ds_write_b128 %[t0], v[208:211]\n" \
  "ds_write_b128 %[t0], v[212:215] offset:1024\n" \
  "v_add_u32 %[t0], 0x1c000, %[b0]\n" \
  "ds_write_b128 %[t0], v[216:219]\n" \
  "ds_write_b128 %[t0], v[220:223] offset:1024\n" \
  "s_cmp_lg_u32 %[hb], 0\n" \
  "s_cbranch_scc1 .Ls_%=\n" \
  "v_xor_b32 %[t0], 0x20000, %[pb]\n" \
  "ds_read_b64_tr_b16 v[230:231], %[t0] offset:0\n" \
  "ds_read_b64_tr_b16 v[110:111], %[t0] offset:16384\n" \
  "v_xor_b32 %[t1], 0x20020, %[pb]\n" \
  "ds_read_b64_tr_b16 v[238:239], %[t1] offset:0\n" \
  "ds_read_b64_tr_b16 v[158:159], %[t1] offset:16384\n" \
  "v_xor_b32 %[t0], 0x21000, %[pb]\n" \
  "ds_read_b64_tr_b16 v[14:15], %[t0] offset:0\n" \
  "ds_read_b64_tr_b16 v[118:119], %[t0] offset:16384\n" \
  "v_xor_b32 %[t1], 0x21020, %[pb]\n" \
  "ds_read_b64_tr_b16 v[62:63], %[t1] offset:0\n" \
  "ds_read_b64_tr_b16 v[166:167], %[t1] offset:16384\n" \
  "v_xor_b32 %[t0], 0x22000, %[pb]\n" \
  "ds_read_b64_tr_b16 v[30:31], %[t0] offset:0\n" \
  "ds_read_b64_tr_b16 v[126:127], %[t0] offset:16384\n" \
  "v_xor_b32 %[t1], 0x22020, %[pb]\n" \
  "ds_read_b64_tr_b16 v[78:79], %[t1] offset:0\n" \
  "ds_read_b64_tr_b16 v[174:175], %[t1] offset:16384\n" \
  "v_xor_b32 %[t0], 0x23000, %[pb]\n" \
  "ds_read_b64_tr_b16 v[46:47], %[t0] offset:0\n" \
  "ds_read_b64_tr_b16 v[142:143], %[t0] offset:16384\n" \
  "v_xor_b32 %[t1], 0x23020, %[pb]\n" \
  "ds_read_b64_tr_b16 v[94:95], %[t1] offset:0\n" \
  "ds_read_b64_tr_b16 v[190:191], %[t1] offset:16384\n" \
  ".Ls_%=:\n" \
  "s_waitcnt lgkmcnt(0)\n"
#define PRO0_15 \
  "s_cmp_lg_u32 %[hb], 1\n" \
  "s_cbranch_scc1 .Ls_%=\n" \
  "v_xor_b32 %[t0], 0x0, %[pb]\n" \
  "ds_read_b64_tr_b16 v[230:231], %[t0] offset:0\n" \
  "ds_read_b64_tr_b16 v[110:111], %[t0] offset:16384\n" \
  "v_xor_b32 %[t1], 0x20, %[pb]\n" \
  "ds_read_b64_tr_b16 v[238:239], %[t1] offset:0\n" \
  "ds_read_b64_tr_b16 v[158:159], %[t1] offset:16384\n" \
  "v_xor_b32 %[t0], 0x1000, %[pb]\n" \
  "ds_read_b64_tr_b16 v[14:15], %[t0] offset:0\n" \
  "ds_read_b64_tr_b16 v[118:119], %[t0] offset:16384\n" \
  "v_xor_b32 %[t1], 0x1020, %[pb]\n" \
  "ds_read_b64_tr_b16 v[62:63], %[t1] offset:0\n" \
  "ds_read_b64_tr_b16 v[166:167], %[t1] offset:16384\n" \
  "v_xor_b32 %[t0], 0x2000, %[pb]\n" \
  "ds_read_b64_tr_b16 v[30:31], %[t0] offset:0\n" \
  "ds_read_b64_tr_b16 v[126:127], %[t0] offset:16384\n" \
  "v_xor_b32 %[t1], 0x2020, %[pb]\n" \
  "ds_read_b64_tr_b16 v[78:79], %[t1] offset:0\n" \
  "ds_read_b64_tr_b16 v[174:175], %[t1] offset:16384\n" \
  "v_xor_b32 %[t0], 0x3000, %[pb]\n" \
  "ds_read_b64_tr_b16 v[46:47], %[t0] offset:0\n" \
  "ds_read_b64_tr_b16 v[142:143], %[t0] offset:16384\n" \
  "v_xor_b32 %[t1], 0x3020, %[pb]\n" \
  "ds_read_b64_tr_b16 v[94:95], %[t1] offset:0\n" \
  "ds_read_b64_tr_b16 v[190:191], %[t1] offset:16384\n" \
  ".Ls_%=:\n" \
  "s_waitcnt lgkmcnt(0)\n" \
  "v_add_u32 %[t0], 0x20000, %[b0]\n" \
  "ds_write_b128 %[t0], v[224:227]\n" \
  "ds_write_b128 %[t0], v[228:231] offset:1024\n" \
  "v_add_u32 %[t0], 0x24000, %[b0]\n" \
  "ds_write_b128 %[t0], v[232:235]\n" \
  "ds_write_b128 %[t0], v[236:239] offset:1024\n"
#define PRO1_0 \
  "v_add_u32 %[t0], 0x0, %[pa]\n" \
  "ds_read_b64_tr_b16 v[96:97], %[t0] offset:0\n" \
  "ds_read_b64_tr_b16 v[100:101], %[t0] offset:1024\n" \
  "ds_read_b64_tr_b16 v[104:105], %[t0] offset:8192\n" \
  "ds_read_b64_tr_b16 v[108:109], %[t0] offset:9216\n" \
  "s_cmp_lg_u32 %[hb], 0\n" \
  "s_cbranch_scc1 .Ls_%=\n" \
  "v_xor_b32 %[t0], 0x0, %[pb]\n" \
  "ds_read_b64_tr_b16 v[192:193], %[t0] offset:0\n" \
  "v_xor_b32 %[t1], 0x80, %[pb]\n" \
  "ds_read_b64_tr_b16 v[200:201], %[t1] offset:0\n" \
  "v_xor_b32 %[t0], 0x1000, %[pb]\n" \
  "ds_read_b64_tr_b16 v[0:1], %[t0] offset:0\n" \
  "v_xor_b32 %[t1], 0x1080, %[pb]\n" \
  "ds_read_b64_tr_b16 v[48:49], %[t1] offset:0\n" \
  "v_xor_b32 %[t0], 0x2000, %[pb]\n" \
  "ds_read_b64_tr_b16 v[16:17], %[t0] offset:0\n" \
  "v_xor_b32 %[t1], 0x2080, %[pb]\n" \
  "ds_read_b64_tr_b16 v[64:65], %[t1] offset:0\n" \
  "v_xor_b32 %[t0], 0x3000, %[pb]\n" \
  "ds_read_b64_tr_b16 v[32:33], %[t0] offset:0\n" \
  "v_xor_b32 %[t1], 0x3080, %[pb]\n" \
  "ds_read_b64_tr_b16 v[80:81], %[t1] offset:0\n" \
  ".Ls_%=:\n" \
  "s_waitcnt lgkmcnt(0)\n"
#define PRO1_1 \
  "v_add_u32 %[t0], 0x8000, %[pa]\n" \
  "ds_read_b64_tr_b16 v[98:99], %[t0] offset:0\n" \
  "ds_read_b64_tr_b16 v[102:103], %[t0] offset:1024\n" \
  "ds_read_b64_tr_b16 v[106:107], %[t0] offset:8192\n" \
  "ds_read_b64_tr_b16 v[110:111], %[t0] offset:9216\n" \
  "s_cmp_lg_u32 %[hb], 1\n" \
  "s_cbranch_scc1 .Ls_%=\n" \
  "v_xor_b32 %[t0], 0x8000, %[pb]\n" \
  "ds_read_b64_tr_b16 v[192:193], %[t0] offset:0\n" \
  "v_xor_b32 %[t1], 0x8080, %[pb]\n" \
  "ds_read_b64_tr_b16 v[200:201], %[t1] offset:0\n" \
  "v_xor_b32 %[t0], 0x9000, %[pb]\n" \
  "ds_read_b64_tr_b16 v[0:1], %[t0] offset:0\n" \
  "v_xor_b32 %[t1], 0x9080, %[pb]\n" \
  "ds_read_b64_tr_b16 v[48:49], %[t1] offset:0\n" \
  "v_xor_b32 %[t0], 0xa000, %[pb]\n" \
  "ds_read_b64_tr_b16 v[16:17], %[t0] offset:0\n" \
  "v_xor_b32 %[t1], 0xa080, %[pb]\n" \
  "ds_read_b64_tr_b16 v[64:65], %[t1] offset:0\n" \
  "v_xor_b32 %[t0], 0xb000, %[pb]\n" \
  "ds_read_b64_tr_b16 v[32:33], %[t0] offset:0\n" \
  "v_xor_b32 %[t1], 0xb080, %[pb]\n" \
  "ds_read_b64_tr_b16 v[80:81], %[t1] offset:0\n" \
  ".Ls_%=:\n" \
  "s_waitcnt lgkmcnt(0)\n" \
  "v_swap_b32 v97, v98\n" \
  "v_swap_b32 v101, v102\n" \
  "v_swap_b32 v105, v106\n" \
  "v_swap_b32 v109, v110\n"
#define PRO1_2 \
  "v_add_u32 %[t0], 0x10000, %[pa]\n" \
  "ds_read_b64_tr_b16 v[112:113], %[t0] offset:0\n" \
  "ds_read_b64_tr_b16 v[116:117], %[t0] offset:1024\n" \
  "ds_read_b64_tr_b16 v[120:121], %[t0] offset:8192\n" \
  "ds_read_b64_tr_b16 v[124:125], %[t0] offset:9216\n" \
  "s_cmp_lg_u32 %[hb], 0\n" \
  "s_cbranch_scc1 .Ls_%=\n" \
  "v_xor_b32 %[t0], 0x10000, %[pb]\n" \
  "ds_read_b64_tr_b16 v[194:195], %[t0] offset:0\n" \
  "v_xor_b32 %[t1], 0x10080, %[pb]\n" \
  "ds_read_b64_tr_b16 v[202:203], %[t1] offset:0\n" \
  "v_xor_b32 %[t0], 0x11000, %[pb]\n" \
  "ds_read_b64_tr_b16 v[2:3], %[t0] offset:0\n" \
  "v_xor_b32 %[t1], 0x11080, %[pb]\n" \
  "ds_read_b64_tr_b16 v[50:51], %[t1] offset:0\n" \
  "v_xor_b32 %[t0], 0x12000, %[pb]\n" \
  "ds_read_b64_tr_b16 v[18:19], %[t0] offset:0\n" \
  "v_xor_b32 %[t1], 0x12080, %[pb]\n" \
  "ds_read_b64_tr_b16 v[66:67], %[t1] offset:0\n" \
  "v_xor_b32 %[t0], 0x13000, %[pb]\n" \
  "ds_read_b64_tr_b16 v[34:35], %[t0] offset:0\n" \
  "v_xor_b32 %[t1], 0x13080, %[pb]\n" \
  "ds_read_b64_tr_b16 v[82:83], %[t1] offset:0\n" \
  ".Ls_%=:\n" \
  "s_waitcnt lgkmcnt(0)\n"
#define PRO1_3 \
  "v_add_u32 %[t0], 0x18000, %[pa]\n" \
  "ds_read_b64_tr_b16 v[114:115], %[t0] offset:0\n" \
  "ds_read_b64_tr_b16 v[118:119], %[t0] offset:1024\n" \
  "ds_read_b64_tr_b16 v[122:123], %[t0] offset:8192\n" \
  "ds_read_b64_tr_b16 v[126:127], %[t0] offset:9216\n" \
  "s_cmp_lg_u32 %[hb], 1\n" \
  "s_cbranch_scc1 .Ls_%=\n" \
  "v_xor_b32 %[t0], 0x18000, %[pb]\n" \
  "ds_read_b64_tr_b16 v[194:195], %[t0] offset:0\n" \
  "v_xor_b32 %[t1], 0x18080, %[pb]\n" \
  "ds_read_b64_tr_b16 v[202:203], %[t1] offset:0\n" \
  "v_xor_b32 %[t0], 0x19000, %[pb]\n" \
  "ds_read_b64_tr_b16 v[2:3], %[t0] offset:0\n" \
  "v_xor_b32 %[t1], 0x19080, %[pb]\n" \
  "ds_read_b64_tr_b16 v[50:51], %[t1] offset:0\n" \
  "v_xor_b32 %[t0], 0x1a000, %[pb]\n" \
  "ds_read_b64_tr_b16 v[18:19], %[t0] offset:0\n" \
  "v_xor_b32 %[t1], 0x1a080, %[pb]\n" \
  "ds_read_b64_tr_b16 v[66:67], %[t1] offset:0\n" \
  "v_xor_b32 %[t0], 0x1b000, %[pb]\n" \
  "ds_read_b64_tr_b16 v[34:35], %[t0] offset:0\n" \
  "v_xor_b32 %[t1], 0x1b080, %[pb]\n" \
  "ds_read_b64_tr_b16 v[82:83], %[t1] offset:0\n" \
  ".Ls_%=:\n" \
  "s_waitcnt lgkmcnt(0)\n" \
  "v_swap_b32 v113, v114\n" \
  "v_swap_b32 v117, v118\n" \
  "v_swap_b32 v121, v122\n" \
  "v_swap_b32 v125, v126\n"
#define PRO1_4 \
  "v_add_u32 %[t0], 0x20000, %[pa]\n" \
  "ds_read_b64_tr_b16 v[128:129], %[t0] offset:0\n" \
  "ds_read_b64_tr_b16 v[132:133], %[t0] offset:1024\n" \
  "ds_read_b64_tr_b16 v[136:137], %[t0] offset:8192\n" \
  "ds_read_b64_tr_b16 v[140:141], %[t0] offset:9216\n" \
  "s_cmp_lg_u32 %[hb], 0\n" \
  "s_cbranch_scc1 .Ls_%=\n" \
  "v_xor_b32 %[t0], 0x20000, %[pb]\n" \
  "ds_read_b64_tr_b16 v[196:197], %[t0] offset:0\n" \
  "v_xor_b32 %[t1], 0x20080, %[pb]\n" \
  "ds_read_b64_tr_b16 v[204:205], %[t1] offset:0\n" \
  "v_xor_b32 %[t0], 0x21000, %[pb]\n" \
  "ds_read_b64_tr_b16 v[4:5], %[t0] offset:0\n" \
  "v_xor_b32 %[t1], 0x21080, %[pb]\n" \
  "ds_read_b64_tr_b16 v[52:53], %[t1] offset:0\n" \
  "v_xor_b32 %[t0], 0x22000, %[pb]\n" \
  "ds_read_b64_tr_b16 v[20:21], %[t0] offset:0\n" \
  "v_xor_b32 %[t1], 0x22080, %[pb]\n" \
  "ds_read_b64_tr_b16 v[68:69], %[t1] offset:0\n" \
  "v_xor_b32 %[t0], 0x23000, %[pb]\n" \
  "ds_read_b64_tr_b16 v[36:37], %[t0] offset:0\n" \
  "v_xor_b32 %[t1], 0x23080, %[pb]\n" \
  "ds_read_b64_tr_b16 v[84:85], %[t1] offset:0\n" \
  ".Ls_%=:\n" \
  "s_waitcnt lgkmcnt(0)\n"
#define PRO1_5 \
  "v_add_u32 %[t0], 0x0, %[pa]\n" \
  "ds_read_b64_tr_b16 v[130:131], %[t0] offset:0\n" \
  "ds_read_b64_tr_b16 v[134:135], %[t0] offset:1024\n" \
  "ds_read_b64_tr_b16 v[138:139], %[t0] offset:8192\n" \
  "ds_read_b64_tr_b16 v[142:143], %[t0] offset:9216\n" \
  "s_cmp_lg_u32 %[hb], 1\n" \
  "s_cbranch_scc1 .Ls_%=\n" \
  "v_xor_b32 %[t0], 0x0, %[pb]\n" \
  "ds_read_b64_tr_b16 v[196:197], %[t0] offset:0\n" \
  "v_xor_b32 %[t1], 0x80, %[pb]\n" \
  "ds_read_b64_tr_b16 v[204:205], %[t1] offset:0\n" \
  "v_xor_b32 %[t0], 0x1000, %[pb]\n" \
  "ds_read_b64_tr_b16 v[4:5], %[t0] offset:0\n" \
  "v_xor_b32 %[t1], 0x1080, %[pb]\n" \
  "ds_read_b64_tr_b16 v[52:53], %[t1] offset:0\n" \
  "v_xor_b32 %[t0], 0x2000, %[pb]\n" \
  "ds_read_b64_tr_b16 v[20:21], %[t0] offset:0\n" \
  "v_xor_b32 %[t1], 0x2080, %[pb]\n" \
  "ds_read_b64_tr_b16 v[68:69], %[t1] offset:0\n" \
  "v_xor_b32 %[t0], 0x3000, %[pb]\n" \
  "ds_read_b64_tr_b16 v[36:37], %[t0] offset:0\n" \
  "v_xor_b32 %[t1], 0x3080, %[pb]\n" \
  "ds_read_b64_tr_b16 v[84:85], %[t1] offset:0\n" \
  ".Ls_%=:\n" \
  "s_waitcnt lgkmcnt(0)\n" \
  "v_swap_b32 v129, v130\n" \
  "v_swap_b32 v133, v134\n" \
  "v_swap_b32 v137, v138\n" \
  "v_swap_b32 v141, v142\n"
#define PRO1_6 \
  "v_add_u32 %[t0], 0x8000, %[pa]\n" \
  "ds_read_b64_tr_b16 v[144:145], %[t0] offset:0\n" \
  "ds_read_b64_tr_b16 v[148:149], %[t0] offset:1024\n" \
  "ds_read_b64_tr_b16 v[152:153], %[t0] offset:8192\n" \
  "ds_read_b64_tr_b16 v[156:157], %[t0] offset:9216\n" \
  "s_cmp_lg_u32 %[hb], 0\n" \
  "s_cbranch_scc1 .Ls_%=\n" \
  "v_xor_b32 %[t0], 0x8000, %[pb]\n" \
  "ds_read_b64_tr_b16 v[198:199], %[t0] offset:0\n" \
  "v_xor_b32 %[t1], 0x8080, %[pb]\n" \
  "ds_read_b64_tr_b16 v[206:207], %[t1] offset:0\n" \
  "v_xor_b32 %[t0], 0x9000, %[pb]\n" \
  "ds_read_b64_tr_b16 v[6:7], %[t0] offset:0\n" \
  "v_xor_b32 %[t1], 0x9080, %[pb]\n" \
  "ds_read_b64_tr_b16 v[54:55], %[t1] offset:0\n" \
  "v_xor_b32 %[t0], 0xa000, %[pb]\n" \
  "ds_read_b64_tr_b16 v[22:23], %[t0] offset:0\n" \
  "v_xor_b32 %[t1], 0xa080, %[pb]\n" \
  "ds_read_b64_tr_b16 v[70:71], %[t1] offset:0\n" \
  "v_xor_b32 %[t0], 0xb000, %[pb]\n" \
  "ds_read_b64_tr_b16 v[38:39], %[t0] offset:0\n" \
  "v_xor_b32 %[t1], 0xb080, %[pb]\n" \
  "ds_read_b64_tr_b16 v[86:87], %[t1] offset:0\n" \
  ".Ls_%=:\n" \
  "s_waitcnt lgkmcnt(0)\n"
#define PRO1_7 \
  "v_add_u32 %[t0], 0x10000, %[pa]\n" \
  "ds_read_b64_tr_b16 v[146:147], %[t0] offset:0\n" \
  "ds_read_b64_tr_b16 v[150:151], %[t0] offset:1024\n" \
  "ds_read_b64_tr_b16 v[154:155], %[t0] offset:8192\n" \
  "ds_read_b64_tr_b16 v[158:159], %[t0] offset:9216\n" \
  "s_cmp_lg_u32 %[hb], 1\n" \
  "s_cbranch_scc1 .Ls_%=\n" \
  "v_xor_b32 %[t0], 0x10000, %[pb]\n" \
  "ds_read_b64_tr_b16 v[198:199], %[t0] offset:0\n" \
  "v_xor_b32 %[t1], 0x10080, %[pb]\n" \
  "ds_read_b64_tr_b16 v[206:207], %[t1] offset:0\n" \
  "v_xor_b32 %[t0], 0x11000, %[pb]\n" \
  "ds_read_b64_tr_b16 v[6:7], %[t0] offset:0\n" \
  "v_xor_b32 %[t1], 0x11080, %[pb]\n" \
  "ds_read_b64_tr_b16 v[54:55], %[t1] offset:0\n" \
  "v_xor_b32 %[t0], 0x12000, %[pb]\n" \
  "ds_read_b64_tr_b16 v[22:23], %[t0] offset:0\n" \
  "v_xor_b32 %[t1], 0x12080, %[pb]\n" \
  "ds_read_b64_tr_b16 v[70:71], %[t1] offset:0\n" \
  "v_xor_b32 %[t0], 0x13000, %[pb]\n" \
  "ds_read_b64_tr_b16 v[38:39], %[t0] offset:0\n" \
  "v_xor_b32 %[t1], 0x13080, %[pb]\n" \
  "ds_read_b64_tr_b16 v[86:87], %[t1] offset:0\n" \
  ".Ls_%=:\n" \
  "s_waitcnt lgkmcnt(0)\n" \
  "v_swap_b32 v145, v146\n" \
  "v_swap_b32 v149, v150\n" \
  "v_swap_b32 v153, v154\n" \
  "v_swap_b32 v157, v158\n"
#define PRO1_8 \
  "v_add_u32 %[t0], 0x18000, %[pa]\n" \
  "ds_read_b64_tr_b16 v[160:161], %[t0] offset:0\n" \
  "ds_read_b64_tr_b16 v[164:165], %[t0] offset:1024\n" \
  "ds_read_b64_tr_b16 v[168:169], %[t0] offset:8192\n" \
  "ds_read_b64_tr_b16 v[172:173], %[t0] offset:9216\n" \
  "s_cmp_lg_u32 %[hb], 0\n" \
  "s_cbranch_scc1 .Ls_%=\n" \
  "v_xor_b32 %[t0], 0x18000, %[pb]\n" \
  "ds_read_b64_tr_b16 v[208:209], %[t0] offset:0\n" \
  "v_xor_b32 %[t1], 0x18080, %[pb]\n" \
  "ds_read_b64_tr_b16 v[216:217], %[t1] offset:0\n" \
  "v_xor_b32 %[t0], 0x19000, %[pb]\n" \
  "ds_read_b64_tr_b16 v[8:9], %[t0] offset:0\n" \
  "v_xor_b32 %[t1], 0x19080, %[pb]\n" \
  "ds_read_b64_tr_b16 v[56:57], %[t1] offset:0\n" \
  "v_xor_b32 %[t0], 0x1a000, %[pb]\n" \
  "ds_read_b64_tr_b16 v[24:25], %[t0] offset:0\n" \
  "v_xor_b32 %[t1], 0x1a080, %[pb]\n" \
  "ds_read_b64_tr_b16 v[72:73], %[t1] offset:0\n" \
  "v_xor_b32 %[t0], 0x1b000, %[pb]\n" \
  "ds_read_b64_tr_b16 v[40:41], %[t0] offset:0\n" \
  "v_xor_b32 %[t1], 0x1b080, %[pb]\n" \
  "ds_read_b64_tr_b16 v[88:89], %[t1] offset:0\n" \
  ".Ls_%=:\n" \
  "s_waitcnt lgkmcnt(0)\n"
#define PRO1_9 \
  "v_add_u32 %[t0], 0x20000, %[pa]\n" \
  "ds_read_b64_tr_b16 v[162:163], %[t0] offset:0\n" \
  "ds_read_b64_tr_b16 v[166:167], %[t0] offset:1024\n" \
  "ds_read_b64_tr_b16 v[170:171], %[t0] offset:8192\n" \
  "ds_read_b64_tr_b16 v[174:175], %[t0] offset:9216\n" \
  "s_cmp_lg_u32 %[hb], 1\n" \
  "s_cbranch_scc1 .Ls_%=\n" \
  "v_xor_b32 %[t0], 0x20000, %[pb]\n" \
  "ds_read_b64_tr_b16 v[208:209], %[t0] offset:0\n" \
  "v_xor_b32 %[t1], 0x20080, %[pb]\n" \
  "ds_read_b64_tr_b16 v[216:217], %[t1] offset:0\n" \
  "v_xor_b32 %[t0], 0x21000, %[pb]\n" \
  "ds_read_b64_tr_b16 v[8:9], %[t0] offset:0\n" \
  "v_xor_b32 %[t1], 0x21080, %[pb]\n" \
  "ds_read_b64_tr_b16 v[56:57], %[t1] offset:0\n" \
  "v_xor_b32 %[t0], 0x22000, %[pb]\n" \
  "ds_read_b64_tr_b16 v[24:25], %[t0] offset:0\n" \
  "v_xor_b32 %[t1], 0x22080, %[pb]\n" \
  "ds_read_b64_tr_b16 v[72:73], %[t1] offset:0\n" \
  "v_xor_b32 %[t0], 0x23000, %[pb]\n" \
  "ds_read_b64_tr_b16 v[40:41], %[t0] offset:0\n" \
  "v_xor_b32 %[t1], 0x23080, %[pb]\n" \
  "ds_read_b64_tr_b16 v[88:89], %[t1] offset:0\n" \
  ".Ls_%=:\n" \
  "s_waitcnt lgkmcnt(0)\n" \
  "v_swap_b32 v161, v162\n" \
  "v_swap_b32 v165, v166\n" \
  "v_swap_b32 v169, v170\n" \
  "v_swap_b32 v173, v174\n"
#define PRO1_10 \
  "v_add_u32 %[t0], 0x0, %[pa]\n" \
  "ds_read_b64_tr_b16 v[176:177], %[t0] offset:0\n" \
  "ds_read_b64_tr_b16 v[180:181], %[t0] offset:1024\n" \
  "ds_read_b64_tr_b16 v[184:185], %[t0] offset:8192\n" \
  "ds_read_b64_tr_b16 v[188:189], %[t0] offset:9216\n" \
  "s_cmp_lg_u32 %[hb], 0\n" \
  "s_cbranch_scc1 .Ls_%=\n" \
  "v_xor_b32 %[t0], 0x0, %[pb]\n" \
  "ds_read_b64_tr_b16 v[210:211], %[t0] offset:0\n" \
  "v_xor_b32 %[t1], 0x80, %[pb]\n" \
  "ds_read_b64_tr_b16 v[218:219], %[t1] offset:0\n" \
  "v_xor_b32 %[t0], 0x1000, %[pb]\n" \
  "ds_read_b64_tr_b16 v[10:11], %[t0] offset:0\n" \
  "v_xor_b32 %[t1], 0x1080, %[pb]\n" \
  "ds_read_b64_tr_b16 v[58:59], %[t1] offset:0\n" \
  "v_xor_b32 %[t0], 0x2000, %[pb]\n" \
  "ds_read_b64_tr_b16 v[26:27], %[t0] offset:0\n" \
  "v_xor_b32 %[t1], 0x2080, %[pb]\n" \
  "ds_read_b64_tr_b16 v[74:75], %[t1] offset:0\n" \
  "v_xor_b32 %[t0], 0x3000, %[pb]\n" \
  "ds_read_b64_tr_b16 v[42:43], %[t0] offset:0\n" \
  "v_xor_b32 %[t1], 0x3080, %[pb]\n" \
  "ds_read_b64_tr_b16 v[90:91], %[t1] offset:0\n" \
  ".Ls_%=:\n" \
  "s_waitcnt lgkmcnt(0)\n"
#define PRO1_11 \
  "v_add_u32 %[t0], 0x8000, %[pa]\n" \
  "ds_read_b64_tr_b16 v[178:179], %[t0] offset:0\n" \
  "ds_read_b64_tr_b16 v[182:183], %[t0] offset:1024\n" \
  "ds_read_b64_tr_b16 v[186:187], %[t0] offset:8192\n" \
  "ds_read_b64_tr_b16 v[190:191], %[t0] offset:9216\n" \
  "s_cmp_lg_u32 %[hb], 1\n" \
  "s_cbranch_scc1 .Ls_%=\n" \
  "v_xor_b32 %[t0], 0x8000, %[pb]\n" \
  "ds_read_b64_tr_b16 v[210:211], %[t0] offset:0\n" \
  "v_xor_b32 %[t1], 0x8080, %[pb]\n" \
  "ds_read_b64_tr_b16 v[218:219], %[t1] offset:0\n" \
  "v_xor_b32 %[t0], 0x9000, %[pb]\n" \
  "ds_read_b64_tr_b16 v[10:11], %[t0] offset:0\n" \
  "v_xor_b32 %[t1], 0x9080, %[pb]\n" \
  "ds_read_b64_tr_b16 v[58:59], %[t1] offset:0\n" \
  "v_xor_b32 %[t0], 0xa000, %[pb]\n" \
  "ds_read_b64_tr_b16 v[26:27], %[t0] offset:0\n" \
  "v_xor_b32 %[t1], 0xa080, %[pb]\n" \
  "ds_read_b64_tr_b16 v[74:75], %[t1] offset:0\n" \
  "v_xor_b32 %[t0], 0xb000, %[pb]\n" \
  "ds_read_b64_tr_b16 v[42:43], %[t0] offset:0\n" \
  "v_xor_b32 %[t1], 0xb080, %[pb]\n" \
  "ds_read_b64_tr_b16 v[90:91], %[t1] offset:0\n" \
  ".Ls_%=:\n" \
  "s_waitcnt lgkmcnt(0)\n" \
  "v_swap_b32 v177, v178\n" \
  "v_swap_b32 v181, v182\n" \
  "v_swap_b32 v185, v186\n" \
  "v_swap_b32 v189, v190\n"
#define PRO1_12 \
  "v_add_u32 %[t0], 0x8000, %[b0]\n" \
  "ds_write_b128 %[t0], v[192:195]\n" \
  "ds_write_b128 %[t0], v[196:199] offset:1024\n" \
  "v_add_u32 %[t0], 0xc000, %[b0]\n" \
  "ds_write_b128 %[t0], v[200:203]\n" \
  "ds_write_b128 %[t0], v[204:207] offset:1024\n" \
  "v_add_u32 %[t0], 0x10000, %[pa]\n" \
  "ds_read_b64_tr_b16 v[224:225], %[t0] offset:0\n" \
  "ds_read_b64_tr_b16 v[228:229], %[t0] offset:1024\n" \
  "ds_read_b64_tr_b16 v[232:233], %[t0] offset:8192\n" \
  "ds_read_b64_tr_b16 v[236:237], %[t0] offset:9216\n" \
  "s_cmp_lg_u32 %[hb], 0\n" \
  "s_cbranch_scc1 .Ls_%=\n" \
  "v_xor_b32 %[t0], 0x10000, %[pb]\n" \
  "ds_read_b64_tr_b16 v[212:213], %[t0] offset:0\n" \
  "v_xor_b32 %[t1], 0x10080, %[pb]\n" \
  "ds_read_b64_tr_b16 v[220:221], %[t1] offset:0\n" \
  "v_xor_b32 %[t0], 0x11000, %[pb]\n" \
  "ds_read_b64_tr_b16 v[12:13], %[t0] offset:0\n" \
  "v_xor_b32 %[t1], 0x11080, %[pb]\n" \
  "ds_read_b64_tr_b16 v[60:61], %[t1] offset:0\n" \
  "v_xor_b32 %[t0], 0x12000, %[pb]\n" \
  "ds_read_b64_tr_b16 v[28:29], %[t0] offset:0\n" \
  "v_xor_b32 %[t1], 0x12080, %[pb]\n" \
  "ds_read_b64_tr_b16 v[76:77], %[t1] offset:0\n" \
  "v_xor_b32 %[t0], 0x13000, %[pb]\n" \
  "ds_read_b64_tr_b16 v[44:45], %[t0] offset:0\n" \
  "v_xor_b32 %[t1], 0x13080, %[pb]\n" \
  "ds_read_b64_tr_b16 v[92:93], %[t1] offset:0\n" \
  ".Ls_%=:\n" \
  "s_waitcnt lgkmcnt(0)\n"
#define PRO1_13 \
  "v_add_u32 %[t0], 0x18000, %[pa]\n" \
  "ds_read_b64_tr_b16 v[226:227], %[t0] offset:0\n" \
  "ds_read_b64_tr_b16 v[230:231], %[t0] offset:1024\n" \
  "ds_read_b64_tr_b16 v[234:235], %[t0] offset:8192\n" \
  "ds_read_b64_tr_b16 v[238:239], %[t0] offset:9216\n" \
  "s_cmp_lg_u32 %[hb], 1\n" \
  "s_cbranch_scc1 .Ls_%=\n" \
  "v_xor_b32 %[t0], 0x18000, %[pb]\n" \
  "ds_read_b64_tr_b16 v[212:213], %[t0] offset:0\n" \
  "v_xor_b32 %[t1], 0x18080, %[pb]\n" \
  "ds_read_b64_tr_b16 v[220:221], %[t1] offset:0\n" \
  "v_xor_b32 %[t0], 0x19000, %[pb]\n" \
  "ds_read_b64_tr_b16 v[12:13], %[t0] offset:0\n" \
  "v_xor_b32 %[t1], 0x19080, %[pb]\n" \
  "ds_read_b64_tr_b16 v[60:61], %[t1] offset:0\n" \
  "v_xor_b32 %[t0], 0x1a000, %[pb]\n" \
  "ds_read_b64_tr_b16 v[28:29], %[t0] offset:0\n" \
  "v_xor_b32 %[t1], 0x1a080, %[pb]\n" \
  "ds_read_b64_tr_b16 v[76:77], %[t1] offset:0\n" \
  "v_xor_b32 %[t0], 0x1b000, %[pb]\n" \
  "ds_read_b64_tr_b16 v[44:45], %[t0] offset:0\n" \
  "v_xor_b32 %[t1], 0x1b080, %[pb]\n" \
  "ds_read_b64_tr_b16 v[92:93], %[t1] offset:0\n" \
  ".Ls_%=:\n" \
  "s_waitcnt lgkmcnt(0)\n" \
  "v_swap_b32 v225, v226\n" \
  "v_swap_b32 v229, v230\n" \
  "v_swap_b32 v233, v234\n" \
  "v_swap_b32 v237, v238\n" \
  "v_add_u32 %[t0], 0x10000, %[b0]\n" \
  "ds_write_b128 %[t0], v[224:227]\n" \
  "ds_write_b128 %[t0], v[228:231] offset:1024\n" \
  "v_add_u32 %[t0], 0x14000, %[b0]\n" \
  "ds_write_b128 %[t0], v[232:235]\n" \
  "ds_write_b128 %[t0], v[236:239] offset:1024\n"
#define PRO1_14 \
  "v_add_u32 %[t0], 0x20000, %[pa]\n" \
  "ds_read_b64_tr_b16 v[224:225], %[t0] offset:0\n" \
  "ds_read_b64_tr_b16 v[228:229], %[t0] offset:1024\n" \
  "ds_read_b64_tr_b16 v[232:233], %[t0] offset:8192\n" \
  "ds_read_b64_tr_b16 v[236:237], %[t0] offset:9216\n" \
  "s_cmp_lg_u32 %[hb], 0\n" \
  "s_cbranch_scc1 .Ls_%=\n" \
  "v_xor_b32 %[t0], 0x20000, %[pb]\n" \
  "ds_read_b64_tr_b16 v[214:215], %[t0] offset:0\n" \
  "v_xor_b32 %[t1], 0x20080, %[pb]\n" \
  "ds_read_b64_tr_b16 v[222:223], %[t1] offset:0\n" \
  "v_xor_b32 %[t0], 0x21000, %[pb]\n" \
  "ds_read_b64_tr_b16 v[14:15], %[t0] offset:0\n" \
  "v_xor_b32 %[t1], 0x21080, %[pb]\n" \
  "ds_read_b64_tr_b16 v[62:63], %[t1] offset:0\n" \
  "v_xor_b32 %[t0], 0x22000, %[pb]\n" \
  "ds_read_b64_tr_b16 v[30:31], %[t0] offset:0\n" \
  "v_xor_b32 %[t1], 0x22080, %[pb]\n" \
  "ds_read_b64_tr_b16 v[78:79], %[t1] offset:0\n" \
  "v_xor_b32 %[t0], 0x23000, %[pb]\n" \
  "ds_read_b64_tr_b16 v[46:47], %[t0] offset:0\n" \
  "v_xor_b32 %[t1], 0x23080, %[pb]\n" \
  "ds_read_b64_tr_b16 v[94:95], %[t1] offset:0\n" \
  ".Ls_%=:\n" \
  "s_waitcnt lgkmcnt(0)\n"
#define PRO1_15 \
  "v_add_u32 %[t0], 0x0, %[pa]\n" \
  "ds_read_b64_tr_b16 v[226:227], %[t0] offset:0\n" \
  "ds_read_b64_tr_b16 v[230:231], %[t0] offset:1024\n" \
  "ds_read_b64_tr_b16 v[234:235], %[t0] offset:8192\n" \
  "ds_read_b64_tr_b16 v[238:239], %[t0] offset:9216\n" \
  "s_cmp_lg_u32 %[hb], 1\n" \
  "s_cbranch_scc1 .Ls_%=\n" \
  "v_xor_b32 %[t0], 0x0, %[pb]\n" \
  "ds_read_b64_tr_b16 v[214:215], %[t0] offset:0\n" \
  "v_xor_b32 %[t1], 0x80, %[pb]\n" \
  "ds_read_b64_tr_b16 v[222:223], %[t1] offset:0\n" \
  "v_xor_b32 %[t0], 0x1000, %[pb]\n" \
  "ds_read_b64_tr_b16 v[14:15], %[t0] offset:0\n" \
  "v_xor_b32 %[t1], 0x1080, %[pb]\n" \
  "ds_read_b64_tr_b16 v[62:63], %[t1] offset:0\n" \
  "v_xor_b32 %[t0], 0x2000, %[pb]\n" \
  "ds_read_b64_tr_b16 v[30:31], %[t0] offset:0\n" \
  "v_xor_b32 %[t1], 0x2080, %[pb]\n" \
  "ds_read_b64_tr_b16 v[78:79], %[t1] offset:0\n" \
  "v_xor_b32 %[t0], 0x3000, %[pb]\n" \
  "ds_read_b64_tr_b16 v[46:47], %[t0] offset:0\n" \
  "v_xor_b32 %[t1], 0x3080, %[pb]\n" \
  "ds_read_b64_tr_b16 v[94:95], %[t1] offset:0\n" \
  ".Ls_%=:\n" \
  "s_waitcnt lgkmcnt(0)\n" \
  "v_swap_b32 v225, v226\n" \
  "v_swap_b32 v229, v230\n" \
  "v_swap_b32 v233, v234\n" \
  "v_swap_b32 v237, v238\n" \
  "v_add_u32 %[t0], 0x18000, %[b0]\n" \
  "ds_write_b128 %[t0], v[224:227]\n" \
  "ds_write_b128 %[t0], v[228:231] offset:1024\n" \
  "v_add_u32 %[t0], 0x1c000, %[b0]\n" \
  "ds_write_b128 %[t0], v[232:235]\n" \
  "ds_write_b128 %[t0], v[236:239] offset:1024\n" \
  "v_add_u32 %[t0], 0x20000, %[b0]\n" \
  "ds_write_b128 %[t0], v[208:211]\n" \
  "ds_write_b128 %[t0], v[212:215] offset:1024\n" \
  "v_add_u32 %[t0], 0x24000, %[b0]\n" \
  "ds_write_b128 %[t0], v[216:219]\n" \
  "ds_write_b128 %[t0], v[220:223] offset:1024\n"
#define VO_CLOB "v192", "v193", "v194", "v195", "v196", "v197", "v198", "v199", "v200", "v201", "v202", "v203", "v204", "v205", "v206", "v207", "v208", "v209", "v210", "v211", "v212", "v213", "v214", "v215", "v216", "v217", "v218", "v219", "v220", "v221", "v222", "v223", "v224", "v225", "v226", "v227", "v228", "v229", "v230", "v231", "v232", "v233", "v234", "v235", "v240", "v241", "v242", "v243", "v244", "v245", "v246", "v247", "v250", "v251", "v252"
#define QK_CLOB "v204", "v205", "v206", "v207", "v208", "v209", "v210", "v211", "v212", "v213", "v214", "v215", "v216", "v217", "v218", "v219", "v220", "v221", "v222", "v223", "v224", "v225", "v226", "v227", "v228", "v229", "v230", "v231", "v236", "v237", "v238", "v239", "v240", "v241", "v242", "v243", "v244", "v245", "v246", "v247"
#define QL_CLOB "v192", "v193", "v194", "v195", "v196", "v197", "v198", "v199", "v200", "v201", "v202", "v203"
#define QH_CLOB "v232", "v233", "v234", "v235"
#define ST_CLOB "v192", "v193", "v194", "v195", "v196", "v197", "v198", "v199", "v200", "v201", "v202", "v203", "v204", "v205", "v206", "v207", "v240", "v241", "v242", "v243", "v244", "v245", "v246", "v247", "v253"
#define XB_CLOB "v192", "v193", "v194", "v195", "v196", "v197", "v198", "v199", "v200", "v201", "v202", "v203", "v204", "v205", "v206", "v207", "v208", "v209", "v210", "v211", "v212", "v213", "v214", "v215", "v216", "v217", "v218", "v219", "v220", "v221", "v222", "v223", "v224", "v225", "v226", "v227", "v228", "v229", "v230", "v231", "v232", "v233", "v234", "v235", "v236", "v237", "v238", "v239", "v240", "v241", "v242", "v243", "v244", "v245", "v246", "v247"
#define OF_CLOB "v32", "v33", "v34", "v35", "v36", "v37", "v38", "v39", "v40", "v41", "v42", "v43", "v44", "v45", "v46", "v47", "v48", "v49", "v50", "v51", "v52", "v53", "v54", "v55", "v56", "v57", "v58", "v59", "v60", "v61", "v62", "v63", "v64", "v65", "v66", "v67", "v68", "v69", "v70", "v71", "v72", "v73", "v74", "v75", "v76", "v77", "v78", "v79", "v80", "v81", "v82", "v83", "v84", "v85", "v86", "v87", "v88", "v89", "v90", "v91", "v92", "v93", "v94", "v95", "v96", "v97", "v98", "v99", "v100", "v101", "v102", "v103"
constexpr int L_W = 32768;
constexpr int L_HI = 16384;
constexpr float CSC = 0.08838834764831845f * 1.4426950408889634f;
#define TWY 3
#define MARG 115
#define ED0 700
#define EA0 1110
#define SDK 360
#define TOL 145
#define XLATE 130
#define TFR 600
#define QT 0
#define HSU 11
#define WNT 1
#define KROT 7
#define VLATE 0
#define VSL 2
#define CG1 18
#define CG2 6
#define KSL2 0
#define KSL 4
#define NCY 1600
#define NCYV NCY
#define NRY 24
#define NRX 31
#define PDU 1
#define PDY 4
#define PDC 6
#define NF1O 64
#define NPUB 2
#define NSER 4
#define NSB 6
#define NSB1 4
#define XTU 20
#define CPU_ 9
#define CPR 12
#define TLP 0
#define CP0 0
#define RSL 2
#define PFL 4
#define EVT2R
template <int role>
__device__ __forceinline__ void wroles(const int h, const int par, const int sl, const int xg, const int w, const u64 nonce, const u16* __restrict__ x,
                                       const u16* __restrict__ Wq, const u16* __restrict__ Wk, const u16* __restrict__ Wv,
                                       const u16* __restrict__ Wo, char* ws) {
  asm volatile(".Lrole%c0:" ::"n"(role));
  LAS char* const L = (LAS char*)g_lds;
  gu32* tmo = (gu32*)(ws + OFF_TMO);
  const __amdgpu_buffer_rsrc_t rs = __builtin_amdgcn_make_buffer_rsrc(ws, 0, (int)WS_USED, 0x00020000);
  constexpr bool vo = role & 1;
  v8u wa[8], wb[16];
  v32u W0, W1, W2, W3, W4, W5;
  constexpr bool fuse = role >= 2;
  constexpr int RG = fuse ? 4 : 5, RB = fuse ? 32768 : 0;
  f32x4 fac[2][2];
  v2u nin = {0u, 0u};
  constexpr int TWV = 5;
  Clk ck = {0u, 0u, 92500u, 46432u};
  const int krot = role >= 2 ? (h & KROT) ^ (par * PX) : 0;
  {
    const int lane = olane();
    const int l15 = lane & 15, g = lane >> 4, trq = l15 >> 2, trp = lane & 3;
    const char* bl = (const char*)((vo ? Wv : Wq) + h * DH);
    const char* br = (const char*)(vo ? Wo + (size_t)h * DH * D : Wk + h * DH);
    const int rot = role == 1 ? (h >> 4) * 4 : 0;
    int vl[2], vr[2];
#pragma unroll
    for (int q = 0; q < 2; ++q) {
      const int row = 4 * (2 * w + q) + g;
      const int cb = ((((l15 >> 1) ^ ((row & 3) | ((row >> 1) & 4))) << 5) | ((l15 & 1) << 4));
      const int i = row & 7;
      const int prow = (row & 32) + ((i & 4) << 2) + ((row >> 1) & 12) + (i & 3);
      vl[q] = role < 2 ? (128 * (w >> 1) + 16 * (w & 1) + 8 * q + g) * 8192 + ((((l15 >> 1) ^ (4 * q + g)) << 5) | ((l15 & 1) << 4))
                       : row * 8192 + cb;
      vr[q] = vo ? (role == 1 ? row : prow) * 2048 + cb : vl[q];
    }
#define W_ISSUE(u)                                                                                   \
  {                                                                                                  \
    const size_t ro = (size_t)(role < 2 ? (8 * ((u) & 1) + ((u) >> 3)) * 64 + (((u) >> 2) & 1) * 32 + (((u) >> 1) & 1) * 4 : ((u) ^ krot) * 64) * 8192; \
    const char* pl = bl + ro;                                                                        \
    const char* pr = br + (role == 1 ? (size_t)(((u) >> 1) & 1) * 64 * 2048 + (2 * ((u) >> 2) + ((u) & 1)) * 256          \
                                     : (vo ? (size_t)((u) & 1) * 64 * 2048 + (((u) ^ (par * PX)) >> 1) * 256 : ro)); \
    _Pragma("unroll") for (int q = 0; q < 2; ++q) {                                                  \
      glds16<((WNT >> (role >> 1)) & 1) != 0>(pl, vl[q], RB + ((u) % RG) * 32768 + (2 * w + q) * 1024);                                \
      glds16<((WNT >> (role >> 1)) & 1) != 0>(pr, vr[q], RB + ((u) % RG) * 32768 + 16384 + (2 * w + q) * 1024);                        \
    }                                                                                                \
  }
    const int ta = role == 1 ? 8 + (w ^ rot) : 2 * w, tb = role < 2 ? (2 - role) * (w & 3) : 2 * w + 1;
    const int sw = trq | ((g & 1) << 2);
    int pa = (ta >> 3) * 16384 + (8 * g + trq) * 256 + (((ta & 7) ^ sw) << 5) + trp * 8;
    int pb = (role < 2 ? (w & 3) * 4096 + (4 * g + trq) * 256 : (tb >> 3) * 16384 + (8 * g + trq) * 256) + (((tb & 7) ^ sw) << 5) + trp * 8;
    v16u K0, K1, K2;
    if constexpr (role < 2)
      asm volatile("" : "={v[0:31]}"(W0), "={v[32:63]}"(W1), "={v[64:95]}"(W2), "={v[96:127]}"(W3), "={v[128:159]}"(W4), "={v[160:191]}"(W5),
                   "={v[192:207]}"(K0), "={v[208:223]}"(K1), "={v[224:239]}"(K2));
    const int b0 = lane * 16 + (w ^ 4) * 2048;
    const int pw = L_W + w * 2048 + lane * 16;
#define W_FRAG(dst, p, u)                                                                            \
  {                                                                                                  \
    int p_ = p + RB + ((u) % RG) * 32768;                                                             \
    asm volatile("" : "+v"(p_));                                                                     \
    const v2u t0 = trd(L + p_), t1 = trd(L + p_ + 1024), t2 = trd(L + p_ + 8192), t3 = trd(L + p_ + 9216); \
    dst = v8u{t0[0], t0[1], t1[0], t1[1], t2[0], t2[1], t3[0], t3[1]};                               \
  }
#define W_PUT(c)                                           \
  {                                                        \
    int p_ = pw + ((c) - 8) * 16384;                       \
    asm volatile("" : "+v"(p_));                           \
    *(LAS v4u*)(L + p_) = lo4(tmp[(c) - 8]);               \
    *(LAS v4u*)(L + p_ + 1024) = hi4(tmp[(c) - 8]);        \
  }
#define PT(i)
#define PT4(i)
    PT(0);
    const int fxv = ((w & 1) + 2 * (lane >> 2)) * 2048 + (((lane ^ -g) & 3) << 4) + ((w >> 1) & 1) * 64;
    const int fxr = l15 * 64 + (((g ^ -trq) & 3) << 4);
    constexpr int XS = role == 3;
#define X_ISSUE(j) glds16((const char*)x + par * 65536 + ((2 * (j) + (w >> 2)) ^ krot) * 128, fxv, (((j) + XS) & 3) * 8192 + w * 1024)
    if (TLP && w >= 6 + vo) {
      unsigned keep;
      asm volatile("s_mov_b32 %0, m0\n\ts_mov_b32 m0, %3\n\ts_nop 0\n\tglobal_load_lds_dword %1, %2\n\ts_mov_b32 m0, %0"
                   : "=&s"(keep) : "v"((olane() & (TLP - 1)) * (8388608 / (TLP ? TLP : 1))), "s"(w == 7 ? bl : br), "s"(RB + w * 2048) : "memory");
    }
    W_ISSUE(0);
    if (((CP0 >> role) & 1) && w == 1) code_ahead(sl + 8 * par, RB + 32768 + 2048);
    if (fuse) {
#pragma unroll
      for (int j = 0; j < 4; ++j) X_ISSUE(j);
    }
#pragma unroll
    for (int u = 1; u < RG; ++u) {
      if (u == PDU && role < 3)
        for (int k = role == 2 ? TN(6, PDY) : TN(5, PDC); k > 0; --k) __builtin_amdgcn_s_sleep(8);
      W_ISSUE(u);
    }
    PT(1);
    v8u tmp[8];
#pragma unroll
    for (int u = 0; u < 16; ++u) {
      {
        int young = 4 * (u + RG - 1 < 16 ? RG - 1 : 15 - u);
        if (fuse)
          for (int i = u - RG; i < u; ++i) young += i >= 0 && i < 8 && (i & 1);
        asm volatile("s_waitcnt vmcnt(%0)" ::"n"(young) : "memory");
      }
      RAW_BARRIER();
      if ((u & 3) == 0 || u == 15) PT(2 + (u + 1) / 4);
      if constexpr (role < 2) {
        unsigned t0, t1;
#define PRO_OPS                                                                                                                    \
  : "+{v[0:31]}"(W0), "+{v[32:63]}"(W1), "+{v[64:95]}"(W2), "+{v[96:127]}"(W3), "+{v[128:159]}"(W4), "+{v[160:191]}"(W5),          \
    "+{v[192:207]}"(K0), "+{v[208:223]}"(K1), "+{v[224:239]}"(K2), [t0] "=&v"(t0), [t1] "=&v"(t1)                                  \
  : [b0] "v"(b0), [pa] "v"(pa), [pb] "v"(pb), [hb] "s"(w >> 2)                                                                     \
  : "scc", "memory"
#define PRO_U(n)                                         \
  if (u == n) {                                          \
    if constexpr (role == 0) asm volatile(PRO0_##n PRO_OPS); \
    else asm volatile(PRO1_##n PRO_OPS);                 \
  }
        PRO_U(0) PRO_U(1) PRO_U(2) PRO_U(3) PRO_U(4) PRO_U(5) PRO_U(6) PRO_U(7)
        PRO_U(8) PRO_U(9) PRO_U(10) PRO_U(11) PRO_U(12) PRO_U(13) PRO_U(14) PRO_U(15)
      } else {
        if (u >= 13) { W_PUT(u >= 13 ? 2 * u - 18 : 8); W_PUT(u >= 13 ? 2 * u - 17 : 8); }
        v8u fa, fb;
        W_FRAG(fa, pa, u)
        W_FRAG(fb, pb, u)
        if (u < 8) {
          wa[u & 7] = fa;
          wb[u] = fb;
        } else {
          wb[u] = cat8(lo4(fa), lo4(fb));
          tmp[u & 7] = cat8(hi4(fa), hi4(fb));
        }
#pragma unroll
        for (int hf = 0; hf < 2; ++hf)
#pragma unroll
          for (int rt = 0; rt < 2; ++rt) {
            const v4u xq = *(const LAS v4u*)(L + fxr + ((u + 2 * XS) & 7) * 4096 + hf * 2048 + rt * 1024);
            fac[0][rt] = mma(hf ? hi4(fa) : lo4(fa), xq, u + hf == 0 ? f32x4{0.f, 0.f, 0.f, 0.f} : fac[0][rt]);
            fac[1][rt] = mma(hf ? hi4(fb) : lo4(fb), xq, u + hf == 0 ? f32x4{0.f, 0.f, 0.f, 0.f} : fac[1][rt]);
          }
        asm volatile("" : "+v"(fac[0][0]), "+v"(fac[0][1]), "+v"(fac[1][0]), "+v"(fac[1][1]));
      }
      if (u == HSU && w == 0) hs_wait(rs, ws, nonce, CNT_HS);
      if (((VWN && role == 3) || (QKN && role == 2)) && u == HSU + 2 && w == 7) nin = numa_load(rs, tmo);
      if (role < 2 && u == HSU + 2 && w == 7) {
        arm_wait(rs, tmo);
        OPQ(off, OFF_PERM);
        nin[0] = __builtin_amdgcn_raw_buffer_load_b32(rs, off, 0, AUX_SC1);
      }
      if (role < 2 && u == 14 && w == TWV) clk_init(ck);
      if (u + RG < 16) {
        RAW_BARRIER();
        if (fuse && u == XTU && w < 2) {
          unsigned keep;
          asm volatile("s_mov_b32 %0, m0\n\ts_mov_b32 m0, %3\n\ts_nop 0\n\tglobal_load_lds_dword %1, %2\n\ts_mov_b32 m0, %0"
                       : "=&s"(keep) : "v"(olane() * 128), "s"((const char*)x + (2 * w + 2 + par) * 65536 + sl * 8192), "s"(RB + (u % RG) * 32768 + w * 2048) : "memory");
        }
        if (u == CPU_ && ((CPR >> role) & 1) && w == 1) code_ahead(sl + 8 * par, RB + (u % RG) * 32768 + 2048);
        W_ISSUE(u + RG);
        if (fuse && u < 8 && (u & 1)) X_ISSUE(4 + (u >> 1));
      }
    }
    PT(7);
    static_assert(!QKN, "");
    if (role == 2 || (role == 3 && w < 4)) {
      const int l16 = olane() * 16;
#pragma unroll
      for (int rt = 0; rt < 2; ++rt) {
        const float ks = role == 2 && w >= 4 ? CSC : 1.f;
        const f32x4 f0 = fac[0][rt] * ks, f1 = fac[1][rt] * ks;
        const v4u o = {pk2(f0[0], f0[1]), pk2(f0[2], f0[3]), pk2(f1[0], f1[1]), pk2(f1[2], f1[3])};
        if (role == 3) *(LAS v4u*)(L + 24576 + rt * 4096 + w * 1024 + l16) = o;
        else st128(v4u{o[0], o[2], o[1], o[3]}, rs, l16, qk_addr(w >> 2, par, h, 0, 0) + rt * QK_RT + (w & 3) * 1024);
      }
    }
    RAW_BARRIER();
    PT(8);
    if (fuse) { W_PUT(14); W_PUT(15); }
  }
  const int lane = olane();
  const int l15 = lane & 15, g = lane >> 4, trq = l15 >> 2, trp = lane & 3;
  int pw0 = L_W + w * 2048 + lane * 16, pw1 = pw0 + 65536;
  asm volatile("" : "+v"(pw0), "+v"(pw1));
#define WA_P(c) (L + ((c) < 12 ? pw0 : pw1) + ((c) & 3) * 16384)
#define WA_LDS(c) cat8(*(const LAS v4u*)WA_P(c), *(const LAS v4u*)(WA_P(c) + 1024))
  const int r_a = (int)(((uintptr_t)ws >> 19) & 7);
  const int r_s = r_a + (int)(OFF_STEP / UNIT);

  typedef int v4i __attribute__((ext_vector_type(4)));
#define WPIN "{v[0:31]}"(W0), "{v[32:63]}"(W1), "{v[64:95]}"(W2), "{v[96:127]}"(W3), "{v[128:159]}"(W4), "{v[160:191]}"(W5)
  const v4u rsv = {(unsigned)(uintptr_t)ws, (unsigned)((uintptr_t)ws >> 32) & 0xffffu, (unsigned)WS_USED, 0x00020000u};
  if constexpr (role >= 2) {
    constexpr bool isx = role == 3;
    W0 = cat32(wa[0], wa[1], wa[2], wa[3]), W1 = cat32(wa[4], wa[5], wa[6], wa[7]), W2 = cat32(wb[0], wb[1], wb[2], wb[3]);
    W3 = cat32(wb[4], wb[5], wb[6], wb[7]), W4 = cat32(wb[8], wb[9], wb[10], wb[11]), W5 = cat32(wb[12], wb[13], wb[14], wb[15]);
    const v4u rxv = {(unsigned)(uintptr_t)x, (unsigned)((uintptr_t)x >> 32) & 0xffffu, (unsigned)(S * D * 2), 0x00020000u};
    v4i adr;
    adr[3] = lane * 16;
    adr[2] = pw0;
    adr[1] = pw1;
    adr[0] = ((w & 1) + 2 * (lane >> 2)) * 2048 + (((lane ^ -g) & 3) << 4) + ((w >> 1) & 1) * 1024 + (isx ? 0 : (w >> 2) * 64);
    const int xr = l15 * 64 + (((g ^ -trq) & 3) << 4);
    constexpr int fo = isx ? F_VW : F_QK;
    const int nrel = isx ? TN(8, NRX) : TN(7, NRY);
    const int nit = par ? nrel : NSEG - nrel;
#define SEGOF(i) ((i) < nrel ? 2 * (i) + par : (i) + nrel)
#define ST_FLAG(sg)                                                                                                   \
  {                                                                                                                   \
    VMCNT(0);                                                                                                         \
    RAW_BARRIER();                                                                                                    \
    if (w == 0) __builtin_amdgcn_raw_buffer_store_b32(0u, rs, olane() << 28, fo + flag_of(sg, h), AUX_SC1);               \
  }
    unsigned vpm_ = 0;
    if ((VWN && isx) || (QKN && !isx)) {
      if (w == 7) *(volatile LAS unsigned*)L = numa_perm(nin, (LAS unsigned*)(L + 512));
      RAW_BARRIER();
      vpm_ = __builtin_amdgcn_readfirstlane(*(volatile LAS unsigned*)L) | (unsigned)((r_a + (int)(OFF_VWX / UNIT)) & 7) << 12;
      RAW_BARRIER();
    }
    const int qcl = cls_of(vpm_, h >> 3) ^ (QKN == 2 ? 2 : 0);
#define XB_OPS(sg)                                                                                                      \
  : [cnt] "=&s"(cnt) EVOUT                                                                                              \
  : WPIN, "{v[248:251]}"(adr), [rs] "s"(rsv), [so] "s"(vw_addr(vpm_, 2 * (w - 4), sg, h >> 2) + (h & 3) * 1024 + par * (PX / 2 * VRING * 262144)), \
    [so2] "s"(vw_addr(vpm_, 2 * (w - 4), sg, h >> 2) + (h & 3) * 1024 - par * (PX / 2 * VRING * 262144)),               \
    [js] "i"(VWN ? 32 * 524288 : VRING * 262144), [ss] "i"(VWN ? 65536 : 32768), [ob] "s"(nit - it)                         \
  : XB_CLOB, "scc", "memory"
    if (!isx || w < 4) {
      constexpr int B0 = isx ? 6 : 8, PER = isx ? 2 : 4, SL = isx ? 12288 : 16384;
#pragma unroll
      for (int k = 0; k < 2; ++k)
#pragma unroll
        for (int j = 0; j < B0 / PER; ++j)
          glds16((const char*)x + SEGOF(1) * 65536 + (((k * B0 + j * PER) / 2 * 64) ^ (krot * 128)), adr[0], k * SL + j * PER * 2048 + w * 1024);
    }
    PT(9);
    if (!isx) {
      VMCNT(4);
      RAW_BARRIER();
    }
    PT(10);
    if (!isx && w == 0) __builtin_amdgcn_raw_buffer_store_b32(0u, rs, olane() << 28, fo + flag_of(par, h), AUX_SC1);
    if (!isx || w < 4) {
      if (isx) VMCNT(3);
      else VMCNT(0);
      RAW_BARRIER();
    }
    PT(11);
    if (!isx || w < 4) {
      v32u xf;
#pragma unroll
      for (int q = 0; q < 8; ++q) {
        const v4u t = *(const LAS v4u*)(L + xr + q * 1024);
#pragma unroll
        for (int j = 0; j < 4; ++j) xf[4 * q + j] = t[j];
      }
#pragma unroll 1
      for (int it = 1; it < nit; ++it) {
        EVDECL;
        const int seg = SEGOF(it), ip = it - (isx ? 2 : 1);
        unsigned cnt, keep;
#define ST_OPS                                                                                                                 \
  : [cnt] "=&s"(cnt), [keep] "=&s"(keep), "+{v[208:239]}"(xf) EVOUT                                                            \
  : WPIN, "{v[248:251]}"(adr), "{v252}"(xr), [rs] "s"(rsv), [rx] "s"(rxv), [sc] "s"(seg * 65536 + krot * 128),                    \
    [sn] "s"(SEGOF(min(it + 1, nit - 1)) * 65536 + krot * 128),                                                                  \
    [mw] "s"(w * 1024), [so] "s"(isx ? w * 1024 : qk_addr(w >> 2, seg, h, qcl, r_a) + (w & 3) * 1024), [rt] "i"(QK_RT), \
    [fo] "s"(fo + flag_of(SEGOF(max(ip, 0)), h)), [fm] "s"(w == 0 && ip >= 0 ? 0 : 1 << 30), [tm] "s"(w == (isx ? 3 : TWY) ? 0 : 1 << 30),            \
    [tc] "s"(min(SEGOF(it + 2), NSEG - 1) * 65536 + sl * 8192), [cs] "s"((w >> 2) * __float_as_uint(CSC))                                                                   \
  : ST_CLOB, "scc", "memory"
        if constexpr (isx) asm volatile(ST_XA ST_OPS);
        else asm volatile(ST_Y ST_OPS);
        EVT2R
        if (!isx && it < NPUB) ST_FLAG(seg)
      }
      if (isx) RAW_BARRIER();
    } else {
      int prog = -1;
#define RING_WAIT(sg)                                                                                     \
  _Pragma("clang loop unroll(disable)") for (unsigned spins = 0; prog <= (sg) - VRING && (sg) >= VRING;) { \
    if (spins) __builtin_amdgcn_s_sleep(16);                                                              \
    OPQ(vp, OFF_PROG);                                                                                    \
    prog = __builtin_amdgcn_readfirstlane(__builtin_amdgcn_raw_buffer_load_b32(rs, vp, 0, AUX_SC1));      \
    if (spin_fail(spins, tmo, 0x30019)) break;                                                            \
  }
#pragma unroll 1
      for (int it = 1;;) {
        RING_WAIT(SEGOF(it - 1))
        {
          EVDECL;
          unsigned cnt;
          asm volatile(ST_XB0 XB_OPS(SEGOF(it - 1)));
        }
        if (it == nit) break;
        if (it <= 2) PT4(8 + 4 * it);
        const int nsb = it == 1 ? NSB1 : NSB;
#pragma unroll 1
        for (int k = (it == 1 ? 0 : 2) + NOB; k < nsb; ++k) BARE_BARRIER();
        if (it <= 2) PT4(9 + 4 * it);
        VMCNT(0);
        if (it <= 2) PT4(10 + 4 * it);
        BARE_BARRIER();
        if (it <= 2) PT4(11 + 4 * it);
        if (w == 4) __builtin_amdgcn_raw_buffer_store_b32(0u, rs, olane() << 28, fo + flag_of(SEGOF(it - 1), h), AUX_SC1);
#pragma unroll 1
        for (int k = nsb; k < 6; ++k) BARE_BARRIER();
        {
          EVDECL;
          EVT2R
        }
        if (++it > NSER) {
#pragma unroll 1
          for (; it < nit; ++it) {
            RING_WAIT(SEGOF(it - 1))
            EVDECL;
            unsigned cnt;
            asm volatile(ST_XB XB_OPS(SEGOF(it - 1)));
            EVT2R
          }
        }
        BARE_BARRIER();
      }
    }
    ST_FLAG(SEGOF(nit - 1))
    if (isx && w == 0) __builtin_amdgcn_raw_buffer_store_b32(0u, rs, olane() << 28, fo + flag_of(SEGOF(nit - 2), h), AUX_SC1);
    return;
  }

  volatile LAS unsigned* sword = (volatile LAS unsigned*)(L + L_HI + 6400);
  if (w == 7) sword[0] = nin[0];
  RAW_BARRIER();
  const unsigned perm = __builtin_amdgcn_readfirstlane(sword[0]);
  PT(9);
  const int cown = cls_of(perm, xg);
  const int sidx = ((l15 & 2) ? 0xeeee : 0x4444) | ((l15 & 8) ? 0xeeee0000 : 0x44440000);
  int ntw = min(w ^ TWV, 1) << 30;
  asm volatile("" : "+s"(ntw));
  v2u bs = {(unsigned)(lane * 16 + (w ^ 4) * 2048), (unsigned)sidx};
  unsigned pl1 = bs[0] + 65536;
  asm volatile("" : "+v"(bs), "+v"(pl1));
  const int mg_ = NS2F(TN(0, MARG)), tol_ = NS2F(TN(13, TOL)), xl_ = NS2F(TN(14, XLATE));
  const int ncy_ = TN(1, NCY), vd_ = role ? NS2F(VLATE) : 0;
  Pace pc = {0u, NS2F(ED0), NS2F(EA0), 0};
  unsigned tg = 0, tw = 0;
  const int mo0 = R_MEM + (w >> 1) * 32768 + (w & 1) * 2048 - (w ^ 4) * 2048;
#define CH_IN WPIN, "{v[248:249]}"(bs), "{v253}"(pk), "{v254}"(pl1), [rs] "s"(rsv)
#define TLB_TOUCH                    \
  if (((r_s + s + 2) & 3) == 0)      \
    tch = __builtin_amdgcn_raw_buffer_load_b32(rs, 0, so + 2 * (int)UNIT + R_DUM + (role * 32 + h) * 128, AUX_SC1);
#define ST_ADR (lst * 32 + 32768 + 2048)
#define TIMING_WAVE(slp)                                                                             \
  {                                                                                                  \
    v2u st;                                                                                          \
    {                                                                                                \
      const int lst = olane();                                                                       \
      st = __builtin_amdgcn_raw_buffer_load_b64(rs, ST_ADR, so + R_ST + ((cown ^ xr) << 12), AUX_SC1); \
    }                                                                                                \
    SB();                                                                                            \
    if (slp) __builtin_amdgcn_s_sleep(slp);                             \
    clk_update(ck, s < 2 ? CG1 : CG2);                                                \
    if (role == 0) EV(7);                                                                            \
    SB();                                                                                            \
    if (__builtin_expect(anysent(st[0]), 0)) _Pragma("clang loop unroll(disable)") for (unsigned spins = 0;;) { \
      if (spins) __builtin_amdgcn_s_sleep(4);                                                        \
      if (spin_fail(spins, tmo, 0x30009)) break;                                                     \
      const int lst = olane();                                                                       \
      OPQ(va, ST_ADR - 2048);                                                                        \
      st = __builtin_amdgcn_raw_buffer_load_b64(rs, va, so + R_ST + ((cown ^ xr) << 12), AUX_SC1);   \
      if (!anysent(st[0])) break;                                                                       \
    }                                                                                                \
    unsigned md;                                                                                     \
    const int dm = stamp_max(ck, st, md);                                                            \
    const unsigned rf = (ck.r << 4) + dm;                                                            \
    pace_step(pc, s, rf, md, mg_, tol_, xl_);                                                     \
    tg = clk_cyc(ck, dm + pc.dq + vd_);                                                                    \
    tw = (unsigned)max(CMAX - (int)(tg - ck.m), 0);                                                  \
    EV(6);                                                                                           \
    EVDUMP(s);                                                                                       \
    EVCLK(s, ck);                                                                                    \
  }

  if constexpr (role == 0) {
    unsigned pk = w < 4 ? (g >> 1) * 512 + (g & 1) * 128 + trq * 32 + 8 * (trp ^ (2 * (g & 1)))
                        : (w & 2) * 1024 + (w & 1) * 512 + trp * 32 + (l15 >> 3) * 128 + (trq & 1) * 256 + 8 * (g ^ (trq & 2));
    asm volatile("" : "+v"(pk));
#define K_STAGE(sn)                                                                                                   \
  {                                                                                                                   \
    v4u kn[4];                                                                                                        \
    _Pragma("clang loop unroll(disable)") for (unsigned spins = 0; krdy <= (sn);) {    \
      OPQ(vfl, F_QK + flag_of(min((sn) + (olane() & 15), NSEG - 1), h));                                           \
      const u64 miss = __builtin_amdgcn_ballot_w64(__builtin_amdgcn_raw_buffer_load_b32(rs, vfl, 0, AUX_SC1) != 0u);  \
      krdy = (sn) + (miss ? __builtin_ctzll(miss) : 16);                                                              \
      if (krdy > (sn)) break;                                                                                         \
      __builtin_amdgcn_s_sleep(8);                                                                                    \
      if (spin_fail(spins, tmo, 0x30000)) break;                                                                      \
    }                                                                                                                 \
    const int lk = olane() * 16;                                                                                      \
    _Pragma("unroll") for (int q = 0; q < 4; ++q) kn[q] = __builtin_amdgcn_raw_buffer_load_b128(                      \
        rs, lk + q * 1024, qk_addr(1, sn, h, cown, r_a) + (w & 1) * QK_RT, AUX_SC1);                 \
    _Pragma("unroll") for (int q = 0; q < 4; ++q) *(LAS v4u*)(L + L_HI + 8192 + (w & 1) * 4096 + q * 1024 + lk) = kn[q]; \
  }
    int krdy = 0;
    const bool kst = w >= 4 && ((w ^ TWV) & 2);
    PT(10);
    if (kst) K_STAGE(0)
    RAW_BARRIER();
    PT(11);
    if (w < 2) {
      {
        const int lane = olane(), l15 = lane & 15, g = lane >> 4, d = ((l15 >> 1) & 2) | (l15 >> 3);
        *(LAS v4u*)(L + L_HI + 5120 + w * 2048 + lane * 16) =
            v4u{(unsigned)(R_EX + (h >> 3) * 32768 + (h & 7) * 512 + w * 256 + g * 16 + ((l15 >> 3) | (l15 & 2)) * 64 + (l15 & 4) * 2 + (l15 & 1) * 4),
                (unsigned)(R_PM + (cls_of(perm, l15 & 3) << 12) + (h >> 4) * 32768 + (h & 15) * 256 + w * 128 + g * 16 + d * 4),
                d == 2 * (h & 1) + w ? 0xffffu << (16 * (g & 1)) : 0u, 0u};
      }
#pragma unroll 1
      for (int s = 0; s < NSEG; ++s) {
        EVDECL;
        const int so = (int)OFF_STEP + s * (int)UNIT, xr = xreg(r_s + s);
#define CG(r) (so + ((cls_of(perm, (r) ^ (2 * w)) ^ xr) << 12))
        unsigned rt;
#define QA_OPS                                                                                                                    \
  : [rt] "=&s"(rt) EVOUT                                                                                                          \
  : CH_IN, [o0] "s"(so + mo0 + ((cown ^ xr) << 12)), [c0] "s"(CG(0)), [c1] "s"(CG(1)), [c2] "s"(CG(2)), [c3] "s"(CG(3)),          \
    [lo] "s"(0x3333333333333333ull), [so] "s"(so), [xr] "s"(xr << 12)                                                                                              \
  : QL_CLOB, QK_CLOB, QH_CLOB, "v250", "v251", "v252", "vcc", "scc", "memory"
        if (w == 0) asm volatile(QK_A0 QA_OPS);
        else asm volatile(QK_A1 QA_OPS);
        EVDUMP(s);
      }
    } else if (w < 4) {
#pragma unroll 1
      for (int s = 0; s < NSEG; ++s) {
        {
          EVDECL;
          const int so = (int)OFF_STEP + s * (int)UNIT, xr = xreg(r_s + s);
          unsigned rt;
          f32x4 sc[3];
          asm volatile(QK_X
                       : [rt] "=&s"(rt), "={v[192:195]}"(sc[0]), "={v[196:199]}"(sc[1]), "={v[200:203]}"(sc[2]) EVOUT
                       : CH_IN, [o0] "s"(so + mo0 + ((cown ^ xr) << 12)), [qo] "s"(qk_addr(0, s, h, cown, r_a) + (w - 2) * QK_RT)
                       : QK_CLOB, QH_CLOB, "v250", "v251", "v252", "vcc", "scc", "memory");
          const int ln = olane(), l15 = ln & 15, g = ln >> 4, l16 = ln * 16;
          EV(4);
          const int tok = 2 * l15 + w - 2 - 8 * g;
#pragma unroll
          for (int t = 0; t < 2; ++t)
#pragma unroll
            for (int j = 0; j < 4; ++j)
              if (2 * j + t > tok) sc[t][j] = -1000000000.0f * 1.4426950408889634f;
          float m0 = sc[2][0] + sc[2][2], m1 = sc[2][1] + sc[2][3];
          float mx;
          asm("v_max3_f32 %0, %1, %2, %3\n\tv_max3_f32 %0, %0, %4, %5\n\tv_max3_f32 %0, %0, %6, %7\n\tv_max3_f32 %0, %0, %8, %9\n\t"
              "v_max_f32 %0, %0, %10\n\ts_nop 1"
              : "=&v"(mx)
              : "v"(sc[0][0]), "v"(sc[0][1]), "v"(sc[0][2]), "v"(sc[0][3]), "v"(sc[1][0]), "v"(sc[1][1]), "v"(sc[1][2]), "v"(sc[1][3]),
                "v"(m0), "v"(m1));
          mx = xmax16_32(mx);
#pragma unroll
          for (int t = 0; t < 2; ++t)
#pragma unroll
            for (int j = 0; j < 4; ++j) sc[t][j] = __builtin_amdgcn_exp2f(sc[t][j] - mx);
          m0 = __builtin_amdgcn_exp2f(m0 - mx);
          m1 = __builtin_amdgcn_exp2f(m1 - mx);
          const f32x4 s4 = sc[0] + sc[1];
          const float inv = __builtin_amdgcn_rcpf(xsum16_32(((s4[0] + s4[1]) + (s4[2] + s4[3])) + (m0 + m1)));
          sc[0] *= inv;
          sc[1] *= inv;
          const v4u o = {pk2(sc[0][0], sc[1][0]), pk2(sc[0][1], sc[1][1]), pk2(sc[0][2], sc[1][2]), pk2(sc[0][3], sc[1][3])};
          const int pb = so + R_PX + (w - 2) * 40960;
          st128(o, rs, l16, pb + h * 1024);
          __builtin_amdgcn_raw_buffer_store_b32(pk2(m0 * inv, m1 * inv), rs, l15 * 16 + g * 4, pb + (32 + (h >> 2)) * 1024 + (h & 3) * 256, AUX_SC1);
          EV(6);
          EVDUMP(s);
        }
      }
    } else {
#pragma unroll 1
      for (int s = 0; s < NSEG; ++s) {
        EVDECL;
        const int so = (int)OFF_STEP + s * (int)UNIT, xr = xreg(r_s + s), sc_ = so + ((cown ^ xr) << 12);
        const int lst = olane();
        const bool fq = kst && krdy <= s + 2;
        int i0 = ((cls_of(perm, lst & 3) ^ xr) << 12) | ((lst & 4) << 9) | ((lst & 8) << 12) | ((lst >> 4) << 28) | ntw;
        int i2 = ((lst & 31) * 128 + (lst >> 5) * QK_RT) | (krdy > s + 1 ? 0 : 1 << 30);
        int i1 = (F_QK + flag_of(min(s + 1 + (lst & 15), NSEG - 1), h)) | (fq ? 0 : 1 << 30);
        v2u st;
        unsigned cnt, rt;
        int o0 = sc_ + mo0, sst = so + R_ST + role * 1024 + h * 32;
        int t1 = qk_addr(w & 1, min(s + 1, NSEG - 1), h, cown, r_a), t0 = 0;
        asm volatile("" : "+{v232}"(i0), "+{v233}"(i1), "+{v234}"(i2));
        asm volatile("" : "+s"(o0), "+s"(sst), "+s"(t0), "+s"(t1));
        asm volatile(QK_K
                     : [cnt] "=&s"(cnt), [rt] "=&s"(rt), "+{v232}"(i0), "+{v233}"(i1), "+{v234}"(i2), "={v[250:251]}"(st) EVOUT
                     : CH_IN, [o0] "s"(o0), [ckm] "s"(ck.m - ncy_), [inv] "s"(ck.inv << 16), [r16] "s"(ck.r << 4), [sst] "s"(sst), [tg] "s"(tg),
                       [tw] "s"(tw), [t0] "s"(t0), [t1] "s"(t1), [cs] "s"(CSC)
                     : QL_CLOB, QK_CLOB, "v235", "v252", "vcc", "scc", "memory");
        if (s < NSEG - 1 && w == TWV) {
          TIMING_WAVE(KSL)
        } else if (s < NSEG - 1 && kst) {
          if (fq) {
            const u64 miss = __builtin_amdgcn_ballot_w64(st[0] != 0u);
            krdy = s + 1 + (miss ? __builtin_ctzll(miss) : 16);
          }
          if (KSL2) __builtin_amdgcn_s_sleep(KSL2);
          EV(7);
          K_STAGE(s + 1)
        } else {
          __builtin_amdgcn_s_sleep(KSL);
          unsigned tch = 0;
          TLB_TOUCH
          asm volatile("" ::"v"(tch));
        }
        if (w != TWV) EV(6);
        if (w != TWV) EVDUMP(s);
      }
    }
  } else {
    int adr[4];
#pragma unroll
    for (int q = 0; q < 2; ++q) {
      const int c = 8 * (2 * q + (g >> 1) + ((g & 1) ? 0 : 4)) + (w ^ ((h >> 4) * 4));
      const int blk = (c % PO) * 2 + (h >> 4);
      adr[q] = (int)OFF_VM + blk * 32768 + ((cls_of(perm, c / PO) ^ xreg(r_a + (int)(OFF_VM / UNIT) + (blk >> 4))) << 12) +
                   (h & 15) * 256 + l15 * 16;
    }
    adr[2] = (16 * (g & 1) + 4 * (g >> 1) + trq) * 24 + (trp & 1) * 8;
    unsigned pk = L_HI + (16 * (w & 3) + l15) * 24 + 4 * g;
    asm volatile("" : "+v"(pk), "+v"(adr[0]), "+v"(adr[1]), "+v"(adr[2]));
    PT(10);
    PT(11);
    if (w < 4) {
#pragma unroll 1
      for (int s = 0; s < NSEG; ++s) {
        EVDECL;
        const int so = (int)OFF_STEP + s * (int)UNIT, xr = xreg(r_s + s);
        unsigned rt;
        asm volatile(VO_A
                     : [rt] "=&s"(rt) EVOUT
                     : CH_IN, "{v236}"(adr[0]), "{v237}"(adr[1]), "{v238}"(adr[2]), [o0] "s"(so + mo0 + ((cown ^ xr) << 12)), [vs] "s"(vm_slot(s))
                     : VO_CLOB, "vcc", "scc", "memory");
        if (w == 0) {
          unsigned tch = 0;
          TLB_TOUCH
          asm volatile("" ::"v"(tch));
        }
        EVDUMP(s);
      }
    } else {
#pragma unroll 1
      for (int s = 0; s < NSEG; ++s) {
        EVDECL;
        const int so = (int)OFF_STEP + s * (int)UNIT, xr = xreg(r_s + s);
        const int lst = olane();
        adr[3] = ((cls_of(perm, lst & 3) ^ xr) << 12) | ((lst & 4) << 9) | ((lst & 8) << 12) | ((lst >> 4) << 28) | ntw;
        unsigned cnt, rt;
        int o0 = so + mo0 + ((cown ^ xr) << 12), vs = vm_slot(s), sst = so + R_ST + role * 1024 + h * 32;
        asm volatile("" : "+{v239}"(adr[3]));
        asm volatile("" : "+s"(o0), "+s"(vs), "+s"(sst));
        asm volatile(VO_B
                     : [cnt] "=&s"(cnt), [rt] "=&s"(rt) EVOUT
                     : CH_IN, "{v236}"(adr[0]), "{v237}"(adr[1]), "{v238}"(adr[2]), "{v239}"(adr[3]), [o0] "s"(o0), [vs] "s"(vs),
                       [ckm] "s"(ck.m - ncy_), [inv] "s"(ck.inv << 16), [r16] "s"(ck.r << 4), [sst] "s"(sst), [tg] "s"(tg), [tw] "s"(tw)
                     : VO_CLOB, "vcc", "scc", "memory");
        if (w == TWV && s < NSEG - 1) TIMING_WAVE(VSL)
        if (w != TWV) EVDUMP(s);
      }
    }
  }
  if ((WBL & 2) && role == 1)
    if (w == 0 && sl == 0) asm volatile("buffer_wbl2 sc1\n\ts_waitcnt vmcnt(0)" ::: "memory");
  __builtin_amdgcn_endpgm();
}
typedef f32x2 c32;
template <bool CJ>
__device__ __forceinline__ c32 cmul(c32 a, c32 b) {
  c32 d;
  if (CJ)
    asm("v_pk_mul_f32 %0, %1, %2 op_sel:[1,1] op_sel_hi:[1,0]\n\tv_pk_fma_f32 %0, %1, %2, %0 op_sel_hi:[0,1,1] neg_hi:[0,1,0]"
        : "=&v"(d) : "v"(a), "v"(b));
  else
    asm("v_pk_mul_f32 %0, %1, %2 op_sel:[1,1] op_sel_hi:[1,0] neg_lo:[0,1]\n\tv_pk_fma_f32 %0, %1, %2, %0 op_sel_hi:[0,1,1]"
        : "=&v"(d) : "v"(a), "v"(b));
  return d;
}
__device__ __forceinline__ c32 cmuls(c32 a, c32 b) {
  c32 d;
  asm("v_pk_mul_f32 %0, %1, %2 op_sel:[1,1] op_sel_hi:[1,0] neg_lo:[0,1]\n\tv_pk_fma_f32 %0, %1, %2, %0 op_sel_hi:[0,1,1]"
      : "=&v"(d) : "v"(a), "s"(b));
  return d;
}
template <int SG>
__device__ __forceinline__ c32 addi(c32 a, c32 b) {
  c32 d;
  if (SG > 0) asm("v_pk_add_f32 %0, %1, %2 op_sel:[0,1] op_sel_hi:[1,0] neg_lo:[0,1]" : "=v"(d) : "v"(a), "v"(b));
  else asm("v_pk_add_f32 %0, %1, %2 op_sel:[0,1] op_sel_hi:[1,0] neg_hi:[0,1]" : "=v"(d) : "v"(a), "v"(b));
  return d;
}
template <int SG, bool I2 = false>
__device__ __forceinline__ void dft4(c32& x0, c32& x1, c32& x2, c32& x3) {
  const c32 a = I2 ? addi<SG>(x0, x2) : x0 + x2, b = I2 ? addi<-SG>(x0, x2) : x0 - x2, c = x1 + x3, d = x1 - x3;
  x0 = a + c;
  x1 = addi<SG>(b, d);
  x2 = a - c;
  x3 = addi<-SG>(b, d);
}
template <int SG>
__device__ __forceinline__ c32 w16(int m) {
  constexpr float C[16] = {1.f, 0.92387953251128674f, 0.70710678118654752f, 0.38268343236508977f, 0.f, -0.38268343236508977f,
                           -0.70710678118654752f, -0.92387953251128674f, -1.f, -0.92387953251128674f, -0.70710678118654752f,
                           -0.38268343236508977f, 0.f, 0.38268343236508977f, 0.70710678118654752f, 0.92387953251128674f};
  return c32{C[m & 15], (float)SG * C[(m + 12) & 15]};
}
template <int SG>
__device__ __forceinline__ void dft16(c32 (&v)[16]) {
#pragma unroll
  for (int n2 = 0; n2 < 4; ++n2) dft4<SG>(v[n2], v[4 + n2], v[8 + n2], v[12 + n2]);
#pragma unroll
  for (int k1 = 1; k1 < 4; ++k1)
#pragma unroll
    for (int n2 = 1; n2 < 4; ++n2)
      if (n2 * k1 != 4) v[4 * k1 + n2] = cmuls(v[4 * k1 + n2], w16<SG>(n2 * k1));
#pragma unroll
  for (int k1 = 0; k1 < 4; ++k1)
    if (k1 == 2) dft4<SG, true>(v[8], v[9], v[10], v[11]);
    else dft4<SG>(v[4 * k1], v[4 * k1 + 1], v[4 * k1 + 2], v[4 * k1 + 3]);
  c32 t[16];
#pragma unroll
  for (int k1 = 0; k1 < 4; ++k1)
#pragma unroll
    for (int k2 = 0; k2 < 4; ++k2) t[k1 + 4 * k2] = v[4 * k1 + k2];
#pragma unroll
  for (int i = 0; i < 16; ++i) v[i] = t[i];
}
template <int SG>
__device__ __forceinline__ void dft8(c32 (&v)[8]) {
#pragma unroll
  for (int n2 = 0; n2 < 2; ++n2) dft4<SG>(v[n2], v[2 + n2], v[4 + n2], v[6 + n2]);
#pragma unroll
  for (int k1 = 1; k1 < 4; k1 += 2) v[2 * k1 + 1] = cmuls(v[2 * k1 + 1], w16<SG>(2 * k1));
  c32 t[8];
#pragma unroll
  for (int k1 = 0; k1 < 4; ++k1) {
    t[k1] = k1 == 2 ? addi<SG>(v[4], v[5]) : v[2 * k1] + v[2 * k1 + 1];
    t[k1 + 4] = k1 == 2 ? addi<-SG>(v[4], v[5]) : v[2 * k1] - v[2 * k1 + 1];
  }
#pragma unroll
  for (int i = 0; i < 8; ++i) v[i] = t[i];
}
__device__ __forceinline__ void lds_fence_wave() {
  __builtin_amdgcn_s_waitcnt(0xc07f);
  asm volatile("" ::: "memory");
}
template <int SG>
__device__ __forceinline__ void twiddle16(c32 (&u)[16], c32 w1, c32 w4) {
  const c32 w2 = cmul<false>(w1, w1), w3 = cmul<false>(w2, w1), w8 = cmul<false>(w4, w4), w12 = cmul<false>(w8, w4);
  const c32 lo[4] = {w1, w1, w2, w3}, hi[4] = {w4, w4, w8, w12};
#pragma unroll
  for (int k = 1; k < 16; ++k)
    u[k] = cmul<(SG > 0)>(u[k], (k & 3) ? ((k >> 2) ? cmul<false>(hi[k >> 2], lo[k & 3]) : lo[k & 3]) : hi[k >> 2]);
}
template <int N, int SG>
__device__ __forceinline__ void wave_fft(c32 (&v)[N / 64], c32* eb, const c32* tw, int lane) {
  constexpr int P = N / 64, B = P / 16, R3 = N / 256, M = N / 16, S1 = M + R3, S2 = 256 + 16 / R3, TS = 2048 / N;
  asm volatile("" : "+v"(lane));
#pragma unroll
  for (int b = 0; b < B; ++b) {
    c32 u[16];
#pragma unroll
    for (int n1 = 0; n1 < 16; ++n1) u[n1] = v[b + B * n1];
    dft16<SG>(u);
    const int n2 = lane + 64 * b;
    twiddle16<SG>(u, tw[n2 * TS], tw[n2 * TS * 4]);
#pragma unroll
    for (int k1 = 0; k1 < 16; ++k1) eb[k1 * S1 + n2] = u[k1];
    __builtin_amdgcn_sched_barrier(0);
  }
  lds_fence_wave();
  {
    c32 u[B][16];
#pragma unroll
    for (int b = 0; b < B; ++b) {
      const int q = lane + 64 * b, k1 = q / R3, m2 = q % R3;
#pragma unroll
      for (int m1 = 0; m1 < 16; ++m1) u[b][m1] = eb[k1 * S1 + m1 * R3 + m2];
    }
    const c32 w1 = tw[16 * (lane % R3) * TS], w4 = tw[64 * (lane % R3) * TS];
    lds_fence_wave();
#pragma unroll
    for (int b = 0; b < B; ++b) {
      const int q = lane + 64 * b, k1 = q / R3, m2 = q % R3;
      dft16<SG>(u[b]);
      twiddle16<SG>(u[b], w1, w4);
#pragma unroll
      for (int j1 = 0; j1 < 16; ++j1) eb[m2 * S2 + k1 + 16 * j1] = u[b][j1];
      __builtin_amdgcn_sched_barrier(0);
    }
  }
  lds_fence_wave();
#pragma unroll
  for (int c = 0; c < 4; ++c) {
    c32 u[R3];
#pragma unroll
    for (int m2 = 0; m2 < R3; ++m2) u[m2] = eb[m2 * S2 + lane + 64 * c];
    if constexpr (R3 == 4) dft4<SG>(u[0], u[1], u[2], u[3]);
    else dft8<SG>(u);
#pragma unroll
    for (int j2 = 0; j2 < R3; ++j2) v[c + 4 * j2] = u[j2];
  }
  lds_fence_wave();
}

constexpr int FFT_EB = 2176;
constexpr int LDS_TW = 0;
constexpr int LDS_EB = 16384;
constexpr int LDS_STG = 16384 + 8 * 1088 * 8;

__device__ __forceinline__ void fft_table(char* lds, int w) {
  c32* tw = (c32*)(lds + LDS_TW);
  {
    const int m = w * 64 + olane();
    float sn, cs;
    sincospif(-(float)m * (1.f / 1024.f), &sn, &cs);
    tw[m] = c32{cs, sn};
  }
}
#define XSK 12
__device__ __forceinline__ int x1_chunk(int kb, int rb) { return kb * 128 + ((rb + XSK * kb) & 127); }
#define F1NT true
__device__ __forceinline__ void fft_in1(const u16* x, const u16* wr, const u16* wi, int task, int w) {
  const int lane = olane(), r0 = task * 16 + 2 * w;
  const int eo = LDS_EB + w * 8704, fo = r0 * DF * 2;
#pragma unroll
  for (int q = 0; q < 4; ++q) glds16<F1NT>(x, r0 * D * 2 + q * 1024 + lane * 16, eo + q * 1024);
#pragma unroll
  for (int q = 0; q < 3; ++q)
    if (q < 2 || lane < 2) {
      const int o = min((fo & ~15) + q * 1024 + lane * 16, S * DF * 2 - 16);
      glds16<F1NT>(wr, o, eo + 4096 + q * 1024);
      glds16<F1NT>(wi, o, eo + 6176 + q * 1024);
    }
}
__device__ __forceinline__ void fft_pass1(char* ws, const u16* __restrict__ x, const u16* __restrict__ wr,
                                          const u16* __restrict__ wi, int task, int w, const u16* xb) {
  char* lds = g_lds;
  const __amdgpu_buffer_rsrc_t rx = __builtin_amdgcn_make_buffer_rsrc(ws + OFF_X1, 0, 65 * 131072, 0x00020000);
  const int lane = olane(), tid_ = w * 64 + lane;
  const c32* tw = (const c32*)(lds + LDS_TW);
  c32* eb = (c32*)(lds + LDS_EB) + w * 1088;
  c32(*stg)[16] = (c32(*)[16])(lds + LDS_STG);
  const int r0 = task * 16 + 2 * w;
#define F1T(i)
  F1T(0);
  const int fo = r0 * DF * 2;
  if (xb) {
    unsigned keep;
    asm volatile("s_mov_b32 %0, m0\n\ts_mov_b32 m0, %3\n\ts_nop 0\n\tglobal_load_lds_dword %1, %2\n\ts_mov_b32 m0, %0"
                 : "=&s"(keep) : "v"(task * 65536 + tid_ * 128), "s"(xb), "s"(160000 + w * 256) : "memory");
  } else {
    fft_in1(x, wr, wi, task, w);
    VMCNT(0);
  }
  F1T(1);
  c32 v[16];
  const u16* in = (const u16*)eb;
#pragma unroll
  for (int t = 0; t < 16; ++t) v[t] = c32{bf2f(in[lane + 64 * t]), bf2f(in[D + lane + 64 * t])};
  unsigned fw[2][9];
#pragma unroll
  for (int r = 0; r < 2; ++r)
#pragma unroll
    for (int t = 0; t < 9; ++t) {
      const int o = 2048 + ((fo & 15) >> 1) + r * DF + min(lane + 64 * t, 512);
      fw[r][t] = (unsigned)in[o] | ((unsigned)in[o + 1040] << 16);
    }
  lds_fence_wave();
  F1T(2);
  wave_fft<1024, -1>(v, eb, tw, lane);
  F1T(3);
#pragma unroll
  for (int t = 0; t < 16; ++t) eb[lane + 64 * t] = v[t];
  lds_fence_wave();
#pragma unroll
  for (int t = 0; t < 9; ++t) {
    const int k = lane + 64 * t;
    if (k <= 512) {
      c32 z = eb[k], zc = eb[(1024 - k) & 1023];
      *(f32x4*)&stg[k][2 * (w ^ (k & 7))] = f32x4{0.5f * (z.x + zc.x), 0.5f * (z.y - zc.y), 0.5f * (z.y + zc.y), -0.5f * (z.x - zc.x)};
    }
  }
  __syncthreads();
  F1T(4);
#pragma unroll
  for (int i = 0; i < 9; ++i) {
    const int idx = tid_ + 512 * i, k = min(idx >> 3, 512), p = idx & 7;
    v4u d = *(const v4u*)&stg[k][2 * (p ^ (k & 7))];
    st128(d, rx, idx < 513 * 8 ? lane * 16 : OOB, x1_chunk(min(w + 8 * i, 64), task) * 1024);
  }
  {
    const __amdgpu_buffer_rsrc_t rt = __builtin_amdgcn_make_buffer_rsrc(ws + OFF_WT, 0, 65 * 65536, 0x00020000);
    v2u* s2 = (v2u*)(lds + LDS_EB);
#pragma unroll
    for (int t = 0; t < 9; ++t) {
      const int k = lane + 64 * t;
      if (k <= 512) s2[k * 8 + (w ^ (k & 6))] = v2u{fw[0][t], fw[1][t]};
    }
    __syncthreads();
#pragma unroll
    for (int i = 0; i < 5; ++i) {
      const int idx = tid_ + 512 * i, k = min(idx >> 2, 512), p = idx & 3;
      const v4u d = *(const v4u*)&s2[k * 8 + ((2 * p) ^ (k & 6))];
      __builtin_amdgcn_raw_buffer_store_b128(d, rt, idx < 514 * 4 ? x1_chunk(idx >> 5, task) * 512 + (idx & 31) * 16 : OOB, 0, AUX_SC1);
    }
  }
}
__device__ __forceinline__ void fft_pass2(char* ws, int task, int w) {
  char* lds = g_lds;
  const __amdgpu_buffer_rsrc_t rx = __builtin_amdgcn_make_buffer_rsrc(ws + OFF_X1, 0, 65 * 131072, 0x00020000);
  const int lane = olane();
  const int k = task * 8 + w;
  const c32* tw = (const c32*)(lds + LDS_TW);
  c32* eb = (c32*)(lds + LDS_EB) + w * FFT_EB;
#define F2T(i)
  if (k > 512) return;
  F2T(0);
  c32 v[32];
  const int xoff = (lane >> 4) * 1024 + w * 128 + (lane & 15) * 8;
#pragma unroll
  for (int t = 0; t < 32; ++t) {
    v2u d = __builtin_amdgcn_raw_buffer_load_b64(rx, xoff, x1_chunk(task, 4 * t) * 1024, AUX_SC1);
    v[t] = c32{__uint_as_float(d[0]), __uint_as_float(d[1])};
  }
  unsigned wv[32];
  asm volatile("" : "+s"(task));
  {
    const __amdgpu_buffer_rsrc_t rt = __builtin_amdgcn_make_buffer_rsrc(ws + OFF_WT, 0, 65 * 65536, 0x00020000);
#pragma unroll
    for (int t = 0; t < 32; ++t)
      wv[t] = __builtin_amdgcn_raw_buffer_load_b32(rt, xoff >> 1, x1_chunk(task, 4 * t) * 512, AUX_SC1);
  }
  F2T(1);
  wave_fft<2048, -1>(v, eb, tw, lane);
  F2T(2);
#pragma unroll
  for (int t = 0; t < 32; ++t) v[t] = cmul<false>(v[t], c32{__uint_as_float(wv[t] << 16), __uint_as_float(wv[t] & 0xffff0000u)});
  F2T(3);
  wave_fft<2048, 1>(v, eb, tw, lane);
  F2T(4);
  asm volatile("" : "+s"(task));
#pragma unroll
  for (int t = 0; t < 32; ++t) {
    v2u d = {__float_as_uint(v[t].x), __float_as_uint(v[t].y)};
    __builtin_amdgcn_raw_buffer_store_b64(d, rx, xoff, x1_chunk(task, 4 * t) * 1024, AUX_SC1);
  }
  F2T(5);
  VMCNT(0);
  F2T(6);
}
#define OAUX3 16
#define OAUXX 0
__device__ __forceinline__ void fft_pass3(const char* ws, u16* __restrict__ out, int task, int w) {
  char* lds = g_lds;
  const __amdgpu_buffer_rsrc_t rx = __builtin_amdgcn_make_buffer_rsrc((void*)(ws + OFF_X1), 0, 65 * 131072, 0x00020000);
  const int lane = olane(), tid_ = w * 64 + lane;
  const c32* tw = (const c32*)(lds + LDS_TW);
  c32* eb = (c32*)(lds + LDS_EB) + w * 1088;
  c32(*stg)[16] = (c32(*)[16])(lds + LDS_STG);
  {
    v4u d[9];
#pragma unroll
    for (int i = 0; i < 9; ++i) {
      d[i] = __builtin_amdgcn_raw_buffer_load_b128(rx, (i < 8 ? lane : lane & 7) * 16, x1_chunk(min(w + 8 * i, 64), task) * 1024, AUX_SC1);
    }
#pragma unroll
    for (int i = 0; i < 9; ++i) {
      const int idx = tid_ + 512 * i, k = min(idx >> 3, 512), p = idx & 7;
      *(v4u*)&stg[k][2 * (p ^ (k & 7))] = d[i];
    }
  }
  __syncthreads();
  c32 v[16];
#pragma unroll
  for (int t = 0; t < 16; ++t) {
    const int n = lane + 64 * t;
    const int k = n <= 512 ? n : 1024 - n;
    const f32x4 xx = *(const f32x4*)&stg[k][2 * (w ^ (k & 7))];
    c32 xa = {xx[0], xx[1]}, xb = {xx[2], xx[3]};
    if (k == 0 || k == 512) {
      xa.y = 0.f;
      xb.y = 0.f;
    }
    v[t] = n <= 512 ? c32{xa.x - xb.y, xa.y + xb.x} : c32{xa.x + xb.y, -xa.y + xb.x};
  }
  wave_fft<1024, 1>(v, eb, tw, lane);
  const float sc = 1.f / (2048.f * 1024.f);
  const int r0 = task * 16 + 2 * w;
  const __amdgpu_buffer_rsrc_t ro = __builtin_amdgcn_make_buffer_rsrc(out, 0, S * 4096, 0x00020000);
#pragma unroll
  for (int t = 0; t < 16; ++t) {
    __builtin_amdgcn_raw_buffer_store_b16(f2bf(v[t].x * sc), ro, lane * 2 + t * 128, r0 * 4096, OAUX3);
    __builtin_amdgcn_raw_buffer_store_b16(f2bf(v[t].y * sc), ro, lane * 2 + t * 128, r0 * 4096 + 4096, OAUX3);
  }
}

#define OB2 0
#define FIE 0
#define FLC 0
#define WBP 8
#define WBQ 2
#define COT 100
#define CUP 24
#define CDN 1
#define CLO -40
#define CHI 30
#define TLW 900
#define OSL 4

#define OCK 800
#define OTRY 3
#define APR 0
#define OGN 5
#define OK1 150
#define NX2 0
#define WTO 2
#define WTS 30
#define WTN 2
#define RSO 5
#define NCAL 6
#define DOX2 200
#define DTC 100
#define NX1 4
#define NXG 2
#define NXK 5
#define OFD 200
#define STI 600
#define TNX 0
#define NXP 0
#define PACE 150
#define PFB 4
#define CV1 -20
#define CV2 125
#define EDL 0
constexpr int L_D = 16384, L_R = 32768, L_F = 122880;
__device__ __forceinline__ volatile LAS unsigned* flw(int i, int m) {
  return (volatile LAS unsigned*)((LAS char*)g_lds + L_F + i * 4 + (olane() & m) * 4);
}
template <int SLP = 0>
__device__ __forceinline__ void lds_wait_ge(int i, int m, unsigned v, gu32* tmo, unsigned code) {
  volatile LAS unsigned* p = flw(i, m);
  _Pragma("clang loop unroll(disable)") for (unsigned spins = 0;;) {
    if (__all(*p >= v)) break;
    if (SLP) __builtin_amdgcn_s_sleep(SLP);
    if (++spins > (kSpinCap << 5)) {
      tmo_report(tmo, code);
      break;
    }
  }
  asm volatile("" ::: "memory");
}
template <bool PRE>
__device__ __forceinline__ unsigned owner(const int c, const int w, const u64 nonce, const u16* __restrict__ mem0, char* ws,
                                          u16* __restrict__ out, unsigned perm, const char* wsl, const u16* ax, const u16* awr,
                                          const u16* awi) {
  LAS char* const L = (LAS char*)g_lds;
  gu32* tmo = (gu32*)(ws + OFF_TMO);
  const int lane = olane();
  const int l15 = lane & 15, g = lane >> 4;
  const __amdgpu_buffer_rsrc_t rs = __builtin_amdgcn_make_buffer_rsrc(ws, 0, (int)WS_USED, 0x00020000);
  const int xg = c / PO, li = c % PO;
  const int r_a = (int)(((uintptr_t)ws >> 19) & 7);
  const int r_s = r_a + (int)(OFF_STEP / UNIT);
  volatile LAS unsigned* fl = (volatile LAS unsigned*)(L + L_F);
  unsigned m0v = 0;
  if constexpr (PRE) {
  if (c == 0 && w == 6) hs_lead<4>(rs, true, CNT_HSO, 64u);
  if (WTO == 1 && wsl) {
    __builtin_amdgcn_s_sleep(WTS);
    for (int k = 0; k < WTN; ++k) w_touch(wsl, (li >> 1) * 128 + 64 + (li & 1) * 32 + (k & 1) * 16 - (k >> 1) * 64, 160000 + w * 256);
  }
  unsigned cal = 0;
  if (w == 0)
    st128(v4u{(unsigned)lane, 0u, 0u, 0u}, rs, (lane & 31) * 16 + (lane >> 5) * 32768, (int)OFF_SCR + (((li & 7) ^ xreg(r_a)) << 12) + (c >> 3) * 512);
  if (w == 0 && lane == 0) {
    const int cl = li & 7;
    int off = (int)OFF_SCR + ((cl ^ xreg(r_a)) << 12) + (c >> 3) * 512;
    unsigned best = 0xfffeu;
#pragma unroll 1
    for (int r = 0; r < NCAL; ++r) {
      VMCNT(0);
      const u64 t1 = __builtin_amdgcn_s_memtime();
      asm volatile("s_waitcnt lgkmcnt(0)" : "+v"(off)::"memory");
      const unsigned v = __builtin_amdgcn_raw_buffer_load_b32(rs, off, (r & 3) * 128 + (r >> 2) * 32768, AUX_SC1);
      asm volatile("s_waitcnt vmcnt(0)" ::"v"(v) : "memory");
      const u64 t2 = __builtin_amdgcn_s_memtime();
      best = min(best, (unsigned)(t2 - t1));
    }
    cal = ((unsigned)cl << 16) | best;
  }
  if (w == 0) {
    m0v = *(const unsigned*)(mem0 + (lane >> 3) * D + 16 * c + 2 * (lane & 7));
    hs_wait(rs, ws, nonce, CNT_HSO);
    unsigned id;
    asm volatile("s_getreg_b32 %0, hwreg(HW_REG_XCC_ID)" : "=s"(id));
    if (lane == 0) {
      __builtin_amdgcn_raw_buffer_store_b32(((id & 15u) << 19) | cal, rs, c * CST, (int)OFF_CAL, AUX_SC1);
    }
  }
  __syncthreads();
  if (FIE == 1) fft_in1(ax, awr, awi, c, w);
  if (WTO == 2 && wsl)
    for (int k = 0; k < WTN; ++k) w_touch(wsl, (li >> 1) * 128 + 64 + (li & 1) * 32 + (k & 1) * 16 - (k >> 1) * 64, 160000 + w * 256);
  if (FIE == 2) fft_in1(ax, awr, awi, c, w);
  if (w == 7) {
    const unsigned pm = numa_perm(numa_load(rs, tmo), (LAS unsigned*)(L + L_F + 512));
    fl[10] = pm;
  }
  RAW_BARRIER();
  perm = __builtin_amdgcn_readfirstlane(fl[10]);
  }
  const int xr0 = xreg(r_s + (PRE ? w : NARM + (li & 1)));
  const v4u sent = {0xffffffffu, 0xffffffffu, 0xffffffffu, 0xffffffffu};
  const int ibs = (int)OFF_VM + li * 65536 + ((cls_of(perm, xg) ^ xreg(r_a + (int)(OFF_VM / UNIT) + (li >> 3))) << 12);

  int am = (int)OFF_STEP + R_MEM + (c >> 4) * 32768 + ((cls_of(perm, g) ^ xr0) << 12) + (c & 15) * 256 + l15 * 16;
  int ap[2];
#pragma unroll
  for (int q = 0; q < 2; ++q) {
    const int o = c * 384 + (lane & 31) * 16;
    ap[q] = (lane & 31) < 24 ? (int)OFF_STEP + R_EX + (o >> 12) * 32768 + ((cls_of(perm, 2 * q + (lane >> 5)) ^ xr0) << 12) + (o & 4095)
                             : OOB;
  }
  int as = lane < 8 * NXC ? (int)OFF_STEP + R_ST + ((cls_of(perm, lane >> 3) ^ xr0) << 12) + (c >> 5) * 32768 + (c & 31) * 128 + (lane & 7) * 16
                          : OOB;
  const int ax1 = (int)OFF_STEP + R_PX + c * 1280 + lane * 16;
  const int ax2 = lane < 16 ? ax1 + 1024 : (lane < 24 ? (int)OFF_STEP + R_DUM + c * 128 + (lane - 16) * 16 : OOB);
#define ARM_REC(r)                                                                                                   \
  {                                                                                                                  \
    const int so_ = (r) * (int)UNIT, dx_ = (xreg(r_s + (r)) ^ xreg(r_s + (r) + 1)) << 12;                            \
    __builtin_amdgcn_raw_buffer_store_b128(sent, rs, am, so_, AUX_SC1);                                              \
    am ^= dx_;                                                                                                       \
    _Pragma("unroll") for (int q = 0; q < 2; ++q) {                                                                  \
      __builtin_amdgcn_raw_buffer_store_b128(sent, rs, ap[q], so_, AUX_SC1);                                         \
      ap[q] ^= dx_;                                                                                                  \
    }                                                                                                                \
    __builtin_amdgcn_raw_buffer_store_b128(sent, rs, as, so_, AUX_SC1);                                              \
    as ^= dx_;                                                                                                       \
    __builtin_amdgcn_raw_buffer_store_b128(sent, rs, ax1, so_, AUX_SC1);                                              \
    __builtin_amdgcn_raw_buffer_store_b128(sent, rs, ax2, so_, AUX_SC1);                                             \
  }
  if constexpr (PRE) {
    if (WTO == 3 && wsl)
      for (int k = 0; k < WTN; ++k) w_touch(wsl, (li >> 1) * 128 + 64 + (li & 1) * 32 + (k & 1) * 16 - (k >> 1) * 64, 160000 + w * 256);
    if (!FIE) fft_in1(ax, awr, awi, c, w);
    if (w < NARM + (li & 1)) {
      if (w == 0) {
        const int row = lane >> 3, np = lane & 7;
#pragma unroll
        for (int r = 0; r < NXC; ++r)
          __builtin_amdgcn_raw_buffer_store_b32(min(m0v, 0xfffffffeu), rs,
                                                (int)OFF_STEP + R_MEM + (c >> 4) * 32768 + ((cls_of(perm, r) ^ xr0) << 12) + (c & 15) * 256 +
                                                    ((row & 1) + 2 * (np & 1) + 4 * (row >> 1)) * 16 + (np >> 1) * 4,
                                                0, AUX_SC1);
        am = OOB;
      }
      ARM_REC(w)
    }
#pragma unroll
    for (int k = 0; k < 4; ++k)
      __builtin_amdgcn_raw_buffer_store_b128(sent, rs, lane * 16 + k * 1024, ibs + vm_slot(w >> 1) + (w & 1) * 32768, AUX_SC1);
    if (c == 0 && w == 7) __builtin_amdgcn_raw_buffer_store_b32(perm, rs, lane << 28, OFF_PERM, AUX_SC1);
  }
  if constexpr (PRE) return perm;
  if (FLC || OB2) RAW_BARRIER();
  if (w == 7) {
    if (lane < 32) fl[lane] = 0;
    arm_wait(rs, tmo);
  }
  if (OB2) RAW_BARRIER();
  if (FLC && (w < 2 || w == 7)) VMCNT(0);

  const int team = li & 1, c0 = c & ~1;
  const int l16 = lane * 16;
  const int cown = cls_of(perm, xg);
  if (w < 2) {
    const int eg = (c0 >> 2) & 1;
    typedef int v8i __attribute__((ext_vector_type(8)));
    v8i adr;
    adr[0] = l16;
    adr[1] = (lane >> 2) * 128 + (lane & 3) * 16;
    adr[2] = L_D + w * 8192 + eg * 4096 + l16;
    adr[3] = ((g & 1) + (l15 & 2) + 4 * ((g & 2) + (l15 & 1))) * 16 + (l15 >> 2) * 4;
    adr[4] = (l15 & 1) ? 0x03020706 : 0x05040100;
    adr[5] = ((l15 & 8) ? 0xeeee : 0x4444) | ((l15 & 2) ? 0xeeee0000 : 0x44440000);
    adr[6] = L_F + 48;
    adr[7] = adr[2] ^ 4096;
    const v4u rsv = {(unsigned)(uintptr_t)ws, (unsigned)((uintptr_t)ws >> 32) & 0xffffu, (unsigned)WS_USED, 0x00020000u};
    const int dtc_ = TN(12, DTC);
#pragma unroll 1
    for (int s = team; s < NSEG; s += 2) {
      EVDECL;
      const int so = (int)OFF_STEP + s * (int)UNIT;
      const int sp = so + R_PM + ((cown ^ xreg(r_s + s)) << 12);
      int sv = ibs + (w - team) * 65536 + vm_slot(s);
      int sv1 = sv + eg * 32768, sv2 = sv + (1 - eg) * 32768, sp1 = sp + eg * 32768, sp2 = sp + (1 - eg) * 32768;
      const int xn = xreg(r_s + s + 1);
      int sm[NXC];
#pragma unroll
      for (int q = 0; q < NXC; ++q) {
        sm[q] = so + (int)UNIT + R_MEM + (c0 >> 4) * 32768 + ((cls_of(perm, q) ^ xn) << 12) + ((c0 + w) & 15) * 256;
        asm volatile("" : "+s"(sm[q]));
      }
      asm volatile("" : "+s"(sv1), "+s"(sv2), "+s"(sp1), "+s"(sp2));
      if (s >= 2) lds_wait_ge<4>(20, 3, s - 1, tmo, 0x30017);
      v4u bl[8];
      u64 tp64 = 0;
      if (s < NSEG - 1) {
        v32u blv;
        unsigned cnt, bad;
        asm volatile(OW_F
                     : [cnt] "=&s"(cnt), [st] "=&s"(bad), [tp] "=&s"(tp64), "={v[0:31]}"(blv) EVOUT
                     : "{v[112:119]}"(adr), [rs] "s"(rsv), [sv1] "s"(sv1), [sv2] "s"(sv2), [sp1] "s"(sp1), [sp2] "s"(sp2),
                       [sm0] "s"(sm[0]), [sm1] "s"(sm[1]), [sm2] "s"(sm[2]), [sm3] "s"(sm[3])
                     : OF_CLOB, "vcc", "scc", "memory");
#pragma unroll
        for (int k = 0; k < 8; ++k) bl[k] = v4u{blv[4 * k], blv[4 * k + 1], blv[4 * k + 2], blv[4 * k + 3]};
        if (__builtin_expect(bad != 0u, 0)) {
          v4u pv[8];
          _Pragma("clang loop unroll(disable)") for (unsigned spins = 0;;) {
            OPQ(va, l16);
            OPQ(v2, adr[1]);
#pragma unroll
            for (int k = 0; k < 8; ++k) bl[k] = __builtin_amdgcn_raw_buffer_load_b128(rs, va + (k & 3) * 1024, k < 4 ? sv1 : sv2, AUX_SC1);
#pragma unroll
            for (int k = 0; k < 8; ++k)
              pv[k] = __builtin_amdgcn_raw_buffer_load_b128(rs, v2 + (k & 1) * 2048 + (k >> 2) * 64, (k & 2) ? sp2 : sp1, AUX_SC1);
            unsigned m = 0;
#pragma unroll
            for (int k = 0; k < 8; ++k) m = mx4(mx4(m, pv[k]), bl[k]);
            if (!anysent(m)) break;
            __builtin_amdgcn_s_sleep(RSO);
            if (spin_fail(spins, tmo, 0x20000 + s)) break;
          }
          f32x4 a0 = {0.f, 0.f, 0.f, 0.f};
#pragma unroll
          for (int k = 0; k < 4; ++k) {
            v8u b = cat8(bl[2 * k], bl[2 * k + 1]);
#pragma unroll
            for (int j = 0; j < 8; j += 2) {
              const auto q = __builtin_amdgcn_permlane16_swap(b[j], b[j + 1], false, false);
              b[j] = q[0];
              b[j + 1] = q[1];
            }
            a0 = smf(pv[k], b, a0, adr[5]);
          }
#pragma unroll
          for (int k = 0; k < 4; ++k) {
            LAS char* const pd = L + adr[k < 2 ? 2 : 7] + (k & 1) * 2048;
            a0 = smf1(pv[4 + k], cat8(*(const LAS v4u*)pd, *(const LAS v4u*)(pd + 1024)), a0, adr[5]);
          }
          const auto q0 = __builtin_amdgcn_permlane32_swap(__float_as_uint(a0[0]), __float_as_uint(a0[2]), false, false);
          const auto q1 = __builtin_amdgcn_permlane32_swap(__float_as_uint(a0[1]), __float_as_uint(a0[3]), false, false);
          const unsigned own = pk2(__uint_as_float(q0[0]) + __uint_as_float(q0[1]), __uint_as_float(q1[0]) + __uint_as_float(q1[1]));
          const unsigned nb = (unsigned)__builtin_amdgcn_mov_dpp((int)own, 0xb1, 0xf, 0xf, true);
          const unsigned o = __builtin_amdgcn_perm(nb, own, (unsigned)adr[4]);
#pragma unroll
          for (int q = 0; q < NXC; ++q) __builtin_amdgcn_raw_buffer_store_b32(o, rs, adr[3], sm[q], AUX_SC1);
          tp64 = cyc32();
          EV(3);
        }
      } else {
        BARE_BARRIER();
        wait_cyc(*flw(12, 0));
        _Pragma("clang loop unroll(disable)") for (unsigned spins = 0;;) {
          OPQ(va, l16);
          unsigned m = 0;
#pragma unroll
          for (int k = 0; k < 8; ++k) bl[k] = __builtin_amdgcn_raw_buffer_load_b128(rs, va + (k & 3) * 1024, k < 4 ? sv1 : sv2, AUX_SC1);
#pragma unroll
          for (int k = 0; k < 8; ++k) m = mx4(m, bl[k]);
          if (!anysent(m)) break;
          __builtin_amdgcn_s_sleep(RSO);
          if (spin_fail(spins, tmo, 0x20000 + s)) break;
        }
      }
      const unsigned tp = (unsigned)tp64;
      *flw(2 + w, 0) = s + 1;
      if (s < NSEG - 1) {
        const v4u tv = *(volatile LAS v4u*)flw(12, 0);
        const int lr = olane();
        __builtin_amdgcn_raw_buffer_store_b32(min(((tp - tv[1]) * tv[2]) >> 16, 60000u), rs,
                                              ((cls_of(perm, lr & 3) ^ xn) << 12) | ((lr & 4) << 9) | ((lr & 8) << 12) | ((lr >> 4) << 28),
                                              so + (int)UNIT + R_ST + (c0 + w) * 32 + 4, AUX_SC1);
      }
      OPQ(vs, l16);
#pragma unroll
      for (int k = 0; k < 8; ++k) *(LAS v4u*)(L + w * 8192 + ((k ^ (4 * eg)) * 1024) + vs) = bl[k];
      asm volatile("s_waitcnt lgkmcnt(0)" ::: "memory");
      *flw(4 + w, 0) = s + 1;
      if (s < NSEG - 4) {
        lds_wait_ge(2, 1, s + 1, tmo, 0x3001a);
        v4u se;
        asm volatile("v_mov_b32 %0, -1" : "=v"(se[0]));
        se[1] = se[2] = se[3] = se[0];
#pragma unroll
        for (int k = 0; k < 8; ++k) __builtin_amdgcn_raw_buffer_store_b128(se, rs, vs + (k & 3) * 1024, sv + (k >> 2) * 32768, AUX_SC1);
        asm volatile("s_nop 1" ::"v"(se));
      }
      if (s < NSEG - 2) {
        wait_cyc(tp + dtc_);
        const int lt = olane();
        const unsigned tc = __builtin_amdgcn_raw_buffer_load_b32(rs, lt < 40 ? lt * 128 : OOB, so + R_PX + ((li & ~1) + w) * 5120, AUX_SC1);
        asm volatile("" ::"v"(tc));
      }
      EVDUMP(s);
    }
  } else if (w < 6) {
    const int n = w - 2;
    const unsigned vpm_ = perm | (unsigned)((r_a + (int)(OFF_VWX / UNIT)) & 7) << 12;
    const int dox_ = TN(10, DOX2), pace_ = TN(11, PACE);
    v4u raw[2][8];
#define RAW_LD(dst, sg)                                                                                       \
  _Pragma("unroll") for (int t = 0; t < 2; ++t) _Pragma("unroll") for (int k = 0; k < 8; ++k) dst[t][k] =     \
      __builtin_amdgcn_raw_buffer_load_b128(rs, l16 + (k & 3) * 1024, vw_addr(vpm_, c0 + t, sg, 2 * n + (k >> 2)), AUX_SC1);
#pragma unroll
    for (int k = 0; k < 16; ++k) asm volatile("" : "=v"(raw[k >> 3][k & 7]));
    _Pragma("clang loop unroll(disable)") for (unsigned spins = 0, got = 0; got != 255u;) {
      OPQ(vfl, F_VW + flag_of(team, 8 * n + (olane() & 7)));
      const unsigned nw = (unsigned)__builtin_amdgcn_ballot_w64(__builtin_amdgcn_raw_buffer_load_b32(rs, vfl, 0, AUX_SC1) == 0u) & 255u & ~got;
      got |= nw;
#pragma unroll
      for (int k = 0; k < 8; ++k)
        if (nw >> k & 1u) {
#pragma unroll
          for (int t = 0; t < 2; ++t)
            raw[t][k] = __builtin_amdgcn_raw_buffer_load_b128(rs, l16 + (k & 3) * 1024, vw_addr(vpm_, c0 + t, team, 2 * n + (k >> 2)), AUX_SC1);
        }
      if (!nw) __builtin_amdgcn_s_sleep(OSL);
      if (spin_fail(spins, tmo, 0x30018)) break;
    }
    if (FLC) VMCNT(0);
    int nrdy = team;
#pragma unroll 1
    for (int s = team; s < NSEG; s += 2) {
      EVDECL;
      const int so = (int)OFF_STEP + s * (int)UNIT;
      if (s < NSEG - 1) {
        const int ln = olane();
        OPQ(eidx, (ln & 4) ? 0xeeee : 0x4444);
        OPQ(vea, (ln & 4) * 128 + (ln & 8) * 32 + (ln >> 4) * 64 + (ln & 3) * 16);
        int se = so + R_EX + n * 32768 + ((cown ^ xreg(r_s + s)) << 12);
        v8u bx[2][4];
#pragma unroll
        for (int t = 0; t < 2; ++t)
#pragma unroll
          for (int k = 0; k < 4; ++k) {
            const v4u u = raw[t][2 * k], v = raw[t][2 * k + 1];
            bx[t][k] = v8u{u[0], v[0], u[1], v[1], u[2], v[2], u[3], v[3]};
            asm volatile("" : "+v"(bx[t][k]));
          }
        OPQ(vd, L_D + n * 2048 + (ln & 31) * 16 + (ln >> 5) * 8);
        f32x4 d[2][4];
#pragma unroll
        for (int t = 0; t < 2; ++t)
#pragma unroll
          for (int k = 0; k < 4; ++k) d[t][k] = zero4();
        asm volatile("" : "+s"(se));
        EV(3);
        asm volatile(AL(A_OW_D));
        BARE_BARRIER();
        if (EDL) asm volatile("s_nop %0" ::"n"(EDL > 0 ? EDL - 1 : 0));
        EV(0);
#define E_PACK(t, k) v2u{pk2(d[t][k][0], d[t][k][1]), pk2(d[t][k][2], d[t][k][3])}
        _Pragma("clang loop unroll(disable)") for (unsigned spins = 0;;) {
          v4u pv[4];
          asm volatile("" : "+v"(vea));
#pragma unroll
          for (int k = 0; k < 4; ++k) pv[k] = __builtin_amdgcn_raw_buffer_load_b128(rs, vea + k * 1024, se, AUX_SC1);
          unsigned m = 0;
          v2u cv[2][4];
          SB();
#pragma unroll
          for (int k = 0; k < 4; ++k) {
            d[0][k] = smf(pv[k], bx[0][k], d[0][k], eidx);
            m = mx4(m, pv[k]);
            asm volatile("" : "+v"(m), "+v"(d[0][k]));
            d[1][k] = smf(pv[k], bx[1][k], d[1][k], eidx);
            asm volatile("" : "+v"(d[1][k]));
            if (k > 0) {
              const int j = k > 0 ? k - 1 : 0;
              asm volatile("" : "+v"(d[0][j]), "+v"(d[1][j]));
              cv[0][j] = E_PACK(0, j);
              cv[1][j] = E_PACK(1, j);
              *(LAS v2u*)(L + vd + j * 512) = cv[0][j];
              *(LAS v2u*)(L + vd + j * 512 + 8192) = cv[1][j];
            }
            SB();
          }
          EV(1);
          *(LAS v2u*)(L + vd + 1536) = E_PACK(0, 3);
          SB();
          *(LAS v2u*)(L + vd + 9728) = E_PACK(1, 3);
          if (__builtin_expect(!anysent(m), 1)) break;
          __builtin_amdgcn_s_sleep(RSO);
          if (spin_fail(spins, tmo, 0x10000 + s)) break;
#pragma unroll
          for (int t = 0; t < 2; ++t)
#pragma unroll
            for (int k = 0; k < 4; ++k) d[t][k] = zero4();
        }
        EV(2);
        RAW_BARRIER();
      } else BARE_BARRIER();
      v4u nx[2][8];
      const int sn = min(s + 2, NSEG - 1);
      unsigned tpb = 0;
      if (s < NSEG - 2) {
        __builtin_amdgcn_s_sleep(5);
        lds_wait_ge(2, 1, s + 1, tmo, 0x30014);
        tpb = cyc32();
        const unsigned tw = tpb + dox_;
        _Pragma("clang loop unroll(disable)") for (unsigned spins = 0; nrdy < s + 2;) {
          const int lf = olane();
          OPQ(vfl, F_VW + flag_of(min(s + 2 + 2 * (lf >> 3), NSEG - 2 + team), 8 * n + (lf & 7)));
          const u64 miss = __builtin_amdgcn_ballot_w64(__builtin_amdgcn_raw_buffer_load_b32(rs, vfl, 0, AUX_SC1) != 0u);
          nrdy = s + 2 * (miss ? __builtin_ctzll(miss) >> 3 : 8);
          if (nrdy >= s + 2) break;
          __builtin_amdgcn_s_sleep(8);
          if (spin_fail(spins, tmo, 0x30018)) break;
        }
        wait_cyc(tw);
      }
      EV(6);
      OPQ(va, s < NSEG - 2 ? l16 : OOB);
#define NX_LD(dst, q0, q1)                                                                              \
  _Pragma("unroll") for (int q = q0; q < q1; ++q) dst[q >> 3][q & 7] = __builtin_amdgcn_raw_buffer_load_b128( \
      rs, va + (q & 3) * 1024, vw_addr(vpm_, c0 + (q >> 3), sn, 2 * n + ((q >> 2) & 1)), AUX_SC1);
      NX_LD(nx, 0, NX1)
      SB();
      if (s < NSEG - 2) wait_cyc(cyc32() + ((n + (li >> 1)) & 3) * pace_);
      else if (s == NSEG - 2 || !TLW) lds_wait_ge(4, 1, s + 1, tmo, 0x30014);
      else wait_cyc(cyc32() + TLW);
      EV(4);
      f32x4 ac[2][2];
      const int sx = so + R_PX + n * 8192, sy = so + R_PX + 32768 + n * 2048;
      v4u pv[2][10], vm[2][2];
      OPQ(vb, l16);
#define P_LD                                                                                         \
  _Pragma("unroll") for (int k = 0; k < 10; ++k) _Pragma("unroll") for (int rt = 0; rt < 2; ++rt) pv[rt][k] = \
      __builtin_amdgcn_raw_buffer_load_b128(rs, vb + (k & 3) * 1024, (k < 8 ? sx + (k >> 2) * 4096 : sy) + rt * 40960, AUX_SC1);
#define P_MM(k0, k1)                                                                                 \
  _Pragma("unroll") for (int k = k0; k < k1; ++k) _Pragma("unroll") for (int rt = 0; rt < 2; ++rt) {  \
    _Pragma("unroll") for (int t = 0; t < 2; ++t)                                                    \
      ac[rt][t] = mma(k < 8 ? raw[t][k & 7] : vm[t][k & 1], pv[rt][k], k == 0 ? f32x4{0.f, 0.f, 0.f, 0.f} : ac[rt][t]); \
    asm volatile("" : "+v"(ac[rt][0]), "+v"(ac[rt][1]));                                             \
  }
      P_LD
      SB();
      NX_LD(nx, NX1, NX1 + NX2)
      SB();
      lds_wait_ge(4, 1, s + 1, tmo, 0x30014);
#pragma unroll
      for (int t = 0; t < 2; ++t)
#pragma unroll
        for (int k = 0; k < 2; ++k) vm[t][k] = *(const LAS v4u*)(L + vb + t * 8192 + n * 2048 + k * 1024);
      SB();
      P_MM(0, 10)
      if (__builtin_expect(__builtin_amdgcn_ballot_w64(__builtin_isunordered(ac[0][0][0], ac[1][0][0])) != 0ull, 0)) {
        _Pragma("clang loop unroll(disable)") for (unsigned spins = 0;;) {
          asm volatile("" : "+v"(vb));
          P_LD
          unsigned m = 0;
#pragma unroll
          for (int k = 0; k < 10; ++k) m = mx4(mx4(m, pv[0][k]), pv[1][k]);
          if (!anysent(m)) break;
          __builtin_amdgcn_s_sleep(8);
          if (spin_fail(spins, tmo, 0x30015)) break;
        }
        P_MM(0, 10)
      }
      EV(7);
      NX_LD(raw, NX1 + NX2, 16)
      SB();
      OPQ(vr, L_R + (s & 2) * 8192 + l16);
#pragma unroll
      for (int q = 0; q < 4; ++q) *(LAS f32x4*)(L + vr + q * 4096 + n * 1024) = ac[q >> 1][q & 1];
      asm volatile("s_waitcnt lgkmcnt(0)" ::: "memory");
      fl[20 + n] = s + 1;
      lds_wait_ge(20, 3, s + 1, tmo, 0x30016);
      {
        const LAS f32x4* pr = (const LAS f32x4*)(L + vr + n * 4096);
        const f32x4 o = (pr[0] + pr[64]) + (pr[128] + pr[192]);
        const int lo = olane();
        __builtin_amdgcn_raw_buffer_store_b64(v2u{pk2(o[0], o[1]), pk2(o[2], o[3])}, __builtin_amdgcn_make_buffer_rsrc(out, 0, S * 4096, 0x00020000),
                                              (lo & 15) * 8192 + (lo >> 4) * 8, (s * 32 + (n >> 1)) * 4096 + 2048 + 32 * (c0 + (n & 1)), OAUXX);
      }
      EV(5);
#pragma unroll
      for (int q = 0; q < NX1 + NX2; ++q) raw[q >> 3][q & 7] = nx[q >> 3][q & 7];
      EVDUMP(s);
    }
  } else if (w == 6) {
    if (c == 1) hs_lead<64>(rs, false, CNT_HS, 192u);
#pragma unroll 1
    for (int s = team; s + NARM - 2 < NSEG; s += 2) {
      VMCNT(0);
      BARE_BARRIER();
      if (FLC && s == team && olane() == 0) __hip_atomic_fetch_add((gu32*)(ws + CNT_FFT1), 1u, RLX_AGENT);
      if (s + NARM >= NSEG) break;
      BARE_BARRIER();
      __builtin_amdgcn_s_sleep(5);
      lds_wait_ge(2, 1, s + 1, tmo, 0x3001b);
      if (APR) __builtin_amdgcn_s_setprio(APR);
      ARM_REC(s + NARM)
      if (s + NARM + 1 < NSEG) ARM_REC(s + NARM + 1)
      if (APR) __builtin_amdgcn_s_setprio(0);
      if ((WBL & 4) && li == 0 && s % WBP == WBQ) asm volatile("buffer_wbl2 sc1" ::: "memory");
    }
  } else {
    const int sdk_ = NS2F(TN(2, SDK)), cv2_ = NS2F(TN(4, CV2));
    int cv1_ = NS2F(TN(3, CV1));
    bool clean = false;
    Clk ck;
    clk_init(ck);
    unsigned t1 = cyc32();
    const int mg_ = NS2F(TN(0, MARG)), tol_ = NS2F(TN(13, TOL)), xl_ = NS2F(TN(14, XLATE));
    Pace pc = {0u, NS2F(ED0), NS2F(EA0), 0};
    const int sti_ = TN(15, STI), ofd_ = NS2F(TN(9, OFD));
#pragma unroll 1
    for (int s = 0; s < NSEG; ++s) {
      EVDECL;
      const bool on = (s & 1) == team;
      asm volatile(AL(A_OW_T));
      EV(3);
      if (on ? (int)(t1 - cyc32()) > OCK : s < 4) {
        wait_cyc(t1 - OCK);
        clk_update<OTRY>(ck, s < 4 ? 12 : OGN);
      }
      int k1 = ((cv1_ * (int)ck.cq) >> 16) - OK1, k2 = (cv2_ * (int)ck.cq) >> 16;
      int sst = (int)OFF_STEP + s * (int)UNIT + R_ST + ((cown ^ xreg(r_s + s)) << 12);
      asm volatile("" : "+s"(k1), "+s"(k2), "+s"(sst));
      OPQ(va, lane * 32);
      OPQ(vt, L_F + 48);
      wait_cyc(t1);
      __builtin_amdgcn_s_setprio(3);
      EV(0);
      v2u st = __builtin_amdgcn_raw_buffer_load_b64(rs, va, sst, AUX_SC1);
      if (__builtin_expect(anysent(st[0]), 0)) _Pragma("clang loop unroll(disable)") for (unsigned spins = 0;;) {
        if (spins >= 2) __builtin_amdgcn_s_sleep(4);
        if (spin_fail(spins, tmo, 0x3000b)) break;
        asm volatile("" : "+v"(va));
        wait_cyc(t1 + spins * sti_);
        st = __builtin_amdgcn_raw_buffer_load_b64(rs, va, sst + (spins == 1 ? 2048 : (spins == 2 ? 32768 : 0)), AUX_SC1);
        if (spins > 2) clk_update(ck, 8);
        if (!anysent(st[0])) break;
      }
      EV(4);
      u64 tn;
      asm volatile("s_memtime %0" : "=s"(tn), "+v"(st));
      unsigned md;
      const int dm = stamp_max(ck, st, md);
      const unsigned tr = clk_cyc(ck, dm);
      if (on) {
        asm volatile("s_waitcnt lgkmcnt(0)" : "+s"(tn));
        *(volatile LAS v4u*)(L + vt) = v4u{tr + k2, tr, ck.inv, 0u};
        EV(1);
        int d = (int)(tr + k1 - (unsigned)tn);
        d = (unsigned)d > (unsigned)CMAX ? 0 : d;
        asm volatile(".p2align 4\n1: s_sub_u32 %0, %0, 28\n\ts_cbranch_scc0 1b\n\ts_barrier" : "+s"(d)::"scc", "memory");
        if (s < NSEG - 1) BARE_BARRIER();
      }
      __builtin_amdgcn_s_setprio(0);
      EV(5);
      const bool ol = md - (unsigned)(pc.ed + NS2F(COT)) < 60000u;
      if (s > 8) cv1_ = ol && clean ? min(cv1_ + CUP, NS2F(CHI)) : max(cv1_ - CDN, NS2F(CLO));
      pace_step(pc, s, (ck.r << 4) + dm, md, mg_, tol_, xl_);
      clean = !ol && pc.dq == pc.ed + mg_;
      if (c == 0 && (s & 3) == 1) __builtin_amdgcn_raw_buffer_store_b32((unsigned)s, rs, olane() << 28, OFF_PROG, AUX_SC1);
      t1 = clk_cyc(ck, dm + pc.dq + pc.ea - sdk_ + (on ? ofd_ : 0));
    }
  }
  return 0u;
}
#define WSH 0
#define STG2 0
#define STG3 0
#define FTL 0
#define CPE 0
#define FSL 32
__global__ __launch_bounds__(512, 2) void mega_kernel(const u16* __restrict__ x, const u16* __restrict__ mem0,
                                                      const u16* __restrict__ Wq, const u16* __restrict__ Wk,
                                                      const u16* __restrict__ Wv, const u16* __restrict__ Wo,
                                                      char* __restrict__ ws, u16* __restrict__ out,
                                                      const u16* __restrict__ ax, const u16* __restrict__ awr,
                                                      const u16* __restrict__ awi) {
  asm volatile(".Lkbeg:");
  if (CPE) {
    unsigned keep, t;
    const int w0 = __builtin_amdgcn_readfirstlane(threadIdx.x >> 6);
    asm volatile("s_getpc_b64 vcc\n.Lce:\n\ts_mov_b32 %0, m0\n\ts_mov_b32 m0, %4\n\ts_mov_b32 %1, .Lrole0-.Lce\n\t"
                 "s_cmp_eq_u32 %3, 1\n\ts_cselect_b32 %1, .Lrole1-.Lce, %1\n\ts_cmp_eq_u32 %3, 2\n\ts_cselect_b32 %1, .Lrole2-.Lce, %1\n\t"
                 "s_cmp_eq_u32 %3, 3\n\ts_cselect_b32 %1, .Lrole3-.Lce, %1\n\t"
                 "s_add_u32 vcc_lo, vcc_lo, %1\n\ts_addc_u32 vcc_hi, vcc_hi, 0\n\t"
                 "global_load_lds_dword %2, vcc\n\ts_mov_b32 m0, %0"
                 : "=&s"(keep), "=&s"(t)
                 : "v"(((threadIdx.x & 7) | (threadIdx.x >> 7) << 3) * 128), "s"((w0 & 1) | (blockIdx.x & 4) >> 1),
                   "s"((blockIdx.x & 132) == 128 ? 160000 + w0 * 256 : 32768 + w0 * 2048)
                 : "vcc", "scc", "memory");
  }
  ws += (size_t)(-(uintptr_t)ws) & (UNIT - 1);
  const int bx = blockIdx.x;
  const int w = __builtin_amdgcn_readfirstlane(threadIdx.x >> 6);
  const int xg = bx & 7, idx = bx >> 3;
#define TEND
#define TMID(k)
  const u64 nonce = hs_post(__builtin_amdgcn_make_buffer_rsrc(ws, 0, 65536, 0x00020000), w);
  TMID(0);
  int role, h, par = 0;
  if (xg < NXC) {
    role = idx >= 2 * PQ ? 4 : idx >= PQ;
    h = xg * PQ + idx % PQ;
  } else {
    role = 2 + (idx / PQ & 1);
    par = idx / (2 * PQ);
    h = (xg - NXC) * PQ + idx % PQ;
  }
  if (CPF && role == 4 && (w == 1 || w == 2)) code_touch(idx - 16 * (w - 1), 160000 + 256 * w);
  if (FTL && role == 4 && w >= 3 && w < 6) {
    unsigned keep;
    asm volatile("s_mov_b32 %0, m0\n\ts_mov_b32 m0, %3\n\ts_nop 0\n\tglobal_load_lds_dword %1, %2\n\ts_mov_b32 m0, %0"
                 : "=&s"(keep)
                 : "v"((xg * PO + idx - 2 * PQ) * (w == 3 ? 32768 : 16416) + (olane() & (FTL - 1)) * (16384 / (FTL ? FTL : 1))),
                   "s"(w == 3 ? ax : (w == 4 ? awr : awi)), "s"(160000 + w * 256)
                 : "memory");
  }
  if (role == 0) wroles<0>(h, par, idx & 7, xg, w, nonce, x, Wq, Wk, Wv, Wo, ws);
  if (role == 1) wroles<1>(h, par, idx & 7, xg, w, nonce, x, Wq, Wk, Wv, Wo, ws);
  if (role == 2) wroles<2>(h, par, idx & 7, xg, w, nonce, x, Wq, Wk, Wv, Wo, ws);
  if (role == 3) wroles<3>(h, par, idx & 7, xg, w, nonce, x, Wq, Wk, Wv, Wo, ws);
  const int oc = xg * PO + idx - 2 * PQ;
  unsigned perm = 0;
  if (role == 4) perm = owner<true>(oc, w, nonce, mem0, ws, out, 0u, w_slow(Wq, Wk, Wv, w, xg), ax, awr, awi);
  TMID(2);
  char* lds = g_lds;
  const __amdgpu_buffer_rsrc_t rs = __builtin_amdgcn_make_buffer_rsrc(ws, 0, 65536, 0x00020000);
  gu32* tmo = (gu32*)(ws + OFF_TMO);
  volatile LAS int* sact = (volatile LAS int*)(g_lds + 163584);
  unsigned idle = 0;
#define TLOG(id)
  auto pick = [&]() __attribute__((always_inline)) {
    int off = CNT_FFT1 + (olane() & 7) * 128;
    asm volatile("" : "+v"(off));
    const unsigned v = __builtin_amdgcn_raw_buffer_load_b32(rs, off, 0, AUX_SC1) + 1u;
    const unsigned d1 = __builtin_amdgcn_readlane(v, 0), d2 = __builtin_amdgcn_readlane(v, 1);
    const unsigned q1 = __builtin_amdgcn_readlane(v, 4), q2 = __builtin_amdgcn_readlane(v, 5), q3 = __builtin_amdgcn_readlane(v, 6);
    int kind = -1, qoff = 0;
    unsigned lim = 0;
    if (q1 < 64u) kind = 1, qoff = Q_FFT1, lim = 64u;
    else if (d1 >= 128u && q2 < 65u) kind = 2, qoff = Q_FFT2, lim = 65u;
    else if (d2 >= 65u && q3 < 128u) kind = 3, qoff = Q_FFT3, lim = 128u;
    else if (q3 >= 128u) kind = -2;
    int act = kind;
    if (kind >= 0) {
      unsigned t = 0;
      if (olane() == 0) t = __hip_atomic_fetch_add((gu32*)(ws + qoff), 1u, RLX_AGENT) + 1u;
      t = __builtin_amdgcn_readfirstlane(t);
      act = t < lim ? (kind << 16) | (int)t : -3;
    }
    return act;
  };
  fft_table(lds, w);
  int done = role == 4 ? CNT_ARM : 0;
  for (;;) {
    if (w == 7) *sact = role == 4 ? (done == CNT_ARM ? 0x10040 | oc : -2) : pick();
    VMCNT(0);
    RAW_BARRIER();
    if (done && w == 0 && olane() == 0) __hip_atomic_fetch_add((gu32*)(ws + done), 1u, RLX_AGENT);
    done = 0;
    const int act = __builtin_amdgcn_readfirstlane(*sact);
    RAW_BARRIER();
    if (act == -2) break;
    if (act >= 0) {
      const int kind = act >> 16, t = act & 0xffff;
      for (int k = kind == 3 ? t * STG3 >> 1 : (kind == 2 && t < 32 ? t * STG2 : 0); k > 0; --k) __builtin_amdgcn_s_sleep(1);
      TLOG(1000 + kind);
      if (kind == 1) {
        fft_pass1(ws, ax, awr, awi, t ^ 64, w, role == 4 ? x : nullptr);
        done = CNT_FFT1;
        if (FLC && role == 4) break;
      } else if (kind == 2) {
        fft_pass2(ws, t == 0 ? 64 : t - 1, w);
        done = CNT_FFT2;
      } else fft_pass3(ws, out, t, w);
      TLOG(999);
    } else if (act == -1) {
      __builtin_amdgcn_s_sleep(FSL);
      if (spin_fail(idle, tmo, 0x30022)) break;
    }
  }
  if (role == 4) {
    owner<false>(oc, w, nonce, mem0, ws, out, perm, nullptr, nullptr, nullptr, nullptr);
    return;
  }
  if (WBL & 1)
    if (w == 0) asm volatile("buffer_wbl2 sc1\n\ts_waitcnt vmcnt(0)" ::: "memory");
  TEND;
}

extern "C" void kernel_launch(void* const* d_in, const int* in_sizes, int n_in, void* d_out, int out_size, void* d_ws,
                              size_t ws_size, hipStream_t stream) {
  const u16* A_x = (const u16*)d_in[0];
  const u16* A_wr = (const u16*)d_in[1];
  const u16* A_wi = (const u16*)d_in[2];
  const u16* B_x = (const u16*)d_in[3];
  const u16* B_mem0 = (const u16*)d_in[4];
  const u16* Wq = (const u16*)d_in[5];
  const u16* Wk = (const u16*)d_in[6];
  const u16* Wv = (const u16*)d_in[7];
  const u16* Wo = (const u16*)d_in[8];
  u16* out = (u16*)d_out;
  char* ws = (char*)d_ws + ((size_t)WSH << 20);

  mega_kernel<<<256, 512, 0, stream>>>(B_x, B_mem0, Wq, Wk, Wv, Wo, ws, out, A_x, A_wr, A_wi);
}
```

```cpp
#include <hip/hip_runtime.h>
#include <hip/hip_bf16.h>
#include <stdint.h>
#include <type_traits>

typedef unsigned short u16;
typedef unsigned long long u64;
using bf16x8 = __attribute__((ext_vector_type(8))) __bf16;
using bf16x16 = __attribute__((ext_vector_type(16))) __bf16;
using f32x4 = __attribute__((ext_vector_type(4))) float;
typedef unsigned v32u __attribute__((ext_vector_type(32)));
typedef unsigned v16u __attribute__((ext_vector_type(16)));
typedef unsigned v8u __attribute__((ext_vector_type(8)));
typedef unsigned v4u __attribute__((ext_vector_type(4)));
typedef unsigned v2u __attribute__((ext_vector_type(2)));
typedef short s16x4 __attribute__((ext_vector_type(4)));
typedef float f32x2 __attribute__((ext_vector_type(2)));
#define GAS __attribute__((address_space(1)))
#define LAS __attribute__((address_space(3)))
typedef GAS unsigned gu32;
#define RLX_AGENT __ATOMIC_RELAXED, __HIP_MEMORY_SCOPE_AGENT

constexpr int S = 2048, D = 1024, H = 32, DH = 128, SEG = 32, NSEG = 64;
constexpr int DF = 513;

constexpr int NXC = 4;
constexpr int PQ = 32 / NXC, PO = 64 / NXC;

__shared__ __attribute__((aligned(16))) char g_lds[163840];
#define SPINCAP (1u << 18)
constexpr unsigned kSpinCap = SPINCAP;
constexpr int AUX_SC1 = 16;
constexpr int OOB = 0x7ff00000;
constexpr size_t UNIT = 524288;
constexpr int F_QK = 0, F_VW = 8192, CNT_FFT1 = 16384, CNT_FFT2 = 16512, CNT_ARM = 16640;
constexpr int Q_FFT1 = 16896, Q_FFT2 = 17024, Q_FFT3 = 17152;
constexpr size_t OFF_TMO = 20480;
#define CST 128
constexpr size_t OFF_CAL = 28672;
constexpr int OFF_PERM = 25600;
constexpr int OFF_PROG = 24576;
constexpr size_t ARM_HOST = 28672 + (64 * CST + 1023) / 1024 * 1024;
constexpr int OFF_REQ = 49152, OFF_ACK = 53248, CNT_HS = 17280, CNT_HSO = 17408;
constexpr size_t OFF_SCR = 65536;
constexpr size_t OFF_VM = UNIT;
constexpr int VM_SLOT = 4 << 20, VM_SLOT2 = 8 << 20;
constexpr size_t OFF_DBG = 3 * UNIT;
constexpr size_t OFF_STEP = 27 * UNIT;
constexpr int R_MEM = 0, R_EX = 131072, R_PM = 262144, R_ST = 327680, R_PX = 393216, R_DUM = 475136;
constexpr size_t OFF_QK = (size_t)48 << 20;
constexpr size_t OFF_X1 = (size_t)80 << 20;
constexpr size_t OFF_VWX = (size_t)89 << 20;
constexpr size_t WS_USED = (size_t)154 << 20;
constexpr size_t OFF_WT = (size_t)154 << 20;
constexpr int NARM = 4;

__device__ __forceinline__ int olane() {
  int t;
  asm volatile("v_mbcnt_lo_u32_b32 %0, -1, 0\n\tv_mbcnt_hi_u32_b32 %0, -1, %0" : "=v"(t));
  __builtin_assume(t >= 0 && t < 64);
  return t;
}
__device__ __forceinline__ float bf2f(u16 v) { return __uint_as_float((unsigned)v << 16); }
__device__ __forceinline__ unsigned pk2(float a, float b) {
  typedef __bf16 b2 __attribute__((ext_vector_type(2)));
  f32x2 f = {a, b};
  b2 r = __builtin_convertvector(f, b2);
  return __builtin_bit_cast(unsigned, r);
}
__device__ __forceinline__ unsigned pk2s(float a, float b) { return min(pk2(a, b), 0xfffffffeu); }
__device__ __forceinline__ u16 f2bf(float f) { return (u16)(pk2(f, 0.f) & 0xffffu); }
__device__ __forceinline__ float rbf(float f) { return __uint_as_float(pk2(0.f, f)); }
__device__ __forceinline__ float opq_one() {
  float o = 1.f;
  asm("" : "+s"(o));
  return o;
}
__device__ __forceinline__ unsigned fold_pk(f32x4 a, float one) { return pk2(a[0] + a[2], __builtin_fmaf(a[1], one, a[3])); }
__device__ __forceinline__ float mul1(float a, float c) {
  float r;
  asm("v_mul_f32 %0, %2, %1" : "=v"(r) : "v"(a), "s"(c));
  return r;
}
__device__ __forceinline__ float fma1(float a, float c, float b) {
  float r;
  asm("v_fma_f32 %0, %1, %2, %3" : "=v"(r) : "v"(a), "s"(c), "v"(b));
  return r;
}
__device__ __forceinline__ int flag_of(int seg, int h) { return h * 256 + (seg & 1) * 128 + (seg >> 1) * 4; }
__device__ __forceinline__ int xreg(int r) { return ((r & 1) << 2) | ((r >> 1) & 1) | ((r >> 1) & 2); }

__device__ __forceinline__ void tmo_report(gu32* tmo, unsigned code) {
  unsigned z = 0, c = code;
  asm volatile("" : "+v"(z), "+v"(c));
  asm volatile("global_store_dword %0, %1, %2 sc1" ::"v"(z), "v"(c), "s"(tmo) : "memory");
}
__device__ __forceinline__ bool spin_fail(unsigned& spins, gu32* tmo, unsigned code) {
  asm volatile("" ::: "memory");
  if (__builtin_expect(++spins > kSpinCap, 0)) {
    tmo_report(tmo, code);
    return true;
  }
  return false;
}
__device__ __forceinline__ unsigned wave_min(unsigned v) {
  v = min(v, (unsigned)__builtin_amdgcn_mov_dpp((int)v, 0x121, 0xf, 0xf, true));
  v = min(v, (unsigned)__builtin_amdgcn_mov_dpp((int)v, 0x122, 0xf, 0xf, true));
  v = min(v, (unsigned)__builtin_amdgcn_mov_dpp((int)v, 0x124, 0xf, 0xf, true));
  v = min(v, (unsigned)__builtin_amdgcn_mov_dpp((int)v, 0x128, 0xf, 0xf, true));
  auto r = __builtin_amdgcn_permlane32_swap(v, v, false, false);
  v = min(r[0], r[1]);
  auto q = __builtin_amdgcn_permlane16_swap(v, v, false, false);
  return min(q[0], q[1]);
}
__device__ __forceinline__ float xmax16_32(float x) {
  auto r = __builtin_amdgcn_permlane32_swap(__float_as_uint(x), __float_as_uint(x), false, false);
  x = __builtin_amdgcn_fmed3f(__uint_as_float(r[0]), __uint_as_float(r[1]), __builtin_inff());
  auto q = __builtin_amdgcn_permlane16_swap(__float_as_uint(x), __float_as_uint(x), false, false);
  return __builtin_amdgcn_fmed3f(__uint_as_float(q[0]), __uint_as_float(q[1]), __builtin_inff());
}
__device__ __forceinline__ float xsum16(float x) {
  auto q = __builtin_amdgcn_permlane16_swap(__float_as_uint(x), __float_as_uint(x), false, false);
  return __uint_as_float(q[0]) + __uint_as_float(q[1]);
}
__device__ __forceinline__ float xsum16_32(float x) {
  auto r = __builtin_amdgcn_permlane32_swap(__float_as_uint(x), __float_as_uint(x), false, false);
  return xsum16(__uint_as_float(r[0]) + __uint_as_float(r[1]));
}
__device__ __forceinline__ unsigned mx4(unsigned m, v4u v) { return max(max(max(max(m, v[0]), v[1]), v[2]), v[3]); }
__device__ __forceinline__ bool anysent(unsigned m) { return __builtin_amdgcn_ballot_w64(m == 0xffffffffu) != 0ull; }

__device__ __forceinline__ unsigned now32() { return (unsigned)__builtin_amdgcn_s_memrealtime(); }
__device__ __forceinline__ unsigned cyc32() { return (unsigned)__builtin_amdgcn_s_memtime(); }
#define CMAX 14000
__device__ __forceinline__ void wait_cyc(unsigned t) {
  t = __builtin_amdgcn_readfirstlane(t);
  int d = (int)(t - cyc32());
  if (d > CMAX) return;
  while (d > 260) {
    __builtin_amdgcn_s_sleep(1);
    d = (int)(t - cyc32());
  }
  d = __builtin_amdgcn_readfirstlane(d - 70);
  if (d > 0) asm volatile(".p2align 4\n1: s_sub_u32 %0, %0, 28\n\ts_cbranch_scc0 1b" : "+s"(d)::"scc", "memory");
}
#define CLKSPAN 124u
struct Clk {
  unsigned r, m;
  unsigned cq, inv;
};
__device__ __forceinline__ bool clk_edge(unsigned& r, unsigned& m) {
  u64 t[14], m0, m1;
  unsigned n;
#define CE_S(i) "s_memrealtime %" #i "\n\ts_nop 0\n\t"
  asm volatile("s_setprio 3\n\ts_memtime %14\n\t" CE_S(0) CE_S(1) CE_S(2) CE_S(3) CE_S(4) CE_S(5) CE_S(6) CE_S(7) CE_S(8) CE_S(9) CE_S(10)
               CE_S(11) CE_S(12) "s_memtime %15\n\ts_memrealtime %13\n\ts_setprio 0\n\ts_waitcnt lgkmcnt(0)"
               : "=s"(t[0]), "=s"(t[1]), "=s"(t[2]), "=s"(t[3]), "=s"(t[4]), "=s"(t[5]), "=s"(t[6]), "=s"(t[7]), "=s"(t[8]), "=s"(t[9]),
                 "=s"(t[10]), "=s"(t[11]), "=s"(t[12]), "=s"(t[13]), "=s"(m0), "=s"(m1));
#define CE_C(i) "s_cmp_eq_u32 %" #i ", %1\n\ts_addc_u32 %0, %0, 0\n\t"
#define CE_T(i) "s"((unsigned)t[i])
  asm("s_mov_b32 %0, 0\n\t" CE_C(2) CE_C(3) CE_C(4) CE_C(5) CE_C(6) CE_C(7) CE_C(8) CE_C(9) CE_C(10) CE_C(11) CE_C(12) CE_C(13) CE_C(14)
      : "=&s"(n)
      : CE_T(0), CE_T(1), CE_T(2), CE_T(3), CE_T(4), CE_T(5), CE_T(6), CE_T(7), CE_T(8), CE_T(9), CE_T(10), CE_T(11), CE_T(12), CE_T(13)
      : "scc");
  r = (unsigned)t[0] + 4;
  m = (unsigned)m0 + n * 8;
  return (unsigned)m1 - (unsigned)m0 <= CLKSPAN;
}
__device__ __forceinline__ void clk_init(Clk& c) {
#pragma unroll 1
  for (int k = 0; k < 3; ++k)
    if (clk_edge(c.r, c.m)) break;
  c.cq = 92500u;
  c.inv = 46432u;
}
template <int TRIES = 1>
__device__ __forceinline__ void clk_update(Clk& c, int gain) {
  unsigned r, m;
  bool ok = clk_edge(r, m);
#pragma unroll 1
  for (int k = 1; k < TRIES && !ok; ++k) ok = clk_edge(r, m);
  ok |= r - c.r >= 1500u;
  const unsigned dr = r - c.r;
  const int err = (int)(m - c.m - ((dr * c.cq) >> 12));
  unsigned q = c.cq + (unsigned)((ok & (dr * gain < 6000u)) ? err * gain : 0);
  asm("s_max_i32 %0, %0, 0x101d0\n\ts_min_i32 %0, %0, 0x1b580" : "+s"(q)::"scc");
  c.cq = q;
  c.inv -= (unsigned)__mulhi((int)c.inv, (int)(q * c.inv));
  c.r = ok ? r : c.r;
  c.m = ok ? m : c.m;
}
__device__ __forceinline__ int stamp_max(const Clk& c, v2u st, unsigned& md) {
  unsigned a = st[0] - (c.r << 4) + 0x40000000u, b = st[1], x;
#define SM_S(p) "v_max_u32_dpp %2, %2, %2 " p "\n\tv_max_u32_dpp %3, %3, %3 " p "\n\ts_nop 0\n\t"
  asm volatile("s_nop 1\n\t" SM_S("row_ror:1 row_mask:0xf bank_mask:0xf") SM_S("row_ror:2 row_mask:0xf bank_mask:0xf")
               SM_S("row_ror:4 row_mask:0xf bank_mask:0xf") SM_S("row_ror:8 row_mask:0xf bank_mask:0xf")
               SM_S("row_bcast:15 row_mask:0xa bank_mask:0xf") SM_S("row_bcast:31 row_mask:0xc bank_mask:0xf")
               "v_readlane_b32 %0, %2, 63\n\tv_readlane_b32 %1, %3, 63"
               : "=s"(x), "=s"(md), "+v"(a), "+v"(b));
  return (int)(x - 0x40000000u);
}
__device__ __forceinline__ unsigned clk_cyc(const Clk& c, int d) { return c.m + (unsigned)(((long long)d * (int)c.cq) >> 16); }
#define NS2F(ns) (((ns) * 8) / 5)
struct Pace {
  unsigned rf;
  int ed, ea, dq;
};
__device__ __forceinline__ void pace_step(Pace& p, int s, unsigned rf, unsigned md, int marg, int tol, int xlate) {
  const int arr = s ? (int)(rf - p.rf) - p.dq : 0x10000000;
  const bool late = arr > p.ea + tol;
  p.ea = min(arr, p.ea + 5);
  p.ed = (int)min(md, (unsigned)p.ed + 5u);
  p.rf = rf;
  p.dq = p.ed + marg + (late ? xlate : 0);
}

template <int SLP>
__device__ __forceinline__ void wait_flag(__amdgpu_buffer_rsrc_t rs, int off, gu32* tmo, unsigned code) {
  _Pragma("clang loop unroll(disable)") for (unsigned spins = 0;;) {
    asm volatile("" : "+v"(off));
    unsigned v = __builtin_amdgcn_raw_buffer_load_b32(rs, off, 0, AUX_SC1);
    if (__all(v == 0u)) break;
    __builtin_amdgcn_s_sleep(SLP);
    if (spin_fail(spins, tmo, code)) break;
  }
}
__device__ __forceinline__ void arm_wait(__amdgpu_buffer_rsrc_t rs, gu32* tmo) {
  _Pragma("clang loop unroll(disable)") for (unsigned spins = 0;;) {
    int off = CNT_ARM;
    asm volatile("" : "+v"(off));
    if (__all(__builtin_amdgcn_raw_buffer_load_b32(rs, off, 0, AUX_SC1) + 1u >= 64u)) break;
    __builtin_amdgcn_s_sleep(8);
    if (spin_fail(spins, tmo, 0x30007)) break;
  }
}
#define RAW_BARRIER()                                    \
  do {                                                   \
    asm volatile("" ::: "memory");                       \
    __builtin_amdgcn_s_waitcnt(0xc07f);                  \
    __builtin_amdgcn_s_barrier();                        \
    asm volatile("" ::: "memory");                       \
  } while (0)
#define BARE_BARRIER()                 \
  do {                                 \
    asm volatile("" ::: "memory");     \
    __builtin_amdgcn_s_barrier();      \
    asm volatile("" ::: "memory");     \
  } while (0)
#define VMCNT(n) asm volatile("s_waitcnt vmcnt(" #n ")" ::: "memory")
#define SB() __builtin_amdgcn_sched_barrier(0)
#define OPQ(v, e) int v = (e); asm volatile("" : "+v"(v))
template <bool NT = false>
__device__ __forceinline__ void glds16(const void* base, int voff, unsigned dst) {
  unsigned keep;
  if constexpr (NT)
    asm volatile("s_mov_b32 %0, m0\n\ts_mov_b32 m0, %3\n\ts_nop 0\n\tglobal_load_lds_dwordx4 %1, %2 nt\n\ts_mov_b32 m0, %0"
                 : "=&s"(keep)
                 : "v"(voff), "s"(base), "s"(dst)
                 : "memory");
  else
  asm volatile("s_mov_b32 %0, m0\n\ts_mov_b32 m0, %3\n\ts_nop 0\n\tglobal_load_lds_dwordx4 %1, %2\n\ts_mov_b32 m0, %0"
               : "=&s"(keep)
               : "v"(voff), "s"(base), "s"(dst)
               : "memory");
}
__device__ __forceinline__ const char* w_slow(const u16* Wq, const u16* Wk, const u16* Wv, int w, int xg) {
  const char* p = (const char*)(w < 2 || w == 6 ? Wq : (w < 4 || w == 7 ? Wk : Wv));
  const int sk = 3 - (int)((uintptr_t)p >> 7);
  const int h = w < 6 ? (xg & 3) * 8 + (w & 1) * 4 + ((sk & 7) >> 1) : ((sk + 60) & 63) >> 1;
  return (h >> 3) == (xg & 3) ? p + h * 256 : nullptr;
}
__device__ __forceinline__ void w_touch(const char* p, int row, unsigned dst) {
  unsigned keep;
  const int ln = olane();
  asm volatile("s_mov_b32 %0, m0\n\ts_mov_b32 m0, %3\n\ts_nop 0\n\tglobal_load_lds_dword %1, %2\n\ts_mov_b32 m0, %0"
               : "=&s"(keep)
               : "v"((row + (ln >> 2)) * 8192 + (ln & 3) * 64), "s"(p), "s"(dst)
               : "memory");
}
#define WBL 0
#define A_VO 4
#define A_QK 3
#define A_OW 0
#define A_VO_A A_VO
#define A_VO_B A_VO
#define A_QK_A0 A_QK
#define A_QK_A1 A_QK
#define A_QK_X A_QK
#define A_QK_K A_QK
#define A_OW_F 3
#define A_OW_D 4
#define A_OW_T 2
#define AL_(k) ".p2align 6\n.fill " #k ",4,0xbf800000\n"
#define AL(k) AL_(k)
#define NSL 8
#define CPF 1
constexpr int CODE_LEN = 168960;
__device__ __forceinline__ void code_touch(int part, unsigned dst) {
  unsigned keep;
  const int v = min(part * 8192 + olane() * 128, CODE_LEN - 128);
  asm volatile("s_getpc_b64 vcc\n.Lct%=:\n\ts_sub_u32 vcc_lo, vcc_lo, .Lct%=-.Lkbeg\n\ts_subb_u32 vcc_hi, vcc_hi, 0\n\t"
               "s_mov_b32 %0, m0\n\ts_mov_b32 m0, %2\n\ts_nop 0\n\t"
               "global_load_lds_dword %1, vcc\n\ts_mov_b32 m0, %0"
               : "=&s"(keep)
               : "v"(v), "s"(dst)
               : "vcc", "scc", "memory");
}
__device__ __forceinline__ void code_ahead(int part, unsigned dst) {
  unsigned keep;
  const int v = part * 2048 + (olane() & 15) * 128;
  asm volatile("s_getpc_b64 vcc\n\ts_mov_b32 %0, m0\n\ts_mov_b32 m0, %2\n\ts_nop 0\n\tglobal_load_lds_dword %1, vcc ; AHEAD\n\ts_mov_b32 m0, %0"
               : "=&s"(keep)
               : "v"(v), "s"(dst)
               : "vcc", "memory");
}
__device__ __forceinline__ f32x4 smf(v4u a, v8u b, f32x4 c, int idx) {
  return __builtin_amdgcn_smfmac_f32_16x16x64_bf16(__builtin_bit_cast(bf16x8, a), __builtin_bit_cast(bf16x16, b), c, idx, 0, 0);
}
__device__ __forceinline__ f32x4 smf1(v4u a, v8u b, f32x4 c, int idx) {
  return __builtin_amdgcn_smfmac_f32_16x16x64_bf16(__builtin_bit_cast(bf16x8, a), __builtin_bit_cast(bf16x16, b), c, idx, 0, 1);
}
__device__ __forceinline__ f32x4 mma(v4u a, v4u b, f32x4 c) {
  return __builtin_amdgcn_mfma_f32_16x16x32_bf16(__builtin_bit_cast(bf16x8, a), __builtin_bit_cast(bf16x8, b), c, 0, 0, 0);
}
__device__ __forceinline__ void st128(v4u d, __amdgpu_buffer_rsrc_t rs, int voff, int soff) {
  __builtin_amdgcn_raw_buffer_store_b128(d, rs, voff, soff, AUX_SC1);
  asm volatile("s_nop 1" ::"v"(d));
}
__device__ __forceinline__ f32x4 zero4() {
  f32x2 a, b;
  asm volatile("v_mov_b64 %0, 0" : "=v"(a));
  asm volatile("v_mov_b64 %0, 0" : "=v"(b));
  return f32x4{a[0], a[1], b[0], b[1]};
}
__device__ __forceinline__ f32x2 zero2() {
  f32x2 a;
  asm volatile("v_mov_b64 %0, 0" : "=v"(a));
  return a;
}
__device__ __forceinline__ v4u lo4(v8u v) { return v4u{v[0], v[1], v[2], v[3]}; }
__device__ __forceinline__ v4u hi4(v8u v) { return v4u{v[4], v[5], v[6], v[7]}; }
__device__ __forceinline__ v8u cat8(v4u a, v4u b) { return v8u{a[0], a[1], a[2], a[3], b[0], b[1], b[2], b[3]}; }
__device__ __forceinline__ v32u cat32(v8u a, v8u b, v8u c, v8u d) {
  const v16u x = __builtin_shufflevector(a, b, 0, 1, 2, 3, 4, 5, 6, 7, 8, 9, 10, 11, 12, 13, 14, 15);
  const v16u y = __builtin_shufflevector(c, d, 0, 1, 2, 3, 4, 5, 6, 7, 8, 9, 10, 11, 12, 13, 14, 15);
  return __builtin_shufflevector(x, y, 0, 1, 2, 3, 4, 5, 6, 7, 8, 9, 10, 11, 12, 13, 14, 15, 16, 17, 18, 19, 20, 21, 22, 23, 24, 25, 26,
                                 27, 28, 29, 30, 31);
}
__device__ __forceinline__ v2u trd(LAS char* p) {
  return __builtin_bit_cast(v2u, __builtin_amdgcn_ds_read_tr16_b64_v4i16((LAS s16x4*)p));
}
__device__ __forceinline__ int vm_slot(int s) { return (s & 1) * VM_SLOT + ((s >> 1) & 1) * VM_SLOT2; }
__device__ __forceinline__ int cls_of(unsigned perm, int x) { return (int)((perm >> (3 * x)) & 7u); }

__device__ __forceinline__ v2u numa_load(__amdgpu_buffer_rsrc_t rs, gu32* tmo) {
  unsigned c, xid;
  _Pragma("clang loop unroll(disable)") for (unsigned spins = 0;;) {
    OPQ(a, olane() * CST);
    c = __builtin_amdgcn_raw_buffer_load_b32(rs, a, (int)OFF_CAL, AUX_SC1);
    if (__all(c != 0xffffffffu)) break;
    __builtin_amdgcn_s_sleep(NSL);
    if (spin_fail(spins, tmo, 0x30008)) break;
  }
  xid = (unsigned)__builtin_amdgcn_ds_bpermute(((olane() >> 3) & (NXC - 1)) * PO * 4, (int)c) >> 19;
  return v2u{c, xid};
}
__device__ __forceinline__ unsigned numa_perm(v2u in, LAS unsigned* tab) {
  const int lane = olane();
  const unsigned c = in[0], xid = in[1];
  tab[lane] = 0xffffu;
  tab[64 + lane] = 0xffffu;
  tab[128 + lane] = 0u;
  __builtin_amdgcn_s_waitcnt(0xc07f);
  __hip_atomic_fetch_min(tab + ((c >> 16) & 127u), c & 0xffffu, __ATOMIC_RELAXED, __HIP_MEMORY_SCOPE_WORKGROUP);
  __hip_atomic_fetch_add(tab + 128 + ((c >> 21) & 3u) * 2 + ((c >> 17) & 1u), c & 0xffffu, __ATOMIC_RELAXED, __HIP_MEMORY_SCOPE_WORKGROUP);
  __builtin_amdgcn_s_waitcnt(0xc07f);
  asm volatile("" ::: "memory");
  const unsigned s0 = tab[128 + ((xid >> 2) & 3u) * 2], s1 = tab[129 + ((xid >> 2) & 3u) * 2];
  const bool far = ((lane >> 1) & 1) != (s1 < s0 ? 1 : 0);
  unsigned key = ((tab[(xid & 15u) * 8 + (lane & 7)] + (far ? 0x10000u : 0u)) << 6) | (unsigned)lane;
  if ((lane >> 3) >= NXC) key = 0xffffffffu;
  unsigned perm = 0;
#pragma unroll 1
  for (int r = 0; r < NXC; ++r) {
    const unsigned m = wave_min(key);
    const unsigned bx = (m >> 3) & 7u, bc = m & 7u;
    perm |= bc << (3 * bx);
    if ((unsigned)(lane >> 3) == bx || (unsigned)(lane & 7) == bc) key = 0xffffffffu;
  }
  return __builtin_amdgcn_readfirstlane(perm);
}
#define VRING 32
static_assert(OFF_VWX + (size_t)VRING * 2097152 <= WS_USED, "");
#define VWN 0
__device__ __forceinline__ int vw_addr(unsigned pm, int tile, int seg, int gq) {
  return (int)OFF_VWX + ((((tile >> 3) * VRING + (seg & (VRING - 1))) * 8 + (tile & 7)) * 8 + gq) * 4096;
}

#define QKN 0
constexpr int QK_RT = QKN ? 32768 : 4096;
__device__ __forceinline__ int qk_addr(int mat, int seg, int h, int cl, int ra) {
  return (int)OFF_QK + ((mat * 64 + seg) * 32 + h) * 8192;
}

__device__ __forceinline__ u64 hs_post(__amdgpu_buffer_rsrc_t rs, int w) {
  const u64 n = __builtin_amdgcn_s_memrealtime();
  if (w == 0) __builtin_amdgcn_raw_buffer_store_b64(v2u{(unsigned)n, (unsigned)(n >> 32)}, rs, olane() == 0 ? 0 : OOB,
                                                    OFF_REQ + (int)blockIdx.x * 8, AUX_SC1);
  return n;
}
__device__ __forceinline__ void hs_wait(__amdgpu_buffer_rsrc_t rs, char* ws, u64 n, int cnt) {
  _Pragma("clang loop unroll(disable)") for (unsigned spins = 0; spins < (kSpinCap << 2); ++spins) {
    int z = 0;
    asm volatile("" : "+v"(z));
    const v2u v = __builtin_amdgcn_raw_buffer_load_b64(rs, z, OFF_ACK + (int)blockIdx.x * 8, AUX_SC1);
    if (__all(v[0] == (unsigned)n && v[1] == (unsigned)(n >> 32))) break;
    __builtin_amdgcn_s_sleep(8);
  }
  if (olane() == 0) __hip_atomic_fetch_add((gu32*)(ws + cnt), 1u, RLX_AGENT);
}
template <int SLP>
__device__ __forceinline__ void hs_lead(__amdgpu_buffer_rsrc_t rs, bool fill, int cnt, unsigned want) {
  const int l16 = olane() * 16;
  if (fill) {
    for (int o = 0; o < (int)ARM_HOST; o += 1024) __builtin_amdgcn_raw_buffer_store_b128(v4u{~0u, ~0u, ~0u, ~0u}, rs, l16, o, AUX_SC1);
    VMCNT(0);
  }
  _Pragma("clang loop unroll(disable)") for (unsigned spins = 0; spins < (kSpinCap << 2); ++spins) {
    int va = l16;
    asm volatile("" : "+v"(va));
    const v4u a = __builtin_amdgcn_raw_buffer_load_b128(rs, va, OFF_REQ, AUX_SC1);
    const v4u b = __builtin_amdgcn_raw_buffer_load_b128(rs, va + 1024, OFF_REQ, AUX_SC1);
    const unsigned n = __builtin_amdgcn_raw_buffer_load_b32(rs, va & 0, cnt, AUX_SC1);
    if (__all(n + 1u >= want)) break;
    __builtin_amdgcn_raw_buffer_store_b128(a, rs, va, OFF_ACK, AUX_SC1);
    __builtin_amdgcn_raw_buffer_store_b128(b, rs, va + 1024, OFF_ACK, AUX_SC1);
    __builtin_amdgcn_s_sleep(SLP);
  }
}

#define TN(k, d) (d)
#define EVDECL
#define EV(k)
#define EVDUMP(s)
#define EVCLK(s, c)
#define EVOUT
#define PX 2
#define NOB 3
#define VO_A \
  "s_branch .Lgo_%=\n" \
  AL(A_VO_A) \
  ".Lr_%=:\n" \
  "s_sub_u32 %[rt], %[rt], 1\n" \
  "s_cbranch_scc1 .Lrb_%=\n" \
  "s_sleep 2\n" \
  "buffer_load_dwordx4 v[192:195], v248, %[rs], %[o0] offen sc1\n" \
  "buffer_load_dwordx4 v[196:199], v248, %[rs], %[o0] offen offset:1024 sc1\n" \
  "v_mov_b64 v[224:225], 0\n" \
  "v_mov_b64 v[226:227], 0\n" \
  "v_mov_b64 v[228:229], 0\n" \
  "v_mov_b64 v[230:231], 0\n" \
  "s_nop 1\n" \
  "s_branch .Lw_%=\n" \
  ".Lgo_%=:\n" \
  "v_add_u32 v252, 0x20000, v248\n" \
  "ds_read_b128 v[200:203], v248 offset:32768\n" \
  "ds_read_b128 v[204:207], v248 offset:33792\n" \
  "ds_read_b128 v[208:211], v248 offset:49152\n" \
  "ds_read_b128 v[212:215], v248 offset:50176\n" \
  "ds_read_b128 v[216:219], v252 offset:0\n" \
  "ds_read_b128 v[220:223], v252 offset:1024\n" \
  "ds_read_b128 v[240:243], v252 offset:16384\n" \
  "ds_read_b128 v[244:247], v252 offset:17408\n" \
  "v_mov_b64 v[224:225], 0\n" \
  "v_mov_b64 v[226:227], 0\n" \
  "v_mov_b64 v[228:229], 0\n" \
  "v_mov_b64 v[230:231], 0\n" \
  "v_xor_b32 v250, 0x800, v248\n" \
  "v_xor_b32 v251, 0x1000, v248\n" \
  "s_mov_b32 %[rt], 0x40000\n" \
  "s_setprio 2\n" \
  "s_waitcnt lgkmcnt(0)\n" \
  "s_barrier\n" \
  "buffer_load_dwordx4 v[192:195], v248, %[rs], %[o0] offen sc1\n" \
  "buffer_load_dwordx4 v[196:199], v248, %[rs], %[o0] offen offset:1024 sc1\n" \
  ".Lw_%=:\n" \
  "s_waitcnt vmcnt(1)\n" \
  "ds_write_b128 v248, v[192:195]\n" \
  "v_smfmac_f32_16x16x64_bf16 v[224:227], v[192:195], v[200:207], v249\n" \
  "v_max3_u32 v252, v192, v193, v194\n" \
  "s_waitcnt vmcnt(0)\n" \
  "ds_write_b128 v248, v[196:199] offset:1024\n" \
  "v_smfmac_f32_16x16x64_bf16 v[228:231], v[192:195], v[208:215], v249\n" \
  "v_max3_u32 v252, v252, v195, v196\n" \
  "v_smfmac_f32_16x16x64_bf16 v[224:227], v[196:199], v[216:223], v249\n" \
  "v_max3_u32 v252, v252, v197, v198\n" \
  "v_max_u32 v252, v252, v199\n" \
  "v_smfmac_f32_16x16x64_bf16 v[228:231], v[196:199], v[240:247], v249\n" \
  "v_cmp_eq_u32 vcc, -1, v252\n" \
  "v_xor_b32 v252, 0x1800, v248\n" \
  "s_cbranch_vccnz .Lr_%=\n" \
  ".Lrb_%=:\n" \
  "s_waitcnt lgkmcnt(0)\n" \
  "s_barrier\n" \
  "ds_read_b128 v[200:203], v250 offset:0\n" \
  "ds_read_b128 v[204:207], v250 offset:1024\n" \
  "ds_read_b128 v[208:211], v251 offset:0\n" \
  "ds_read_b128 v[212:215], v251 offset:1024\n" \
  "ds_read_b128 v[216:219], v252 offset:0\n" \
  "ds_read_b128 v[220:223], v252 offset:1024\n" \
  "s_waitcnt lgkmcnt(5)\n" \
  "v_smfmac_f32_16x16x64_bf16 v[224:227], v[200:203], v[0:7], v249\n" \
  "v_smfmac_f32_16x16x64_bf16 v[228:231], v[200:203], v[48:55], v249\n" \
  "s_waitcnt lgkmcnt(4)\n" \
  "v_smfmac_f32_16x16x64_bf16 v[224:227], v[204:207], v[8:15], v249\n" \
  "v_smfmac_f32_16x16x64_bf16 v[228:231], v[204:207], v[56:63], v249\n" \
  "s_waitcnt lgkmcnt(3)\n" \
  "v_smfmac_f32_16x16x64_bf16 v[224:227], v[208:211], v[16:23], v249\n" \
  "v_smfmac_f32_16x16x64_bf16 v[228:231], v[208:211], v[64:71], v249\n" \
  "s_waitcnt lgkmcnt(2)\n" \
  "v_smfmac_f32_16x16x64_bf16 v[224:227], v[212:215], v[24:31], v249\n" \
  "s_waitcnt lgkmcnt(1)\n" \
  "v_smfmac_f32_16x16x64_bf16 v[224:227], v[216:219], v[32:39], v249\n" \
  "s_waitcnt lgkmcnt(0)\n" \
  "v_smfmac_f32_16x16x64_bf16 v[224:227], v[220:223], v[40:47], v249\n" \
  "v_smfmac_f32_16x16x64_bf16 v[228:231], v[212:215], v[72:79], v249\n" \
  "v_smfmac_f32_16x16x64_bf16 v[228:231], v[216:219], v[80:87], v249\n" \
  "s_nop 0\n" \
  "v_add_f32 v224, v224, v226\n" \
  "v_smfmac_f32_16x16x64_bf16 v[228:231], v[220:223], v[88:95], v249\n" \
  "v_add_f32 v225, v225, v227\n" \
  "ds_write_b64 v248, v[224:225] offset:16384\n" \
  "s_nop 6\n" \
  "v_add_f32 v228, v228, v230\n" \
  "v_add_f32 v229, v229, v231\n" \
  "ds_write_b64 v248, v[228:229] offset:16392\n" \
  "s_setprio 0\n" \
  "ds_read_b128 v[240:243], v254 offset:0\n" \
  "ds_read_b128 v[244:247], v254 offset:1024\n" \
  "s_waitcnt lgkmcnt(2)\n" \
  "s_barrier\n" \
  "s_nop 1\n" \
  "ds_read_b128 v[224:227], v254 offset:16384\n" \
  "ds_read_b128 v[228:231], v254 offset:17408\n" \
  "v_mov_b64 v[192:193], 0\n" \
  "v_mov_b64 v[194:195], 0\n" \
  "v_mov_b64 v[196:197], 0\n" \
  "v_mov_b64 v[198:199], 0\n" \
  "v_mov_b64 v[200:201], 0\n" \
  "v_mov_b64 v[202:203], 0\n" \
  "v_mov_b64 v[204:205], 0\n" \
  "v_mov_b64 v[206:207], 0\n" \
  "s_setprio 2\n" \
  "s_barrier\n" \
  "ds_read_b64_tr_b16 v[208:209], v238 offset:16384\n" \
  "ds_read_b64_tr_b16 v[210:211], v238 offset:16576\n" \
  "ds_read_b64_tr_b16 v[212:213], v238 offset:17152\n" \
  "ds_read_b64_tr_b16 v[214:215], v238 offset:17344\n" \
  "ds_read_b64_tr_b16 v[216:217], v238 offset:17920\n" \
  "ds_read_b64_tr_b16 v[218:219], v238 offset:18112\n" \
  "ds_read_b64_tr_b16 v[220:221], v238 offset:18688\n" \
  "ds_read_b64_tr_b16 v[222:223], v238 offset:18880\n" \
  "s_waitcnt lgkmcnt(6)\n" \
  "v_smfmac_f32_16x16x64_bf16 v[200:203], v[208:211], v[240:247], v249 abid:1\n" \
  "ds_read_b128 v[240:243], v254 offset:32768\n" \
  "v_smfmac_f32_16x16x64_bf16 v[192:195], v[208:211], v[160:167], v249 abid:1\n" \
  "ds_read_b128 v[244:247], v254 offset:33792\n" \
  "v_smfmac_f32_16x16x64_bf16 v[196:199], v[208:211], v[96:103], v249 abid:1\n" \
  "v_smfmac_f32_16x16x64_bf16 v[204:207], v[208:211], v[128:135], v249 abid:1\n" \
  "s_waitcnt lgkmcnt(6)\n" \
  "v_smfmac_f32_16x16x64_bf16 v[192:195], v[212:215], v[168:175], v249 abid:1\n" \
  "v_smfmac_f32_16x16x64_bf16 v[200:203], v[212:215], v[224:231], v249 abid:1\n" \
  "ds_read_b128 v[224:227], v254 offset:49152\n" \
  "v_smfmac_f32_16x16x64_bf16 v[196:199], v[212:215], v[104:111], v249 abid:1\n" \
  "ds_read_b128 v[228:231], v254 offset:50176\n" \
  "v_smfmac_f32_16x16x64_bf16 v[204:207], v[212:215], v[136:143], v249 abid:1\n" \
  "s_waitcnt lgkmcnt(6)\n" \
  "v_smfmac_f32_16x16x64_bf16 v[192:195], v[216:219], v[176:183], v249 abid:1\n" \
  "v_smfmac_f32_16x16x64_bf16 v[196:199], v[216:219], v[112:119], v249 abid:1\n" \
  "s_waitcnt lgkmcnt(4)\n" \
  "v_smfmac_f32_16x16x64_bf16 v[192:195], v[220:223], v[184:191], v249 abid:1\n" \
  "v_smfmac_f32_16x16x64_bf16 v[196:199], v[220:223], v[120:127], v249 abid:1\n" \
  "v_smfmac_f32_16x16x64_bf16 v[204:207], v[216:219], v[144:151], v249 abid:1\n" \
  "s_nop 0\n" \
  "v_cvt_pk_bf16_f32 v192, v192, v193\n" \
  "v_smfmac_f32_16x16x64_bf16 v[204:207], v[220:223], v[152:159], v249 abid:1\n" \
  "v_cvt_pk_bf16_f32 v193, v194, v195\n" \
  "s_waitcnt lgkmcnt(2)\n" \
  "v_smfmac_f32_16x16x64_bf16 v[200:203], v[216:219], v[240:247], v249 abid:1\n" \
  "v_cvt_pk_bf16_f32 v194, v196, v197\n" \
  "s_waitcnt lgkmcnt(0)\n" \
  "v_smfmac_f32_16x16x64_bf16 v[200:203], v[220:223], v[224:231], v249 abid:1\n" \
  "v_cvt_pk_bf16_f32 v195, v198, v199\n" \
  "v_cvt_pk_bf16_f32 v204, v204, v205\n" \
  "v_permlane16_swap_b32 v192, v194\n" \
  "v_permlane16_swap_b32 v193, v195\n" \
  "buffer_store_dwordx4 v[192:195], v236, %[rs], %[vs] offen sc1\n" \
  "v_cvt_pk_bf16_f32 v205, v206, v207\n" \
  "s_nop 2\n" \
  "v_cvt_pk_bf16_f32 v203, v202, v203\n" \
  "v_cvt_pk_bf16_f32 v202, v200, v201\n" \
  "s_nop 0\n" \
  "v_permlane16_swap_b32 v203, v205\n" \
  "v_permlane16_swap_b32 v202, v204\n" \
  "buffer_store_dwordx4 v[202:205], v237, %[rs], %[vs] offen sc1\n" \
  "s_setprio 0\n" \
  "s_nop 1\n"
#define VO_B \
  "s_branch .Lgo_%=\n" \
  AL(A_VO_B) \
  ".Lr_%=:\n" \
  "s_sub_u32 %[rt], %[rt], 1\n" \
  "s_cbranch_scc1 .Lrb_%=\n" \
  "s_sleep 2\n" \
  "buffer_load_dwordx4 v[192:195], v248, %[rs], %[o0] offen sc1\n" \
  "buffer_load_dwordx4 v[196:199], v248, %[rs], %[o0] offen offset:1024 sc1\n" \
  "v_mov_b64 v[224:225], 0\n" \
  "v_mov_b64 v[226:227], 0\n" \
  "v_mov_b64 v[228:229], 0\n" \
  "v_mov_b64 v[230:231], 0\n" \
  "s_nop 1\n" \
  "s_branch .Lw_%=\n" \
  ".Lgo_%=:\n" \
  "v_add_u32 v252, 0x20000, v248\n" \
  "ds_read_b128 v[200:203], v248 offset:32768\n" \
  "ds_read_b128 v[204:207], v248 offset:33792\n" \
  "ds_read_b128 v[208:211], v248 offset:49152\n" \
  "ds_read_b128 v[212:215], v248 offset:50176\n" \
  "ds_read_b128 v[216:219], v252 offset:0\n" \
  "ds_read_b128 v[220:223], v252 offset:1024\n" \
  "ds_read_b128 v[240:243], v252 offset:16384\n" \
  "ds_read_b128 v[244:247], v252 offset:17408\n" \
  "v_mov_b64 v[224:225], 0\n" \
  "v_mov_b64 v[226:227], 0\n" \
  "v_mov_b64 v[228:229], 0\n" \
  "v_mov_b64 v[230:231], 0\n" \
  "v_xor_b32 v250, 0x800, v248\n" \
  "v_xor_b32 v251, 0x1000, v248\n" \
  "s_mov_b32 %[rt], 0x40000\n" \
  "s_setprio 1\n" \
  "s_waitcnt lgkmcnt(0)\n" \
  "s_cmp_eq_u32 %[tw], 0\n" \
  "s_cbranch_scc1 .Ltq_%=\n" \
  ".Ltl_%=:\n" \
  "s_memtime vcc\n" \
  "s_waitcnt lgkmcnt(0)\n" \
  "s_sub_i32 %[cnt], %[tg], vcc_lo\n" \
  "s_cmp_lt_i32 %[cnt], 260\n" \
  "s_cbranch_scc1 .Ltf_%=\n" \
  "s_sleep 1\n" \
  "s_branch .Ltl_%=\n" \
  ".Ltf_%=:\n" \
  "s_max_i32 %[cnt], %[cnt], 0\n" \
  ".Ltg_%=:\n" \
  "s_sub_u32 %[cnt], %[cnt], 28\n" \
  "s_cbranch_scc0 .Ltg_%=\n" \
  ".Ltq_%=:\n" \
  "s_barrier\n" \
  "buffer_load_dwordx4 v[192:195], v248, %[rs], %[o0] offen sc1\n" \
  "buffer_load_dwordx4 v[196:199], v248, %[rs], %[o0] offen offset:1024 sc1\n" \
  ".Lw_%=:\n" \
  "s_waitcnt vmcnt(1)\n" \
  "ds_write_b128 v248, v[192:195]\n" \
  "v_smfmac_f32_16x16x64_bf16 v[224:227], v[192:195], v[200:207], v249\n" \
  "v_max3_u32 v252, v192, v193, v194\n" \
  "s_waitcnt vmcnt(0)\n" \
  "ds_write_b128 v248, v[196:199] offset:1024\n" \
  "v_smfmac_f32_16x16x64_bf16 v[228:231], v[192:195], v[208:215], v249\n" \
  "v_max3_u32 v252, v252, v195, v196\n" \
  "v_smfmac_f32_16x16x64_bf16 v[224:227], v[196:199], v[216:223], v249\n" \
  "v_max3_u32 v252, v252, v197, v198\n" \
  "v_max_u32 v252, v252, v199\n" \
  "v_smfmac_f32_16x16x64_bf16 v[228:231], v[196:199], v[240:247], v249\n" \
  "v_cmp_eq_u32 vcc, -1, v252\n" \
  "v_xor_b32 v252, 0x1800, v248\n" \
  "s_cbranch_vccnz .Lr_%=\n" \
  ".Lrb_%=:\n" \
  "s_waitcnt lgkmcnt(0)\n" \
  "s_barrier\n" \
  "s_memtime vcc\n" \
  "s_setprio 3\n" \
  "v_mov_b64 v[192:193], 0\n" \
  "v_mov_b64 v[194:195], 0\n" \
  "v_mov_b64 v[196:197], 0\n" \
  "v_mov_b64 v[198:199], 0\n" \
  "s_waitcnt lgkmcnt(0)\n" \
  "s_sub_u32 %[cnt], vcc_lo, %[ckm]\n" \
  "s_mul_hi_u32 %[cnt], %[cnt], %[inv]\n" \
  "s_add_u32 %[cnt], %[cnt], %[r16]\n" \
  "s_and_b32 %[cnt], %[cnt], -2\n" \
  "v_mov_b32 v233, %[cnt]\n" \
  "buffer_store_dword v233, v239, %[rs], %[sst] offen sc1\n" \
  "s_nop 5\n" \
  "ds_read_b128 v[200:203], v250 offset:0\n" \
  "ds_read_b128 v[204:207], v250 offset:1024\n" \
  "ds_read_b128 v[208:211], v251 offset:0\n" \
  "ds_read_b128 v[212:215], v251 offset:1024\n" \
  "ds_read_b128 v[216:219], v252 offset:0\n" \
  "ds_read_b128 v[220:223], v252 offset:1024\n" \
  "s_setprio 1\n" \
  "s_waitcnt lgkmcnt(5)\n" \
  "v_smfmac_f32_16x16x64_bf16 v[224:227], v[200:203], v[0:7], v249\n" \
  "v_smfmac_f32_16x16x64_bf16 v[228:231], v[200:203], v[48:55], v249\n" \
  "s_waitcnt lgkmcnt(4)\n" \
  "v_smfmac_f32_16x16x64_bf16 v[224:227], v[204:207], v[8:15], v249\n" \
  "v_smfmac_f32_16x16x64_bf16 v[228:231], v[204:207], v[56:63], v249\n" \
  "s_waitcnt lgkmcnt(3)\n" \
  "v_smfmac_f32_16x16x64_bf16 v[224:227], v[208:211], v[16:23], v249\n" \
  "ds_read_b128 v[240:243], v254 offset:0\n" \
  "v_smfmac_f32_16x16x64_bf16 v[228:231], v[208:211], v[64:71], v249\n" \
  "ds_read_b128 v[244:247], v254 offset:1024\n" \
  "s_waitcnt lgkmcnt(4)\n" \
  "v_smfmac_f32_16x16x64_bf16 v[224:227], v[212:215], v[24:31], v249\n" \
  "s_waitcnt lgkmcnt(3)\n" \
  "v_smfmac_f32_16x16x64_bf16 v[224:227], v[216:219], v[32:39], v249\n" \
  "s_waitcnt lgkmcnt(2)\n" \
  "v_smfmac_f32_16x16x64_bf16 v[224:227], v[220:223], v[40:47], v249\n" \
  "v_smfmac_f32_16x16x64_bf16 v[228:231], v[212:215], v[72:79], v249\n" \
  "s_barrier\n" \
  "ds_read_b128 v[232:235], v248 offset:24576\n" \
  "v_smfmac_f32_16x16x64_bf16 v[228:231], v[216:219], v[80:87], v249\n" \
  "s_nop 0\n" \
  "v_add_f32 v224, v224, v226\n" \
  "v_smfmac_f32_16x16x64_bf16 v[228:231], v[220:223], v[88:95], v249\n" \
  "v_add_f32 v225, v225, v227\n" \
  "s_waitcnt lgkmcnt(0)\n" \
  "v_add_f32 v232, v232, v224\n" \
  "v_add_f32 v233, v233, v225\n" \
  "v_cvt_pk_bf16_f32 v232, v232, v233\n" \
  "ds_write_b32 v253, v232\n" \
  "s_nop 2\n" \
  "v_add_f32 v234, v234, v228\n" \
  "v_add_f32 v235, v235, v229\n" \
  "v_add_f32 v234, v234, v230\n" \
  "v_add_f32 v235, v235, v231\n" \
  "v_cvt_pk_bf16_f32 v234, v234, v235\n" \
  "ds_write_b32 v253, v234 offset:1536\n" \
  "v_mov_b64 v[200:201], 0\n" \
  "v_mov_b64 v[202:203], 0\n" \
  "v_mov_b64 v[204:205], 0\n" \
  "v_mov_b64 v[206:207], 0\n" \
  "s_waitcnt lgkmcnt(0)\n" \
  "s_barrier\n" \
  "s_nop 7\n" \
  "ds_read_b64_tr_b16 v[208:209], v238 offset:16384\n" \
  "ds_read_b64_tr_b16 v[210:211], v238 offset:16576\n" \
  "ds_read_b128 v[224:227], v254 offset:16384\n" \
  "ds_read_b128 v[228:231], v254 offset:17408\n" \
  "ds_read_b64_tr_b16 v[212:213], v238 offset:17152\n" \
  "ds_read_b64_tr_b16 v[214:215], v238 offset:17344\n" \
  "ds_read_b64_tr_b16 v[216:217], v238 offset:17920\n" \
  "ds_read_b64_tr_b16 v[218:219], v238 offset:18112\n" \
  "ds_read_b64_tr_b16 v[220:221], v238 offset:18688\n" \
  "ds_read_b64_tr_b16 v[222:223], v238 offset:18880\n" \
  "s_waitcnt lgkmcnt(8)\n" \
  "v_smfmac_f32_16x16x64_bf16 v[200:203], v[208:211], v[240:247], v249 abid:1\n" \
  "ds_read_b128 v[240:243], v254 offset:32768\n" \
  "v_smfmac_f32_16x16x64_bf16 v[192:195], v[208:211], v[160:167], v249 abid:1\n" \
  "ds_read_b128 v[244:247], v254 offset:33792\n" \
  "v_smfmac_f32_16x16x64_bf16 v[196:199], v[208:211], v[96:103], v249 abid:1\n" \
  "v_smfmac_f32_16x16x64_bf16 v[204:207], v[208:211], v[128:135], v249 abid:1\n" \
  "s_waitcnt lgkmcnt(6)\n" \
  "v_smfmac_f32_16x16x64_bf16 v[192:195], v[212:215], v[168:175], v249 abid:1\n" \
  "v_smfmac_f32_16x16x64_bf16 v[200:203], v[212:215], v[224:231], v249 abid:1\n" \
  "ds_read_b128 v[224:227], v254 offset:49152\n" \
  "v_smfmac_f32_16x16x64_bf16 v[196:199], v[212:215], v[104:111], v249 abid:1\n" \
  "ds_read_b128 v[228:231], v254 offset:50176\n" \
  "v_smfmac_f32_16x16x64_bf16 v[204:207], v[212:215], v[136:143], v249 abid:1\n" \
  "s_waitcnt lgkmcnt(6)\n" \
  "v_smfmac_f32_16x16x64_bf16 v[192:195], v[216:219], v[176:183], v249 abid:1\n" \
  "v_smfmac_f32_16x16x64_bf16 v[196:199], v[216:219], v[112:119], v249 abid:1\n" \
  "s_waitcnt lgkmcnt(4)\n" \
  "v_smfmac_f32_16x16x64_bf16 v[192:195], v[220:223], v[184:191], v249 abid:1\n" \
  "v_smfmac_f32_16x16x64_bf16 v[196:199], v[220:223], v[120:127], v249 abid:1\n" \
  "v_smfmac_f32_16x16x64_bf16 v[204:207], v[216:219], v[144:151], v249 abid:1\n" \
  "s_nop 0\n" \
  "v_cvt_pk_bf16_f32 v192, v192, v193\n" \
  "v_smfmac_f32_16x16x64_bf16 v[204:207], v[220:223], v[152:159], v249 abid:1\n" \
  "v_cvt_pk_bf16_f32 v193, v194, v195\n" \
  "s_waitcnt lgkmcnt(2)\n" \
  "v_smfmac_f32_16x16x64_bf16 v[200:203], v[216:219], v[240:247], v249 abid:1\n" \
  "v_cvt_pk_bf16_f32 v194, v196, v197\n" \
  "s_waitcnt lgkmcnt(0)\n" \
  "v_smfmac_f32_16x16x64_bf16 v[200:203], v[220:223], v[224:231], v249 abid:1\n" \
  "v_cvt_pk_bf16_f32 v195, v198, v199\n" \
  "v_cvt_pk_bf16_f32 v204, v204, v205\n" \
  "v_permlane16_swap_b32 v192, v194\n" \
  "v_permlane16_swap_b32 v193, v195\n" \
  "buffer_store_dwordx4 v[192:195], v236, %[rs], %[vs] offen sc1\n" \
  "v_cvt_pk_bf16_f32 v205, v206, v207\n" \
  "s_nop 2\n" \
  "v_cvt_pk_bf16_f32 v203, v202, v203\n" \
  "v_cvt_pk_bf16_f32 v202, v200, v201\n" \
  "s_nop 0\n" \
  "v_permlane16_swap_b32 v203, v205\n" \
  "v_permlane16_swap_b32 v202, v204\n" \
  "buffer_store_dwordx4 v[202:205], v237, %[rs], %[vs] offen sc1\n" \
  "s_setprio 0\n" \
  "s_nop 1\n"
#define QK_A0 \
  "s_branch .Lgo_%=\n" \
  AL(A_QK_A0) \
  ".Lr_%=:\n" \
  "s_sub_u32 %[rt], %[rt], 1\n" \
  "s_cbranch_scc1 .Lrb_%=\n" \
  "s_sleep 2\n" \
  "buffer_load_dwordx4 v[192:195], v248, %[rs], %[o0] offen sc1\n" \
  "buffer_load_dwordx4 v[196:199], v248, %[rs], %[o0] offen offset:1024 sc1\n" \
  "v_mov_b64 v[224:225], 0\n" \
  "v_mov_b64 v[226:227], 0\n" \
  "v_mov_b64 v[228:229], 0\n" \
  "v_mov_b64 v[230:231], 0\n" \
  "v_mov_b64 v[232:233], 0\n" \
  "v_mov_b64 v[234:235], 0\n" \
  "v_mov_b64 v[236:237], 0\n" \
  "v_mov_b64 v[238:239], 0\n" \
  "s_nop 1\n" \
  "s_branch .Lw_%=\n" \
  ".Lw1_%=:\n" \
  "v_max_f32 v200, v192, v193\n" \
  "s_nop 1\n" \
  "v_max_f32_dpp v200, v200, v200 row_ror:8 row_mask:0xf bank_mask:0xf\n" \
  "s_nop 1\n" \
  "v_max_f32_dpp v200, v200, v200 row_ror:4 row_mask:0xf bank_mask:0xf\n" \
  "s_nop 1\n" \
  "v_max_f32_dpp v200, v200, v200 row_ror:2 row_mask:0xf bank_mask:0xf\n" \
  "s_nop 1\n" \
  "v_max_f32_dpp v200, v200, v200 row_ror:1 row_mask:0xf bank_mask:0xf\n" \
  "v_sub_f32 v194, v192, v200\n" \
  "v_sub_f32 v195, v193, v200\n" \
  "v_exp_f32 v194, v194\n" \
  "v_exp_f32 v195, v195\n" \
  "s_mov_b32 %[rt], -1.0\n" \
  "s_nop 0\n" \
  "v_cvt_pk_bf16_f32 v196, v194, v195\n" \
  "v_and_b32 v196, 0x7fffffff, v196\n" \
  "s_branch .Lw1b_%=\n" \
  ".Lw2_%=:\n" \
  "s_cmp_lt_i32 %[rt], 0\n" \
  "s_cbranch_scc1 .Lw3_%=\n" \
  "v_mov_b32 v200, 0\n" \
  ".Lw3_%=:\n" \
  "s_nop 1\n" \
  "v_max_f32_dpp v197, v192, v200 quad_perm:[0,0,0,0] row_mask:0xf bank_mask:0xf\n" \
  "s_nop 1\n" \
  "v_max_f32_dpp v197, v192, v197 quad_perm:[1,1,1,1] row_mask:0xf bank_mask:0xf\n" \
  "s_nop 1\n" \
  "v_max_f32_dpp v197, v197, v197 row_ror:4 row_mask:0xf bank_mask:0xf\n" \
  "s_nop 1\n" \
  "v_max_f32_dpp v197, v197, v197 row_ror:8 row_mask:0xf bank_mask:0xf\n" \
  "v_sub_f32 v194, v192, v197\n" \
  "v_sub_f32 v201, v200, v197\n" \
  "v_exp_f32 v194, v194\n" \
  "v_exp_f32 v201, v201\n" \
  "s_nop 1\n" \
  "v_mul_f32 v199, v201, v199\n" \
  "s_nop 1\n" \
  "v_add_f32_dpp v198, v194, v199 quad_perm:[0,0,0,0] row_mask:0xf bank_mask:0xf\n" \
  "s_nop 1\n" \
  "v_mov_b32_dpp v193, v194 quad_perm:[1,1,1,1] row_mask:0xf bank_mask:0xf\n" \
  "s_nop 1\n" \
  "v_add_f32 v198, v198, v193\n" \
  "s_nop 1\n" \
  "v_mov_b32_dpp v195, v194 quad_perm:[0,0,0,0] row_mask:0xf bank_mask:0xf\n" \
  "s_nop 1\n" \
  "v_add_f32_dpp v198, v198, v198 row_ror:4 row_mask:0xf bank_mask:0xf\n" \
  "s_nop 1\n" \
  "v_add_f32_dpp v198, v198, v198 row_ror:8 row_mask:0xf bank_mask:0xf\n" \
  "s_nop 1\n" \
  "v_rcp_f32 v198, v198\n" \
  "s_nop 1\n" \
  "v_mul_f32 v195, v195, v198\n" \
  "s_nop 1\n" \
  "v_mul_f32 v193, v193, v198\n" \
  "s_nop 1\n" \
  "v_cvt_pk_bf16_f32 v196, v195, v193\n" \
  "s_nop 1\n" \
  "v_and_b32 v196, 0x7fffffff, v196\n" \
  "s_nop 1\n" \
  "v_mul_f32 v198, v201, v198\n" \
  "s_nop 1\n" \
  "buffer_store_dword v196, v251, %[rs], %[so] offen sc1\n" \
  "s_nop 1\n" \
  "v_cvt_pk_bf16_f32 v197, v198, v198\n" \
  "s_nop 1\n" \
  "v_and_b32 v197, v252, v197\n" \
  "s_nop 1\n" \
  "buffer_store_dword v197, v251, %[rs], %[so] offen offset:64 sc1\n" \
  "s_branch .Lw2b_%=\n" \
  ".Lgo_%=:\n" \
  "v_add_u32 v252, 0x20000, v248\n" \
  "ds_read_b128 v[200:203], v248 offset:32768\n" \
  "ds_read_b128 v[204:207], v248 offset:33792\n" \
  "ds_read_b128 v[208:211], v248 offset:49152\n" \
  "ds_read_b128 v[212:215], v248 offset:50176\n" \
  "ds_read_b128 v[216:219], v252 offset:0\n" \
  "ds_read_b128 v[220:223], v252 offset:1024\n" \
  "ds_read_b128 v[240:243], v252 offset:16384\n" \
  "ds_read_b128 v[244:247], v252 offset:17408\n" \
  "v_mov_b64 v[224:225], 0\n" \
  "v_mov_b64 v[226:227], 0\n" \
  "v_mov_b64 v[228:229], 0\n" \
  "v_mov_b64 v[230:231], 0\n" \
  "v_mov_b64 v[232:233], 0\n" \
  "v_mov_b64 v[234:235], 0\n" \
  "v_mov_b64 v[236:237], 0\n" \
  "v_mov_b64 v[238:239], 0\n" \
  "v_xor_b32 v250, 0x800, v248\n" \
  "v_xor_b32 v251, 0x1000, v248\n" \
  "s_mov_b32 %[rt], 0x40000\n" \
  "s_setprio 2\n" \
  "s_waitcnt lgkmcnt(0)\n" \
  "s_barrier\n" \
  "buffer_load_dwordx4 v[192:195], v248, %[rs], %[o0] offen sc1\n" \
  "buffer_load_dwordx4 v[196:199], v248, %[rs], %[o0] offen offset:1024 sc1\n" \
  ".Lw_%=:\n" \
  "s_waitcnt vmcnt(1)\n" \
  "ds_write_b128 v248, v[192:195]\n" \
  "v_smfmac_f32_16x16x64_bf16 v[224:227], v[192:195], v[200:207], v249\n" \
  "v_max3_u32 v252, v192, v193, v194\n" \
  "s_waitcnt vmcnt(0)\n" \
  "ds_write_b128 v248, v[196:199] offset:1024\n" \
  "v_smfmac_f32_16x16x64_bf16 v[228:231], v[192:195], v[208:215], v249\n" \
  "v_max3_u32 v252, v252, v195, v196\n" \
  "v_smfmac_f32_16x16x64_bf16 v[224:227], v[196:199], v[216:223], v249\n" \
  "v_max3_u32 v252, v252, v197, v198\n" \
  "v_smfmac_f32_16x16x64_bf16 v[228:231], v[196:199], v[240:247], v249\n" \
  "v_max_u32 v252, v252, v199\n" \
  "v_smfmac_f32_16x16x64_bf16 v[232:235], v[192:195], v[96:103], v249\n" \
  "v_cmp_eq_u32 vcc, -1, v252\n" \
  "v_smfmac_f32_16x16x64_bf16 v[236:239], v[192:195], v[144:151], v249\n" \
  "v_xor_b32 v252, 0x1800, v248\n" \
  "s_cbranch_vccnz .Lr_%=\n" \
  ".Lrb_%=:\n" \
  "v_smfmac_f32_16x16x64_bf16 v[232:235], v[196:199], v[104:111], v249\n" \
  "v_smfmac_f32_16x16x64_bf16 v[236:239], v[196:199], v[152:159], v249\n" \
  "s_waitcnt lgkmcnt(0)\n" \
  "s_barrier\n" \
  "ds_read_b128 v[200:203], v250 offset:0\n" \
  "ds_read_b128 v[204:207], v250 offset:1024\n" \
  "ds_read_b128 v[208:211], v251 offset:0\n" \
  "ds_read_b128 v[212:215], v251 offset:1024\n" \
  "ds_read_b128 v[216:219], v252 offset:0\n" \
  "ds_read_b128 v[220:223], v252 offset:1024\n" \
  "s_waitcnt lgkmcnt(5)\n" \
  "v_smfmac_f32_16x16x64_bf16 v[224:227], v[200:203], v[0:7], v249\n" \
  "v_smfmac_f32_16x16x64_bf16 v[228:231], v[200:203], v[48:55], v249\n" \
  "s_waitcnt lgkmcnt(4)\n" \
  "v_smfmac_f32_16x16x64_bf16 v[224:227], v[204:207], v[8:15], v249\n" \
  "v_smfmac_f32_16x16x64_bf16 v[228:231], v[204:207], v[56:63], v249\n" \
  "s_waitcnt lgkmcnt(3)\n" \
  "v_smfmac_f32_16x16x64_bf16 v[224:227], v[208:211], v[16:23], v249\n" \
  "v_smfmac_f32_16x16x64_bf16 v[228:231], v[208:211], v[64:71], v249\n" \
  "s_waitcnt lgkmcnt(2)\n" \
  "v_smfmac_f32_16x16x64_bf16 v[224:227], v[212:215], v[24:31], v249\n" \
  "s_waitcnt lgkmcnt(1)\n" \
  "v_smfmac_f32_16x16x64_bf16 v[224:227], v[216:219], v[32:39], v249\n" \
  "s_waitcnt lgkmcnt(0)\n" \
  "v_smfmac_f32_16x16x64_bf16 v[224:227], v[220:223], v[40:47], v249\n" \
  "v_smfmac_f32_16x16x64_bf16 v[228:231], v[212:215], v[72:79], v249\n" \
  "v_smfmac_f32_16x16x64_bf16 v[228:231], v[216:219], v[80:87], v249\n" \
  "s_nop 0\n" \
  "v_add_f32 v224, v224, v226\n" \
  "v_smfmac_f32_16x16x64_bf16 v[228:231], v[220:223], v[88:95], v249\n" \
  "v_add_f32 v225, v225, v227\n" \
  "ds_write_b64 v248, v[224:225] offset:8192\n" \
  "s_nop 6\n" \
  "v_add_f32 v228, v228, v230\n" \
  "v_add_f32 v229, v229, v231\n" \
  "ds_write_b64 v248, v[228:229] offset:8200\n" \
  "s_setprio 0\n" \
  "ds_read_b128 v[240:243], v254 offset:0\n" \
  "ds_read_b128 v[244:247], v254 offset:1024\n" \
  "s_waitcnt lgkmcnt(2)\n" \
  "s_barrier\n" \
  "s_nop 1\n" \
  "ds_read_b128 v[192:195], v254 offset:16384\n" \
  "ds_read_b128 v[196:199], v254 offset:17408\n" \
  "ds_read_b128 v[224:227], v254 offset:32768\n" \
  "ds_read_b128 v[228:231], v254 offset:33792\n" \
  "s_waitcnt lgkmcnt(4)\n" \
  "v_smfmac_f32_16x16x64_bf16 v[232:235], v[200:203], v[240:247], v249\n" \
  "ds_read_b128 v[240:243], v254 offset:49152\n" \
  "ds_read_b128 v[244:247], v254 offset:50176\n" \
  "s_waitcnt lgkmcnt(4)\n" \
  "v_smfmac_f32_16x16x64_bf16 v[236:239], v[200:203], v[192:199], v249\n" \
  "v_smfmac_f32_16x16x64_bf16 v[232:235], v[204:207], v[112:119], v249\n" \
  "ds_read_b128 v[192:195], v248 offset:16384\n" \
  "v_smfmac_f32_16x16x64_bf16 v[236:239], v[204:207], v[160:167], v249\n" \
  "ds_read_b128 v[196:199], v248 offset:17408\n" \
  "v_smfmac_f32_16x16x64_bf16 v[232:235], v[212:215], v[120:127], v249\n" \
  "ds_read_b128 v[200:203], v248 offset:20480\n" \
  "v_smfmac_f32_16x16x64_bf16 v[236:239], v[212:215], v[168:175], v249\n" \
  "ds_read_b128 v[204:207], v248 offset:21504\n" \
  "s_waitcnt lgkmcnt(6)\n" \
  "v_smfmac_f32_16x16x64_bf16 v[232:235], v[208:211], v[224:231], v249\n" \
  "ds_read_b128 v[212:215], v248 offset:23552\n" \
  "s_waitcnt lgkmcnt(5)\n" \
  "v_smfmac_f32_16x16x64_bf16 v[236:239], v[208:211], v[240:247], v249\n" \
  "ds_read_b128 v[240:243], v248 offset:18432\n" \
  "v_smfmac_f32_16x16x64_bf16 v[232:235], v[216:219], v[128:135], v249\n" \
  "ds_read_b128 v[244:247], v248 offset:19456\n" \
  "v_smfmac_f32_16x16x64_bf16 v[232:235], v[220:223], v[136:143], v249\n" \
  "ds_read_b128 v[208:211], v248 offset:22528\n" \
  "v_smfmac_f32_16x16x64_bf16 v[236:239], v[216:219], v[176:183], v249\n" \
  "ds_read_b96 v[250:252], v248 offset:13312\n" \
  "v_smfmac_f32_16x16x64_bf16 v[236:239], v[220:223], v[184:191], v249\n" \
  "s_setprio 3\n" \
  "s_barrier\n" \
  "ds_read_b64_tr_b16 v[224:225], v253 offset:17408\n" \
  "ds_read_b64_tr_b16 v[226:227], v253 offset:17664\n" \
  "ds_read_b64_tr_b16 v[228:229], v253 offset:19456\n" \
  "ds_read_b64_tr_b16 v[230:231], v253 offset:19712\n" \
  "v_mov_b64 v[216:217], 0\n" \
  "v_mov_b64 v[218:219], 0\n" \
  "v_add_f32 v232, v232, v234\n" \
  "v_add_f32 v233, v233, v235\n" \
  "ds_write_b64 v248, v[232:233] offset:0\n" \
  "v_mov_b64 v[220:221], 0\n" \
  "v_mov_b64 v[222:223], 0\n" \
  "v_add_f32 v236, v236, v238\n" \
  "v_add_f32 v237, v237, v239\n" \
  "ds_write_b64 v248, v[236:237] offset:8\n" \
  "s_waitcnt lgkmcnt(4)\n" \
  "v_smfmac_f32_16x16x64_bf16 v[216:219], v[224:227], v[192:199], v249\n" \
  "v_smfmac_f32_16x16x64_bf16 v[220:223], v[224:227], v[200:207], v249\n" \
  "s_waitcnt lgkmcnt(2)\n" \
  "v_smfmac_f32_16x16x64_bf16 v[216:219], v[228:231], v[240:247], v249\n" \
  "v_smfmac_f32_16x16x64_bf16 v[220:223], v[228:231], v[208:215], v249\n" \
  "s_mov_b32 %[rt], 0x422c0000\n" \
  "s_waitcnt lgkmcnt(0)\n" \
  "s_barrier\n" \
  "s_nop 1\n" \
  "v_add_f32 v192, v216, v218\n" \
  "s_nop 2\n" \
  "v_add_f32 v193, v220, v222\n" \
  "v_exp_f32 v194, v192\n" \
  "v_exp_f32 v195, v193\n" \
  "v_max_f32 v197, |v192|, |v193|\n" \
  "v_cvt_pk_bf16_f32 v196, v194, v195\n" \
  "v_cmp_nge_f32 vcc, %[rt], v197\n" \
  "s_cbranch_vccnz .Lw1_%=\n" \
  ".Lw1b_%=:\n" \
  "buffer_store_dword v196, v250, %[rs], %[c0] offen sc1\n" \
  "buffer_store_dword v196, v250, %[rs], %[c1] offen sc1\n" \
  "buffer_store_dword v196, v250, %[rs], %[c2] offen sc1\n" \
  "buffer_store_dword v196, v250, %[rs], %[c3] offen sc1\n" \
  "v_add_f32 v199, v194, v195\n" \
  "v_xor_b32 v251, %[xr], v251\n" \
  "s_nop 0\n" \
  "v_add_f32_dpp v199, v199, v199 quad_perm:[1,0,3,2] row_mask:0xf bank_mask:0xf\n" \
  "s_nop 1\n" \
  "v_add_f32_dpp v199, v199, v199 quad_perm:[2,3,0,1] row_mask:0xf bank_mask:0xf\n" \
  "s_barrier\n" \
  "ds_read_b64_tr_b16 v[232:233], v253 offset:9216\n" \
  "ds_read_b64_tr_b16 v[234:235], v253 offset:9472\n" \
  "ds_read_b64_tr_b16 v[236:237], v253 offset:11264\n" \
  "ds_read_b64_tr_b16 v[238:239], v253 offset:11520\n" \
  "s_waitcnt lgkmcnt(2)\n" \
  "v_mfma_f32_16x16x32_bf16 v[216:219], v[224:227], v[232:235], 0\n" \
  "s_waitcnt lgkmcnt(0)\n" \
  "v_mfma_f32_16x16x32_bf16 v[216:219], v[228:231], v[236:239], v[216:219]\n" \
  "s_nop 8\n" \
  "v_add_f32_dpp v192, v218, v216 quad_perm:[2,3,2,3] row_mask:0xf bank_mask:0xf\n" \
  "v_exp_f32 v194, v192\n" \
  "v_cmp_nge_f32 vcc, %[rt], |v192|\n" \
  "s_and_b64 vcc, vcc, %[lo]\n" \
  "v_add_f32_dpp v198, v194, v199 quad_perm:[0,0,0,0] row_mask:0xf bank_mask:0xf\n" \
  "v_mov_b32_dpp v193, v194 quad_perm:[1,1,1,1] row_mask:0xf bank_mask:0xf\n" \
  "v_add_f32 v198, v198, v193\n" \
  "v_mov_b32_dpp v195, v194 quad_perm:[0,0,0,0] row_mask:0xf bank_mask:0xf\n" \
  "s_cbranch_scc1 .Lw2_%=\n" \
  "v_add_f32_dpp v198, v198, v198 row_ror:4 row_mask:0xf bank_mask:0xf\n" \
  "s_nop 1\n" \
  "v_add_f32_dpp v198, v198, v198 row_ror:8 row_mask:0xf bank_mask:0xf\n" \
  "v_rcp_f32 v198, v198\n" \
  "s_nop 0\n" \
  "v_mul_f32 v195, v195, v198\n" \
  "v_mul_f32 v193, v193, v198\n" \
  "v_cvt_pk_bf16_f32 v196, v195, v193\n" \
  "buffer_store_dword v196, v251, %[rs], %[so] offen sc1\n" \
  "v_cvt_pk_bf16_f32 v197, v198, v198\n" \
  "v_and_b32 v197, v252, v197\n" \
  "buffer_store_dword v197, v251, %[rs], %[so] offen offset:64 sc1\n" \
  ".Lw2b_%=:\n" \
  "s_setprio 0\n"
#define QK_A1 \
  "s_branch .Lgo_%=\n" \
  AL(A_QK_A1) \
  ".Lr_%=:\n" \
  "s_sub_u32 %[rt], %[rt], 1\n" \
  "s_cbranch_scc1 .Lrb_%=\n" \
  "s_sleep 2\n" \
  "buffer_load_dwordx4 v[192:195], v248, %[rs], %[o0] offen sc1\n" \
  "buffer_load_dwordx4 v[196:199], v248, %[rs], %[o0] offen offset:1024 sc1\n" \
  "v_mov_b64 v[224:225], 0\n" \
  "v_mov_b64 v[226:227], 0\n" \
  "v_mov_b64 v[228:229], 0\n" \
  "v_mov_b64 v[230:231], 0\n" \
  "v_mov_b64 v[232:233], 0\n" \
  "v_mov_b64 v[234:235], 0\n" \
  "v_mov_b64 v[236:237], 0\n" \
  "v_mov_b64 v[238:239], 0\n" \
  "s_nop 1\n" \
  "s_branch .Lw_%=\n" \
  ".Lw1_%=:\n" \
  "v_max_f32 v200, v192, v193\n" \
  "s_nop 1\n" \
  "v_max_f32_dpp v200, v200, v200 row_ror:8 row_mask:0xf bank_mask:0xf\n" \
  "s_nop 1\n" \
  "v_max_f32_dpp v200, v200, v200 row_ror:4 row_mask:0xf bank_mask:0xf\n" \
  "s_nop 1\n" \
  "v_max_f32_dpp v200, v200, v200 row_ror:2 row_mask:0xf bank_mask:0xf\n" \
  "s_nop 1\n" \
  "v_max_f32_dpp v200, v200, v200 row_ror:1 row_mask:0xf bank_mask:0xf\n" \
  "v_sub_f32 v194, v192, v200\n" \
  "v_sub_f32 v195, v193, v200\n" \
  "v_exp_f32 v194, v194\n" \
  "v_exp_f32 v195, v195\n" \
  "s_mov_b32 %[rt], -1.0\n" \
  "s_nop 0\n" \
  "v_cvt_pk_bf16_f32 v196, v194, v195\n" \
  "v_and_b32 v196, 0x7fffffff, v196\n" \
  "s_branch .Lw1b_%=\n" \
  ".Lw2_%=:\n" \
  "s_cmp_lt_i32 %[rt], 0\n" \
  "s_cbranch_scc1 .Lw3_%=\n" \
  "v_mov_b32 v200, 0\n" \
  ".Lw3_%=:\n" \
  "s_nop 1\n" \
  "v_max_f32_dpp v197, v192, v200 quad_perm:[0,0,0,0] row_mask:0xf bank_mask:0xf\n" \
  "s_nop 1\n" \
  "v_max_f32_dpp v197, v192, v197 quad_perm:[1,1,1,1] row_mask:0xf bank_mask:0xf\n" \
  "s_nop 1\n" \
  "v_max_f32_dpp v197, v197, v197 row_ror:4 row_mask:0xf bank_mask:0xf\n" \
  "s_nop 1\n" \
  "v_max_f32_dpp v197, v197, v197 row_ror:8 row_mask:0xf bank_mask:0xf\n" \
  "v_sub_f32 v194, v192, v197\n" \
  "v_sub_f32 v201, v200, v197\n" \
  "v_exp_f32 v194, v194\n" \
  "v_exp_f32 v201, v201\n" \
  "s_nop 1\n" \
  "v_mul_f32 v199, v201, v199\n" \
  "s_nop 1\n" \
  "v_add_f32_dpp v198, v194, v199 quad_perm:[0,0,0,0] row_mask:0xf bank_mask:0xf\n" \
  "s_nop 1\n" \
  "v_mov_b32_dpp v193, v194 quad_perm:[1,1,1,1] row_mask:0xf bank_mask:0xf\n" \
  "s_nop 1\n" \
  "v_add_f32 v198, v198, v193\n" \
  "s_nop 1\n" \
  "v_mov_b32_dpp v195, v194 quad_perm:[0,0,0,0] row_mask:0xf bank_mask:0xf\n" \
  "s_nop 1\n" \
  "v_add_f32_dpp v198, v198, v198 row_ror:4 row_mask:0xf bank_mask:0xf\n" \
  "s_nop 1\n" \
  "v_add_f32_dpp v198, v198, v198 row_ror:8 row_mask:0xf bank_mask:0xf\n" \
  "s_nop 1\n" \
  "v_rcp_f32 v198, v198\n" \
  "s_nop 1\n" \
  "v_mul_f32 v195, v195, v198\n" \
  "s_nop 1\n" \
  "v_mul_f32 v193, v193, v198\n" \
  "s_nop 1\n" \
  "v_cvt_pk_bf16_f32 v196, v195, v193\n" \
  "s_nop 1\n" \
  "v_and_b32 v196, 0x7fffffff, v196\n" \
  "s_nop 1\n" \
  "v_mul_f32 v198, v201, v198\n" \
  "s_nop 1\n" \
  "buffer_store_dword v196, v251, %[rs], %[so] offen sc1\n" \
  "s_nop 1\n" \
  "v_cvt_pk_bf16_f32 v197, v198, v198\n" \
  "s_nop 1\n" \
  "v_and_b32 v197, v252, v197\n" \
  "s_nop 1\n" \
  "buffer_store_dword v197, v251, %[rs], %[so] offen offset:64 sc1\n" \
  "s_branch .Lw2b_%=\n" \
  ".Lgo_%=:\n" \
  "v_add_u32 v252, 0x20000, v248\n" \
  "ds_read_b128 v[200:203], v248 offset:32768\n" \
  "ds_read_b128 v[204:207], v248 offset:33792\n" \
  "ds_read_b128 v[208:211], v248 offset:49152\n" \
  "ds_read_b128 v[212:215], v248 offset:50176\n" \
  "ds_read_b128 v[216:219], v252 offset:0\n" \
  "ds_read_b128 v[220:223], v252 offset:1024\n" \
  "ds_read_b128 v[240:243], v252 offset:16384\n" \
  "ds_read_b128 v[244:247], v252 offset:17408\n" \
  "v_mov_b64 v[224:225], 0\n" \
  "v_mov_b64 v[226:227], 0\n" \
  "v_mov_b64 v[228:229], 0\n" \
  "v_mov_b64 v[230:231], 0\n" \
  "v_mov_b64 v[232:233], 0\n" \
  "v_mov_b64 v[234:235], 0\n" \
  "v_mov_b64 v[236:237], 0\n" \
  "v_mov_b64 v[238:239], 0\n" \
  "v_xor_b32 v250, 0x800, v248\n" \
  "v_xor_b32 v251, 0x1000, v248\n" \
  "s_mov_b32 %[rt], 0x40000\n" \
  "s_setprio 2\n" \
  "s_waitcnt lgkmcnt(0)\n" \
  "s_barrier\n" \
  "buffer_load_dwordx4 v[192:195], v248, %[rs], %[o0] offen sc1\n" \
  "buffer_load_dwordx4 v[196:199], v248, %[rs], %[o0] offen offset:1024 sc1\n" \
  ".Lw_%=:\n" \
  "s_waitcnt vmcnt(1)\n" \
  "ds_write_b128 v248, v[192:195]\n" \
  "v_smfmac_f32_16x16x64_bf16 v[224:227], v[192:195], v[200:207], v249\n" \
  "v_max3_u32 v252, v192, v193, v194\n" \
  "s_waitcnt vmcnt(0)\n" \
  "ds_write_b128 v248, v[196:199] offset:1024\n" \
  "v_smfmac_f32_16x16x64_bf16 v[228:231], v[192:195], v[208:215], v249\n" \
  "v_max3_u32 v252, v252, v195, v196\n" \
  "v_smfmac_f32_16x16x64_bf16 v[224:227], v[196:199], v[216:223], v249\n" \
  "v_max3_u32 v252, v252, v197, v198\n" \
  "v_smfmac_f32_16x16x64_bf16 v[228:231], v[196:199], v[240:247], v249\n" \
  "v_max_u32 v252, v252, v199\n" \
  "v_smfmac_f32_16x16x64_bf16 v[232:235], v[192:195], v[96:103], v249\n" \
  "v_cmp_eq_u32 vcc, -1, v252\n" \
  "v_smfmac_f32_16x16x64_bf16 v[236:239], v[192:195], v[144:151], v249\n" \
  "v_xor_b32 v252, 0x1800, v248\n" \
  "s_cbranch_vccnz .Lr_%=\n" \
  ".Lrb_%=:\n" \
  "v_smfmac_f32_16x16x64_bf16 v[232:235], v[196:199], v[104:111], v249\n" \
  "v_smfmac_f32_16x16x64_bf16 v[236:239], v[196:199], v[152:159], v249\n" \
  "s_waitcnt lgkmcnt(0)\n" \
  "s_barrier\n" \
  "ds_read_b128 v[200:203], v250 offset:0\n" \
  "ds_read_b128 v[204:207], v250 offset:1024\n" \
  "ds_read_b128 v[208:211], v251 offset:0\n" \
  "ds_read_b128 v[212:215], v251 offset:1024\n" \
  "ds_read_b128 v[216:219], v252 offset:0\n" \
  "ds_read_b128 v[220:223], v252 offset:1024\n" \
  "s_waitcnt lgkmcnt(5)\n" \
  "v_smfmac_f32_16x16x64_bf16 v[224:227], v[200:203], v[0:7], v249\n" \
  "v_smfmac_f32_16x16x64_bf16 v[228:231], v[200:203], v[48:55], v249\n" \
  "s_waitcnt lgkmcnt(4)\n" \
  "v_smfmac_f32_16x16x64_bf16 v[224:227], v[204:207], v[8:15], v249\n" \
  "v_smfmac_f32_16x16x64_bf16 v[228:231], v[204:207], v[56:63], v249\n" \
  "s_waitcnt lgkmcnt(3)\n" \
  "v_smfmac_f32_16x16x64_bf16 v[224:227], v[208:211], v[16:23], v249\n" \
  "v_smfmac_f32_16x16x64_bf16 v[228:231], v[208:211], v[64:71], v249\n" \
  "s_waitcnt lgkmcnt(2)\n" \
  "v_smfmac_f32_16x16x64_bf16 v[224:227], v[212:215], v[24:31], v249\n" \
  "s_waitcnt lgkmcnt(1)\n" \
  "v_smfmac_f32_16x16x64_bf16 v[224:227], v[216:219], v[32:39], v249\n" \
  "s_waitcnt lgkmcnt(0)\n" \
  "v_smfmac_f32_16x16x64_bf16 v[224:227], v[220:223], v[40:47], v249\n" \
  "v_smfmac_f32_16x16x64_bf16 v[228:231], v[212:215], v[72:79], v249\n" \
  "v_smfmac_f32_16x16x64_bf16 v[228:231], v[216:219], v[80:87], v249\n" \
  "s_nop 0\n" \
  "v_add_f32 v224, v224, v226\n" \
  "v_smfmac_f32_16x16x64_bf16 v[228:231], v[220:223], v[88:95], v249\n" \
  "v_add_f32 v225, v225, v227\n" \
  "ds_write_b64 v248, v[224:225] offset:8192\n" \
  "s_nop 6\n" \
  "v_add_f32 v228, v228, v230\n" \
  "v_add_f32 v229, v229, v231\n" \
  "ds_write_b64 v248, v[228:229] offset:8200\n" \
  "s_setprio 0\n" \
  "ds_read_b128 v[240:243], v254 offset:0\n" \
  "ds_read_b128 v[244:247], v254 offset:1024\n" \
  "s_waitcnt lgkmcnt(2)\n" \
  "s_barrier\n" \
  "s_nop 1\n" \
  "ds_read_b128 v[192:195], v254 offset:16384\n" \
  "ds_read_b128 v[196:199], v254 offset:17408\n" \
  "ds_read_b128 v[224:227], v254 offset:32768\n" \
  "ds_read_b128 v[228:231], v254 offset:33792\n" \
  "s_waitcnt lgkmcnt(4)\n" \
  "v_smfmac_f32_16x16x64_bf16 v[232:235], v[200:203], v[240:247], v249\n" \
  "ds_read_b128 v[240:243], v254 offset:49152\n" \
  "ds_read_b128 v[244:247], v254 offset:50176\n" \
  "s_waitcnt lgkmcnt(4)\n" \
  "v_smfmac_f32_16x16x64_bf16 v[236:239], v[200:203], v[192:199], v249\n" \
  "v_smfmac_f32_16x16x64_bf16 v[232:235], v[204:207], v[112:119], v249\n" \
  "ds_read_b128 v[192:195], v248 offset:14336\n" \
  "v_smfmac_f32_16x16x64_bf16 v[236:239], v[204:207], v[160:167], v249\n" \
  "ds_read_b128 v[196:199], v248 offset:15360\n" \
  "v_smfmac_f32_16x16x64_bf16 v[232:235], v[212:215], v[120:127], v249\n" \
  "ds_read_b128 v[200:203], v248 offset:18432\n" \
  "v_smfmac_f32_16x16x64_bf16 v[236:239], v[212:215], v[168:175], v249\n" \
  "ds_read_b128 v[204:207], v248 offset:19456\n" \
  "s_waitcnt lgkmcnt(6)\n" \
  "v_smfmac_f32_16x16x64_bf16 v[232:235], v[208:211], v[224:231], v249\n" \
  "ds_read_b128 v[212:215], v248 offset:21504\n" \
  "s_waitcnt lgkmcnt(5)\n" \
  "v_smfmac_f32_16x16x64_bf16 v[236:239], v[208:211], v[240:247], v249\n" \
  "ds_read_b128 v[240:243], v248 offset:16384\n" \
  "v_smfmac_f32_16x16x64_bf16 v[232:235], v[216:219], v[128:135], v249\n" \
  "ds_read_b128 v[244:247], v248 offset:17408\n" \
  "v_smfmac_f32_16x16x64_bf16 v[232:235], v[220:223], v[136:143], v249\n" \
  "ds_read_b128 v[208:211], v248 offset:20480\n" \
  "v_smfmac_f32_16x16x64_bf16 v[236:239], v[216:219], v[176:183], v249\n" \
  "ds_read_b96 v[250:252], v248 offset:13312\n" \
  "v_smfmac_f32_16x16x64_bf16 v[236:239], v[220:223], v[184:191], v249\n" \
  "s_setprio 3\n" \
  "s_barrier\n" \
  "ds_read_b64_tr_b16 v[224:225], v253 offset:17408\n" \
  "ds_read_b64_tr_b16 v[226:227], v253 offset:17664\n" \
  "ds_read_b64_tr_b16 v[228:229], v253 offset:19456\n" \
  "ds_read_b64_tr_b16 v[230:231], v253 offset:19712\n" \
  "v_mov_b64 v[216:217], 0\n" \
  "v_mov_b64 v[218:219], 0\n" \
  "v_add_f32 v232, v232, v234\n" \
  "v_add_f32 v233, v233, v235\n" \
  "ds_write_b64 v248, v[232:233] offset:0\n" \
  "v_mov_b64 v[220:221], 0\n" \
  "v_mov_b64 v[222:223], 0\n" \
  "v_add_f32 v236, v236, v238\n" \
  "v_add_f32 v237, v237, v239\n" \
  "ds_write_b64 v248, v[236:237] offset:8\n" \
  "s_waitcnt lgkmcnt(4)\n" \
  "v_smfmac_f32_16x16x64_bf16 v[216:219], v[224:227], v[192:199], v249\n" \
  "v_smfmac_f32_16x16x64_bf16 v[220:223], v[224:227], v[200:207], v249\n" \
  "s_waitcnt lgkmcnt(2)\n" \
  "v_smfmac_f32_16x16x64_bf16 v[216:219], v[228:231], v[240:247], v249\n" \
  "v_smfmac_f32_16x16x64_bf16 v[220:223], v[228:231], v[208:215], v249\n" \
  "s_mov_b32 %[rt], 0x422c0000\n" \
  "s_waitcnt lgkmcnt(0)\n" \
  "s_barrier\n" \
  "s_nop 1\n" \
  "v_add_f32 v192, v217, v219\n" \
  "s_nop 2\n" \
  "v_add_f32 v193, v221, v223\n" \
  "v_exp_f32 v194, v192\n" \
  "v_exp_f32 v195, v193\n" \
  "v_max_f32 v197, |v192|, |v193|\n" \
  "v_cvt_pk_bf16_f32 v196, v194, v195\n" \
  "v_cmp_nge_f32 vcc, %[rt], v197\n" \
  "s_cbranch_vccnz .Lw1_%=\n" \
  ".Lw1b_%=:\n" \
  "buffer_store_dword v196, v250, %[rs], %[c0] offen sc1\n" \
  "buffer_store_dword v196, v250, %[rs], %[c1] offen sc1\n" \
  "buffer_store_dword v196, v250, %[rs], %[c2] offen sc1\n" \
  "buffer_store_dword v196, v250, %[rs], %[c3] offen sc1\n" \
  "v_add_f32 v199, v194, v195\n" \
  "v_xor_b32 v251, %[xr], v251\n" \
  "s_nop 0\n" \
  "v_add_f32_dpp v199, v199, v199 quad_perm:[1,0,3,2] row_mask:0xf bank_mask:0xf\n" \
  "s_nop 1\n" \
  "v_add_f32_dpp v199, v199, v199 quad_perm:[2,3,0,1] row_mask:0xf bank_mask:0xf\n" \
  "s_barrier\n" \
  "ds_read_b64_tr_b16 v[232:233], v253 offset:9216\n" \
  "ds_read_b64_tr_b16 v[234:235], v253 offset:9472\n" \
  "ds_read_b64_tr_b16 v[236:237], v253 offset:11264\n" \
  "ds_read_b64_tr_b16 v[238:239], v253 offset:11520\n" \
  "s_waitcnt lgkmcnt(2)\n" \
  "v_mfma_f32_16x16x32_bf16 v[216:219], v[224:227], v[232:235], 0\n" \
  "s_waitcnt lgkmcnt(0)\n" \
  "v_mfma_f32_16x16x32_bf16 v[216:219], v[228:231], v[236:239], v[216:219]\n" \
  "s_nop 8\n" \
  "v_add_f32_dpp v192, v219, v217 quad_perm:[2,3,2,3] row_mask:0xf bank_mask:0xf\n" \
  "v_exp_f32 v194, v192\n" \
  "v_cmp_nge_f32 vcc, %[rt], |v192|\n" \
  "s_and_b64 vcc, vcc, %[lo]\n" \
  "v_add_f32_dpp v198, v194, v199 quad_perm:[0,0,0,0] row_mask:0xf bank_mask:0xf\n" \
  "v_mov_b32_dpp v193, v194 quad_perm:[1,1,1,1] row_mask:0xf bank_mask:0xf\n" \
  "v_add_f32 v198, v198, v193\n" \
  "v_mov_b32_dpp v195, v194 quad_perm:[0,0,0,0] row_mask:0xf bank_mask:0xf\n" \
  "s_cbranch_scc1 .Lw2_%=\n" \
  "v_add_f32_dpp v198, v198, v198 row_ror:4 row_mask:0xf bank_mask:0xf\n" \
  "s_nop 1\n" \
  "v_add_f32_dpp v198, v198, v198 row_ror:8 row_mask:0xf bank_mask:0xf\n" \
  "v_rcp_f32 v198, v198\n" \
  "s_nop 0\n" \
  "v_mul_f32 v195, v195, v198\n" \
  "v_mul_f32 v193, v193, v198\n" \
  "v_cvt_pk_bf16_f32 v196, v195, v193\n" \
  "buffer_store_dword v196, v251, %[rs], %[so] offen sc1\n" \
  "v_cvt_pk_bf16_f32 v197, v198, v198\n" \
  "v_and_b32 v197, v252, v197\n" \
  "buffer_store_dword v197, v251, %[rs], %[so] offen offset:64 sc1\n" \
  ".Lw2b_%=:\n" \
  "s_setprio 0\n"
#define QK_X \
  "s_branch .Lgo_%=\n" \
  AL(A_QK_X) \
  ".Lr_%=:\n" \
  "s_sub_u32 %[rt], %[rt], 1\n" \
  "s_cbranch_scc1 .Lrb_%=\n" \
  "s_sleep 2\n" \
  "buffer_load_dwordx4 v[192:195], v248, %[rs], %[o0] offen sc1\n" \
  "buffer_load_dwordx4 v[196:199], v248, %[rs], %[o0] offen offset:1024 sc1\n" \
  "v_mov_b64 v[224:225], 0\n" \
  "v_mov_b64 v[226:227], 0\n" \
  "v_mov_b64 v[228:229], 0\n" \
  "v_mov_b64 v[230:231], 0\n" \
  "v_mov_b64 v[232:233], 0\n" \
  "v_mov_b64 v[234:235], 0\n" \
  "v_mov_b64 v[236:237], 0\n" \
  "v_mov_b64 v[238:239], 0\n" \
  "s_nop 1\n" \
  "s_branch .Lw_%=\n" \
  ".Lgo_%=:\n" \
  "v_add_u32 v252, 0x20000, v248\n" \
  "ds_read_b128 v[200:203], v248 offset:32768\n" \
  "ds_read_b128 v[204:207], v248 offset:33792\n" \
  "ds_read_b128 v[208:211], v248 offset:49152\n" \
  "ds_read_b128 v[212:215], v248 offset:50176\n" \
  "ds_read_b128 v[216:219], v252 offset:0\n" \
  "ds_read_b128 v[220:223], v252 offset:1024\n" \
  "ds_read_b128 v[240:243], v252 offset:16384\n" \
  "ds_read_b128 v[244:247], v252 offset:17408\n" \
  "v_mov_b64 v[224:225], 0\n" \
  "v_mov_b64 v[226:227], 0\n" \
  "v_mov_b64 v[228:229], 0\n" \
  "v_mov_b64 v[230:231], 0\n" \
  "v_mov_b64 v[232:233], 0\n" \
  "v_mov_b64 v[234:235], 0\n" \
  "v_mov_b64 v[236:237], 0\n" \
  "v_mov_b64 v[238:239], 0\n" \
  "v_xor_b32 v250, 0x800, v248\n" \
  "v_xor_b32 v251, 0x1000, v248\n" \
  "s_mov_b32 %[rt], 0x40000\n" \
  "s_setprio 2\n" \
  "s_waitcnt lgkmcnt(0)\n" \
  "s_barrier\n" \
  "s_nop 3\n" \
  "buffer_load_dwordx4 v[192:195], v248, %[rs], %[o0] offen sc1\n" \
  "buffer_load_dwordx4 v[196:199], v248, %[rs], %[o0] offen offset:1024 sc1\n" \
  ".Lw_%=:\n" \
  "s_waitcnt vmcnt(1)\n" \
  "ds_write_b128 v248, v[192:195]\n" \
  "v_smfmac_f32_16x16x64_bf16 v[224:227], v[192:195], v[200:207], v249\n" \
  "v_max3_u32 v252, v192, v193, v194\n" \
  "s_waitcnt vmcnt(0)\n" \
  "ds_write_b128 v248, v[196:199] offset:1024\n" \
  "v_smfmac_f32_16x16x64_bf16 v[228:231], v[192:195], v[208:215], v249\n" \
  "v_max3_u32 v252, v252, v195, v196\n" \
  "v_smfmac_f32_16x16x64_bf16 v[224:227], v[196:199], v[216:223], v249\n" \
  "v_max3_u32 v252, v252, v197, v198\n" \
  "v_smfmac_f32_16x16x64_bf16 v[228:231], v[196:199], v[240:247], v249\n" \
  "v_max_u32 v252, v252, v199\n" \
  "v_smfmac_f32_16x16x64_bf16 v[232:235], v[192:195], v[96:103], v249\n" \
  "v_cmp_eq_u32 vcc, -1, v252\n" \
  "v_smfmac_f32_16x16x64_bf16 v[236:239], v[192:195], v[144:151], v249\n" \
  "v_xor_b32 v252, 0x1800, v248\n" \
  "s_cbranch_vccnz .Lr_%=\n" \
  ".Lrb_%=:\n" \
  "v_smfmac_f32_16x16x64_bf16 v[232:235], v[196:199], v[104:111], v249\n" \
  "v_smfmac_f32_16x16x64_bf16 v[236:239], v[196:199], v[152:159], v249\n" \
  "s_waitcnt lgkmcnt(0)\n" \
  "s_barrier\n" \
  "ds_read_b128 v[200:203], v250 offset:0\n" \
  "ds_read_b128 v[204:207], v250 offset:1024\n" \
  "ds_read_b128 v[208:211], v251 offset:0\n" \
  "ds_read_b128 v[212:215], v251 offset:1024\n" \
  "ds_read_b128 v[216:219], v252 offset:0\n" \
  "ds_read_b128 v[220:223], v252 offset:1024\n" \
  "s_waitcnt lgkmcnt(5)\n" \
  "v_smfmac_f32_16x16x64_bf16 v[224:227], v[200:203], v[0:7], v249\n" \
  "v_smfmac_f32_16x16x64_bf16 v[228:231], v[200:203], v[48:55], v249\n" \
  "s_waitcnt lgkmcnt(4)\n" \
  "v_smfmac_f32_16x16x64_bf16 v[224:227], v[204:207], v[8:15], v249\n" \
  "v_smfmac_f32_16x16x64_bf16 v[228:231], v[204:207], v[56:63], v249\n" \
  "s_waitcnt lgkmcnt(3)\n" \
  "v_smfmac_f32_16x16x64_bf16 v[224:227], v[208:211], v[16:23], v249\n" \
  "v_smfmac_f32_16x16x64_bf16 v[228:231], v[208:211], v[64:71], v249\n" \
  "s_waitcnt lgkmcnt(2)\n" \
  "v_smfmac_f32_16x16x64_bf16 v[224:227], v[212:215], v[24:31], v249\n" \
  "s_waitcnt lgkmcnt(1)\n" \
  "v_smfmac_f32_16x16x64_bf16 v[224:227], v[216:219], v[32:39], v249\n" \
  "s_waitcnt lgkmcnt(0)\n" \
  "v_smfmac_f32_16x16x64_bf16 v[224:227], v[220:223], v[40:47], v249\n" \
  "v_smfmac_f32_16x16x64_bf16 v[228:231], v[212:215], v[72:79], v249\n" \
  "v_and_b32 v252, 0x3ff, v248\n" \
  "v_smfmac_f32_16x16x64_bf16 v[228:231], v[216:219], v[80:87], v249\n" \
  "s_nop 0\n" \
  "v_add_f32 v224, v224, v226\n" \
  "v_smfmac_f32_16x16x64_bf16 v[228:231], v[220:223], v[88:95], v249\n" \
  "v_add_f32 v225, v225, v227\n" \
  "ds_write_b64 v248, v[224:225] offset:8192\n" \
  "s_nop 6\n" \
  "v_add_f32 v228, v228, v230\n" \
  "v_add_f32 v229, v229, v231\n" \
  "ds_write_b64 v248, v[228:229] offset:8200\n" \
  "s_setprio 0\n" \
  "ds_read_b128 v[240:243], v254 offset:0\n" \
  "ds_read_b128 v[244:247], v254 offset:1024\n" \
  "s_waitcnt lgkmcnt(2)\n" \
  "s_barrier\n" \
  "s_nop 1\n" \
  "ds_read_b128 v[192:195], v254 offset:16384\n" \
  "ds_read_b128 v[196:199], v254 offset:17408\n" \
  "ds_read_b128 v[224:227], v254 offset:32768\n" \
  "ds_read_b128 v[228:231], v254 offset:33792\n" \
  "s_waitcnt lgkmcnt(4)\n" \
  "v_smfmac_f32_16x16x64_bf16 v[232:235], v[200:203], v[240:247], v249\n" \
  "ds_read_b128 v[240:243], v254 offset:49152\n" \
  "ds_read_b128 v[244:247], v254 offset:50176\n" \
  "s_waitcnt lgkmcnt(4)\n" \
  "v_smfmac_f32_16x16x64_bf16 v[236:239], v[200:203], v[192:199], v249\n" \
  "v_smfmac_f32_16x16x64_bf16 v[232:235], v[204:207], v[112:119], v249\n" \
  "v_smfmac_f32_16x16x64_bf16 v[236:239], v[204:207], v[160:167], v249\n" \
  "v_smfmac_f32_16x16x64_bf16 v[232:235], v[212:215], v[120:127], v249\n" \
  "v_smfmac_f32_16x16x64_bf16 v[236:239], v[212:215], v[168:175], v249\n" \
  "s_waitcnt lgkmcnt(2)\n" \
  "v_smfmac_f32_16x16x64_bf16 v[232:235], v[208:211], v[224:231], v249\n" \
  "s_waitcnt lgkmcnt(0)\n" \
  "v_smfmac_f32_16x16x64_bf16 v[236:239], v[208:211], v[240:247], v249\n" \
  "v_smfmac_f32_16x16x64_bf16 v[232:235], v[216:219], v[128:135], v249\n" \
  "v_smfmac_f32_16x16x64_bf16 v[232:235], v[220:223], v[136:143], v249\n" \
  "v_smfmac_f32_16x16x64_bf16 v[236:239], v[216:219], v[176:183], v249\n" \
  "buffer_load_dwordx4 v[224:227], v252, %[rs], %[qo] offen offset:0 sc1\n" \
  "buffer_load_dwordx4 v[228:231], v252, %[rs], %[qo] offen offset:1024 sc1\n" \
  "buffer_load_dwordx4 v[240:243], v252, %[rs], %[qo] offen offset:2048 sc1\n" \
  "buffer_load_dwordx4 v[244:247], v252, %[rs], %[qo] offen offset:3072 sc1\n" \
  "v_smfmac_f32_16x16x64_bf16 v[236:239], v[220:223], v[184:191], v249\n" \
  "s_setprio 3\n" \
  "s_barrier\n" \
  "v_add_f32 v232, v232, v234\n" \
  "v_add_f32 v233, v233, v235\n" \
  "ds_write_b64 v248, v[232:233] offset:0\n" \
  "s_nop 3\n" \
  "v_add_f32 v236, v236, v238\n" \
  "v_add_f32 v237, v237, v239\n" \
  "ds_write_b64 v248, v[236:237] offset:8\n" \
  "s_waitcnt lgkmcnt(0)\n" \
  "s_barrier\n" \
  "s_setprio 0\n" \
  "s_nop 15\n" \
  "s_nop 13\n" \
  "ds_read_b128 v[192:195], v252 offset:24576\n" \
  "ds_read_b128 v[196:199], v252 offset:28672\n" \
  "ds_read_b128 v[200:203], v252 offset:25600\n" \
  "ds_read_b128 v[204:207], v252 offset:29696\n" \
  "ds_read_b128 v[208:211], v252 offset:26624\n" \
  "ds_read_b128 v[212:215], v252 offset:30720\n" \
  "ds_read_b128 v[216:219], v252 offset:27648\n" \
  "ds_read_b128 v[220:223], v252 offset:31744\n" \
  "s_waitcnt lgkmcnt(0)\n" \
  "s_barrier\n" \
  "s_nop 15\n" \
  "s_nop 3\n" \
  "ds_read_b64_tr_b16 v[232:233], v253 offset:9216\n" \
  "ds_read_b64_tr_b16 v[234:235], v253 offset:9472\n" \
  "ds_read_b64_tr_b16 v[236:237], v253 offset:11264\n" \
  "ds_read_b64_tr_b16 v[238:239], v253 offset:11520\n" \
  "s_waitcnt vmcnt(3)\n" \
  "v_mfma_f32_16x16x32_bf16 v[192:195], v[192:195], v[224:227], 0\n" \
  "v_mfma_f32_16x16x32_bf16 v[196:199], v[196:199], v[224:227], 0\n" \
  "s_waitcnt vmcnt(2)\n" \
  "v_mfma_f32_16x16x32_bf16 v[192:195], v[200:203], v[228:231], v[192:195]\n" \
  "v_mfma_f32_16x16x32_bf16 v[196:199], v[204:207], v[228:231], v[196:199]\n" \
  "s_waitcnt vmcnt(1)\n" \
  "v_mfma_f32_16x16x32_bf16 v[192:195], v[208:211], v[240:243], v[192:195]\n" \
  "v_mov_b64 v[200:201], 0\n" \
  "v_mfma_f32_16x16x32_bf16 v[196:199], v[212:215], v[240:243], v[196:199]\n" \
  "v_mov_b64 v[202:203], 0\n" \
  "s_waitcnt vmcnt(0)\n" \
  "v_mfma_f32_16x16x32_bf16 v[192:195], v[216:219], v[244:247], v[192:195]\n" \
  "v_mfma_f32_16x16x32_bf16 v[196:199], v[220:223], v[244:247], v[196:199]\n" \
  "s_waitcnt lgkmcnt(2)\n" \
  "v_smfmac_f32_16x16x64_bf16 v[200:203], v[232:235], v[224:231], v249\n" \
  "s_waitcnt lgkmcnt(0)\n" \
  "v_smfmac_f32_16x16x64_bf16 v[200:203], v[236:239], v[240:247], v249\n" \
  "s_nop 8\n"
#define QK_K \
  "s_branch .Lgo_%=\n" \
  AL(A_QK_K) \
  ".Lr_%=:\n" \
  "s_sub_u32 %[rt], %[rt], 1\n" \
  "s_cbranch_scc1 .Lrb_%=\n" \
  "s_sleep 2\n" \
  "buffer_load_dwordx4 v[192:195], v248, %[rs], %[o0] offen sc1\n" \
  "buffer_load_dwordx4 v[196:199], v248, %[rs], %[o0] offen offset:1024 sc1\n" \
  "v_mov_b64 v[224:225], 0\n" \
  "v_mov_b64 v[226:227], 0\n" \
  "v_mov_b64 v[228:229], 0\n" \
  "v_mov_b64 v[230:231], 0\n" \
  "s_nop 1\n" \
  "s_branch .Lw_%=\n" \
  ".Lgo_%=:\n" \
  "v_add_u32 v252, 0x20000, v248\n" \
  "ds_read_b128 v[200:203], v248 offset:32768\n" \
  "ds_read_b128 v[204:207], v248 offset:33792\n" \
  "ds_read_b128 v[208:211], v248 offset:49152\n" \
  "ds_read_b128 v[212:215], v248 offset:50176\n" \
  "ds_read_b128 v[216:219], v252 offset:0\n" \
  "ds_read_b128 v[220:223], v252 offset:1024\n" \
  "ds_read_b128 v[240:243], v252 offset:16384\n" \
  "ds_read_b128 v[244:247], v252 offset:17408\n" \
  "v_mov_b64 v[224:225], 0\n" \
  "v_mov_b64 v[226:227], 0\n" \
  "v_mov_b64 v[228:229], 0\n" \
  "v_mov_b64 v[230:231], 0\n" \
  "v_xor_b32 v250, 0x800, v248\n" \
  "v_xor_b32 v251, 0x1000, v248\n" \
  "s_mov_b32 %[rt], 0x40000\n" \
  "s_setprio 1\n" \
  "s_waitcnt lgkmcnt(0)\n" \
  "s_cmp_eq_u32 %[tw], 0\n" \
  "s_cbranch_scc1 .Ltq_%=\n" \
  ".Ltl_%=:\n" \
  "s_memtime vcc\n" \
  "s_waitcnt lgkmcnt(0)\n" \
  "s_sub_i32 %[cnt], %[tg], vcc_lo\n" \
  "s_cmp_lt_i32 %[cnt], 260\n" \
  "s_cbranch_scc1 .Ltf_%=\n" \
  "s_sleep 1\n" \
  "s_branch .Ltl_%=\n" \
  ".Ltf_%=:\n" \
  "s_max_i32 %[cnt], %[cnt], 0\n" \
  ".Ltg_%=:\n" \
  "s_sub_u32 %[cnt], %[cnt], 28\n" \
  "s_cbranch_scc0 .Ltg_%=\n" \
  ".Ltq_%=:\n" \
  "s_barrier\n" \
  "buffer_load_dwordx4 v[192:195], v248, %[rs], %[o0] offen sc1\n" \
  "buffer_load_dwordx4 v[196:199], v248, %[rs], %[o0] offen offset:1024 sc1\n" \
  ".Lw_%=:\n" \
  "s_waitcnt vmcnt(1)\n" \
  "ds_write_b128 v248, v[192:195]\n" \
  "v_smfmac_f32_16x16x64_bf16 v[224:227], v[192:195], v[200:207], v249\n" \
  "v_max3_u32 v252, v192, v193, v194\n" \
  "s_waitcnt vmcnt(0)\n" \
  "ds_write_b128 v248, v[196:199] offset:1024\n" \
  "v_smfmac_f32_16x16x64_bf16 v[228:231], v[192:195], v[208:215], v249\n" \
  "v_max3_u32 v252, v252, v195, v196\n" \
  "v_smfmac_f32_16x16x64_bf16 v[224:227], v[196:199], v[216:223], v249\n" \
  "v_max3_u32 v252, v252, v197, v198\n" \
  "v_max_u32 v252, v252, v199\n" \
  "v_smfmac_f32_16x16x64_bf16 v[228:231], v[196:199], v[240:247], v249\n" \
  "v_cmp_eq_u32 vcc, -1, v252\n" \
  "v_xor_b32 v252, 0x1800, v248\n" \
  "s_cbranch_vccnz .Lr_%=\n" \
  ".Lrb_%=:\n" \
  "s_waitcnt lgkmcnt(0)\n" \
  "s_barrier\n" \
  "s_memtime vcc\n" \
  "s_setprio 3\n" \
  "s_waitcnt lgkmcnt(0)\n" \
  "s_sub_u32 %[cnt], vcc_lo, %[ckm]\n" \
  "s_mul_hi_u32 %[cnt], %[cnt], %[inv]\n" \
  "s_add_u32 %[cnt], %[cnt], %[r16]\n" \
  "s_and_b32 %[cnt], %[cnt], -2\n" \
  "v_mov_b32 v235, %[cnt]\n" \
  "buffer_store_dword v235, v232, %[rs], %[sst] offen sc1\n" \
  "s_nop 5\n" \
  "ds_read_b128 v[200:203], v250 offset:0\n" \
  "ds_read_b128 v[204:207], v250 offset:1024\n" \
  "ds_read_b128 v[208:211], v251 offset:0\n" \
  "ds_read_b128 v[212:215], v251 offset:1024\n" \
  "ds_read_b128 v[216:219], v252 offset:0\n" \
  "ds_read_b128 v[220:223], v252 offset:1024\n" \
  "buffer_load_dword v250, v233, %[rs], %[t0] offen sc1\n" \
  "buffer_load_dword v251, v234, %[rs], %[t1] offen sc1\n" \
  "s_setprio 1\n" \
  "s_waitcnt lgkmcnt(5)\n" \
  "v_smfmac_f32_16x16x64_bf16 v[224:227], v[200:203], v[0:7], v249\n" \
  "v_smfmac_f32_16x16x64_bf16 v[228:231], v[200:203], v[48:55], v249\n" \
  "s_waitcnt lgkmcnt(4)\n" \
  "v_smfmac_f32_16x16x64_bf16 v[224:227], v[204:207], v[8:15], v249\n" \
  "v_mov_b64 v[232:233], 0\n" \
  "v_smfmac_f32_16x16x64_bf16 v[228:231], v[204:207], v[56:63], v249\n" \
  "v_mov_b64 v[234:235], 0\n" \
  "s_waitcnt lgkmcnt(3)\n" \
  "v_smfmac_f32_16x16x64_bf16 v[224:227], v[208:211], v[16:23], v249\n" \
  "ds_read_b128 v[240:243], v254 offset:0\n" \
  "v_smfmac_f32_16x16x64_bf16 v[228:231], v[208:211], v[64:71], v249\n" \
  "ds_read_b128 v[244:247], v254 offset:1024\n" \
  "s_waitcnt lgkmcnt(4)\n" \
  "v_smfmac_f32_16x16x64_bf16 v[224:227], v[212:215], v[24:31], v249\n" \
  "s_waitcnt lgkmcnt(3)\n" \
  "v_smfmac_f32_16x16x64_bf16 v[224:227], v[216:219], v[32:39], v249\n" \
  "s_waitcnt lgkmcnt(2)\n" \
  "v_smfmac_f32_16x16x64_bf16 v[224:227], v[220:223], v[40:47], v249\n" \
  "s_barrier\n" \
  "ds_read_b128 v[236:239], v248 offset:16384\n" \
  "v_smfmac_f32_16x16x64_bf16 v[228:231], v[212:215], v[72:79], v249\n" \
  "v_smfmac_f32_16x16x64_bf16 v[228:231], v[216:219], v[80:87], v249\n" \
  "s_nop 0\n" \
  "v_add_f32 v224, v224, v226\n" \
  "v_smfmac_f32_16x16x64_bf16 v[228:231], v[220:223], v[88:95], v249\n" \
  "v_add_f32 v225, v225, v227\n" \
  "s_waitcnt lgkmcnt(0)\n" \
  "v_add_f32 v236, v236, v224\n" \
  "v_add_f32 v237, v237, v225\n" \
  "v_cvt_pk_bf16_f32 v236, v236, v237\n" \
  "s_nop 3\n" \
  "v_add_f32 v238, v238, v228\n" \
  "v_add_f32 v239, v239, v229\n" \
  "v_add_f32 v238, v238, v230\n" \
  "v_add_f32 v239, v239, v231\n" \
  "v_cvt_pk_bf16_f32 v237, v238, v239\n" \
  "ds_write_b64 v253, v[236:237] offset:17408\n" \
  "s_setprio 0\n" \
  "ds_read_b128 v[224:227], v254 offset:16384\n" \
  "ds_read_b128 v[228:231], v254 offset:17408\n" \
  "v_mov_b64 v[236:237], 0\n" \
  "v_mov_b64 v[238:239], 0\n" \
  "v_smfmac_f32_16x16x64_bf16 v[232:235], v[192:195], v[96:103], v249\n" \
  "v_smfmac_f32_16x16x64_bf16 v[236:239], v[192:195], v[144:151], v249\n" \
  "s_waitcnt lgkmcnt(2)\n" \
  "s_barrier\n" \
  "v_smfmac_f32_16x16x64_bf16 v[232:235], v[196:199], v[104:111], v249\n" \
  "v_smfmac_f32_16x16x64_bf16 v[236:239], v[196:199], v[152:159], v249\n" \
  "v_smfmac_f32_16x16x64_bf16 v[232:235], v[200:203], v[240:247], v249\n" \
  "ds_read_b128 v[192:195], v254 offset:32768\n" \
  "ds_read_b128 v[196:199], v254 offset:33792\n" \
  "s_waitcnt lgkmcnt(2)\n" \
  "v_smfmac_f32_16x16x64_bf16 v[236:239], v[200:203], v[224:231], v249\n" \
  "ds_read_b128 v[240:243], v254 offset:49152\n" \
  "ds_read_b128 v[244:247], v254 offset:50176\n" \
  "v_smfmac_f32_16x16x64_bf16 v[232:235], v[204:207], v[112:119], v249\n" \
  "v_smfmac_f32_16x16x64_bf16 v[236:239], v[204:207], v[160:167], v249\n" \
  "s_barrier\n" \
  "ds_read_b128 v[200:203], v248 offset:8192\n" \
  "v_smfmac_f32_16x16x64_bf16 v[232:235], v[212:215], v[120:127], v249\n" \
  "v_smfmac_f32_16x16x64_bf16 v[236:239], v[212:215], v[168:175], v249\n" \
  "v_smfmac_f32_16x16x64_bf16 v[232:235], v[216:219], v[128:135], v249\n" \
  "v_smfmac_f32_16x16x64_bf16 v[232:235], v[220:223], v[136:143], v249\n" \
  "s_waitcnt lgkmcnt(0)\n" \
  "v_mul_f32 v200, %[cs], v200\n" \
  "v_smfmac_f32_16x16x64_bf16 v[232:235], v[208:211], v[192:199], v249\n" \
  "v_mul_f32 v201, %[cs], v201\n" \
  "v_smfmac_f32_16x16x64_bf16 v[236:239], v[216:219], v[176:183], v249\n" \
  "v_mul_f32 v202, %[cs], v202\n" \
  "v_smfmac_f32_16x16x64_bf16 v[236:239], v[220:223], v[184:191], v249\n" \
  "v_mul_f32 v203, %[cs], v203\n" \
  "v_smfmac_f32_16x16x64_bf16 v[236:239], v[208:211], v[240:247], v249\n" \
  "v_fma_f32 v200, v232, %[cs], v200\n" \
  "v_fma_f32 v201, v233, %[cs], v201\n" \
  "v_fma_f32 v200, v234, %[cs], v200\n" \
  "v_fma_f32 v201, v235, %[cs], v201\n" \
  "v_cvt_pk_bf16_f32 v200, v200, v201\n" \
  "s_nop 3\n" \
  "v_fma_f32 v202, v236, %[cs], v202\n" \
  "v_fma_f32 v203, v237, %[cs], v203\n" \
  "v_fma_f32 v202, v238, %[cs], v202\n" \
  "v_fma_f32 v203, v239, %[cs], v203\n" \
  "v_cvt_pk_bf16_f32 v201, v202, v203\n" \
  "ds_write_b64 v253, v[200:201] offset:9216\n" \
  "s_waitcnt lgkmcnt(0)\n" \
  "s_barrier\n" \
  "s_waitcnt vmcnt(0)\n"
#define OW_F \
  "s_branch .Lgo_%=\n" \
  AL(A_OW_F) \
  ".Lbad_%=:\n" \
  "s_mov_b32 %[st], 1\n" \
  "s_branch .Lend_%=\n" \
  ".Lgo_%=:\n" \
  "v_mov_b64 v[96:97], 0\n" \
  "v_mov_b64 v[98:99], 0\n" \
  "s_mov_b32 %[st], 0\n" \
  "s_barrier\n" \
  "s_nop 15\n" \
  "s_nop 15\n" \
  "s_nop 12\n" \
  "ds_read_b32 v100, v118\n" \
  "buffer_load_dwordx4 v[0:3], v112, %[rs], %[sv1] offen offset:0 sc1\n" \
  "buffer_load_dwordx4 v[4:7], v112, %[rs], %[sv1] offen offset:1024 sc1\n" \
  "buffer_load_dwordx4 v[8:11], v112, %[rs], %[sv1] offen offset:2048 sc1\n" \
  "buffer_load_dwordx4 v[12:15], v112, %[rs], %[sv1] offen offset:3072 sc1\n" \
  "s_memtime vcc\n" \
  "s_waitcnt lgkmcnt(0)\n" \
  "v_readfirstlane_b32 %[cnt], v100\n" \
  "s_nop 0\n" \
  "s_sub_i32 %[cnt], %[cnt], vcc_lo\n" \
  "s_max_i32 %[cnt], %[cnt], 0\n" \
  "s_min_i32 %[cnt], %[cnt], 2000\n" \
  ".Ltg_%=:\n" \
  "s_sub_u32 %[cnt], %[cnt], 28\n" \
  "s_cbranch_scc0 .Ltg_%=\n" \
  "buffer_load_dwordx4 v[16:19], v112, %[rs], %[sv2] offen offset:0 sc1\n" \
  "buffer_load_dwordx4 v[20:23], v112, %[rs], %[sv2] offen offset:1024 sc1\n" \
  "buffer_load_dwordx4 v[24:27], v112, %[rs], %[sv2] offen offset:2048 sc1\n" \
  "buffer_load_dwordx4 v[28:31], v112, %[rs], %[sv2] offen offset:3072 sc1\n" \
  "s_nop 3\n" \
  "buffer_load_dwordx4 v[64:67], v113, %[rs], %[sp1] offen offset:0 sc1\n" \
  "buffer_load_dwordx4 v[68:71], v113, %[rs], %[sp1] offen offset:2048 sc1\n" \
  "buffer_load_dwordx4 v[72:75], v113, %[rs], %[sp2] offen offset:0 sc1\n" \
  "buffer_load_dwordx4 v[76:79], v113, %[rs], %[sp2] offen offset:2048 sc1\n" \
  "buffer_load_dwordx4 v[80:83], v113, %[rs], %[sp1] offen offset:64 sc1\n" \
  "buffer_load_dwordx4 v[84:87], v113, %[rs], %[sp1] offen offset:2112 sc1\n" \
  "buffer_load_dwordx4 v[88:91], v113, %[rs], %[sp2] offen offset:64 sc1\n" \
  "buffer_load_dwordx4 v[92:95], v113, %[rs], %[sp2] offen offset:2112 sc1\n" \
  "s_waitcnt vmcnt(15)\n" \
  "v_permlane16_swap_b32 v0, v1\n" \
  "v_permlane16_swap_b32 v2, v3\n" \
  "s_waitcnt vmcnt(14)\n" \
  "v_permlane16_swap_b32 v4, v5\n" \
  "v_permlane16_swap_b32 v6, v7\n" \
  "s_waitcnt vmcnt(13)\n" \
  "v_permlane16_swap_b32 v8, v9\n" \
  "v_permlane16_swap_b32 v10, v11\n" \
  "s_waitcnt vmcnt(12)\n" \
  "v_permlane16_swap_b32 v12, v13\n" \
  "v_permlane16_swap_b32 v14, v15\n" \
  "s_waitcnt vmcnt(11)\n" \
  "v_permlane16_swap_b32 v16, v17\n" \
  "v_permlane16_swap_b32 v18, v19\n" \
  "s_waitcnt vmcnt(10)\n" \
  "v_permlane16_swap_b32 v20, v21\n" \
  "v_permlane16_swap_b32 v22, v23\n" \
  "s_waitcnt vmcnt(9)\n" \
  "v_permlane16_swap_b32 v24, v25\n" \
  "v_permlane16_swap_b32 v26, v27\n" \
  "s_waitcnt vmcnt(8)\n" \
  "v_permlane16_swap_b32 v28, v29\n" \
  "v_permlane16_swap_b32 v30, v31\n" \
  "s_barrier\n" \
  "ds_read_b128 v[32:35], v114 offset:0\n" \
  "ds_read_b128 v[36:39], v114 offset:1024\n" \
  "ds_read_b128 v[40:43], v114 offset:2048\n" \
  "ds_read_b128 v[44:47], v114 offset:3072\n" \
  "ds_read_b128 v[48:51], v119 offset:0\n" \
  "ds_read_b128 v[52:55], v119 offset:1024\n" \
  "ds_read_b128 v[56:59], v119 offset:2048\n" \
  "ds_read_b128 v[60:63], v119 offset:3072\n" \
  "s_waitcnt vmcnt(7)\n" \
  "v_smfmac_f32_16x16x64_bf16 v[96:99], v[64:67], v[0:7], v117\n" \
  "s_waitcnt vmcnt(6)\n" \
  "v_smfmac_f32_16x16x64_bf16 v[96:99], v[68:71], v[8:15], v117\n" \
  "s_waitcnt vmcnt(5)\n" \
  "v_smfmac_f32_16x16x64_bf16 v[96:99], v[72:75], v[16:23], v117\n" \
  "s_waitcnt vmcnt(4)\n" \
  "v_smfmac_f32_16x16x64_bf16 v[96:99], v[76:79], v[24:31], v117\n" \
  "s_waitcnt vmcnt(3) lgkmcnt(6)\n" \
  "v_smfmac_f32_16x16x64_bf16 v[96:99], v[80:83], v[32:39], v117 abid:1\n" \
  "s_waitcnt vmcnt(2) lgkmcnt(4)\n" \
  "v_smfmac_f32_16x16x64_bf16 v[96:99], v[84:87], v[40:47], v117 abid:1\n" \
  "s_waitcnt vmcnt(1) lgkmcnt(2)\n" \
  "v_smfmac_f32_16x16x64_bf16 v[96:99], v[88:91], v[48:55], v117 abid:1\n" \
  "s_waitcnt vmcnt(0) lgkmcnt(0)\n" \
  "v_smfmac_f32_16x16x64_bf16 v[96:99], v[92:95], v[56:63], v117 abid:1\n" \
  "s_nop 8\n" \
  "v_permlane32_swap_b32 v96, v98\n" \
  "v_permlane32_swap_b32 v97, v99\n" \
  "v_add_f32 v96, v96, v98\n" \
  "v_add_f32 v97, v97, v99\n" \
  "v_cvt_pk_bf16_f32 v100, v96, v97\n" \
  "v_cmp_u_f32 vcc, v96, v97\n" \
  "s_nop 0\n" \
  "v_mov_b32_dpp v101, v100 quad_perm:[1,0,3,2] row_mask:0xf bank_mask:0xf\n" \
  "v_perm_b32 v100, v101, v100, v116\n" \
  "s_cbranch_vccnz .Lbad_%=\n" \
  "buffer_store_dword v100, v115, %[rs], %[sm0] offen sc1\n" \
  "buffer_store_dword v100, v115, %[rs], %[sm1] offen sc1\n" \
  "buffer_store_dword v100, v115, %[rs], %[sm2] offen sc1\n" \
  "buffer_store_dword v100, v115, %[rs], %[sm3] offen sc1\n" \
  ".Lend_%=:\n" \
  "s_memtime %[tp]\n" \
  "v_permlane16_swap_b32 v0, v1\n" \
  "v_permlane16_swap_b32 v2, v3\n" \
  "v_permlane16_swap_b32 v4, v5\n" \
  "v_permlane16_swap_b32 v6, v7\n" \
  "v_permlane16_swap_b32 v8, v9\n" \
  "v_permlane16_swap_b32 v10, v11\n" \
  "v_permlane16_swap_b32 v12, v13\n" \
  "v_permlane16_swap_b32 v14, v15\n" \
  "v_permlane16_swap_b32 v16, v17\n" \
  "v_permlane16_swap_b32 v18, v19\n" \
  "v_permlane16_swap_b32 v20, v21\n" \
  "v_permlane16_swap_b32 v22, v23\n" \
  "v_permlane16_swap_b32 v24, v25\n" \
  "v_permlane16_swap_b32 v26, v27\n" \
  "v_permlane16_swap_b32 v28, v29\n" \
  "v_permlane16_swap_b32 v30, v31\n" \
  "s_waitcnt lgkmcnt(0)\n"
#define ST_Y \
  "s_mov_b32 %[keep], m0\n" \
  "ds_read_b128 v[240:243], v250 offset:0\n" \
  "ds_read_b128 v[244:247], v250 offset:1024\n" \
  "v_mfma_f32_16x16x32_bf16 v[192:195], v[0:3], v[208:211], 0\n" \
  "v_mfma_f32_16x16x32_bf16 v[196:199], v[64:67], v[208:211], 0\n" \
  "ds_read_b128 v[208:211], v252 offset:8192\n" \
  "v_mfma_f32_16x16x32_bf16 v[200:203], v[0:3], v[212:215], 0\n" \
  "v_mfma_f32_16x16x32_bf16 v[204:207], v[64:67], v[212:215], 0\n" \
  "ds_read_b128 v[212:215], v252 offset:9216\n" \
  "v_mfma_f32_16x16x32_bf16 v[192:195], v[128:131], v[216:219], v[192:195]\n" \
  "v_mfma_f32_16x16x32_bf16 v[196:199], v[132:135], v[216:219], v[196:199]\n" \
  "ds_read_b128 v[216:219], v252 offset:10240\n" \
  "v_mfma_f32_16x16x32_bf16 v[200:203], v[128:131], v[220:223], v[200:203]\n" \
  "v_mfma_f32_16x16x32_bf16 v[204:207], v[132:135], v[220:223], v[204:207]\n" \
  "ds_read_b128 v[220:223], v252 offset:11264\n" \
  "v_mfma_f32_16x16x32_bf16 v[192:195], v[4:7], v[224:227], v[192:195]\n" \
  "v_mfma_f32_16x16x32_bf16 v[196:199], v[68:71], v[224:227], v[196:199]\n" \
  "ds_read_b128 v[224:227], v252 offset:12288\n" \
  "v_mfma_f32_16x16x32_bf16 v[200:203], v[4:7], v[228:231], v[200:203]\n" \
  "v_mfma_f32_16x16x32_bf16 v[204:207], v[68:71], v[228:231], v[204:207]\n" \
  "ds_read_b128 v[228:231], v252 offset:13312\n" \
  "s_waitcnt lgkmcnt(7)\n" \
  "v_mfma_f32_16x16x32_bf16 v[192:195], v[240:243], v[232:235], v[192:195]\n" \
  "s_waitcnt lgkmcnt(6)\n" \
  "v_mfma_f32_16x16x32_bf16 v[196:199], v[244:247], v[232:235], v[196:199]\n" \
  "ds_read_b128 v[232:235], v252 offset:14336\n" \
  "v_mfma_f32_16x16x32_bf16 v[200:203], v[240:243], v[236:239], v[200:203]\n" \
  "v_mfma_f32_16x16x32_bf16 v[204:207], v[244:247], v[236:239], v[204:207]\n" \
  "ds_read_b128 v[236:239], v252 offset:15360\n" \
  "ds_read_b128 v[240:243], v250 offset:16384\n" \
  "ds_read_b128 v[244:247], v250 offset:17408\n" \
  "s_waitcnt lgkmcnt(9)\n" \
  "v_mfma_f32_16x16x32_bf16 v[192:195], v[8:11], v[208:211], v[192:195]\n" \
  "v_mfma_f32_16x16x32_bf16 v[196:199], v[72:75], v[208:211], v[196:199]\n" \
  "s_waitcnt lgkmcnt(8)\n" \
  "v_mfma_f32_16x16x32_bf16 v[200:203], v[8:11], v[212:215], v[200:203]\n" \
  "v_mfma_f32_16x16x32_bf16 v[204:207], v[72:75], v[212:215], v[204:207]\n" \
  "s_waitcnt vmcnt(2)\n" \
  "s_waitcnt lgkmcnt(0)\n" \
  "s_barrier\n" \
  "ds_read_b128 v[208:211], v252 offset:16384\n" \
  "ds_read_b128 v[212:215], v252 offset:17408\n" \
  "s_add_u32 m0, %[mw], 0\n" \
  "s_xor_b32 %[cnt], %[sc], 512\n" \
  "buffer_load_dwordx4 v248, %[rx], %[cnt] offen lds\n" \
  "s_add_u32 m0, %[mw], 8192\n" \
  "s_xor_b32 %[cnt], %[sc], 640\n" \
  "buffer_load_dwordx4 v248, %[rx], %[cnt] offen lds\n" \
  "v_mfma_f32_16x16x32_bf16 v[192:195], v[136:139], v[216:219], v[192:195]\n" \
  "v_mfma_f32_16x16x32_bf16 v[196:199], v[140:143], v[216:219], v[196:199]\n" \
  "ds_read_b128 v[216:219], v252 offset:18432\n" \
  "v_mfma_f32_16x16x32_bf16 v[200:203], v[136:139], v[220:223], v[200:203]\n" \
  "v_mfma_f32_16x16x32_bf16 v[204:207], v[140:143], v[220:223], v[204:207]\n" \
  "ds_read_b128 v[220:223], v252 offset:19456\n" \
  "v_mfma_f32_16x16x32_bf16 v[192:195], v[12:15], v[224:227], v[192:195]\n" \
  "v_mfma_f32_16x16x32_bf16 v[196:199], v[76:79], v[224:227], v[196:199]\n" \
  "ds_read_b128 v[224:227], v252 offset:20480\n" \
  "v_mfma_f32_16x16x32_bf16 v[200:203], v[12:15], v[228:231], v[200:203]\n" \
  "v_mfma_f32_16x16x32_bf16 v[204:207], v[76:79], v[228:231], v[204:207]\n" \
  "ds_read_b128 v[228:231], v252 offset:21504\n" \
  "v_mfma_f32_16x16x32_bf16 v[192:195], v[240:243], v[232:235], v[192:195]\n" \
  "v_mfma_f32_16x16x32_bf16 v[196:199], v[244:247], v[232:235], v[196:199]\n" \
  "ds_read_b128 v[232:235], v252 offset:22528\n" \
  "v_mfma_f32_16x16x32_bf16 v[200:203], v[240:243], v[236:239], v[200:203]\n" \
  "v_mfma_f32_16x16x32_bf16 v[204:207], v[244:247], v[236:239], v[204:207]\n" \
  "ds_read_b128 v[236:239], v252 offset:23552\n" \
  "ds_read_b128 v[240:243], v250 offset:32768\n" \
  "ds_read_b128 v[244:247], v250 offset:33792\n" \
  "s_waitcnt lgkmcnt(9)\n" \
  "v_mfma_f32_16x16x32_bf16 v[192:195], v[16:19], v[208:211], v[192:195]\n" \
  "v_mfma_f32_16x16x32_bf16 v[196:199], v[80:83], v[208:211], v[196:199]\n" \
  "ds_read_b128 v[208:211], v252 offset:24576\n" \
  "s_waitcnt lgkmcnt(9)\n" \
  "v_mfma_f32_16x16x32_bf16 v[200:203], v[16:19], v[212:215], v[200:203]\n" \
  "v_mfma_f32_16x16x32_bf16 v[204:207], v[80:83], v[212:215], v[204:207]\n" \
  "ds_read_b128 v[212:215], v252 offset:25600\n" \
  "s_waitcnt lgkmcnt(9)\n" \
  "v_mfma_f32_16x16x32_bf16 v[192:195], v[144:147], v[216:219], v[192:195]\n" \
  "v_mfma_f32_16x16x32_bf16 v[196:199], v[148:151], v[216:219], v[196:199]\n" \
  "ds_read_b128 v[216:219], v252 offset:26624\n" \
  "s_waitcnt lgkmcnt(9)\n" \
  "v_mfma_f32_16x16x32_bf16 v[200:203], v[144:147], v[220:223], v[200:203]\n" \
  "v_mfma_f32_16x16x32_bf16 v[204:207], v[148:151], v[220:223], v[204:207]\n" \
  "ds_read_b128 v[220:223], v252 offset:27648\n" \
  "s_waitcnt lgkmcnt(9)\n" \
  "v_mfma_f32_16x16x32_bf16 v[192:195], v[20:23], v[224:227], v[192:195]\n" \
  "v_mfma_f32_16x16x32_bf16 v[196:199], v[84:87], v[224:227], v[196:199]\n" \
  "ds_read_b128 v[224:227], v252 offset:28672\n" \
  "s_waitcnt lgkmcnt(9)\n" \
  "v_mfma_f32_16x16x32_bf16 v[200:203], v[20:23], v[228:231], v[200:203]\n" \
  "v_mfma_f32_16x16x32_bf16 v[204:207], v[84:87], v[228:231], v[204:207]\n" \
  "ds_read_b128 v[228:231], v252 offset:29696\n" \
  "s_waitcnt lgkmcnt(7)\n" \
  "v_mfma_f32_16x16x32_bf16 v[192:195], v[240:243], v[232:235], v[192:195]\n" \
  "s_waitcnt lgkmcnt(6)\n" \
  "v_mfma_f32_16x16x32_bf16 v[196:199], v[244:247], v[232:235], v[196:199]\n" \
  "ds_read_b128 v[232:235], v252 offset:30720\n" \
  "v_mfma_f32_16x16x32_bf16 v[200:203], v[240:243], v[236:239], v[200:203]\n" \
  "v_mfma_f32_16x16x32_bf16 v[204:207], v[244:247], v[236:239], v[204:207]\n" \
  "ds_read_b128 v[236:239], v252 offset:31744\n" \
  "ds_read_b128 v[240:243], v250 offset:49152\n" \
  "ds_read_b128 v[244:247], v250 offset:50176\n" \
  "s_waitcnt lgkmcnt(9)\n" \
  "v_mfma_f32_16x16x32_bf16 v[192:195], v[24:27], v[208:211], v[192:195]\n" \
  "v_mfma_f32_16x16x32_bf16 v[196:199], v[88:91], v[208:211], v[196:199]\n" \
  "s_waitcnt lgkmcnt(8)\n" \
  "v_mfma_f32_16x16x32_bf16 v[200:203], v[24:27], v[212:215], v[200:203]\n" \
  "v_mfma_f32_16x16x32_bf16 v[204:207], v[88:91], v[212:215], v[204:207]\n" \
  "s_waitcnt vmcnt(0)\n" \
  "s_waitcnt lgkmcnt(0)\n" \
  "s_barrier\n" \
  "ds_read_b128 v[208:211], v252 offset:0\n" \
  "ds_read_b128 v[212:215], v252 offset:1024\n" \
  "s_add_u32 m0, %[mw], 16384\n" \
  "s_xor_b32 %[cnt], %[sc], 768\n" \
  "buffer_load_dwordx4 v248, %[rx], %[cnt] offen lds\n" \
  "s_add_u32 m0, %[mw], 24576\n" \
  "s_xor_b32 %[cnt], %[sc], 896\n" \
  "buffer_load_dwordx4 v248, %[rx], %[cnt] offen lds\n" \
  "v_lshl_or_b32 v253, v251, 24, %[fm]\n" \
  "buffer_store_dword v253, v253, %[rs], %[fo] offen sc1\n" \
  "v_mfma_f32_16x16x32_bf16 v[192:195], v[152:155], v[216:219], v[192:195]\n" \
  "v_mfma_f32_16x16x32_bf16 v[196:199], v[156:159], v[216:219], v[196:199]\n" \
  "ds_read_b128 v[216:219], v252 offset:2048\n" \
  "v_mfma_f32_16x16x32_bf16 v[200:203], v[152:155], v[220:223], v[200:203]\n" \
  "v_mfma_f32_16x16x32_bf16 v[204:207], v[156:159], v[220:223], v[204:207]\n" \
  "ds_read_b128 v[220:223], v252 offset:3072\n" \
  "v_mfma_f32_16x16x32_bf16 v[192:195], v[28:31], v[224:227], v[192:195]\n" \
  "v_mfma_f32_16x16x32_bf16 v[196:199], v[92:95], v[224:227], v[196:199]\n" \
  "ds_read_b128 v[224:227], v252 offset:4096\n" \
  "v_mfma_f32_16x16x32_bf16 v[200:203], v[28:31], v[228:231], v[200:203]\n" \
  "v_mfma_f32_16x16x32_bf16 v[204:207], v[92:95], v[228:231], v[204:207]\n" \
  "ds_read_b128 v[228:231], v252 offset:5120\n" \
  "v_mfma_f32_16x16x32_bf16 v[192:195], v[240:243], v[232:235], v[192:195]\n" \
  "v_mfma_f32_16x16x32_bf16 v[196:199], v[244:247], v[232:235], v[196:199]\n" \
  "ds_read_b128 v[232:235], v252 offset:6144\n" \
  "v_mfma_f32_16x16x32_bf16 v[200:203], v[240:243], v[236:239], v[200:203]\n" \
  "v_mfma_f32_16x16x32_bf16 v[204:207], v[244:247], v[236:239], v[204:207]\n" \
  "ds_read_b128 v[236:239], v252 offset:7168\n" \
  "ds_read_b128 v[240:243], v249 offset:0\n" \
  "ds_read_b128 v[244:247], v249 offset:1024\n" \
  "s_waitcnt lgkmcnt(9)\n" \
  "v_mfma_f32_16x16x32_bf16 v[192:195], v[32:35], v[208:211], v[192:195]\n" \
  "v_mfma_f32_16x16x32_bf16 v[196:199], v[96:99], v[208:211], v[196:199]\n" \
  "ds_read_b128 v[208:211], v252 offset:8192\n" \
  "s_waitcnt lgkmcnt(9)\n" \
  "v_mfma_f32_16x16x32_bf16 v[200:203], v[32:35], v[212:215], v[200:203]\n" \
  "v_mfma_f32_16x16x32_bf16 v[204:207], v[96:99], v[212:215], v[204:207]\n" \
  "ds_read_b128 v[212:215], v252 offset:9216\n" \
  "s_waitcnt lgkmcnt(9)\n" \
  "v_mfma_f32_16x16x32_bf16 v[192:195], v[160:163], v[216:219], v[192:195]\n" \
  "v_mfma_f32_16x16x32_bf16 v[196:199], v[164:167], v[216:219], v[196:199]\n" \
  "ds_read_b128 v[216:219], v252 offset:10240\n" \
  "s_waitcnt lgkmcnt(9)\n" \
  "v_mfma_f32_16x16x32_bf16 v[200:203], v[160:163], v[220:223], v[200:203]\n" \
  "v_mfma_f32_16x16x32_bf16 v[204:207], v[164:167], v[220:223], v[204:207]\n" \
  "ds_read_b128 v[220:223], v252 offset:11264\n" \
  "s_waitcnt lgkmcnt(9)\n" \
  "v_mfma_f32_16x16x32_bf16 v[192:195], v[36:39], v[224:227], v[192:195]\n" \
  "v_mfma_f32_16x16x32_bf16 v[196:199], v[100:103], v[224:227], v[196:199]\n" \
  "ds_read_b128 v[224:227], v252 offset:12288\n" \
  "s_waitcnt lgkmcnt(9)\n" \
  "v_mfma_f32_16x16x32_bf16 v[200:203], v[36:39], v[228:231], v[200:203]\n" \
  "v_mfma_f32_16x16x32_bf16 v[204:207], v[100:103], v[228:231], v[204:207]\n" \
  "ds_read_b128 v[228:231], v252 offset:13312\n" \
  "s_waitcnt lgkmcnt(7)\n" \
  "v_mfma_f32_16x16x32_bf16 v[192:195], v[240:243], v[232:235], v[192:195]\n" \
  "s_waitcnt lgkmcnt(6)\n" \
  "v_mfma_f32_16x16x32_bf16 v[196:199], v[244:247], v[232:235], v[196:199]\n" \
  "ds_read_b128 v[232:235], v252 offset:14336\n" \
  "v_mfma_f32_16x16x32_bf16 v[200:203], v[240:243], v[236:239], v[200:203]\n" \
  "v_mfma_f32_16x16x32_bf16 v[204:207], v[244:247], v[236:239], v[204:207]\n" \
  "ds_read_b128 v[236:239], v252 offset:15360\n" \
  "ds_read_b128 v[240:243], v249 offset:16384\n" \
  "ds_read_b128 v[244:247], v249 offset:17408\n" \
  "s_waitcnt lgkmcnt(9)\n" \
  "v_mfma_f32_16x16x32_bf16 v[192:195], v[40:43], v[208:211], v[192:195]\n" \
  "v_mfma_f32_16x16x32_bf16 v[196:199], v[104:107], v[208:211], v[196:199]\n" \
  "s_waitcnt lgkmcnt(8)\n" \
  "v_mfma_f32_16x16x32_bf16 v[200:203], v[40:43], v[212:215], v[200:203]\n" \
  "v_mfma_f32_16x16x32_bf16 v[204:207], v[104:107], v[212:215], v[204:207]\n" \
  "s_waitcnt vmcnt(1)\n" \
  "s_waitcnt lgkmcnt(0)\n" \
  "s_barrier\n" \
  "ds_read_b128 v[208:211], v252 offset:16384\n" \
  "ds_read_b128 v[212:215], v252 offset:17408\n" \
  "s_add_u32 m0, %[mw], 0\n" \
  "s_xor_b32 %[cnt], %[sn], 0\n" \
  "buffer_load_dwordx4 v248, %[rx], %[cnt] offen lds\n" \
  "s_add_u32 m0, %[mw], 8192\n" \
  "s_xor_b32 %[cnt], %[sn], 128\n" \
  "buffer_load_dwordx4 v248, %[rx], %[cnt] offen lds\n" \
  "v_lshl_or_b32 v253, v251, 3, %[tm]\n" \
  "buffer_load_dword v253, v253, %[rx], %[tc] offen\n" \
  "v_mfma_f32_16x16x32_bf16 v[192:195], v[168:171], v[216:219], v[192:195]\n" \
  "v_mfma_f32_16x16x32_bf16 v[196:199], v[172:175], v[216:219], v[196:199]\n" \
  "ds_read_b128 v[216:219], v252 offset:18432\n" \
  "v_mfma_f32_16x16x32_bf16 v[200:203], v[168:171], v[220:223], v[200:203]\n" \
  "v_mfma_f32_16x16x32_bf16 v[204:207], v[172:175], v[220:223], v[204:207]\n" \
  "ds_read_b128 v[220:223], v252 offset:19456\n" \
  "v_mfma_f32_16x16x32_bf16 v[192:195], v[44:47], v[224:227], v[192:195]\n" \
  "v_mfma_f32_16x16x32_bf16 v[196:199], v[108:111], v[224:227], v[196:199]\n" \
  "ds_read_b128 v[224:227], v252 offset:20480\n" \
  "v_mfma_f32_16x16x32_bf16 v[200:203], v[44:47], v[228:231], v[200:203]\n" \
  "v_mfma_f32_16x16x32_bf16 v[204:207], v[108:111], v[228:231], v[204:207]\n" \
  "ds_read_b128 v[228:231], v252 offset:21504\n" \
  "v_mfma_f32_16x16x32_bf16 v[192:195], v[240:243], v[232:235], v[192:195]\n" \
  "v_mfma_f32_16x16x32_bf16 v[196:199], v[244:247], v[232:235], v[196:199]\n" \
  "ds_read_b128 v[232:235], v252 offset:22528\n" \
  "v_mfma_f32_16x16x32_bf16 v[200:203], v[240:243], v[236:239], v[200:203]\n" \
  "v_mfma_f32_16x16x32_bf16 v[204:207], v[244:247], v[236:239], v[204:207]\n" \
  "ds_read_b128 v[236:239], v252 offset:23552\n" \
  "ds_read_b128 v[240:243], v249 offset:32768\n" \
  "ds_read_b128 v[244:247], v249 offset:33792\n" \
  "s_waitcnt lgkmcnt(9)\n" \
  "v_mfma_f32_16x16x32_bf16 v[192:195], v[48:51], v[208:211], v[192:195]\n" \
  "v_mfma_f32_16x16x32_bf16 v[196:199], v[112:115], v[208:211], v[196:199]\n" \
  "ds_read_b128 v[208:211], v252 offset:24576\n" \
  "s_waitcnt lgkmcnt(9)\n" \
  "v_mfma_f32_16x16x32_bf16 v[200:203], v[48:51], v[212:215], v[200:203]\n" \
  "v_mfma_f32_16x16x32_bf16 v[204:207], v[112:115], v[212:215], v[204:207]\n" \
  "ds_read_b128 v[212:215], v252 offset:25600\n" \
  "s_waitcnt lgkmcnt(9)\n" \
  "v_mfma_f32_16x16x32_bf16 v[192:195], v[176:179], v[216:219], v[192:195]\n" \
  "v_mfma_f32_16x16x32_bf16 v[196:199], v[180:183], v[216:219], v[196:199]\n" \
  "ds_read_b128 v[216:219], v252 offset:26624\n" \
  "s_waitcnt lgkmcnt(9)\n" \
  "v_mfma_f32_16x16x32_bf16 v[200:203], v[176:179], v[220:223], v[200:203]\n" \
  "v_mfma_f32_16x16x32_bf16 v[204:207], v[180:183], v[220:223], v[204:207]\n" \
  "ds_read_b128 v[220:223], v252 offset:27648\n" \
  "s_waitcnt lgkmcnt(9)\n" \
  "v_mfma_f32_16x16x32_bf16 v[192:195], v[52:55], v[224:227], v[192:195]\n" \
  "v_mfma_f32_16x16x32_bf16 v[196:199], v[116:119], v[224:227], v[196:199]\n" \
  "ds_read_b128 v[224:227], v252 offset:28672\n" \
  "s_waitcnt lgkmcnt(9)\n" \
  "v_mfma_f32_16x16x32_bf16 v[200:203], v[52:55], v[228:231], v[200:203]\n" \
  "v_mfma_f32_16x16x32_bf16 v[204:207], v[116:119], v[228:231], v[204:207]\n" \
  "ds_read_b128 v[228:231], v252 offset:29696\n" \
  "s_waitcnt lgkmcnt(7)\n" \
  "v_mfma_f32_16x16x32_bf16 v[192:195], v[240:243], v[232:235], v[192:195]\n" \
  "s_waitcnt lgkmcnt(6)\n" \
  "v_mfma_f32_16x16x32_bf16 v[196:199], v[244:247], v[232:235], v[196:199]\n" \
  "ds_read_b128 v[232:235], v252 offset:30720\n" \
  "v_mfma_f32_16x16x32_bf16 v[200:203], v[240:243], v[236:239], v[200:203]\n" \
  "v_mfma_f32_16x16x32_bf16 v[204:207], v[244:247], v[236:239], v[204:207]\n" \
  "ds_read_b128 v[236:239], v252 offset:31744\n" \
  "ds_read_b128 v[240:243], v249 offset:49152\n" \
  "ds_read_b128 v[244:247], v249 offset:50176\n" \
  "s_waitcnt lgkmcnt(9)\n" \
  "v_mfma_f32_16x16x32_bf16 v[192:195], v[56:59], v[208:211], v[192:195]\n" \
  "v_mfma_f32_16x16x32_bf16 v[196:199], v[120:123], v[208:211], v[196:199]\n" \
  "s_waitcnt lgkmcnt(8)\n" \
  "v_mfma_f32_16x16x32_bf16 v[200:203], v[56:59], v[212:215], v[200:203]\n" \
  "v_mfma_f32_16x16x32_bf16 v[204:207], v[120:123], v[212:215], v[204:207]\n" \
  "s_waitcnt vmcnt(1)\n" \
  "s_waitcnt lgkmcnt(0)\n" \
  "s_barrier\n" \
  "ds_read_b128 v[208:211], v252 offset:0\n" \
  "ds_read_b128 v[212:215], v252 offset:1024\n" \
  "s_add_u32 m0, %[mw], 16384\n" \
  "s_xor_b32 %[cnt], %[sn], 256\n" \
  "buffer_load_dwordx4 v248, %[rx], %[cnt] offen lds\n" \
  "s_add_u32 m0, %[mw], 24576\n" \
  "s_xor_b32 %[cnt], %[sn], 384\n" \
  "buffer_load_dwordx4 v248, %[rx], %[cnt] offen lds\n" \
  "v_mfma_f32_16x16x32_bf16 v[192:195], v[184:187], v[216:219], v[192:195]\n" \
  "v_mfma_f32_16x16x32_bf16 v[196:199], v[188:191], v[216:219], v[196:199]\n" \
  "ds_read_b128 v[216:219], v252 offset:2048\n" \
  "v_mfma_f32_16x16x32_bf16 v[200:203], v[184:187], v[220:223], v[200:203]\n" \
  "v_mfma_f32_16x16x32_bf16 v[204:207], v[188:191], v[220:223], v[204:207]\n" \
  "ds_read_b128 v[220:223], v252 offset:3072\n" \
  "v_mfma_f32_16x16x32_bf16 v[192:195], v[60:63], v[224:227], v[192:195]\n" \
  "v_mfma_f32_16x16x32_bf16 v[196:199], v[124:127], v[224:227], v[196:199]\n" \
  "ds_read_b128 v[224:227], v252 offset:4096\n" \
  "v_mfma_f32_16x16x32_bf16 v[200:203], v[60:63], v[228:231], v[200:203]\n" \
  "v_mfma_f32_16x16x32_bf16 v[204:207], v[124:127], v[228:231], v[204:207]\n" \
  "ds_read_b128 v[228:231], v252 offset:5120\n" \
  "v_mfma_f32_16x16x32_bf16 v[192:195], v[240:243], v[232:235], v[192:195]\n" \
  "v_mfma_f32_16x16x32_bf16 v[196:199], v[244:247], v[232:235], v[196:199]\n" \
  "ds_read_b128 v[232:235], v252 offset:6144\n" \
  "v_mfma_f32_16x16x32_bf16 v[200:203], v[240:243], v[236:239], v[200:203]\n" \
  "v_mfma_f32_16x16x32_bf16 v[204:207], v[244:247], v[236:239], v[204:207]\n" \
  "ds_read_b128 v[236:239], v252 offset:7168\n" \
  "s_cmp_eq_u32 %[cs], 0\n" \
  "s_cbranch_scc1 .Lq_%=\n" \
  "v_mul_f32 v192, %[cs], v192\n" \
  "v_mul_f32 v193, %[cs], v193\n" \
  "v_mul_f32 v194, %[cs], v194\n" \
  "v_mul_f32 v195, %[cs], v195\n" \
  "v_mul_f32 v196, %[cs], v196\n" \
  "v_mul_f32 v197, %[cs], v197\n" \
  "v_mul_f32 v198, %[cs], v198\n" \
  "v_mul_f32 v199, %[cs], v199\n" \
  "v_cvt_pk_bf16_f32 v192, v192, v193\n" \
  "v_cvt_pk_bf16_f32 v193, v196, v197\n" \
  "v_cvt_pk_bf16_f32 v194, v194, v195\n" \
  "v_cvt_pk_bf16_f32 v195, v198, v199\n" \
  "buffer_store_dwordx4 v[192:195], v251, %[rs], %[so] offen sc1\n" \
  "v_mul_f32 v200, %[cs], v200\n" \
  "v_mul_f32 v201, %[cs], v201\n" \
  "v_mul_f32 v202, %[cs], v202\n" \
  "v_mul_f32 v203, %[cs], v203\n" \
  "v_mul_f32 v204, %[cs], v204\n" \
  "v_mul_f32 v205, %[cs], v205\n" \
  "v_mul_f32 v206, %[cs], v206\n" \
  "v_mul_f32 v207, %[cs], v207\n" \
  "v_cvt_pk_bf16_f32 v200, v200, v201\n" \
  "v_cvt_pk_bf16_f32 v201, v204, v205\n" \
  "v_cvt_pk_bf16_f32 v202, v202, v203\n" \
  "v_cvt_pk_bf16_f32 v203, v206, v207\n" \
  "s_add_u32 %[cnt], %[so], %c[rt]\n" \
  "buffer_store_dwordx4 v[200:203], v251, %[rs], %[cnt] offen sc1\n" \
  "s_branch .Lqe_%=\n" \
  ".Lq_%=:\n" \
  "v_cvt_pk_bf16_f32 v192, v192, v193\n" \
  "v_cvt_pk_bf16_f32 v193, v196, v197\n" \
  "v_cvt_pk_bf16_f32 v194, v194, v195\n" \
  "v_cvt_pk_bf16_f32 v195, v198, v199\n" \
  "buffer_store_dwordx4 v[192:195], v251, %[rs], %[so] offen sc1\n" \
  "v_cvt_pk_bf16_f32 v200, v200, v201\n" \
  "v_cvt_pk_bf16_f32 v201, v204, v205\n" \
  "v_cvt_pk_bf16_f32 v202, v202, v203\n" \
  "v_cvt_pk_bf16_f32 v203, v206, v207\n" \
  "s_add_u32 %[cnt], %[so], %c[rt]\n" \
  "buffer_store_dwordx4 v[200:203], v251, %[rs], %[cnt] offen sc1\n" \
  ".Lqe_%=:\n" \
  "s_mov_b32 m0, %[keep]\n" \
  "s_waitcnt lgkmcnt(0)\n"
#define ST_XA \
  "s_mov_b32 %[keep], m0\n" \
  "ds_read_b128 v[240:243], v250 offset:0\n" \
  "ds_read_b128 v[244:247], v250 offset:1024\n" \
  "v_mfma_f32_16x16x32_bf16 v[192:195], v[0:3], v[208:211], 0\n" \
  "v_mfma_f32_16x16x32_bf16 v[196:199], v[64:67], v[208:211], 0\n" \
  "ds_read_b128 v[208:211], v252 offset:8192\n" \
  "v_mfma_f32_16x16x32_bf16 v[200:203], v[0:3], v[212:215], 0\n" \
  "v_mfma_f32_16x16x32_bf16 v[204:207], v[64:67], v[212:215], 0\n" \
  "ds_read_b128 v[212:215], v252 offset:9216\n" \
  "v_mfma_f32_16x16x32_bf16 v[192:195], v[128:131], v[216:219], v[192:195]\n" \
  "v_mfma_f32_16x16x32_bf16 v[196:199], v[132:135], v[216:219], v[196:199]\n" \
  "ds_read_b128 v[216:219], v252 offset:10240\n" \
  "v_mfma_f32_16x16x32_bf16 v[200:203], v[128:131], v[220:223], v[200:203]\n" \
  "v_mfma_f32_16x16x32_bf16 v[204:207], v[132:135], v[220:223], v[204:207]\n" \
  "ds_read_b128 v[220:223], v252 offset:11264\n" \
  "v_mfma_f32_16x16x32_bf16 v[192:195], v[4:7], v[224:227], v[192:195]\n" \
  "v_mfma_f32_16x16x32_bf16 v[196:199], v[68:71], v[224:227], v[196:199]\n" \
  "v_mfma_f32_16x16x32_bf16 v[200:203], v[4:7], v[228:231], v[200:203]\n" \
  "v_mfma_f32_16x16x32_bf16 v[204:207], v[68:71], v[228:231], v[204:207]\n" \
  "s_waitcnt vmcnt(0)\n" \
  "s_waitcnt lgkmcnt(0)\n" \
  "s_barrier\n" \
  "ds_read_b128 v[224:227], v252 offset:12288\n" \
  "ds_read_b128 v[228:231], v252 offset:13312\n" \
  "s_add_u32 m0, %[mw], 0\n" \
  "s_xor_b32 %[cnt], %[sc], 384\n" \
  "buffer_load_dwordx4 v248, %[rx], %[cnt] offen lds\n" \
  "s_add_u32 m0, %[mw], 4096\n" \
  "s_xor_b32 %[cnt], %[sc], 448\n" \
  "buffer_load_dwordx4 v248, %[rx], %[cnt] offen lds\n" \
  "v_mfma_f32_16x16x32_bf16 v[192:195], v[240:243], v[232:235], v[192:195]\n" \
  "v_mfma_f32_16x16x32_bf16 v[196:199], v[244:247], v[232:235], v[196:199]\n" \
  "ds_read_b128 v[232:235], v252 offset:14336\n" \
  "v_mfma_f32_16x16x32_bf16 v[200:203], v[240:243], v[236:239], v[200:203]\n" \
  "v_mfma_f32_16x16x32_bf16 v[204:207], v[244:247], v[236:239], v[204:207]\n" \
  "ds_read_b128 v[236:239], v252 offset:15360\n" \
  "ds_read_b128 v[240:243], v250 offset:16384\n" \
  "ds_read_b128 v[244:247], v250 offset:17408\n" \
  "v_mfma_f32_16x16x32_bf16 v[192:195], v[8:11], v[208:211], v[192:195]\n" \
  "v_mfma_f32_16x16x32_bf16 v[196:199], v[72:75], v[208:211], v[196:199]\n" \
  "ds_read_b128 v[208:211], v252 offset:16384\n" \
  "v_mfma_f32_16x16x32_bf16 v[200:203], v[8:11], v[212:215], v[200:203]\n" \
  "v_mfma_f32_16x16x32_bf16 v[204:207], v[72:75], v[212:215], v[204:207]\n" \
  "ds_read_b128 v[212:215], v252 offset:17408\n" \
  "v_mfma_f32_16x16x32_bf16 v[192:195], v[136:139], v[216:219], v[192:195]\n" \
  "v_mfma_f32_16x16x32_bf16 v[196:199], v[140:143], v[216:219], v[196:199]\n" \
  "ds_read_b128 v[216:219], v252 offset:18432\n" \
  "v_mfma_f32_16x16x32_bf16 v[200:203], v[136:139], v[220:223], v[200:203]\n" \
  "v_mfma_f32_16x16x32_bf16 v[204:207], v[140:143], v[220:223], v[204:207]\n" \
  "ds_read_b128 v[220:223], v252 offset:19456\n" \
  "s_waitcnt lgkmcnt(9)\n" \
  "v_mfma_f32_16x16x32_bf16 v[192:195], v[12:15], v[224:227], v[192:195]\n" \
  "v_mfma_f32_16x16x32_bf16 v[196:199], v[76:79], v[224:227], v[196:199]\n" \
  "ds_read_b128 v[224:227], v252 offset:20480\n" \
  "s_waitcnt lgkmcnt(9)\n" \
  "v_mfma_f32_16x16x32_bf16 v[200:203], v[12:15], v[228:231], v[200:203]\n" \
  "v_mfma_f32_16x16x32_bf16 v[204:207], v[76:79], v[228:231], v[204:207]\n" \
  "ds_read_b128 v[228:231], v252 offset:21504\n" \
  "s_waitcnt lgkmcnt(7)\n" \
  "v_mfma_f32_16x16x32_bf16 v[192:195], v[240:243], v[232:235], v[192:195]\n" \
  "s_waitcnt lgkmcnt(6)\n" \
  "v_mfma_f32_16x16x32_bf16 v[196:199], v[244:247], v[232:235], v[196:199]\n" \
  "ds_read_b128 v[232:235], v252 offset:22528\n" \
  "v_mfma_f32_16x16x32_bf16 v[200:203], v[240:243], v[236:239], v[200:203]\n" \
  "v_mfma_f32_16x16x32_bf16 v[204:207], v[244:247], v[236:239], v[204:207]\n" \
  "ds_read_b128 v[236:239], v252 offset:23552\n" \
  "ds_read_b128 v[240:243], v250 offset:32768\n" \
  "ds_read_b128 v[244:247], v250 offset:33792\n" \
  "s_waitcnt lgkmcnt(9)\n" \
  "v_mfma_f32_16x16x32_bf16 v[192:195], v[16:19], v[208:211], v[192:195]\n" \
  "v_mfma_f32_16x16x32_bf16 v[196:199], v[80:83], v[208:211], v[196:199]\n" \
  "s_waitcnt lgkmcnt(8)\n" \
  "v_mfma_f32_16x16x32_bf16 v[200:203], v[16:19], v[212:215], v[200:203]\n" \
  "v_mfma_f32_16x16x32_bf16 v[204:207], v[80:83], v[212:215], v[204:207]\n" \
  "s_waitcnt vmcnt(0)\n" \
  "s_waitcnt lgkmcnt(0)\n" \
  "s_barrier\n" \
  "ds_read_b128 v[208:211], v252 offset:0\n" \
  "ds_read_b128 v[212:215], v252 offset:1024\n" \
  "s_add_u32 m0, %[mw], 12288\n" \
  "s_xor_b32 %[cnt], %[sc], 512\n" \
  "buffer_load_dwordx4 v248, %[rx], %[cnt] offen lds\n" \
  "s_add_u32 m0, %[mw], 16384\n" \
  "s_xor_b32 %[cnt], %[sc], 576\n" \
  "buffer_load_dwordx4 v248, %[rx], %[cnt] offen lds\n" \
  "s_add_u32 m0, %[mw], 20480\n" \
  "s_xor_b32 %[cnt], %[sc], 640\n" \
  "buffer_load_dwordx4 v248, %[rx], %[cnt] offen lds\n" \
  "v_lshl_or_b32 v253, v251, 24, %[fm]\n" \
  "buffer_store_dword v253, v253, %[rs], %[fo] offen sc1\n" \
  "v_mfma_f32_16x16x32_bf16 v[192:195], v[144:147], v[216:219], v[192:195]\n" \
  "v_mfma_f32_16x16x32_bf16 v[196:199], v[148:151], v[216:219], v[196:199]\n" \
  "ds_read_b128 v[216:219], v252 offset:2048\n" \
  "v_mfma_f32_16x16x32_bf16 v[200:203], v[144:147], v[220:223], v[200:203]\n" \
  "v_mfma_f32_16x16x32_bf16 v[204:207], v[148:151], v[220:223], v[204:207]\n" \
  "ds_read_b128 v[220:223], v252 offset:3072\n" \
  "v_mfma_f32_16x16x32_bf16 v[192:195], v[20:23], v[224:227], v[192:195]\n" \
  "v_mfma_f32_16x16x32_bf16 v[196:199], v[84:87], v[224:227], v[196:199]\n" \
  "ds_read_b128 v[224:227], v252 offset:4096\n" \
  "v_mfma_f32_16x16x32_bf16 v[200:203], v[20:23], v[228:231], v[200:203]\n" \
  "v_mfma_f32_16x16x32_bf16 v[204:207], v[84:87], v[228:231], v[204:207]\n" \
  "ds_read_b128 v[228:231], v252 offset:5120\n" \
  "v_mfma_f32_16x16x32_bf16 v[192:195], v[240:243], v[232:235], v[192:195]\n" \
  "v_mfma_f32_16x16x32_bf16 v[196:199], v[244:247], v[232:235], v[196:199]\n" \
  "ds_read_b128 v[232:235], v252 offset:6144\n" \
  "v_mfma_f32_16x16x32_bf16 v[200:203], v[240:243], v[236:239], v[200:203]\n" \
  "v_mfma_f32_16x16x32_bf16 v[204:207], v[244:247], v[236:239], v[204:207]\n" \
  "ds_read_b128 v[236:239], v252 offset:7168\n" \
  "ds_read_b128 v[240:243], v250 offset:49152\n" \
  "ds_read_b128 v[244:247], v250 offset:50176\n" \
  "s_waitcnt lgkmcnt(9)\n" \
  "v_mfma_f32_16x16x32_bf16 v[192:195], v[24:27], v[208:211], v[192:195]\n" \
  "v_mfma_f32_16x16x32_bf16 v[196:199], v[88:91], v[208:211], v[196:199]\n" \
  "s_waitcnt lgkmcnt(8)\n" \
  "v_mfma_f32_16x16x32_bf16 v[200:203], v[24:27], v[212:215], v[200:203]\n" \
  "v_mfma_f32_16x16x32_bf16 v[204:207], v[88:91], v[212:215], v[204:207]\n" \
  "s_waitcnt vmcnt(1)\n" \
  "s_waitcnt lgkmcnt(0)\n" \
  "s_barrier\n" \
  "ds_read_b128 v[208:211], v252 offset:12288\n" \
  "ds_read_b128 v[212:215], v252 offset:13312\n" \
  "s_add_u32 m0, %[mw], 0\n" \
  "s_xor_b32 %[cnt], %[sc], 704\n" \
  "buffer_load_dwordx4 v248, %[rx], %[cnt] offen lds\n" \
  "s_add_u32 m0, %[mw], 4096\n" \
  "s_xor_b32 %[cnt], %[sc], 768\n" \
  "buffer_load_dwordx4 v248, %[rx], %[cnt] offen lds\n" \
  "s_add_u32 m0, %[mw], 8192\n" \
  "s_xor_b32 %[cnt], %[sc], 832\n" \
  "buffer_load_dwordx4 v248, %[rx], %[cnt] offen lds\n" \
  "v_lshl_or_b32 v253, v251, 3, %[tm]\n" \
  "buffer_load_dword v253, v253, %[rx], %[tc] offen\n" \
  "v_mfma_f32_16x16x32_bf16 v[192:195], v[152:155], v[216:219], v[192:195]\n" \
  "v_mfma_f32_16x16x32_bf16 v[196:199], v[156:159], v[216:219], v[196:199]\n" \
  "ds_read_b128 v[216:219], v252 offset:14336\n" \
  "v_mfma_f32_16x16x32_bf16 v[200:203], v[152:155], v[220:223], v[200:203]\n" \
  "v_mfma_f32_16x16x32_bf16 v[204:207], v[156:159], v[220:223], v[204:207]\n" \
  "ds_read_b128 v[220:223], v252 offset:15360\n" \
  "v_mfma_f32_16x16x32_bf16 v[192:195], v[28:31], v[224:227], v[192:195]\n" \
  "v_mfma_f32_16x16x32_bf16 v[196:199], v[92:95], v[224:227], v[196:199]\n" \
  "ds_read_b128 v[224:227], v252 offset:16384\n" \
  "v_mfma_f32_16x16x32_bf16 v[200:203], v[28:31], v[228:231], v[200:203]\n" \
  "v_mfma_f32_16x16x32_bf16 v[204:207], v[92:95], v[228:231], v[204:207]\n" \
  "ds_read_b128 v[228:231], v252 offset:17408\n" \
  "v_mfma_f32_16x16x32_bf16 v[192:195], v[240:243], v[232:235], v[192:195]\n" \
  "v_mfma_f32_16x16x32_bf16 v[196:199], v[244:247], v[232:235], v[196:199]\n" \
  "ds_read_b128 v[232:235], v252 offset:18432\n" \
  "v_mfma_f32_16x16x32_bf16 v[200:203], v[240:243], v[236:239], v[200:203]\n" \
  "v_mfma_f32_16x16x32_bf16 v[204:207], v[244:247], v[236:239], v[204:207]\n" \
  "ds_read_b128 v[236:239], v252 offset:19456\n" \
  "ds_read_b128 v[240:243], v249 offset:0\n" \
  "ds_read_b128 v[244:247], v249 offset:1024\n" \
  "s_waitcnt lgkmcnt(9)\n" \
  "v_mfma_f32_16x16x32_bf16 v[192:195], v[32:35], v[208:211], v[192:195]\n" \
  "v_mfma_f32_16x16x32_bf16 v[196:199], v[96:99], v[208:211], v[196:199]\n" \
  "ds_read_b128 v[208:211], v252 offset:20480\n" \
  "s_waitcnt lgkmcnt(9)\n" \
  "v_mfma_f32_16x16x32_bf16 v[200:203], v[32:35], v[212:215], v[200:203]\n" \
  "v_mfma_f32_16x16x32_bf16 v[204:207], v[96:99], v[212:215], v[204:207]\n" \
  "ds_read_b128 v[212:215], v252 offset:21504\n" \
  "s_waitcnt lgkmcnt(9)\n" \
  "v_mfma_f32_16x16x32_bf16 v[192:195], v[160:163], v[216:219], v[192:195]\n" \
  "v_mfma_f32_16x16x32_bf16 v[196:199], v[164:167], v[216:219], v[196:199]\n" \
  "ds_read_b128 v[216:219], v252 offset:22528\n" \
  "s_waitcnt lgkmcnt(9)\n" \
  "v_mfma_f32_16x16x32_bf16 v[200:203], v[160:163], v[220:223], v[200:203]\n" \
  "v_mfma_f32_16x16x32_bf16 v[204:207], v[164:167], v[220:223], v[204:207]\n" \
  "ds_read_b128 v[220:223], v252 offset:23552\n" \
  "s_waitcnt lgkmcnt(9)\n" \
  "v_mfma_f32_16x16x32_bf16 v[192:195], v[36:39], v[224:227], v[192:195]\n" \
  "v_mfma_f32_16x16x32_bf16 v[196:199], v[100:103], v[224:227], v[196:199]\n" \
  "s_waitcnt lgkmcnt(8)\n" \
  "v_mfma_f32_16x16x32_bf16 v[200:203], v[36:39], v[228:231], v[200:203]\n" \
  "v_mfma_f32_16x16x32_bf16 v[204:207], v[100:103], v[228:231], v[204:207]\n" \
  "s_waitcnt vmcnt(1)\n" \
  "s_waitcnt lgkmcnt(0)\n" \
  "s_barrier\n" \
  "ds_read_b128 v[224:227], v252 offset:0\n" \
  "ds_read_b128 v[228:231], v252 offset:1024\n" \
  "s_add_u32 m0, %[mw], 12288\n" \
  "s_xor_b32 %[cnt], %[sc], 896\n" \
  "buffer_load_dwordx4 v248, %[rx], %[cnt] offen lds\n" \
  "s_add_u32 m0, %[mw], 16384\n" \
  "s_xor_b32 %[cnt], %[sc], 960\n" \
  "buffer_load_dwordx4 v248, %[rx], %[cnt] offen lds\n" \
  "v_mfma_f32_16x16x32_bf16 v[192:195], v[240:243], v[232:235], v[192:195]\n" \
  "v_mfma_f32_16x16x32_bf16 v[196:199], v[244:247], v[232:235], v[196:199]\n" \
  "ds_read_b128 v[232:235], v252 offset:2048\n" \
  "v_mfma_f32_16x16x32_bf16 v[200:203], v[240:243], v[236:239], v[200:203]\n" \
  "v_mfma_f32_16x16x32_bf16 v[204:207], v[244:247], v[236:239], v[204:207]\n" \
  "ds_read_b128 v[236:239], v252 offset:3072\n" \
  "ds_read_b128 v[240:243], v249 offset:16384\n" \
  "ds_read_b128 v[244:247], v249 offset:17408\n" \
  "v_mfma_f32_16x16x32_bf16 v[192:195], v[40:43], v[208:211], v[192:195]\n" \
  "v_mfma_f32_16x16x32_bf16 v[196:199], v[104:107], v[208:211], v[196:199]\n" \
  "ds_read_b128 v[208:211], v252 offset:4096\n" \
  "v_mfma_f32_16x16x32_bf16 v[200:203], v[40:43], v[212:215], v[200:203]\n" \
  "v_mfma_f32_16x16x32_bf16 v[204:207], v[104:107], v[212:215], v[204:207]\n" \
  "ds_read_b128 v[212:215], v252 offset:5120\n" \
  "v_mfma_f32_16x16x32_bf16 v[192:195], v[168:171], v[216:219], v[192:195]\n" \
  "v_mfma_f32_16x16x32_bf16 v[196:199], v[172:175], v[216:219], v[196:199]\n" \
  "ds_read_b128 v[216:219], v252 offset:6144\n" \
  "v_mfma_f32_16x16x32_bf16 v[200:203], v[168:171], v[220:223], v[200:203]\n" \
  "v_mfma_f32_16x16x32_bf16 v[204:207], v[172:175], v[220:223], v[204:207]\n" \
  "ds_read_b128 v[220:223], v252 offset:7168\n" \
  "s_waitcnt lgkmcnt(9)\n" \
  "v_mfma_f32_16x16x32_bf16 v[192:195], v[44:47], v[224:227], v[192:195]\n" \
  "v_mfma_f32_16x16x32_bf16 v[196:199], v[108:111], v[224:227], v[196:199]\n" \
  "ds_read_b128 v[224:227], v252 offset:8192\n" \
  "s_waitcnt lgkmcnt(9)\n" \
  "v_mfma_f32_16x16x32_bf16 v[200:203], v[44:47], v[228:231], v[200:203]\n" \
  "v_mfma_f32_16x16x32_bf16 v[204:207], v[108:111], v[228:231], v[204:207]\n" \
  "ds_read_b128 v[228:231], v252 offset:9216\n" \
  "s_waitcnt lgkmcnt(7)\n" \
  "v_mfma_f32_16x16x32_bf16 v[192:195], v[240:243], v[232:235], v[192:195]\n" \
  "s_waitcnt lgkmcnt(6)\n" \
  "v_mfma_f32_16x16x32_bf16 v[196:199], v[244:247], v[232:235], v[196:199]\n" \
  "ds_read_b128 v[232:235], v252 offset:10240\n" \
  "v_mfma_f32_16x16x32_bf16 v[200:203], v[240:243], v[236:239], v[200:203]\n" \
  "v_mfma_f32_16x16x32_bf16 v[204:207], v[244:247], v[236:239], v[204:207]\n" \
  "ds_read_b128 v[236:239], v252 offset:11264\n" \
  "ds_read_b128 v[240:243], v249 offset:32768\n" \
  "ds_read_b128 v[244:247], v249 offset:33792\n" \
  "s_waitcnt lgkmcnt(9)\n" \
  "v_mfma_f32_16x16x32_bf16 v[192:195], v[48:51], v[208:211], v[192:195]\n" \
  "v_mfma_f32_16x16x32_bf16 v[196:199], v[112:115], v[208:211], v[196:199]\n" \
  "s_waitcnt lgkmcnt(8)\n" \
  "v_mfma_f32_16x16x32_bf16 v[200:203], v[48:51], v[212:215], v[200:203]\n" \
  "v_mfma_f32_16x16x32_bf16 v[204:207], v[112:115], v[212:215], v[204:207]\n" \
  "s_waitcnt vmcnt(0)\n" \
  "s_waitcnt lgkmcnt(0)\n" \
  "s_barrier\n" \
  "ds_read_b128 v[208:211], v252 offset:12288\n" \
  "ds_read_b128 v[212:215], v252 offset:13312\n" \
  "s_add_u32 m0, %[mw], 0\n" \
  "s_xor_b32 %[cnt], %[sn], 0\n" \
  "buffer_load_dwordx4 v248, %[rx], %[cnt] offen lds\n" \
  "s_add_u32 m0, %[mw], 4096\n" \
  "s_xor_b32 %[cnt], %[sn], 64\n" \
  "buffer_load_dwordx4 v248, %[rx], %[cnt] offen lds\n" \
  "s_add_u32 m0, %[mw], 8192\n" \
  "s_xor_b32 %[cnt], %[sn], 128\n" \
  "buffer_load_dwordx4 v248, %[rx], %[cnt] offen lds\n" \
  "v_mfma_f32_16x16x32_bf16 v[192:195], v[176:179], v[216:219], v[192:195]\n" \
  "v_mfma_f32_16x16x32_bf16 v[196:199], v[180:183], v[216:219], v[196:199]\n" \
  "ds_read_b128 v[216:219], v252 offset:14336\n" \
  "v_mfma_f32_16x16x32_bf16 v[200:203], v[176:179], v[220:223], v[200:203]\n" \
  "v_mfma_f32_16x16x32_bf16 v[204:207], v[180:183], v[220:223], v[204:207]\n" \
  "ds_read_b128 v[220:223], v252 offset:15360\n" \
  "v_mfma_f32_16x16x32_bf16 v[192:195], v[52:55], v[224:227], v[192:195]\n" \
  "v_mfma_f32_16x16x32_bf16 v[196:199], v[116:119], v[224:227], v[196:199]\n" \
  "ds_read_b128 v[224:227], v252 offset:16384\n" \
  "v_mfma_f32_16x16x32_bf16 v[200:203], v[52:55], v[228:231], v[200:203]\n" \
  "v_mfma_f32_16x16x32_bf16 v[204:207], v[116:119], v[228:231], v[204:207]\n" \
  "ds_read_b128 v[228:231], v252 offset:17408\n" \
  "v_mfma_f32_16x16x32_bf16 v[192:195], v[240:243], v[232:235], v[192:195]\n" \
  "v_mfma_f32_16x16x32_bf16 v[196:199], v[244:247], v[232:235], v[196:199]\n" \
  "ds_read_b128 v[232:235], v252 offset:18432\n" \
  "v_mfma_f32_16x16x32_bf16 v[200:203], v[240:243], v[236:239], v[200:203]\n" \
  "v_mfma_f32_16x16x32_bf16 v[204:207], v[244:247], v[236:239], v[204:207]\n" \
  "ds_read_b128 v[236:239], v252 offset:19456\n" \
  "ds_read_b128 v[240:243], v249 offset:49152\n" \
  "ds_read_b128 v[244:247], v249 offset:50176\n" \
  "s_waitcnt lgkmcnt(9)\n" \
  "v_mfma_f32_16x16x32_bf16 v[192:195], v[56:59], v[208:211], v[192:195]\n" \
  "v_mfma_f32_16x16x32_bf16 v[196:199], v[120:123], v[208:211], v[196:199]\n" \
  "s_waitcnt lgkmcnt(8)\n" \
  "v_mfma_f32_16x16x32_bf16 v[200:203], v[56:59], v[212:215], v[200:203]\n" \
  "v_mfma_f32_16x16x32_bf16 v[204:207], v[120:123], v[212:215], v[204:207]\n" \
  "s_waitcnt vmcnt(0)\n" \
  "s_waitcnt lgkmcnt(0)\n" \
  "s_barrier\n" \
  "ds_read_b128 v[208:211], v252 offset:0\n" \
  "ds_read_b128 v[212:215], v252 offset:1024\n" \
  "s_add_u32 m0, %[mw], 12288\n" \
  "s_xor_b32 %[cnt], %[sn], 192\n" \
  "buffer_load_dwordx4 v248, %[rx], %[cnt] offen lds\n" \
  "s_add_u32 m0, %[mw], 16384\n" \
  "s_xor_b32 %[cnt], %[sn], 256\n" \
  "buffer_load_dwordx4 v248, %[rx], %[cnt] offen lds\n" \
  "s_add_u32 m0, %[mw], 20480\n" \
  "s_xor_b32 %[cnt], %[sn], 320\n" \
  "buffer_load_dwordx4 v248, %[rx], %[cnt] offen lds\n" \
  "v_mfma_f32_16x16x32_bf16 v[192:195], v[184:187], v[216:219], v[192:195]\n" \
  "v_mfma_f32_16x16x32_bf16 v[196:199], v[188:191], v[216:219], v[196:199]\n" \
  "ds_read_b128 v[216:219], v252 offset:2048\n" \
  "v_mfma_f32_16x16x32_bf16 v[200:203], v[184:187], v[220:223], v[200:203]\n" \
  "v_mfma_f32_16x16x32_bf16 v[204:207], v[188:191], v[220:223], v[204:207]\n" \
  "ds_read_b128 v[220:223], v252 offset:3072\n" \
  "v_mfma_f32_16x16x32_bf16 v[192:195], v[60:63], v[224:227], v[192:195]\n" \
  "v_mfma_f32_16x16x32_bf16 v[196:199], v[124:127], v[224:227], v[196:199]\n" \
  "ds_read_b128 v[224:227], v252 offset:4096\n" \
  "v_mfma_f32_16x16x32_bf16 v[200:203], v[60:63], v[228:231], v[200:203]\n" \
  "v_mfma_f32_16x16x32_bf16 v[204:207], v[124:127], v[228:231], v[204:207]\n" \
  "ds_read_b128 v[228:231], v252 offset:5120\n" \
  "v_mfma_f32_16x16x32_bf16 v[192:195], v[240:243], v[232:235], v[192:195]\n" \
  "v_mfma_f32_16x16x32_bf16 v[196:199], v[244:247], v[232:235], v[196:199]\n" \
  "ds_read_b128 v[232:235], v252 offset:6144\n" \
  "v_mfma_f32_16x16x32_bf16 v[200:203], v[240:243], v[236:239], v[200:203]\n" \
  "v_mfma_f32_16x16x32_bf16 v[204:207], v[244:247], v[236:239], v[204:207]\n" \
  "ds_read_b128 v[236:239], v252 offset:7168\n" \
  "v_cvt_pk_bf16_f32 v192, v192, v193\n" \
  "v_cvt_pk_bf16_f32 v193, v194, v195\n" \
  "v_cvt_pk_bf16_f32 v194, v196, v197\n" \
  "v_cvt_pk_bf16_f32 v195, v198, v199\n" \
  "v_add_u32 v196, %[so], v251\n" \
  "ds_write_b128 v196, v[192:195] offset:24576\n" \
  "v_cvt_pk_bf16_f32 v200, v200, v201\n" \
  "v_cvt_pk_bf16_f32 v201, v202, v203\n" \
  "v_cvt_pk_bf16_f32 v202, v204, v205\n" \
  "v_cvt_pk_bf16_f32 v203, v206, v207\n" \
  "ds_write_b128 v196, v[200:203] offset:28672\n" \
  ".Lqe_%=:\n" \
  "s_mov_b32 m0, %[keep]\n" \
  "s_waitcnt lgkmcnt(0)\n"
#define ST_XB \
  "s_barrier\n" \
  "ds_read_b128 v[192:195], v251 offset:24576\n" \
  "ds_read_b128 v[208:211], v251 offset:28672\n" \
  "ds_read_b128 v[196:199], v251 offset:25600\n" \
  "ds_read_b128 v[212:215], v251 offset:29696\n" \
  "ds_read_b128 v[200:203], v251 offset:26624\n" \
  "ds_read_b128 v[216:219], v251 offset:30720\n" \
  "ds_read_b128 v[204:207], v251 offset:27648\n" \
  "ds_read_b128 v[220:223], v251 offset:31744\n" \
  "ds_read_b128 v[240:243], v250 offset:0\n" \
  "ds_read_b128 v[244:247], v250 offset:16384\n" \
  "s_waitcnt lgkmcnt(9)\n" \
  "v_mfma_f32_16x16x32_bf16 v[224:227], v[192:195], v[0:3], 0\n" \
  "s_waitcnt lgkmcnt(8)\n" \
  "v_mfma_f32_16x16x32_bf16 v[228:231], v[208:211], v[0:3], 0\n" \
  "s_waitcnt lgkmcnt(7)\n" \
  "v_mfma_f32_16x16x32_bf16 v[224:227], v[196:199], v[4:7], v[224:227]\n" \
  "s_waitcnt lgkmcnt(6)\n" \
  "v_mfma_f32_16x16x32_bf16 v[228:231], v[212:215], v[4:7], v[228:231]\n" \
  "s_waitcnt lgkmcnt(5)\n" \
  "v_mfma_f32_16x16x32_bf16 v[224:227], v[200:203], v[8:11], v[224:227]\n" \
  "s_waitcnt lgkmcnt(4)\n" \
  "v_mfma_f32_16x16x32_bf16 v[228:231], v[216:219], v[8:11], v[228:231]\n" \
  "s_waitcnt lgkmcnt(3)\n" \
  "v_mfma_f32_16x16x32_bf16 v[224:227], v[204:207], v[12:15], v[224:227]\n" \
  "s_waitcnt lgkmcnt(2)\n" \
  "v_mfma_f32_16x16x32_bf16 v[228:231], v[220:223], v[12:15], v[228:231]\n" \
  "v_mfma_f32_16x16x32_bf16 v[232:235], v[192:195], v[128:131], 0\n" \
  "v_mfma_f32_16x16x32_bf16 v[236:239], v[208:211], v[128:131], 0\n" \
  "v_mfma_f32_16x16x32_bf16 v[232:235], v[200:203], v[136:139], v[232:235]\n" \
  "v_cvt_pk_bf16_f32 v224, v224, v228\n" \
  "v_cvt_pk_bf16_f32 v225, v225, v229\n" \
  "v_mfma_f32_16x16x32_bf16 v[236:239], v[216:219], v[136:139], v[236:239]\n" \
  "v_cvt_pk_bf16_f32 v226, v226, v230\n" \
  "v_cvt_pk_bf16_f32 v227, v227, v231\n" \
  "s_waitcnt lgkmcnt(1)\n" \
  "v_mfma_f32_16x16x32_bf16 v[232:235], v[196:199], v[240:243], v[232:235]\n" \
  "s_add_u32 %[cnt], %[so], 0*%c[js]+0*%c[ss]\n" \
  "buffer_store_dwordx4 v[224:227], v251, %[rs], %[cnt] offen sc1\n" \
  "v_mfma_f32_16x16x32_bf16 v[236:239], v[212:215], v[240:243], v[236:239]\n" \
  "s_waitcnt lgkmcnt(0)\n" \
  "v_mfma_f32_16x16x32_bf16 v[232:235], v[204:207], v[244:247], v[232:235]\n" \
  "v_mfma_f32_16x16x32_bf16 v[236:239], v[220:223], v[244:247], v[236:239]\n" \
  "ds_read_b128 v[240:243], v250 offset:1024\n" \
  "ds_read_b128 v[244:247], v250 offset:17408\n" \
  "v_mfma_f32_16x16x32_bf16 v[224:227], v[192:195], v[64:67], 0\n" \
  "v_mfma_f32_16x16x32_bf16 v[228:231], v[208:211], v[64:67], 0\n" \
  "v_mfma_f32_16x16x32_bf16 v[224:227], v[196:199], v[68:71], v[224:227]\n" \
  "v_cvt_pk_bf16_f32 v232, v232, v236\n" \
  "v_cvt_pk_bf16_f32 v233, v233, v237\n" \
  "v_mfma_f32_16x16x32_bf16 v[228:231], v[212:215], v[68:71], v[228:231]\n" \
  "v_cvt_pk_bf16_f32 v234, v234, v238\n" \
  "v_cvt_pk_bf16_f32 v235, v235, v239\n" \
  "v_mfma_f32_16x16x32_bf16 v[224:227], v[200:203], v[72:75], v[224:227]\n" \
  "s_add_u32 %[cnt], %[so], 4*%c[js]+0*%c[ss]\n" \
  "buffer_store_dwordx4 v[232:235], v251, %[rs], %[cnt] offen sc1\n" \
  "v_mfma_f32_16x16x32_bf16 v[228:231], v[216:219], v[72:75], v[228:231]\n" \
  "v_mfma_f32_16x16x32_bf16 v[224:227], v[204:207], v[76:79], v[224:227]\n" \
  "v_mfma_f32_16x16x32_bf16 v[228:231], v[220:223], v[76:79], v[228:231]\n" \
  "s_waitcnt vmcnt(2)\n" \
  "s_barrier\n" \
  "v_mfma_f32_16x16x32_bf16 v[232:235], v[192:195], v[132:135], 0\n" \
  "v_mfma_f32_16x16x32_bf16 v[236:239], v[208:211], v[132:135], 0\n" \
  "v_mfma_f32_16x16x32_bf16 v[232:235], v[200:203], v[140:143], v[232:235]\n" \
  "v_cvt_pk_bf16_f32 v224, v224, v228\n" \
  "v_cvt_pk_bf16_f32 v225, v225, v229\n" \
  "v_mfma_f32_16x16x32_bf16 v[236:239], v[216:219], v[140:143], v[236:239]\n" \
  "v_cvt_pk_bf16_f32 v226, v226, v230\n" \
  "v_cvt_pk_bf16_f32 v227, v227, v231\n" \
  "s_waitcnt lgkmcnt(1)\n" \
  "v_mfma_f32_16x16x32_bf16 v[232:235], v[196:199], v[240:243], v[232:235]\n" \
  "s_add_u32 %[cnt], %[so], 0*%c[js]+1*%c[ss]\n" \
  "buffer_store_dwordx4 v[224:227], v251, %[rs], %[cnt] offen sc1\n" \
  "v_mfma_f32_16x16x32_bf16 v[236:239], v[212:215], v[240:243], v[236:239]\n" \
  "s_waitcnt lgkmcnt(0)\n" \
  "v_mfma_f32_16x16x32_bf16 v[232:235], v[204:207], v[244:247], v[232:235]\n" \
  "v_mfma_f32_16x16x32_bf16 v[236:239], v[220:223], v[244:247], v[236:239]\n" \
  "ds_read_b128 v[240:243], v250 offset:32768\n" \
  "ds_read_b128 v[244:247], v250 offset:49152\n" \
  "v_mfma_f32_16x16x32_bf16 v[224:227], v[192:195], v[16:19], 0\n" \
  "v_mfma_f32_16x16x32_bf16 v[228:231], v[208:211], v[16:19], 0\n" \
  "v_mfma_f32_16x16x32_bf16 v[224:227], v[196:199], v[20:23], v[224:227]\n" \
  "v_cvt_pk_bf16_f32 v232, v232, v236\n" \
  "v_cvt_pk_bf16_f32 v233, v233, v237\n" \
  "v_mfma_f32_16x16x32_bf16 v[228:231], v[212:215], v[20:23], v[228:231]\n" \
  "v_cvt_pk_bf16_f32 v234, v234, v238\n" \
  "v_cvt_pk_bf16_f32 v235, v235, v239\n" \
  "v_mfma_f32_16x16x32_bf16 v[224:227], v[200:203], v[24:27], v[224:227]\n" \
  "s_add_u32 %[cnt], %[so], 4*%c[js]+1*%c[ss]\n" \
  "buffer_store_dwordx4 v[232:235], v251, %[rs], %[cnt] offen sc1\n" \
  "v_mfma_f32_16x16x32_bf16 v[228:231], v[216:219], v[24:27], v[228:231]\n" \
  "v_mfma_f32_16x16x32_bf16 v[224:227], v[204:207], v[28:31], v[224:227]\n" \
  "v_mfma_f32_16x16x32_bf16 v[228:231], v[220:223], v[28:31], v[228:231]\n" \
  "s_barrier\n" \
  "v_mfma_f32_16x16x32_bf16 v[232:235], v[192:195], v[144:147], 0\n" \
  "v_mfma_f32_16x16x32_bf16 v[236:239], v[208:211], v[144:147], 0\n" \
  "v_mfma_f32_16x16x32_bf16 v[232:235], v[200:203], v[152:155], v[232:235]\n" \
  "v_cvt_pk_bf16_f32 v224, v224, v228\n" \
  "v_cvt_pk_bf16_f32 v225, v225, v229\n" \
  "v_mfma_f32_16x16x32_bf16 v[236:239], v[216:219], v[152:155], v[236:239]\n" \
  "v_cvt_pk_bf16_f32 v226, v226, v230\n" \
  "v_cvt_pk_bf16_f32 v227, v227, v231\n" \
  "s_waitcnt lgkmcnt(1)\n" \
  "v_mfma_f32_16x16x32_bf16 v[232:235], v[196:199], v[240:243], v[232:235]\n" \
  "s_add_u32 %[cnt], %[so2], 1*%c[js]+0*%c[ss]\n" \
  "buffer_store_dwordx4 v[224:227], v251, %[rs], %[cnt] offen sc1\n" \
  "v_mfma_f32_16x16x32_bf16 v[236:239], v[212:215], v[240:243], v[236:239]\n" \
  "s_waitcnt lgkmcnt(0)\n" \
  "v_mfma_f32_16x16x32_bf16 v[232:235], v[204:207], v[244:247], v[232:235]\n" \
  "v_mfma_f32_16x16x32_bf16 v[236:239], v[220:223], v[244:247], v[236:239]\n" \
  "ds_read_b128 v[240:243], v250 offset:33792\n" \
  "ds_read_b128 v[244:247], v250 offset:50176\n" \
  "v_mfma_f32_16x16x32_bf16 v[224:227], v[192:195], v[80:83], 0\n" \
  "v_mfma_f32_16x16x32_bf16 v[228:231], v[208:211], v[80:83], 0\n" \
  "v_mfma_f32_16x16x32_bf16 v[224:227], v[196:199], v[84:87], v[224:227]\n" \
  "v_cvt_pk_bf16_f32 v232, v232, v236\n" \
  "v_cvt_pk_bf16_f32 v233, v233, v237\n" \
  "v_mfma_f32_16x16x32_bf16 v[228:231], v[212:215], v[84:87], v[228:231]\n" \
  "v_cvt_pk_bf16_f32 v234, v234, v238\n" \
  "v_cvt_pk_bf16_f32 v235, v235, v239\n" \
  "v_mfma_f32_16x16x32_bf16 v[224:227], v[200:203], v[88:91], v[224:227]\n" \
  "s_add_u32 %[cnt], %[so2], 5*%c[js]+0*%c[ss]\n" \
  "buffer_store_dwordx4 v[232:235], v251, %[rs], %[cnt] offen sc1\n" \
  "v_mfma_f32_16x16x32_bf16 v[228:231], v[216:219], v[88:91], v[228:231]\n" \
  "v_mfma_f32_16x16x32_bf16 v[224:227], v[204:207], v[92:95], v[224:227]\n" \
  "v_mfma_f32_16x16x32_bf16 v[228:231], v[220:223], v[92:95], v[228:231]\n" \
  "v_mfma_f32_16x16x32_bf16 v[232:235], v[192:195], v[148:151], 0\n" \
  "v_mfma_f32_16x16x32_bf16 v[236:239], v[208:211], v[148:151], 0\n" \
  "v_mfma_f32_16x16x32_bf16 v[232:235], v[200:203], v[156:159], v[232:235]\n" \
  "v_cvt_pk_bf16_f32 v224, v224, v228\n" \
  "v_cvt_pk_bf16_f32 v225, v225, v229\n" \
  "v_mfma_f32_16x16x32_bf16 v[236:239], v[216:219], v[156:159], v[236:239]\n" \
  "v_cvt_pk_bf16_f32 v226, v226, v230\n" \
  "v_cvt_pk_bf16_f32 v227, v227, v231\n" \
  "s_waitcnt lgkmcnt(1)\n" \
  "v_mfma_f32_16x16x32_bf16 v[232:235], v[196:199], v[240:243], v[232:235]\n" \
  "s_add_u32 %[cnt], %[so2], 1*%c[js]+1*%c[ss]\n" \
  "buffer_store_dwordx4 v[224:227], v251, %[rs], %[cnt] offen sc1\n" \
  "v_mfma_f32_16x16x32_bf16 v[236:239], v[212:215], v[240:243], v[236:239]\n" \
  "s_waitcnt lgkmcnt(0)\n" \
  "v_mfma_f32_16x16x32_bf16 v[232:235], v[204:207], v[244:247], v[232:235]\n" \
  "v_mfma_f32_16x16x32_bf16 v[236:239], v[220:223], v[244:247], v[236:239]\n" \
  "ds_read_b128 v[240:243], v249 offset:0\n" \
  "ds_read_b128 v[244:247], v249 offset:16384\n" \
  "s_barrier\n" \
  "v_mfma_f32_16x16x32_bf16 v[224:227], v[192:195], v[32:35], 0\n" \
  "v_mfma_f32_16x16x32_bf16 v[228:231], v[208:211], v[32:35], 0\n" \
  "v_mfma_f32_16x16x32_bf16 v[224:227], v[196:199], v[36:39], v[224:227]\n" \
  "v_cvt_pk_bf16_f32 v232, v232, v236\n" \
  "v_cvt_pk_bf16_f32 v233, v233, v237\n" \
  "v_mfma_f32_16x16x32_bf16 v[228:231], v[212:215], v[36:39], v[228:231]\n" \
  "v_cvt_pk_bf16_f32 v234, v234, v238\n" \
  "v_cvt_pk_bf16_f32 v235, v235, v239\n" \
  "v_mfma_f32_16x16x32_bf16 v[224:227], v[200:203], v[40:43], v[224:227]\n" \
  "s_add_u32 %[cnt], %[so2], 5*%c[js]+1*%c[ss]\n" \
  "buffer_store_dwordx4 v[232:235], v251, %[rs], %[cnt] offen sc1\n" \
  "v_mfma_f32_16x16x32_bf16 v[228:231], v[216:219], v[40:43], v[228:231]\n" \
  "v_mfma_f32_16x16x32_bf16 v[224:227], v[204:207], v[44:47], v[224:227]\n" \
  "v_mfma_f32_16x16x32_bf16 v[228:231], v[220:223], v[44:47], v[228:231]\n" \
  "v_mfma_f32_16x16x32_bf16 v[232:235], v[192:195], v[160:163], 0\n" \
  "v_mfma_f32_16x16x32_bf16 v[236:239], v[208:211], v[160:163], 0\n" \
  "v_mfma_f32_16x16x32_bf16 v[232:235], v[200:203], v[168:171], v[232:235]\n" \
  "v_cvt_pk_bf16_f32 v224, v224, v228\n" \
  "v_cvt_pk_bf16_f32 v225, v225, v229\n" \
  "v_mfma_f32_16x16x32_bf16 v[236:239], v[216:219], v[168:171], v[236:239]\n" \
  "v_cvt_pk_bf16_f32 v226, v226, v230\n" \
  "v_cvt_pk_bf16_f32 v227, v227, v231\n" \
  "s_waitcnt lgkmcnt(1)\n" \
  "v_mfma_f32_16x16x32_bf16 v[232:235], v[196:199], v[240:243], v[232:235]\n" \
  "s_add_u32 %[cnt], %[so], 2*%c[js]+0*%c[ss]\n" \
  "buffer_store_dwordx4 v[224:227], v251, %[rs], %[cnt] offen sc1\n" \
  "v_mfma_f32_16x16x32_bf16 v[236:239], v[212:215], v[240:243], v[236:239]\n" \
  "s_waitcnt lgkmcnt(0)\n" \
  "v_mfma_f32_16x16x32_bf16 v[232:235], v[204:207], v[244:247], v[232:235]\n" \
  "v_mfma_f32_16x16x32_bf16 v[236:239], v[220:223], v[244:247], v[236:239]\n" \
  "ds_read_b128 v[240:243], v249 offset:1024\n" \
  "ds_read_b128 v[244:247], v249 offset:17408\n" \
  "v_mfma_f32_16x16x32_bf16 v[224:227], v[192:195], v[96:99], 0\n" \
  "v_mfma_f32_16x16x32_bf16 v[228:231], v[208:211], v[96:99], 0\n" \
  "v_mfma_f32_16x16x32_bf16 v[224:227], v[196:199], v[100:103], v[224:227]\n" \
  "v_cvt_pk_bf16_f32 v232, v232, v236\n" \
  "v_cvt_pk_bf16_f32 v233, v233, v237\n" \
  "v_mfma_f32_16x16x32_bf16 v[228:231], v[212:215], v[100:103], v[228:231]\n" \
  "v_cvt_pk_bf16_f32 v234, v234, v238\n" \
  "v_cvt_pk_bf16_f32 v235, v235, v239\n" \
  "v_mfma_f32_16x16x32_bf16 v[224:227], v[200:203], v[104:107], v[224:227]\n" \
  "s_add_u32 %[cnt], %[so], 6*%c[js]+0*%c[ss]\n" \
  "buffer_store_dwordx4 v[232:235], v251, %[rs], %[cnt] offen sc1\n" \
  "v_mfma_f32_16x16x32_bf16 v[228:231], v[216:219], v[104:107], v[228:231]\n" \
  "v_mfma_f32_16x16x32_bf16 v[224:227], v[204:207], v[108:111], v[224:227]\n" \
  "v_mfma_f32_16x16x32_bf16 v[228:231], v[220:223], v[108:111], v[228:231]\n" \
  "s_barrier\n" \
  "v_mfma_f32_16x16x32_bf16 v[232:235], v[192:195], v[164:167], 0\n" \
  "v_mfma_f32_16x16x32_bf16 v[236:239], v[208:211], v[164:167], 0\n" \
  "v_mfma_f32_16x16x32_bf16 v[232:235], v[200:203], v[172:175], v[232:235]\n" \
  "v_cvt_pk_bf16_f32 v224, v224, v228\n" \
  "v_cvt_pk_bf16_f32 v225, v225, v229\n" \
  "v_mfma_f32_16x16x32_bf16 v[236:239], v[216:219], v[172:175], v[236:239]\n" \
  "v_cvt_pk_bf16_f32 v226, v226, v230\n" \
  "v_cvt_pk_bf16_f32 v227, v227, v231\n" \
  "s_waitcnt lgkmcnt(1)\n" \
  "v_mfma_f32_16x16x32_bf16 v[232:235], v[196:199], v[240:243], v[232:235]\n" \
  "s_add_u32 %[cnt], %[so], 2*%c[js]+1*%c[ss]\n" \
  "buffer_store_dwordx4 v[224:227], v251, %[rs], %[cnt] offen sc1\n" \
  "v_mfma_f32_16x16x32_bf16 v[236:239], v[212:215], v[240:243], v[236:239]\n" \
  "s_waitcnt lgkmcnt(0)\n" \
  "v_mfma_f32_16x16x32_bf16 v[232:235], v[204:207], v[244:247], v[232:235]\n" \
  "v_mfma_f32_16x16x32_bf16 v[236:239], v[220:223], v[244:247], v[236:239]\n" \
  "ds_read_b128 v[240:243], v249 offset:32768\n" \
  "ds_read_b128 v[244:247], v249 offset:49152\n" \
  "v_mfma_f32_16x16x32_bf16 v[224:227], v[192:195], v[48:51], 0\n" \
  "v_mfma_f32_16x16x32_bf16 v[228:231], v[208:211], v[48:51], 0\n" \
  "v_mfma_f32_16x16x32_bf16 v[224:227], v[196:199], v[52:55], v[224:227]\n" \
  "v_cvt_pk_bf16_f32 v232, v232, v236\n" \
  "v_cvt_pk_bf16_f32 v233, v233, v237\n" \
  "v_mfma_f32_16x16x32_bf16 v[228:231], v[212:215], v[52:55], v[228:231]\n" \
  "v_cvt_pk_bf16_f32 v234, v234, v238\n" \
  "v_cvt_pk_bf16_f32 v235, v235, v239\n" \
  "v_mfma_f32_16x16x32_bf16 v[224:227], v[200:203], v[56:59], v[224:227]\n" \
  "s_add_u32 %[cnt], %[so], 6*%c[js]+1*%c[ss]\n" \
  "buffer_store_dwordx4 v[232:235], v251, %[rs], %[cnt] offen sc1\n" \
  "v_mfma_f32_16x16x32_bf16 v[228:231], v[216:219], v[56:59], v[228:231]\n" \
  "v_mfma_f32_16x16x32_bf16 v[224:227], v[204:207], v[60:63], v[224:227]\n" \
  "v_mfma_f32_16x16x32_bf16 v[228:231], v[220:223], v[60:63], v[228:231]\n" \
  "s_barrier\n" \
  "v_mfma_f32_16x16x32_bf16 v[232:235], v[192:195], v[176:179], 0\n" \
  "v_mfma_f32_16x16x32_bf16 v[236:239], v[208:211], v[176:179], 0\n" \
  "v_mfma_f32_16x16x32_bf16 v[232:235], v[200:203], v[184:187], v[232:235]\n" \
  "v_cvt_pk_bf16_f32 v224, v224, v228\n" \
  "v_cvt_pk_bf16_f32 v225, v225, v229\n" \
  "v_mfma_f32_16x16x32_bf16 v[236:239], v[216:219], v[184:187], v[236:239]\n" \
  "v_cvt_pk_bf16_f32 v226, v226, v230\n" \
  "v_cvt_pk_bf16_f32 v227, v227, v231\n" \
  "s_waitcnt lgkmcnt(1)\n" \
  "v_mfma_f32_16x16x32_bf16 v[232:235], v[196:199], v[240:243], v[232:235]\n" \
  "s_add_u32 %[cnt], %[so2], 3*%c[js]+0*%c[ss]\n" \
  "buffer_store_dwordx4 v[224:227], v251, %[rs], %[cnt] offen sc1\n" \
  "v_mfma_f32_16x16x32_bf16 v[236:239], v[212:215], v[240:243], v[236:239]\n" \
  "s_waitcnt lgkmcnt(0)\n" \
  "v_mfma_f32_16x16x32_bf16 v[232:235], v[204:207], v[244:247], v[232:235]\n" \
  "v_mfma_f32_16x16x32_bf16 v[236:239], v[220:223], v[244:247], v[236:239]\n" \
  "ds_read_b128 v[240:243], v249 offset:33792\n" \
  "ds_read_b128 v[244:247], v249 offset:50176\n" \
  "v_mfma_f32_16x16x32_bf16 v[224:227], v[192:195], v[112:115], 0\n" \
  "v_mfma_f32_16x16x32_bf16 v[228:231], v[208:211], v[112:115], 0\n" \
  "v_mfma_f32_16x16x32_bf16 v[224:227], v[196:199], v[116:119], v[224:227]\n" \
  "v_cvt_pk_bf16_f32 v232, v232, v236\n" \
  "v_cvt_pk_bf16_f32 v233, v233, v237\n" \
  "v_mfma_f32_16x16x32_bf16 v[228:231], v[212:215], v[116:119], v[228:231]\n" \
  "v_cvt_pk_bf16_f32 v234, v234, v238\n" \
  "v_cvt_pk_bf16_f32 v235, v235, v239\n" \
  "v_mfma_f32_16x16x32_bf16 v[224:227], v[200:203], v[120:123], v[224:227]\n" \
  "s_add_u32 %[cnt], %[so2], 7*%c[js]+0*%c[ss]\n" \
  "buffer_store_dwordx4 v[232:235], v251, %[rs], %[cnt] offen sc1\n" \
  "v_mfma_f32_16x16x32_bf16 v[228:231], v[216:219], v[120:123], v[228:231]\n" \
  "v_mfma_f32_16x16x32_bf16 v[224:227], v[204:207], v[124:127], v[224:227]\n" \
  "v_mfma_f32_16x16x32_bf16 v[228:231], v[220:223], v[124:127], v[228:231]\n" \
  "v_mfma_f32_16x16x32_bf16 v[232:235], v[192:195], v[180:183], 0\n" \
  "v_mfma_f32_16x16x32_bf16 v[236:239], v[208:211], v[180:183], 0\n" \
  "v_mfma_f32_16x16x32_bf16 v[232:235], v[200:203], v[188:191], v[232:235]\n" \
  "v_cvt_pk_bf16_f32 v224, v224, v228\n" \
  "v_cvt_pk_bf16_f32 v225, v225, v229\n" \
  "v_mfma_f32_16x16x32_bf16 v[236:239], v[216:219], v[188:191], v[236:239]\n" \
  "v_cvt_pk_bf16_f32 v226, v226, v230\n" \
  "v_cvt_pk_bf16_f32 v227, v227, v231\n" \
  "s_waitcnt lgkmcnt(1)\n" \
  "v_mfma_f32_16x16x32_bf16 v[232:235], v[196:199], v[240:243], v[232:235]\n" \
  "s_add_u32 %[cnt], %[so2], 3*%c[js]+1*%c[ss]\n" \
  "buffer_store_dwordx4 v[224:227], v251, %[rs], %[cnt] offen sc1\n" \
  "v_mfma_f32_16x16x32_bf16 v[236:239], v[212:215], v[240:243], v[236:239]\n" \
  "s_waitcnt lgkmcnt(0)\n" \
  "v_mfma_f32_16x16x32_bf16 v[232:235], v[204:207], v[244:247], v[232:235]\n" \
  "v_mfma_f32_16x16x32_bf16 v[236:239], v[220:223], v[244:247], v[236:239]\n" \
  "s_nop 8\n" \
  "v_cvt_pk_bf16_f32 v232, v232, v236\n" \
  "v_cvt_pk_bf16_f32 v233, v233, v237\n" \
  "v_cvt_pk_bf16_f32 v234, v234, v238\n" \
  "v_cvt_pk_bf16_f32 v235, v235, v239\n" \
  "s_add_u32 %[cnt], %[so2], 7*%c[js]+1*%c[ss]\n" \
  "buffer_store_dwordx4 v[232:235], v251, %[rs], %[cnt] offen sc1\n" \
  "s_nop 1\n"
#define ST_XB0 \
  "s_setprio 1\n" \
  "ds_read_b128 v[192:195], v251 offset:24576\n" \
  "ds_read_b128 v[208:211], v251 offset:28672\n" \
  "ds_read_b128 v[196:199], v251 offset:25600\n" \
  "ds_read_b128 v[212:215], v251 offset:29696\n" \
  "ds_read_b128 v[200:203], v251 offset:26624\n" \
  "ds_read_b128 v[216:219], v251 offset:30720\n" \
  "ds_read_b128 v[204:207], v251 offset:27648\n" \
  "ds_read_b128 v[220:223], v251 offset:31744\n" \
  "ds_read_b128 v[240:243], v250 offset:0\n" \
  "ds_read_b128 v[244:247], v250 offset:16384\n" \
  "s_waitcnt lgkmcnt(9)\n" \
  "v_mfma_f32_16x16x32_bf16 v[224:227], v[192:195], v[0:3], 0\n" \
  "s_waitcnt lgkmcnt(8)\n" \
  "v_mfma_f32_16x16x32_bf16 v[228:231], v[208:211], v[0:3], 0\n" \
  "s_waitcnt lgkmcnt(7)\n" \
  "v_mfma_f32_16x16x32_bf16 v[224:227], v[196:199], v[4:7], v[224:227]\n" \
  "s_waitcnt lgkmcnt(6)\n" \
  "v_mfma_f32_16x16x32_bf16 v[228:231], v[212:215], v[4:7], v[228:231]\n" \
  "s_waitcnt lgkmcnt(5)\n" \
  "v_mfma_f32_16x16x32_bf16 v[224:227], v[200:203], v[8:11], v[224:227]\n" \
  "s_waitcnt lgkmcnt(4)\n" \
  "v_mfma_f32_16x16x32_bf16 v[228:231], v[216:219], v[8:11], v[228:231]\n" \
  "s_waitcnt lgkmcnt(3)\n" \
  "v_mfma_f32_16x16x32_bf16 v[224:227], v[204:207], v[12:15], v[224:227]\n" \
  "s_waitcnt lgkmcnt(2)\n" \
  "v_mfma_f32_16x16x32_bf16 v[228:231], v[220:223], v[12:15], v[228:231]\n" \
  "v_mfma_f32_16x16x32_bf16 v[232:235], v[192:195], v[128:131], 0\n" \
  "v_mfma_f32_16x16x32_bf16 v[236:239], v[208:211], v[128:131], 0\n" \
  "v_mfma_f32_16x16x32_bf16 v[232:235], v[200:203], v[136:139], v[232:235]\n" \
  "v_cvt_pk_bf16_f32 v224, v224, v228\n" \
  "v_cvt_pk_bf16_f32 v225, v225, v229\n" \
  "v_mfma_f32_16x16x32_bf16 v[236:239], v[216:219], v[136:139], v[236:239]\n" \
  "v_cvt_pk_bf16_f32 v226, v226, v230\n" \
  "v_cvt_pk_bf16_f32 v227, v227, v231\n" \
  "s_waitcnt lgkmcnt(1)\n" \
  "v_mfma_f32_16x16x32_bf16 v[232:235], v[196:199], v[240:243], v[232:235]\n" \
  "s_add_u32 %[cnt], %[so], 0*%c[js]+0*%c[ss]\n" \
  "buffer_store_dwordx4 v[224:227], v251, %[rs], %[cnt] offen sc1\n" \
  "v_mfma_f32_16x16x32_bf16 v[236:239], v[212:215], v[240:243], v[236:239]\n" \
  "s_waitcnt lgkmcnt(0)\n" \
  "v_mfma_f32_16x16x32_bf16 v[232:235], v[204:207], v[244:247], v[232:235]\n" \
  "v_mfma_f32_16x16x32_bf16 v[236:239], v[220:223], v[244:247], v[236:239]\n" \
  "ds_read_b128 v[240:243], v250 offset:1024\n" \
  "ds_read_b128 v[244:247], v250 offset:17408\n" \
  "v_mfma_f32_16x16x32_bf16 v[224:227], v[192:195], v[64:67], 0\n" \
  "v_mfma_f32_16x16x32_bf16 v[228:231], v[208:211], v[64:67], 0\n" \
  "v_mfma_f32_16x16x32_bf16 v[224:227], v[196:199], v[68:71], v[224:227]\n" \
  "v_cvt_pk_bf16_f32 v232, v232, v236\n" \
  "v_cvt_pk_bf16_f32 v233, v233, v237\n" \
  "v_mfma_f32_16x16x32_bf16 v[228:231], v[212:215], v[68:71], v[228:231]\n" \
  "v_cvt_pk_bf16_f32 v234, v234, v238\n" \
  "v_cvt_pk_bf16_f32 v235, v235, v239\n" \
  "v_mfma_f32_16x16x32_bf16 v[224:227], v[200:203], v[72:75], v[224:227]\n" \
  "s_add_u32 %[cnt], %[so], 4*%c[js]+0*%c[ss]\n" \
  "buffer_store_dwordx4 v[232:235], v251, %[rs], %[cnt] offen sc1\n" \
  "v_mfma_f32_16x16x32_bf16 v[228:231], v[216:219], v[72:75], v[228:231]\n" \
  "v_mfma_f32_16x16x32_bf16 v[224:227], v[204:207], v[76:79], v[224:227]\n" \
  "v_mfma_f32_16x16x32_bf16 v[228:231], v[220:223], v[76:79], v[228:231]\n" \
  "v_mfma_f32_16x16x32_bf16 v[232:235], v[192:195], v[132:135], 0\n" \
  "v_mfma_f32_16x16x32_bf16 v[236:239], v[208:211], v[132:135], 0\n" \
  "v_mfma_f32_16x16x32_bf16 v[232:235], v[200:203], v[140:143], v[232:235]\n" \
  "v_cvt_pk_bf16_f32 v224, v224, v228\n" \
  "v_cvt_pk_bf16_f32 v225, v225, v229\n" \
  "v_mfma_f32_16x16x32_bf16 v[236:239], v[216:219], v[140:143], v[236:239]\n" \
  "v_cvt_pk_bf16_f32 v226, v226, v230\n" \
  "v_cvt_pk_bf16_f32 v227, v227, v231\n" \
  "s_waitcnt lgkmcnt(1)\n" \
  "v_mfma_f32_16x16x32_bf16 v[232:235], v[196:199], v[240:243], v[232:235]\n" \
  "s_add_u32 %[cnt], %[so], 0*%c[js]+1*%c[ss]\n" \
  "buffer_store_dwordx4 v[224:227], v251, %[rs], %[cnt] offen sc1\n" \
  "v_mfma_f32_16x16x32_bf16 v[236:239], v[212:215], v[240:243], v[236:239]\n" \
  "s_waitcnt lgkmcnt(0)\n" \
  "v_mfma_f32_16x16x32_bf16 v[232:235], v[204:207], v[244:247], v[232:235]\n" \
  "v_mfma_f32_16x16x32_bf16 v[236:239], v[220:223], v[244:247], v[236:239]\n" \
  "ds_read_b128 v[240:243], v250 offset:32768\n" \
  "ds_read_b128 v[244:247], v250 offset:49152\n" \
  "v_mfma_f32_16x16x32_bf16 v[224:227], v[192:195], v[16:19], 0\n" \
  "v_mfma_f32_16x16x32_bf16 v[228:231], v[208:211], v[16:19], 0\n" \
  "v_mfma_f32_16x16x32_bf16 v[224:227], v[196:199], v[20:23], v[224:227]\n" \
  "v_cvt_pk_bf16_f32 v232, v232, v236\n" \
  "v_cvt_pk_bf16_f32 v233, v233, v237\n" \
  "v_mfma_f32_16x16x32_bf16 v[228:231], v[212:215], v[20:23], v[228:231]\n" \
  "v_cvt_pk_bf16_f32 v234, v234, v238\n" \
  "v_cvt_pk_bf16_f32 v235, v235, v239\n" \
  "v_mfma_f32_16x16x32_bf16 v[224:227], v[200:203], v[24:27], v[224:227]\n" \
  "s_add_u32 %[cnt], %[so], 4*%c[js]+1*%c[ss]\n" \
  "buffer_store_dwordx4 v[232:235], v251, %[rs], %[cnt] offen sc1\n" \
  "v_mfma_f32_16x16x32_bf16 v[228:231], v[216:219], v[24:27], v[228:231]\n" \
  "v_mfma_f32_16x16x32_bf16 v[224:227], v[204:207], v[28:31], v[224:227]\n" \
  "v_mfma_f32_16x16x32_bf16 v[228:231], v[220:223], v[28:31], v[228:231]\n" \
  "v_mfma_f32_16x16x32_bf16 v[232:235], v[192:195], v[144:147], 0\n" \
  "v_mfma_f32_16x16x32_bf16 v[236:239], v[208:211], v[144:147], 0\n" \
  "v_mfma_f32_16x16x32_bf16 v[232:235], v[200:203], v[152:155], v[232:235]\n" \
  "v_cvt_pk_bf16_f32 v224, v224, v228\n" \
  "v_cvt_pk_bf16_f32 v225, v225, v229\n" \
  "v_mfma_f32_16x16x32_bf16 v[236:239], v[216:219], v[152:155], v[236:239]\n" \
  "v_cvt_pk_bf16_f32 v226, v226, v230\n" \
  "v_cvt_pk_bf16_f32 v227, v227, v231\n" \
  "s_waitcnt lgkmcnt(1)\n" \
  "v_mfma_f32_16x16x32_bf16 v[232:235], v[196:199], v[240:243], v[232:235]\n" \
  "s_add_u32 %[cnt], %[so2], 1*%c[js]+0*%c[ss]\n" \
  "buffer_store_dwordx4 v[224:227], v251, %[rs], %[cnt] offen sc1\n" \
  "v_mfma_f32_16x16x32_bf16 v[236:239], v[212:215], v[240:243], v[236:239]\n" \
  "s_waitcnt lgkmcnt(0)\n" \
  "v_mfma_f32_16x16x32_bf16 v[232:235], v[204:207], v[244:247], v[232:235]\n" \
  "v_mfma_f32_16x16x32_bf16 v[236:239], v[220:223], v[244:247], v[236:239]\n" \
  "ds_read_b128 v[240:243], v250 offset:33792\n" \
  "ds_read_b128 v[244:247], v250 offset:50176\n" \
  "s_cmp_eq_u32 %[ob], 0\n" \
  "s_cbranch_scc1 .Lo6_%=\n" \
  "s_barrier\n" \
  ".Lo6_%=:\n" \
  "v_mfma_f32_16x16x32_bf16 v[224:227], v[192:195], v[80:83], 0\n" \
  "v_mfma_f32_16x16x32_bf16 v[228:231], v[208:211], v[80:83], 0\n" \
  "v_mfma_f32_16x16x32_bf16 v[224:227], v[196:199], v[84:87], v[224:227]\n" \
  "v_cvt_pk_bf16_f32 v232, v232, v236\n" \
  "v_cvt_pk_bf16_f32 v233, v233, v237\n" \
  "v_mfma_f32_16x16x32_bf16 v[228:231], v[212:215], v[84:87], v[228:231]\n" \
  "v_cvt_pk_bf16_f32 v234, v234, v238\n" \
  "v_cvt_pk_bf16_f32 v235, v235, v239\n" \
  "v_mfma_f32_16x16x32_bf16 v[224:227], v[200:203], v[88:91], v[224:227]\n" \
  "s_add_u32 %[cnt], %[so2], 5*%c[js]+0*%c[ss]\n" \
  "buffer_store_dwordx4 v[232:235], v251, %[rs], %[cnt] offen sc1\n" \
  "v_mfma_f32_16x16x32_bf16 v[228:231], v[216:219], v[88:91], v[228:231]\n" \
  "v_mfma_f32_16x16x32_bf16 v[224:227], v[204:207], v[92:95], v[224:227]\n" \
  "v_mfma_f32_16x16x32_bf16 v[228:231], v[220:223], v[92:95], v[228:231]\n" \
  "v_mfma_f32_16x16x32_bf16 v[232:235], v[192:195], v[148:151], 0\n" \
  "v_mfma_f32_16x16x32_bf16 v[236:239], v[208:211], v[148:151], 0\n" \
  "v_mfma_f32_16x16x32_bf16 v[232:235], v[200:203], v[156:159], v[232:235]\n" \
  "v_cvt_pk_bf16_f32 v224, v224, v228\n" \
  "v_cvt_pk_bf16_f32 v225, v225, v229\n" \
  "v_mfma_f32_16x16x32_bf16 v[236:239], v[216:219], v[156:159], v[236:239]\n" \
  "v_cvt_pk_bf16_f32 v226, v226, v230\n" \
  "v_cvt_pk_bf16_f32 v227, v227, v231\n" \
  "s_waitcnt lgkmcnt(1)\n" \
  "v_mfma_f32_16x16x32_bf16 v[232:235], v[196:199], v[240:243], v[232:235]\n" \
  "s_add_u32 %[cnt], %[so2], 1*%c[js]+1*%c[ss]\n" \
  "buffer_store_dwordx4 v[224:227], v251, %[rs], %[cnt] offen sc1\n" \
  "v_mfma_f32_16x16x32_bf16 v[236:239], v[212:215], v[240:243], v[236:239]\n" \
  "s_waitcnt lgkmcnt(0)\n" \
  "v_mfma_f32_16x16x32_bf16 v[232:235], v[204:207], v[244:247], v[232:235]\n" \
  "v_mfma_f32_16x16x32_bf16 v[236:239], v[220:223], v[244:247], v[236:239]\n" \
  "ds_read_b128 v[240:243], v249 offset:0\n" \
  "ds_read_b128 v[244:247], v249 offset:16384\n" \
  "s_cmp_eq_u32 %[ob], 0\n" \
  "s_cbranch_scc1 .Lo8_%=\n" \
  "s_barrier\n" \
  ".Lo8_%=:\n" \
  "v_mfma_f32_16x16x32_bf16 v[224:227], v[192:195], v[32:35], 0\n" \
  "v_mfma_f32_16x16x32_bf16 v[228:231], v[208:211], v[32:35], 0\n" \
  "v_mfma_f32_16x16x32_bf16 v[224:227], v[196:199], v[36:39], v[224:227]\n" \
  "v_cvt_pk_bf16_f32 v232, v232, v236\n" \
  "v_cvt_pk_bf16_f32 v233, v233, v237\n" \
  "v_mfma_f32_16x16x32_bf16 v[228:231], v[212:215], v[36:39], v[228:231]\n" \
  "v_cvt_pk_bf16_f32 v234, v234, v238\n" \
  "v_cvt_pk_bf16_f32 v235, v235, v239\n" \
  "v_mfma_f32_16x16x32_bf16 v[224:227], v[200:203], v[40:43], v[224:227]\n" \
  "s_add_u32 %[cnt], %[so2], 5*%c[js]+1*%c[ss]\n" \
  "buffer_store_dwordx4 v[232:235], v251, %[rs], %[cnt] offen sc1\n" \
  "v_mfma_f32_16x16x32_bf16 v[228:231], v[216:219], v[40:43], v[228:231]\n" \
  "v_mfma_f32_16x16x32_bf16 v[224:227], v[204:207], v[44:47], v[224:227]\n" \
  "v_mfma_f32_16x16x32_bf16 v[228:231], v[220:223], v[44:47], v[228:231]\n" \
  "v_mfma_f32_16x16x32_bf16 v[232:235], v[192:195], v[160:163], 0\n" \
  "v_mfma_f32_16x16x32_bf16 v[236:239], v[208:211], v[160:163], 0\n" \
  "v_mfma_f32_16x16x32_bf16 v[232:235], v[200:203], v[168:171], v[232:235]\n" \
  "v_cvt_pk_bf16_f32 v224, v224, v228\n" \
  "v_cvt_pk_bf16_f32 v225, v225, v229\n" \
  "v_mfma_f32_16x16x32_bf16 v[236:239], v[216:219], v[168:171], v[236:239]\n" \
  "v_cvt_pk_bf16_f32 v226, v226, v230\n" \
  "v_cvt_pk_bf16_f32 v227, v227, v231\n" \
  "s_waitcnt lgkmcnt(1)\n" \
  "v_mfma_f32_16x16x32_bf16 v[232:235], v[196:199], v[240:243], v[232:235]\n" \
  "s_add_u32 %[cnt], %[so], 2*%c[js]+0*%c[ss]\n" \
  "buffer_store_dwordx4 v[224:227], v251, %[rs], %[cnt] offen sc1\n" \
  "v_mfma_f32_16x16x32_bf16 v[236:239], v[212:215], v[240:243], v[236:239]\n" \
  "s_waitcnt lgkmcnt(0)\n" \
  "v_mfma_f32_16x16x32_bf16 v[232:235], v[204:207], v[244:247], v[232:235]\n" \
  "v_mfma_f32_16x16x32_bf16 v[236:239], v[220:223], v[244:247], v[236:239]\n" \
  "ds_read_b128 v[240:243], v249 offset:1024\n" \
  "ds_read_b128 v[244:247], v249 offset:17408\n" \
  "v_mfma_f32_16x16x32_bf16 v[224:227], v[192:195], v[96:99], 0\n" \
  "v_mfma_f32_16x16x32_bf16 v[228:231], v[208:211], v[96:99], 0\n" \
  "v_mfma_f32_16x16x32_bf16 v[224:227], v[196:199], v[100:103], v[224:227]\n" \
  "v_cvt_pk_bf16_f32 v232, v232, v236\n" \
  "v_cvt_pk_bf16_f32 v233, v233, v237\n" \
  "v_mfma_f32_16x16x32_bf16 v[228:231], v[212:215], v[100:103], v[228:231]\n" \
  "v_cvt_pk_bf16_f32 v234, v234, v238\n" \
  "v_cvt_pk_bf16_f32 v235, v235, v239\n" \
  "v_mfma_f32_16x16x32_bf16 v[224:227], v[200:203], v[104:107], v[224:227]\n" \
  "s_add_u32 %[cnt], %[so], 6*%c[js]+0*%c[ss]\n" \
  "buffer_store_dwordx4 v[232:235], v251, %[rs], %[cnt] offen sc1\n" \
  "v_mfma_f32_16x16x32_bf16 v[228:231], v[216:219], v[104:107], v[228:231]\n" \
  "v_mfma_f32_16x16x32_bf16 v[224:227], v[204:207], v[108:111], v[224:227]\n" \
  "v_mfma_f32_16x16x32_bf16 v[228:231], v[220:223], v[108:111], v[228:231]\n" \
  "v_mfma_f32_16x16x32_bf16 v[232:235], v[192:195], v[164:167], 0\n" \
  "v_mfma_f32_16x16x32_bf16 v[236:239], v[208:211], v[164:167], 0\n" \
  "v_mfma_f32_16x16x32_bf16 v[232:235], v[200:203], v[172:175], v[232:235]\n" \
  "v_cvt_pk_bf16_f32 v224, v224, v228\n" \
  "v_cvt_pk_bf16_f32 v225, v225, v229\n" \
  "v_mfma_f32_16x16x32_bf16 v[236:239], v[216:219], v[172:175], v[236:239]\n" \
  "v_cvt_pk_bf16_f32 v226, v226, v230\n" \
  "v_cvt_pk_bf16_f32 v227, v227, v231\n" \
  "s_waitcnt lgkmcnt(1)\n" \
  "v_mfma_f32_16x16x32_bf16 v[232:235], v[196:199], v[240:243], v[232:235]\n" \
  "s_add_u32 %[cnt], %[so], 2*%c[js]+1*%c[ss]\n" \
  "buffer_store_dwordx4 v[224:227], v251, %[rs], %[cnt] offen sc1\n" \
  "v_mfma_f32_16x16x32_bf16 v[236:239], v[212:215], v[240:243], v[236:239]\n" \
  "s_waitcnt lgkmcnt(0)\n" \
  "v_mfma_f32_16x16x32_bf16 v[232:235], v[204:207], v[244:247], v[232:235]\n" \
  "v_mfma_f32_16x16x32_bf16 v[236:239], v[220:223], v[244:247], v[236:239]\n" \
  "ds_read_b128 v[240:243], v249 offset:32768\n" \
  "ds_read_b128 v[244:247], v249 offset:49152\n" \
  "s_cmp_eq_u32 %[ob], 0\n" \
  "s_cbranch_scc1 .Lo12_%=\n" \
  "s_barrier\n" \
  ".Lo12_%=:\n" \
  "v_mfma_f32_16x16x32_bf16 v[224:227], v[192:195], v[48:51], 0\n" \
  "v_mfma_f32_16x16x32_bf16 v[228:231], v[208:211], v[48:51], 0\n" \
  "v_mfma_f32_16x16x32_bf16 v[224:227], v[196:199], v[52:55], v[224:227]\n" \
  "v_cvt_pk_bf16_f32 v232, v232, v236\n" \
  "v_cvt_pk_bf16_f32 v233, v233, v237\n" \
  "v_mfma_f32_16x16x32_bf16 v[228:231], v[212:215], v[52:55], v[228:231]\n" \
  "v_cvt_pk_bf16_f32 v234, v234, v238\n" \
  "v_cvt_pk_bf16_f32 v235, v235, v239\n" \
  "v_mfma_f32_16x16x32_bf16 v[224:227], v[200:203], v[56:59], v[224:227]\n" \
  "s_add_u32 %[cnt], %[so], 6*%c[js]+1*%c[ss]\n" \
  "buffer_store_dwordx4 v[232:235], v251, %[rs], %[cnt] offen sc1\n" \
  "v_mfma_f32_16x16x32_bf16 v[228:231], v[216:219], v[56:59], v[228:231]\n" \
  "v_mfma_f32_16x16x32_bf16 v[224:227], v[204:207], v[60:63], v[224:227]\n" \
  "v_mfma_f32_16x16x32_bf16 v[228:231], v[220:223], v[60:63], v[228:231]\n" \
  "v_mfma_f32_16x16x32_bf16 v[232:235], v[192:195], v[176:179], 0\n" \
  "v_mfma_f32_16x16x32_bf16 v[236:239], v[208:211], v[176:179], 0\n" \
  "v_mfma_f32_16x16x32_bf16 v[232:235], v[200:203], v[184:187], v[232:235]\n" \
  "v_cvt_pk_bf16_f32 v224, v224, v228\n" \
  "v_cvt_pk_bf16_f32 v225, v225, v229\n" \
  "v_mfma_f32_16x16x32_bf16 v[236:239], v[216:219], v[184:187], v[236:239]\n" \
  "v_cvt_pk_bf16_f32 v226, v226, v230\n" \
  "v_cvt_pk_bf16_f32 v227, v227, v231\n" \
  "s_waitcnt lgkmcnt(1)\n" \
  "v_mfma_f32_16x16x32_bf16 v[232:235], v[196:199], v[240:243], v[232:235]\n" \
  "s_add_u32 %[cnt], %[so2], 3*%c[js]+0*%c[ss]\n" \
  "buffer_store_dwordx4 v[224:227], v251, %[rs], %[cnt] offen sc1\n" \
  "v_mfma_f32_16x16x32_bf16 v[236:239], v[212:215], v[240:243], v[236:239]\n" \
  "s_waitcnt lgkmcnt(0)\n" \
  "v_mfma_f32_16x16x32_bf16 v[232:235], v[204:207], v[244:247], v[232:235]\n" \
  "v_mfma_f32_16x16x32_bf16 v[236:239], v[220:223], v[244:247], v[236:239]\n" \
  "ds_read_b128 v[240:243], v249 offset:33792\n" \
  "ds_read_b128 v[244:247], v249 offset:50176\n" \
  "v_mfma_f32_16x16x32_bf16 v[224:227], v[192:195], v[112:115], 0\n" \
  "v_mfma_f32_16x16x32_bf16 v[228:231], v[208:211], v[112:115], 0\n" \
  "v_mfma_f32_16x16x32_bf16 v[224:227], v[196:199], v[116:119], v[224:227]\n" \
  "v_cvt_pk_bf16_f32 v232, v232, v236\n" \
  "v_cvt_pk_bf16_f32 v233, v233, v237\n" \
  "v_mfma_f32_16x16x32_bf16 v[228:231], v[212:215], v[116:119], v[228:231]\n" \
  "v_cvt_pk_bf16_f32 v234, v234, v238\n" \
  "v_cvt_pk_bf16_f32 v235, v235, v239\n" \
  "v_mfma_f32_16x16x32_bf16 v[224:227], v[200:203], v[120:123], v[224:227]\n" \
  "s_add_u32 %[cnt], %[so2], 7*%c[js]+0*%c[ss]\n" \
  "buffer_store_dwordx4 v[232:235], v251, %[rs], %[cnt] offen sc1\n" \
  "v_mfma_f32_16x16x32_bf16 v[228:231], v[216:219], v[120:123], v[228:231]\n" \
  "v_mfma_f32_16x16x32_bf16 v[224:227], v[204:207], v[124:127], v[224:227]\n" \
  "v_mfma_f32_16x16x32_bf16 v[228:231], v[220:223], v[124:127], v[228:231]\n" \
  "v_mfma_f32_16x16x32_bf16 v[232:235], v[192:195], v[180:183], 0\n" \
  "v_mfma_f32_16x16x32_bf16 v[236:239], v[208:211], v[180:183], 0\n" \
  "v_mfma_f32_16x16x32_bf16 v[232:235], v[200:203], v[188:191], v[232:235]\n" \
  "v_cvt_pk_bf16_f32 v224, v224, v228\n" \
  "v_cvt_pk_bf16_f32 v225, v225, v229\n" \
  "v_mfma_f32_16x16x32_bf16 v[236:239], v[216:219], v[188:191], v[236:239]\n" \
  "v_cvt_pk_bf16_f32 v226, v226, v230\n" \
  "v_cvt_pk_bf16_f32 v227, v227, v231\n" \
  "s_waitcnt lgkmcnt(1)\n" \
  "v_mfma_f32_16x16x32_bf16 v[232:235], v[196:199], v[240:243], v[232:235]\n" \
  "s_add_u32 %[cnt], %[so2], 3*%c[js]+1*%c[ss]\n" \
  "buffer_store_dwordx4 v[224:227], v251, %[rs], %[cnt] offen sc1\n" \
  "v_mfma_f32_16x16x32_bf16 v[236:239], v[212:215], v[240:243], v[236:239]\n" \
  "s_waitcnt lgkmcnt(0)\n" \
  "v_mfma_f32_16x16x32_bf16 v[232:235], v[204:207], v[244:247], v[232:235]\n" \
  "v_mfma_f32_16x16x32_bf16 v[236:239], v[220:223], v[244:247], v[236:239]\n" \
  "s_nop 8\n" \
  "v_cvt_pk_bf16_f32 v232, v232, v236\n" \
  "v_cvt_pk_bf16_f32 v233, v233, v237\n" \
  "v_cvt_pk_bf16_f32 v234, v234, v238\n" \
  "v_cvt_pk_bf16_f32 v235, v235, v239\n" \
  "s_add_u32 %[cnt], %[so2], 7*%c[js]+1*%c[ss]\n" \
  "buffer_store_dwordx4 v[232:235], v251, %[rs], %[cnt] offen sc1\n" \
  "s_setprio 0\n" \
  "s_nop 1\n"
#define PRO0_0 \
  "s_cmp_lg_u32 %[hb], 0\n" \
  "s_cbranch_scc1 .Ls_%=\n" \
  "v_xor_b32 %[t0], 0x0, %[pb]\n" \
  "ds_read_b64_tr_b16 v[12:13], %[t0] offset:0\n" \
  "ds_read_b64_tr_b16 v[96:97], %[t0] offset:16384\n" \
  "v_xor_b32 %[t1], 0x20, %[pb]\n" \
  "ds_read_b64_tr_b16 v[28:29], %[t1] offset:0\n" \
  "ds_read_b64_tr_b16 v[144:145], %[t1] offset:16384\n" \
  "v_xor_b32 %[t0], 0x1000, %[pb]\n" \
  "ds_read_b64_tr_b16 v[0:1], %[t0] offset:0\n" \
  "ds_read_b64_tr_b16 v[192:193], %[t0] offset:16384\n" \
  "v_xor_b32 %[t1], 0x1020, %[pb]\n" \
  "ds_read_b64_tr_b16 v[48:49], %[t1] offset:0\n" \
  "ds_read_b64_tr_b16 v[200:201], %[t1] offset:16384\n" \
  "v_xor_b32 %[t0], 0x2000, %[pb]\n" \
  "ds_read_b64_tr_b16 v[16:17], %[t0] offset:0\n" \
  "ds_read_b64_tr_b16 v[208:209], %[t0] offset:16384\n" \
  "v_xor_b32 %[t1], 0x2020, %[pb]\n" \
  "ds_read_b64_tr_b16 v[64:65], %[t1] offset:0\n" \
  "ds_read_b64_tr_b16 v[216:217], %[t1] offset:16384\n" \
  "v_xor_b32 %[t0], 0x3000, %[pb]\n" \
  "ds_read_b64_tr_b16 v[32:33], %[t0] offset:0\n" \
  "ds_read_b64_tr_b16 v[128:129], %[t0] offset:16384\n" \
  "v_xor_b32 %[t1], 0x3020, %[pb]\n" \
  "ds_read_b64_tr_b16 v[80:81], %[t1] offset:0\n" \
  "ds_read_b64_tr_b16 v[176:177], %[t1] offset:16384\n" \
  ".Ls_%=:\n" \
  "s_waitcnt lgkmcnt(0)\n"
#define PRO0_1 \
  "s_cmp_lg_u32 %[hb], 1\n" \
  "s_cbranch_scc1 .Ls_%=\n" \
  "v_xor_b32 %[t0], 0x8000, %[pb]\n" \
  "ds_read_b64_tr_b16 v[12:13], %[t0] offset:0\n" \
  "ds_read_b64_tr_b16 v[96:97], %[t0] offset:16384\n" \
  "v_xor_b32 %[t1], 0x8020, %[pb]\n" \
  "ds_read_b64_tr_b16 v[28:29], %[t1] offset:0\n" \
  "ds_read_b64_tr_b16 v[144:145], %[t1] offset:16384\n" \
  "v_xor_b32 %[t0], 0x9000, %[pb]\n" \
  "ds_read_b64_tr_b16 v[0:1], %[t0] offset:0\n" \
  "ds_read_b64_tr_b16 v[192:193], %[t0] offset:16384\n" \
  "v_xor_b32 %[t1], 0x9020, %[pb]\n" \
  "ds_read_b64_tr_b16 v[48:49], %[t1] offset:0\n" \
  "ds_read_b64_tr_b16 v[200:201], %[t1] offset:16384\n" \
  "v_xor_b32 %[t0], 0xa000, %[pb]\n" \
  "ds_read_b64_tr_b16 v[16:17], %[t0] offset:0\n" \
  "ds_read_b64_tr_b16 v[208:209], %[t0] offset:16384\n" \
  "v_xor_b32 %[t1], 0xa020, %[pb]\n" \
  "ds_read_b64_tr_b16 v[64:65], %[t1] offset:0\n" \
  "ds_read_b64_tr_b16 v[216:217], %[t1] offset:16384\n" \
  "v_xor_b32 %[t0], 0xb000, %[pb]\n" \
  "ds_read_b64_tr_b16 v[32:33], %[t0] offset:0\n" \
  "ds_read_b64_tr_b16 v[128:129], %[t0] offset:16384\n" \
  "v_xor_b32 %[t1], 0xb020, %[pb]\n" \
  "ds_read_b64_tr_b16 v[80:81], %[t1] offset:0\n" \
  "ds_read_b64_tr_b16 v[176:177], %[t1] offset:16384\n" \
  ".Ls_%=:\n" \
  "s_waitcnt lgkmcnt(0)\n"
#define PRO0_2 \
  "s_cmp_lg_u32 %[hb], 0\n" \
  "s_cbranch_scc1 .Ls_%=\n" \
  "v_xor_b32 %[t0], 0x10000, %[pb]\n" \
  "ds_read_b64_tr_b16 v[14:15], %[t0] offset:0\n" \
  "ds_read_b64_tr_b16 v[98:99], %[t0] offset:16384\n" \
  "v_xor_b32 %[t1], 0x10020, %[pb]\n" \
  "ds_read_b64_tr_b16 v[30:31], %[t1] offset:0\n" \
  "ds_read_b64_tr_b16 v[146:147], %[t1] offset:16384\n" \
  "v_xor_b32 %[t0], 0x11000, %[pb]\n" \
  "ds_read_b64_tr_b16 v[2:3], %[t0] offset:0\n" \
  "ds_read_b64_tr_b16 v[194:195], %[t0] offset:16384\n" \
  "v_xor_b32 %[t1], 0x11020, %[pb]\n" \
  "ds_read_b64_tr_b16 v[50:51], %[t1] offset:0\n" \
  "ds_read_b64_tr_b16 v[202:203], %[t1] offset:16384\n" \
  "v_xor_b32 %[t0], 0x12000, %[pb]\n" \
  "ds_read_b64_tr_b16 v[18:19], %[t0] offset:0\n" \
  "ds_read_b64_tr_b16 v[210:211], %[t0] offset:16384\n" \
  "v_xor_b32 %[t1], 0x12020, %[pb]\n" \
  "ds_read_b64_tr_b16 v[66:67], %[t1] offset:0\n" \
  "ds_read_b64_tr_b16 v[218:219], %[t1] offset:16384\n" \
  "v_xor_b32 %[t0], 0x13000, %[pb]\n" \
  "ds_read_b64_tr_b16 v[34:35], %[t0] offset:0\n" \
  "ds_read_b64_tr_b16 v[130:131], %[t0] offset:16384\n" \
  "v_xor_b32 %[t1], 0x13020, %[pb]\n" \
  "ds_read_b64_tr_b16 v[82:83], %[t1] offset:0\n" \
  "ds_read_b64_tr_b16 v[178:179], %[t1] offset:16384\n" \
  ".Ls_%=:\n" \
  "s_waitcnt lgkmcnt(0)\n"
#define PRO0_3 \
  "s_cmp_lg_u32 %[hb], 1\n" \
  "s_cbranch_scc1 .Ls_%=\n" \
  "v_xor_b32 %[t0], 0x18000, %[pb]\n" \
  "ds_read_b64_tr_b16 v[14:15], %[t0] offset:0\n" \
  "ds_read_b64_tr_b16 v[98:99], %[t0] offset:16384\n" \
  "v_xor_b32 %[t1], 0x18020, %[pb]\n" \
  "ds_read_b64_tr_b16 v[30:31], %[t1] offset:0\n" \
  "ds_read_b64_tr_b16 v[146:147], %[t1] offset:16384\n" \
  "v_xor_b32 %[t0], 0x19000, %[pb]\n" \
  "ds_read_b64_tr_b16 v[2:3], %[t0] offset:0\n" \
  "ds_read_b64_tr_b16 v[194:195], %[t0] offset:16384\n" \
  "v_xor_b32 %[t1], 0x19020, %[pb]\n" \
  "ds_read_b64_tr_b16 v[50:51], %[t1] offset:0\n" \
  "ds_read_b64_tr_b16 v[202:203], %[t1] offset:16384\n" \
  "v_xor_b32 %[t0], 0x1a000, %[pb]\n" \
  "ds_read_b64_tr_b16 v[18:19], %[t0] offset:0\n" \
  "ds_read_b64_tr_b16 v[210:211], %[t0] offset:16384\n" \
  "v_xor_b32 %[t1], 0x1a020, %[pb]\n" \
  "ds_read_b64_tr_b16 v[66:67], %[t1] offset:0\n" \
  "ds_read_b64_tr_b16 v[218:219], %[t1] offset:16384\n" \
  "v_xor_b32 %[t0], 0x1b000, %[pb]\n" \
  "ds_read_b64_tr_b16 v[34:35], %[t0] offset:0\n" \
  "ds_read_b64_tr_b16 v[130:131], %[t0] offset:16384\n" \
  "v_xor_b32 %[t1], 0x1b020, %[pb]\n" \
  "ds_read_b64_tr_b16 v[82:83], %[t1] offset:0\n" \
  "ds_read_b64_tr_b16 v[178:179], %[t1] offset:16384\n" \
  ".Ls_%=:\n" \
  "s_waitcnt lgkmcnt(0)\n"
#define PRO0_4 \
  "s_cmp_lg_u32 %[hb], 0\n" \
  "s_cbranch_scc1 .Ls_%=\n" \
  "v_xor_b32 %[t0], 0x20000, %[pb]\n" \
  "ds_read_b64_tr_b16 v[60:61], %[t0] offset:0\n" \
  "ds_read_b64_tr_b16 v[100:101], %[t0] offset:16384\n" \
  "v_xor_b32 %[t1], 0x20020, %[pb]\n" \
  "ds_read_b64_tr_b16 v[76:77], %[t1] offset:0\n" \
  "ds_read_b64_tr_b16 v[148:149], %[t1] offset:16384\n" \
  "v_xor_b32 %[t0], 0x21000, %[pb]\n" \
  "ds_read_b64_tr_b16 v[4:5], %[t0] offset:0\n" \
  "ds_read_b64_tr_b16 v[196:197], %[t0] offset:16384\n" \
  "v_xor_b32 %[t1], 0x21020, %[pb]\n" \
  "ds_read_b64_tr_b16 v[52:53], %[t1] offset:0\n" \
  "ds_read_b64_tr_b16 v[204:205], %[t1] offset:16384\n" \
  "v_xor_b32 %[t0], 0x22000, %[pb]\n" \
  "ds_read_b64_tr_b16 v[20:21], %[t0] offset:0\n" \
  "ds_read_b64_tr_b16 v[212:213], %[t0] offset:16384\n" \
  "v_xor_b32 %[t1], 0x22020, %[pb]\n" \
  "ds_read_b64_tr_b16 v[68:69], %[t1] offset:0\n" \
  "ds_read_b64_tr_b16 v[220:221], %[t1] offset:16384\n" \
  "v_xor_b32 %[t0], 0x23000, %[pb]\n" \
  "ds_read_b64_tr_b16 v[36:37], %[t0] offset:0\n" \
  "ds_read_b64_tr_b16 v[132:133], %[t0] offset:16384\n" \
  "v_xor_b32 %[t1], 0x23020, %[pb]\n" \
  "ds_read_b64_tr_b16 v[84:85], %[t1] offset:0\n" \
  "ds_read_b64_tr_b16 v[180:181], %[t1] offset:16384\n" \
  ".Ls_%=:\n" \
  "s_waitcnt lgkmcnt(0)\n"
#define PRO0_5 \
  "s_cmp_lg_u32 %[hb], 1\n" \
  "s_cbranch_scc1 .Ls_%=\n" \
  "v_xor_b32 %[t0], 0x0, %[pb]\n" \
  "ds_read_b64_tr_b16 v[60:61], %[t0] offset:0\n" \
  "ds_read_b64_tr_b16 v[100:101], %[t0] offset:16384\n" \
  "v_xor_b32 %[t1], 0x20, %[pb]\n" \
  "ds_read_b64_tr_b16 v[76:77], %[t1] offset:0\n" \
  "ds_read_b64_tr_b16 v[148:149], %[t1] offset:16384\n" \
  "v_xor_b32 %[t0], 0x1000, %[pb]\n" \
  "ds_read_b64_tr_b16 v[4:5], %[t0] offset:0\n" \
  "ds_read_b64_tr_b16 v[196:197], %[t0] offset:16384\n" \
  "v_xor_b32 %[t1], 0x1020, %[pb]\n" \
  "ds_read_b64_tr_b16 v[52:53], %[t1] offset:0\n" \
  "ds_read_b64_tr_b16 v[204:205], %[t1] offset:16384\n" \
  "v_xor_b32 %[t0], 0x2000, %[pb]\n" \
  "ds_read_b64_tr_b16 v[20:21], %[t0] offset:0\n" \
  "ds_read_b64_tr_b16 v[212:213], %[t0] offset:16384\n" \
  "v_xor_b32 %[t1], 0x2020, %[pb]\n" \
  "ds_read_b64_tr_b16 v[68:69], %[t1] offset:0\n" \
  "ds_read_b64_tr_b16 v[220:221], %[t1] offset:16384\n" \
  "v_xor_b32 %[t0], 0x3000, %[pb]\n" \
  "ds_read_b64_tr_b16 v[36:37], %[t0] offset:0\n" \
  "ds_read_b64_tr_b16 v[132:133], %[t0] offset:16384\n" \
  "v_xor_b32 %[t1], 0x3020, %[pb]\n" \
  "ds_read_b64_tr_b16 v[84:85], %[t1] offset:0\n" \
  "ds_read_b64_tr_b16 v[180:181], %[t1] offset:16384\n" \
  ".Ls_%=:\n" \
  "s_waitcnt lgkmcnt(0)\n"
#define PRO0_6 \
  "s_cmp_lg_u32 %[hb], 0\n" \
  "s_cbranch_scc1 .Ls_%=\n" \
  "v_xor_b32 %[t0], 0x8000, %[pb]\n" \
  "ds_read_b64_tr_b16 v[62:63], %[t0] offset:0\n" \
  "ds_read_b64_tr_b16 v[102:103], %[t0] offset:16384\n" \
  "v_xor_b32 %[t1], 0x8020, %[pb]\n" \
  "ds_read_b64_tr_b16 v[78:79], %[t1] offset:0\n" \
  "ds_read_b64_tr_b16 v[150:151], %[t1] offset:16384\n" \
  "v_xor_b32 %[t0], 0x9000, %[pb]\n" \
  "ds_read_b64_tr_b16 v[6:7], %[t0] offset:0\n" \
  "ds_read_b64_tr_b16 v[198:199], %[t0] offset:16384\n" \
  "v_xor_b32 %[t1], 0x9020, %[pb]\n" \
  "ds_read_b64_tr_b16 v[54:55], %[t1] offset:0\n" \
  "ds_read_b64_tr_b16 v[206:207], %[t1] offset:16384\n" \
  "v_xor_b32 %[t0], 0xa000, %[pb]\n" \
  "ds_read_b64_tr_b16 v[22:23], %[t0] offset:0\n" \
  "ds_read_b64_tr_b16 v[214:215], %[t0] offset:16384\n" \
  "v_xor_b32 %[t1], 0xa020, %[pb]\n" \
  "ds_read_b64_tr_b16 v[70:71], %[t1] offset:0\n" \
  "ds_read_b64_tr_b16 v[222:223], %[t1] offset:16384\n" \
  "v_xor_b32 %[t0], 0xb000, %[pb]\n" \
  "ds_read_b64_tr_b16 v[38:39], %[t0] offset:0\n" \
  "ds_read_b64_tr_b16 v[134:135], %[t0] offset:16384\n" \
  "v_xor_b32 %[t1], 0xb020, %[pb]\n" \
  "ds_read_b64_tr_b16 v[86:87], %[t1] offset:0\n" \
  "ds_read_b64_tr_b16 v[182:183], %[t1] offset:16384\n" \
  ".Ls_%=:\n" \
  "s_waitcnt lgkmcnt(0)\n"
#define PRO0_7 \
  "s_cmp_lg_u32 %[hb], 1\n" \
  "s_cbranch_scc1 .Ls_%=\n" \
  "v_xor_b32 %[t0], 0x10000, %[pb]\n" \
  "ds_read_b64_tr_b16 v[62:63], %[t0] offset:0\n" \
  "ds_read_b64_tr_b16 v[102:103], %[t0] offset:16384\n" \
  "v_xor_b32 %[t1], 0x10020, %[pb]\n" \
  "ds_read_b64_tr_b16 v[78:79], %[t1] offset:0\n" \
  "ds_read_b64_tr_b16 v[150:151], %[t1] offset:16384\n" \
  "v_xor_b32 %[t0], 0x11000, %[pb]\n" \
  "ds_read_b64_tr_b16 v[6:7], %[t0] offset:0\n" \
  "ds_read_b64_tr_b16 v[198:199], %[t0] offset:16384\n" \
  "v_xor_b32 %[t1], 0x11020, %[pb]\n" \
  "ds_read_b64_tr_b16 v[54:55], %[t1] offset:0\n" \
  "ds_read_b64_tr_b16 v[206:207], %[t1] offset:16384\n" \
  "v_xor_b32 %[t0], 0x12000, %[pb]\n" \
  "ds_read_b64_tr_b16 v[22:23], %[t0] offset:0\n" \
  "ds_read_b64_tr_b16 v[214:215], %[t0] offset:16384\n" \
  "v_xor_b32 %[t1], 0x12020, %[pb]\n" \
  "ds_read_b64_tr_b16 v[70:71], %[t1] offset:0\n" \
  "ds_read_b64_tr_b16 v[222:223], %[t1] offset:16384\n" \
  "v_xor_b32 %[t0], 0x13000, %[pb]\n" \
  "ds_read_b64_tr_b16 v[38:39], %[t0] offset:0\n" \
  "ds_read_b64_tr_b16 v[134:135], %[t0] offset:16384\n" \
  "v_xor_b32 %[t1], 0x13020, %[pb]\n" \
  "ds_read_b64_tr_b16 v[86:87], %[t1] offset:0\n" \
  "ds_read_b64_tr_b16 v[182:183], %[t1] offset:16384\n" \
  ".Ls_%=:\n" \
  "s_waitcnt lgkmcnt(0)\n"
#define PRO0_8 \
  "s_cmp_lg_u32 %[hb], 0\n" \
  "s_cbranch_scc1 .Ls_%=\n" \
  "v_xor_b32 %[t0], 0x18000, %[pb]\n" \
  "ds_read_b64_tr_b16 v[224:225], %[t0] offset:0\n" \
  "ds_read_b64_tr_b16 v[104:105], %[t0] offset:16384\n" \
  "v_xor_b32 %[t1], 0x18020, %[pb]\n" \
  "ds_read_b64_tr_b16 v[232:233], %[t1] offset:0\n" \
  "ds_read_b64_tr_b16 v[152:153], %[t1] offset:16384\n" \
  "v_xor_b32 %[t0], 0x19000, %[pb]\n" \
  "ds_read_b64_tr_b16 v[8:9], %[t0] offset:0\n" \
  "ds_read_b64_tr_b16 v[112:113], %[t0] offset:16384\n" \
  "v_xor_b32 %[t1], 0x19020, %[pb]\n" \
  "ds_read_b64_tr_b16 v[56:57], %[t1] offset:0\n" \
  "ds_read_b64_tr_b16 v[160:161], %[t1] offset:16384\n" \
  "v_xor_b32 %[t0], 0x1a000, %[pb]\n" \
  "ds_read_b64_tr_b16 v[24:25], %[t0] offset:0\n" \
  "ds_read_b64_tr_b16 v[120:121], %[t0] offset:16384\n" \
  "v_xor_b32 %[t1], 0x1a020, %[pb]\n" \
  "ds_read_b64_tr_b16 v[72:73], %[t1] offset:0\n" \
  "ds_read_b64_tr_b16 v[168:169], %[t1] offset:16384\n" \
  "v_xor_b32 %[t0], 0x1b000, %[pb]\n" \
  "ds_read_b64_tr_b16 v[40:41], %[t0] offset:0\n" \
  "ds_read_b64_tr_b16 v[136:137], %[t0] offset:16384\n" \
  "v_xor_b32 %[t1], 0x1b020, %[pb]\n" \
  "ds_read_b64_tr_b16 v[88:89], %[t1] offset:0\n" \
  "ds_read_b64_tr_b16 v[184:185], %[t1] offset:16384\n" \
  ".Ls_%=:\n" \
  "s_waitcnt lgkmcnt(0)\n"
#define PRO0_9 \
  "s_cmp_lg_u32 %[hb], 1\n" \
  "s_cbranch_scc1 .Ls_%=\n" \
  "v_xor_b32 %[t0], 0x20000, %[pb]\n" \
  "ds_read_b64_tr_b16 v[224:225], %[t0] offset:0\n" \
  "ds_read_b64_tr_b16 v[104:105], %[t0] offset:16384\n" \
  "v_xor_b32 %[t1], 0x20020, %[pb]\n" \
  "ds_read_b64_tr_b16 v[232:233], %[t1] offset:0\n" \
  "ds_read_b64_tr_b16 v[152:153], %[t1] offset:16384\n" \
  "v_xor_b32 %[t0], 0x21000, %[pb]\n" \
  "ds_read_b64_tr_b16 v[8:9], %[t0] offset:0\n" \
  "ds_read_b64_tr_b16 v[112:113], %[t0] offset:16384\n" \
  "v_xor_b32 %[t1], 0x21020, %[pb]\n" \
  "ds_read_b64_tr_b16 v[56:57], %[t1] offset:0\n" \
  "ds_read_b64_tr_b16 v[160:161], %[t1] offset:16384\n" \
  "v_xor_b32 %[t0], 0x22000, %[pb]\n" \
  "ds_read_b64_tr_b16 v[24:25], %[t0] offset:0\n" \
  "ds_read_b64_tr_b16 v[120:121], %[t0] offset:16384\n" \
  "v_xor_b32 %[t1], 0x22020, %[pb]\n" \
  "ds_read_b64_tr_b16 v[72:73], %[t1] offset:0\n" \
  "ds_read_b64_tr_b16 v[168:169], %[t1] offset:16384\n" \
  "v_xor_b32 %[t0], 0x23000, %[pb]\n" \
  "ds_read_b64_tr_b16 v[40:41], %[t0] offset:0\n" \
  "ds_read_b64_tr_b16 v[136:137], %[t0] offset:16384\n" \
  "v_xor_b32 %[t1], 0x23020, %[pb]\n" \
  "ds_read_b64_tr_b16 v[88:89], %[t1] offset:0\n" \
  "ds_read_b64_tr_b16 v[184:185], %[t1] offset:16384\n" \
  ".Ls_%=:\n" \
  "s_waitcnt lgkmcnt(0)\n"
#define PRO0_10 \
  "s_cmp_lg_u32 %[hb], 0\n" \
  "s_cbranch_scc1 .Ls_%=\n" \
  "v_xor_b32 %[t0], 0x0, %[pb]\n" \
  "ds_read_b64_tr_b16 v[226:227], %[t0] offset:0\n" \
  "ds_read_b64_tr_b16 v[106:107], %[t0] offset:16384\n" \
  "v_xor_b32 %[t1], 0x20, %[pb]\n" \
  "ds_read_b64_tr_b16 v[234:235], %[t1] offset:0\n" \
  "ds_read_b64_tr_b16 v[154:155], %[t1] offset:16384\n" \
  "v_xor_b32 %[t0], 0x1000, %[pb]\n" \
  "ds_read_b64_tr_b16 v[10:11], %[t0] offset:0\n" \
  "ds_read_b64_tr_b16 v[114:115], %[t0] offset:16384\n" \
  "v_xor_b32 %[t1], 0x1020, %[pb]\n" \
  "ds_read_b64_tr_b16 v[58:59], %[t1] offset:0\n" \
  "ds_read_b64_tr_b16 v[162:163], %[t1] offset:16384\n" \
  "v_xor_b32 %[t0], 0x2000, %[pb]\n" \
  "ds_read_b64_tr_b16 v[26:27], %[t0] offset:0\n" \
  "ds_read_b64_tr_b16 v[122:123], %[t0] offset:16384\n" \
  "v_xor_b32 %[t1], 0x2020, %[pb]\n" \
  "ds_read_b64_tr_b16 v[74:75], %[t1] offset:0\n" \
  "ds_read_b64_tr_b16 v[170:171], %[t1] offset:16384\n" \
  "v_xor_b32 %[t0], 0x3000, %[pb]\n" \
  "ds_read_b64_tr_b16 v[42:43], %[t0] offset:0\n" \
  "ds_read_b64_tr_b16 v[138:139], %[t0] offset:16384\n" \
  "v_xor_b32 %[t1], 0x3020, %[pb]\n" \
  "ds_read_b64_tr_b16 v[90:91], %[t1] offset:0\n" \
  "ds_read_b64_tr_b16 v[186:187], %[t1] offset:16384\n" \
  ".Ls_%=:\n" \
  "s_waitcnt lgkmcnt(0)\n"
#define PRO0_11 \
  "s_cmp_lg_u32 %[hb], 1\n" \
  "s_cbranch_scc1 .Ls_%=\n" \
  "v_xor_b32 %[t0], 0x8000, %[pb]\n" \
  "ds_read_b64_tr_b16 v[226:227], %[t0] offset:0\n" \
  "ds_read_b64_tr_b16 v[106:107], %[t0] offset:16384\n" \
  "v_xor_b32 %[t1], 0x8020, %[pb]\n" \
  "ds_read_b64_tr_b16 v[234:235], %[t1] offset:0\n" \
  "ds_read_b64_tr_b16 v[154:155], %[t1] offset:16384\n" \
  "v_xor_b32 %[t0], 0x9000, %[pb]\n" \
  "ds_read_b64_tr_b16 v[10:11], %[t0] offset:0\n" \
  "ds_read_b64_tr_b16 v[114:115], %[t0] offset:16384\n" \
  "v_xor_b32 %[t1], 0x9020, %[pb]\n" \
  "ds_read_b64_tr_b16 v[58:59], %[t1] offset:0\n" \
  "ds_read_b64_tr_b16 v[162:163], %[t1] offset:16384\n" \
  "v_xor_b32 %[t0], 0xa000, %[pb]\n" \
  "ds_read_b64_tr_b16 v[26:27], %[t0] offset:0\n" \
  "ds_read_b64_tr_b16 v[122:123], %[t0] offset:16384\n" \
  "v_xor_b32 %[t1], 0xa020, %[pb]\n" \
  "ds_read_b64_tr_b16 v[74:75], %[t1] offset:0\n" \
  "ds_read_b64_tr_b16 v[170:171], %[t1] offset:16384\n" \
  "v_xor_b32 %[t0], 0xb000, %[pb]\n" \
  "ds_read_b64_tr_b16 v[42:43], %[t0] offset:0\n" \
  "ds_read_b64_tr_b16 v[138:139], %[t0] offset:16384\n" \
  "v_xor_b32 %[t1], 0xb020, %[pb]\n" \
  "ds_read_b64_tr_b16 v[90:91], %[t1] offset:0\n" \
  "ds_read_b64_tr_b16 v[186:187], %[t1] offset:16384\n" \
  ".Ls_%=:\n" \
  "s_waitcnt lgkmcnt(0)\n"
#define PRO0_12 \
  "v_add_u32 %[t0], 0x8000, %[b0]\n" \
  "ds_write_b128 %[t0], v[12:15]\n" \
  "ds_write_b128 %[t0], v[60:63] offset:1024\n" \
  "v_add_u32 %[t0], 0xc000, %[b0]\n" \
  "ds_write_b128 %[t0], v[28:31]\n" \
  "ds_write_b128 %[t0], v[76:79] offset:1024\n" \
  "s_cmp_lg_u32 %[hb], 0\n" \
  "s_cbranch_scc1 .Ls_%=\n" \
  "v_xor_b32 %[t0], 0x10000, %[pb]\n" \
  "ds_read_b64_tr_b16 v[228:229], %[t0] offset:0\n" \
  "ds_read_b64_tr_b16 v[108:109], %[t0] offset:16384\n" \
  "v_xor_b32 %[t1], 0x10020, %[pb]\n" \
  "ds_read_b64_tr_b16 v[236:237], %[t1] offset:0\n" \
  "ds_read_b64_tr_b16 v[156:157], %[t1] offset:16384\n" \
  "v_xor_b32 %[t0], 0x11000, %[pb]\n" \
  "ds_read_b64_tr_b16 v[12:13], %[t0] offset:0\n" \
  "ds_read_b64_tr_b16 v[116:117], %[t0] offset:16384\n" \
  "v_xor_b32 %[t1], 0x11020, %[pb]\n" \
  "ds_read_b64_tr_b16 v[60:61], %[t1] offset:0\n" \
  "ds_read_b64_tr_b16 v[164:165], %[t1] offset:16384\n" \
  "v_xor_b32 %[t0], 0x12000, %[pb]\n" \
  "ds_read_b64_tr_b16 v[28:29], %[t0] offset:0\n" \
  "ds_read_b64_tr_b16 v[124:125], %[t0] offset:16384\n" \
  "v_xor_b32 %[t1], 0x12020, %[pb]\n" \
  "ds_read_b64_tr_b16 v[76:77], %[t1] offset:0\n" \
  "ds_read_b64_tr_b16 v[172:173], %[t1] offset:16384\n" \
  "v_xor_b32 %[t0], 0x13000, %[pb]\n" \
  "ds_read_b64_tr_b16 v[44:45], %[t0] offset:0\n" \
  "ds_read_b64_tr_b16 v[140:141], %[t0] offset:16384\n" \
  "v_xor_b32 %[t1], 0x13020, %[pb]\n" \
  "ds_read_b64_tr_b16 v[92:93], %[t1] offset:0\n" \
  "ds_read_b64_tr_b16 v[188:189], %[t1] offset:16384\n" \
  ".Ls_%=:\n" \
  "s_waitcnt lgkmcnt(0)\n"
#define PRO0_13 \
  "v_add_u32 %[t0], 0x10000, %[b0]\n" \
  "ds_write_b128 %[t0], v[192:195]\n" \
  "ds_write_b128 %[t0], v[196:199] offset:1024\n" \
  "v_add_u32 %[t0], 0x14000, %[b0]\n" \
  "ds_write_b128 %[t0], v[200:203]\n" \
  "ds_write_b128 %[t0], v[204:207] offset:1024\n" \
  "s_cmp_lg_u32 %[hb], 1\n" \
  "s_cbranch_scc1 .Ls_%=\n" \
  "v_xor_b32 %[t0], 0x18000, %[pb]\n" \
  "ds_read_b64_tr_b16 v[228:229], %[t0] offset:0\n" \
  "ds_read_b64_tr_b16 v[108:109], %[t0] offset:16384\n" \
  "v_xor_b32 %[t1], 0x18020, %[pb]\n" \
  "ds_read_b64_tr_b16 v[236:237], %[t1] offset:0\n" \
  "ds_read_b64_tr_b16 v[156:157], %[t1] offset:16384\n" \
  "v_xor_b32 %[t0], 0x19000, %[pb]\n" \
  "ds_read_b64_tr_b16 v[12:13], %[t0] offset:0\n" \
  "ds_read_b64_tr_b16 v[116:117], %[t0] offset:16384\n" \
  "v_xor_b32 %[t1], 0x19020, %[pb]\n" \
  "ds_read_b64_tr_b16 v[60:61], %[t1] offset:0\n" \
  "ds_read_b64_tr_b16 v[164:165], %[t1] offset:16384\n" \
  "v_xor_b32 %[t0], 0x1a000, %[pb]\n" \
  "ds_read_b64_tr_b16 v[28:29], %[t0] offset:0\n" \
  "ds_read_b64_tr_b16 v[124:125], %[t0] offset:16384\n" \
  "v_xor_b32 %[t1], 0x1a020, %[pb]\n" \
  "ds_read_b64_tr_b16 v[76:77], %[t1] offset:0\n" \
  "ds_read_b64_tr_b16 v[172:173], %[t1] offset:16384\n" \
  "v_xor_b32 %[t0], 0x1b000, %[pb]\n" \
  "ds_read_b64_tr_b16 v[44:45], %[t0] offset:0\n" \
  "ds_read_b64_tr_b16 v[140:141], %[t0] offset:16384\n" \
  "v_xor_b32 %[t1], 0x1b020, %[pb]\n" \
  "ds_read_b64_tr_b16 v[92:93], %[t1] offset:0\n" \
  "ds_read_b64_tr_b16 v[188:189], %[t1] offset:16384\n" \
  ".Ls_%=:\n" \
  "s_waitcnt lgkmcnt(0)\n"
#define PRO0_14 \
  "v_add_u32 %[t0], 0x18000, %[b0]\n" \
  "ds_write_b128 %[t0], v[208:211]\n" \
  "ds_write_b128 %[t0], v[212:215] offset:1024\n" \
  "v_add_u32 %[t0], 0x1c000, %[b0]\n" \
  "ds_write_b128 %[t0], v[216:219]\n" \
  "ds_write_b128 %[t0], v[220:223] offset:1024\n" \
  "s_cmp_lg_u32 %[hb], 0\n" \
  "s_cbranch_scc1 .Ls_%=\n" \
  "v_xor_b32 %[t0], 0x20000, %[pb]\n" \
  "ds_read_b64_tr_b16 v[230:231], %[t0] offset:0\n" \
  "ds_read_b64_tr_b16 v[110:111], %[t0] offset:16384\n" \
  "v_xor_b32 %[t1], 0x20020, %[pb]\n" \
  "ds_read_b64_tr_b16 v[238:239], %[t1] offset:0\n" \
  "ds_read_b64_tr_b16 v[158:159], %[t1] offset:16384\n" \
  "v_xor_b32 %[t0], 0x21000, %[pb]\n" \
  "ds_read_b64_tr_b16 v[14:15], %[t0] offset:0\n" \
  "ds_read_b64_tr_b16 v[118:119], %[t0] offset:16384\n" \
  "v_xor_b32 %[t1], 0x21020, %[pb]\n" \
  "ds_read_b64_tr_b16 v[62:63], %[t1] offset:0\n" \
  "ds_read_b64_tr_b16 v[166:167], %[t1] offset:16384\n" \
  "v_xor_b32 %[t0], 0x22000, %[pb]\n" \
  "ds_read_b64_tr_b16 v[30:31], %[t0] offset:0\n" \
  "ds_read_b64_tr_b16 v[126:127], %[t0] offset:16384\n" \
  "v_xor_b32 %[t1], 0x22020, %[pb]\n" \
  "ds_read_b64_tr_b16 v[78:79], %[t1] offset:0\n" \
  "ds_read_b64_tr_b16 v[174:175], %[t1] offset:16384\n" \
  "v_xor_b32 %[t0], 0x23000, %[pb]\n" \
  "ds_read_b64_tr_b16 v[46:47], %[t0] offset:0\n" \
  "ds_read_b64_tr_b16 v[142:143], %[t0] offset:16384\n" \
  "v_xor_b32 %[t1], 0x23020, %[pb]\n" \
  "ds_read_b64_tr_b16 v[94:95], %[t1] offset:0\n" \
  "ds_read_b64_tr_b16 v[190:191], %[t1] offset:16384\n" \
  ".Ls_%=:\n" \
  "s_waitcnt lgkmcnt(0)\n"
#define PRO0_15 \
  "s_cmp_lg_u32 %[hb], 1\n" \
  "s_cbranch_scc1 .Ls_%=\n" \
  "v_xor_b32 %[t0], 0x0, %[pb]\n" \
  "ds_read_b64_tr_b16 v[230:231], %[t0] offset:0\n" \
  "ds_read_b64_tr_b16 v[110:111], %[t0] offset:16384\n" \
  "v_xor_b32 %[t1], 0x20, %[pb]\n" \
  "ds_read_b64_tr_b16 v[238:239], %[t1] offset:0\n" \
  "ds_read_b64_tr_b16 v[158:159], %[t1] offset:16384\n" \
  "v_xor_b32 %[t0], 0x1000, %[pb]\n" \
  "ds_read_b64_tr_b16 v[14:15], %[t0] offset:0\n" \
  "ds_read_b64_tr_b16 v[118:119], %[t0] offset:16384\n" \
  "v_xor_b32 %[t1], 0x1020, %[pb]\n" \
  "ds_read_b64_tr_b16 v[62:63], %[t1] offset:0\n" \
  "ds_read_b64_tr_b16 v[166:167], %[t1] offset:16384\n" \
  "v_xor_b32 %[t0], 0x2000, %[pb]\n" \
  "ds_read_b64_tr_b16 v[30:31], %[t0] offset:0\n" \
  "ds_read_b64_tr_b16 v[126:127], %[t0] offset:16384\n" \
  "v_xor_b32 %[t1], 0x2020, %[pb]\n" \
  "ds_read_b64_tr_b16 v[78:79], %[t1] offset:0\n" \
  "ds_read_b64_tr_b16 v[174:175], %[t1] offset:16384\n" \
  "v_xor_b32 %[t0], 0x3000, %[pb]\n" \
  "ds_read_b64_tr_b16 v[46:47], %[t0] offset:0\n" \
  "ds_read_b64_tr_b16 v[142:143], %[t0] offset:16384\n" \
  "v_xor_b32 %[t1], 0x3020, %[pb]\n" \
  "ds_read_b64_tr_b16 v[94:95], %[t1] offset:0\n" \
  "ds_read_b64_tr_b16 v[190:191], %[t1] offset:16384\n" \
  ".Ls_%=:\n" \
  "s_waitcnt lgkmcnt(0)\n" \
  "v_add_u32 %[t0], 0x20000, %[b0]\n" \
  "ds_write_b128 %[t0], v[224:227]\n" \
  "ds_write_b128 %[t0], v[228:231] offset:1024\n" \
  "v_add_u32 %[t0], 0x24000, %[b0]\n" \
  "ds_write_b128 %[t0], v[232:235]\n" \
  "ds_write_b128 %[t0], v[236:239] offset:1024\n"
#define PRO1_0 \
  "v_add_u32 %[t0], 0x0, %[pa]\n" \
  "ds_read_b64_tr_b16 v[96:97], %[t0] offset:0\n" \
  "ds_read_b64_tr_b16 v[100:101], %[t0] offset:1024\n" \
  "ds_read_b64_tr_b16 v[104:105], %[t0] offset:8192\n" \
  "ds_read_b64_tr_b16 v[108:109], %[t0] offset:9216\n" \
  "s_cmp_lg_u32 %[hb], 0\n" \
  "s_cbranch_scc1 .Ls_%=\n" \
  "v_xor_b32 %[t0], 0x0, %[pb]\n" \
  "ds_read_b64_tr_b16 v[192:193], %[t0] offset:0\n" \
  "v_xor_b32 %[t1], 0x80, %[pb]\n" \
  "ds_read_b64_tr_b16 v[200:201], %[t1] offset:0\n" \
  "v_xor_b32 %[t0], 0x1000, %[pb]\n" \
  "ds_read_b64_tr_b16 v[0:1], %[t0] offset:0\n" \
  "v_xor_b32 %[t1], 0x1080, %[pb]\n" \
  "ds_read_b64_tr_b16 v[48:49], %[t1] offset:0\n" \
  "v_xor_b32 %[t0], 0x2000, %[pb]\n" \
  "ds_read_b64_tr_b16 v[16:17], %[t0] offset:0\n" \
  "v_xor_b32 %[t1], 0x2080, %[pb]\n" \
  "ds_read_b64_tr_b16 v[64:65], %[t1] offset:0\n" \
  "v_xor_b32 %[t0], 0x3000, %[pb]\n" \
  "ds_read_b64_tr_b16 v[32:33], %[t0] offset:0\n" \
  "v_xor_b32 %[t1], 0x3080, %[pb]\n" \
  "ds_read_b64_tr_b16 v[80:81], %[t1] offset:0\n" \
  ".Ls_%=:\n" \
  "s_waitcnt lgkmcnt(0)\n"
#define PRO1_1 \
  "v_add_u32 %[t0], 0x8000, %[pa]\n" \
  "ds_read_b64_tr_b16 v[98:99], %[t0] offset:0\n" \
  "ds_read_b64_tr_b16 v[102:103], %[t0] offset:1024\n" \
  "ds_read_b64_tr_b16 v[106:107], %[t0] offset:8192\n" \
  "ds_read_b64_tr_b16 v[110:111], %[t0] offset:9216\n" \
  "s_cmp_lg_u32 %[hb], 1\n" \
  "s_cbranch_scc1 .Ls_%=\n" \
  "v_xor_b32 %[t0], 0x8000, %[pb]\n" \
  "ds_read_b64_tr_b16 v[192:193], %[t0] offset:0\n" \
  "v_xor_b32 %[t1], 0x8080, %[pb]\n" \
  "ds_read_b64_tr_b16 v[200:201], %[t1] offset:0\n" \
  "v_xor_b32 %[t0], 0x9000, %[pb]\n" \
  "ds_read_b64_tr_b16 v[0:1], %[t0] offset:0\n" \
  "v_xor_b32 %[t1], 0x9080, %[pb]\n" \
  "ds_read_b64_tr_b16 v[48:49], %[t1] offset:0\n" \
  "v_xor_b32 %[t0], 0xa000, %[pb]\n" \
  "ds_read_b64_tr_b16 v[16:17], %[t0] offset:0\n" \
  "v_xor_b32 %[t1], 0xa080, %[pb]\n" \
  "ds_read_b64_tr_b16 v[64:65], %[t1] offset:0\n" \
  "v_xor_b32 %[t0], 0xb000, %[pb]\n" \
  "ds_read_b64_tr_b16 v[32:33], %[t0] offset:0\n" \
  "v_xor_b32 %[t1], 0xb080, %[pb]\n" \
  "ds_read_b64_tr_b16 v[80:81], %[t1] offset:0\n" \
  ".Ls_%=:\n" \
  "s_waitcnt lgkmcnt(0)\n" \
  "v_swap_b32 v97, v98\n" \
  "v_swap_b32 v101, v102\n" \
  "v_swap_b32 v105, v106\n" \
  "v_swap_b32 v109, v110\n"
#define PRO1_2 \
  "v_add_u32 %[t0], 0x10000, %[pa]\n" \
  "ds_read_b64_tr_b16 v[112:113], %[t0] offset:0\n" \
  "ds_read_b64_tr_b16 v[116:117], %[t0] offset:1024\n" \
  "ds_read_b64_tr_b16 v[120:121], %[t0] offset:8192\n" \
  "ds_read_b64_tr_b16 v[124:125], %[t0] offset:9216\n" \
  "s_cmp_lg_u32 %[hb], 0\n" \
  "s_cbranch_scc1 .Ls_%=\n" \
  "v_xor_b32 %[t0], 0x10000, %[pb]\n" \
  "ds_read_b64_tr_b16 v[194:195], %[t0] offset:0\n" \
  "v_xor_b32 %[t1], 0x10080, %[pb]\n" \
  "ds_read_b64_tr_b16 v[202:203], %[t1] offset:0\n" \
  "v_xor_b32 %[t0], 0x11000, %[pb]\n" \
  "ds_read_b64_tr_b16 v[2:3], %[t0] offset:0\n" \
  "v_xor_b32 %[t1], 0x11080, %[pb]\n" \
  "ds_read_b64_tr_b16 v[50:51], %[t1] offset:0\n" \
  "v_xor_b32 %[t0], 0x12000, %[pb]\n" \
  "ds_read_b64_tr_b16 v[18:19], %[t0] offset:0\n" \
  "v_xor_b32 %[t1], 0x12080, %[pb]\n" \
  "ds_read_b64_tr_b16 v[66:67], %[t1] offset:0\n" \
  "v_xor_b32 %[t0], 0x13000, %[pb]\n" \
  "ds_read_b64_tr_b16 v[34:35], %[t0] offset:0\n" \
  "v_xor_b32 %[t1], 0x13080, %[pb]\n" \
  "ds_read_b64_tr_b16 v[82:83], %[t1] offset:0\n" \
  ".Ls_%=:\n" \
  "s_waitcnt lgkmcnt(0)\n"
#define PRO1_3 \
  "v_add_u32 %[t0], 0x18000, %[pa]\n" \
  "ds_read_b64_tr_b16 v[114:115], %[t0] offset:0\n" \
  "ds_read_b64_tr_b16 v[118:119], %[t0] offset:1024\n" \
  "ds_read_b64_tr_b16 v[122:123], %[t0] offset:8192\n" \
  "ds_read_b64_tr_b16 v[126:127], %[t0] offset:9216\n" \
  "s_cmp_lg_u32 %[hb], 1\n" \
  "s_cbranch_scc1 .Ls_%=\n" \
  "v_xor_b32 %[t0], 0x18000, %[pb]\n" \
  "ds_read_b64_tr_b16 v[194:195], %[t0] offset:0\n" \
  "v_xor_b32 %[t1], 0x18080, %[pb]\n" \
  "ds_read_b64_tr_b16 v[202:203], %[t1] offset:0\n" \
  "v_xor_b32 %[t0], 0x19000, %[pb]\n" \
  "ds_read_b64_tr_b16 v[2:3], %[t0] offset:0\n" \
  "v_xor_b32 %[t1], 0x19080, %[pb]\n" \
  "ds_read_b64_tr_b16 v[50:51], %[t1] offset:0\n" \
  "v_xor_b32 %[t0], 0x1a000, %[pb]\n" \
  "ds_read_b64_tr_b16 v[18:19], %[t0] offset:0\n" \
  "v_xor_b32 %[t1], 0x1a080, %[pb]\n" \
  "ds_read_b64_tr_b16 v[66:67], %[t1] offset:0\n" \
  "v_xor_b32 %[t0], 0x1b000, %[pb]\n" \
  "ds_read_b64_tr_b16 v[34:35], %[t0] offset:0\n" \
  "v_xor_b32 %[t1], 0x1b080, %[pb]\n" \
  "ds_read_b64_tr_b16 v[82:83], %[t1] offset:0\n" \
  ".Ls_%=:\n" \
  "s_waitcnt lgkmcnt(0)\n" \
  "v_swap_b32 v113, v114\n" \
  "v_swap_b32 v117, v118\n" \
  "v_swap_b32 v121, v122\n" \
  "v_swap_b32 v125, v126\n"
#define PRO1_4 \
  "v_add_u32 %[t0], 0x20000, %[pa]\n" \
  "ds_read_b64_tr_b16 v[128:129], %[t0] offset:0\n" \
  "ds_read_b64_tr_b16 v[132:133], %[t0] offset:1024\n" \
  "ds_read_b64_tr_b16 v[136:137], %[t0] offset:8192\n" \
  "ds_read_b64_tr_b16 v[140:141], %[t0] offset:9216\n" \
  "s_cmp_lg_u32 %[hb], 0\n" \
  "s_cbranch_scc1 .Ls_%=\n" \
  "v_xor_b32 %[t0], 0x20000, %[pb]\n" \
  "ds_read_b64_tr_b16 v[196:197], %[t0] offset:0\n" \
  "v_xor_b32 %[t1], 0x20080, %[pb]\n" \
  "ds_read_b64_tr_b16 v[204:205], %[t1] offset:0\n" \
  "v_xor_b32 %[t0], 0x21000, %[pb]\n" \
  "ds_read_b64_tr_b16 v[4:5], %[t0] offset:0\n" \
  "v_xor_b32 %[t1], 0x21080, %[pb]\n" \
  "ds_read_b64_tr_b16 v[52:53], %[t1] offset:0\n" \
  "v_xor_b32 %[t0], 0x22000, %[pb]\n" \
  "ds_read_b64_tr_b16 v[20:21], %[t0] offset:0\n" \
  "v_xor_b32 %[t1], 0x22080, %[pb]\n" \
  "ds_read_b64_tr_b16 v[68:69], %[t1] offset:0\n" \
  "v_xor_b32 %[t0], 0x23000, %[pb]\n" \
  "ds_read_b64_tr_b16 v[36:37], %[t0] offset:0\n" \
  "v_xor_b32 %[t1], 0x23080, %[pb]\n" \
  "ds_read_b64_tr_b16 v[84:85], %[t1] offset:0\n" \
  ".Ls_%=:\n" \
  "s_waitcnt lgkmcnt(0)\n"
#define PRO1_5 \
  "v_add_u32 %[t0], 0x0, %[pa]\n" \
  "ds_read_b64_tr_b16 v[130:131], %[t0] offset:0\n" \
  "ds_read_b64_tr_b16 v[134:135], %[t0] offset:1024\n" \
  "ds_read_b64_tr_b16 v[138:139], %[t0] offset:8192\n" \
  "ds_read_b64_tr_b16 v[142:143], %[t0] offset:9216\n" \
  "s_cmp_lg_u32 %[hb], 1\n" \
  "s_cbranch_scc1 .Ls_%=\n" \
  "v_xor_b32 %[t0], 0x0, %[pb]\n" \
  "ds_read_b64_tr_b16 v[196:197], %[t0] offset:0\n" \
  "v_xor_b32 %[t1], 0x80, %[pb]\n" \
  "ds_read_b64_tr_b16 v[204:205], %[t1] offset:0\n" \
  "v_xor_b32 %[t0], 0x1000, %[pb]\n" \
  "ds_read_b64_tr_b16 v[4:5], %[t0] offset:0\n" \
  "v_xor_b32 %[t1], 0x1080, %[pb]\n" \
  "ds_read_b64_tr_b16 v[52:53], %[t1] offset:0\n" \
  "v_xor_b32 %[t0], 0x2000, %[pb]\n" \
  "ds_read_b64_tr_b16 v[20:21], %[t0] offset:0\n" \
  "v_xor_b32 %[t1], 0x2080, %[pb]\n" \
  "ds_read_b64_tr_b16 v[68:69], %[t1] offset:0\n" \
  "v_xor_b32 %[t0], 0x3000, %[pb]\n" \
  "ds_read_b64_tr_b16 v[36:37], %[t0] offset:0\n" \
  "v_xor_b32 %[t1], 0x3080, %[pb]\n" \
  "ds_read_b64_tr_b16 v[84:85], %[t1] offset:0\n" \
  ".Ls_%=:\n" \
  "s_waitcnt lgkmcnt(0)\n" \
  "v_swap_b32 v129, v130\n" \
  "v_swap_b32 v133, v134\n" \
  "v_swap_b32 v137, v138\n" \
  "v_swap_b32 v141, v142\n"
#define PRO1_6 \
  "v_add_u32 %[t0], 0x8000, %[pa]\n" \
  "ds_read_b64_tr_b16 v[144:145], %[t0] offset:0\n" \
  "ds_read_b64_tr_b16 v[148:149], %[t0] offset:1024\n" \
  "ds_read_b64_tr_b16 v[152:153], %[t0] offset:8192\n" \
  "ds_read_b64_tr_b16 v[156:157], %[t0] offset:9216\n" \
  "s_cmp_lg_u32 %[hb], 0\n" \
  "s_cbranch_scc1 .Ls_%=\n" \
  "v_xor_b32 %[t0], 0x8000, %[pb]\n" \
  "ds_read_b64_tr_b16 v[198:199], %[t0] offset:0\n" \
  "v_xor_b32 %[t1], 0x8080, %[pb]\n" \
  "ds_read_b64_tr_b16 v[206:207], %[t1] offset:0\n" \
  "v_xor_b32 %[t0], 0x9000, %[pb]\n" \
  "ds_read_b64_tr_b16 v[6:7], %[t0] offset:0\n" \
  "v_xor_b32 %[t1], 0x9080, %[pb]\n" \
  "ds_read_b64_tr_b16 v[54:55], %[t1] offset:0\n" \
  "v_xor_b32 %[t0], 0xa000, %[pb]\n" \
  "ds_read_b64_tr_b16 v[22:23], %[t0] offset:0\n" \
  "v_xor_b32 %[t1], 0xa080, %[pb]\n" \
  "ds_read_b64_tr_b16 v[70:71], %[t1] offset:0\n" \
  "v_xor_b32 %[t0], 0xb000, %[pb]\n" \
  "ds_read_b64_tr_b16 v[38:39], %[t0] offset:0\n" \
  "v_xor_b32 %[t1], 0xb080, %[pb]\n" \
  "ds_read_b64_tr_b16 v[86:87], %[t1] offset:0\n" \
  ".Ls_%=:\n" \
  "s_waitcnt lgkmcnt(0)\n"
#define PRO1_7 \
  "v_add_u32 %[t0], 0x10000, %[pa]\n" \
  "ds_read_b64_tr_b16 v[146:147], %[t0] offset:0\n" \
  "ds_read_b64_tr_b16 v[150:151], %[t0] offset:1024\n" \
  "ds_read_b64_tr_b16 v[154:155], %[t0] offset:8192\n" \
  "ds_read_b64_tr_b16 v[158:159], %[t0] offset:9216\n" \
  "s_cmp_lg_u32 %[hb], 1\n" \
  "s_cbranch_scc1 .Ls_%=\n" \
  "v_xor_b32 %[t0], 0x10000, %[pb]\n" \
  "ds_read_b64_tr_b16 v[198:199], %[t0] offset:0\n" \
  "v_xor_b32 %[t1], 0x10080, %[pb]\n" \
  "ds_read_b64_tr_b16 v[206:207], %[t1] offset:0\n" \
  "v_xor_b32 %[t0], 0x11000, %[pb]\n" \
  "ds_read_b64_tr_b16 v[6:7], %[t0] offset:0\n" \
  "v_xor_b32 %[t1], 0x11080, %[pb]\n" \
  "ds_read_b64_tr_b16 v[54:55], %[t1] offset:0\n" \
  "v_xor_b32 %[t0], 0x12000, %[pb]\n" \
  "ds_read_b64_tr_b16 v[22:23], %[t0] offset:0\n" \
  "v_xor_b32 %[t1], 0x12080, %[pb]\n" \
  "ds_read_b64_tr_b16 v[70:71], %[t1] offset:0\n" \
  "v_xor_b32 %[t0], 0x13000, %[pb]\n" \
  "ds_read_b64_tr_b16 v[38:39], %[t0] offset:0\n" \
  "v_xor_b32 %[t1], 0x13080, %[pb]\n" \
  "ds_read_b64_tr_b16 v[86:87], %[t1] offset:0\n" \
  ".Ls_%=:\n" \
  "s_waitcnt lgkmcnt(0)\n" \
  "v_swap_b32 v145, v146\n" \
  "v_swap_b32 v149, v150\n" \
  "v_swap_b32 v153, v154\n" \
  "v_swap_b32 v157, v158\n"
#define PRO1_8 \
  "v_add_u32 %[t0], 0x18000, %[pa]\n" \
  "ds_read_b64_tr_b16 v[160:161], %[t0] offset:0\n" \
  "ds_read_b64_tr_b16 v[164:165], %[t0] offset:1024\n" \
  "ds_read_b64_tr_b16 v[168:169], %[t0] offset:8192\n" \
  "ds_read_b64_tr_b16 v[172:173], %[t0] offset:9216\n" \
  "s_cmp_lg_u32 %[hb], 0\n" \
  "s_cbranch_scc1 .Ls_%=\n" \
  "v_xor_b32 %[t0], 0x18000, %[pb]\n" \
  "ds_read_b64_tr_b16 v[208:209], %[t0] offset:0\n" \
  "v_xor_b32 %[t1], 0x18080, %[pb]\n" \
  "ds_read_b64_tr_b16 v[216:217], %[t1] offset:0\n" \
  "v_xor_b32 %[t0], 0x19000, %[pb]\n" \
  "ds_read_b64_tr_b16 v[8:9], %[t0] offset:0\n" \
  "v_xor_b32 %[t1], 0x19080, %[pb]\n" \
  "ds_read_b64_tr_b16 v[56:57], %[t1] offset:0\n" \
  "v_xor_b32 %[t0], 0x1a000, %[pb]\n" \
  "ds_read_b64_tr_b16 v[24:25], %[t0] offset:0\n" \
  "v_xor_b32 %[t1], 0x1a080, %[pb]\n" \
  "ds_read_b64_tr_b16 v[72:73], %[t1] offset:0\n" \
  "v_xor_b32 %[t0], 0x1b000, %[pb]\n" \
  "ds_read_b64_tr_b16 v[40:41], %[t0] offset:0\n" \
  "v_xor_b32 %[t1], 0x1b080, %[pb]\n" \
  "ds_read_b64_tr_b16 v[88:89], %[t1] offset:0\n" \
  ".Ls_%=:\n" \
  "s_waitcnt lgkmcnt(0)\n"
#define PRO1_9 \
  "v_add_u32 %[t0], 0x20000, %[pa]\n" \
  "ds_read_b64_tr_b16 v[162:163], %[t0] offset:0\n" \
  "ds_read_b64_tr_b16 v[166:167], %[t0] offset:1024\n" \
  "ds_read_b64_tr_b16 v[170:171], %[t0] offset:8192\n" \
  "ds_read_b64_tr_b16 v[174:175], %[t0] offset:9216\n" \
  "s_cmp_lg_u32 %[hb], 1\n" \
  "s_cbranch_scc1 .Ls_%=\n" \
  "v_xor_b32 %[t0], 0x20000, %[pb]\n" \
  "ds_read_b64_tr_b16 v[208:209], %[t0] offset:0\n" \
  "v_xor_b32 %[t1], 0x20080, %[pb]\n" \
  "ds_read_b64_tr_b16 v[216:217], %[t1] offset:0\n" \
  "v_xor_b32 %[t0], 0x21000, %[pb]\n" \
  "ds_read_b64_tr_b16 v[8:9], %[t0] offset:0\n" \
  "v_xor_b32 %[t1], 0x21080, %[pb]\n" \
  "ds_read_b64_tr_b16 v[56:57], %[t1] offset:0\n" \
  "v_xor_b32 %[t0], 0x22000, %[pb]\n" \
  "ds_read_b64_tr_b16 v[24:25], %[t0] offset:0\n" \
  "v_xor_b32 %[t1], 0x22080, %[pb]\n" \
  "ds_read_b64_tr_b16 v[72:73], %[t1] offset:0\n" \
  "v_xor_b32 %[t0], 0x23000, %[pb]\n" \
  "ds_read_b64_tr_b16 v[40:41], %[t0] offset:0\n" \
  "v_xor_b32 %[t1], 0x23080, %[pb]\n" \
  "ds_read_b64_tr_b16 v[88:89], %[t1] offset:0\n" \
  ".Ls_%=:\n" \
  "s_waitcnt lgkmcnt(0)\n" \
  "v_swap_b32 v161, v162\n" \
  "v_swap_b32 v165, v166\n" \
  "v_swap_b32 v169, v170\n" \
  "v_swap_b32 v173, v174\n"
#define PRO1_10 \
  "v_add_u32 %[t0], 0x0, %[pa]\n" \
  "ds_read_b64_tr_b16 v[176:177], %[t0] offset:0\n" \
  "ds_read_b64_tr_b16 v[180:181], %[t0] offset:1024\n" \
  "ds_read_b64_tr_b16 v[184:185], %[t0] offset:8192\n" \
  "ds_read_b64_tr_b16 v[188:189], %[t0] offset:9216\n" \
  "s_cmp_lg_u32 %[hb], 0\n" \
  "s_cbranch_scc1 .Ls_%=\n" \
  "v_xor_b32 %[t0], 0x0, %[pb]\n" \
  "ds_read_b64_tr_b16 v[210:211], %[t0] offset:0\n" \
  "v_xor_b32 %[t1], 0x80, %[pb]\n" \
  "ds_read_b64_tr_b16 v[218:219], %[t1] offset:0\n" \
  "v_xor_b32 %[t0], 0x1000, %[pb]\n" \
  "ds_read_b64_tr_b16 v[10:11], %[t0] offset:0\n" \
  "v_xor_b32 %[t1], 0x1080, %[pb]\n" \
  "ds_read_b64_tr_b16 v[58:59], %[t1] offset:0\n" \
  "v_xor_b32 %[t0], 0x2000, %[pb]\n" \
  "ds_read_b64_tr_b16 v[26:27], %[t0] offset:0\n" \
  "v_xor_b32 %[t1], 0x2080, %[pb]\n" \
  "ds_read_b64_tr_b16 v[74:75], %[t1] offset:0\n" \
  "v_xor_b32 %[t0], 0x3000, %[pb]\n" \
  "ds_read_b64_tr_b16 v[42:43], %[t0] offset:0\n" \
  "v_xor_b32 %[t1], 0x3080, %[pb]\n" \
  "ds_read_b64_tr_b16 v[90:91], %[t1] offset:0\n" \
  ".Ls_%=:\n" \
  "s_waitcnt lgkmcnt(0)\n"
#define PRO1_11 \
  "v_add_u32 %[t0], 0x8000, %[pa]\n" \
  "ds_read_b64_tr_b16 v[178:179], %[t0] offset:0\n" \
  "ds_read_b64_tr_b16 v[182:183], %[t0] offset:1024\n" \
  "ds_read_b64_tr_b16 v[186:187], %[t0] offset:8192\n" \
  "ds_read_b64_tr_b16 v[190:191], %[t0] offset:9216\n" \
  "s_cmp_lg_u32 %[hb], 1\n" \
  "s_cbranch_scc1 .Ls_%=\n" \
  "v_xor_b32 %[t0], 0x8000, %[pb]\n" \
  "ds_read_b64_tr_b16 v[210:211], %[t0] offset:0\n" \
  "v_xor_b32 %[t1], 0x8080, %[pb]\n" \
  "ds_read_b64_tr_b16 v[218:219], %[t1] offset:0\n" \
  "v_xor_b32 %[t0], 0x9000, %[pb]\n" \
  "ds_read_b64_tr_b16 v[10:11], %[t0] offset:0\n" \
  "v_xor_b32 %[t1], 0x9080, %[pb]\n" \
  "ds_read_b64_tr_b16 v[58:59], %[t1] offset:0\n" \
  "v_xor_b32 %[t0], 0xa000, %[pb]\n" \
  "ds_read_b64_tr_b16 v[26:27], %[t0] offset:0\n" \
  "v_xor_b32 %[t1], 0xa080, %[pb]\n" \
  "ds_read_b64_tr_b16 v[74:75], %[t1] offset:0\n" \
  "v_xor_b32 %[t0], 0xb000, %[pb]\n" \
  "ds_read_b64_tr_b16 v[42:43], %[t0] offset:0\n" \
  "v_xor_b32 %[t1], 0xb080, %[pb]\n" \
  "ds_read_b64_tr_b16 v[90:91], %[t1] offset:0\n" \
  ".Ls_%=:\n" \
  "s_waitcnt lgkmcnt(0)\n" \
  "v_swap_b32 v177, v178\n" \
  "v_swap_b32 v181, v182\n" \
  "v_swap_b32 v185, v186\n" \
  "v_swap_b32 v189, v190\n"
#define PRO1_12 \
  "v_add_u32 %[t0], 0x8000, %[b0]\n" \
  "ds_write_b128 %[t0], v[192:195]\n" \
  "ds_write_b128 %[t0], v[196:199] offset:1024\n" \
  "v_add_u32 %[t0], 0xc000, %[b0]\n" \
  "ds_write_b128 %[t0], v[200:203]\n" \
  "ds_write_b128 %[t0], v[204:207] offset:1024\n" \
  "v_add_u32 %[t0], 0x10000, %[pa]\n" \
  "ds_read_b64_tr_b16 v[224:225], %[t0] offset:0\n" \
  "ds_read_b64_tr_b16 v[228:229], %[t0] offset:1024\n" \
  "ds_read_b64_tr_b16 v[232:233], %[t0] offset:8192\n" \
  "ds_read_b64_tr_b16 v[236:237], %[t0] offset:9216\n" \
  "s_cmp_lg_u32 %[hb], 0\n" \
  "s_cbranch_scc1 .Ls_%=\n" \
  "v_xor_b32 %[t0], 0x10000, %[pb]\n" \
  "ds_read_b64_tr_b16 v[212:213], %[t0] offset:0\n" \
  "v_xor_b32 %[t1], 0x10080, %[pb]\n" \
  "ds_read_b64_tr_b16 v[220:221], %[t1] offset:0\n" \
  "v_xor_b32 %[t0], 0x11000, %[pb]\n" \
  "ds_read_b64_tr_b16 v[12:13], %[t0] offset:0\n" \
  "v_xor_b32 %[t1], 0x11080, %[pb]\n" \
  "ds_read_b64_tr_b16 v[60:61], %[t1] offset:0\n" \
  "v_xor_b32 %[t0], 0x12000, %[pb]\n" \
  "ds_read_b64_tr_b16 v[28:29], %[t0] offset:0\n" \
  "v_xor_b32 %[t1], 0x12080, %[pb]\n" \
  "ds_read_b64_tr_b16 v[76:77], %[t1] offset:0\n" \
  "v_xor_b32 %[t0], 0x13000, %[pb]\n" \
  "ds_read_b64_tr_b16 v[44:45], %[t0] offset:0\n" \
  "v_xor_b32 %[t1], 0x13080, %[pb]\n" \
  "ds_read_b64_tr_b16 v[92:93], %[t1] offset:0\n" \
  ".Ls_%=:\n" \
  "s_waitcnt lgkmcnt(0)\n"
#define PRO1_13 \
  "v_add_u32 %[t0], 0x18000, %[pa]\n" \
  "ds_read_b64_tr_b16 v[226:227], %[t0] offset:0\n" \
  "ds_read_b64_tr_b16 v[230:231], %[t0] offset:1024\n" \
  "ds_read_b64_tr_b16 v[234:235], %[t0] offset:8192\n" \
  "ds_read_b64_tr_b16 v[238:239], %[t0] offset:9216\n" \
  "s_cmp_lg_u32 %[hb], 1\n" \
  "s_cbranch_scc1 .Ls_%=\n" \
  "v_xor_b32 %[t0], 0x18000, %[pb]\n" \
  "ds_read_b64_tr_b16 v[212:213], %[t0] offset:0\n" \
  "v_xor_b32 %[t1], 0x18080, %[pb]\n" \
  "ds_read_b64_tr_b16 v[220:221], %[t1] offset:0\n" \
  "v_xor_b32 %[t0], 0x19000, %[pb]\n" \
  "ds_read_b64_tr_b16 v[12:13], %[t0] offset:0\n" \
  "v_xor_b32 %[t1], 0x19080, %[pb]\n" \
  "ds_read_b64_tr_b16 v[60:61], %[t1] offset:0\n" \
  "v_xor_b32 %[t0], 0x1a000, %[pb]\n" \
  "ds_read_b64_tr_b16 v[28:29], %[t0] offset:0\n" \
  "v_xor_b32 %[t1], 0x1a080, %[pb]\n" \
  "ds_read_b64_tr_b16 v[76:77], %[t1] offset:0\n" \
  "v_xor_b32 %[t0], 0x1b000, %[pb]\n" \
  "ds_read_b64_tr_b16 v[44:45], %[t0] offset:0\n" \
  "v_xor_b32 %[t1], 0x1b080, %[pb]\n" \
  "ds_read_b64_tr_b16 v[92:93], %[t1] offset:0\n" \
  ".Ls_%=:\n" \
  "s_waitcnt lgkmcnt(0)\n" \
  "v_swap_b32 v225, v226\n" \
  "v_swap_b32 v229, v230\n" \
  "v_swap_b32 v233, v234\n" \
  "v_swap_b32 v237, v238\n" \
  "v_add_u32 %[t0], 0x10000, %[b0]\n" \
  "ds_write_b128 %[t0], v[224:227]\n" \
  "ds_write_b128 %[t0], v[228:231] offset:1024\n" \
  "v_add_u32 %[t0], 0x14000, %[b0]\n" \
  "ds_write_b128 %[t0], v[232:235]\n" \
  "ds_write_b128 %[t0], v[236:239] offset:1024\n"
#define PRO1_14 \
  "v_add_u32 %[t0], 0x20000, %[pa]\n" \
  "ds_read_b64_tr_b16 v[224:225], %[t0] offset:0\n" \
  "ds_read_b64_tr_b16 v[228:229], %[t0] offset:1024\n" \
  "ds_read_b64_tr_b16 v[232:233], %[t0] offset:8192\n" \
  "ds_read_b64_tr_b16 v[236:237], %[t0] offset:9216\n" \
  "s_cmp_lg_u32 %[hb], 0\n" \
  "s_cbranch_scc1 .Ls_%=\n" \
  "v_xor_b32 %[t0], 0x20000, %[pb]\n" \
  "ds_read_b64_tr_b16 v[214:215], %[t0] offset:0\n" \
  "v_xor_b32 %[t1], 0x20080, %[pb]\n" \
  "ds_read_b64_tr_b16 v[222:223], %[t1] offset:0\n" \
  "v_xor_b32 %[t0], 0x21000, %[pb]\n" \
  "ds_read_b64_tr_b16 v[14:15], %[t0] offset:0\n" \
  "v_xor_b32 %[t1], 0x21080, %[pb]\n" \
  "ds_read_b64_tr_b16 v[62:63], %[t1] offset:0\n" \
  "v_xor_b32 %[t0], 0x22000, %[pb]\n" \
  "ds_read_b64_tr_b16 v[30:31], %[t0] offset:0\n" \
  "v_xor_b32 %[t1], 0x22080, %[pb]\n" \
  "ds_read_b64_tr_b16 v[78:79], %[t1] offset:0\n" \
  "v_xor_b32 %[t0], 0x23000, %[pb]\n" \
  "ds_read_b64_tr_b16 v[46:47], %[t0] offset:0\n" \
  "v_xor_b32 %[t1], 0x23080, %[pb]\n" \
  "ds_read_b64_tr_b16 v[94:95], %[t1] offset:0\n" \
  ".Ls_%=:\n" \
  "s_waitcnt lgkmcnt(0)\n"
#define PRO1_15 \
  "v_add_u32 %[t0], 0x0, %[pa]\n" \
  "ds_read_b64_tr_b16 v[226:227], %[t0] offset:0\n" \
  "ds_read_b64_tr_b16 v[230:231], %[t0] offset:1024\n" \
  "ds_read_b64_tr_b16 v[234:235], %[t0] offset:8192\n" \
  "ds_read_b64_tr_b16 v[238:239], %[t0] offset:9216\n" \
  "s_cmp_lg_u32 %[hb], 1\n" \
  "s_cbranch_scc1 .Ls_%=\n" \
  "v_xor_b32 %[t0], 0x0, %[pb]\n" \
  "ds_read_b64_tr_b16 v[214:215], %[t0] offset:0\n" \
  "v_xor_b32 %[t1], 0x80, %[pb]\n" \
  "ds_read_b64_tr_b16 v[222:223], %[t1] offset:0\n" \
  "v_xor_b32 %[t0], 0x1000, %[pb]\n" \
  "ds_read_b64_tr_b16 v[14:15], %[t0] offset:0\n" \
  "v_xor_b32 %[t1], 0x1080, %[pb]\n" \
  "ds_read_b64_tr_b16 v[62:63], %[t1] offset:0\n" \
  "v_xor_b32 %[t0], 0x2000, %[pb]\n" \
  "ds_read_b64_tr_b16 v[30:31], %[t0] offset:0\n" \
  "v_xor_b32 %[t1], 0x2080, %[pb]\n" \
  "ds_read_b64_tr_b16 v[78:79], %[t1] offset:0\n" \
  "v_xor_b32 %[t0], 0x3000, %[pb]\n" \
  "ds_read_b64_tr_b16 v[46:47], %[t0] offset:0\n" \
  "v_xor_b32 %[t1], 0x3080, %[pb]\n" \
  "ds_read_b64_tr_b16 v[94:95], %[t1] offset:0\n" \
  ".Ls_%=:\n" \
  "s_waitcnt lgkmcnt(0)\n" \
  "v_swap_b32 v225, v226\n" \
  "v_swap_b32 v229, v230\n" \
  "v_swap_b32 v233, v234\n" \
  "v_swap_b32 v237, v238\n" \
  "v_add_u32 %[t0], 0x18000, %[b0]\n" \
  "ds_write_b128 %[t0], v[224:227]\n" \
  "ds_write_b128 %[t0], v[228:231] offset:1024\n" \
  "v_add_u32 %[t0], 0x1c000, %[b0]\n" \
  "ds_write_b128 %[t0], v[232:235]\n" \
  "ds_write_b128 %[t0], v[236:239] offset:1024\n" \
  "v_add_u32 %[t0], 0x20000, %[b0]\n" \
  "ds_write_b128 %[t0], v[208:211]\n" \
  "ds_write_b128 %[t0], v[212:215] offset:1024\n" \
  "v_add_u32 %[t0], 0x24000, %[b0]\n" \
  "ds_write_b128 %[t0], v[216:219]\n" \
  "ds_write_b128 %[t0], v[220:223] offset:1024\n"
#define VO_CLOB "v192", "v193", "v194", "v195", "v196", "v197", "v198", "v199", "v200", "v201", "v202", "v203", "v204", "v205", "v206", "v207", "v208", "v209", "v210", "v211", "v212", "v213", "v214", "v215", "v216", "v217", "v218", "v219", "v220", "v221", "v222", "v223", "v224", "v225", "v226", "v227", "v228", "v229", "v230", "v231", "v232", "v233", "v234", "v235", "v240", "v241", "v242", "v243", "v244", "v245", "v246", "v247", "v250", "v251", "v252"
#define QK_CLOB "v204", "v205", "v206", "v207", "v208", "v209", "v210", "v211", "v212", "v213", "v214", "v215", "v216", "v217", "v218", "v219", "v220", "v221", "v222", "v223", "v224", "v225", "v226", "v227", "v228", "v229", "v230", "v231", "v236", "v237", "v238", "v239", "v240", "v241", "v242", "v243", "v244", "v245", "v246", "v247"
#define QL_CLOB "v192", "v193", "v194", "v195", "v196", "v197", "v198", "v199", "v200", "v201", "v202", "v203"
#define QH_CLOB "v232", "v233", "v234", "v235"
#define ST_CLOB "v192", "v193", "v194", "v195", "v196", "v197", "v198", "v199", "v200", "v201", "v202", "v203", "v204", "v205", "v206", "v207", "v240", "v241", "v242", "v243", "v244", "v245", "v246", "v247", "v253"
#define XB_CLOB "v192", "v193", "v194", "v195", "v196", "v197", "v198", "v199", "v200", "v201", "v202", "v203", "v204", "v205", "v206", "v207", "v208", "v209", "v210", "v211", "v212", "v213", "v214", "v215", "v216", "v217", "v218", "v219", "v220", "v221", "v222", "v223", "v224", "v225", "v226", "v227", "v228", "v229", "v230", "v231", "v232", "v233", "v234", "v235", "v236", "v237", "v238", "v239", "v240", "v241", "v242", "v243", "v244", "v245", "v246", "v247"
#define OF_CLOB "v32", "v33", "v34", "v35", "v36", "v37", "v38", "v39", "v40", "v41", "v42", "v43", "v44", "v45", "v46", "v47", "v48", "v49", "v50", "v51", "v52", "v53", "v54", "v55", "v56", "v57", "v58", "v59", "v60", "v61", "v62", "v63", "v64", "v65", "v66", "v67", "v68", "v69", "v70", "v71", "v72", "v73", "v74", "v75", "v76", "v77", "v78", "v79", "v80", "v81", "v82", "v83", "v84", "v85", "v86", "v87", "v88", "v89", "v90", "v91", "v92", "v93", "v94", "v95", "v96", "v97", "v98", "v99", "v100", "v101", "v102", "v103"
constexpr int L_W = 32768;
constexpr int L_HI = 16384;
constexpr float CSC = 0.08838834764831845f * 1.4426950408889634f;
#define TWY 3
#define MARG 108
#define ED0 700
#define EA0 1110
#define SDK 360
#define TOL 145
#define XLATE 130
#define TFR 600
#define QT 0
#define HSU 11
#define WNT 1
#define KROT 7
#define VLATE 0
#define VSL 2
#define CG1 18
#define CG2 6
#define KSL2 0
#define KSL 4
#define NCY 1600
#define NCYV NCY
#define NRY 24
#define NRX 31
#define PDU 1
#define PDY 4
#define PDC 6
#define NF1O 64
#define NPUB 2
#define NSER 4
#define NSB 6
#define NSB1 4
#define XTU 20
#define CPU_ 9
#define CPR 12
#define TLP 0
#define CP0 0
#define RSL 2
#define PFL 4
#define EVT2R
template <int role>
__device__ __forceinline__ void wroles(const int h, const int par, const int sl, const int xg, const int w, const u64 nonce, const u16* __restrict__ x,
                                       const u16* __restrict__ Wq, const u16* __restrict__ Wk, const u16* __restrict__ Wv,
                                       const u16* __restrict__ Wo, char* ws) {
  asm volatile(".Lrole%c0:" ::"n"(role));
  LAS char* const L = (LAS char*)g_lds;
  gu32* tmo = (gu32*)(ws + OFF_TMO);
  const __amdgpu_buffer_rsrc_t rs = __builtin_amdgcn_make_buffer_rsrc(ws, 0, (int)WS_USED, 0x00020000);
  constexpr bool vo = role & 1;
  v8u wa[8], wb[16];
  v32u W0, W1, W2, W3, W4, W5;
  constexpr bool fuse = role >= 2;
  constexpr int RG = fuse ? 4 : 5, RB = fuse ? 32768 : 0;
  f32x4 fac[2][2];
  v2u nin = {0u, 0u};
  constexpr int TWV = 5;
  Clk ck = {0u, 0u, 92500u, 46432u};
  const int krot = role >= 2 ? (h & KROT) ^ (par * PX) : 0;
  {
    const int lane = olane();
    const int l15 = lane & 15, g = lane >> 4, trq = l15 >> 2, trp = lane & 3;
    const char* bl = (const char*)((vo ? Wv : Wq) + h * DH);
    const char* br = (const char*)(vo ? Wo + (size_t)h * DH * D : Wk + h * DH);
    const int rot = role == 1 ? (h >> 4) * 4 : 0;
    int vl[2], vr[2];
#pragma unroll
    for (int q = 0; q < 2; ++q) {
      const int row = 4 * (2 * w + q) + g;
      const int cb = ((((l15 >> 1) ^ ((row & 3) | ((row >> 1) & 4))) << 5) | ((l15 & 1) << 4));
      const int i = row & 7;
      const int prow = (row & 32) + ((i & 4) << 2) + ((row >> 1) & 12) + (i & 3);
      vl[q] = role < 2 ? (128 * (w >> 1) + 16 * (w & 1) + 8 * q + g) * 8192 + ((((l15 >> 1) ^ (4 * q + g)) << 5) | ((l15 & 1) << 4))
                       : row * 8192 + cb;
      vr[q] = vo ? (role == 1 ? row : prow) * 2048 + cb : vl[q];
    }
#define W_ISSUE(u)                                                                                   \
  {                                                                                                  \
    const size_t ro = (size_t)(role < 2 ? (8 * ((u) & 1) + ((u) >> 3)) * 64 + (((u) >> 2) & 1) * 32 + (((u) >> 1) & 1) * 4 : ((u) ^ krot) * 64) * 8192; \
    const char* pl = bl + ro;                                                                        \
    const char* pr = br + (role == 1 ? (size_t)(((u) >> 1) & 1) * 64 * 2048 + (2 * ((u) >> 2) + ((u) & 1)) * 256          \
                                     : (vo ? (size_t)((u) & 1) * 64 * 2048 + (((u) ^ (par * PX)) >> 1) * 256 : ro)); \
    _Pragma("unroll") for (int q = 0; q < 2; ++q) {                                                  \
      glds16<((WNT >> (role >> 1)) & 1) != 0>(pl, vl[q], RB + ((u) % RG) * 32768 + (2 * w + q) * 1024);                                \
      glds16<((WNT >> (role >> 1)) & 1) != 0>(pr, vr[q], RB + ((u) % RG) * 32768 + 16384 + (2 * w + q) * 1024);                        \
    }                                                                                                \
  }
    const int ta = role == 1 ? 8 + (w ^ rot) : 2 * w, tb = role < 2 ? (2 - role) * (w & 3) : 2 * w + 1;
    const int sw = trq | ((g & 1) << 2);
    int pa = (ta >> 3) * 16384 + (8 * g + trq) * 256 + (((ta & 7) ^ sw) << 5) + trp * 8;
    int pb = (role < 2 ? (w & 3) * 4096 + (4 * g + trq) * 256 : (tb >> 3) * 16384 + (8 * g + trq) * 256) + (((tb & 7) ^ sw) << 5) + trp * 8;
    v16u K0, K1, K2;
    if constexpr (role < 2)
      asm volatile("" : "={v[0:31]}"(W0), "={v[32:63]}"(W1), "={v[64:95]}"(W2), "={v[96:127]}"(W3), "={v[128:159]}"(W4), "={v[160:191]}"(W5),
                   "={v[192:207]}"(K0), "={v[208:223]}"(K1), "={v[224:239]}"(K2));
    const int b0 = lane * 16 + (w ^ 4) * 2048;
    const int pw = L_W + w * 2048 + lane * 16;
#define W_FRAG(dst, p, u)                                                                            \
  {                                                                                                  \
    int p_ = p + RB + ((u) % RG) * 32768;                                                             \
    asm volatile("" : "+v"(p_));                                                                     \
    const v2u t0 = trd(L + p_), t1 = trd(L + p_ + 1024), t2 = trd(L + p_ + 8192), t3 = trd(L + p_ + 9216); \
    dst = v8u{t0[0], t0[1], t1[0], t1[1], t2[0], t2[1], t3[0], t3[1]};                               \
  }
#define W_PUT(c)                                           \
  {                                                        \
    int p_ = pw + ((c) - 8) * 16384;                       \
    asm volatile("" : "+v"(p_));                           \
    *(LAS v4u*)(L + p_) = lo4(tmp[(c) - 8]);               \
    *(LAS v4u*)(L + p_ + 1024) = hi4(tmp[(c) - 8]);        \
  }
#define PT(i)
#define PT4(i)
    PT(0);
    const int fxv = ((w & 1) + 2 * (lane >> 2)) * 2048 + (((lane ^ -g) & 3) << 4) + ((w >> 1) & 1) * 64;
    const int fxr = l15 * 64 + (((g ^ -trq) & 3) << 4);
    constexpr int XS = role == 3;
#define X_ISSUE(j) glds16((const char*)x + par * 65536 + ((2 * (j) + (w >> 2)) ^ krot) * 128, fxv, (((j) + XS) & 3) * 8192 + w * 1024)
    if (TLP && w >= 6 + vo) {
      unsigned keep;
      asm volatile("s_mov_b32 %0, m0\n\ts_mov_b32 m0, %3\n\ts_nop 0\n\tglobal_load_lds_dword %1, %2\n\ts_mov_b32 m0, %0"
                   : "=&s"(keep) : "v"((olane() & (TLP - 1)) * (8388608 / (TLP ? TLP : 1))), "s"(w == 7 ? bl : br), "s"(RB + w * 2048) : "memory");
    }
    W_ISSUE(0);
    if (((CP0 >> role) & 1) && w == 1) code_ahead(sl + 8 * par, RB + 32768 + 2048);
    if (fuse) {
#pragma unroll
      for (int j = 0; j < 4; ++j) X_ISSUE(j);
    }
#pragma unroll
    for (int u = 1; u < RG; ++u) {
      if (u == PDU && role < 3)
        for (int k = role == 2 ? TN(6, PDY) : TN(5, PDC); k > 0; --k) __builtin_amdgcn_s_sleep(8);
      W_ISSUE(u);
    }
    PT(1);
    v8u tmp[8];
#pragma unroll
    for (int u = 0; u < 16; ++u) {
      {
        int young = 4 * (u + RG - 1 < 16 ? RG - 1 : 15 - u);
        if (fuse)
          for (int i = u - RG; i < u; ++i) young += i >= 0 && i < 8 && (i & 1);
        asm volatile("s_waitcnt vmcnt(%0)" ::"n"(young) : "memory");
      }
      RAW_BARRIER();
      if ((u & 3) == 0 || u == 15) PT(2 + (u + 1) / 4);
      if constexpr (role < 2) {
        unsigned t0, t1;
#define PRO_OPS                                                                                                                    \
  : "+{v[0:31]}"(W0), "+{v[32:63]}"(W1), "+{v[64:95]}"(W2), "+{v[96:127]}"(W3), "+{v[128:159]}"(W4), "+{v[160:191]}"(W5),          \
    "+{v[192:207]}"(K0), "+{v[208:223]}"(K1), "+{v[224:239]}"(K2), [t0] "=&v"(t0), [t1] "=&v"(t1)                                  \
  : [b0] "v"(b0), [pa] "v"(pa), [pb] "v"(pb), [hb] "s"(w >> 2)                                                                     \
  : "scc", "memory"
#define PRO_U(n)                                         \
  if (u == n) {                                          \
    if constexpr (role == 0) asm volatile(PRO0_##n PRO_OPS); \
    else asm volatile(PRO1_##n PRO_OPS);                 \
  }
        PRO_U(0) PRO_U(1) PRO_U(2) PRO_U(3) PRO_U(4) PRO_U(5) PRO_U(6) PRO_U(7)
        PRO_U(8) PRO_U(9) PRO_U(10) PRO_U(11) PRO_U(12) PRO_U(13) PRO_U(14) PRO_U(15)
      } else {
        if (u >= 13) { W_PUT(u >= 13 ? 2 * u - 18 : 8); W_PUT(u >= 13 ? 2 * u - 17 : 8); }
        v8u fa, fb;
        W_FRAG(fa, pa, u)
        W_FRAG(fb, pb, u)
        if (u < 8) {
          wa[u & 7] = fa;
          wb[u] = fb;
        } else {
          wb[u] = cat8(lo4(fa), lo4(fb));
          tmp[u & 7] = cat8(hi4(fa), hi4(fb));
        }
#pragma unroll
        for (int hf = 0; hf < 2; ++hf)
#pragma unroll
          for (int rt = 0; rt < 2; ++rt) {
            const v4u xq = *(const LAS v4u*)(L + fxr + ((u + 2 * XS) & 7) * 4096 + hf * 2048 + rt * 1024);
            fac[0][rt] = mma(hf ? hi4(fa) : lo4(fa), xq, u + hf == 0 ? f32x4{0.f, 0.f, 0.f, 0.f} : fac[0][rt]);
            fac[1][rt] = mma(hf ? hi4(fb) : lo4(fb), xq, u + hf == 0 ? f32x4{0.f, 0.f, 0.f, 0.f} : fac[1][rt]);
          }
        asm volatile("" : "+v"(fac[0][0]), "+v"(fac[0][1]), "+v"(fac[1][0]), "+v"(fac[1][1]));
      }
      if (u == HSU && w == 0) hs_wait(rs, ws, nonce, CNT_HS);
      if (((VWN && role == 3) || (QKN && role == 2)) && u == HSU + 2 && w == 7) nin = numa_load(rs, tmo);
      if (role < 2 && u == HSU + 2 && w == 7) {
        arm_wait(rs, tmo);
        OPQ(off, OFF_PERM);
        nin[0] = __builtin_amdgcn_raw_buffer_load_b32(rs, off, 0, AUX_SC1);
      }
      if (role < 2 && u == 14 && w == TWV) clk_init(ck);
      if (u + RG < 16) {
        RAW_BARRIER();
        if (fuse && u == XTU && w < 2) {
          unsigned keep;
          asm volatile("s_mov_b32 %0, m0\n\ts_mov_b32 m0, %3\n\ts_nop 0\n\tglobal_load_lds_dword %1, %2\n\ts_mov_b32 m0, %0"
                       : "=&s"(keep) : "v"(olane() * 128), "s"((const char*)x + (2 * w + 2 + par) * 65536 + sl * 8192), "s"(RB + (u % RG) * 32768 + w * 2048) : "memory");
        }
        if (u == CPU_ && ((CPR >> role) & 1) && w == 1) code_ahead(sl + 8 * par, RB + (u % RG) * 32768 + 2048);
        W_ISSUE(u + RG);
        if (fuse && u < 8 && (u & 1)) X_ISSUE(4 + (u >> 1));
      }
    }
    PT(7);
    static_assert(!QKN, "");
    if (role == 2 || (role == 3 && w < 4)) {
      const int l16 = olane() * 16;
#pragma unroll
      for (int rt = 0; rt < 2; ++rt) {
        const float ks = role == 2 && w >= 4 ? CSC : 1.f;
        const f32x4 f0 = fac[0][rt] * ks, f1 = fac[1][rt] * ks;
        const v4u o = {pk2(f0[0], f0[1]), pk2(f0[2], f0[3]), pk2(f1[0], f1[1]), pk2(f1[2], f1[3])};
        if (role == 3) *(LAS v4u*)(L + 24576 + rt * 4096 + w * 1024 + l16) = o;
        else st128(v4u{o[0], o[2], o[1], o[3]}, rs, l16, qk_addr(w >> 2, par, h, 0, 0) + rt * QK_RT + (w & 3) * 1024);
      }
    }
    RAW_BARRIER();
    PT(8);
    if (fuse) { W_PUT(14); W_PUT(15); }
  }
  const int lane = olane();
  const int l15 = lane & 15, g = lane >> 4, trq = l15 >> 2, trp = lane & 3;
  int pw0 = L_W + w * 2048 + lane * 16, pw1 = pw0 + 65536;
  asm volatile("" : "+v"(pw0), "+v"(pw1));
#define WA_P(c) (L + ((c) < 12 ? pw0 : pw1) + ((c) & 3) * 16384)
#define WA_LDS(c) cat8(*(const LAS v4u*)WA_P(c), *(const LAS v4u*)(WA_P(c) + 1024))
  const int r_a = (int)(((uintptr_t)ws >> 19) & 7);
  const int r_s = r_a + (int)(OFF_STEP / UNIT);

  typedef int v4i __attribute__((ext_vector_type(4)));
#define WPIN "{v[0:31]}"(W0), "{v[32:63]}"(W1), "{v[64:95]}"(W2), "{v[96:127]}"(W3), "{v[128:159]}"(W4), "{v[160:191]}"(W5)
  const v4u rsv = {(unsigned)(uintptr_t)ws, (unsigned)((uintptr_t)ws >> 32) & 0xffffu, (unsigned)WS_USED, 0x00020000u};
  if constexpr (role >= 2) {
    constexpr bool isx = role == 3;
    W0 = cat32(wa[0], wa[1], wa[2], wa[3]), W1 = cat32(wa[4], wa[5], wa[6], wa[7]), W2 = cat32(wb[0], wb[1], wb[2], wb[3]);
    W3 = cat32(wb[4], wb[5], wb[6], wb[7]), W4 = cat32(wb[8], wb[9], wb[10], wb[11]), W5 = cat32(wb[12], wb[13], wb[14], wb[15]);
    const v4u rxv = {(unsigned)(uintptr_t)x, (unsigned)((uintptr_t)x >> 32) & 0xffffu, (unsigned)(S * D * 2), 0x00020000u};
    v4i adr;
    adr[3] = lane * 16;
    adr[2] = pw0;
    adr[1] = pw1;
    adr[0] = ((w & 1) + 2 * (lane >> 2)) * 2048 + (((lane ^ -g) & 3) << 4) + ((w >> 1) & 1) * 1024 + (isx ? 0 : (w >> 2) * 64);
    const int xr = l15 * 64 + (((g ^ -trq) & 3) << 4);
    constexpr int fo = isx ? F_VW : F_QK;
    const int nrel = isx ? TN(8, NRX) : TN(7, NRY);
    const int nit = par ? nrel : NSEG - nrel;
#define SEGOF(i) ((i) < nrel ? 2 * (i) + par : (i) + nrel)
#define ST_FLAG(sg)                                                                                                   \
  {                                                                                                                   \
    VMCNT(0);                                                                                                         \
    RAW_BARRIER();                                                                                                    \
    if (w == 0) __builtin_amdgcn_raw_buffer_store_b32(0u, rs, olane() << 28, fo + flag_of(sg, h), AUX_SC1);               \
  }
    unsigned vpm_ = 0;
    if ((VWN && isx) || (QKN && !isx)) {
      if (w == 7) *(volatile LAS unsigned*)L = numa_perm(nin, (LAS unsigned*)(L + 512));
      RAW_BARRIER();
      vpm_ = __builtin_amdgcn_readfirstlane(*(volatile LAS unsigned*)L) | (unsigned)((r_a + (int)(OFF_VWX / UNIT)) & 7) << 12;
      RAW_BARRIER();
    }
    const int qcl = cls_of(vpm_, h >> 3) ^ (QKN == 2 ? 2 : 0);
#define XB_OPS(sg)                                                                                                      \
  : [cnt] "=&s"(cnt) EVOUT                                                                                              \
  : WPIN, "{v[248:251]}"(adr), [rs] "s"(rsv), [so] "s"(vw_addr(vpm_, 2 * (w - 4), sg, h >> 2) + (h & 3) * 1024 + par * (PX / 2 * VRING * 262144)), \
    [so2] "s"(vw_addr(vpm_, 2 * (w - 4), sg, h >> 2) + (h & 3) * 1024 - par * (PX / 2 * VRING * 262144)),               \
    [js] "i"(VWN ? 32 * 524288 : VRING * 262144), [ss] "i"(VWN ? 65536 : 32768), [ob] "s"(nit - it)                         \
  : XB_CLOB, "scc", "memory"
    if (!isx || w < 4) {
      constexpr int B0 = isx ? 6 : 8, PER = isx ? 2 : 4, SL = isx ? 12288 : 16384;
#pragma unroll
      for (int k = 0; k < 2; ++k)
#pragma unroll
        for (int j = 0; j < B0 / PER; ++j)
          glds16((const char*)x + SEGOF(1) * 65536 + (((k * B0 + j * PER) / 2 * 64) ^ (krot * 128)), adr[0], k * SL + j * PER * 2048 + w * 1024);
    }
    PT(9);
    if (!isx) {
      VMCNT(4);
      RAW_BARRIER();
    }
    PT(10);
    if (!isx && w == 0) __builtin_amdgcn_raw_buffer_store_b32(0u, rs, olane() << 28, fo + flag_of(par, h), AUX_SC1);
    if (!isx || w < 4) {
      if (isx) VMCNT(3);
      else VMCNT(0);
      RAW_BARRIER();
    }
    PT(11);
    if (!isx || w < 4) {
      v32u xf;
#pragma unroll
      for (int q = 0; q < 8; ++q) {
        const v4u t = *(const LAS v4u*)(L + xr + q * 1024);
#pragma unroll
        for (int j = 0; j < 4; ++j) xf[4 * q + j] = t[j];
      }
#pragma unroll 1
      for (int it = 1; it < nit; ++it) {
        EVDECL;
        const int seg = SEGOF(it), ip = it - (isx ? 2 : 1);
        unsigned cnt, keep;
#define ST_OPS                                                                                                                 \
  : [cnt] "=&s"(cnt), [keep] "=&s"(keep), "+{v[208:239]}"(xf) EVOUT                                                            \
  : WPIN, "{v[248:251]}"(adr), "{v252}"(xr), [rs] "s"(rsv), [rx] "s"(rxv), [sc] "s"(seg * 65536 + krot * 128),                    \
    [sn] "s"(SEGOF(min(it + 1, nit - 1)) * 65536 + krot * 128),                                                                  \
    [mw] "s"(w * 1024), [so] "s"(isx ? w * 1024 : qk_addr(w >> 2, seg, h, qcl, r_a) + (w & 3) * 1024), [rt] "i"(QK_RT), \
    [fo] "s"(fo + flag_of(SEGOF(max(ip, 0)), h)), [fm] "s"(w == 0 && ip >= 0 ? 0 : 1 << 30), [tm] "s"(w == (isx ? 3 : TWY) ? 0 : 1 << 30),            \
    [tc] "s"(min(SEGOF(it + 2), NSEG - 1) * 65536 + sl * 8192), [cs] "s"((w >> 2) * __float_as_uint(CSC))                                                                   \
  : ST_CLOB, "scc", "memory"
        if constexpr (isx) asm volatile(ST_XA ST_OPS);
        else asm volatile(ST_Y ST_OPS);
        EVT2R
        if (!isx && it < NPUB) ST_FLAG(seg)
      }
      if (isx) RAW_BARRIER();
    } else {
      int prog = -1;
#define RING_WAIT(sg)                                                                                     \
  _Pragma("clang loop unroll(disable)") for (unsigned spins = 0; prog <= (sg) - VRING + 1 && (sg) >= VRING;) { \
    if (spins) __builtin_amdgcn_s_sleep(16);                                                              \
    OPQ(vp, OFF_PROG);                                                                                    \
    prog = __builtin_amdgcn_readfirstlane(__builtin_amdgcn_raw_buffer_load_b32(rs, vp, 0, AUX_SC1));      \
    if (spin_fail(spins, tmo, 0x30019)) break;                                                            \
  }
#pragma unroll 1
      for (int it = 1;;) {
        RING_WAIT(SEGOF(it - 1))
        {
          EVDECL;
          unsigned cnt;
          asm volatile(ST_XB0 XB_OPS(SEGOF(it - 1)));
        }
        if (it == nit) break;
        if (it <= 2) PT4(8 + 4 * it);
        const int nsb = it == 1 ? NSB1 : NSB;
#pragma unroll 1
        for (int k = (it == 1 ? 0 : 2) + NOB; k < nsb; ++k) BARE_BARRIER();
        if (it <= 2) PT4(9 + 4 * it);
        VMCNT(0);
        if (it <= 2) PT4(10 + 4 * it);
        BARE_BARRIER();
        if (it <= 2) PT4(11 + 4 * it);
        if (w == 4) __builtin_amdgcn_raw_buffer_store_b32(0u, rs, olane() << 28, fo + flag_of(SEGOF(it - 1), h), AUX_SC1);
#pragma unroll 1
        for (int k = nsb; k < 6; ++k) BARE_BARRIER();
        {
          EVDECL;
          EVT2R
        }
        if (++it > NSER) {
#pragma unroll 1
          for (; it < nit; ++it) {
            RING_WAIT(SEGOF(it - 1))
            EVDECL;
            unsigned cnt;
            asm volatile(ST_XB XB_OPS(SEGOF(it - 1)));
            EVT2R
          }
        }
        BARE_BARRIER();
      }
    }
    ST_FLAG(SEGOF(nit - 1))
    if (isx && w == 0) __builtin_amdgcn_raw_buffer_store_b32(0u, rs, olane() << 28, fo + flag_of(SEGOF(nit - 2), h), AUX_SC1);
    return;
  }

  volatile LAS unsigned* sword = (volatile LAS unsigned*)(L + L_HI + 6400);
  if (w == 7) sword[0] = nin[0];
  RAW_BARRIER();
  const unsigned perm = __builtin_amdgcn_readfirstlane(sword[0]);
  PT(9);
  const int cown = cls_of(perm, xg);
  const int sidx = ((l15 & 2) ? 0xeeee : 0x4444) | ((l15 & 8) ? 0xeeee0000 : 0x44440000);
  int ntw = min(w ^ TWV, 1) << 30;
  asm volatile("" : "+s"(ntw));
  v2u bs = {(unsigned)(lane * 16 + (w ^ 4) * 2048), (unsigned)sidx};
  unsigned pl1 = bs[0] + 65536;
  asm volatile("" : "+v"(bs), "+v"(pl1));
  const int mg_ = NS2F(TN(0, MARG)), tol_ = NS2F(TN(13, TOL)), xl_ = NS2F(TN(14, XLATE));
  const int ncy_ = TN(1, NCY), vd_ = role ? NS2F(VLATE) : 0;
  Pace pc = {0u, NS2F(ED0), NS2F(EA0), 0};
  unsigned tg = 0, tw = 0;
  const int mo0 = R_MEM + (w >> 1) * 32768 + (w & 1) * 2048 - (w ^ 4) * 2048;
#define CH_IN WPIN, "{v[248:249]}"(bs), "{v253}"(pk), "{v254}"(pl1), [rs] "s"(rsv)
#define TLB_TOUCH                    \
  if (((r_s + s + 2) & 3) == 0)      \
    tch = __builtin_amdgcn_raw_buffer_load_b32(rs, 0, so + 2 * (int)UNIT + R_DUM + (role * 32 + h) * 128, AUX_SC1);
#define ST_ADR (lst * 32 + 32768 + 2048)
#define TIMING_WAVE(slp)                                                                             \
  {                                                                                                  \
    v2u st;                                                                                          \
    {                                                                                                \
      const int lst = olane();                                                                       \
      st = __builtin_amdgcn_raw_buffer_load_b64(rs, ST_ADR, so + R_ST + ((cown ^ xr) << 12), AUX_SC1); \
    }                                                                                                \
    SB();                                                                                            \
    if (slp) __builtin_amdgcn_s_sleep(slp);                             \
    clk_update(ck, s < 2 ? CG1 : CG2);                                                \
    if (role == 0) EV(7);                                                                            \
    SB();                                                                                            \
    if (__builtin_expect(anysent(st[0]), 0)) _Pragma("clang loop unroll(disable)") for (unsigned spins = 0;;) { \
      if (spins) __builtin_amdgcn_s_sleep(4);                                                        \
      if (spin_fail(spins, tmo, 0x30009)) break;                                                     \
      const int lst = olane();                                                                       \
      OPQ(va, ST_ADR - 2048);                                                                        \
      st = __builtin_amdgcn_raw_buffer_load_b64(rs, va, so + R_ST + ((cown ^ xr) << 12), AUX_SC1);   \
      if (!anysent(st[0])) break;                                                                       \
    }                                                                                                \
    unsigned md;                                                                                     \
    const int dm = stamp_max(ck, st, md);                                                            \
    const unsigned rf = (ck.r << 4) + dm;                                                            \
    pace_step(pc, s, rf, md, mg_, tol_, xl_);                                                     \
    tg = clk_cyc(ck, dm + pc.dq + vd_);                                                                    \
    tw = (unsigned)max(CMAX - (int)(tg - ck.m), 0);                                                  \
    EV(6);                                                                                           \
    EVDUMP(s);                                                                                       \
    EVCLK(s, ck);                                                                                    \
  }

  if constexpr (role == 0) {
    unsigned pk = w < 4 ? (g >> 1) * 512 + (g & 1) * 128 + trq * 32 + 8 * (trp ^ (2 * (g & 1)))
                        : (w & 2) * 1024 + (w & 1) * 512 + trp * 32 + (l15 >> 3) * 128 + (trq & 1) * 256 + 8 * (g ^ (trq & 2));
    asm volatile("" : "+v"(pk));
#define K_STAGE(sn)                                                                                                   \
  {                                                                                                                   \
    v4u kn[4];                                                                                                        \
    _Pragma("clang loop unroll(disable)") for (unsigned spins = 0; krdy <= (sn);) {    \
      OPQ(vfl, F_QK + flag_of(min((sn) + (olane() & 15), NSEG - 1), h));                                           \
      const u64 miss = __builtin_amdgcn_ballot_w64(__builtin_amdgcn_raw_buffer_load_b32(rs, vfl, 0, AUX_SC1) != 0u);  \
      krdy = (sn) + (miss ? __builtin_ctzll(miss) : 16);                                                              \
      if (krdy > (sn)) break;                                                                                         \
      __builtin_amdgcn_s_sleep(8);                                                                                    \
      if (spin_fail(spins, tmo, 0x30000)) break;                                                                      \
    }                                                                                                                 \
    const int lk = olane() * 16;                                                                                      \
    _Pragma("unroll") for (int q = 0; q < 4; ++q) kn[q] = __builtin_amdgcn_raw_buffer_load_b128(                      \
        rs, lk + q * 1024, qk_addr(1, sn, h, cown, r_a) + (w & 1) * QK_RT, AUX_SC1);                 \
    _Pragma("unroll") for (int q = 0; q < 4; ++q) *(LAS v4u*)(L + L_HI + 8192 + (w & 1) * 4096 + q * 1024 + lk) = kn[q]; \
  }
    int krdy = 0;
    const bool kst = w >= 4 && ((w ^ TWV) & 2);
    PT(10);
    if (kst) K_STAGE(0)
    RAW_BARRIER();
    PT(11);
    if (w < 2) {
      {
        const int lane = olane(), l15 = lane & 15, g = lane >> 4, d = ((l15 >> 1) & 2) | (l15 >> 3);
        *(LAS v4u*)(L + L_HI + 5120 + w * 2048 + lane * 16) =
            v4u{(unsigned)(R_EX + (h >> 3) * 32768 + (h & 7) * 512 + w * 256 + g * 16 + ((l15 >> 3) | (l15 & 2)) * 64 + (l15 & 4) * 2 + (l15 & 1) * 4),
                (unsigned)(R_PM + (cls_of(perm, l15 & 3) << 12) + (h >> 4) * 32768 + (h & 15) * 256 + w * 128 + g * 16 + d * 4),
                d == 2 * (h & 1) + w ? 0xffffu << (16 * (g & 1)) : 0u, 0u};
      }
#pragma unroll 1
      for (int s = 0; s < NSEG; ++s) {
        EVDECL;
        const int so = (int)OFF_STEP + s * (int)UNIT, xr = xreg(r_s + s);
#define CG(r) (so + ((cls_of(perm, (r) ^ (2 * w)) ^ xr) << 12))
        unsigned rt;
#define QA_OPS                                                                                                                    \
  : [rt] "=&s"(rt) EVOUT                                                                                                          \
  : CH_IN, [o0] "s"(so + mo0 + ((cown ^ xr) << 12)), [c0] "s"(CG(0)), [c1] "s"(CG(1)), [c2] "s"(CG(2)), [c3] "s"(CG(3)),          \
    [lo] "s"(0x3333333333333333ull), [so] "s"(so), [xr] "s"(xr << 12)                                                                                              \
  : QL_CLOB, QK_CLOB, QH_CLOB, "v250", "v251", "v252", "vcc", "scc", "memory"
        if (w == 0) asm volatile(QK_A0 QA_OPS);
        else asm volatile(QK_A1 QA_OPS);
        EVDUMP(s);
      }
    } else if (w < 4) {
#pragma unroll 1
      for (int s = 0; s < NSEG; ++s) {
        {
          EVDECL;
          const int so = (int)OFF_STEP + s * (int)UNIT, xr = xreg(r_s + s);
          unsigned rt;
          f32x4 sc[3];
          asm volatile(QK_X
                       : [rt] "=&s"(rt), "={v[192:195]}"(sc[0]), "={v[196:199]}"(sc[1]), "={v[200:203]}"(sc[2]) EVOUT
                       : CH_IN, [o0] "s"(so + mo0 + ((cown ^ xr) << 12)), [qo] "s"(qk_addr(0, s, h, cown, r_a) + (w - 2) * QK_RT)
                       : QK_CLOB, QH_CLOB, "v250", "v251", "v252", "vcc", "scc", "memory");
          const int ln = olane(), l15 = ln & 15, g = ln >> 4, l16 = ln * 16;
          EV(4);
          const int tok = 2 * l15 + w - 2 - 8 * g;
#pragma unroll
          for (int t = 0; t < 2; ++t)
#pragma unroll
            for (int j = 0; j < 4; ++j)
              if (2 * j + t > tok) sc[t][j] = -1000000000.0f * 1.4426950408889634f;
          float m0 = sc[2][0] + sc[2][2], m1 = sc[2][1] + sc[2][3];
          float mx;
          asm("v_max3_f32 %0, %1, %2, %3\n\tv_max3_f32 %0, %0, %4, %5\n\tv_max3_f32 %0, %0, %6, %7\n\tv_max3_f32 %0, %0, %8, %9\n\t"
              "v_max_f32 %0, %0, %10\n\ts_nop 1"
              : "=&v"(mx)
              : "v"(sc[0][0]), "v"(sc[0][1]), "v"(sc[0][2]), "v"(sc[0][3]), "v"(sc[1][0]), "v"(sc[1][1]), "v"(sc[1][2]), "v"(sc[1][3]),
                "v"(m0), "v"(m1));
          mx = xmax16_32(mx);
#pragma unroll
          for (int t = 0; t < 2; ++t)
#pragma unroll
            for (int j = 0; j < 4; ++j) sc[t][j] = __builtin_amdgcn_exp2f(sc[t][j] - mx);
          m0 = __builtin_amdgcn_exp2f(m0 - mx);
          m1 = __builtin_amdgcn_exp2f(m1 - mx);
          const f32x4 s4 = sc[0] + sc[1];
          const float inv = __builtin_amdgcn_rcpf(xsum16_32(((s4[0] + s4[1]) + (s4[2] + s4[3])) + (m0 + m1)));
          sc[0] *= inv;
          sc[1] *= inv;
          const v4u o = {pk2(sc[0][0], sc[1][0]), pk2(sc[0][1], sc[1][1]), pk2(sc[0][2], sc[1][2]), pk2(sc[0][3], sc[1][3])};
          const int pb = so + R_PX + (w - 2) * 40960;
          st128(o, rs, l16, pb + h * 1024);
          __builtin_amdgcn_raw_buffer_store_b32(pk2(m0 * inv, m1 * inv), rs, l15 * 16 + g * 4, pb + (32 + (h >> 2)) * 1024 + (h & 3) * 256, AUX_SC1);
          EV(6);
          EVDUMP(s);
        }
      }
    } else {
#pragma unroll 1
      for (int s = 0; s < NSEG; ++s) {
        EVDECL;
        const int so = (int)OFF_STEP + s * (int)UNIT, xr = xreg(r_s + s), sc_ = so + ((cown ^ xr) << 12);
        const int lst = olane();
        const bool fq = kst && krdy <= s + 2;
        int i0 = ((cls_of(perm, lst & 3) ^ xr) << 12) | ((lst & 4) << 9) | ((lst & 8) << 12) | ((lst >> 4) << 28) | ntw;
        int i2 = ((lst & 31) * 128 + (lst >> 5) * QK_RT) | (krdy > s + 1 ? 0 : 1 << 30);
        int i1 = (F_QK + flag_of(min(s + 1 + (lst & 15), NSEG - 1), h)) | (fq ? 0 : 1 << 30);
        v2u st;
        unsigned cnt, rt;
        int o0 = sc_ + mo0, sst = so + R_ST + role * 1024 + h * 32;
        int t1 = qk_addr(w & 1, min(s + 1, NSEG - 1), h, cown, r_a), t0 = 0;
        asm volatile("" : "+{v232}"(i0), "+{v233}"(i1), "+{v234}"(i2));
        asm volatile("" : "+s"(o0), "+s"(sst), "+s"(t0), "+s"(t1));
        asm volatile(QK_K
                     : [cnt] "=&s"(cnt), [rt] "=&s"(rt), "+{v232}"(i0), "+{v233}"(i1), "+{v234}"(i2), "={v[250:251]}"(st) EVOUT
                     : CH_IN, [o0] "s"(o0), [ckm] "s"(ck.m - ncy_), [inv] "s"(ck.inv << 16), [r16] "s"(ck.r << 4), [sst] "s"(sst), [tg] "s"(tg),
                       [tw] "s"(tw), [t0] "s"(t0), [t1] "s"(t1), [cs] "s"(CSC)
                     : QL_CLOB, QK_CLOB, "v235", "v252", "vcc", "scc", "memory");
        if (s < NSEG - 1 && w == TWV) {
          TIMING_WAVE(KSL)
        } else if (s < NSEG - 1 && kst) {
          if (fq) {
            const u64 miss = __builtin_amdgcn_ballot_w64(st[0] != 0u);
            krdy = s + 1 + (miss ? __builtin_ctzll(miss) : 16);
          }
          if (KSL2) __builtin_amdgcn_s_sleep(KSL2);
          EV(7);
          K_STAGE(s + 1)
        } else {
          __builtin_amdgcn_s_sleep(KSL);
          unsigned tch = 0;
          TLB_TOUCH
          asm volatile("" ::"v"(tch));
        }
        if (w != TWV) EV(6);
        if (w != TWV) EVDUMP(s);
      }
    }
  } else {
    int adr[4];
#pragma unroll
    for (int q = 0; q < 2; ++q) {
      const int c = 8 * (2 * q + (g >> 1) + ((g & 1) ? 0 : 4)) + (w ^ ((h >> 4) * 4));
      const int blk = (c % PO) * 2 + (h >> 4);
      adr[q] = (int)OFF_VM + blk * 32768 + ((cls_of(perm, c / PO) ^ xreg(r_a + (int)(OFF_VM / UNIT) + (blk >> 4))) << 12) +
                   (h & 15) * 256 + l15 * 16;
    }
    adr[2] = (16 * (g & 1) + 4 * (g >> 1) + trq) * 24 + (trp & 1) * 8;
    unsigned pk = L_HI + (16 * (w & 3) + l15) * 24 + 4 * g;
    asm volatile("" : "+v"(pk), "+v"(adr[0]), "+v"(adr[1]), "+v"(adr[2]));
    PT(10);
    PT(11);
    if (w < 4) {
#pragma unroll 1
      for (int s = 0; s < NSEG; ++s) {
        EVDECL;
        const int so = (int)OFF_STEP + s * (int)UNIT, xr = xreg(r_s + s);
        unsigned rt;
        asm volatile(VO_A
                     : [rt] "=&s"(rt) EVOUT
                     : CH_IN, "{v236}"(adr[0]), "{v237}"(adr[1]), "{v238}"(adr[2]), [o0] "s"(so + mo0 + ((cown ^ xr) << 12)), [vs] "s"(vm_slot(s))
                     : VO_CLOB, "vcc", "scc", "memory");
        if (w == 0) {
          unsigned tch = 0;
          TLB_TOUCH
          asm volatile("" ::"v"(tch));
        }
        EVDUMP(s);
      }
    } else {
#pragma unroll 1
      for (int s = 0; s < NSEG; ++s) {
        EVDECL;
        const int so = (int)OFF_STEP + s * (int)UNIT, xr = xreg(r_s + s);
        const int lst = olane();
        adr[3] = ((cls_of(perm, lst & 3) ^ xr) << 12) | ((lst & 4) << 9) | ((lst & 8) << 12) | ((lst >> 4) << 28) | ntw;
        unsigned cnt, rt;
        int o0 = so + mo0 + ((cown ^ xr) << 12), vs = vm_slot(s), sst = so + R_ST + role * 1024 + h * 32;
        asm volatile("" : "+{v239}"(adr[3]));
        asm volatile("" : "+s"(o0), "+s"(vs), "+s"(sst));
        asm volatile(VO_B
                     : [cnt] "=&s"(cnt), [rt] "=&s"(rt) EVOUT
                     : CH_IN, "{v236}"(adr[0]), "{v237}"(adr[1]), "{v238}"(adr[2]), "{v239}"(adr[3]), [o0] "s"(o0), [vs] "s"(vs),
                       [ckm] "s"(ck.m - ncy_), [inv] "s"(ck.inv << 16), [r16] "s"(ck.r << 4), [sst] "s"(sst), [tg] "s"(tg), [tw] "s"(tw)
                     : VO_CLOB, "vcc", "scc", "memory");
        if (w == TWV && s < NSEG - 1) TIMING_WAVE(VSL)
        if (w != TWV) EVDUMP(s);
      }
    }
  }
  if ((WBL & 2) && role == 1)
    if (w == 0 && sl == 0) asm volatile("buffer_wbl2 sc1\n\ts_waitcnt vmcnt(0)" ::: "memory");
  __builtin_amdgcn_endpgm();
}
typedef f32x2 c32;
template <bool CJ>
__device__ __forceinline__ c32 cmul(c32 a, c32 b) {
  c32 d;
  if (CJ)
    asm("v_pk_mul_f32 %0, %1, %2 op_sel:[1,1] op_sel_hi:[1,0]\n\tv_pk_fma_f32 %0, %1, %2, %0 op_sel_hi:[0,1,1] neg_hi:[0,1,0]"
        : "=&v"(d) : "v"(a), "v"(b));
  else
    asm("v_pk_mul_f32 %0, %1, %2 op_sel:[1,1] op_sel_hi:[1,0] neg_lo:[0,1]\n\tv_pk_fma_f32 %0, %1, %2, %0 op_sel_hi:[0,1,1]"
        : "=&v"(d) : "v"(a), "v"(b));
  return d;
}
__device__ __forceinline__ c32 cmuls(c32 a, c32 b) {
  c32 d;
  asm("v_pk_mul_f32 %0, %1, %2 op_sel:[1,1] op_sel_hi:[1,0] neg_lo:[0,1]\n\tv_pk_fma_f32 %0, %1, %2, %0 op_sel_hi:[0,1,1]"
      : "=&v"(d) : "v"(a), "s"(b));
  return d;
}
template <int SG>
__device__ __forceinline__ c32 addi(c32 a, c32 b) {
  c32 d;
  if (SG > 0) asm("v_pk_add_f32 %0, %1, %2 op_sel:[0,1] op_sel_hi:[1,0] neg_lo:[0,1]" : "=v"(d) : "v"(a), "v"(b));
  else asm("v_pk_add_f32 %0, %1, %2 op_sel:[0,1] op_sel_hi:[1,0] neg_hi:[0,1]" : "=v"(d) : "v"(a), "v"(b));
  return d;
}
template <int SG, bool I2 = false>
__device__ __forceinline__ void dft4(c32& x0, c32& x1, c32& x2, c32& x3) {
  const c32 a = I2 ? addi<SG>(x0, x2) : x0 + x2, b = I2 ? addi<-SG>(x0, x2) : x0 - x2, c = x1 + x3, d = x1 - x3;
  x0 = a + c;
  x1 = addi<SG>(b, d);
  x2 = a - c;
  x3 = addi<-SG>(b, d);
}
template <int SG>
__device__ __forceinline__ c32 w16(int m) {
  constexpr float C[16] = {1.f, 0.92387953251128674f, 0.70710678118654752f, 0.38268343236508977f, 0.f, -0.38268343236508977f,
                           -0.70710678118654752f, -0.92387953251128674f, -1.f, -0.92387953251128674f, -0.70710678118654752f,
                           -0.38268343236508977f, 0.f, 0.38268343236508977f, 0.70710678118654752f, 0.92387953251128674f};
  return c32{C[m & 15], (float)SG * C[(m + 12) & 15]};
}
template <int SG>
__device__ __forceinline__ void dft16(c32 (&v)[16]) {
#pragma unroll
  for (int n2 = 0; n2 < 4; ++n2) dft4<SG>(v[n2], v[4 + n2], v[8 + n2], v[12 + n2]);
#pragma unroll
  for (int k1 = 1; k1 < 4; ++k1)
#pragma unroll
    for (int n2 = 1; n2 < 4; ++n2)
      if (n2 * k1 != 4) v[4 * k1 + n2] = cmuls(v[4 * k1 + n2], w16<SG>(n2 * k1));
#pragma unroll
  for (int k1 = 0; k1 < 4; ++k1)
    if (k1 == 2) dft4<SG, true>(v[8], v[9], v[10], v[11]);
    else dft4<SG>(v[4 * k1], v[4 * k1 + 1], v[4 * k1 + 2], v[4 * k1 + 3]);
  c32 t[16];
#pragma unroll
  for (int k1 = 0; k1 < 4; ++k1)
#pragma unroll
    for (int k2 = 0; k2 < 4; ++k2) t[k1 + 4 * k2] = v[4 * k1 + k2];
#pragma unroll
  for (int i = 0; i < 16; ++i) v[i] = t[i];
}
template <int SG>
__device__ __forceinline__ void dft8(c32 (&v)[8]) {
#pragma unroll
  for (int n2 = 0; n2 < 2; ++n2) dft4<SG>(v[n2], v[2 + n2], v[4 + n2], v[6 + n2]);
#pragma unroll
  for (int k1 = 1; k1 < 4; k1 += 2) v[2 * k1 + 1] = cmuls(v[2 * k1 + 1], w16<SG>(2 * k1));
  c32 t[8];
#pragma unroll
  for (int k1 = 0; k1 < 4; ++k1) {
    t[k1] = k1 == 2 ? addi<SG>(v[4], v[5]) : v[2 * k1] + v[2 * k1 + 1];
    t[k1 + 4] = k1 == 2 ? addi<-SG>(v[4], v[5]) : v[2 * k1] - v[2 * k1 + 1];
  }
#pragma unroll
  for (int i = 0; i < 8; ++i) v[i] = t[i];
}
__device__ __forceinline__ void lds_fence_wave() {
  __builtin_amdgcn_s_waitcnt(0xc07f);
  asm volatile("" ::: "memory");
}
template <int SG>
__device__ __forceinline__ void twiddle16(c32 (&u)[16], c32 w1, c32 w4) {
  const c32 w2 = cmul<false>(w1, w1), w3 = cmul<false>(w2, w1), w8 = cmul<false>(w4, w4), w12 = cmul<false>(w8, w4);
  const c32 lo[4] = {w1, w1, w2, w3}, hi[4] = {w4, w4, w8, w12};
#pragma unroll
  for (int k = 1; k < 16; ++k)
    u[k] = cmul<(SG > 0)>(u[k], (k & 3) ? ((k >> 2) ? cmul<false>(hi[k >> 2], lo[k & 3]) : lo[k & 3]) : hi[k >> 2]);
}
template <int N, int SG>
__device__ __forceinline__ void wave_fft(c32 (&v)[N / 64], c32* eb, const c32* tw, int lane) {
  constexpr int P = N / 64, B = P / 16, R3 = N / 256, M = N / 16, S1 = M + R3, S2 = 256 + 16 / R3, TS = 2048 / N;
  asm volatile("" : "+v"(lane));
#pragma unroll
  for (int b = 0; b < B; ++b) {
    c32 u[16];
#pragma unroll
    for (int n1 = 0; n1 < 16; ++n1) u[n1] = v[b + B * n1];
    dft16<SG>(u);
    const int n2 = lane + 64 * b;
    twiddle16<SG>(u, tw[n2 * TS], tw[n2 * TS * 4]);
#pragma unroll
    for (int k1 = 0; k1 < 16; ++k1) eb[k1 * S1 + n2] = u[k1];
    __builtin_amdgcn_sched_barrier(0);
  }
  lds_fence_wave();
  {
    c32 u[B][16];
#pragma unroll
    for (int b = 0; b < B; ++b) {
      const int q = lane + 64 * b, k1 = q / R3, m2 = q % R3;
#pragma unroll
      for (int m1 = 0; m1 < 16; ++m1) u[b][m1] = eb[k1 * S1 + m1 * R3 + m2];
    }
    const c32 w1 = tw[16 * (lane % R3) * TS], w4 = tw[64 * (lane % R3) * TS];
    lds_fence_wave();
#pragma unroll
    for (int b = 0; b < B; ++b) {
      const int q = lane + 64 * b, k1 = q / R3, m2 = q % R3;
      dft16<SG>(u[b]);
      twiddle16<SG>(u[b], w1, w4);
#pragma unroll
      for (int j1 = 0; j1 < 16; ++j1) eb[m2 * S2 + k1 + 16 * j1] = u[b][j1];
      __builtin_amdgcn_sched_barrier(0);
    }
  }
  lds_fence_wave();
#pragma unroll
  for (int c = 0; c < 4; ++c) {
    c32 u[R3];
#pragma unroll
    for (int m2 = 0; m2 < R3; ++m2) u[m2] = eb[m2 * S2 + lane + 64 * c];
    if constexpr (R3 == 4) dft4<SG>(u[0], u[1], u[2], u[3]);
    else dft8<SG>(u);
#pragma unroll
    for (int j2 = 0; j2 < R3; ++j2) v[c + 4 * j2] = u[j2];
  }
  lds_fence_wave();
}

constexpr int FFT_EB = 2176;
constexpr int LDS_TW = 0;
constexpr int LDS_EB = 16384;
constexpr int LDS_STG = 16384 + 8 * 1088 * 8;

__device__ __forceinline__ void fft_table(char* lds, int w) {
  c32* tw = (c32*)(lds + LDS_TW);
  {
    const int m = w * 64 + olane();
    float sn, cs;
    sincospif(-(float)m * (1.f / 1024.f), &sn, &cs);
    tw[m] = c32{cs, sn};
  }
}
#define XSK 12
__device__ __forceinline__ int x1_chunk(int kb, int rb) { return kb * 128 + ((rb + XSK * kb) & 127); }
#define F1NT true
#define FOW 0
template <bool FILT>
__device__ __forceinline__ void fft_in1(const u16* x, const u16* wr, const u16* wi, int task, int w) {
  const int lane = olane(), r0 = task * 16 + 2 * w;
  const int eo = LDS_EB + w * 8704, fo = r0 * DF * 2;
#pragma unroll
  for (int q = 0; q < 4; ++q) glds16<F1NT>(x, r0 * D * 2 + q * 1024 + lane * 16, eo + q * 1024);
#pragma unroll
  for (int q = 0; q < 3; ++q)
    if (FILT && (q < 2 || lane < 2)) {
      const int o = min((fo & ~15) + q * 1024 + lane * 16, S * DF * 2 - 16);
      glds16<F1NT>(wr, o, eo + 4096 + q * 1024);
      glds16<F1NT>(wi, o, eo + 6176 + q * 1024);
    }
}
__device__ __forceinline__ void fft_pass1(char* ws, const u16* __restrict__ x, const u16* __restrict__ wr,
                                          const u16* __restrict__ wi, int task, int w, const u16* xb) {
  char* lds = g_lds;
  const __amdgpu_buffer_rsrc_t rx = __builtin_amdgcn_make_buffer_rsrc(ws + OFF_X1, 0, 65 * 131072, 0x00020000);
  const int lane = olane(), tid_ = w * 64 + lane;
  const c32* tw = (const c32*)(lds + LDS_TW);
  c32* eb = (c32*)(lds + LDS_EB) + w * 1088;
  c32(*stg)[16] = (c32(*)[16])(lds + LDS_STG);
  const int r0 = task * 16 + 2 * w;
#define F1T(i)
  F1T(0);
  const int fo = r0 * DF * 2;
  if (xb) {
    unsigned keep;
    asm volatile("s_mov_b32 %0, m0\n\ts_mov_b32 m0, %3\n\ts_nop 0\n\tglobal_load_lds_dword %1, %2\n\ts_mov_b32 m0, %0"
                 : "=&s"(keep) : "v"(task * 65536 + tid_ * 128), "s"(xb), "s"(160000 + w * 256) : "memory");
  } else {
    fft_in1<true>(x, wr, wi, task, w);
    VMCNT(0);
  }
  F1T(1);
  c32 v[16];
  const u16* in = (const u16*)eb;
#pragma unroll
  for (int t = 0; t < 16; ++t) v[t] = c32{bf2f(in[lane + 64 * t]), bf2f(in[D + lane + 64 * t])};
  unsigned fw[2][9];
#pragma unroll
  for (int r = 0; r < 2; ++r)
#pragma unroll
    for (int t = 0; t < 9; ++t) {
      const int o = 2048 + ((fo & 15) >> 1) + r * DF + min(lane + 64 * t, 512);
      fw[r][t] = (unsigned)in[o] | ((unsigned)in[o + 1040] << 16);
    }
  lds_fence_wave();
  F1T(2);
  wave_fft<1024, -1>(v, eb, tw, lane);
  F1T(3);
#pragma unroll
  for (int t = 0; t < 16; ++t) eb[lane + 64 * t] = v[t];
  lds_fence_wave();
#pragma unroll
  for (int t = 0; t < 9; ++t) {
    const int k = lane + 64 * t;
    if (k <= 512) {
      c32 z = eb[k], zc = eb[(1024 - k) & 1023];
      *(f32x4*)&stg[k][2 * (w ^ (k & 7))] = f32x4{0.5f * (z.x + zc.x), 0.5f * (z.y - zc.y), 0.5f * (z.y + zc.y), -0.5f * (z.x - zc.x)};
    }
  }
  __syncthreads();
  F1T(4);
#pragma unroll
  for (int i = 0; i < 9; ++i) {
    const int idx = tid_ + 512 * i, k = min(idx >> 3, 512), p = idx & 7;
    v4u d = *(const v4u*)&stg[k][2 * (p ^ (k & 7))];
    st128(d, rx, idx < 513 * 8 ? lane * 16 : OOB, x1_chunk(min(w + 8 * i, 64), task) * 1024);
  }
  if (!(FOW && xb)) {
    const __amdgpu_buffer_rsrc_t rt = __builtin_amdgcn_make_buffer_rsrc(ws + OFF_WT, 0, 65 * 65536, 0x00020000);
    v2u* s2 = (v2u*)(lds + LDS_EB);
    u16 fr[2][9], fi[2][9];
    if (FOW) {
#pragma unroll
      for (int r = 0; r < 2; ++r)
#pragma unroll
        for (int t = 0; t < 9; ++t) {
          const int o = ((r0 ^ 1024) + r) * DF + min(lane + 64 * t, 512);
          fr[r][t] = __builtin_nontemporal_load(wr + o);
          fi[r][t] = __builtin_nontemporal_load(wi + o);
        }
    }
#pragma unroll 1
    for (int z = 0; z <= FOW; ++z) {
#pragma unroll
      for (int t = 0; t < 9; ++t) {
        const int k = lane + 64 * t;
        if (k <= 512) s2[k * 8 + (w ^ (k & 6))] = v2u{fw[0][t], fw[1][t]};
      }
      RAW_BARRIER();
#pragma unroll
      for (int i = 0; i < 5; ++i) {
        const int idx = tid_ + 512 * i, k = min(idx >> 2, 512), p = idx & 3;
        const v4u d = *(const v4u*)&s2[k * 8 + ((2 * p) ^ (k & 6))];
        __builtin_amdgcn_raw_buffer_store_b128(d, rt, idx < 514 * 4 ? x1_chunk(idx >> 5, task ^ (64 * z)) * 512 + (idx & 31) * 16 : OOB, 0, AUX_SC1);
      }
      if (FOW) {
        RAW_BARRIER();
#pragma unroll
        for (int k = 0; k < 18; ++k) fw[k / 9][k % 9] = (unsigned)fr[k / 9][k % 9] | ((unsigned)fi[k / 9][k % 9] << 16);
      }
    }
  }
}
__device__ __forceinline__ void fft_pass2(char* ws, int task, int w) {
  char* lds = g_lds;
  const __amdgpu_buffer_rsrc_t rx = __builtin_amdgcn_make_buffer_rsrc(ws + OFF_X1, 0, 65 * 131072, 0x00020000);
  const int lane = olane();
  const int k = task * 8 + w;
  const c32* tw = (const c32*)(lds + LDS_TW);
  c32* eb = (c32*)(lds + LDS_EB) + w * FFT_EB;
#define F2T(i)
  if (k > 512) return;
  F2T(0);
  c32 v[32];
  const int xoff = (lane >> 4) * 1024 + w * 128 + (lane & 15) * 8;
#pragma unroll
  for (int t = 0; t < 32; ++t) {
    v2u d = __builtin_amdgcn_raw_buffer_load_b64(rx, xoff, x1_chunk(task, 4 * t) * 1024, AUX_SC1);
    v[t] = c32{__uint_as_float(d[0]), __uint_as_float(d[1])};
  }
  unsigned wv[32];
  asm volatile("" : "+s"(task));
  {
    const __amdgpu_buffer_rsrc_t rt = __builtin_amdgcn_make_buffer_rsrc(ws + OFF_WT, 0, 65 * 65536, 0x00020000);
#pragma unroll
    for (int t = 0; t < 32; ++t)
      wv[t] = __builtin_amdgcn_raw_buffer_load_b32(rt, xoff >> 1, x1_chunk(task, 4 * t) * 512, AUX_SC1);
  }
  F2T(1);
  wave_fft<2048, -1>(v, eb, tw, lane);
  F2T(2);
#pragma unroll
  for (int t = 0; t < 32; ++t) v[t] = cmul<false>(v[t], c32{__uint_as_float(wv[t] << 16), __uint_as_float(wv[t] & 0xffff0000u)});
  F2T(3);
  wave_fft<2048, 1>(v, eb, tw, lane);
  F2T(4);
  asm volatile("" : "+s"(task));
#pragma unroll
  for (int t = 0; t < 32; ++t) {
    v2u d = {__float_as_uint(v[t].x), __float_as_uint(v[t].y)};
    __builtin_amdgcn_raw_buffer_store_b64(d, rx, xoff, x1_chunk(task, 4 * t) * 1024, AUX_SC1);
  }
  F2T(5);
  VMCNT(0);
  F2T(6);
}
#define OAUX3 16
#define OAUXX 0
__device__ __forceinline__ void fft_pass3(const char* ws, u16* __restrict__ out, int task, int w) {
  char* lds = g_lds;
  const __amdgpu_buffer_rsrc_t rx = __builtin_amdgcn_make_buffer_rsrc((void*)(ws + OFF_X1), 0, 65 * 131072, 0x00020000);
  const int lane = olane(), tid_ = w * 64 + lane;
  const c32* tw = (const c32*)(lds + LDS_TW);
  c32* eb = (c32*)(lds + LDS_EB) + w * 1088;
  c32(*stg)[16] = (c32(*)[16])(lds + LDS_STG);
  {
    v4u d[9];
#pragma unroll
    for (int i = 0; i < 9; ++i) {
      d[i] = __builtin_amdgcn_raw_buffer_load_b128(rx, (i < 8 ? lane : lane & 7) * 16, x1_chunk(min(w + 8 * i, 64), task) * 1024, AUX_SC1);
    }
#pragma unroll
    for (int i = 0; i < 9; ++i) {
      const int idx = tid_ + 512 * i, k = min(idx >> 3, 512), p = idx & 7;
      *(v4u*)&stg[k][2 * (p ^ (k & 7))] = d[i];
    }
  }
  __syncthreads();
  c32 v[16];
#pragma unroll
  for (int t = 0; t < 16; ++t) {
    const int n = lane + 64 * t;
    const int k = n <= 512 ? n : 1024 - n;
    const f32x4 xx = *(const f32x4*)&stg[k][2 * (w ^ (k & 7))];
    c32 xa = {xx[0], xx[1]}, xb = {xx[2], xx[3]};
    if (k == 0 || k == 512) {
      xa.y = 0.f;
      xb.y = 0.f;
    }
    v[t] = n <= 512 ? c32{xa.x - xb.y, xa.y + xb.x} : c32{xa.x + xb.y, -xa.y + xb.x};
  }
  wave_fft<1024, 1>(v, eb, tw, lane);
  const float sc = 1.f / (2048.f * 1024.f);
  const int r0 = task * 16 + 2 * w;
  const __amdgpu_buffer_rsrc_t ro = __builtin_amdgcn_make_buffer_rsrc(out, 0, S * 4096, 0x00020000);
#pragma unroll
  for (int t = 0; t < 16; ++t) {
    __builtin_amdgcn_raw_buffer_store_b16(f2bf(v[t].x * sc), ro, lane * 2 + t * 128, r0 * 4096, OAUX3);
    __builtin_amdgcn_raw_buffer_store_b16(f2bf(v[t].y * sc), ro, lane * 2 + t * 128, r0 * 4096 + 4096, OAUX3);
  }
}

#define OB2 0
#define FIE 0
#define FLC 0
#define WBP 8
#define WBQ 2
#define COT 100
#define CUP 24
#define CDN 1
#define CLO -40
#define CHI 30
#define TLW 900
#define OSL 4

#define OCK 800
#define OTRY 3
#define APR 0
#define OGN 5
#define OK1 150
#define NX2 0
#define WTO 2
#define WTS 30
#define WTN 2
#define RSO 5
#define NCAL 6
#define DOX2 200
#define DTC 100
#define NX1 4
#define NXG 2
#define NXK 5
#define OFD 200
#define STI 600
#define TNX 0
#define NXP 0
#define PACE 150
#define PFB 4
#define CV1 -20
#define CV2 125
#define EDL 0
constexpr int L_D = 16384, L_R = 32768, L_F = 122880;
__device__ __forceinline__ volatile LAS unsigned* flw(int i, int m) {
  return (volatile LAS unsigned*)((LAS char*)g_lds + L_F + i * 4 + (olane() & m) * 4);
}
template <int SLP = 0>
__device__ __forceinline__ void lds_wait_ge(int i, int m, unsigned v, gu32* tmo, unsigned code) {
  volatile LAS unsigned* p = flw(i, m);
  _Pragma("clang loop unroll(disable)") for (unsigned spins = 0;;) {
    if (__all(*p >= v)) break;
    if (SLP) __builtin_amdgcn_s_sleep(SLP);
    if (++spins > (kSpinCap << 5)) {
      tmo_report(tmo, code);
      break;
    }
  }
  asm volatile("" ::: "memory");
}
template <bool PRE>
__device__ __forceinline__ unsigned owner(const int c, const int w, const u64 nonce, const u16* __restrict__ mem0, char* ws,
                                          u16* __restrict__ out, unsigned perm, const char* wsl, const u16* ax, const u16* awr,
                                          const u16* awi) {
  LAS char* const L = (LAS char*)g_lds;
  gu32* tmo = (gu32*)(ws + OFF_TMO);
  const int lane = olane();
  const int l15 = lane & 15, g = lane >> 4;
  const __amdgpu_buffer_rsrc_t rs = __builtin_amdgcn_make_buffer_rsrc(ws, 0, (int)WS_USED, 0x00020000);
  const int xg = c / PO, li = c % PO;
  const int r_a = (int)(((uintptr_t)ws >> 19) & 7);
  const int r_s = r_a + (int)(OFF_STEP / UNIT);
  volatile LAS unsigned* fl = (volatile LAS unsigned*)(L + L_F);
  unsigned m0v = 0;
  if constexpr (PRE) {
  if (c == 0 && w == 6) hs_lead<4>(rs, true, CNT_HSO, 64u);
  if (WTO == 1 && wsl) {
    __builtin_amdgcn_s_sleep(WTS);
    for (int k = 0; k < WTN; ++k) w_touch(wsl, (li >> 1) * 128 + 64 + (li & 1) * 32 + (k & 1) * 16 - (k >> 1) * 64, 160000 + w * 256);
  }
  unsigned cal = 0;
  if (w == 0)
    st128(v4u{(unsigned)lane, 0u, 0u, 0u}, rs, (lane & 31) * 16 + (lane >> 5) * 32768, (int)OFF_SCR + (((li & 7) ^ xreg(r_a)) << 12) + (c >> 3) * 512);
  if (w == 0 && lane == 0) {
    const int cl = li & 7;
    int off = (int)OFF_SCR + ((cl ^ xreg(r_a)) << 12) + (c >> 3) * 512;
    unsigned best = 0xfffeu;
#pragma unroll 1
    for (int r = 0; r < NCAL; ++r) {
      VMCNT(0);
      const u64 t1 = __builtin_amdgcn_s_memtime();
      asm volatile("s_waitcnt lgkmcnt(0)" : "+v"(off)::"memory");
      const unsigned v = __builtin_amdgcn_raw_buffer_load_b32(rs, off, (r & 3) * 128 + (r >> 2) * 32768, AUX_SC1);
      asm volatile("s_waitcnt vmcnt(0)" ::"v"(v) : "memory");
      const u64 t2 = __builtin_amdgcn_s_memtime();
      best = min(best, (unsigned)(t2 - t1));
    }
    cal = ((unsigned)cl << 16) | best;
  }
  if (w == 0) {
    m0v = *(const unsigned*)(mem0 + (lane >> 3) * D + 16 * c + 2 * (lane & 7));
    hs_wait(rs, ws, nonce, CNT_HSO);
    unsigned id;
    asm volatile("s_getreg_b32 %0, hwreg(HW_REG_XCC_ID)" : "=s"(id));
    if (lane == 0) {
      __builtin_amdgcn_raw_buffer_store_b32(((id & 15u) << 19) | cal, rs, c * CST, (int)OFF_CAL, AUX_SC1);
    }
  }
  __syncthreads();
  if (FIE == 1) fft_in1<!FOW>(ax, awr, awi, c, w);
  if (WTO == 2 && wsl)
    for (int k = 0; k < WTN; ++k) w_touch(wsl, (li >> 1) * 128 + 64 + (li & 1) * 32 + (k & 1) * 16 - (k >> 1) * 64, 160000 + w * 256);
  if (FIE == 2) fft_in1<!FOW>(ax, awr, awi, c, w);
  if (w == 7) {
    const unsigned pm = numa_perm(numa_load(rs, tmo), (LAS unsigned*)(L + L_F + 512));
    fl[10] = pm;
  }
  RAW_BARRIER();
  perm = __builtin_amdgcn_readfirstlane(fl[10]);
  }
  const int xr0 = xreg(r_s + (PRE ? w : NARM + (li & 1)));
  const v4u sent = {0xffffffffu, 0xffffffffu, 0xffffffffu, 0xffffffffu};
  const int ibs = (int)OFF_VM + li * 65536 + ((cls_of(perm, xg) ^ xreg(r_a + (int)(OFF_VM / UNIT) + (li >> 3))) << 12);

  int am = (int)OFF_STEP + R_MEM + (c >> 4) * 32768 + ((cls_of(perm, g) ^ xr0) << 12) + (c & 15) * 256 + l15 * 16;
  int ap[2];
#pragma unroll
  for (int q = 0; q < 2; ++q) {
    const int o = c * 384 + (lane & 31) * 16;
    ap[q] = (lane & 31) < 24 ? (int)OFF_STEP + R_EX + (o >> 12) * 32768 + ((cls_of(perm, 2 * q + (lane >> 5)) ^ xr0) << 12) + (o & 4095)
                             : OOB;
  }
  int as = lane < 8 * NXC ? (int)OFF_STEP + R_ST + ((cls_of(perm, lane >> 3) ^ xr0) << 12) + (c >> 5) * 32768 + (c & 31) * 128 + (lane & 7) * 16
                          : OOB;
  const int ax1 = (int)OFF_STEP + R_PX + c * 1280 + lane * 16;
  const int ax2 = lane < 16 ? ax1 + 1024 : (lane < 24 ? (int)OFF_STEP + R_DUM + c * 128 + (lane - 16) * 16 : OOB);
#define ARM_REC(r)                                                                                                   \
  {                                                                                                                  \
    const int so_ = (r) * (int)UNIT, dx_ = (xreg(r_s + (r)) ^ xreg(r_s + (r) + 1)) << 12;                            \
    __builtin_amdgcn_raw_buffer_store_b128(sent, rs, am, so_, AUX_SC1);                                              \
    am ^= dx_;                                                                                                       \
    _Pragma("unroll") for (int q = 0; q < 2; ++q) {                                                                  \
      __builtin_amdgcn_raw_buffer_store_b128(sent, rs, ap[q], so_, AUX_SC1);                                         \
      ap[q] ^= dx_;                                                                                                  \
    }                                                                                                                \
    __builtin_amdgcn_raw_buffer_store_b128(sent, rs, as, so_, AUX_SC1);                                              \
    as ^= dx_;                                                                                                       \
    __builtin_amdgcn_raw_buffer_store_b128(sent, rs, ax1, so_, AUX_SC1);                                              \
    __builtin_amdgcn_raw_buffer_store_b128(sent, rs, ax2, so_, AUX_SC1);                                             \
  }
  if constexpr (PRE) {
    if (WTO == 3 && wsl)
      for (int k = 0; k < WTN; ++k) w_touch(wsl, (li >> 1) * 128 + 64 + (li & 1) * 32 + (k & 1) * 16 - (k >> 1) * 64, 160000 + w * 256);
    if (!FIE) fft_in1<!FOW>(ax, awr, awi, c, w);
    if (w < NARM + (li & 1)) {
      if (w == 0) {
        const int row = lane >> 3, np = lane & 7;
#pragma unroll
        for (int r = 0; r < NXC; ++r)
          __builtin_amdgcn_raw_buffer_store_b32(min(m0v, 0xfffffffeu), rs,
                                                (int)OFF_STEP + R_MEM + (c >> 4) * 32768 + ((cls_of(perm, r) ^ xr0) << 12) + (c & 15) * 256 +
                                                    ((row & 1) + 2 * (np & 1) + 4 * (row >> 1)) * 16 + (np >> 1) * 4,
                                                0, AUX_SC1);
        am = OOB;
      }
      ARM_REC(w)
    }
#pragma unroll
    for (int k = 0; k < 4; ++k)
      __builtin_amdgcn_raw_buffer_store_b128(sent, rs, lane * 16 + k * 1024, ibs + vm_slot(w >> 1) + (w & 1) * 32768, AUX_SC1);
    if (c == 0 && w == 7) __builtin_amdgcn_raw_buffer_store_b32(perm, rs, lane << 28, OFF_PERM, AUX_SC1);
  }
  if constexpr (PRE) return perm;
  if (FLC || OB2) RAW_BARRIER();
  if (w == 7) {
    if (lane < 32) fl[lane] = 0;
    arm_wait(rs, tmo);
  }
  if (OB2) RAW_BARRIER();
  if (FLC && (w < 2 || w == 7)) VMCNT(0);

  const int team = li & 1, c0 = c & ~1;
  const int l16 = lane * 16;
  const int cown = cls_of(perm, xg);
  if (w < 2) {
    const int eg = (c0 >> 2) & 1;
    typedef int v8i __attribute__((ext_vector_type(8)));
    v8i adr;
    adr[0] = l16;
    adr[1] = (lane >> 2) * 128 + (lane & 3) * 16;
    adr[2] = L_D + w * 8192 + eg * 4096 + l16;
    adr[3] = ((g & 1) + (l15 & 2) + 4 * ((g & 2) + (l15 & 1))) * 16 + (l15 >> 2) * 4;
    adr[4] = (l15 & 1) ? 0x03020706 : 0x05040100;
    adr[5] = ((l15 & 8) ? 0xeeee : 0x4444) | ((l15 & 2) ? 0xeeee0000 : 0x44440000);
    adr[6] = L_F + 48;
    adr[7] = adr[2] ^ 4096;
    const v4u rsv = {(unsigned)(uintptr_t)ws, (unsigned)((uintptr_t)ws >> 32) & 0xffffu, (unsigned)WS_USED, 0x00020000u};
    const int dtc_ = TN(12, DTC);
#pragma unroll 1
    for (int s = team; s < NSEG; s += 2) {
      EVDECL;
      const int so = (int)OFF_STEP + s * (int)UNIT;
      const int sp = so + R_PM + ((cown ^ xreg(r_s + s)) << 12);
      int sv = ibs + (w - team) * 65536 + vm_slot(s);
      int sv1 = sv + eg * 32768, sv2 = sv + (1 - eg) * 32768, sp1 = sp + eg * 32768, sp2 = sp + (1 - eg) * 32768;
      const int xn = xreg(r_s + s + 1);
      int sm[NXC];
#pragma unroll
      for (int q = 0; q < NXC; ++q) {
        sm[q] = so + (int)UNIT + R_MEM + (c0 >> 4) * 32768 + ((cls_of(perm, q) ^ xn) << 12) + ((c0 + w) & 15) * 256;
        asm volatile("" : "+s"(sm[q]));
      }
      asm volatile("" : "+s"(sv1), "+s"(sv2), "+s"(sp1), "+s"(sp2));
      if (s >= 2) lds_wait_ge<4>(20, 3, s - 1, tmo, 0x30017);
      v4u bl[8];
      u64 tp64 = 0;
      if (s < NSEG - 1) {
        v32u blv;
        unsigned cnt, bad;
        asm volatile(OW_F
                     : [cnt] "=&s"(cnt), [st] "=&s"(bad), [tp] "=&s"(tp64), "={v[0:31]}"(blv) EVOUT
                     : "{v[112:119]}"(adr), [rs] "s"(rsv), [sv1] "s"(sv1), [sv2] "s"(sv2), [sp1] "s"(sp1), [sp2] "s"(sp2),
                       [sm0] "s"(sm[0]), [sm1] "s"(sm[1]), [sm2] "s"(sm[2]), [sm3] "s"(sm[3])
                     : OF_CLOB, "vcc", "scc", "memory");
#pragma unroll
        for (int k = 0; k < 8; ++k) bl[k] = v4u{blv[4 * k], blv[4 * k + 1], blv[4 * k + 2], blv[4 * k + 3]};
        if (__builtin_expect(bad != 0u, 0)) {
          v4u pv[8];
          _Pragma("clang loop unroll(disable)") for (unsigned spins = 0;;) {
            OPQ(va, l16);
            OPQ(v2, adr[1]);
#pragma unroll
            for (int k = 0; k < 8; ++k) bl[k] = __builtin_amdgcn_raw_buffer_load_b128(rs, va + (k & 3) * 1024, k < 4 ? sv1 : sv2, AUX_SC1);
#pragma unroll
            for (int k = 0; k < 8; ++k)
              pv[k] = __builtin_amdgcn_raw_buffer_load_b128(rs, v2 + (k & 1) * 2048 + (k >> 2) * 64, (k & 2) ? sp2 : sp1, AUX_SC1);
            unsigned m = 0;
#pragma unroll
            for (int k = 0; k < 8; ++k) m = mx4(mx4(m, pv[k]), bl[k]);
            if (!anysent(m)) break;
            __builtin_amdgcn_s_sleep(RSO);
            if (spin_fail(spins, tmo, 0x20000 + s)) break;
          }
          f32x4 a0 = {0.f, 0.f, 0.f, 0.f};
#pragma unroll
          for (int k = 0; k < 4; ++k) {
            v8u b = cat8(bl[2 * k], bl[2 * k + 1]);
#pragma unroll
            for (int j = 0; j < 8; j += 2) {
              const auto q = __builtin_amdgcn_permlane16_swap(b[j], b[j + 1], false, false);
              b[j] = q[0];
              b[j + 1] = q[1];
            }
            a0 = smf(pv[k], b, a0, adr[5]);
          }
#pragma unroll
          for (int k = 0; k < 4; ++k) {
            LAS char* const pd = L + adr[k < 2 ? 2 : 7] + (k & 1) * 2048;
            a0 = smf1(pv[4 + k], cat8(*(const LAS v4u*)pd, *(const LAS v4u*)(pd + 1024)), a0, adr[5]);
          }
          const auto q0 = __builtin_amdgcn_permlane32_swap(__float_as_uint(a0[0]), __float_as_uint(a0[2]), false, false);
          const auto q1 = __builtin_amdgcn_permlane32_swap(__float_as_uint(a0[1]), __float_as_uint(a0[3]), false, false);
          const unsigned own = pk2(__uint_as_float(q0[0]) + __uint_as_float(q0[1]), __uint_as_float(q1[0]) + __uint_as_float(q1[1]));
          const unsigned nb = (unsigned)__builtin_amdgcn_mov_dpp((int)own, 0xb1, 0xf, 0xf, true);
          const unsigned o = __builtin_amdgcn_perm(nb, own, (unsigned)adr[4]);
#pragma unroll
          for (int q = 0; q < NXC; ++q) __builtin_amdgcn_raw_buffer_store_b32(o, rs, adr[3], sm[q], AUX_SC1);
          tp64 = cyc32();
          EV(3);
        }
      } else {
        BARE_BARRIER();
        wait_cyc(*flw(12, 0));
        _Pragma("clang loop unroll(disable)") for (unsigned spins = 0;;) {
          OPQ(va, l16);
          unsigned m = 0;
#pragma unroll
          for (int k = 0; k < 8; ++k) bl[k] = __builtin_amdgcn_raw_buffer_load_b128(rs, va + (k & 3) * 1024, k < 4 ? sv1 : sv2, AUX_SC1);
#pragma unroll
          for (int k = 0; k < 8; ++k) m = mx4(m, bl[k]);
          if (!anysent(m)) break;
          __builtin_amdgcn_s_sleep(RSO);
          if (spin_fail(spins, tmo, 0x20000 + s)) break;
        }
      }
      const unsigned tp = (unsigned)tp64;
      *flw(2 + w, 0) = s + 1;
      if (s < NSEG - 1) {
        const v4u tv = *(volatile LAS v4u*)flw(12, 0);
        const int lr = olane();
        __builtin_amdgcn_raw_buffer_store_b32(min(((tp - tv[1]) * tv[2]) >> 16, 60000u), rs,
                                              ((cls_of(perm, lr & 3) ^ xn) << 12) | ((lr & 4) << 9) | ((lr & 8) << 12) | ((lr >> 4) << 28),
                                              so + (int)UNIT + R_ST + (c0 + w) * 32 + 4, AUX_SC1);
      }
      OPQ(vs, l16);
#pragma unroll
      for (int k = 0; k < 8; ++k) *(LAS v4u*)(L + w * 8192 + ((k ^ (4 * eg)) * 1024) + vs) = bl[k];
      asm volatile("s_waitcnt lgkmcnt(0)" ::: "memory");
      *flw(4 + w, 0) = s + 1;
      if (s < NSEG - 4) {
        lds_wait_ge(2, 1, s + 1, tmo, 0x3001a);
        v4u se;
        asm volatile("v_mov_b32 %0, -1" : "=v"(se[0]));
        se[1] = se[2] = se[3] = se[0];
#pragma unroll
        for (int k = 0; k < 8; ++k) __builtin_amdgcn_raw_buffer_store_b128(se, rs, vs + (k & 3) * 1024, sv + (k >> 2) * 32768, AUX_SC1);
        asm volatile("s_nop 1" ::"v"(se));
      }
      if (s < NSEG - 2) {
        wait_cyc(tp + dtc_);
        const int lt = olane();
        const unsigned tc = __builtin_amdgcn_raw_buffer_load_b32(rs, lt < 40 ? lt * 128 : OOB, so + R_PX + ((li & ~1) + w) * 5120, AUX_SC1);
        asm volatile("" ::"v"(tc));
      }
      EVDUMP(s);
    }
  } else if (w < 6) {
    const int n = w - 2;
    const unsigned vpm_ = perm | (unsigned)((r_a + (int)(OFF_VWX / UNIT)) & 7) << 12;
    const int dox_ = TN(10, DOX2), pace_ = TN(11, PACE);
    v4u raw[2][8];
#define RAW_LD(dst, sg)                                                                                       \
  _Pragma("unroll") for (int t = 0; t < 2; ++t) _Pragma("unroll") for (int k = 0; k < 8; ++k) dst[t][k] =     \
      __builtin_amdgcn_raw_buffer_load_b128(rs, l16 + (k & 3) * 1024, vw_addr(vpm_, c0 + t, sg, 2 * n + (k >> 2)), AUX_SC1);
#pragma unroll
    for (int k = 0; k < 16; ++k) asm volatile("" : "=v"(raw[k >> 3][k & 7]));
    _Pragma("clang loop unroll(disable)") for (unsigned spins = 0, got = 0; got != 255u;) {
      OPQ(vfl, F_VW + flag_of(team, 8 * n + (olane() & 7)));
      const unsigned nw = (unsigned)__builtin_amdgcn_ballot_w64(__builtin_amdgcn_raw_buffer_load_b32(rs, vfl, 0, AUX_SC1) == 0u) & 255u & ~got;
      got |= nw;
#pragma unroll
      for (int k = 0; k < 8; ++k)
        if (nw >> k & 1u) {
#pragma unroll
          for (int t = 0; t < 2; ++t)
            raw[t][k] = __builtin_amdgcn_raw_buffer_load_b128(rs, l16 + (k & 3) * 1024, vw_addr(vpm_, c0 + t, team, 2 * n + (k >> 2)), AUX_SC1);
        }
      if (!nw) __builtin_amdgcn_s_sleep(OSL);
      if (spin_fail(spins, tmo, 0x30018)) break;
    }
    if (FLC) VMCNT(0);
    int nrdy = team;
#pragma unroll 1
    for (int s = team; s < NSEG; s += 2) {
      EVDECL;
      const int so = (int)OFF_STEP + s * (int)UNIT;
      if (s < NSEG - 1) {
        const int ln = olane();
        OPQ(eidx, (ln & 4) ? 0xeeee : 0x4444);
        OPQ(vea, (ln & 4) * 128 + (ln & 8) * 32 + (ln >> 4) * 64 + (ln & 3) * 16);
        int se = so + R_EX + n * 32768 + ((cown ^ xreg(r_s + s)) << 12);
        v8u bx[2][4];
#pragma unroll
        for (int t = 0; t < 2; ++t)
#pragma unroll
          for (int k = 0; k < 4; ++k) {
            const v4u u = raw[t][2 * k], v = raw[t][2 * k + 1];
            bx[t][k] = v8u{u[0], v[0], u[1], v[1], u[2], v[2], u[3], v[3]};
            asm volatile("" : "+v"(bx[t][k]));
          }
        OPQ(vd, L_D + n * 2048 + (ln & 31) * 16 + (ln >> 5) * 8);
        f32x4 d[2][4];
#pragma unroll
        for (int t = 0; t < 2; ++t)
#pragma unroll
          for (int k = 0; k < 4; ++k) d[t][k] = zero4();
        asm volatile("" : "+s"(se));
        EV(3);
        asm volatile(AL(A_OW_D));
        BARE_BARRIER();
        if (EDL) asm volatile("s_nop %0" ::"n"(EDL > 0 ? EDL - 1 : 0));
        EV(0);
#define E_PACK(t, k) v2u{pk2(d[t][k][0], d[t][k][1]), pk2(d[t][k][2], d[t][k][3])}
        _Pragma("clang loop unroll(disable)") for (unsigned spins = 0;;) {
          v4u pv[4];
          asm volatile("" : "+v"(vea));
#pragma unroll
          for (int k = 0; k < 4; ++k) pv[k] = __builtin_amdgcn_raw_buffer_load_b128(rs, vea + k * 1024, se, AUX_SC1);
          unsigned m = 0;
          v2u cv[2][4];
          SB();
#pragma unroll
          for (int k = 0; k < 4; ++k) {
            d[0][k] = smf(pv[k], bx[0][k], d[0][k], eidx);
            m = mx4(m, pv[k]);
            asm volatile("" : "+v"(m), "+v"(d[0][k]));
            d[1][k] = smf(pv[k], bx[1][k], d[1][k], eidx);
            asm volatile("" : "+v"(d[1][k]));
            if (k > 0) {
              const int j = k > 0 ? k - 1 : 0;
              asm volatile("" : "+v"(d[0][j]), "+v"(d[1][j]));
              cv[0][j] = E_PACK(0, j);
              cv[1][j] = E_PACK(1, j);
              *(LAS v2u*)(L + vd + j * 512) = cv[0][j];
              *(LAS v2u*)(L + vd + j * 512 + 8192) = cv[1][j];
            }
            SB();
          }
          EV(1);
          *(LAS v2u*)(L + vd + 1536) = E_PACK(0, 3);
          SB();
          *(LAS v2u*)(L + vd + 9728) = E_PACK(1, 3);
          if (__builtin_expect(!anysent(m), 1)) break;
          __builtin_amdgcn_s_sleep(RSO);
          if (spin_fail(spins, tmo, 0x10000 + s)) break;
#pragma unroll
          for (int t = 0; t < 2; ++t)
#pragma unroll
            for (int k = 0; k < 4; ++k) d[t][k] = zero4();
        }
        EV(2);
        RAW_BARRIER();
      } else BARE_BARRIER();
      v4u nx[2][8];
      const int sn = min(s + 2, NSEG - 1);
      unsigned tpb = 0;
      if (s < NSEG - 2) {
        __builtin_amdgcn_s_sleep(5);
        lds_wait_ge(2, 1, s + 1, tmo, 0x30014);
        tpb = cyc32();
        const unsigned tw = tpb + dox_;
        _Pragma("clang loop unroll(disable)") for (unsigned spins = 0; nrdy < s + 2;) {
          const int lf = olane();
          OPQ(vfl, F_VW + flag_of(min(s + 2 + 2 * (lf >> 3), NSEG - 2 + team), 8 * n + (lf & 7)));
          const u64 miss = __builtin_amdgcn_ballot_w64(__builtin_amdgcn_raw_buffer_load_b32(rs, vfl, 0, AUX_SC1) != 0u);
          nrdy = s + 2 * (miss ? __builtin_ctzll(miss) >> 3 : 8);
          if (nrdy >= s + 2) break;
          __builtin_amdgcn_s_sleep(8);
          if (spin_fail(spins, tmo, 0x30018)) break;
        }
        wait_cyc(tw);
      }
      EV(6);
      OPQ(va, s < NSEG - 2 ? l16 : OOB);
#define NX_LD(dst, q0, q1)                                                                              \
  _Pragma("unroll") for (int q = q0; q < q1; ++q) dst[q >> 3][q & 7] = __builtin_amdgcn_raw_buffer_load_b128( \
      rs, va + (q & 3) * 1024, vw_addr(vpm_, c0 + (q >> 3), sn, 2 * n + ((q >> 2) & 1)), AUX_SC1);
      NX_LD(nx, 0, NX1)
      SB();
      if (s < NSEG - 2) wait_cyc(cyc32() + ((n + (li >> 1)) & 3) * pace_);
      else if (s == NSEG - 2 || !TLW) lds_wait_ge(4, 1, s + 1, tmo, 0x30014);
      else wait_cyc(cyc32() + TLW);
      EV(4);
      f32x4 ac[2][2];
      const int sx = so + R_PX + n * 8192, sy = so + R_PX + 32768 + n * 2048;
      v4u pv[2][10], vm[2][2];
      OPQ(vb, l16);
#define P_LD                                                                                         \
  _Pragma("unroll") for (int k = 0; k < 10; ++k) _Pragma("unroll") for (int rt = 0; rt < 2; ++rt) pv[rt][k] = \
      __builtin_amdgcn_raw_buffer_load_b128(rs, vb + (k & 3) * 1024, (k < 8 ? sx + (k >> 2) * 4096 : sy) + rt * 40960, AUX_SC1);
#define P_MM(k0, k1)                                                                                 \
  _Pragma("unroll") for (int k = k0; k < k1; ++k) _Pragma("unroll") for (int rt = 0; rt < 2; ++rt) {  \
    _Pragma("unroll") for (int t = 0; t < 2; ++t)                                                    \
      ac[rt][t] = mma(k < 8 ? raw[t][k & 7] : vm[t][k & 1], pv[rt][k], k == 0 ? f32x4{0.f, 0.f, 0.f, 0.f} : ac[rt][t]); \
    asm volatile("" : "+v"(ac[rt][0]), "+v"(ac[rt][1]));                                             \
  }
      P_LD
      SB();
      NX_LD(nx, NX1, NX1 + NX2)
      SB();
      lds_wait_ge(4, 1, s + 1, tmo, 0x30014);
#pragma unroll
      for (int t = 0; t < 2; ++t)
#pragma unroll
        for (int k = 0; k < 2; ++k) vm[t][k] = *(const LAS v4u*)(L + vb + t * 8192 + n * 2048 + k * 1024);
      SB();
      P_MM(0, 10)
      if (__builtin_expect(__builtin_amdgcn_ballot_w64(__builtin_isunordered(ac[0][0][0], ac[1][0][0])) != 0ull, 0)) {
        _Pragma("clang loop unroll(disable)") for (unsigned spins = 0;;) {
          asm volatile("" : "+v"(vb));
          P_LD
          unsigned m = 0;
#pragma unroll
          for (int k = 0; k < 10; ++k) m = mx4(mx4(m, pv[0][k]), pv[1][k]);
          if (!anysent(m)) break;
          __builtin_amdgcn_s_sleep(8);
          if (spin_fail(spins, tmo, 0x30015)) break;
        }
        P_MM(0, 10)
      }
      EV(7);
      NX_LD(raw, NX1 + NX2, 16)
      SB();
      OPQ(vr, L_R + (s & 2) * 8192 + l16);
#pragma unroll
      for (int q = 0; q < 4; ++q) *(LAS f32x4*)(L + vr + q * 4096 + n * 1024) = ac[q >> 1][q & 1];
      asm volatile("s_waitcnt lgkmcnt(0)" ::: "memory");
      fl[20 + n] = s + 1;
      lds_wait_ge(20, 3, s + 1, tmo, 0x30016);
      {
        const LAS f32x4* pr = (const LAS f32x4*)(L + vr + n * 4096);
        const f32x4 o = (pr[0] + pr[64]) + (pr[128] + pr[192]);
        const int lo = olane();
        __builtin_amdgcn_raw_buffer_store_b64(v2u{pk2(o[0], o[1]), pk2(o[2], o[3])}, __builtin_amdgcn_make_buffer_rsrc(out, 0, S * 4096, 0x00020000),
                                              (lo & 15) * 8192 + (lo >> 4) * 8, (s * 32 + (n >> 1)) * 4096 + 2048 + 32 * (c0 + (n & 1)), OAUXX);
      }
      EV(5);
#pragma unroll
      for (int q = 0; q < NX1 + NX2; ++q) raw[q >> 3][q & 7] = nx[q >> 3][q & 7];
      EVDUMP(s);
    }
  } else if (w == 6) {
    if (c == 1) hs_lead<64>(rs, false, CNT_HS, 192u);
#pragma unroll 1
    for (int s = team; s + NARM - 2 < NSEG; s += 2) {
      VMCNT(0);
      BARE_BARRIER();
      if (FLC && s == team && olane() == 0) __hip_atomic_fetch_add((gu32*)(ws + CNT_FFT1), 1u, RLX_AGENT);
      if (s + NARM >= NSEG) break;
      BARE_BARRIER();
      __builtin_amdgcn_s_sleep(5);
      lds_wait_ge(2, 1, s + 1, tmo, 0x3001b);
      if (APR) __builtin_amdgcn_s_setprio(APR);
      ARM_REC(s + NARM)
      if (s + NARM + 1 < NSEG) ARM_REC(s + NARM + 1)
      if (APR) __builtin_amdgcn_s_setprio(0);
      if ((WBL & 4) && li == 0 && s % WBP == WBQ) asm volatile("buffer_wbl2 sc1" ::: "memory");
    }
  } else {
    const int sdk_ = NS2F(TN(2, SDK)), cv2_ = NS2F(TN(4, CV2));
    int cv1_ = NS2F(TN(3, CV1));
    bool clean = false;
    Clk ck;
    clk_init(ck);
    unsigned t1 = cyc32();
    const int mg_ = NS2F(TN(0, MARG)), tol_ = NS2F(TN(13, TOL)), xl_ = NS2F(TN(14, XLATE));
    Pace pc = {0u, NS2F(ED0), NS2F(EA0), 0};
    const int sti_ = TN(15, STI), ofd_ = NS2F(TN(9, OFD));
#pragma unroll 1
    for (int s = 0; s < NSEG; ++s) {
      EVDECL;
      const bool on = (s & 1) == team;
      asm volatile(AL(A_OW_T));
      EV(3);
      if (on ? (int)(t1 - cyc32()) > OCK : s < 4) {
        wait_cyc(t1 - OCK);
        clk_update<OTRY>(ck, s < 4 ? 12 : OGN);
      }
      int k1 = ((cv1_ * (int)ck.cq) >> 16) - OK1, k2 = (cv2_ * (int)ck.cq) >> 16;
      int sst = (int)OFF_STEP + s * (int)UNIT + R_ST + ((cown ^ xreg(r_s + s)) << 12);
      asm volatile("" : "+s"(k1), "+s"(k2), "+s"(sst));
      OPQ(va, lane * 32);
      OPQ(vt, L_F + 48);
      wait_cyc(t1);
      __builtin_amdgcn_s_setprio(3);
      EV(0);
      v2u st = __builtin_amdgcn_raw_buffer_load_b64(rs, va, sst, AUX_SC1);
      if (__builtin_expect(anysent(st[0]), 0)) _Pragma("clang loop unroll(disable)") for (unsigned spins = 0;;) {
        if (spins >= 2) __builtin_amdgcn_s_sleep(4);
        if (spin_fail(spins, tmo, 0x3000b)) break;
        asm volatile("" : "+v"(va));
        wait_cyc(t1 + spins * sti_);
        st = __builtin_amdgcn_raw_buffer_load_b64(rs, va, sst + (spins == 1 ? 2048 : (spins == 2 ? 32768 : 0)), AUX_SC1);
        if (spins > 2) clk_update(ck, 8);
        if (!anysent(st[0])) break;
      }
      EV(4);
      u64 tn;
      asm volatile("s_memtime %0" : "=s"(tn), "+v"(st));
      unsigned md;
      const int dm = stamp_max(ck, st, md);
      const unsigned tr = clk_cyc(ck, dm);
      if (on) {
        asm volatile("s_waitcnt lgkmcnt(0)" : "+s"(tn));
        *(volatile LAS v4u*)(L + vt) = v4u{tr + k2, tr, ck.inv, 0u};
        EV(1);
        int d = (int)(tr + k1 - (unsigned)tn);
        d = (unsigned)d > (unsigned)CMAX ? 0 : d;
        asm volatile(".p2align 4\n1: s_sub_u32 %0, %0, 28\n\ts_cbranch_scc0 1b\n\ts_barrier" : "+s"(d)::"scc", "memory");
        if (s < NSEG - 1) BARE_BARRIER();
      }
      __builtin_amdgcn_s_setprio(0);
      EV(5);
      const bool ol = md - (unsigned)(pc.ed + NS2F(COT)) < 60000u;
      if (s > 8) cv1_ = ol && clean ? min(cv1_ + CUP, NS2F(CHI)) : max(cv1_ - CDN, NS2F(CLO));
      pace_step(pc, s, (ck.r << 4) + dm, md, mg_, tol_, xl_);
      clean = !ol && pc.dq == pc.ed + mg_;
      if (c == 0 && (s & 3) == 1) __builtin_amdgcn_raw_buffer_store_b32((unsigned)s, rs, olane() << 28, OFF_PROG, AUX_SC1);
      t1 = clk_cyc(ck, dm + pc.dq + pc.ea - sdk_ + (on ? ofd_ : 0));
    }
  }
  return 0u;
}
#define WSH 0
#define STG2 0
#define STG3 0
#define FTL 0
#define CPE 0
#define FSL 32
__global__ __launch_bounds__(512, 2) void mega_kernel(const u16* __restrict__ x, const u16* __restrict__ mem0,
                                                      const u16* __restrict__ Wq, const u16* __restrict__ Wk,
                                                      const u16* __restrict__ Wv, const u16* __restrict__ Wo,
                                                      char* __restrict__ ws, u16* __restrict__ out,
                                                      const u16* __restrict__ ax, const u16* __restrict__ awr,
                                                      const u16* __restrict__ awi) {
  asm volatile(".Lkbeg:");
  if (CPE) {
    unsigned keep, t;
    const int w0 = __builtin_amdgcn_readfirstlane(threadIdx.x >> 6);
    asm volatile("s_getpc_b64 vcc\n.Lce:\n\ts_mov_b32 %0, m0\n\ts_mov_b32 m0, %4\n\ts_mov_b32 %1, .Lrole0-.Lce\n\t"
                 "s_cmp_eq_u32 %3, 1\n\ts_cselect_b32 %1, .Lrole1-.Lce, %1\n\ts_cmp_eq_u32 %3, 2\n\ts_cselect_b32 %1, .Lrole2-.Lce, %1\n\t"
                 "s_cmp_eq_u32 %3, 3\n\ts_cselect_b32 %1, .Lrole3-.Lce, %1\n\t"
                 "s_add_u32 vcc_lo, vcc_lo, %1\n\ts_addc_u32 vcc_hi, vcc_hi, 0\n\t"
                 "global_load_lds_dword %2, vcc\n\ts_mov_b32 m0, %0"
                 : "=&s"(keep), "=&s"(t)
                 : "v"(((threadIdx.x & 7) | (threadIdx.x >> 7) << 3) * 128), "s"((w0 & 1) | (blockIdx.x & 4) >> 1),
                   "s"((blockIdx.x & 132) == 128 ? 160000 + w0 * 256 : 32768 + w0 * 2048)
                 : "vcc", "scc", "memory");
  }
  ws += (size_t)(-(uintptr_t)ws) & (UNIT - 1);
  const int bx = blockIdx.x;
  const int w = __builtin_amdgcn_readfirstlane(threadIdx.x >> 6);
  const int xg = bx & 7, idx = bx >> 3;
#define TEND
#define TMID(k)
  const u64 nonce = hs_post(__builtin_amdgcn_make_buffer_rsrc(ws, 0, 65536, 0x00020000), w);
  TMID(0);
  int role, h, par = 0;
  if (xg < NXC) {
    role = idx >= 2 * PQ ? 4 : idx >= PQ;
    h = xg * PQ + idx % PQ;
  } else {
    role = 2 + (idx / PQ & 1);
    par = idx / (2 * PQ);
    h = (xg - NXC) * PQ + idx % PQ;
  }
  if (CPF && role == 4 && (w == 1 || w == 2)) code_touch(idx - 16 * (w - 1), 160000 + 256 * w);
  if (FTL && role == 4 && w >= 3 && w < 6) {
    unsigned keep;
    asm volatile("s_mov_b32 %0, m0\n\ts_mov_b32 m0, %3\n\ts_nop 0\n\tglobal_load_lds_dword %1, %2\n\ts_mov_b32 m0, %0"
                 : "=&s"(keep)
                 : "v"((xg * PO + idx - 2 * PQ) * (w == 3 ? 32768 : 16416) + (olane() & (FTL - 1)) * (16384 / (FTL ? FTL : 1))),
                   "s"(w == 3 ? ax : (w == 4 ? awr : awi)), "s"(160000 + w * 256)
                 : "memory");
  }
  if (role == 0) wroles<0>(h, par, idx & 7, xg, w, nonce, x, Wq, Wk, Wv, Wo, ws);
  if (role == 1) wroles<1>(h, par, idx & 7, xg, w, nonce, x, Wq, Wk, Wv, Wo, ws);
  if (role == 2) wroles<2>(h, par, idx & 7, xg, w, nonce, x, Wq, Wk, Wv, Wo, ws);
  if (role == 3) wroles<3>(h, par, idx & 7, xg, w, nonce, x, Wq, Wk, Wv, Wo, ws);
  const int oc = xg * PO + idx - 2 * PQ;
  unsigned perm = 0;
  if (role == 4) perm = owner<true>(oc, w, nonce, mem0, ws, out, 0u, w_slow(Wq, Wk, Wv, w, xg), ax, awr, awi);
  TMID(2);
  char* lds = g_lds;
  const __amdgpu_buffer_rsrc_t rs = __builtin_amdgcn_make_buffer_rsrc(ws, 0, 65536, 0x00020000);
  gu32* tmo = (gu32*)(ws + OFF_TMO);
  volatile LAS int* sact = (volatile LAS int*)(g_lds + 163584);
  unsigned idle = 0;
#define TLOG(id)
  auto pick = [&]() __attribute__((always_inline)) {
    int off = CNT_FFT1 + (olane() & 7) * 128;
    asm volatile("" : "+v"(off));
    const unsigned v = __builtin_amdgcn_raw_buffer_load_b32(rs, off, 0, AUX_SC1) + 1u;
    const unsigned d1 = __builtin_amdgcn_readlane(v, 0), d2 = __builtin_amdgcn_readlane(v, 1);
    const unsigned q1 = __builtin_amdgcn_readlane(v, 4), q2 = __builtin_amdgcn_readlane(v, 5), q3 = __builtin_amdgcn_readlane(v, 6);
    int kind = -1, qoff = 0;
    unsigned lim = 0;
    if (q1 < 64u) kind = 1, qoff = Q_FFT1, lim = 64u;
    else if (d1 >= 128u && q2 < 65u) kind = 2, qoff = Q_FFT2, lim = 65u;
    else if (d2 >= 65u && q3 < 128u) kind = 3, qoff = Q_FFT3, lim = 128u;
    else if (q3 >= 128u) kind = -2;
    int act = kind;
    if (kind >= 0) {
      unsigned t = 0;
      if (olane() == 0) t = __hip_atomic_fetch_add((gu32*)(ws + qoff), 1u, RLX_AGENT) + 1u;
      t = __builtin_amdgcn_readfirstlane(t);
      act = t < lim ? (kind << 16) | (int)t : -3;
    }
    return act;
  };
  fft_table(lds, w);
  int done = role == 4 ? CNT_ARM : 0;
  for (;;) {
    if (w == 7) *sact = role == 4 ? (done == CNT_ARM ? 0x10040 | oc : -2) : pick();
    VMCNT(0);
    RAW_BARRIER();
    if (done && w == 0 && olane() == 0) __hip_atomic_fetch_add((gu32*)(ws + done), 1u, RLX_AGENT);
    done = 0;
    const int act = __builtin_amdgcn_readfirstlane(*sact);
    RAW_BARRIER();
    if (act == -2) break;
    if (act >= 0) {
      const int kind = act >> 16, t = act & 0xffff;
      for (int k = kind == 3 ? t * STG3 >> 1 : (kind == 2 && t < 32 ? t * STG2 : 0); k > 0; --k) __builtin_amdgcn_s_sleep(1);
      TLOG(1000 + kind);
      if (kind == 1) {
        fft_pass1(ws, ax, awr, awi, t ^ 64, w, role == 4 ? x : nullptr);
        done = CNT_FFT1;
        if (FLC && role == 4) break;
      } else if (kind == 2) {
        fft_pass2(ws, t == 0 ? 64 : t - 1, w);
        done = CNT_FFT2;
      } else fft_pass3(ws, out, t, w);
      TLOG(999);
    } else if (act == -1) {
      __builtin_amdgcn_s_sleep(FSL);
      if (spin_fail(idle, tmo, 0x30022)) break;
    }
  }
  if (role == 4) {
    owner<false>(oc, w, nonce, mem0, ws, out, perm, nullptr, nullptr, nullptr, nullptr);
    return;
  }
  if (WBL & 1)
    if (w == 0) asm volatile("buffer_wbl2 sc1\n\ts_waitcnt vmcnt(0)" ::: "memory");
  TEND;
}

extern "C" void kernel_launch(void* const* d_in, const int* in_sizes, int n_in, void* d_out, int out_size, void* d_ws,
                              size_t ws_size, hipStream_t stream) {
  const u16* A_x = (const u16*)d_in[0];
  const u16* A_wr = (const u16*)d_in[1];
  const u16* A_wi = (const u16*)d_in[2];
  const u16* B_x = (const u16*)d_in[3];
  const u16* B_mem0 = (const u16*)d_in[4];
  const u16* Wq = (const u16*)d_in[5];
  const u16* Wk = (const u16*)d_in[6];
  const u16* Wv = (const u16*)d_in[7];
  const u16* Wo = (const u16*)d_in[8];
  u16* out = (u16*)d_out;
  char* ws = (char*)d_ws + ((size_t)WSH << 20);

  mega_kernel<<<256, 512, 0, stream>>>(B_x, B_mem0, Wq, Wk, Wv, Wo, ws, out, A_x, A_wr, A_wi);
}
```
